```python
import torch

def _softmax(x, axis=-1):
    return torch.softmax(x, dim=axis)
def _gelu(x):
    return torch.nn.functional.gelu(x, approximate="tanh")
def _silu(x):
    return torch.nn.functional.silu(x)
def _sigmoid(x):
    return torch.sigmoid(x)
def _layernorm(x, axis=-1, eps=1e-5):
    m = x.mean(dim=axis, keepdim=True)
    v = x.var(dim=axis, keepdim=True, unbiased=False)
    return (x - m) / torch.sqrt(v + eps)
def _scatter_add(tgt, idx, src, axis=0):
    return tgt.clone().index_add_(axis, idx, src)
def _segment_sum(src, seg, n):
    out = torch.zeros((n,) + src.shape[1:], dtype=src.dtype)
    return out.index_add_(0, seg, src)
def _pad(x, pw):
    flat = tuple(v for pair in reversed(pw) for v in pair)
    return torch.nn.functional.pad(x, flat)

kernel_name = 'recurrent_scan__sliding_local_dilated__moe_arch_mod__prompt'

def setup_inputs():

    _w = (lambda a: (lambda t: t.to(torch.bfloat16) if t.is_floating_point() else t)(torch.as_tensor(a)))
    def _A_setup():
        B, S, D = 1, 2048, 640
        return {
            "x": _w((lambda *s: torch.randn(*s, dtype=torch.bfloat16) * 0.05)(B, S, D)),
            "wqkv": _w((lambda *s: torch.randn(*s, dtype=torch.bfloat16) * 0.05)(D, 3 * D)),
            "wo": _w((lambda *s: torch.randn(*s, dtype=torch.bfloat16) * 0.05)(D, D)),
        }
    _A_in = _A_setup()

    def _B_setup():
        B, S, D, F, C = 1, 2048, 640, 1728, 2
        return {
            "x": _w((lambda *s: torch.randn(*s, dtype=torch.bfloat16) * 0.05)(B, S, D)),
            "w_r": _w((lambda *s: torch.randn(*s, dtype=torch.bfloat16) * 0.05)(D)),
            "w_gate": _w((lambda *s: torch.randn(*s, dtype=torch.bfloat16) * 0.05)(D, F)),
            "w_up": _w((lambda *s: torch.randn(*s, dtype=torch.bfloat16) * 0.05)(D, F)),
            "w_down": _w((lambda *s: torch.randn(*s, dtype=torch.bfloat16) * 0.05)(F, D)),
        }
    _B_in = _B_setup()

    return {
        "A_x": _A_in["x"],
        "A_wqkv": _A_in["wqkv"],
        "A_wo": _A_in["wo"],
        "B_x": _B_in["x"],
        "B_w_r": _B_in["w_r"],
        "B_w_gate": _B_in["w_gate"],
        "B_w_up": _B_in["w_up"],
        "B_w_down": _B_in["w_down"],
    }

def reference(A_x, A_wqkv, A_wo, B_x, B_w_r, B_w_gate, B_w_up, B_w_down):
    def _A_ref(x, wqkv, wo):
        B, S, D = x.shape
        H = 32; Dh = D // H
        W, dil = 8, 2
        qkv = (lambda x, *d: x.permute(*d))((x @ wqkv).reshape(B, S, 3, H, Dh), 2, 0, 3, 1, 4)
        q, k, v = qkv[0], qkv[1], qkv[2]
        scale = Dh ** -0.5
        pos = torch.arange(S)
        off = torch.arange(W) * dil
        idx = pos[:, None] - off[None, :]
        mask = idx >= 0
        idx_c = torch.where(mask, idx, 0).reshape(-1)
        kg = k[:, :, idx_c, :].reshape(B, H, S, W, Dh)
        vg = v[:, :, idx_c, :].reshape(B, H, S, W, Dh)
        logits = torch.einsum("bhsd,bhswd->bhsw", q, kg) * scale
        logits = torch.where(mask[None, None, :, :], logits, -1e9)
        attn = _softmax(logits, axis=-1)
        o = torch.einsum("bhsw,bhswd->bhsd", attn, vg)
        return (lambda x, *d: x.permute(*d))(o, 0, 2, 1, 3).reshape(B, S, D) @ wo

    def _B_ref(x, w_r, w_gate, w_up, w_down):
        B, S, D = x.shape
        C = 2
        r = x @ w_r
        idx = torch.argsort(-r, axis=-1)[:, :C]
        idx_e = (lambda x, n, axis: torch.repeat_interleave(x, n, dim=axis))(idx.reshape(B, C, 1), D, axis=2)
        x_sel = (lambda x, i, axis: torch.take_along_dim(x, i, dim=axis))(x, idx_e, axis=1)
        g = _silu(x_sel @ w_gate)
        u = x_sel @ w_up
        y = (g * u) @ w_down
        r_sel = _sigmoid((lambda x, i, axis: torch.take_along_dim(x, i, dim=axis))(r, idx, axis=1))
        y = y * r_sel.reshape(B, C, 1)
        b_off = (torch.arange(B) * S).reshape(B, 1)
        flat_idx = (idx + b_off).reshape(B * C)
        zero = (x * 0.0).reshape(B * S, D)
        upd = _scatter_add(zero, flat_idx, y.reshape(B * C, D), axis=0)
        return x + upd.reshape(B, S, D)

    x = A_x
    xn = _layernorm(x, axis=-1)
    s = _A_ref(xn, A_wqkv, A_wo)
    g = _silu(_B_ref(xn, B_w_r, B_w_gate, B_w_up, B_w_down))
    return x + s * g
```

```cpp
#include <hip/hip_runtime.h>
#include <type_traits>

typedef __attribute__((ext_vector_type(8))) short bf16x8;
typedef __attribute__((ext_vector_type(4))) short bf16x4;
typedef __attribute__((ext_vector_type(4))) float f32x4;
typedef __attribute__((ext_vector_type(2))) float f32x2;
typedef __attribute__((ext_vector_type(4))) unsigned u32x4;
typedef __attribute__((ext_vector_type(2))) unsigned u32x2;
typedef __attribute__((ext_vector_type(2))) __bf16 bf16x2v;
typedef __attribute__((ext_vector_type(8))) unsigned u32x8;
typedef __attribute__((ext_vector_type(16))) unsigned u32x16;
typedef unsigned short u16;
typedef __attribute__((address_space(3))) char lchar;

#define F_ 1728
constexpr int W_EP = 0;
constexpr int W_KEY = 4096;
constexpr int W_ACK = 8192;
constexpr int W_H = 12288;
constexpr int W_ST = 65536;
constexpr int W_Y = 98304;
constexpr int W_O = 155648;
constexpr int W_END = W_O + 80 * 40960;
#define SENT 0xffffffffu
constexpr int XS = 14336, SLOT = XS + 24576;
constexpr int WO0 = 2 * SLOT;
constexpr int MISC = WO0 + 81920;
constexpr int FL_S = MISC, FL_P = MISC + 16, FL_E = MISC + 32, FL_W = MISC + 48, FL_R = MISC + 64, FL_AUX = MISC + 80, ONES = MISC + 128, ROUT = MISC + 144, LTAG = MISC + 160;
constexpr int L_NM = MISC + 256, L_RS = L_NM + 512 + 64, L_RM = L_RS + 512, L_RG = L_RM + 512, L_DUM = L_RG + 512;
constexpr int LS_ROW = L_NM;
constexpr int LDSZ = 163840;
static_assert(L_DUM + 512 <= LDSZ, "lds");
#define SPIN_MAX (1 << 20)
#define SPIN _Pragma("nounroll") for (int it = 0; it < SPIN_MAX; ++it)

__device__ __forceinline__ unsigned cvtpk(float lo, float hi) { return __builtin_bit_cast(unsigned, __builtin_convertvector((f32x2){lo, hi}, bf16x2v)); }
__device__ __forceinline__ unsigned nosent(unsigned u) { return u < 0xfffffffeu ? u : 0xfffffffeu; }
__device__ __forceinline__ float lo2f(unsigned u) { return __uint_as_float(u << 16); }
__device__ __forceinline__ float hi2f(unsigned u) { return __uint_as_float(u & 0xffff0000u); }
__device__ __forceinline__ float rbf(float f) { return lo2f(cvtpk(f, f)); }
template <int CTRL> __device__ __forceinline__ float dppf(float x) { return __builtin_bit_cast(float, __builtin_amdgcn_mov_dpp(__builtin_bit_cast(int, x), CTRL, 0xf, 0xf, true)); }
template <int CTRL> __device__ __forceinline__ unsigned dppu(unsigned x) { return (unsigned)__builtin_amdgcn_mov_dpp((int)x, CTRL, 0xf, 0xf, true); }
__device__ __forceinline__ void sw16(unsigned& a, unsigned& b) { const auto r = __builtin_amdgcn_permlane16_swap(a, b, false, false); a = r[0]; b = r[1]; }
__device__ __forceinline__ void sw32(unsigned& a, unsigned& b) { const auto r = __builtin_amdgcn_permlane32_swap(a, b, false, false); a = r[0]; b = r[1]; }
__device__ __forceinline__ void sw16f(float& a, float& b) { unsigned x = __float_as_uint(a), y = __float_as_uint(b); sw16(x, y); a = __uint_as_float(x); b = __uint_as_float(y); }
__device__ __forceinline__ void sw32f(float& a, float& b) { unsigned x = __float_as_uint(a), y = __float_as_uint(b); sw32(x, y); a = __uint_as_float(x); b = __uint_as_float(y); }
__device__ __forceinline__ float vmax(float a, float b) { return __builtin_elementwise_maximum(a, b); }
__device__ __forceinline__ float sum16(float x) { x += dppf<0xB1>(x); x += dppf<0x4E>(x); x += dppf<0x141>(x); x += dppf<0x140>(x); return x; }
__device__ __forceinline__ float dot2(unsigned a, unsigned b, float c) { return __builtin_amdgcn_fdot2_f32_bf16(__builtin_bit_cast(bf16x2v, a), __builtin_bit_cast(bf16x2v, b), c, false); }
__device__ __forceinline__ float silu_b(float v) { return v * __builtin_amdgcn_rcpf(__builtin_amdgcn_exp2f(v * -1.4426950408889634f) + 1.f); }
__device__ __forceinline__ unsigned rkey(float rb, int row) {
    unsigned b = __float_as_uint(rb) >> 16; b = (b & 0x8000u) ? (~b & 0xffffu) : (b | 0x8000u);
    return (b << 16) | (unsigned)(0xffff - row);
}
__device__ __forceinline__ float keyscore(unsigned k) { const unsigned b = k >> 16; return __uint_as_float(((b & 0x8000u) ? (b & 0x7fffu) : (~b & 0xffffu)) << 16); }
#define T2M(b0, b1) { const unsigned _b0 = (b0), _b1 = (b1); const unsigned _m = min(k0, _b0); k0 = max(k0, _b0); k1 = max(_m, max(k1, _b1)); }
__device__ __forceinline__ uint2 top2wave(unsigned k0, unsigned k1) {
    T2M(dppu<0xB1>(k0), dppu<0xB1>(k1)); T2M(dppu<0x4E>(k0), dppu<0x4E>(k1));
    T2M(dppu<0x141>(k0), dppu<0x141>(k1)); T2M(dppu<0x140>(k0), dppu<0x140>(k1));
    const unsigned a0 = __builtin_amdgcn_readlane(k0, 0), a1 = __builtin_amdgcn_readlane(k1, 0);
    const unsigned c0 = __builtin_amdgcn_readlane(k0, 16), c1 = __builtin_amdgcn_readlane(k1, 16);
    const unsigned d0 = __builtin_amdgcn_readlane(k0, 32), d1 = __builtin_amdgcn_readlane(k1, 32);
    const unsigned e0 = __builtin_amdgcn_readlane(k0, 48), e1 = __builtin_amdgcn_readlane(k1, 48);
    k0 = a0; k1 = a1; T2M(c0, c1); T2M(d0, d1); T2M(e0, e1);
    return make_uint2(k0, k1);
}
template <int N> __device__ __forceinline__ void wvm() { asm volatile("" ::: "memory"); __builtin_amdgcn_s_waitcnt((N & 15) | ((N >> 4) << 14) | 0x0F70); asm volatile("" ::: "memory"); }
__device__ __forceinline__ bf16x4 trread(unsigned a) { return __builtin_amdgcn_ds_read_tr16_b64_v4i16((__attribute__((address_space(3))) bf16x4*)(size_t)a); }
template <class T> __device__ __forceinline__ T ldsr(unsigned a) { return *(const __attribute__((address_space(3))) T*)(size_t)a; }
template <class T> __device__ __forceinline__ T ldsrv(unsigned a) { return *(volatile __attribute__((address_space(3))) T*)(size_t)a; }
template <class T> __device__ __forceinline__ void ldsw(unsigned a, T v) { *(volatile __attribute__((address_space(3))) T*)(size_t)a = v; }
__device__ __forceinline__ bf16x8 cat8(bf16x4 lo, bf16x4 hi) { return (bf16x8){lo[0], lo[1], lo[2], lo[3], hi[0], hi[1], hi[2], hi[3]}; }
__device__ __forceinline__ bf16x8 frag4(unsigned a, unsigned b, unsigned c, unsigned d) { return __builtin_bit_cast(bf16x8, (u32x4){a, b, c, d}); }
__device__ __forceinline__ f32x4 mfma32(bf16x8 a, bf16x8 b, f32x4 c) { return __builtin_amdgcn_mfma_f32_16x16x32_bf16(a, b, c, 0, 0, 0); }
__device__ __forceinline__ int lane_id() { int l; asm volatile("v_mbcnt_lo_u32_b32 %0, -1, 0\n\tv_mbcnt_hi_u32_b32 %0, -1, %0" : "=v"(l)); __builtin_assume(l >= 0 && l < 64); return l; }
#define SB() __builtin_amdgcn_sched_barrier(0)
#define BAR() asm volatile("s_waitcnt lgkmcnt(0)\n\ts_barrier" ::: "memory")
#define MEMB() asm volatile("" ::: "memory")
#define RFL(x) ((unsigned)__builtin_amdgcn_readfirstlane((int)(x)))
#define SC1 16

#define WOPF 1
#define DSEL "0"
#define TRG1D
#define TRG2D
#define PRIA "s_setprio 2\n\t"
#define TCHD
#define RSLP "s_sleep 3\n\t"
#define PRIR "s_setprio 0\n\t"
#define PRIO
#define RSL0 "s_sleep 4\n\t"
#define RSL1 "s_sleep 3\n\t"
#define ASLP "s_sleep 1\n\t"
#define PRIS "s_setprio 3\n\t"
#define PRIG "s_setprio 0\n\t"
#define PSLP "s_sleep 1\n\t"
#define HSL 8
#define HS2 4
#define EARLYK 1
#define NOPK(v) asm("" : "+v"(v))
#define OPQ(v) asm volatile("" : "+v"(v))
#define IB(m) __builtin_amdgcn_inverse_ballot_w64(m)
#define END() do { asm volatile("s_endpgm"); __builtin_amdgcn_s_waitcnt(0); return; } while (0)
#define BARN() asm volatile("s_barrier" ::: "memory")
#define TL(i)
#define TA(i)
#define TLEND()
__device__ __forceinline__ constexpr int phys(int S) { return (S + 1) % 3; }

__global__ __launch_bounds__(512, 2) void fused(const u16* x_, const u16* wqkv_, const u16* wo_, const u16* w_r_, const u16* wg_, const u16* wu_, const u16* wd_, char* ws_, u16* out_) {
    extern __shared__ __attribute__((aligned(16))) char smem[];
#define SL(k)
#define DUMP
#define SZ
#define SB_(k)
#define SO(k)
#define RINC
#define RZ
#define SA(k)
#define ST(h)
#define SP0
#define LINC
#define RCNT
    unsigned tag, bid = blockIdx.x; int tid = threadIdx.x;
    typedef __attribute__((address_space(1))) char gchar;
    typedef gchar* const __attribute__((address_space(4)))* karg_t;
    karg_t ka = (karg_t)__builtin_amdgcn_kernarg_segment_ptr();
    asm volatile(
            SL(0) SZ
            "v_readfirstlane_b32 s69, %[t]\n\ts_lshr_b32 s70, %[b], 3\n\ts_cmpk_lg_u32 s69, 0xc0\n\ts_cbranch_scc1 1f\n\ts_getpc_b64 s[72:73]\n\t.Lch:\n"
            "s_lshl3_add_u32 s71, s70, 0xffffff40\n\tv_add_lshl_u32 v36, s71, %[t], 7\n\tv_min_u32 v36, .Lce-.Lch-4, v36\n\ts_mov_b32 m0, 162368\n\ts_mov_b64 exec, 0xff\n\tglobal_load_lds_dword v36, s[72:73]\n\t"
            "s_mov_b64 exec, -1\n\t1:\ns_add_i32 s70, s70, 25\n\ts_and_b32 s70, s70, 31\n\ts_cmp_lt_u32 s70, 25\n\ts_cbranch_scc0 .Lout\n\t"
            "s_load_dwordx2 s[40:41], %[k], 0x8\n\ts_load_dwordx2 s[36:37], %[k], 0x0\n\tv_bfe_u32 v36, %[t], 3, 3\n\tv_and_b32 v37, 7, %[t]\n\ts_mul_i32 s71, s70, 13\n\ts_lshr_b32 s71, s71, 6\n\t"
            "v_and_b32 v38, 2, v36\n\tv_xor_b32 v46, v38, v37\n\ts_lshl_b32 s64, s71, 2\n\ts_sub_i32 s64, s70, s64\n\ts_sub_i32 s57, s64, s71\n\tv_mul_u32_u24 v40, 0xf00, v36\n\t"
            "s_sub_i32 s72, s64, 5\n\ts_min_u32 s64, s64, s72\n\tv_lshl_add_u32 v40, v46, 4, v40\n\ts_bfe_u32 s56, s69, 0x20006\n\ts_and_b32 s72, %[b], 1\n\ts_mul_i32 s72, s72, 5\n\t"
            "s_add_i32 s58, s57, s72\n\ts_lshl_b32 s58, s58, 7\n\ts_mul_i32 s61, s64, 0x78000\n\ts_mul_i32 s73, s56, 0xf000\n\ts_add_i32 s73, s73, s58\n\ts_add_i32 s61, s61, s73\n\t"
            "s_mul_i32 s66, s56, 0x1800\n\ts_add_i32 m0, s66, 53248\n\ts_mov_b32 s42, 0x258000\n\ts_mov_b32 s43, 0x20000\n\tv_add_u32 v41, 0x100, v40\n\tv_add_u32 v42, 0x200, v40\n\t"
            "v_xor_b32 v43, 64, v40\n\tv_add_u32 v43, 0x6c00, v43\n\tv_add_u32 v44, 0x1100, v43\n\tv_add_u32 v45, 0x1200, v43\n\ts_cmpk_lt_u32 s69, 0x100\n\ts_cbranch_scc1 1f\n\t"
            "s_waitcnt lgkmcnt(0)\n\tbuffer_load_dwordx4 v40, s[40:43], s61 offen lds\n\tbuffer_load_dwordx4 v41, s[40:43], s61 offen offset:1024 lds\n\tbuffer_load_dwordx4 v42, s[40:43], s61 offen offset:2048 lds\n\tbuffer_load_dwordx4 v43, s[40:43], s61 offen offset:3072 lds\n\ts_addk_i32 m0, 0x1000\n\t"
            "s_nop 0\n\tbuffer_load_dwordx4 v44, s[40:43], s61 offen lds\n\tbuffer_load_dwordx4 v45, s[40:43], s61 offen offset:1024 lds\n\t1:\ns_mul_i32 s72, s56, 28\n\tv_add_u32 v39, s72, v36\n\t"
            "s_bfe_u32 s62, %[b], 0x10001\n\ts_mul_i32 s62, s62, 5\n\ts_add_i32 s62, s62, s71\n\ts_mul_i32 s62, s62, 104\n\ts_sub_i32 s62, s62, 8\n\tv_and_b32 v47, 3, v39\n\t"
            "v_lshl_add_u32 v47, v47, 2, s62\n\tv_and_b32 v48, 0x70, v39\n\tv_bfe_u32 v49, v39, 2, 2\n\tv_add3_u32 v48, v48, v49, v47\n\tv_bfe_u32 v49, v39, 1, 3\n\tv_xor_b32 v50, v49, v37\n\t"
            "v_mul_i32_i24 v48, 0xa00, v48\n\ts_bfe_u32 s63, %[b], 0x10002\n\ts_mul_i32 s63, s63, 0x500\n\ts_lshl_b32 s60, s64, 8\n\tv_mov_b32 v51, v36\n\tv_lshl_add_u32 v36, v50, 4, v48\n\t"
            "s_add_i32 s60, s60, s63\n\ts_mul_i32 s65, s56, 0xe00\n\ts_add_i32 m0, s65, 38912\n\ts_mov_b32 s38, 0x280000\n\ts_mov_b32 s39, 0x20000\n\tv_add_u32 v38, 0x9800, v36\n\t"
            "s_cmpk_lt_u32 s69, 0x100\n\ts_cbranch_scc1 1f\n\tbuffer_load_dwordx4 v36, s[36:39], s60 offen lds\n\tbuffer_load_dwordx4 v38, s[36:39], s60 offen offset:2048 lds\n\t1:\ns_load_dwordx2 s[44:45], %[k], 0x38\n\t"
            "s_load_dwordx2 s[48:49], %[k], 0x10\n\ts_load_dwordx2 s[52:53], %[k], 0x40\n\tv_add_u32 v39, 8, v39\n\tv_and_b32 v48, 0x70, v39\n\tv_bfe_u32 v49, v39, 2, 2\n\tv_add3_u32 v48, v48, v49, v47\n\t"
            "v_xor_b32 v50, 4, v50\n\tv_mul_i32_i24 v48, 0xa00, v48\n\tv_lshl_add_u32 v37, v50, 4, v48\n\tv_subrev_u32 v37, 0x400, v37\n\tv_add_u32 v39, 0x9800, v37\n\ts_addk_i32 s66, 14336\n\t"
            "s_bfe_u32 s59, %[b], 0x20001\n\ts_mul_i32 s59, s59, 5\n\ts_add_i32 s59, s59, s71\n\ts_mov_b32 s46, 3432448\n\ts_mov_b32 s47, 0x20000\n\ts_mov_b32 s54, 0x280000\n\t"
            "s_mov_b32 s55, 0x20000\n\tv_and_b32 v52, 63, %[t]\n\tv_bfe_u32 v53, %[t], 4, 2\n\tv_and_b32 v54, 15, %[t]\n\tv_and_b32 v55, 3, %[t]\n\tv_bfe_u32 v56, %[t], 2, 2\n\t"
            "v_lshl_add_u32 v57, v55, 2, v56\n\ts_lshr_b32 s69, s69, 6\n\ts_cmp_lt_u32 s69, 4\n\ts_cbranch_scc0 .LB\n\tv_lshrrev_b32 v2, 1, v54\n\tv_xor_b32 v2, v2, v53\n\t"
            "v_lshlrev_b32 v3, 7, v54\n\tv_lshl_add_u32 v2, v2, 4, v3\n\tv_xor_b32 v3, 64, v2\n\tv_add_u32 v4, 38912, v2\n\tv_add_u32 v5, 38912, v3\n\tv_add_u32 v10, 77824, v2\n\t"
            "v_add_u32 v11, 77824, v3\n\tv_mov_b32 v12, 159872\n\tv_cmp_eq_u32 vcc, 0, v54\n\tv_mov_b32 v60, 0\n\tv_mov_b32 v61, 0\n\tv_mov_b32 v62, 0x3f803f80\n\t"
            "v_lshlrev_b32 v63, 3, v52\n\tv_cndmask_b32 v6, v2, v12, vcc\n\tv_cndmask_b32 v7, v3, v12, vcc\n\tv_cndmask_b32 v8, v4, v12, vcc\n\tv_cndmask_b32 v9, v5, v12, vcc\n\tv_cndmask_b32 v10, v10, v12, vcc\n\t"
            "v_cndmask_b32 v11, v11, v12, vcc\n\tv_lshlrev_b32 v64, 2, v52\n\tv_lshrrev_b32 v65, 2, v52\n\tv_cmp_eq_u32 vcc, 8, v65\n\tv_add_u32 v66, 161088, v63\n\tv_add_u32 v64, 159744, v64\n\t"
            "v_cndmask_b32 v62, 0, v62, vcc\n\ts_cmp_lg_u32 s69, 1\n\ts_cbranch_scc1 1f\n\tds_write_b32 v64, v62\n\tds_write_b64 v66, v[60:61]\n\t1:\n"
            "v_bfe_u32 v12, v52, 3, 2\n\tv_xor_b32 v12, s69, v12\n\tv_lshlrev_b32 v13, 7, v56\n\tv_lshl_add_u32 v13, v55, 3, v13\n\tv_lshl_add_u32 v12, v12, 5, v13\n\tv_mul_u32_u24 v13, 0xc00, v53\n\t"
            "s_movk_i32 s72, 14336\n\tv_add3_u32 v12, v12, v13, s72\n\tv_add_u32 v13, 38912, v12\n\ts_mov_b32 s72, 160000\n\tv_lshl_add_u32 v14, v54, 2, s72\n\tv_lshl_add_u32 v15, v53, 4, s72\n\t"
            "s_cmp_eq_u32 s69, 3\n\ts_cselect_b32 s74, 1, 0\n\ts_cselect_b64 s[76:77], -1, 0\n\ts_cselect_b32 s75, 4, 0\n\ts_add_i32 s72, s69, 1\n\ts_bfe_u32 s72, s72, 0x10001\n\t"
            "s_xor_b32 s72, s72, 1\n\ts_lshr_b32 s73, s69, 1\n\ts_lshl1_add_u32 s78, s72, s73\n\ts_lshl2_add_u32 s78, s78, 159776\n\tv_mov_b32 v34, s78\n\ts_lshl2_add_u32 s78, s69, 159744\n\t"
            "v_mov_b32 v33, s78\n\ts_mul_i32 s78, s59, 163840\n\ts_lshl_b32 s79, s58, 5\n\ts_add_i32 s78, s78, s79\n\ts_lshl_b32 s79, s69, 10\n\ts_add_i32 s78, s78, s79\n\t"
            "s_add_i32 s78, s78, 155648\n\tv_lshl_add_u32 v16, v52, 4, s78\n\tv_and_b32 v215, 1, v53\n\tv_lshl_add_u32 v215, v215, 30, v16\n\ts_lshl_b32 s79, s74, 11\n\ts_add_i32 s78, s78, s79\n\t"
            "s_sub_i32 s78, s78, 0x400\n\ts_cmp_lg_u32 s57, s75\n\ts_cselect_b32 s79, s72, 0\n\ts_cmp_lg_u32 s79, 0\n\ts_cselect_b32 s78, s78, 0x40000000\n\tv_lshl_add_u32 v17, v52, 4, s78\n\t"
            "s_sub_i32 s78, s69, s57\n\ts_add_i32 s79, s78, 5\n\ts_min_u32 s78, s78, s79\n\tv_cmp_gt_u32 s[80:81], s78, v53\n\tv_cmp_gt_u32 s[82:83], s78, v56\n\ts_mov_b32 s14, 0x33363333\n\t"
            "s_mov_b32 s15, 0x36663366\n\ts_mov_b32 s16, 0x666c6666\n\ts_mov_b32 s17, 0x6ccc66cc\n\ts_mov_b32 s18, 0x33333333\n\ts_mov_b32 s19, 0x33333333\n\ts_xor_b64 s[80:81], s[80:81], s[76:77]\n\t"
            "s_xor_b64 s[82:83], s[82:83], s[76:77]\n\ts_andn2_b64 vcc, s[80:81], s[18:19]\n\tv_cndmask_b32 v18, 0, -1, vcc\n\ts_and_b64 vcc, s[80:81], s[18:19]\n\tv_cndmask_b32 v19, 0, -1, vcc\n\ts_nor_b64 vcc, s[80:81], s[18:19]\n\t"
            "v_cndmask_b32 v20, 0, -1, vcc\n\ts_andn2_b64 vcc, s[18:19], s[80:81]\n\tv_cndmask_b32 v21, 0, -1, vcc\n\tv_cndmask_b32 v22, 0, -1, s[82:83]\n\tv_cndmask_b32 v23, -1, 0, s[82:83]\n\tv_sub_u32 v58, v57, v53\n\t"
            "v_add_u32 v58, 4, v58\n\tv_and_b32 v58, 28, v58\n\tv_lshlrev_b32 v58, 1, v58\n\tv_mov_b32 v24, -1\n\tv_mov_b32 v25, 0\n\tv_lshlrev_b64 v[24:25], v58, v[24:25]\n\t"
            "v_lshlrev_b64 v[24:25], v58, v[24:25]\n\tv_sub_u32 v58, 32, v58\n\tv_min_u32 v58, 16, v58\n\tv_lshrrev_b32 v26, v58, -1\n\tv_lshrrev_b32 v26, v58, v26\n\ts_lshl1_add_u32 s78, s69, s74\n\t"
            "s_xor_b32 s79, s78, 1\n\ts_mul_i32 s78, s78, 0xc00\n\ts_mul_i32 s79, s79, 0xc00\n\ts_addk_i32 s78, 14336\n\ts_addk_i32 s79, 14336\n\tv_add_u32 v28, s78, v63\n\t"
            "v_add_u32 v29, s79, v63\n\ts_cmp_eq_u32 s69, s57\n\ts_addc_u32 s78, s69, 0\n\ts_add_i32 s79, s69, 1\n\ts_cmp_eq_u32 s79, s57\n\ts_subb_u32 s79, s79, 0\n\t"
            "s_cmp_eq_u32 s74, 1\n\ts_cselect_b32 s80, s79, s78\n\ts_cselect_b32 s81, s78, s79\n\tv_lshlrev_b32 v58, 2, v53\n\tv_lshl_add_u32 v58, v54, 4, v58\n\ts_cmp_gt_u32 s80, s57\n\t"
            "s_subb_u32 s82, s80, 0\n\ts_lshl2_add_u32 s82, s82, 159760\n\tv_mov_b32 v59, s82\n\ts_lshl_b32 s82, s80, 1\n\ts_max_u32 s82, s82, 1\n\ts_mul_i32 s82, s82, 0xc00\n\t"
            "s_addk_i32 s82, 11264\n\tv_add_u32 v30, s82, v58\n\ts_cmp_gt_u32 s81, s57\n\ts_subb_u32 s82, s81, 0\n\ts_lshl2_add_u32 s82, s82, 159760\n\tv_mov_b32 v60, s82\n\t"
            "s_lshl_b32 s82, s81, 1\n\ts_max_u32 s82, s82, 1\n\ts_mul_i32 s82, s82, 0xc00\n\ts_addk_i32 s82, 11264\n\tv_add_u32 v31, s82, v58\n\tv_cndmask_b32 v32, v59, v60, s[18:19]\n\t"
            "v_mov_b32 v35, 1\n\ts_waitcnt lgkmcnt(0)\n\ts_add_u32 m0, s65, 38912\n\ts_load_dword s67, s[44:45], 0x0\n\tbuffer_load_dwordx4 v37, s[36:39], s60 offen offset:1024 lds\n\ts_mov_b32 exec_hi, 0\n\t"
            "buffer_load_dwordx4 v39, s[36:39], s60 offen offset:3072 lds\n\ts_mov_b32 exec_hi, -1\n\ts_add_u32 m0, s66, 77824\n\ts_add_u32 s61, s61, 0x3c000\n\tbuffer_load_dwordx4 v40, s[40:43], s61 offen lds\n\tbuffer_load_dwordx4 v41, s[40:43], s61 offen offset:1024 lds\n\t"
            "buffer_load_dwordx4 v42, s[40:43], s61 offen offset:2048 lds\n\tbuffer_load_dwordx4 v43, s[40:43], s61 offen offset:3072 lds\n\ts_addk_i32 m0, 0x1000\n\ts_add_u32 s60, s60, 128\n\tbuffer_load_dwordx4 v44, s[40:43], s61 offen lds\n\tbuffer_load_dwordx4 v45, s[40:43], s61 offen offset:1024 lds\n\t"
            "s_add_u32 m0, s65, 77824\n\ts_mov_b32 s20, 0\n\ts_mov_b32 s21, 40960\n\ts_mov_b32 s22, 81920\n\ts_mov_b32 s23, 122880\n\tbuffer_load_dwordx4 v36, s[36:39], s60 offen lds\n\t"
            "buffer_load_dwordx4 v37, s[36:39], s60 offen offset:1024 lds\n\tbuffer_load_dwordx4 v38, s[36:39], s60 offen offset:2048 lds\n\ts_mov_b32 exec_hi, 0\n\tbuffer_load_dwordx4 v39, s[36:39], s60 offen offset:3072 lds\n\ts_mov_b32 exec_hi, -1\n\ts_mov_b32 s68, 0x40000\n\t"
            "s_waitcnt lgkmcnt(0)\n\ts_mul_i32 s67, s67, 0x19660d\n\ts_add_u32 s67, s67, 0x3c6ef35f\n\ts_waitcnt vmcnt(10)\n\ts_barrier\n\t"
            SA(1) ST(-1)
            "ds_read_b64_tr_b16 v[176:177], v13 offset:0\n\tds_read_b64_tr_b16 v[178:179], v13 offset:512\n\tds_read_b128 v[120:123], v8\n\tds_read_b128 v[124:127], v4 offset:2048\n\tds_read_b128 v[128:131], v4 offset:4096\n\tds_read_b128 v[132:135], v4 offset:6144\n\t"
            "ds_read_b128 v[136:139], v4 offset:8192\n\tds_read_b128 v[140:143], v4 offset:10240\n\tds_read_b128 v[144:147], v4 offset:12288\n\tds_read_b64_tr_b16 v[180:181], v13 offset:1024\n\tds_read_b64_tr_b16 v[182:183], v13 offset:1536\n\tds_read_b64_tr_b16 v[184:185], v13 offset:2048\n\t"
            "ds_read_b64_tr_b16 v[186:187], v13 offset:2560\n\ts_waitcnt lgkmcnt(10)\n\tv_mfma_f32_16x16x32_bf16 v[36:39], v[176:179], v[120:123], 0\n\ts_waitcnt lgkmcnt(9)\n\tv_mfma_f32_16x16x32_bf16 v[48:51], v[176:179], v[124:127], 0\n\ts_waitcnt lgkmcnt(8)\n\t"
            "v_mfma_f32_16x16x32_bf16 v[60:63], v[176:179], v[128:131], 0\n\tds_read_b64_tr_b16 v[188:189], v13 offset:12288\n\tds_read_b64_tr_b16 v[190:191], v13 offset:12800\n\ts_waitcnt lgkmcnt(9)\n\tv_mfma_f32_16x16x32_bf16 v[72:75], v[176:179], v[132:135], 0\n\tds_read_b128 v[148:151], v9\n\t"
            "s_waitcnt lgkmcnt(9)\n\tv_mfma_f32_16x16x32_bf16 v[84:87], v[176:179], v[136:139], 0\n\tds_read_b128 v[152:155], v5 offset:2048\n\ts_waitcnt lgkmcnt(9)\n\tv_mfma_f32_16x16x32_bf16 v[96:99], v[176:179], v[140:143], 0\n\tds_read_b128 v[156:159], v5 offset:4096\n\t"
            "s_waitcnt lgkmcnt(9)\n\tv_mfma_f32_16x16x32_bf16 v[108:111], v[176:179], v[144:147], 0\n\tds_read_b128 v[160:163], v5 offset:6144\n\ts_waitcnt lgkmcnt(8)\n\tv_mfma_f32_16x16x32_bf16 v[40:43], v[180:183], v[120:123], 0\n\tds_read_b128 v[164:167], v5 offset:8192\n\t"
            "v_mfma_f32_16x16x32_bf16 v[52:55], v[180:183], v[124:127], 0\n\tds_read_b128 v[168:171], v5 offset:10240\n\tv_mfma_f32_16x16x32_bf16 v[64:67], v[180:183], v[128:131], 0\n\tds_read_b128 v[172:175], v5 offset:12288\n\tv_mfma_f32_16x16x32_bf16 v[76:79], v[180:183], v[132:135], 0\n\tds_read_b64_tr_b16 v[192:193], v13 offset:13312\n\t"
            "ds_read_b64_tr_b16 v[194:195], v13 offset:13824\n\tv_mfma_f32_16x16x32_bf16 v[88:91], v[180:183], v[136:139], 0\n\tds_read_b64_tr_b16 v[196:197], v13 offset:14336\n\tds_read_b64_tr_b16 v[198:199], v13 offset:14848\n\tv_mfma_f32_16x16x32_bf16 v[100:103], v[180:183], v[140:143], 0\n\tv_mfma_f32_16x16x32_bf16 v[112:115], v[180:183], v[144:147], 0\n\t"
            "s_waitcnt lgkmcnt(13)\n\tv_mfma_f32_16x16x32_bf16 v[44:47], v[120:123], v[184:187], 0\n\tv_mfma_f32_16x16x32_bf16 v[56:59], v[124:127], v[184:187], 0\n\tv_mfma_f32_16x16x32_bf16 v[68:71], v[128:131], v[184:187], 0\n\tv_mfma_f32_16x16x32_bf16 v[80:83], v[132:135], v[184:187], 0\n\tv_mfma_f32_16x16x32_bf16 v[92:95], v[136:139], v[184:187], 0\n\t"
            "v_mfma_f32_16x16x32_bf16 v[104:107], v[140:143], v[184:187], 0\n\tv_mfma_f32_16x16x32_bf16 v[116:119], v[144:147], v[184:187], 0\n\ts_waitcnt lgkmcnt(10)\n\tv_mfma_f32_16x16x32_bf16 v[36:39], v[188:191], v[148:151], v[36:39]\n\ts_waitcnt lgkmcnt(9)\n\tv_mfma_f32_16x16x32_bf16 v[48:51], v[188:191], v[152:155], v[48:51]\n\t"
            "s_waitcnt lgkmcnt(8)\n\tv_mfma_f32_16x16x32_bf16 v[60:63], v[188:191], v[156:159], v[60:63]\n\ts_waitcnt lgkmcnt(7)\n\tv_mfma_f32_16x16x32_bf16 v[72:75], v[188:191], v[160:163], v[72:75]\n\ts_waitcnt lgkmcnt(6)\n\tv_mfma_f32_16x16x32_bf16 v[84:87], v[188:191], v[164:167], v[84:87]\n\t"
            "s_waitcnt lgkmcnt(5)\n\tv_mfma_f32_16x16x32_bf16 v[96:99], v[188:191], v[168:171], v[96:99]\n\ts_waitcnt lgkmcnt(4)\n\tv_mfma_f32_16x16x32_bf16 v[108:111], v[188:191], v[172:175], v[108:111]\n\ts_waitcnt lgkmcnt(2)\n\tv_mfma_f32_16x16x32_bf16 v[40:43], v[192:195], v[148:151], v[40:43]\n\t"
            "v_mfma_f32_16x16x32_bf16 v[52:55], v[192:195], v[152:155], v[52:55]\n\ts_waitcnt vmcnt(0) lgkmcnt(0)\n\ts_barrier\n\t"
            ST(1)
            "ds_read_b64_tr_b16 v[176:177], v13 offset:38912\n\tds_read_b64_tr_b16 v[178:179], v13 offset:39424\n\tv_mfma_f32_16x16x32_bf16 v[64:67], v[192:195], v[156:159], v[64:67]\n\tds_read_b128 v[120:123], v10\n\tv_mfma_f32_16x16x32_bf16 v[76:79], v[192:195], v[160:163], v[76:79]\n\tds_read_b128 v[124:127], v4 offset:40960\n\t"
            "v_mfma_f32_16x16x32_bf16 v[88:91], v[192:195], v[164:167], v[88:91]\n\tds_read_b128 v[128:131], v4 offset:43008\n\tv_mfma_f32_16x16x32_bf16 v[100:103], v[192:195], v[168:171], v[100:103]\n\tds_read_b128 v[132:135], v4 offset:45056\n\tv_mfma_f32_16x16x32_bf16 v[112:115], v[192:195], v[172:175], v[112:115]\n\tds_read_b128 v[136:139], v4 offset:47104\n\t"
            "v_mfma_f32_16x16x32_bf16 v[44:47], v[148:151], v[196:199], v[44:47]\n\tds_read_b128 v[140:143], v4 offset:49152\n\tv_mfma_f32_16x16x32_bf16 v[56:59], v[152:155], v[196:199], v[56:59]\n\tds_read_b128 v[144:147], v4 offset:51200\n\tv_mfma_f32_16x16x32_bf16 v[68:71], v[156:159], v[196:199], v[68:71]\n\tds_read_b64_tr_b16 v[180:181], v13 offset:39936\n\t"
            "ds_read_b64_tr_b16 v[182:183], v13 offset:40448\n\tv_mfma_f32_16x16x32_bf16 v[80:83], v[160:163], v[196:199], v[80:83]\n\tds_read_b64_tr_b16 v[184:185], v13 offset:40960\n\tds_read_b64_tr_b16 v[186:187], v13 offset:41472\n\tv_mfma_f32_16x16x32_bf16 v[92:95], v[164:167], v[196:199], v[92:95]\n\tv_mfma_f32_16x16x32_bf16 v[104:107], v[168:171], v[196:199], v[104:107]\n\t"
            "v_mfma_f32_16x16x32_bf16 v[116:119], v[172:175], v[196:199], v[116:119]\n\ts_waitcnt lgkmcnt(10)\n\tv_mfma_f32_16x16x32_bf16 v[36:39], v[176:179], v[120:123], v[36:39]\n\ts_waitcnt lgkmcnt(9)\n\tv_mfma_f32_16x16x32_bf16 v[48:51], v[176:179], v[124:127], v[48:51]\n\ts_waitcnt lgkmcnt(8)\n\t"
            "v_mfma_f32_16x16x32_bf16 v[60:63], v[176:179], v[128:131], v[60:63]\n\tds_read_b64_tr_b16 v[188:189], v13 offset:51200\n\tds_read_b64_tr_b16 v[190:191], v13 offset:51712\n\ts_waitcnt lgkmcnt(9)\n\tv_mfma_f32_16x16x32_bf16 v[72:75], v[176:179], v[132:135], v[72:75]\n\tds_read_b128 v[148:151], v11\n\t"
            "s_waitcnt lgkmcnt(9)\n\tv_mfma_f32_16x16x32_bf16 v[84:87], v[176:179], v[136:139], v[84:87]\n\tds_read_b128 v[152:155], v5 offset:40960\n\ts_waitcnt lgkmcnt(9)\n\tv_mfma_f32_16x16x32_bf16 v[96:99], v[176:179], v[140:143], v[96:99]\n\tds_read_b128 v[156:159], v5 offset:43008\n\t"
            "s_waitcnt lgkmcnt(9)\n\tv_mfma_f32_16x16x32_bf16 v[108:111], v[176:179], v[144:147], v[108:111]\n\tds_read_b128 v[160:163], v5 offset:45056\n\ts_waitcnt lgkmcnt(8)\n\tv_mfma_f32_16x16x32_bf16 v[40:43], v[180:183], v[120:123], v[40:43]\n\tds_read_b128 v[164:167], v5 offset:47104\n\t"
            "v_mfma_f32_16x16x32_bf16 v[52:55], v[180:183], v[124:127], v[52:55]\n\tds_read_b128 v[168:171], v5 offset:49152\n\tv_mfma_f32_16x16x32_bf16 v[64:67], v[180:183], v[128:131], v[64:67]\n\tds_read_b128 v[172:175], v5 offset:51200\n\tv_mfma_f32_16x16x32_bf16 v[76:79], v[180:183], v[132:135], v[76:79]\n\tds_read_b64_tr_b16 v[192:193], v13 offset:52224\n\t"
            "ds_read_b64_tr_b16 v[194:195], v13 offset:52736\n\tv_mfma_f32_16x16x32_bf16 v[88:91], v[180:183], v[136:139], v[88:91]\n\tds_read_b64_tr_b16 v[196:197], v13 offset:53248\n\tds_read_b64_tr_b16 v[198:199], v13 offset:53760\n\tv_mfma_f32_16x16x32_bf16 v[100:103], v[180:183], v[140:143], v[100:103]\n\tv_mfma_f32_16x16x32_bf16 v[112:115], v[180:183], v[144:147], v[112:115]\n\t"
            "s_waitcnt lgkmcnt(13)\n\tv_mfma_f32_16x16x32_bf16 v[44:47], v[120:123], v[184:187], v[44:47]\n\tv_mfma_f32_16x16x32_bf16 v[56:59], v[124:127], v[184:187], v[56:59]\n\tv_mfma_f32_16x16x32_bf16 v[68:71], v[128:131], v[184:187], v[68:71]\n\tv_mfma_f32_16x16x32_bf16 v[80:83], v[132:135], v[184:187], v[80:83]\n\tv_mfma_f32_16x16x32_bf16 v[92:95], v[136:139], v[184:187], v[92:95]\n\t"
            "v_mfma_f32_16x16x32_bf16 v[104:107], v[140:143], v[184:187], v[104:107]\n\tv_mfma_f32_16x16x32_bf16 v[116:119], v[144:147], v[184:187], v[116:119]\n\ts_waitcnt lgkmcnt(10)\n\tv_mfma_f32_16x16x32_bf16 v[36:39], v[188:191], v[148:151], v[36:39]\n\ts_waitcnt lgkmcnt(9)\n\tv_mfma_f32_16x16x32_bf16 v[48:51], v[188:191], v[152:155], v[48:51]\n\t"
            "s_waitcnt lgkmcnt(8)\n\tv_mfma_f32_16x16x32_bf16 v[60:63], v[188:191], v[156:159], v[60:63]\n\ts_waitcnt lgkmcnt(7)\n\tv_mfma_f32_16x16x32_bf16 v[72:75], v[188:191], v[160:163], v[72:75]\n\ts_waitcnt lgkmcnt(6)\n\tv_mfma_f32_16x16x32_bf16 v[84:87], v[188:191], v[164:167], v[84:87]\n\t"
            "s_waitcnt lgkmcnt(5)\n\tv_mfma_f32_16x16x32_bf16 v[96:99], v[188:191], v[168:171], v[96:99]\n\ts_waitcnt lgkmcnt(4)\n\tv_mfma_f32_16x16x32_bf16 v[108:111], v[188:191], v[172:175], v[108:111]\n\ts_waitcnt lgkmcnt(2)\n\tv_mfma_f32_16x16x32_bf16 v[40:43], v[192:195], v[148:151], v[40:43]\n\t"
            "v_mfma_f32_16x16x32_bf16 v[52:55], v[192:195], v[152:155], v[52:55]\n\ts_waitcnt lgkmcnt(0)\n\ts_barrier\n\t"
            ST(3)
            "ds_read_b64_tr_b16 v[176:177], v12 offset:0\n\tds_read_b64_tr_b16 v[178:179], v12 offset:512\n\tv_mfma_f32_16x16x32_bf16 v[64:67], v[192:195], v[156:159], v[64:67]\n\tds_read_b128 v[120:123], v6\n\tv_mfma_f32_16x16x32_bf16 v[76:79], v[192:195], v[160:163], v[76:79]\n\tds_read_b128 v[124:127], v2 offset:2048\n\t"
            "v_mfma_f32_16x16x32_bf16 v[88:91], v[192:195], v[164:167], v[88:91]\n\tds_read_b128 v[128:131], v2 offset:4096\n\tv_mfma_f32_16x16x32_bf16 v[100:103], v[192:195], v[168:171], v[100:103]\n\tds_read_b128 v[132:135], v2 offset:6144\n\tv_mfma_f32_16x16x32_bf16 v[112:115], v[192:195], v[172:175], v[112:115]\n\tds_read_b128 v[136:139], v2 offset:8192\n\t"
            "v_mfma_f32_16x16x32_bf16 v[44:47], v[148:151], v[196:199], v[44:47]\n\tds_read_b128 v[140:143], v2 offset:10240\n\tv_mfma_f32_16x16x32_bf16 v[56:59], v[152:155], v[196:199], v[56:59]\n\tds_read_b128 v[144:147], v2 offset:12288\n\tv_mfma_f32_16x16x32_bf16 v[68:71], v[156:159], v[196:199], v[68:71]\n\tds_read_b64_tr_b16 v[180:181], v12 offset:1024\n\t"
            "ds_read_b64_tr_b16 v[182:183], v12 offset:1536\n\tv_mfma_f32_16x16x32_bf16 v[80:83], v[160:163], v[196:199], v[80:83]\n\tds_read_b64_tr_b16 v[184:185], v12 offset:2048\n\tds_read_b64_tr_b16 v[186:187], v12 offset:2560\n\tv_mfma_f32_16x16x32_bf16 v[92:95], v[164:167], v[196:199], v[92:95]\n\tv_mfma_f32_16x16x32_bf16 v[104:107], v[168:171], v[196:199], v[104:107]\n\t"
            "v_mfma_f32_16x16x32_bf16 v[116:119], v[172:175], v[196:199], v[116:119]\n\ts_waitcnt lgkmcnt(10)\n\tv_mfma_f32_16x16x32_bf16 v[36:39], v[176:179], v[120:123], v[36:39]\n\ts_waitcnt lgkmcnt(9)\n\tv_mfma_f32_16x16x32_bf16 v[48:51], v[176:179], v[124:127], v[48:51]\n\ts_waitcnt lgkmcnt(8)\n\t"
            "v_mfma_f32_16x16x32_bf16 v[60:63], v[176:179], v[128:131], v[60:63]\n\tds_read_b64_tr_b16 v[188:189], v12 offset:12288\n\tds_read_b64_tr_b16 v[190:191], v12 offset:12800\n\ts_waitcnt lgkmcnt(9)\n\tv_mfma_f32_16x16x32_bf16 v[72:75], v[176:179], v[132:135], v[72:75]\n\tds_read_b128 v[148:151], v7\n\t"
            "s_waitcnt lgkmcnt(9)\n\tv_mfma_f32_16x16x32_bf16 v[84:87], v[176:179], v[136:139], v[84:87]\n\tds_read_b128 v[152:155], v3 offset:2048\n\ts_waitcnt lgkmcnt(9)\n\tv_mfma_f32_16x16x32_bf16 v[96:99], v[176:179], v[140:143], v[96:99]\n\tds_read_b128 v[156:159], v3 offset:4096\n\t"
            "s_waitcnt lgkmcnt(9)\n\tv_mfma_f32_16x16x32_bf16 v[108:111], v[176:179], v[144:147], v[108:111]\n\tds_read_b128 v[160:163], v3 offset:6144\n\ts_waitcnt lgkmcnt(8)\n\tv_mfma_f32_16x16x32_bf16 v[40:43], v[180:183], v[120:123], v[40:43]\n\tds_read_b128 v[164:167], v3 offset:8192\n\t"
            "v_mfma_f32_16x16x32_bf16 v[52:55], v[180:183], v[124:127], v[52:55]\n\tds_read_b128 v[168:171], v3 offset:10240\n\tv_mfma_f32_16x16x32_bf16 v[64:67], v[180:183], v[128:131], v[64:67]\n\tds_read_b128 v[172:175], v3 offset:12288\n\tv_mfma_f32_16x16x32_bf16 v[76:79], v[180:183], v[132:135], v[76:79]\n\tds_read_b64_tr_b16 v[192:193], v12 offset:13312\n\t"
            "ds_read_b64_tr_b16 v[194:195], v12 offset:13824\n\tv_mfma_f32_16x16x32_bf16 v[88:91], v[180:183], v[136:139], v[88:91]\n\tds_read_b64_tr_b16 v[196:197], v12 offset:14336\n\tds_read_b64_tr_b16 v[198:199], v12 offset:14848\n\tv_mfma_f32_16x16x32_bf16 v[100:103], v[180:183], v[140:143], v[100:103]\n\tv_mfma_f32_16x16x32_bf16 v[112:115], v[180:183], v[144:147], v[112:115]\n\t"
            "s_waitcnt lgkmcnt(13)\n\tv_mfma_f32_16x16x32_bf16 v[44:47], v[120:123], v[184:187], v[44:47]\n\tv_mfma_f32_16x16x32_bf16 v[56:59], v[124:127], v[184:187], v[56:59]\n\tv_mfma_f32_16x16x32_bf16 v[68:71], v[128:131], v[184:187], v[68:71]\n\tv_mfma_f32_16x16x32_bf16 v[80:83], v[132:135], v[184:187], v[80:83]\n\tv_mfma_f32_16x16x32_bf16 v[92:95], v[136:139], v[184:187], v[92:95]\n\t"
            "v_mfma_f32_16x16x32_bf16 v[104:107], v[140:143], v[184:187], v[104:107]\n\tv_mfma_f32_16x16x32_bf16 v[116:119], v[144:147], v[184:187], v[116:119]\n\ts_waitcnt lgkmcnt(10)\n\tv_mfma_f32_16x16x32_bf16 v[36:39], v[188:191], v[148:151], v[36:39]\n\ts_waitcnt lgkmcnt(9)\n\tv_mfma_f32_16x16x32_bf16 v[48:51], v[188:191], v[152:155], v[48:51]\n\t"
            "s_waitcnt lgkmcnt(8)\n\tv_mfma_f32_16x16x32_bf16 v[60:63], v[188:191], v[156:159], v[60:63]\n\ts_waitcnt lgkmcnt(7)\n\tv_mfma_f32_16x16x32_bf16 v[72:75], v[188:191], v[160:163], v[72:75]\n\ts_waitcnt lgkmcnt(6)\n\tv_mfma_f32_16x16x32_bf16 v[84:87], v[188:191], v[164:167], v[84:87]\n\t"
            "s_waitcnt lgkmcnt(5)\n\tv_mfma_f32_16x16x32_bf16 v[96:99], v[188:191], v[168:171], v[96:99]\n\ts_waitcnt lgkmcnt(4)\n\tv_mfma_f32_16x16x32_bf16 v[108:111], v[188:191], v[172:175], v[108:111]\n\ts_waitcnt lgkmcnt(2)\n\tv_mfma_f32_16x16x32_bf16 v[40:43], v[192:195], v[148:151], v[40:43]\n\t"
            "v_mfma_f32_16x16x32_bf16 v[52:55], v[192:195], v[152:155], v[52:55]\n\ts_waitcnt lgkmcnt(0)\n\ts_barrier\n\t"
            ST(5)
            "ds_read_b64_tr_b16 v[176:177], v13 offset:0\n\tds_read_b64_tr_b16 v[178:179], v13 offset:512\n\tv_mfma_f32_16x16x32_bf16 v[64:67], v[192:195], v[156:159], v[64:67]\n\tds_read_b128 v[120:123], v8\n\tv_mfma_f32_16x16x32_bf16 v[76:79], v[192:195], v[160:163], v[76:79]\n\tds_read_b128 v[124:127], v4 offset:2048\n\t"
            "v_mfma_f32_16x16x32_bf16 v[88:91], v[192:195], v[164:167], v[88:91]\n\tds_read_b128 v[128:131], v4 offset:4096\n\tv_mfma_f32_16x16x32_bf16 v[100:103], v[192:195], v[168:171], v[100:103]\n\tds_read_b128 v[132:135], v4 offset:6144\n\tv_mfma_f32_16x16x32_bf16 v[112:115], v[192:195], v[172:175], v[112:115]\n\tds_read_b128 v[136:139], v4 offset:8192\n\t"
            "v_mfma_f32_16x16x32_bf16 v[44:47], v[148:151], v[196:199], v[44:47]\n\tds_read_b128 v[140:143], v4 offset:10240\n\tv_mfma_f32_16x16x32_bf16 v[56:59], v[152:155], v[196:199], v[56:59]\n\tds_read_b128 v[144:147], v4 offset:12288\n\tv_mfma_f32_16x16x32_bf16 v[68:71], v[156:159], v[196:199], v[68:71]\n\tds_read_b64_tr_b16 v[180:181], v13 offset:1024\n\t"
            "ds_read_b64_tr_b16 v[182:183], v13 offset:1536\n\tv_mfma_f32_16x16x32_bf16 v[80:83], v[160:163], v[196:199], v[80:83]\n\tds_read_b64_tr_b16 v[184:185], v13 offset:2048\n\tds_read_b64_tr_b16 v[186:187], v13 offset:2560\n\tv_mfma_f32_16x16x32_bf16 v[92:95], v[164:167], v[196:199], v[92:95]\n\tv_mfma_f32_16x16x32_bf16 v[104:107], v[168:171], v[196:199], v[104:107]\n\t"
            "v_mfma_f32_16x16x32_bf16 v[116:119], v[172:175], v[196:199], v[116:119]\n\ts_waitcnt lgkmcnt(10)\n\tv_mfma_f32_16x16x32_bf16 v[36:39], v[176:179], v[120:123], v[36:39]\n\ts_waitcnt lgkmcnt(9)\n\tv_mfma_f32_16x16x32_bf16 v[48:51], v[176:179], v[124:127], v[48:51]\n\ts_waitcnt lgkmcnt(8)\n\t"
            "v_mfma_f32_16x16x32_bf16 v[60:63], v[176:179], v[128:131], v[60:63]\n\tds_read_b64_tr_b16 v[188:189], v13 offset:12288\n\tds_read_b64_tr_b16 v[190:191], v13 offset:12800\n\ts_waitcnt lgkmcnt(9)\n\tv_mfma_f32_16x16x32_bf16 v[72:75], v[176:179], v[132:135], v[72:75]\n\tds_read_b128 v[148:151], v9\n\t"
            "s_waitcnt lgkmcnt(9)\n\tv_mfma_f32_16x16x32_bf16 v[84:87], v[176:179], v[136:139], v[84:87]\n\tds_read_b128 v[152:155], v5 offset:2048\n\ts_waitcnt lgkmcnt(9)\n\tv_mfma_f32_16x16x32_bf16 v[96:99], v[176:179], v[140:143], v[96:99]\n\tds_read_b128 v[156:159], v5 offset:4096\n\t"
            "s_waitcnt lgkmcnt(9)\n\tv_mfma_f32_16x16x32_bf16 v[108:111], v[176:179], v[144:147], v[108:111]\n\tds_read_b128 v[160:163], v5 offset:6144\n\ts_waitcnt lgkmcnt(8)\n\tv_mfma_f32_16x16x32_bf16 v[40:43], v[180:183], v[120:123], v[40:43]\n\tds_read_b128 v[164:167], v5 offset:8192\n\t"
            "v_mfma_f32_16x16x32_bf16 v[52:55], v[180:183], v[124:127], v[52:55]\n\tds_read_b128 v[168:171], v5 offset:10240\n\tv_mfma_f32_16x16x32_bf16 v[64:67], v[180:183], v[128:131], v[64:67]\n\tds_read_b128 v[172:175], v5 offset:12288\n\tv_mfma_f32_16x16x32_bf16 v[76:79], v[180:183], v[132:135], v[76:79]\n\tds_read_b64_tr_b16 v[192:193], v13 offset:13312\n\t"
            "ds_read_b64_tr_b16 v[194:195], v13 offset:13824\n\tv_mfma_f32_16x16x32_bf16 v[88:91], v[180:183], v[136:139], v[88:91]\n\tds_read_b64_tr_b16 v[196:197], v13 offset:14336\n\tds_read_b64_tr_b16 v[198:199], v13 offset:14848\n\tv_mfma_f32_16x16x32_bf16 v[100:103], v[180:183], v[140:143], v[100:103]\n\tv_mfma_f32_16x16x32_bf16 v[112:115], v[180:183], v[144:147], v[112:115]\n\t"
            "s_waitcnt lgkmcnt(13)\n\tv_mfma_f32_16x16x32_bf16 v[44:47], v[120:123], v[184:187], v[44:47]\n\tv_mfma_f32_16x16x32_bf16 v[56:59], v[124:127], v[184:187], v[56:59]\n\tv_mfma_f32_16x16x32_bf16 v[68:71], v[128:131], v[184:187], v[68:71]\n\tv_mfma_f32_16x16x32_bf16 v[80:83], v[132:135], v[184:187], v[80:83]\n\tv_mfma_f32_16x16x32_bf16 v[92:95], v[136:139], v[184:187], v[92:95]\n\t"
            "v_mfma_f32_16x16x32_bf16 v[104:107], v[140:143], v[184:187], v[104:107]\n\tv_mfma_f32_16x16x32_bf16 v[116:119], v[144:147], v[184:187], v[116:119]\n\ts_waitcnt lgkmcnt(10)\n\tv_mfma_f32_16x16x32_bf16 v[36:39], v[188:191], v[148:151], v[36:39]\n\ts_waitcnt lgkmcnt(9)\n\tv_mfma_f32_16x16x32_bf16 v[48:51], v[188:191], v[152:155], v[48:51]\n\t"
            "s_waitcnt lgkmcnt(8)\n\tv_mfma_f32_16x16x32_bf16 v[60:63], v[188:191], v[156:159], v[60:63]\n\ts_waitcnt lgkmcnt(7)\n\tv_mfma_f32_16x16x32_bf16 v[72:75], v[188:191], v[160:163], v[72:75]\n\ts_waitcnt lgkmcnt(6)\n\tv_mfma_f32_16x16x32_bf16 v[84:87], v[188:191], v[164:167], v[84:87]\n\t"
            "s_waitcnt lgkmcnt(5)\n\tv_mfma_f32_16x16x32_bf16 v[96:99], v[188:191], v[168:171], v[96:99]\n\ts_waitcnt lgkmcnt(4)\n\tv_mfma_f32_16x16x32_bf16 v[108:111], v[188:191], v[172:175], v[108:111]\n\ts_waitcnt lgkmcnt(2)\n\tv_mfma_f32_16x16x32_bf16 v[40:43], v[192:195], v[148:151], v[40:43]\n\t"
            "v_mfma_f32_16x16x32_bf16 v[52:55], v[192:195], v[152:155], v[52:55]\n\ts_waitcnt lgkmcnt(0)\n\ts_barrier\n\t"
            ST(7)
            "ds_read_b64_tr_b16 v[176:177], v13 offset:38912\n\tds_read_b64_tr_b16 v[178:179], v13 offset:39424\n\tv_mfma_f32_16x16x32_bf16 v[64:67], v[192:195], v[156:159], v[64:67]\n\tds_read_b128 v[120:123], v10\n\tv_mfma_f32_16x16x32_bf16 v[76:79], v[192:195], v[160:163], v[76:79]\n\tds_read_b128 v[124:127], v4 offset:40960\n\t"
            "v_mfma_f32_16x16x32_bf16 v[88:91], v[192:195], v[164:167], v[88:91]\n\tds_read_b128 v[128:131], v4 offset:43008\n\tv_mfma_f32_16x16x32_bf16 v[100:103], v[192:195], v[168:171], v[100:103]\n\tds_read_b128 v[132:135], v4 offset:45056\n\tv_mfma_f32_16x16x32_bf16 v[112:115], v[192:195], v[172:175], v[112:115]\n\tds_read_b128 v[136:139], v4 offset:47104\n\t"
            "v_mfma_f32_16x16x32_bf16 v[44:47], v[148:151], v[196:199], v[44:47]\n\tds_read_b128 v[140:143], v4 offset:49152\n\tv_mfma_f32_16x16x32_bf16 v[56:59], v[152:155], v[196:199], v[56:59]\n\tds_read_b128 v[144:147], v4 offset:51200\n\tv_mfma_f32_16x16x32_bf16 v[68:71], v[156:159], v[196:199], v[68:71]\n\tds_read_b64_tr_b16 v[180:181], v13 offset:39936\n\t"
            "ds_read_b64_tr_b16 v[182:183], v13 offset:40448\n\tv_mfma_f32_16x16x32_bf16 v[80:83], v[160:163], v[196:199], v[80:83]\n\tds_read_b64_tr_b16 v[184:185], v13 offset:40960\n\tds_read_b64_tr_b16 v[186:187], v13 offset:41472\n\tv_mfma_f32_16x16x32_bf16 v[92:95], v[164:167], v[196:199], v[92:95]\n\tv_mfma_f32_16x16x32_bf16 v[104:107], v[168:171], v[196:199], v[104:107]\n\t"
            "v_mfma_f32_16x16x32_bf16 v[116:119], v[172:175], v[196:199], v[116:119]\n\ts_waitcnt lgkmcnt(10)\n\tv_mfma_f32_16x16x32_bf16 v[36:39], v[176:179], v[120:123], v[36:39]\n\ts_waitcnt lgkmcnt(9)\n\tv_mfma_f32_16x16x32_bf16 v[48:51], v[176:179], v[124:127], v[48:51]\n\ts_waitcnt lgkmcnt(8)\n\t"
            "v_mfma_f32_16x16x32_bf16 v[60:63], v[176:179], v[128:131], v[60:63]\n\tds_read_b64_tr_b16 v[188:189], v13 offset:51200\n\tds_read_b64_tr_b16 v[190:191], v13 offset:51712\n\ts_waitcnt lgkmcnt(9)\n\tv_mfma_f32_16x16x32_bf16 v[72:75], v[176:179], v[132:135], v[72:75]\n\tds_read_b128 v[148:151], v11\n\t"
            "s_waitcnt lgkmcnt(9)\n\tv_mfma_f32_16x16x32_bf16 v[84:87], v[176:179], v[136:139], v[84:87]\n\tds_read_b128 v[152:155], v5 offset:40960\n\ts_waitcnt lgkmcnt(9)\n\tv_mfma_f32_16x16x32_bf16 v[96:99], v[176:179], v[140:143], v[96:99]\n\tds_read_b128 v[156:159], v5 offset:43008\n\t"
            "s_waitcnt lgkmcnt(9)\n\tv_mfma_f32_16x16x32_bf16 v[108:111], v[176:179], v[144:147], v[108:111]\n\tds_read_b128 v[160:163], v5 offset:45056\n\ts_waitcnt lgkmcnt(8)\n\tv_mfma_f32_16x16x32_bf16 v[40:43], v[180:183], v[120:123], v[40:43]\n\tds_read_b128 v[164:167], v5 offset:47104\n\t"
            "v_mfma_f32_16x16x32_bf16 v[52:55], v[180:183], v[124:127], v[52:55]\n\tds_read_b128 v[168:171], v5 offset:49152\n\tv_mfma_f32_16x16x32_bf16 v[64:67], v[180:183], v[128:131], v[64:67]\n\tds_read_b128 v[172:175], v5 offset:51200\n\tv_mfma_f32_16x16x32_bf16 v[76:79], v[180:183], v[132:135], v[76:79]\n\tds_read_b64_tr_b16 v[192:193], v13 offset:52224\n\t"
            "ds_read_b64_tr_b16 v[194:195], v13 offset:52736\n\tv_mfma_f32_16x16x32_bf16 v[88:91], v[180:183], v[136:139], v[88:91]\n\tds_read_b64_tr_b16 v[196:197], v13 offset:53248\n\tds_read_b64_tr_b16 v[198:199], v13 offset:53760\n\tv_mfma_f32_16x16x32_bf16 v[100:103], v[180:183], v[140:143], v[100:103]\n\tv_mfma_f32_16x16x32_bf16 v[112:115], v[180:183], v[144:147], v[112:115]\n\t"
            "s_waitcnt lgkmcnt(13)\n\tv_mfma_f32_16x16x32_bf16 v[44:47], v[120:123], v[184:187], v[44:47]\n\tv_mfma_f32_16x16x32_bf16 v[56:59], v[124:127], v[184:187], v[56:59]\n\tv_mfma_f32_16x16x32_bf16 v[68:71], v[128:131], v[184:187], v[68:71]\n\tv_mfma_f32_16x16x32_bf16 v[80:83], v[132:135], v[184:187], v[80:83]\n\tv_mfma_f32_16x16x32_bf16 v[92:95], v[136:139], v[184:187], v[92:95]\n\t"
            "v_mfma_f32_16x16x32_bf16 v[104:107], v[140:143], v[184:187], v[104:107]\n\tv_mfma_f32_16x16x32_bf16 v[116:119], v[144:147], v[184:187], v[116:119]\n\ts_waitcnt lgkmcnt(10)\n\tv_mfma_f32_16x16x32_bf16 v[36:39], v[188:191], v[148:151], v[36:39]\n\ts_waitcnt lgkmcnt(9)\n\tv_mfma_f32_16x16x32_bf16 v[48:51], v[188:191], v[152:155], v[48:51]\n\t"
            "s_waitcnt lgkmcnt(8)\n\tv_mfma_f32_16x16x32_bf16 v[60:63], v[188:191], v[156:159], v[60:63]\n\ts_waitcnt lgkmcnt(7)\n\tv_mfma_f32_16x16x32_bf16 v[72:75], v[188:191], v[160:163], v[72:75]\n\ts_waitcnt lgkmcnt(6)\n\tv_mfma_f32_16x16x32_bf16 v[84:87], v[188:191], v[164:167], v[84:87]\n\t"
            "s_waitcnt lgkmcnt(5)\n\tv_mfma_f32_16x16x32_bf16 v[96:99], v[188:191], v[168:171], v[96:99]\n\ts_waitcnt lgkmcnt(4)\n\tv_mfma_f32_16x16x32_bf16 v[108:111], v[188:191], v[172:175], v[108:111]\n\ts_waitcnt lgkmcnt(2)\n\tv_mfma_f32_16x16x32_bf16 v[40:43], v[192:195], v[148:151], v[40:43]\n\t"
            "v_mfma_f32_16x16x32_bf16 v[52:55], v[192:195], v[152:155], v[52:55]\n\ts_waitcnt lgkmcnt(0)\n\ts_barrier\n\t"
            ST(9)
            "ds_read_b64_tr_b16 v[176:177], v12 offset:0\n\tds_read_b64_tr_b16 v[178:179], v12 offset:512\n\tv_mfma_f32_16x16x32_bf16 v[64:67], v[192:195], v[156:159], v[64:67]\n\tds_read_b128 v[120:123], v6\n\tv_mfma_f32_16x16x32_bf16 v[76:79], v[192:195], v[160:163], v[76:79]\n\tds_read_b128 v[124:127], v2 offset:2048\n\t"
            "v_mfma_f32_16x16x32_bf16 v[88:91], v[192:195], v[164:167], v[88:91]\n\tds_read_b128 v[128:131], v2 offset:4096\n\tv_mfma_f32_16x16x32_bf16 v[100:103], v[192:195], v[168:171], v[100:103]\n\tds_read_b128 v[132:135], v2 offset:6144\n\tv_mfma_f32_16x16x32_bf16 v[112:115], v[192:195], v[172:175], v[112:115]\n\tds_read_b128 v[136:139], v2 offset:8192\n\t"
            "v_mfma_f32_16x16x32_bf16 v[44:47], v[148:151], v[196:199], v[44:47]\n\tds_read_b128 v[140:143], v2 offset:10240\n\tv_mfma_f32_16x16x32_bf16 v[56:59], v[152:155], v[196:199], v[56:59]\n\tds_read_b128 v[144:147], v2 offset:12288\n\tv_mfma_f32_16x16x32_bf16 v[68:71], v[156:159], v[196:199], v[68:71]\n\tds_read_b64_tr_b16 v[180:181], v12 offset:1024\n\t"
            "ds_read_b64_tr_b16 v[182:183], v12 offset:1536\n\tv_mfma_f32_16x16x32_bf16 v[80:83], v[160:163], v[196:199], v[80:83]\n\tds_read_b64_tr_b16 v[184:185], v12 offset:2048\n\tds_read_b64_tr_b16 v[186:187], v12 offset:2560\n\tv_mfma_f32_16x16x32_bf16 v[92:95], v[164:167], v[196:199], v[92:95]\n\tv_mfma_f32_16x16x32_bf16 v[104:107], v[168:171], v[196:199], v[104:107]\n\t"
            "v_mfma_f32_16x16x32_bf16 v[116:119], v[172:175], v[196:199], v[116:119]\n\ts_waitcnt lgkmcnt(10)\n\tv_mfma_f32_16x16x32_bf16 v[36:39], v[176:179], v[120:123], v[36:39]\n\ts_waitcnt lgkmcnt(9)\n\tv_mfma_f32_16x16x32_bf16 v[48:51], v[176:179], v[124:127], v[48:51]\n\ts_waitcnt lgkmcnt(8)\n\t"
            "v_mfma_f32_16x16x32_bf16 v[60:63], v[176:179], v[128:131], v[60:63]\n\tds_read_b64_tr_b16 v[188:189], v12 offset:12288\n\tds_read_b64_tr_b16 v[190:191], v12 offset:12800\n\ts_waitcnt lgkmcnt(9)\n\tv_mfma_f32_16x16x32_bf16 v[72:75], v[176:179], v[132:135], v[72:75]\n\tds_read_b128 v[148:151], v7\n\t"
            "s_waitcnt lgkmcnt(9)\n\tv_mfma_f32_16x16x32_bf16 v[84:87], v[176:179], v[136:139], v[84:87]\n\tds_read_b128 v[152:155], v3 offset:2048\n\ts_waitcnt lgkmcnt(9)\n\tv_mfma_f32_16x16x32_bf16 v[96:99], v[176:179], v[140:143], v[96:99]\n\tds_read_b128 v[156:159], v3 offset:4096\n\t"
            "s_waitcnt lgkmcnt(9)\n\tv_mfma_f32_16x16x32_bf16 v[108:111], v[176:179], v[144:147], v[108:111]\n\tds_read_b128 v[160:163], v3 offset:6144\n\ts_waitcnt lgkmcnt(8)\n\tv_mfma_f32_16x16x32_bf16 v[40:43], v[180:183], v[120:123], v[40:43]\n\tds_read_b128 v[164:167], v3 offset:8192\n\t"
            "v_mfma_f32_16x16x32_bf16 v[52:55], v[180:183], v[124:127], v[52:55]\n\tds_read_b128 v[168:171], v3 offset:10240\n\tv_mfma_f32_16x16x32_bf16 v[64:67], v[180:183], v[128:131], v[64:67]\n\tds_read_b128 v[172:175], v3 offset:12288\n\tv_mfma_f32_16x16x32_bf16 v[76:79], v[180:183], v[132:135], v[76:79]\n\tds_read_b64_tr_b16 v[192:193], v12 offset:13312\n\t"
            "ds_read_b64_tr_b16 v[194:195], v12 offset:13824\n\tv_mfma_f32_16x16x32_bf16 v[88:91], v[180:183], v[136:139], v[88:91]\n\tds_read_b64_tr_b16 v[196:197], v12 offset:14336\n\tds_read_b64_tr_b16 v[198:199], v12 offset:14848\n\tv_mfma_f32_16x16x32_bf16 v[100:103], v[180:183], v[140:143], v[100:103]\n\tv_mfma_f32_16x16x32_bf16 v[112:115], v[180:183], v[144:147], v[112:115]\n\t"
            "s_waitcnt lgkmcnt(13)\n\tv_mfma_f32_16x16x32_bf16 v[44:47], v[120:123], v[184:187], v[44:47]\n\tv_mfma_f32_16x16x32_bf16 v[56:59], v[124:127], v[184:187], v[56:59]\n\tv_mfma_f32_16x16x32_bf16 v[68:71], v[128:131], v[184:187], v[68:71]\n\tv_mfma_f32_16x16x32_bf16 v[80:83], v[132:135], v[184:187], v[80:83]\n\tv_mfma_f32_16x16x32_bf16 v[92:95], v[136:139], v[184:187], v[92:95]\n\t"
            "v_mfma_f32_16x16x32_bf16 v[104:107], v[140:143], v[184:187], v[104:107]\n\tv_mfma_f32_16x16x32_bf16 v[116:119], v[144:147], v[184:187], v[116:119]\n\ts_waitcnt lgkmcnt(10)\n\tv_mfma_f32_16x16x32_bf16 v[36:39], v[188:191], v[148:151], v[36:39]\n\ts_waitcnt lgkmcnt(9)\n\tv_mfma_f32_16x16x32_bf16 v[48:51], v[188:191], v[152:155], v[48:51]\n\t"
            "s_waitcnt lgkmcnt(8)\n\tv_mfma_f32_16x16x32_bf16 v[60:63], v[188:191], v[156:159], v[60:63]\n\ts_waitcnt lgkmcnt(7)\n\tv_mfma_f32_16x16x32_bf16 v[72:75], v[188:191], v[160:163], v[72:75]\n\ts_waitcnt lgkmcnt(6)\n\tv_mfma_f32_16x16x32_bf16 v[84:87], v[188:191], v[164:167], v[84:87]\n\t"
            "s_waitcnt lgkmcnt(5)\n\tv_mfma_f32_16x16x32_bf16 v[96:99], v[188:191], v[168:171], v[96:99]\n\ts_waitcnt lgkmcnt(4)\n\tv_mfma_f32_16x16x32_bf16 v[108:111], v[188:191], v[172:175], v[108:111]\n\ts_waitcnt lgkmcnt(2)\n\tv_mfma_f32_16x16x32_bf16 v[40:43], v[192:195], v[148:151], v[40:43]\n\t"
            "v_mfma_f32_16x16x32_bf16 v[52:55], v[192:195], v[152:155], v[52:55]\n\ts_waitcnt lgkmcnt(0)\n\ts_barrier\n\t"
            ST(11)
            "ds_read_b64_tr_b16 v[176:177], v13 offset:0\n\tds_read_b64_tr_b16 v[178:179], v13 offset:512\n\tv_mfma_f32_16x16x32_bf16 v[64:67], v[192:195], v[156:159], v[64:67]\n\tds_read_b128 v[120:123], v8\n\tv_mfma_f32_16x16x32_bf16 v[76:79], v[192:195], v[160:163], v[76:79]\n\tds_read_b128 v[124:127], v4 offset:2048\n\t"
            "v_mfma_f32_16x16x32_bf16 v[88:91], v[192:195], v[164:167], v[88:91]\n\tds_read_b128 v[128:131], v4 offset:4096\n\tv_mfma_f32_16x16x32_bf16 v[100:103], v[192:195], v[168:171], v[100:103]\n\tds_read_b128 v[132:135], v4 offset:6144\n\tv_mfma_f32_16x16x32_bf16 v[112:115], v[192:195], v[172:175], v[112:115]\n\tds_read_b128 v[136:139], v4 offset:8192\n\t"
            "v_mfma_f32_16x16x32_bf16 v[44:47], v[148:151], v[196:199], v[44:47]\n\tds_read_b128 v[140:143], v4 offset:10240\n\tv_mfma_f32_16x16x32_bf16 v[56:59], v[152:155], v[196:199], v[56:59]\n\tds_read_b128 v[144:147], v4 offset:12288\n\tv_mfma_f32_16x16x32_bf16 v[68:71], v[156:159], v[196:199], v[68:71]\n\tds_read_b64_tr_b16 v[180:181], v13 offset:1024\n\t"
            "ds_read_b64_tr_b16 v[182:183], v13 offset:1536\n\tv_mfma_f32_16x16x32_bf16 v[80:83], v[160:163], v[196:199], v[80:83]\n\tds_read_b64_tr_b16 v[184:185], v13 offset:2048\n\tds_read_b64_tr_b16 v[186:187], v13 offset:2560\n\tv_mfma_f32_16x16x32_bf16 v[92:95], v[164:167], v[196:199], v[92:95]\n\tv_mfma_f32_16x16x32_bf16 v[104:107], v[168:171], v[196:199], v[104:107]\n\t"
            "v_mfma_f32_16x16x32_bf16 v[116:119], v[172:175], v[196:199], v[116:119]\n\ts_waitcnt lgkmcnt(10)\n\tv_mfma_f32_16x16x32_bf16 v[36:39], v[176:179], v[120:123], v[36:39]\n\ts_waitcnt lgkmcnt(9)\n\tv_mfma_f32_16x16x32_bf16 v[48:51], v[176:179], v[124:127], v[48:51]\n\ts_waitcnt lgkmcnt(8)\n\t"
            "v_mfma_f32_16x16x32_bf16 v[60:63], v[176:179], v[128:131], v[60:63]\n\tds_read_b64_tr_b16 v[188:189], v13 offset:12288\n\tds_read_b64_tr_b16 v[190:191], v13 offset:12800\n\ts_waitcnt lgkmcnt(9)\n\tv_mfma_f32_16x16x32_bf16 v[72:75], v[176:179], v[132:135], v[72:75]\n\tds_read_b128 v[148:151], v9\n\t"
            "s_waitcnt lgkmcnt(9)\n\tv_mfma_f32_16x16x32_bf16 v[84:87], v[176:179], v[136:139], v[84:87]\n\tds_read_b128 v[152:155], v5 offset:2048\n\ts_waitcnt lgkmcnt(9)\n\tv_mfma_f32_16x16x32_bf16 v[96:99], v[176:179], v[140:143], v[96:99]\n\tds_read_b128 v[156:159], v5 offset:4096\n\t"
            "s_waitcnt lgkmcnt(9)\n\tv_mfma_f32_16x16x32_bf16 v[108:111], v[176:179], v[144:147], v[108:111]\n\tds_read_b128 v[160:163], v5 offset:6144\n\ts_waitcnt lgkmcnt(8)\n\tv_mfma_f32_16x16x32_bf16 v[40:43], v[180:183], v[120:123], v[40:43]\n\tds_read_b128 v[164:167], v5 offset:8192\n\t"
            "v_mfma_f32_16x16x32_bf16 v[52:55], v[180:183], v[124:127], v[52:55]\n\tds_read_b128 v[168:171], v5 offset:10240\n\tv_mfma_f32_16x16x32_bf16 v[64:67], v[180:183], v[128:131], v[64:67]\n\tds_read_b128 v[172:175], v5 offset:12288\n\tv_mfma_f32_16x16x32_bf16 v[76:79], v[180:183], v[132:135], v[76:79]\n\tds_read_b64_tr_b16 v[192:193], v13 offset:13312\n\t"
            "ds_read_b64_tr_b16 v[194:195], v13 offset:13824\n\tv_mfma_f32_16x16x32_bf16 v[88:91], v[180:183], v[136:139], v[88:91]\n\tds_read_b64_tr_b16 v[196:197], v13 offset:14336\n\tds_read_b64_tr_b16 v[198:199], v13 offset:14848\n\tv_mfma_f32_16x16x32_bf16 v[100:103], v[180:183], v[140:143], v[100:103]\n\tv_mfma_f32_16x16x32_bf16 v[112:115], v[180:183], v[144:147], v[112:115]\n\t"
            "s_waitcnt lgkmcnt(13)\n\tv_mfma_f32_16x16x32_bf16 v[44:47], v[120:123], v[184:187], v[44:47]\n\tv_mfma_f32_16x16x32_bf16 v[56:59], v[124:127], v[184:187], v[56:59]\n\tv_mfma_f32_16x16x32_bf16 v[68:71], v[128:131], v[184:187], v[68:71]\n\tv_mfma_f32_16x16x32_bf16 v[80:83], v[132:135], v[184:187], v[80:83]\n\tv_mfma_f32_16x16x32_bf16 v[92:95], v[136:139], v[184:187], v[92:95]\n\t"
            "v_mfma_f32_16x16x32_bf16 v[104:107], v[140:143], v[184:187], v[104:107]\n\tv_mfma_f32_16x16x32_bf16 v[116:119], v[144:147], v[184:187], v[116:119]\n\ts_waitcnt lgkmcnt(10)\n\tv_mfma_f32_16x16x32_bf16 v[36:39], v[188:191], v[148:151], v[36:39]\n\ts_waitcnt lgkmcnt(9)\n\tv_mfma_f32_16x16x32_bf16 v[48:51], v[188:191], v[152:155], v[48:51]\n\t"
            "s_waitcnt lgkmcnt(8)\n\tv_mfma_f32_16x16x32_bf16 v[60:63], v[188:191], v[156:159], v[60:63]\n\ts_waitcnt lgkmcnt(7)\n\tv_mfma_f32_16x16x32_bf16 v[72:75], v[188:191], v[160:163], v[72:75]\n\ts_waitcnt lgkmcnt(6)\n\tv_mfma_f32_16x16x32_bf16 v[84:87], v[188:191], v[164:167], v[84:87]\n\t"
            "s_waitcnt lgkmcnt(5)\n\tv_mfma_f32_16x16x32_bf16 v[96:99], v[188:191], v[168:171], v[96:99]\n\ts_waitcnt lgkmcnt(4)\n\tv_mfma_f32_16x16x32_bf16 v[108:111], v[188:191], v[172:175], v[108:111]\n\ts_waitcnt lgkmcnt(2)\n\tv_mfma_f32_16x16x32_bf16 v[40:43], v[192:195], v[148:151], v[40:43]\n\t"
            "v_mfma_f32_16x16x32_bf16 v[52:55], v[192:195], v[152:155], v[52:55]\n\ts_waitcnt lgkmcnt(0)\n\ts_barrier\n\t"
            ST(13)
            "ds_read_b64_tr_b16 v[176:177], v13 offset:38912\n\tds_read_b64_tr_b16 v[178:179], v13 offset:39424\n\tv_mfma_f32_16x16x32_bf16 v[64:67], v[192:195], v[156:159], v[64:67]\n\tds_read_b128 v[120:123], v10\n\tv_mfma_f32_16x16x32_bf16 v[76:79], v[192:195], v[160:163], v[76:79]\n\tds_read_b128 v[124:127], v4 offset:40960\n\t"
            "v_mfma_f32_16x16x32_bf16 v[88:91], v[192:195], v[164:167], v[88:91]\n\tds_read_b128 v[128:131], v4 offset:43008\n\tv_mfma_f32_16x16x32_bf16 v[100:103], v[192:195], v[168:171], v[100:103]\n\tds_read_b128 v[132:135], v4 offset:45056\n\tv_mfma_f32_16x16x32_bf16 v[112:115], v[192:195], v[172:175], v[112:115]\n\tds_read_b128 v[136:139], v4 offset:47104\n\t"
            "v_mfma_f32_16x16x32_bf16 v[44:47], v[148:151], v[196:199], v[44:47]\n\tds_read_b128 v[140:143], v4 offset:49152\n\tv_mfma_f32_16x16x32_bf16 v[56:59], v[152:155], v[196:199], v[56:59]\n\tds_read_b128 v[144:147], v4 offset:51200\n\tv_mfma_f32_16x16x32_bf16 v[68:71], v[156:159], v[196:199], v[68:71]\n\tds_read_b64_tr_b16 v[180:181], v13 offset:39936\n\t"
            "ds_read_b64_tr_b16 v[182:183], v13 offset:40448\n\tv_mfma_f32_16x16x32_bf16 v[80:83], v[160:163], v[196:199], v[80:83]\n\tds_read_b64_tr_b16 v[184:185], v13 offset:40960\n\tds_read_b64_tr_b16 v[186:187], v13 offset:41472\n\tv_mfma_f32_16x16x32_bf16 v[92:95], v[164:167], v[196:199], v[92:95]\n\tv_mfma_f32_16x16x32_bf16 v[104:107], v[168:171], v[196:199], v[104:107]\n\t"
            "v_mfma_f32_16x16x32_bf16 v[116:119], v[172:175], v[196:199], v[116:119]\n\ts_waitcnt lgkmcnt(10)\n\tv_mfma_f32_16x16x32_bf16 v[36:39], v[176:179], v[120:123], v[36:39]\n\ts_waitcnt lgkmcnt(9)\n\tv_mfma_f32_16x16x32_bf16 v[48:51], v[176:179], v[124:127], v[48:51]\n\ts_waitcnt lgkmcnt(8)\n\t"
            "v_mfma_f32_16x16x32_bf16 v[60:63], v[176:179], v[128:131], v[60:63]\n\tds_read_b64_tr_b16 v[188:189], v13 offset:51200\n\tds_read_b64_tr_b16 v[190:191], v13 offset:51712\n\ts_waitcnt lgkmcnt(9)\n\tv_mfma_f32_16x16x32_bf16 v[72:75], v[176:179], v[132:135], v[72:75]\n\tds_read_b128 v[148:151], v11\n\t"
            "s_waitcnt lgkmcnt(9)\n\tv_mfma_f32_16x16x32_bf16 v[84:87], v[176:179], v[136:139], v[84:87]\n\tds_read_b128 v[152:155], v5 offset:40960\n\ts_waitcnt lgkmcnt(9)\n\tv_mfma_f32_16x16x32_bf16 v[96:99], v[176:179], v[140:143], v[96:99]\n\tds_read_b128 v[156:159], v5 offset:43008\n\t"
            "s_waitcnt lgkmcnt(9)\n\tv_mfma_f32_16x16x32_bf16 v[108:111], v[176:179], v[144:147], v[108:111]\n\tds_read_b128 v[160:163], v5 offset:45056\n\ts_waitcnt lgkmcnt(8)\n\tv_mfma_f32_16x16x32_bf16 v[40:43], v[180:183], v[120:123], v[40:43]\n\tds_read_b128 v[164:167], v5 offset:47104\n\t"
            "v_mfma_f32_16x16x32_bf16 v[52:55], v[180:183], v[124:127], v[52:55]\n\tds_read_b128 v[168:171], v5 offset:49152\n\tv_mfma_f32_16x16x32_bf16 v[64:67], v[180:183], v[128:131], v[64:67]\n\tds_read_b128 v[172:175], v5 offset:51200\n\tv_mfma_f32_16x16x32_bf16 v[76:79], v[180:183], v[132:135], v[76:79]\n\tds_read_b64_tr_b16 v[192:193], v13 offset:52224\n\t"
            "ds_read_b64_tr_b16 v[194:195], v13 offset:52736\n\tv_mfma_f32_16x16x32_bf16 v[88:91], v[180:183], v[136:139], v[88:91]\n\tds_read_b64_tr_b16 v[196:197], v13 offset:53248\n\tds_read_b64_tr_b16 v[198:199], v13 offset:53760\n\tv_mfma_f32_16x16x32_bf16 v[100:103], v[180:183], v[140:143], v[100:103]\n\tv_mfma_f32_16x16x32_bf16 v[112:115], v[180:183], v[144:147], v[112:115]\n\t"
            "s_waitcnt lgkmcnt(13)\n\tv_mfma_f32_16x16x32_bf16 v[44:47], v[120:123], v[184:187], v[44:47]\n\tv_mfma_f32_16x16x32_bf16 v[56:59], v[124:127], v[184:187], v[56:59]\n\tv_mfma_f32_16x16x32_bf16 v[68:71], v[128:131], v[184:187], v[68:71]\n\tv_mfma_f32_16x16x32_bf16 v[80:83], v[132:135], v[184:187], v[80:83]\n\tv_mfma_f32_16x16x32_bf16 v[92:95], v[136:139], v[184:187], v[92:95]\n\t"
            "v_mfma_f32_16x16x32_bf16 v[104:107], v[140:143], v[184:187], v[104:107]\n\tv_mfma_f32_16x16x32_bf16 v[116:119], v[144:147], v[184:187], v[116:119]\n\ts_waitcnt lgkmcnt(10)\n\tv_mfma_f32_16x16x32_bf16 v[36:39], v[188:191], v[148:151], v[36:39]\n\ts_waitcnt lgkmcnt(9)\n\tv_mfma_f32_16x16x32_bf16 v[48:51], v[188:191], v[152:155], v[48:51]\n\t"
            "s_waitcnt lgkmcnt(8)\n\tv_mfma_f32_16x16x32_bf16 v[60:63], v[188:191], v[156:159], v[60:63]\n\ts_waitcnt lgkmcnt(7)\n\tv_mfma_f32_16x16x32_bf16 v[72:75], v[188:191], v[160:163], v[72:75]\n\ts_waitcnt lgkmcnt(6)\n\tv_mfma_f32_16x16x32_bf16 v[84:87], v[188:191], v[164:167], v[84:87]\n\t"
            "s_waitcnt lgkmcnt(5)\n\tv_mfma_f32_16x16x32_bf16 v[96:99], v[188:191], v[168:171], v[96:99]\n\ts_waitcnt lgkmcnt(4)\n\tv_mfma_f32_16x16x32_bf16 v[108:111], v[188:191], v[172:175], v[108:111]\n\ts_waitcnt lgkmcnt(2)\n\tv_mfma_f32_16x16x32_bf16 v[40:43], v[192:195], v[148:151], v[40:43]\n\t"
            "v_mfma_f32_16x16x32_bf16 v[52:55], v[192:195], v[152:155], v[52:55]\n\ts_waitcnt lgkmcnt(0)\n\ts_barrier\n\t"
            ST(15)
            "ds_read_b64_tr_b16 v[176:177], v12 offset:0\n\tds_read_b64_tr_b16 v[178:179], v12 offset:512\n\tv_mfma_f32_16x16x32_bf16 v[64:67], v[192:195], v[156:159], v[64:67]\n\tds_read_b128 v[120:123], v6\n\tv_mfma_f32_16x16x32_bf16 v[76:79], v[192:195], v[160:163], v[76:79]\n\tds_read_b128 v[124:127], v2 offset:2048\n\t"
            "v_mfma_f32_16x16x32_bf16 v[88:91], v[192:195], v[164:167], v[88:91]\n\tds_read_b128 v[128:131], v2 offset:4096\n\tv_mfma_f32_16x16x32_bf16 v[100:103], v[192:195], v[168:171], v[100:103]\n\tds_read_b128 v[132:135], v2 offset:6144\n\tv_mfma_f32_16x16x32_bf16 v[112:115], v[192:195], v[172:175], v[112:115]\n\tds_read_b128 v[136:139], v2 offset:8192\n\t"
            "v_mfma_f32_16x16x32_bf16 v[44:47], v[148:151], v[196:199], v[44:47]\n\tds_read_b128 v[140:143], v2 offset:10240\n\tv_mfma_f32_16x16x32_bf16 v[56:59], v[152:155], v[196:199], v[56:59]\n\tds_read_b128 v[144:147], v2 offset:12288\n\tv_mfma_f32_16x16x32_bf16 v[68:71], v[156:159], v[196:199], v[68:71]\n\tds_read_b64_tr_b16 v[180:181], v12 offset:1024\n\t"
            "ds_read_b64_tr_b16 v[182:183], v12 offset:1536\n\tv_mfma_f32_16x16x32_bf16 v[80:83], v[160:163], v[196:199], v[80:83]\n\tv_mfma_f32_16x16x32_bf16 v[92:95], v[164:167], v[196:199], v[92:95]\n\tv_mfma_f32_16x16x32_bf16 v[104:107], v[168:171], v[196:199], v[104:107]\n\tv_mfma_f32_16x16x32_bf16 v[116:119], v[172:175], v[196:199], v[116:119]\n\ts_waitcnt lgkmcnt(8)\n\t"
            "v_mfma_f32_16x16x32_bf16 v[36:39], v[176:179], v[120:123], v[36:39]\n\ts_waitcnt lgkmcnt(7)\n\tv_mfma_f32_16x16x32_bf16 v[48:51], v[176:179], v[124:127], v[48:51]\n\ts_waitcnt lgkmcnt(6)\n\tv_mfma_f32_16x16x32_bf16 v[60:63], v[176:179], v[128:131], v[60:63]\n\tds_read_b64_tr_b16 v[188:189], v12 offset:12288\n\t"
            "ds_read_b64_tr_b16 v[190:191], v12 offset:12800\n\ts_waitcnt lgkmcnt(7)\n\tv_mfma_f32_16x16x32_bf16 v[72:75], v[176:179], v[132:135], v[72:75]\n\tds_read_b128 v[148:151], v7\n\ts_waitcnt lgkmcnt(7)\n\tv_mfma_f32_16x16x32_bf16 v[84:87], v[176:179], v[136:139], v[84:87]\n\t"
            "ds_read_b128 v[152:155], v3 offset:2048\n\ts_waitcnt lgkmcnt(7)\n\tv_mfma_f32_16x16x32_bf16 v[96:99], v[176:179], v[140:143], v[96:99]\n\tds_read_b128 v[156:159], v3 offset:4096\n\ts_waitcnt lgkmcnt(7)\n\tv_mfma_f32_16x16x32_bf16 v[108:111], v[176:179], v[144:147], v[108:111]\n\t"
            "ds_read_b128 v[160:163], v3 offset:6144\n\ts_waitcnt lgkmcnt(6)\n\tv_mfma_f32_16x16x32_bf16 v[40:43], v[180:183], v[120:123], v[40:43]\n\tds_read_b128 v[164:167], v3 offset:8192\n\tv_mfma_f32_16x16x32_bf16 v[52:55], v[180:183], v[124:127], v[52:55]\n\tds_read_b128 v[168:171], v3 offset:10240\n\t"
            "v_mfma_f32_16x16x32_bf16 v[64:67], v[180:183], v[128:131], v[64:67]\n\tds_read_b128 v[172:175], v3 offset:12288\n\tv_mfma_f32_16x16x32_bf16 v[76:79], v[180:183], v[132:135], v[76:79]\n\tds_read_b64_tr_b16 v[192:193], v12 offset:13312\n\tds_read_b64_tr_b16 v[194:195], v12 offset:13824\n\tv_mfma_f32_16x16x32_bf16 v[88:91], v[180:183], v[136:139], v[88:91]\n\t"
            "v_mfma_f32_16x16x32_bf16 v[100:103], v[180:183], v[140:143], v[100:103]\n\tv_mfma_f32_16x16x32_bf16 v[112:115], v[180:183], v[144:147], v[112:115]\n\ts_waitcnt lgkmcnt(8)\n\tv_mfma_f32_16x16x32_bf16 v[36:39], v[188:191], v[148:151], v[36:39]\n\ts_waitcnt lgkmcnt(7)\n\tv_mfma_f32_16x16x32_bf16 v[48:51], v[188:191], v[152:155], v[48:51]\n\t"
            "s_waitcnt lgkmcnt(6)\n\tv_mfma_f32_16x16x32_bf16 v[60:63], v[188:191], v[156:159], v[60:63]\n\ts_waitcnt lgkmcnt(5)\n\tv_mfma_f32_16x16x32_bf16 v[72:75], v[188:191], v[160:163], v[72:75]\n\ts_waitcnt lgkmcnt(0)\n\ts_barrier\n\t"
            ST(17)
            "ds_read_b64_tr_b16 v[176:177], v13 offset:0\n\tds_read_b64_tr_b16 v[178:179], v13 offset:512\n\tv_mfma_f32_16x16x32_bf16 v[84:87], v[188:191], v[164:167], v[84:87]\n\tds_read_b128 v[120:123], v8\n\tv_mfma_f32_16x16x32_bf16 v[96:99], v[188:191], v[168:171], v[96:99]\n\tds_read_b128 v[124:127], v4 offset:2048\n\t"
            "v_mfma_f32_16x16x32_bf16 v[108:111], v[188:191], v[172:175], v[108:111]\n\tds_read_b128 v[128:131], v4 offset:4096\n\tv_mfma_f32_16x16x32_bf16 v[40:43], v[192:195], v[148:151], v[40:43]\n\tds_read_b128 v[132:135], v4 offset:6144\n\tv_mfma_f32_16x16x32_bf16 v[52:55], v[192:195], v[152:155], v[52:55]\n\tds_read_b128 v[136:139], v4 offset:8192\n\t"
            "v_mfma_f32_16x16x32_bf16 v[64:67], v[192:195], v[156:159], v[64:67]\n\tds_read_b128 v[140:143], v4 offset:10240\n\tv_mfma_f32_16x16x32_bf16 v[76:79], v[192:195], v[160:163], v[76:79]\n\tds_read_b128 v[144:147], v4 offset:12288\n\tv_mfma_f32_16x16x32_bf16 v[88:91], v[192:195], v[164:167], v[88:91]\n\tds_read_b64_tr_b16 v[180:181], v13 offset:1024\n\t"
            "ds_read_b64_tr_b16 v[182:183], v13 offset:1536\n\tv_mfma_f32_16x16x32_bf16 v[100:103], v[192:195], v[168:171], v[100:103]\n\tds_read_b64_tr_b16 v[184:185], v13 offset:2048\n\tds_read_b64_tr_b16 v[186:187], v13 offset:2560\n\tv_mfma_f32_16x16x32_bf16 v[112:115], v[192:195], v[172:175], v[112:115]\n\tds_read2_b32 v[208:209], v14 offset1:16\n\t"
            SA(2) PRIA
            "ds_read_b64_tr_b16 v[188:189], v13 offset:12288\n\tds_read_b64_tr_b16 v[190:191], v13 offset:12800\n\tds_read_b128 v[148:151], v9\n\tds_read_b128 v[152:155], v5 offset:2048\n\tds_read_b128 v[156:159], v5 offset:4096\n\tds_read_b128 v[160:163], v5 offset:6144\n\t"
            "ds_read_b128 v[164:167], v5 offset:8192\n\tds_read_b128 v[168:171], v5 offset:10240\n\tds_read_b128 v[172:175], v5 offset:12288\n\tds_read_b64_tr_b16 v[192:193], v13 offset:13312\n\tds_read_b64_tr_b16 v[194:195], v13 offset:13824\n\tds_read_b64_tr_b16 v[196:197], v13 offset:14336\n\t"
            "ds_read_b64_tr_b16 v[198:199], v13 offset:14848\n\tv_mov_b32 v252, -1\n\tv_mov_b32 v254, -1\n\ts_waitcnt lgkmcnt(15)\n\tv_mfma_f32_16x16x32_bf16 v[40:43], v[180:183], v[120:123], v[40:43]\n\tds_read2_b32 v[210:211], v14 offset0:32 offset1:48\n\t"
            "ds_read2_b32 v[212:213], v14 offset0:64 offset1:80\n\tv_mfma_f32_16x16x32_bf16 v[36:39], v[176:179], v[120:123], v[36:39]\n\tds_read_b32 v214, v14 offset:384\n\tv_mfma_f32_16x16x32_bf16 v[52:55], v[180:183], v[124:127], v[52:55]\n\tv_mfma_f32_16x16x32_bf16 v[48:51], v[176:179], v[124:127], v[48:51]\n\ts_waitcnt lgkmcnt(13)\n\t"
            "v_mfma_f32_16x16x32_bf16 v[36:39], v[188:191], v[148:151], v[36:39]\n\ts_waitcnt lgkmcnt(12)\n\tv_mfma_f32_16x16x32_bf16 v[48:51], v[188:191], v[152:155], v[48:51]\n\ts_waitcnt lgkmcnt(5)\n\tv_mfma_f32_16x16x32_bf16 v[40:43], v[192:195], v[148:151], v[40:43]\n\ts_nop 3\n\t"
            "v_mov_b32_dpp v200, v36 row_newbcast:0 row_mask:0xf bank_mask:0xf bound_ctrl:1\n\tv_mov_b32_dpp v201, v37 row_newbcast:0 row_mask:0xf bank_mask:0xf bound_ctrl:1\n\tv_mfma_f32_16x16x32_bf16 v[52:55], v[192:195], v[152:155], v[52:55]\n\tv_mov_b32_dpp v202, v38 row_newbcast:0 row_mask:0xf bank_mask:0xf bound_ctrl:1\n\tv_mov_b32_dpp v203, v39 row_newbcast:0 row_mask:0xf bank_mask:0xf bound_ctrl:1\n\tv_mfma_f32_16x16x32_bf16 v[64:67], v[180:183], v[128:131], v[64:67]\n\t"
            "v_mov_b32_dpp v204, v40 row_newbcast:0 row_mask:0xf bank_mask:0xf bound_ctrl:1\n\tv_mov_b32_dpp v205, v41 row_newbcast:0 row_mask:0xf bank_mask:0xf bound_ctrl:1\n\tv_mfma_f32_16x16x32_bf16 v[60:63], v[176:179], v[128:131], v[60:63]\n\tv_mov_b32_dpp v206, v42 row_newbcast:0 row_mask:0xf bank_mask:0xf bound_ctrl:1\n\tv_mov_b32_dpp v207, v43 row_newbcast:0 row_mask:0xf bank_mask:0xf bound_ctrl:1\n\tv_mfma_f32_16x16x32_bf16 v[64:67], v[192:195], v[156:159], v[64:67]\n\t"
            "v_fma_f32 v40, v208, v204, v40\n\tv_fma_f32 v41, v208, v205, v41\n\tv_mfma_f32_16x16x32_bf16 v[60:63], v[188:191], v[156:159], v[60:63]\n\tv_fma_f32 v42, v208, v206, v42\n\tv_fma_f32 v43, v208, v207, v43\n\tv_mfma_f32_16x16x32_bf16 v[76:79], v[180:183], v[132:135], v[76:79]\n\t"
            "v_fma_f32 v36, v208, v200, v36\n\tv_fma_f32 v37, v208, v201, v37\n\tv_mfma_f32_16x16x32_bf16 v[72:75], v[176:179], v[132:135], v[72:75]\n\tv_fma_f32 v38, v208, v202, v38\n\tv_fma_f32 v39, v208, v203, v39\n\tv_mfma_f32_16x16x32_bf16 v[76:79], v[192:195], v[160:163], v[76:79]\n\t"
            "v_cvt_pk_bf16_f32 v40, v40, v41\n\tv_cvt_pk_bf16_f32 v41, v42, v43\n\tv_mfma_f32_16x16x32_bf16 v[72:75], v[188:191], v[160:163], v[72:75]\n\tv_cvt_pk_bf16_f32 v250, v36, v37\n\tv_cvt_pk_bf16_f32 v251, v38, v39\n\tv_mfma_f32_16x16x32_bf16 v[88:91], v[180:183], v[136:139], v[88:91]\n\t"
            "v_cndmask_b32 v42, v40, v40, s[18:19]\n\tv_cndmask_b32 v43, v41, v41, s[18:19]\n\tv_mfma_f32_16x16x32_bf16 v[84:87], v[176:179], v[136:139], v[84:87]\n\tv_and_b32 v36, v250, v18\n\tv_and_b32 v37, v251, v18\n\tv_mfma_f32_16x16x32_bf16 v[88:91], v[192:195], v[164:167], v[88:91]\n\t"
            "v_and_b32 v38, v250, v19\n\tv_and_b32 v39, v251, v19\n\tv_mfma_f32_16x16x32_bf16 v[84:87], v[188:191], v[164:167], v[84:87]\n\tv_and_b32 v222, v250, v20\n\tv_and_b32 v223, v251, v20\n\tv_mfma_f32_16x16x32_bf16 v[218:221], v[40:43], v[36:39], 0\n\t"
            "v_and_b32 v224, v250, v21\n\tv_and_b32 v225, v251, v21\n\tv_mfma_f32_16x16x32_bf16 v[100:103], v[180:183], v[140:143], v[100:103]\n\tv_fma_f32 v52, v209, v204, v52\n\tv_fma_f32 v53, v209, v205, v53\n\tv_mfma_f32_16x16x32_bf16 v[226:229], v[40:43], v[222:225], 0\n\t"
            "s_nop 1\n\tv_cndmask_b32 v218, v220, v218, s[14:15]\n\tv_cndmask_b32 v219, v221, v219, s[16:17]\n\tv_mfma_f32_16x16x32_bf16 v[96:99], v[176:179], v[140:143], v[96:99]\n\tds_write_b64 v28, v[218:219] offset:0\n\tv_fma_f32 v54, v209, v206, v54\n\t"
            "v_mfma_f32_16x16x32_bf16 v[100:103], v[192:195], v[168:171], v[100:103]\n\tv_cndmask_b32 v226, v228, v226, s[14:15]\n\tv_cndmask_b32 v227, v229, v227, s[16:17]\n\tv_mfma_f32_16x16x32_bf16 v[96:99], v[188:191], v[168:171], v[96:99]\n\tds_write_b64 v29, v[226:227] offset:0\n\tv_fma_f32 v55, v209, v207, v55\n\t"
            "v_mfma_f32_16x16x32_bf16 v[112:115], v[180:183], v[144:147], v[112:115]\n\tv_fma_f32 v48, v209, v200, v48\n\tv_fma_f32 v49, v209, v201, v49\n\tv_mfma_f32_16x16x32_bf16 v[108:111], v[176:179], v[144:147], v[108:111]\n\tv_fma_f32 v50, v209, v202, v50\n\tv_fma_f32 v51, v209, v203, v51\n\t"
            "v_mfma_f32_16x16x32_bf16 v[112:115], v[192:195], v[172:175], v[112:115]\n\tv_cvt_pk_bf16_f32 v52, v52, v53\n\tv_cvt_pk_bf16_f32 v53, v54, v55\n\tv_mfma_f32_16x16x32_bf16 v[108:111], v[188:191], v[172:175], v[108:111]\n\tv_cvt_pk_bf16_f32 v250, v48, v49\n\tv_cvt_pk_bf16_f32 v251, v50, v51\n\t"
            "v_cndmask_b32 v54, v40, v52, s[18:19]\n\tv_cndmask_b32 v55, v41, v53, s[18:19]\n\tv_and_b32 v48, v250, v18\n\tv_and_b32 v49, v251, v18\n\tv_and_b32 v50, v250, v19\n\tv_and_b32 v51, v251, v19\n\t"
            "v_and_b32 v234, v250, v20\n\tv_and_b32 v235, v251, v20\n\tv_mfma_f32_16x16x32_bf16 v[230:233], v[52:55], v[48:51], 0\n\tv_and_b32 v236, v250, v21\n\tv_and_b32 v237, v251, v21\n\tv_cvt_pk_bf16_f32 v253, v218, v219\n\t"
            "v_min_u32 v253, -2, v253\n\tv_mfma_f32_16x16x32_bf16 v[238:241], v[52:55], v[234:237], 0\n\ts_waitcnt lgkmcnt(4)\n\tv_fma_f32 v64, v210, v204, v64\n\ts_nop 0\n\tv_cndmask_b32 v230, v232, v230, s[14:15]\n\t"
            "v_cndmask_b32 v231, v233, v231, s[16:17]\n\tv_cvt_pk_bf16_f32 v255, v230, v231\n\tv_min_u32 v255, -2, v255\n\tbuffer_store_dwordx4 v[252:255], v17, s[44:47], s20 offen sc1\n\tds_write_b64 v28, v[230:231] offset:512\n\tv_cndmask_b32 v238, v240, v238, s[14:15]\n\t"
            "v_cndmask_b32 v239, v241, v239, s[16:17]\n\tds_write_b64 v29, v[238:239] offset:512\n\tv_fma_f32 v65, v210, v205, v65\n\tv_fma_f32 v66, v210, v206, v66\n\tv_fma_f32 v67, v210, v207, v67\n\tv_fma_f32 v60, v210, v200, v60\n\t"
            "v_fma_f32 v61, v210, v201, v61\n\tv_fma_f32 v62, v210, v202, v62\n\tv_fma_f32 v63, v210, v203, v63\n\tv_cvt_pk_bf16_f32 v64, v64, v65\n\tv_cvt_pk_bf16_f32 v65, v66, v67\n\tv_cvt_pk_bf16_f32 v250, v60, v61\n\t"
            "v_cvt_pk_bf16_f32 v251, v62, v63\n\tv_cndmask_b32 v66, v52, v64, s[18:19]\n\tv_cndmask_b32 v67, v53, v65, s[18:19]\n\tv_and_b32 v60, v250, v18\n\tv_and_b32 v61, v251, v18\n\tv_and_b32 v62, v250, v19\n\t"
            "v_and_b32 v63, v251, v19\n\tv_and_b32 v246, v250, v20\n\tv_and_b32 v247, v251, v20\n\tv_mfma_f32_16x16x32_bf16 v[242:245], v[64:67], v[60:63], 0\n\tv_and_b32 v248, v250, v21\n\tv_and_b32 v249, v251, v21\n\t"
            "v_mfma_f32_16x16x32_bf16 v[44:47], v[120:123], v[184:187], v[44:47]\n\tv_fma_f32 v76, v211, v204, v76\n\tv_fma_f32 v77, v211, v205, v77\n\tv_mfma_f32_16x16x32_bf16 v[218:221], v[64:67], v[246:249], 0\n\ts_nop 1\n\tv_cndmask_b32 v242, v244, v242, s[14:15]\n\t"
            "v_cndmask_b32 v243, v245, v243, s[16:17]\n\tds_write_b64 v28, v[242:243] offset:1024\n\tv_mfma_f32_16x16x32_bf16 v[56:59], v[124:127], v[184:187], v[56:59]\n\tv_fma_f32 v78, v211, v206, v78\n\tv_fma_f32 v79, v211, v207, v79\n\tv_cndmask_b32 v218, v220, v218, s[14:15]\n\t"
            "v_cndmask_b32 v219, v221, v219, s[16:17]\n\tds_write_b64 v29, v[218:219] offset:1024\n\tv_mfma_f32_16x16x32_bf16 v[68:71], v[128:131], v[184:187], v[68:71]\n\tv_fma_f32 v72, v211, v200, v72\n\tv_fma_f32 v73, v211, v201, v73\n\tv_mfma_f32_16x16x32_bf16 v[80:83], v[132:135], v[184:187], v[80:83]\n\t"
            "v_fma_f32 v74, v211, v202, v74\n\tv_fma_f32 v75, v211, v203, v75\n\tv_mfma_f32_16x16x32_bf16 v[92:95], v[136:139], v[184:187], v[92:95]\n\tv_cvt_pk_bf16_f32 v76, v76, v77\n\tv_cvt_pk_bf16_f32 v77, v78, v79\n\tv_mfma_f32_16x16x32_bf16 v[104:107], v[140:143], v[184:187], v[104:107]\n\t"
            "v_cvt_pk_bf16_f32 v250, v72, v73\n\tv_cvt_pk_bf16_f32 v251, v74, v75\n\tv_mfma_f32_16x16x32_bf16 v[116:119], v[144:147], v[184:187], v[116:119]\n\tv_cndmask_b32 v78, v64, v76, s[18:19]\n\tv_cndmask_b32 v79, v65, v77, s[18:19]\n\tv_mfma_f32_16x16x32_bf16 v[44:47], v[148:151], v[196:199], v[44:47]\n\t"
            "v_and_b32 v72, v250, v18\n\tv_and_b32 v73, v251, v18\n\tv_mfma_f32_16x16x32_bf16 v[56:59], v[152:155], v[196:199], v[56:59]\n\tv_and_b32 v74, v250, v19\n\tv_and_b32 v75, v251, v19\n\tv_mfma_f32_16x16x32_bf16 v[68:71], v[156:159], v[196:199], v[68:71]\n\t"
            "v_and_b32 v226, v250, v20\n\tv_and_b32 v227, v251, v20\n\tv_mfma_f32_16x16x32_bf16 v[222:225], v[76:79], v[72:75], 0\n\tv_and_b32 v228, v250, v21\n\tv_and_b32 v229, v251, v21\n\tv_mfma_f32_16x16x32_bf16 v[80:83], v[160:163], v[196:199], v[80:83]\n\t"
            "v_cvt_pk_bf16_f32 v253, v242, v243\n\tv_min_u32 v253, -2, v253\n\tv_mfma_f32_16x16x32_bf16 v[230:233], v[76:79], v[226:229], 0\n\ts_nop 1\n\tv_cndmask_b32 v222, v224, v222, s[14:15]\n\tv_cndmask_b32 v223, v225, v223, s[16:17]\n\t"
            "v_mfma_f32_16x16x32_bf16 v[92:95], v[164:167], v[196:199], v[92:95]\n\tds_write_b64 v28, v[222:223] offset:1536\n\tv_cvt_pk_bf16_f32 v255, v222, v223\n\tv_mfma_f32_16x16x32_bf16 v[104:107], v[168:171], v[196:199], v[104:107]\n\tv_cndmask_b32 v230, v232, v230, s[14:15]\n\tv_cndmask_b32 v231, v233, v231, s[16:17]\n\t"
            "v_mfma_f32_16x16x32_bf16 v[116:119], v[172:175], v[196:199], v[116:119]\n\tds_write_b64 v29, v[230:231] offset:1536\n\tv_min_u32 v255, -2, v255\n\tbuffer_store_dwordx4 v[252:255], v17, s[44:47], s21 offen sc1\n\ts_waitcnt lgkmcnt(9)\n\tv_fma_f32 v88, v212, v204, v88\n\t"
            "v_fma_f32 v89, v212, v205, v89\n\tv_fma_f32 v90, v212, v206, v90\n\tv_fma_f32 v91, v212, v207, v91\n\tv_fma_f32 v84, v212, v200, v84\n\tv_fma_f32 v85, v212, v201, v85\n\tv_fma_f32 v86, v212, v202, v86\n\t"
            "v_fma_f32 v87, v212, v203, v87\n\tv_cvt_pk_bf16_f32 v88, v88, v89\n\ts_waitcnt lgkmcnt(0)\n\ts_barrier\n\tv_cvt_pk_bf16_f32 v89, v90, v91\n\tv_cvt_pk_bf16_f32 v250, v84, v85\n\t"
            "v_cvt_pk_bf16_f32 v251, v86, v87\n\tv_cndmask_b32 v90, v76, v88, s[18:19]\n\tv_cndmask_b32 v91, v77, v89, s[18:19]\n\tv_and_b32 v84, v250, v18\n\tv_and_b32 v85, v251, v18\n\tv_and_b32 v86, v250, v19\n\t"
            "v_and_b32 v87, v251, v19\n\tv_and_b32 v238, v250, v20\n\tv_and_b32 v239, v251, v20\n\tv_mfma_f32_16x16x32_bf16 v[234:237], v[88:91], v[84:87], 0\n\tv_and_b32 v240, v250, v21\n\tv_and_b32 v241, v251, v21\n\t"
            "v_fma_f32 v100, v213, v204, v100\n\tv_fma_f32 v101, v213, v205, v101\n\tv_mfma_f32_16x16x32_bf16 v[242:245], v[88:91], v[238:241], 0\n\tv_fma_f32 v102, v213, v206, v102\n\ts_nop 1\n\tv_cndmask_b32 v234, v236, v234, s[14:15]\n\t"
            "v_cndmask_b32 v235, v237, v235, s[16:17]\n\tds_write_b64 v28, v[234:235] offset:38912\n\tv_fma_f32 v103, v213, v207, v103\n\tv_fma_f32 v96, v213, v200, v96\n\tv_fma_f32 v97, v213, v201, v97\n\tv_cndmask_b32 v242, v244, v242, s[14:15]\n\t"
            "v_cndmask_b32 v243, v245, v243, s[16:17]\n\tds_write_b64 v29, v[242:243] offset:38912\n\tv_fma_f32 v98, v213, v202, v98\n\tv_fma_f32 v99, v213, v203, v99\n\tv_cvt_pk_bf16_f32 v100, v100, v101\n\tv_cvt_pk_bf16_f32 v101, v102, v103\n\t"
            "v_cvt_pk_bf16_f32 v250, v96, v97\n\tv_cvt_pk_bf16_f32 v251, v98, v99\n\tv_cndmask_b32 v102, v88, v100, s[18:19]\n\tv_cndmask_b32 v103, v89, v101, s[18:19]\n\tv_and_b32 v96, v250, v18\n\tv_and_b32 v97, v251, v18\n\t"
            "v_and_b32 v98, v250, v19\n\tv_and_b32 v99, v251, v19\n\tv_and_b32 v218, v250, v20\n\tv_and_b32 v219, v251, v20\n\tv_mfma_f32_16x16x32_bf16 v[246:249], v[100:103], v[96:99], 0\n\tv_and_b32 v220, v250, v21\n\t"
            "v_and_b32 v221, v251, v21\n\tv_cvt_pk_bf16_f32 v253, v234, v235\n\tv_min_u32 v253, -2, v253\n\tv_mfma_f32_16x16x32_bf16 v[222:225], v[100:103], v[218:221], 0\n\tv_fma_f32 v112, v214, v204, v112\n\ts_nop 1\n\t"
            "v_cndmask_b32 v246, v248, v246, s[14:15]\n\tv_cndmask_b32 v247, v249, v247, s[16:17]\n\tv_cvt_pk_bf16_f32 v255, v246, v247\n\tv_min_u32 v255, -2, v255\n\tbuffer_store_dwordx4 v[252:255], v17, s[44:47], s22 offen sc1\n\tds_write_b64 v28, v[246:247] offset:39424\n\t"
            "v_cndmask_b32 v222, v224, v222, s[14:15]\n\tv_cndmask_b32 v223, v225, v223, s[16:17]\n\tds_write_b64 v29, v[222:223] offset:39424\n\tv_fma_f32 v113, v214, v205, v113\n\tv_fma_f32 v114, v214, v206, v114\n\tv_fma_f32 v115, v214, v207, v115\n\t"
            "v_fma_f32 v108, v214, v200, v108\n\tv_fma_f32 v109, v214, v201, v109\n\tv_fma_f32 v110, v214, v202, v110\n\tv_fma_f32 v111, v214, v203, v111\n\tv_cvt_pk_bf16_f32 v112, v112, v113\n\tv_cvt_pk_bf16_f32 v113, v114, v115\n\t"
            "v_cvt_pk_bf16_f32 v250, v108, v109\n\tv_cvt_pk_bf16_f32 v251, v110, v111\n\tv_cndmask_b32 v114, v100, v112, s[18:19]\n\tv_cndmask_b32 v115, v101, v113, s[18:19]\n\tv_and_b32 v108, v250, v18\n\tv_and_b32 v109, v251, v18\n\t"
            "v_and_b32 v110, v250, v19\n\tv_and_b32 v111, v251, v19\n\tv_and_b32 v230, v250, v20\n\tv_and_b32 v231, v251, v20\n\tv_mfma_f32_16x16x32_bf16 v[226:229], v[112:115], v[108:111], 0\n\tv_and_b32 v232, v250, v21\n\t"
            "v_and_b32 v233, v251, v21\n\tds_read_b64_tr_b16 v[184:185], v12 offset:2048\n\tds_read_b64_tr_b16 v[186:187], v12 offset:2560\n\tv_mfma_f32_16x16x32_bf16 v[234:237], v[112:115], v[230:233], 0\n\tds_read_b128 v[120:123], v6\n\ts_nop 1\n\t"
            "v_cndmask_b32 v226, v228, v226, s[14:15]\n\tv_cndmask_b32 v227, v229, v227, s[16:17]\n\tv_cvt_pk_bf16_f32 v253, v226, v227\n\tv_min_u32 v253, -2, v253\n\tds_write_b64 v28, v[226:227] offset:39936\n\tbuffer_store_dwordx4 v[252:255], v17, s[44:47], s23 offen sc1\n\t"
            "v_cndmask_b32 v234, v236, v234, s[14:15]\n\tv_cndmask_b32 v235, v237, v235, s[16:17]\n\tds_write_b64 v29, v[234:235] offset:39936\n\tds_write_b32 v33, v33\n\t"
            SA(3)
            "ds_read_b64_tr_b16 v[196:197], v12 offset:14336\n\tds_read_b64_tr_b16 v[198:199], v12 offset:14848\n\tds_read_b128 v[148:151], v7\n\tds_read_b128 v[124:127], v2 offset:2048\n\tds_read_b128 v[128:131], v2 offset:4096\n\tds_read_b128 v[152:155], v3 offset:2048\n\t"
            "ds_read_b128 v[132:135], v2 offset:6144\n\tds_read_b128 v[156:159], v3 offset:4096\n\tds_read_b128 v[36:39], v15 offset:0\n\tds_read_b128 v[160:163], v3 offset:6144\n\tds_read_b128 v[48:51], v15 offset:64\n\tds_read_b128 v[60:63], v15 offset:128\n\t"
            "ds_read_b128 v[72:75], v15 offset:192\n\tds_read_b128 v[136:139], v2 offset:8192\n\tds_read_b128 v[140:143], v2 offset:10240\n\tds_read_b128 v[144:147], v2 offset:12288\n\tds_read_b128 v[164:167], v3 offset:8192\n\ts_waitcnt lgkmcnt(15)\n\t"
            "v_mfma_f32_16x16x32_bf16 v[44:47], v[120:123], v[184:187], v[44:47]\n\tds_read_b128 v[168:171], v3 offset:10240\n\tds_read_b128 v[172:175], v3 offset:12288\n\tds_read_b128 v[84:87], v15 offset:256\n\tds_read_b128 v[96:99], v15 offset:320\n\tds_read_b128 v[108:111], v15 offset:384\n\t"
            "v_mov_b32 v218, 0\n\tv_mov_b32 v219, 0\n\tv_mov_b32 v234, 0\n\tv_mov_b32 v235, 0\n\tv_mov_b32 v176, 0\n\tv_mov_b32 v180, 0\n\t"
            "s_waitcnt lgkmcnt(15)\n\tv_mfma_f32_16x16x32_bf16 v[44:47], v[148:151], v[196:199], v[44:47]\n\tv_mov_b32 v188, 0\n\tv_mov_b32 v192, 0\n\tv_mfma_f32_16x16x32_bf16 v[56:59], v[124:127], v[184:187], v[56:59]\n\tv_mfma_f32_16x16x32_bf16 v[68:71], v[128:131], v[184:187], v[68:71]\n\t"
            "s_nop 3\n\tv_mov_b32 v216, v44\n\tv_mov_b32 v217, v44\n\tv_mfma_f32_16x16x32_bf16 v[56:59], v[152:155], v[196:199], v[56:59]\n\ts_nop 0\n\tv_permlane32_swap_b32 v216, v217\n\t"
            "v_mov_b32 v217, v216\n\tv_mfma_f32_16x16x32_bf16 v[80:83], v[132:135], v[184:187], v[80:83]\n\ts_nop 0\n\tv_permlane16_swap_b32 v216, v217\n\ts_waitcnt lgkmcnt(13)\n\tv_fma_f32 v44, v36, v216, v44\n\t"
            "v_mfma_f32_16x16x32_bf16 v[68:71], v[156:159], v[196:199], v[68:71]\n\tv_fma_f32 v45, v37, v216, v45\n\tv_fma_f32 v46, v38, v216, v46\n\ts_waitcnt lgkmcnt(12)\n\tv_mfma_f32_16x16x32_bf16 v[80:83], v[160:163], v[196:199], v[80:83]\n\tv_fma_f32 v47, v39, v216, v47\n\t"
            "v_cvt_pk_bf16_f32 v44, v44, v45\n\ts_waitcnt lgkmcnt(8)\n\tv_mfma_f32_16x16x32_bf16 v[92:95], v[136:139], v[184:187], v[92:95]\n\tv_cvt_pk_bf16_f32 v45, v46, v47\n\tv_and_b32 v220, v44, v22\n\ts_waitcnt lgkmcnt(7)\n\t"
            "v_mfma_f32_16x16x32_bf16 v[104:107], v[140:143], v[184:187], v[104:107]\n\tv_and_b32 v221, v45, v22\n\tv_and_b32 v236, v44, v23\n\ts_waitcnt lgkmcnt(6)\n\tv_mfma_f32_16x16x32_bf16 v[116:119], v[144:147], v[184:187], v[116:119]\n\tv_and_b32 v237, v45, v23\n\t"
            "v_fma_f32 v56, v48, v216, v56\n\ts_waitcnt lgkmcnt(5)\n\tv_mfma_f32_16x16x32_bf16 v[92:95], v[164:167], v[196:199], v[92:95]\n\tv_fma_f32 v57, v49, v216, v57\n\tv_fma_f32 v58, v50, v216, v58\n\ts_waitcnt lgkmcnt(4)\n\t"
            "v_mfma_f32_16x16x32_bf16 v[104:107], v[168:171], v[196:199], v[104:107]\n\tv_fma_f32 v59, v51, v216, v59\n\tv_cvt_pk_bf16_f32 v56, v56, v57\n\ts_waitcnt lgkmcnt(3)\n\tv_mfma_f32_16x16x32_bf16 v[116:119], v[172:175], v[196:199], v[116:119]\n\tv_cvt_pk_bf16_f32 v57, v58, v59\n\t"
            "v_and_b32 v222, v56, v22\n\tv_and_b32 v223, v57, v22\n\tv_and_b32 v238, v56, v23\n\tv_and_b32 v239, v57, v23\n\tv_fma_f32 v68, v60, v216, v68\n\tv_fma_f32 v69, v61, v216, v69\n\t"
            "v_fma_f32 v70, v62, v216, v70\n\tv_fma_f32 v71, v63, v216, v71\n\tds_read_b32 v27, v32\n\tds_read2st64_b32 v[42:43], v31 offset0:0 offset1:1\n\tds_read2st64_b32 v[40:41], v30 offset0:0 offset1:1\n\tds_read2st64_b32 v[66:67], v31 offset0:2 offset1:3\n\t"
            "ds_read2st64_b32 v[64:65], v30 offset0:2 offset1:3\n\tv_cvt_pk_bf16_f32 v68, v68, v69\n\tv_cvt_pk_bf16_f32 v69, v70, v71\n\tv_and_b32 v224, v68, v22\n\tv_and_b32 v225, v69, v22\n\tv_and_b32 v240, v68, v23\n\t"
            "v_and_b32 v241, v69, v23\n\tv_fma_f32 v80, v72, v216, v80\n\tv_fma_f32 v81, v73, v216, v81\n\tv_fma_f32 v82, v74, v216, v82\n\tv_fma_f32 v83, v75, v216, v83\n\tv_cvt_pk_bf16_f32 v80, v80, v81\n\t"
            "v_cvt_pk_bf16_f32 v81, v82, v83\n\tv_and_b32 v226, v80, v22\n\tv_and_b32 v227, v81, v22\n\tv_and_b32 v242, v80, v23\n\tv_and_b32 v243, v81, v23\n\ts_waitcnt lgkmcnt(4)\n\t"
            "v_cmp_ge_u32 vcc, 0, v27\n\ts_cbranch_vccnz 10f\n\t20:\ns_waitcnt lgkmcnt(3)\n\tv_and_b32 v177, v42, v24\n\tv_and_b32 v178, v42, v25\n\t"
            "v_and_b32 v179, v42, v26\n\tv_and_b32 v189, v43, v24\n\tv_and_b32 v190, v43, v25\n\tv_and_b32 v191, v43, v26\n\tv_mfma_f32_16x16x32_bf16 v[44:47], v[234:237], v[176:179], 0\n\ts_waitcnt lgkmcnt(2)\n\t"
            "v_and_b32 v181, v40, v24\n\tv_and_b32 v182, v40, v25\n\tv_mfma_f32_16x16x32_bf16 v[56:59], v[236:239], v[188:191], 0\n\tv_and_b32 v183, v40, v26\n\tv_and_b32 v193, v41, v24\n\tv_and_b32 v194, v41, v25\n\t"
            "v_and_b32 v195, v41, v26\n\tv_mfma_f32_16x16x32_bf16 v[44:47], v[218:221], v[180:183], v[44:47]\n\ts_waitcnt lgkmcnt(1)\n\tv_and_b32 v177, v66, v24\n\tv_and_b32 v178, v66, v25\n\tv_mfma_f32_16x16x32_bf16 v[56:59], v[220:223], v[192:195], v[56:59]\n\t"
            "v_and_b32 v179, v66, v26\n\tv_and_b32 v189, v67, v24\n\tv_and_b32 v190, v67, v25\n\ts_nop 0\n\tv_cvt_pk_bf16_f32 v36, v44, v45\n\tv_cvt_pk_bf16_f32 v37, v46, v47\n\t"
            "v_min_u32 v36, -2, v36\n\tv_and_b32 v191, v67, v26\n\tv_cvt_pk_bf16_f32 v38, v56, v57\n\tv_cvt_pk_bf16_f32 v39, v58, v59\n\tv_min_u32 v38, -2, v38\n\tv_mfma_f32_16x16x32_bf16 v[68:71], v[238:241], v[176:179], 0\n\t"
            "s_nop 0\n\tv_permlane16_swap_b32 v36, v38\n\tv_permlane16_swap_b32 v37, v39\n\tbuffer_store_dwordx4 v[36:39], v16, s[44:47], s20 offen sc1\n\tv_mfma_f32_16x16x32_bf16 v[80:83], v[240:243], v[188:191], 0\n\ts_waitcnt lgkmcnt(0)\n\t"
            "v_and_b32 v181, v64, v24\n\tv_and_b32 v182, v64, v25\n\tv_and_b32 v183, v64, v26\n\tv_and_b32 v193, v65, v24\n\tv_and_b32 v194, v65, v25\n\tv_and_b32 v195, v65, v26\n\t"
            "v_mfma_f32_16x16x32_bf16 v[68:71], v[222:225], v[180:183], v[68:71]\n\tv_fma_f32 v92, v84, v216, v92\n\tv_fma_f32 v93, v85, v216, v93\n\tv_mfma_f32_16x16x32_bf16 v[80:83], v[224:227], v[192:195], v[80:83]\n\tv_fma_f32 v94, v86, v216, v94\n\tv_fma_f32 v95, v87, v216, v95\n\t"
            "v_cvt_pk_bf16_f32 v92, v92, v93\n\ts_nop 1\n\tv_cvt_pk_bf16_f32 v60, v68, v69\n\tv_cvt_pk_bf16_f32 v61, v70, v71\n\tv_min_u32 v60, -2, v60\n\tv_fma_f32 v104, v96, v216, v104\n\t"
            "v_cvt_pk_bf16_f32 v62, v80, v81\n\tv_cvt_pk_bf16_f32 v63, v82, v83\n\tv_min_u32 v62, -2, v62\n\tv_fma_f32 v105, v97, v216, v105\n\tv_permlane16_swap_b32 v61, v63\n\tv_permlane16_swap_b32 v60, v62\n\t"
            "buffer_store_dwordx4 v[60:63], v16, s[44:47], s21 offen sc1\n\tds_write_b32 v34, v35\n\t"
            SA(4)
            "v_cvt_pk_bf16_f32 v93, v94, v95\n\tv_fma_f32 v106, v98, v216, v106\n\tv_fma_f32 v107, v99, v216, v107\n\tv_cvt_pk_bf16_f32 v104, v104, v105\n\tv_fma_f32 v116, v108, v216, v116\n\tv_fma_f32 v117, v109, v216, v117\n\t"
            "v_and_b32 v228, v92, v22\n\tds_read_b32 v27, v32\n\tds_read2st64_b32 v[102:103], v31 offset0:152 offset1:153\n\tds_read2st64_b32 v[100:101], v30 offset0:152 offset1:153\n\tds_read2st64_b32 v[78:79], v31 offset0:154 offset1:155\n\tds_read2st64_b32 v[76:77], v30 offset0:154 offset1:155\n\t"
            "v_and_b32 v229, v93, v22\n\tv_and_b32 v244, v92, v23\n\tv_and_b32 v245, v93, v23\n\tv_cvt_pk_bf16_f32 v105, v106, v107\n\tv_fma_f32 v118, v110, v216, v118\n\tv_fma_f32 v119, v111, v216, v119\n\t"
            "v_cvt_pk_bf16_f32 v116, v116, v117\n\tv_and_b32 v230, v104, v22\n\tv_and_b32 v231, v105, v22\n\tv_and_b32 v246, v104, v23\n\tv_and_b32 v247, v105, v23\n\tv_cvt_pk_bf16_f32 v117, v118, v119\n\t"
            "v_and_b32 v232, v116, v22\n\tv_and_b32 v233, v117, v22\n\tv_and_b32 v248, v116, v23\n\tv_and_b32 v249, v117, v23\n\ts_waitcnt lgkmcnt(4)\n\tv_cmp_ge_u32 vcc, 1, v27\n\t"
            "s_cbranch_vccnz 11f\n\t21:\ns_waitcnt lgkmcnt(3)\n\tv_and_b32 v177, v102, v24\n\tv_and_b32 v178, v102, v25\n\tv_and_b32 v179, v102, v26\n\t"
            "v_and_b32 v189, v103, v24\n\tv_and_b32 v190, v103, v25\n\tv_and_b32 v191, v103, v26\n\tv_mfma_f32_16x16x32_bf16 v[92:95], v[242:245], v[176:179], 0\n\ts_waitcnt lgkmcnt(2)\n\tv_and_b32 v181, v100, v24\n\t"
            "v_and_b32 v182, v100, v25\n\tv_mfma_f32_16x16x32_bf16 v[104:107], v[244:247], v[188:191], 0\n\tv_and_b32 v183, v100, v26\n\tv_and_b32 v193, v101, v24\n\tv_and_b32 v194, v101, v25\n\tv_and_b32 v195, v101, v26\n\t"
            "v_mfma_f32_16x16x32_bf16 v[92:95], v[226:229], v[180:183], v[92:95]\n\ts_waitcnt lgkmcnt(1)\n\tv_and_b32 v177, v78, v24\n\tv_and_b32 v178, v78, v25\n\tv_mfma_f32_16x16x32_bf16 v[104:107], v[228:231], v[192:195], v[104:107]\n\tv_and_b32 v179, v78, v26\n\t"
            "s_waitcnt lgkmcnt(0)\n\tv_and_b32 v181, v76, v24\n\tv_and_b32 v182, v76, v25\n\tv_mfma_f32_16x16x32_bf16 v[116:119], v[246:249], v[176:179], 0\n\tv_and_b32 v183, v76, v26\n\tv_cvt_pk_bf16_f32 v84, v92, v93\n\t"
            "s_nop 0\n\tv_cvt_pk_bf16_f32 v86, v104, v105\n\tv_mfma_f32_16x16x32_bf16 v[116:119], v[230:233], v[180:183], v[116:119]\n\tv_cvt_pk_bf16_f32 v85, v94, v95\n\tv_min_u32 v84, -2, v84\n\tv_cvt_pk_bf16_f32 v87, v106, v107\n\t"
            "v_min_u32 v86, -2, v86\n\ts_nop 0\n\tv_permlane16_swap_b32 v85, v87\n\tv_permlane16_swap_b32 v84, v86\n\tbuffer_store_dwordx4 v[84:87], v16, s[44:47], s22 offen sc1\n\tv_cvt_pk_bf16_f32 v108, v116, v117\n\t"
            "v_cvt_pk_bf16_f32 v109, v118, v119\n\tv_min_u32 v108, -2, v108\n\ts_nop 0\n\tv_permlane16_swap_b32 v109, v111\n\tv_permlane16_swap_b32 v108, v110\n\tbuffer_store_dwordx4 v[108:111], v215, s[44:47], s23 offen sc1\n\t"
            "ds_write_b32 v34, v34\n\t"
            SA(5)
            "s_setprio 0\n\t"
            DUMP
            "s_cmp_eq_u32 s69, 1\n\ts_cbranch_scc1 .Lout\n\ts_endpgm\n\t10:\nds_read_b32 v27, v32\n\tds_read2st64_b32 v[42:43], v31 offset0:0 offset1:1\n\t"
            "ds_read2st64_b32 v[40:41], v30 offset0:0 offset1:1\n\tds_read2st64_b32 v[66:67], v31 offset0:2 offset1:3\n\tds_read2st64_b32 v[64:65], v30 offset0:2 offset1:3\n\ts_waitcnt lgkmcnt(0)\n\tv_cmp_ge_u32 vcc, 0, v27\n\ts_cbranch_vccz 20b\n\t"
            ASLP
            "s_sub_u32 s68, s68, 1\n\ts_cbranch_scc0 10b\n\ts_branch 20b\n\t11:\nds_read_b32 v27, v32\n\tds_read2st64_b32 v[102:103], v31 offset0:152 offset1:153\n\t"
            "ds_read2st64_b32 v[100:101], v30 offset0:152 offset1:153\n\tds_read2st64_b32 v[78:79], v31 offset0:154 offset1:155\n\tds_read2st64_b32 v[76:77], v30 offset0:154 offset1:155\n\ts_waitcnt lgkmcnt(0)\n\tv_cmp_ge_u32 vcc, 1, v27\n\ts_cbranch_vccz 21b\n\t"
            ASLP
            "s_sub_u32 s68, s68, 1\n\ts_cbranch_scc0 11b\n\ts_branch 21b\n\t.LB:\ns_and_b32 s72, s56, 1\n\ts_lshr_b32 s73, s56, 1\n\t"
            "v_and_b32 v92, 2, v51\n\tv_lshlrev_b32 v92, 1, v92\n\tv_and_b32 v93, 7, v52\n\tv_xor_b32 v93, v92, v93\n\tv_mul_u32_u24 v92, 0x500, v51\n\tv_lshl_add_u32 v92, v93, 4, v92\n\t"
            "s_mul_i32 s74, s72, 0x2800\n\ts_mul_i32 s75, s73, 0xf000\n\ts_add_i32 s75, s75, s74\n\tv_add_u32 v93, s75, v92\n\ts_mul_i32 s75, s73, 0x5000\n\ts_add_i32 s75, s75, s74\n\t"
            "s_addk_i32 s75, 0x5000\n\tv_add_u32 v92, s75, v92\n\ts_cmp_lg_u32 s63, 0\n\ts_cselect_b32 s74, 81920, 0\n\ts_max_u32 s74, s74, 65536\n\tv_add_u32 v94, s62, v52\n\t"
            "v_min_u32 v95, 47, v52\n\ts_add_i32 s75, s62, 64\n\tv_add_u32 v95, s75, v95\n\tv_max_i32 v94, 0, v94\n\tv_min_i32 v95, 0x3ff, v95\n\tv_lshl_add_u32 v94, v94, 4, s74\n\t"
            "v_lshl_add_u32 v95, v95, 4, s74\n\tv_lshl_add_u32 v59, v53, 4, v57\n\ts_cmp_lg_u32 s73, 0\n\ts_cselect_b64 vcc, -1, 0\n\tv_cndmask_b32 v97, v59, v52, vcc\n\ts_lshl_b32 s75, s72, 6\n\t"
            "s_add_i32 s75, s75, s62\n\tv_add_u32 v97, s75, v97\n\tv_mov_b32 v60, s62\n\tv_add_u32 v60, 0x6f, v60\n\tv_min_i32 v60, 0x3ff, v60\n\tv_med3_i32 v97, v97, 0, v60\n\t"
            "s_lshl3_add_u32 s74, s73, s74\n\tv_lshl_add_u32 v97, v97, 4, s74\n\ts_mul_i32 s30, s73, 576\n\ts_lshl_b32 s75, s72, 8\n\ts_add_i32 s30, s30, s75\n\ts_add_i32 s30, s30, 160000\n\t"
            "s_lshl_b32 s74, s59, 7\n\ts_add_i32 s74, s74, 8192\n\tv_min_u32 v96, 9, v52\n\tv_lshl_add_u32 v96, v96, 2, s74\n\ts_lshr_b32 s75, s58, 5\n\ts_add_i32 s74, s74, s75\n\t"
            "s_cmp_eq_u32 s56, 3\n\ts_cselect_b32 s74, s74, 0x40000000\n\tv_lshl_add_u32 v98, v52, 24, s74\n\ts_cselect_b32 s75, 1, 0\n\ts_lshl2_add_u32 s74, s59, s56\n\ts_mul_i32 s74, s74, 0xa000\n\t"
            "s_add_i32 s74, s74, 155648\n\tv_lshl_add_u32 v101, v52, 4, s74\n\tv_and_b32 v60, s75, v53\n\tv_lshl_add_u32 v2, v60, 30, v101\n\ts_cmp_ge_u32 s56, s57\n\ts_addc_u32 s76, s56, 0\n\t"
            "s_and_b32 s16, s76, 3\n\ts_lshl_b32 s77, s76, 1\n\ts_max_u32 s78, s77, 1\n\ts_sub_u32 s78, s78, 1\n\ts_min_u32 s79, s77, 7\n\tv_lshlrev_b32 v60, 9, v53\n\t"
            "v_lshl_add_u32 v60, v54, 3, v60\n\ts_mul_i32 s80, s78, 0xc00\n\ts_mul_i32 s81, s79, 0xc00\n\ts_addk_i32 s80, 14336\n\ts_addk_i32 s81, 14336\n\tv_add_u32 v24, s80, v60\n\t"
            "v_add_u32 v26, s81, v60\n\tv_add_u32 v25, 38912, v24\n\tv_add_u32 v27, 38912, v26\n\tv_lshl_add_u32 v29, v52, 4, s80\n\ts_lshr_b32 s78, s78, 1\n\ts_lshr_b32 s79, s79, 1\n\t"
            "s_sub_u32 s79, s79, s78\n\ts_lshl2_add_u32 s78, s78, 159744\n\tv_and_b32 v28, s79, v52\n\tv_lshl_add_u32 v28, v28, 2, s78\n\ts_lshl2_add_u32 s78, s56, 159744\n\tv_mov_b32 v30, s78\n\t"
            "s_mov_b32 s78, 160576\n\tv_lshl_add_u32 v99, v59, 2, s78\n\ts_mul_i32 s79, s59, 163840\n\ts_lshl_b32 s80, s58, 5\n\ts_add_i32 s79, s79, s80\n\ts_cmp_eq_u32 s76, 0\n\t"
            "s_cselect_b32 s80, 0, 0xc00\n\ts_add_i32 s79, s79, s80\n\ts_add_i32 s79, s79, 155652\n\tv_lshl_add_u32 v31, v54, 4, s79\n\tv_lshrrev_b32 v60, 1, v53\n\tv_mul_u32_u24 v60, 0xa000, v60\n\t"
            "v_add_u32 v31, v60, v31\n\tv_and_b32 v60, 1, v53\n\tv_lshl_add_u32 v31, v60, 3, v31\n\ts_mov_b32 s31, 0x14000\n\ts_cmp_lt_i32 s62, 0\n\ts_cselect_b32 s79, 8, -16\n\t"
            "v_mov_b32 v61, 0xff800000\n\tv_mov_b32 v142, 1\n\tv_add_u32 v60, 0, v59\n\tv_and_b32 v60, -8, v60\n\tv_add_u32 v60, 0, v60\n\tv_cmp_gt_i32 vcc, s79, v60\n\t"
            "v_lshl_add_u32 v76, v60, 2, s78\n\ts_nop 0\n\tv_cndmask_b32 v128, 0, v61, vcc\n\tv_add_u32 v60, 4, v59\n\tv_and_b32 v60, -8, v60\n\tv_add_u32 v60, -4, v60\n\t"
            "v_cmp_gt_i32 vcc, s79, v60\n\tv_lshl_add_u32 v77, v60, 2, s78\n\ts_nop 0\n\tv_cndmask_b32 v129, 0, v61, vcc\n\tv_add_u32 v60, -1, v59\n\tv_and_b32 v60, -8, v60\n\t"
            "v_add_u32 v60, 1, v60\n\tv_cmp_gt_i32 vcc, s79, v60\n\tv_lshl_add_u32 v78, v60, 2, s78\n\ts_nop 0\n\tv_cndmask_b32 v130, 0, v61, vcc\n\tv_add_u32 v60, 3, v59\n\t"
            "v_and_b32 v60, -8, v60\n\tv_add_u32 v60, -3, v60\n\tv_cmp_gt_i32 vcc, s79, v60\n\tv_lshl_add_u32 v79, v60, 2, s78\n\ts_nop 0\n\tv_cndmask_b32 v131, 0, v61, vcc\n\t"
            "v_add_u32 v60, -2, v59\n\tv_and_b32 v60, -8, v60\n\tv_add_u32 v60, 2, v60\n\tv_cmp_gt_i32 vcc, s79, v60\n\tv_lshl_add_u32 v80, v60, 2, s78\n\ts_nop 0\n\t"
            "v_cndmask_b32 v132, 0, v61, vcc\n\tv_add_u32 v60, 2, v59\n\tv_and_b32 v60, -8, v60\n\tv_add_u32 v60, -2, v60\n\tv_cmp_gt_i32 vcc, s79, v60\n\tv_lshl_add_u32 v81, v60, 2, s78\n\t"
            "s_nop 0\n\tv_cndmask_b32 v133, 0, v61, vcc\n\tv_add_u32 v60, -3, v59\n\tv_and_b32 v60, -8, v60\n\tv_add_u32 v60, 3, v60\n\tv_cmp_gt_i32 vcc, s79, v60\n\t"
            "v_lshl_add_u32 v82, v60, 2, s78\n\ts_nop 0\n\tv_cndmask_b32 v134, 0, v61, vcc\n\tv_add_u32 v60, 1, v59\n\tv_and_b32 v60, -8, v60\n\tv_add_u32 v60, -1, v60\n\t"
            "v_cmp_gt_i32 vcc, s79, v60\n\tv_lshl_add_u32 v83, v60, 2, s78\n\ts_nop 0\n\tv_cndmask_b32 v135, 0, v61, vcc\n\ts_lshl2_add_u32 s80, s71, s56\n\ts_lshl_b32 s80, s80, 5\n\t"
            "v_add_u32 v100, s80, v52\n\tv_mul_u32_u24 v100, 0x500, v100\n\ts_add_i32 s80, s58, 3072\n\tv_add_u32 v100, s80, v100\n\ts_lshl_b32 s4, s64, 13\n\ts_add_u32 s5, s4, 0x1000\n\t"
            "s_add_u32 s6, s5, 0x1000\n\ts_cmpk_eq_u32 s6, 0xa000\n\ts_cselect_b32 s6, 0, s6\n\ts_add_u32 s7, s6, 0x1000\n\ts_add_u32 s8, s7, 0x1000\n\ts_cmpk_eq_u32 s8, 0xa000\n\t"
            "s_cselect_b32 s8, 0, s8\n\ts_add_u32 s9, s8, 0x1000\n\ts_add_u32 s10, s9, 0x1000\n\ts_cmpk_eq_u32 s10, 0xa000\n\ts_cselect_b32 s10, 0, s10\n\ts_add_u32 s11, s10, 0x1000\n\t"
            "s_add_u32 s12, s11, 0x1000\n\ts_cmpk_eq_u32 s12, 0xa000\n\ts_cselect_b32 s12, 0, s12\n\ts_add_u32 s13, s12, 0x1000\n\ts_mul_i32 s20, s4, 20\n\ts_add_i32 s20, s20, s58\n\t"
            "s_addk_i32 s20, 3072\n\ts_mul_i32 s21, s5, 20\n\ts_add_i32 s21, s21, s58\n\ts_addk_i32 s21, 2048\n\ts_mul_i32 s22, s6, 20\n\ts_add_i32 s22, s22, s58\n\t"
            "s_addk_i32 s22, 1024\n\ts_mul_i32 s23, s7, 20\n\ts_add_i32 s23, s23, s58\n\ts_mul_i32 s24, s8, 20\n\ts_add_i32 s24, s24, s58\n\ts_addk_i32 s24, 3072\n\t"
            "s_mul_i32 s25, s9, 20\n\ts_add_i32 s25, s25, s58\n\ts_addk_i32 s25, 2048\n\ts_mul_i32 s26, s10, 20\n\ts_add_i32 s26, s26, s58\n\ts_addk_i32 s26, 1024\n\t"
            "s_mul_i32 s27, s11, 20\n\ts_add_i32 s27, s27, s58\n\ts_mul_i32 s28, s12, 20\n\ts_add_i32 s28, s28, s58\n\ts_addk_i32 s28, 3072\n\ts_mul_i32 s29, s13, 20\n\t"
            "s_add_i32 s29, s29, s58\n\ts_addk_i32 s29, 2048\n\ts_mul_i32 s72, s60, 1920\n\ts_sub_u32 s14, s61, s72\n\ts_add_u32 s17, s63, 0x500\n\ts_add_u32 s60, s60, 256\n\t"
            "s_cmp_eq_u32 s60, s17\n\ts_cselect_b32 s60, s63, s60\n\ts_mul_i32 s15, s56, 0x2800\n\ts_add_u32 s15, s15, 77824\n\ts_lshl_b32 s18, s58, 5\n\ts_mov_b32 s50, 822272\n\t"
            "s_mov_b32 s51, 0x20000\n\ts_waitcnt lgkmcnt(0)\n\ts_load_dword s67, s[44:45], 0x0\n\ts_sub_u32 s48, s48, 3072\n\ts_subb_u32 s49, s49, 0\n\ts_lshl_b32 s78, s56, 5\n\t"
            "v_add_u32 v12, s78, v57\n\tv_add_u32 v3, s62, v12\n\tv_and_b32 v13, 1, v53\n\tv_lshrrev_b32 v14, 1, v53\n\tv_lshl_add_u32 v15, v13, 1, v14\n\ts_add_u32 s79, s63, s58\n\t"
            "v_lshl_add_u32 v15, v15, 4, s79\n\ts_movk_i32 s79, 0xa00\n\tv_mad_i32_i24 v3, v3, s79, v15\n\tv_cmp_gt_u32 vcc, 8, v12\n\ts_cmp_eq_u32 s56, 3\n\ts_cselect_b32 s79, 2.0, 0xa000\n\t"
            "v_add_u32 v11, s79, v3\n\tv_cndmask_b32 v3, v3, 2.0, vcc\n\tv_mov_b32 v8, 159744\n\tv_and_b32 v9, 31, v52\n\tv_add_u32 v9, s78, v9\n\tv_lshl_add_u32 v9, v9, 2, v8\n\t"
            "v_lshl_add_u32 v10, v13, 4, v12\n\tv_lshl_add_u32 v10, v10, 2, v8\n\tv_mul_u32_u24 v4, 0x2800, v14\n\tv_lshl_add_u32 v4, v56, 7, v4\n\tv_lshl_add_u32 v4, v55, 3, v4\n\tv_lshl_add_u32 v4, v13, 5, v4\n\t"
            "v_bfe_u32 v5, v52, 3, 1\n\tv_lshl_add_u32 v4, v5, 6, v4\n\tv_add_u32 v4, 77824, v4\n\tv_xor_b32 v5, 64, v4\n\tv_add_u32 v6, 0x5000, v4\n\tv_add_u32 v7, 0x5000, v5\n\t"
            "v_lshlrev_b32 v102, 16, v52\n\tv_add_u32 v102, 0xc00, v102\n\ts_cmp_eq_u32 s56, 0\n\ts_cbranch_scc0 1f\n\tbuffer_load_dword v103, v102, s[48:51], s58 offen\n\t1:\n"
            "s_cmp_lt_u32 s56, 1\n\ts_cbranch_scc1 1f\n\ts_waitcnt vmcnt(0)\n\t1:\ns_waitcnt vmcnt(1) lgkmcnt(0)\n\ts_barrier\n\t"
            "s_add_u32 m0, s66, 0\n\ts_mul_i32 s61, s60, 1920\n\ts_add_u32 s61, s61, s14\n\tbuffer_load_dwordx4 v40, s[40:43], s61 offen lds\n\tbuffer_load_dwordx4 v41, s[40:43], s61 offen offset:1024 lds\n\tbuffer_load_dwordx4 v42, s[40:43], s61 offen offset:2048 lds\n\t"
            "buffer_load_dwordx4 v43, s[40:43], s61 offen offset:3072 lds\n\ts_addk_i32 m0, 0x1000\n\ts_add_u32 s68, s60, 128\n\tbuffer_load_dwordx4 v44, s[40:43], s61 offen lds\n\tbuffer_load_dwordx4 v45, s[40:43], s61 offen offset:1024 lds\n\ts_add_u32 m0, s65, 0\n\t"
            "s_cmp_eq_u32 s68, s17\n\tbuffer_load_dwordx4 v36, s[36:39], s60 offen lds\n\tbuffer_load_dwordx4 v37, s[36:39], s60 offen offset:1024 lds\n\tbuffer_load_dwordx4 v38, s[36:39], s60 offen offset:2048 lds\n\ts_mov_b32 exec_hi, 0\n\tbuffer_load_dwordx4 v39, s[36:39], s60 offen offset:3072 lds\n\t"
            "s_mov_b32 exec_hi, -1\n\ts_cselect_b32 s60, s63, s68\n\ts_mul_i32 s67, s67, 0x19660d\n\ts_add_u32 s67, s67, 0x3c6ef35f\n\tv_mov_b32 v136, -1\n\tv_mov_b32 v137, -1\n\t"
            "v_mov_b32 v138, -1\n\tv_mov_b32 v139, -1\n\tbuffer_store_dwordx4 v[136:139], v101, s[44:47], s18 offen offset:0 sc1\n\tbuffer_store_dwordx4 v[136:139], v2, s[44:47], s18 offen offset:1024 sc1\n\tbuffer_store_dwordx4 v[136:139], v2, s[44:47], s18 offen offset:2048 sc1\n\tbuffer_store_dwordx4 v[136:139], v101, s[44:47], s18 offen offset:3072 sc1\n\t"
            "s_barrier\n\ts_add_u32 m0, s66, 38912\n\ts_mul_i32 s61, s60, 1920\n\ts_add_u32 s61, s61, s14\n\tbuffer_load_dwordx4 v40, s[40:43], s61 offen lds\n\tbuffer_load_dwordx4 v41, s[40:43], s61 offen offset:1024 lds\n\t"
            "buffer_load_dwordx4 v42, s[40:43], s61 offen offset:2048 lds\n\tbuffer_load_dwordx4 v43, s[40:43], s61 offen offset:3072 lds\n\ts_addk_i32 m0, 0x1000\n\ts_add_u32 s68, s60, 128\n\tbuffer_load_dwordx4 v44, s[40:43], s61 offen lds\n\tbuffer_load_dwordx4 v45, s[40:43], s61 offen offset:1024 lds\n\t"
            "s_add_u32 m0, s65, 38912\n\ts_cmp_eq_u32 s68, s17\n\tbuffer_load_dwordx4 v36, s[36:39], s60 offen lds\n\tbuffer_load_dwordx4 v37, s[36:39], s60 offen offset:1024 lds\n\tbuffer_load_dwordx4 v38, s[36:39], s60 offen offset:2048 lds\n\ts_mov_b32 exec_hi, 0\n\t"
            "buffer_load_dwordx4 v39, s[36:39], s60 offen offset:3072 lds\n\ts_mov_b32 exec_hi, -1\n\ts_cselect_b32 s60, s63, s68\n\ts_waitcnt vmcnt(14)\n\ts_barrier\n\ts_add_u32 m0, s66, 77824\n\t"
            "s_mul_i32 s61, s60, 1920\n\ts_add_u32 s61, s61, s14\n\tbuffer_load_dwordx4 v40, s[40:43], s61 offen lds\n\tbuffer_load_dwordx4 v41, s[40:43], s61 offen offset:1024 lds\n\tbuffer_load_dwordx4 v42, s[40:43], s61 offen offset:2048 lds\n\tbuffer_load_dwordx4 v43, s[40:43], s61 offen offset:3072 lds\n\t"
            "s_addk_i32 m0, 0x1000\n\ts_add_u32 s68, s60, 128\n\tbuffer_load_dwordx4 v44, s[40:43], s61 offen lds\n\tbuffer_load_dwordx4 v45, s[40:43], s61 offen offset:1024 lds\n\ts_add_u32 m0, s65, 77824\n\ts_cmp_eq_u32 s68, s17\n\t"
            "buffer_load_dwordx4 v36, s[36:39], s60 offen lds\n\tbuffer_load_dwordx4 v37, s[36:39], s60 offen offset:1024 lds\n\tbuffer_load_dwordx4 v38, s[36:39], s60 offen offset:2048 lds\n\ts_mov_b32 exec_hi, 0\n\tbuffer_load_dwordx4 v39, s[36:39], s60 offen offset:3072 lds\n\ts_mov_b32 exec_hi, -1\n\t"
            "s_cselect_b32 s60, s63, s68\n\ts_waitcnt vmcnt(10)\n\ts_barrier\n\ts_add_u32 m0, s66, 0\n\ts_mul_i32 s61, s60, 1920\n\ts_add_u32 s61, s61, s14\n\t"
            "buffer_load_dwordx4 v40, s[40:43], s61 offen lds\n\tbuffer_load_dwordx4 v41, s[40:43], s61 offen offset:1024 lds\n\tbuffer_load_dwordx4 v42, s[40:43], s61 offen offset:2048 lds\n\tbuffer_load_dwordx4 v43, s[40:43], s61 offen offset:3072 lds\n\ts_addk_i32 m0, 0x1000\n\ts_add_u32 s68, s60, 128\n\t"
            "buffer_load_dwordx4 v44, s[40:43], s61 offen lds\n\tbuffer_load_dwordx4 v45, s[40:43], s61 offen offset:1024 lds\n\ts_add_u32 m0, s65, 0\n\ts_cmp_eq_u32 s68, s17\n\tbuffer_load_dwordx4 v36, s[36:39], s60 offen lds\n\tbuffer_load_dwordx4 v37, s[36:39], s60 offen offset:1024 lds\n\t"
            "buffer_load_dwordx4 v38, s[36:39], s60 offen offset:2048 lds\n\ts_mov_b32 exec_hi, 0\n\tbuffer_load_dwordx4 v39, s[36:39], s60 offen offset:3072 lds\n\ts_mov_b32 exec_hi, -1\n\ts_cselect_b32 s60, s63, s68\n\tv_mov_b32 v140, s67\n\t"
            "buffer_store_dword v140, v98, s[44:47], 0 offen sc1\n\ts_waitcnt vmcnt(11)\n\ts_barrier\n\ts_add_u32 m0, s66, 38912\n\ts_mul_i32 s61, s60, 1920\n\ts_add_u32 s61, s61, s14\n\t"
            "buffer_load_dwordx4 v40, s[40:43], s61 offen lds\n\tbuffer_load_dwordx4 v41, s[40:43], s61 offen offset:1024 lds\n\tbuffer_load_dwordx4 v42, s[40:43], s61 offen offset:2048 lds\n\tbuffer_load_dwordx4 v43, s[40:43], s61 offen offset:3072 lds\n\ts_addk_i32 m0, 0x1000\n\ts_add_u32 s68, s60, 128\n\t"
            "buffer_load_dwordx4 v44, s[40:43], s61 offen lds\n\tbuffer_load_dwordx4 v45, s[40:43], s61 offen offset:1024 lds\n\ts_add_u32 m0, s65, 38912\n\ts_cmp_eq_u32 s68, s17\n\tbuffer_load_dwordx4 v36, s[36:39], s60 offen lds\n\tbuffer_load_dwordx4 v37, s[36:39], s60 offen offset:1024 lds\n\t"
            "buffer_load_dwordx4 v38, s[36:39], s60 offen offset:2048 lds\n\ts_mov_b32 exec_hi, 0\n\tbuffer_load_dwordx4 v39, s[36:39], s60 offen offset:3072 lds\n\ts_mov_b32 exec_hi, -1\n\ts_cselect_b32 s60, s63, s68\n\ts_waitcnt vmcnt(11)\n\t"
            "s_barrier\n\ts_add_u32 m0, s66, 77824\n\ts_mul_i32 s61, s60, 1920\n\ts_add_u32 s61, s61, s14\n\tbuffer_load_dwordx4 v40, s[40:43], s61 offen lds\n\tbuffer_load_dwordx4 v41, s[40:43], s61 offen offset:1024 lds\n\t"
            "buffer_load_dwordx4 v42, s[40:43], s61 offen offset:2048 lds\n\tbuffer_load_dwordx4 v43, s[40:43], s61 offen offset:3072 lds\n\ts_addk_i32 m0, 0x1000\n\ts_add_u32 s68, s60, 128\n\tbuffer_load_dwordx4 v44, s[40:43], s61 offen lds\n\tbuffer_load_dwordx4 v45, s[40:43], s61 offen offset:1024 lds\n\t"
            "s_add_u32 m0, s65, 77824\n\ts_cmp_eq_u32 s68, s17\n\tbuffer_load_dwordx4 v36, s[36:39], s60 offen lds\n\tbuffer_load_dwordx4 v37, s[36:39], s60 offen offset:1024 lds\n\tbuffer_load_dwordx4 v38, s[36:39], s60 offen offset:2048 lds\n\ts_mov_b32 exec_hi, 0\n\t"
            "buffer_load_dwordx4 v39, s[36:39], s60 offen offset:3072 lds\n\ts_mov_b32 exec_hi, -1\n\ts_cselect_b32 s60, s63, s68\n\ts_waitcnt vmcnt(10)\n\ts_barrier\n\ts_cmp_lg_u32 s56, 3\n\t"
            "s_cbranch_scc1 1f\n\tbuffer_load_dwordx4 v[106:109], v94, s[44:47], 0 offen sc1\n\tbuffer_load_dwordx4 v[110:113], v95, s[44:47], 0 offen sc1\n\tbuffer_load_dword v114, v96, s[44:47], 0 offen sc1\n\t1:\ns_add_u32 m0, s66, 0\n\t"
            "s_mul_i32 s61, s60, 1920\n\ts_add_u32 s61, s61, s14\n\tbuffer_load_dwordx4 v40, s[40:43], s61 offen lds\n\tbuffer_load_dwordx4 v41, s[40:43], s61 offen offset:1024 lds\n\tbuffer_load_dwordx4 v42, s[40:43], s61 offen offset:2048 lds\n\tbuffer_load_dwordx4 v43, s[40:43], s61 offen offset:3072 lds\n\t"
            "s_addk_i32 m0, 0x1000\n\ts_add_u32 s68, s60, 128\n\tbuffer_load_dwordx4 v44, s[40:43], s61 offen lds\n\tbuffer_load_dwordx4 v45, s[40:43], s61 offen offset:1024 lds\n\ts_add_u32 m0, s65, 0\n\ts_cmp_eq_u32 s68, s17\n\t"
            "buffer_load_dwordx4 v36, s[36:39], s60 offen lds\n\tbuffer_load_dwordx4 v37, s[36:39], s60 offen offset:1024 lds\n\tbuffer_load_dwordx4 v38, s[36:39], s60 offen offset:2048 lds\n\ts_mov_b32 exec_hi, 0\n\tbuffer_load_dwordx4 v39, s[36:39], s60 offen offset:3072 lds\n\ts_mov_b32 exec_hi, -1\n\t"
            "s_cselect_b32 s60, s63, s68\n\ts_waitcnt vmcnt(10)\n\ts_cmp_lg_u32 s56, 3\n\ts_cbranch_scc1 2f\n\ts_mov_b32 s68, 0x40000\n\tv_xor_b32 v106, s67, v107\n\t"
            "v_xor_b32 v108, s67, v109\n\tv_xor_b32 v110, s67, v111\n\tv_xor_b32 v112, s67, v113\n\tv_xor_b32 v114, s67, v114\n\tv_or3_b32 v106, v106, v108, v110\n\tv_or3_b32 v106, v106, v112, v114\n\t"
            "v_cmp_ne_u32 vcc, 0, v106\n\ts_cbranch_vccnz 30f\n\t31:\n2:\ns_waitcnt vmcnt(10)\n\ts_barrier\n\t"
            "s_add_u32 m0, s66, 38912\n\ts_mul_i32 s61, s60, 1920\n\ts_add_u32 s61, s61, s14\n\tbuffer_load_dwordx4 v40, s[40:43], s61 offen lds\n\tbuffer_load_dwordx4 v41, s[40:43], s61 offen offset:1024 lds\n\tbuffer_load_dwordx4 v42, s[40:43], s61 offen offset:2048 lds\n\t"
            "buffer_load_dwordx4 v43, s[40:43], s61 offen offset:3072 lds\n\ts_addk_i32 m0, 0x1000\n\ts_add_u32 s68, s60, 128\n\tbuffer_load_dwordx4 v44, s[40:43], s61 offen lds\n\tbuffer_load_dwordx4 v45, s[40:43], s61 offen offset:1024 lds\n\ts_add_u32 m0, s65, 38912\n\t"
            "s_cmp_eq_u32 s68, s17\n\tbuffer_load_dwordx4 v36, s[36:39], s60 offen lds\n\tbuffer_load_dwordx4 v37, s[36:39], s60 offen offset:1024 lds\n\tbuffer_load_dwordx4 v38, s[36:39], s60 offen offset:2048 lds\n\ts_mov_b32 exec_hi, 0\n\tbuffer_load_dwordx4 v39, s[36:39], s60 offen offset:3072 lds\n\t"
            "s_mov_b32 exec_hi, -1\n\ts_cselect_b32 s60, s63, s68\n\ts_mov_b32 m0, s30\n\ts_nop 0\n\tbuffer_load_dword v97, s[44:47], 0 offen sc1 lds\n\ts_waitcnt vmcnt(11)\n\t"
            "s_barrier\n\ts_add_u32 m0, s15, 40960\n\ts_nop 0\n\tbuffer_load_dwordx4 v92, s[48:51], s20 offen lds\n\tbuffer_load_dwordx4 v92, s[48:51], s21 offen offset:1024 lds\n\tbuffer_load_dwordx4 v92, s[48:51], s22 offen offset:2048 lds\n\t"
            "buffer_load_dwordx4 v92, s[48:51], s23 offen offset:3072 lds\n\ts_add_u32 m0, s15, 45056\n\ts_nop 0\n\tbuffer_load_dwordx4 v92, s[48:51], s24 offen lds\n\tbuffer_load_dwordx4 v92, s[48:51], s25 offen offset:1024 lds\n\tbuffer_load_dwordx4 v92, s[48:51], s26 offen offset:2048 lds\n\t"
            "s_waitcnt vmcnt(7)\n\ts_barrier\n\ts_add_u32 m0, s15, 45056\n\ts_nop 0\n\tbuffer_load_dwordx4 v92, s[48:51], s27 offen offset:3072 lds\n\ts_add_u32 m0, s15, 49152\n\t"
            "s_nop 0\n\tbuffer_load_dwordx4 v92, s[48:51], s28 offen lds\n\tbuffer_load_dwordx4 v92, s[48:51], s29 offen offset:1024 lds\n\ts_add_u32 m0, s15, 0\n\ts_nop 0\n\tbuffer_load_dwordx4 v93, s[48:51], s20 offen lds\n\t"
            "buffer_load_dwordx4 v93, s[48:51], s21 offen offset:1024 lds\n\tbuffer_load_dwordx4 v93, s[48:51], s22 offen offset:2048 lds\n\tbuffer_load_dwordx4 v93, s[48:51], s23 offen offset:3072 lds\n\ts_add_u32 m0, s15, 4096\n\ts_nop 0\n\tbuffer_load_dwordx4 v93, s[48:51], s24 offen lds\n\t"
            "buffer_load_dwordx4 v93, s[48:51], s25 offen offset:1024 lds\n\tbuffer_load_dwordx4 v93, s[48:51], s26 offen offset:2048 lds\n\tbuffer_load_dwordx4 v93, s[48:51], s27 offen offset:3072 lds\n\ts_add_u32 m0, s15, 8192\n\ts_nop 0\n\tbuffer_load_dwordx4 v93, s[48:51], s28 offen lds\n\t"
            "buffer_load_dwordx4 v93, s[48:51], s29 offen offset:1024 lds\n\tbuffer_load_dwordx4 v[240:243], v3, s[36:39], 0 offen offset:0\n\tbuffer_load_dwordx4 v[244:247], v11, s[36:39], 0 offen offset:0\n\tbuffer_load_dwordx4 v[248:251], v3, s[36:39], 0 offen offset:64\n\tbuffer_load_dwordx4 v[252:255], v11, s[36:39], 0 offen offset:64\n\tds_read_b32 v20, v9 offset:256\n\t"
            "ds_read_b32 v21, v10 offset:832\n\tds_read2st64_b32 v[138:139], v99 offset1:1\n\tds_read2st64_b32 v[176:177], v76 offset1:1\n\tds_read2st64_b32 v[178:179], v77 offset1:1\n\tds_read2st64_b32 v[180:181], v78 offset1:1\n\tds_read2st64_b32 v[182:183], v79 offset1:1\n\t"
            "ds_read2st64_b32 v[184:185], v80 offset1:1\n\tds_read2st64_b32 v[186:187], v81 offset1:1\n\tds_read2st64_b32 v[188:189], v82 offset1:1\n\tds_read2st64_b32 v[190:191], v83 offset1:1\n\ts_mov_b32 s68, 0x40000\n\ts_waitcnt lgkmcnt(8)\n\t"
            "v_mul_f32 v138, 0x3ea52b5c, v138\n\tv_mul_f32 v139, 0x3ea52b5c, v139\n\ts_waitcnt lgkmcnt(7)\n\tv_mul_f32 v192, v176, v138\n\tv_mul_f32 v200, v177, v139\n\ts_waitcnt lgkmcnt(6)\n\t"
            "v_mul_f32 v193, v178, v138\n\tv_mul_f32 v201, v179, v139\n\ts_waitcnt lgkmcnt(5)\n\tv_mul_f32 v194, v180, v138\n\tv_mul_f32 v202, v181, v139\n\ts_waitcnt lgkmcnt(4)\n\t"
            "v_mul_f32 v195, v182, v138\n\tv_mul_f32 v203, v183, v139\n\ts_waitcnt lgkmcnt(3)\n\tv_mul_f32 v196, v184, v138\n\tv_mul_f32 v204, v185, v139\n\ts_waitcnt lgkmcnt(2)\n\t"
            "v_mul_f32 v197, v186, v138\n\tv_mul_f32 v205, v187, v139\n\ts_waitcnt lgkmcnt(1)\n\tv_mul_f32 v198, v188, v138\n\tv_mul_f32 v206, v189, v139\n\ts_waitcnt lgkmcnt(0)\n\t"
            "v_mul_f32 v199, v190, v138\n\tv_mul_f32 v207, v191, v139\n\ts_barrier\n\t"
            SB_(1) PRIS
            "50:\nds_read2_b64 v[208:211], v24 offset0:0 offset1:16\n\tds_read2_b64 v[212:215], v24 offset0:32 offset1:48\n\tds_read2_b64 v[216:219], v26 offset0:0 offset1:16\n\tds_read2_b64 v[220:223], v26 offset0:32 offset1:48\n\ts_cmp_eq_u32 s16, 0\n\t"
            "s_cbranch_scc1 56f\n\t58:\ns_cbranch_scc0 1f\n\tbuffer_load_dword v232, v31, s[44:47], s31 offen offset:0 sc1\n\tbuffer_load_dword v233, v31, s[44:47], s31 offen offset:256 sc1\n\tbuffer_load_dword v234, v31, s[44:47], s31 offen offset:512 sc1\n\t"
            "buffer_load_dword v235, v31, s[44:47], s31 offen offset:768 sc1\n\t1:\ns_waitcnt lgkmcnt(1)\n\tv_add_f32 v208, v208, v216\n\tv_add_f32 v209, v209, v217\n\tv_add_f32 v210, v210, v218\n\t"
            "v_add_f32 v211, v211, v219\n\ts_waitcnt lgkmcnt(0)\n\tv_add_f32 v212, v212, v220\n\tv_add_f32 v213, v213, v221\n\tv_add_f32 v214, v214, v222\n\tv_add_f32 v215, v215, v223\n\t"
            "v_fma_f32 v208, v208, v192, v128\n\tv_fma_f32 v209, v209, v193, v129\n\tv_fma_f32 v210, v210, v194, v130\n\tv_fma_f32 v211, v211, v195, v131\n\tv_fma_f32 v212, v212, v196, v132\n\tv_fma_f32 v213, v213, v197, v133\n\t"
            "v_fma_f32 v214, v214, v198, v134\n\tv_fma_f32 v215, v215, v199, v135\n\tv_max3_f32 v136, v208, v209, v210\n\tv_max3_f32 v137, v211, v212, v213\n\tv_max3_f32 v136, v136, v214, v215\n\tv_max_f32 v136, v136, v137\n\t"
            "v_sub_f32 v208, v208, v136\n\tv_sub_f32 v209, v209, v136\n\tv_sub_f32 v210, v210, v136\n\tv_sub_f32 v211, v211, v136\n\tv_sub_f32 v212, v212, v136\n\tv_sub_f32 v213, v213, v136\n\t"
            "v_sub_f32 v214, v214, v136\n\tv_sub_f32 v215, v215, v136\n\tv_exp_f32 v208, v208\n\tv_exp_f32 v209, v209\n\tv_exp_f32 v210, v210\n\tv_exp_f32 v211, v211\n\t"
            "v_exp_f32 v212, v212\n\tv_exp_f32 v213, v213\n\tv_exp_f32 v214, v214\n\tv_exp_f32 v215, v215\n\tv_add_f32 v216, v208, v209\n\tv_add_f32 v217, v210, v211\n\t"
            "v_add_f32 v218, v212, v213\n\tv_add_f32 v219, v214, v215\n\tv_add_f32 v216, v216, v217\n\tv_add_f32 v218, v218, v219\n\tv_mul_f32 v208, v208, v176\n\tv_add_f32 v216, v216, v218\n\t"
            "v_rcp_f32 v216, v216\n\tv_mul_f32 v209, v209, v178\n\tv_mul_f32 v210, v210, v180\n\tv_mul_f32 v211, v211, v182\n\tv_mul_f32 v212, v212, v184\n\tv_mul_f32 v213, v213, v186\n\t"
            "v_mul_f32 v214, v214, v188\n\tv_mul_f32 v215, v215, v190\n\tv_mul_f32 v208, v208, v216\n\tv_mul_f32 v209, v209, v216\n\tv_mul_f32 v210, v210, v216\n\tv_mul_f32 v211, v211, v216\n\t"
            "v_mul_f32 v212, v212, v216\n\tv_mul_f32 v213, v213, v216\n\tv_mul_f32 v214, v214, v216\n\tv_mul_f32 v215, v215, v216\n\tv_cvt_pk_bf16_f32 v220, v208, v209\n\tv_cvt_pk_bf16_f32 v221, v210, v211\n\t"
            "v_cvt_pk_bf16_f32 v222, v212, v213\n\tv_cvt_pk_bf16_f32 v223, v214, v215\n\tds_write_b128 v29, v[220:223] offset:0\n\tds_write_b32 v30, v142 offset:16\n\t"
            SP0
            "51:\nds_read_b32 v136, v28\n\tds_read2_b64 v[208:211], v25 offset0:0 offset1:16\n\tds_read2_b64 v[212:215], v25 offset0:32 offset1:48\n\tds_read2_b64 v[216:219], v27 offset0:0 offset1:16\n\tds_read2_b64 v[220:223], v27 offset0:32 offset1:48\n\t"
            "s_waitcnt lgkmcnt(4)\n\tv_cmp_eq_u32 vcc, 0, v136\n\ts_cbranch_vccnz 53f\n\t55:\ns_cmp_eq_u32 s16, 0\n\ts_cbranch_scc1 57f\n\t"
            "59:\ns_waitcnt lgkmcnt(1)\n\tv_add_f32 v208, v208, v216\n\tv_add_f32 v209, v209, v217\n\tv_add_f32 v210, v210, v218\n\tv_add_f32 v211, v211, v219\n\t"
            "s_waitcnt lgkmcnt(0)\n\tv_add_f32 v212, v212, v220\n\tv_add_f32 v213, v213, v221\n\tv_add_f32 v214, v214, v222\n\tv_add_f32 v215, v215, v223\n\tv_mul_f32 v208, v208, v200\n\t"
            "v_mul_f32 v209, v209, v201\n\tv_mul_f32 v210, v210, v202\n\tv_mul_f32 v211, v211, v203\n\tv_mul_f32 v212, v212, v204\n\tv_mul_f32 v213, v213, v205\n\tv_mul_f32 v214, v214, v206\n\t"
            "v_mul_f32 v215, v215, v207\n\tv_max3_f32 v136, v208, v209, v210\n\tv_max3_f32 v137, v211, v212, v213\n\tv_max3_f32 v136, v136, v214, v215\n\tv_max_f32 v136, v136, v137\n\tv_sub_f32 v208, v208, v136\n\t"
            "v_sub_f32 v209, v209, v136\n\tv_sub_f32 v210, v210, v136\n\tv_sub_f32 v211, v211, v136\n\tv_sub_f32 v212, v212, v136\n\tv_sub_f32 v213, v213, v136\n\tv_sub_f32 v214, v214, v136\n\t"
            "v_sub_f32 v215, v215, v136\n\tv_exp_f32 v208, v208\n\tv_exp_f32 v209, v209\n\tv_exp_f32 v210, v210\n\tv_exp_f32 v211, v211\n\tv_exp_f32 v212, v212\n\t"
            "v_exp_f32 v213, v213\n\tv_exp_f32 v214, v214\n\tv_exp_f32 v215, v215\n\tv_add_f32 v216, v208, v209\n\tv_add_f32 v217, v210, v211\n\tv_add_f32 v218, v212, v213\n\t"
            "v_add_f32 v219, v214, v215\n\tv_add_f32 v216, v216, v217\n\tv_add_f32 v218, v218, v219\n\tv_mul_f32 v208, v208, v177\n\tv_add_f32 v216, v216, v218\n\tv_rcp_f32 v216, v216\n\t"
            "v_mul_f32 v209, v209, v179\n\tv_mul_f32 v210, v210, v181\n\tv_mul_f32 v211, v211, v183\n\tv_mul_f32 v212, v212, v185\n\tv_mul_f32 v213, v213, v187\n\tv_mul_f32 v214, v214, v189\n\t"
            "v_mul_f32 v215, v215, v191\n\tv_mul_f32 v208, v208, v216\n\tv_mul_f32 v209, v209, v216\n\tv_mul_f32 v210, v210, v216\n\tv_mul_f32 v211, v211, v216\n\tv_mul_f32 v212, v212, v216\n\t"
            "v_mul_f32 v213, v213, v216\n\tv_mul_f32 v214, v214, v216\n\tv_mul_f32 v215, v215, v216\n\tv_cvt_pk_bf16_f32 v220, v208, v209\n\tv_cvt_pk_bf16_f32 v221, v210, v211\n\tv_cvt_pk_bf16_f32 v222, v212, v213\n\t"
            "v_cvt_pk_bf16_f32 v223, v214, v215\n\tds_write_b128 v29, v[220:223] offset:38912\n\tds_write_b32 v30, v30 offset:16\n\t"
            SB_(2) PRIG
            "s_waitcnt vmcnt(0)\n\tds_write_b32 v30, v30 offset:48\n\ts_branch .LOL\n\t30:\nbuffer_load_dwordx4 v[106:109], v94, s[44:47], 0 offen sc1\n\tbuffer_load_dwordx4 v[110:113], v95, s[44:47], 0 offen sc1\n\t"
            "buffer_load_dword v114, v96, s[44:47], 0 offen sc1\n\ts_waitcnt vmcnt(2)\n\tv_xor_b32 v106, s67, v107\n\tv_xor_b32 v108, s67, v109\n\ts_waitcnt vmcnt(1)\n\tv_xor_b32 v110, s67, v111\n\t"
            "v_xor_b32 v112, s67, v113\n\ts_waitcnt vmcnt(0)\n\tv_xor_b32 v114, s67, v114\n\tv_or3_b32 v106, v106, v108, v110\n\tv_or3_b32 v106, v106, v112, v114\n\tv_cmp_ne_u32 vcc, 0, v106\n\t"
            "s_cbranch_vccz 31b\n\ts_sub_u32 s68, s68, 1\n\ts_cbranch_scc0 30b\n\ts_branch 31b\n\t53:\n"
            PSLP
            "s_sub_u32 s68, s68, 1\n\ts_cbranch_scc0 51b\n\ts_branch 55b\n\t56:\n"
            RSL0
            "buffer_load_dword v224, v31, s[44:47], 0 offen offset:0 sc1\n\tbuffer_load_dword v225, v31, s[44:47], 0 offen offset:256 sc1\n\tbuffer_load_dword v226, v31, s[44:47], 0 offen offset:512 sc1\n\tbuffer_load_dword v227, v31, s[44:47], 0 offen offset:768 sc1\n\ts_waitcnt vmcnt(4)\n\t"
            PRIR
            "s_call_b64 s[76:77], .LG0\n\t"
            PRIS
            "s_branch 2f\n\t1:\ns_waitcnt vmcnt(3)\n\tbuffer_load_dword v224, v31, s[44:47], 0 offen offset:0 sc1\n\ts_waitcnt vmcnt(3)\n\tbuffer_load_dword v225, v31, s[44:47], 0 offen offset:256 sc1\n\t"
            "s_waitcnt vmcnt(3)\n\tbuffer_load_dword v226, v31, s[44:47], 0 offen offset:512 sc1\n\ts_waitcnt vmcnt(3)\n\tbuffer_load_dword v227, v31, s[44:47], 0 offen offset:768 sc1\n\t2:\ns_waitcnt vmcnt(0) lgkmcnt(0)\n\t"
            "ds_write_b32 v30, v30 offset:48\n\tv_max3_u32 v136, v224, v225, v226\n\tv_max_u32 v136, v136, v227\n\tv_cmp_eq_u32 vcc, -1, v136\n\tv_lshlrev_b32 v216, 16, v224\n\tv_and_b32 v217, 0xffff0000, v224\n\t"
            "v_lshlrev_b32 v218, 16, v225\n\tv_and_b32 v219, 0xffff0000, v225\n\tv_lshlrev_b32 v220, 16, v226\n\tv_and_b32 v221, 0xffff0000, v226\n\tv_lshlrev_b32 v222, 16, v227\n\tv_and_b32 v223, 0xffff0000, v227\n\t"
            LINC
            "s_cmp_eq_u32 0, 0\n\ts_cbranch_vccz 58b\n\ts_sub_u32 s68, s68, 1\n\ts_cbranch_scc0 1b\n\ts_branch 58b\n\t57:\n"
            "s_branch 2f\n\t1:\nbuffer_load_dword v232, v31, s[44:47], s31 offen offset:0 sc1\n\tbuffer_load_dword v233, v31, s[44:47], s31 offen offset:256 sc1\n\tbuffer_load_dword v234, v31, s[44:47], s31 offen offset:512 sc1\n\tbuffer_load_dword v235, v31, s[44:47], s31 offen offset:768 sc1\n\t"
            "2:\ns_waitcnt vmcnt(0) lgkmcnt(0)\n\tv_max3_u32 v136, v232, v233, v234\n\tv_max_u32 v136, v136, v235\n\tv_cmp_eq_u32 vcc, -1, v136\n\tv_lshlrev_b32 v216, 16, v232\n\t"
            "v_and_b32 v217, 0xffff0000, v232\n\tv_lshlrev_b32 v218, 16, v233\n\tv_and_b32 v219, 0xffff0000, v233\n\tv_lshlrev_b32 v220, 16, v234\n\tv_and_b32 v221, 0xffff0000, v234\n\tv_lshlrev_b32 v222, 16, v235\n\t"
            "v_and_b32 v223, 0xffff0000, v235\n\t"
            LINC
            "s_cmp_eq_u32 0, 0\n\ts_cbranch_vccz 59b\n\ts_sub_u32 s68, s68, 1\n\ts_cbranch_scc0 1b\n\ts_branch 59b\n\t.LOL:\n"
            "s_mov_b32 s68, 0x40000\n\ts_cmp_eq_u32 s16, 0\n\ts_cbranch_scc1 3f\n\ts_call_b64 s[76:77], .LG0\n\t3:\nds_read_b64 v[14:15], v8 offset:32\n\t"
            "s_waitcnt lgkmcnt(0)\n\tv_min_u32 v12, v14, v15\n\tv_cmp_lt_u32 vcc, 1, v12\n\ts_cbranch_vccnz 4f\n\ts_sleep 1\n\ts_sub_u32 s68, s68, 1\n\t"
            "s_cbranch_scc0 3b\n\t4:\nv_fma_f32 v96, v64, v21, v20\n\tv_fma_f32 v97, v65, v21, v20\n\tv_fma_f32 v98, v66, v21, v20\n\tv_fma_f32 v99, v67, v21, v20\n\t"
            "v_fma_f32 v100, v68, v21, v20\n\tv_fma_f32 v101, v69, v21, v20\n\tv_fma_f32 v102, v70, v21, v20\n\tv_fma_f32 v103, v71, v21, v20\n\tv_fma_f32 v104, v72, v21, v20\n\tv_fma_f32 v105, v73, v21, v20\n\t"
            "v_fma_f32 v106, v74, v21, v20\n\tv_fma_f32 v107, v75, v21, v20\n\tv_fma_f32 v108, v76, v21, v20\n\tv_fma_f32 v109, v77, v21, v20\n\tv_fma_f32 v110, v78, v21, v20\n\tv_fma_f32 v111, v79, v21, v20\n\t"
            "v_fma_f32 v112, v80, v21, v20\n\tv_fma_f32 v113, v81, v21, v20\n\tv_fma_f32 v114, v82, v21, v20\n\tv_fma_f32 v115, v83, v21, v20\n\tv_fma_f32 v116, v84, v21, v20\n\tv_fma_f32 v117, v85, v21, v20\n\t"
            "v_fma_f32 v118, v86, v21, v20\n\tv_fma_f32 v119, v87, v21, v20\n\tv_fma_f32 v120, v88, v21, v20\n\tv_fma_f32 v121, v89, v21, v20\n\tv_fma_f32 v122, v90, v21, v20\n\tv_fma_f32 v123, v91, v21, v20\n\t"
            "v_fma_f32 v124, v92, v21, v20\n\tv_fma_f32 v125, v93, v21, v20\n\tv_fma_f32 v126, v94, v21, v20\n\tv_fma_f32 v127, v95, v21, v20\n\tv_exp_f32 v32, v32\n\tv_exp_f32 v33, v33\n\t"
            "v_exp_f32 v34, v34\n\tv_exp_f32 v35, v35\n\tv_exp_f32 v36, v36\n\tv_exp_f32 v37, v37\n\tv_exp_f32 v38, v38\n\tv_exp_f32 v39, v39\n\t"
            "v_exp_f32 v40, v40\n\tv_exp_f32 v41, v41\n\tv_exp_f32 v42, v42\n\tv_exp_f32 v43, v43\n\tv_exp_f32 v44, v44\n\tv_exp_f32 v45, v45\n\t"
            "v_exp_f32 v46, v46\n\tv_exp_f32 v47, v47\n\tv_exp_f32 v48, v48\n\tv_exp_f32 v49, v49\n\tv_exp_f32 v50, v50\n\tv_exp_f32 v51, v51\n\t"
            "v_exp_f32 v52, v52\n\tv_exp_f32 v53, v53\n\tv_exp_f32 v54, v54\n\tv_exp_f32 v55, v55\n\tv_exp_f32 v56, v56\n\tv_exp_f32 v57, v57\n\t"
            "v_exp_f32 v58, v58\n\tv_exp_f32 v59, v59\n\tv_exp_f32 v60, v60\n\tv_exp_f32 v61, v61\n\tv_exp_f32 v62, v62\n\tv_exp_f32 v63, v63\n\t"
            SO(4) RZ
            "buffer_load_dwordx4 v[176:179], v2, s[44:47], s4 offen offset:1024 sc1\n\tbuffer_load_dwordx4 v[180:183], v2, s[44:47], s4 offen offset:2048 sc1\n\tbuffer_load_dwordx4 v[184:187], v2, s[44:47], s5 offen offset:1024 sc1\n\tbuffer_load_dwordx4 v[188:191], v2, s[44:47], s5 offen offset:2048 sc1\n\tbuffer_load_dwordx4 v[192:195], v2, s[44:47], s6 offen offset:1024 sc1\n\tbuffer_load_dwordx4 v[196:199], v2, s[44:47], s6 offen offset:2048 sc1\n\t"
            "buffer_load_dwordx4 v[200:203], v2, s[44:47], s7 offen offset:1024 sc1\n\tbuffer_load_dwordx4 v[204:207], v2, s[44:47], s7 offen offset:2048 sc1\n\tbuffer_load_dwordx4 v[208:211], v2, s[44:47], s8 offen offset:1024 sc1\n\tbuffer_load_dwordx4 v[212:215], v2, s[44:47], s8 offen offset:2048 sc1\n\tv_add_f32 v32, 1.0, v32\n\tv_add_f32 v33, 1.0, v33\n\t"
            "v_add_f32 v34, 1.0, v34\n\tv_add_f32 v35, 1.0, v35\n\tv_add_f32 v36, 1.0, v36\n\tv_add_f32 v37, 1.0, v37\n\tv_add_f32 v38, 1.0, v38\n\tv_add_f32 v39, 1.0, v39\n\t"
            "v_add_f32 v40, 1.0, v40\n\tv_add_f32 v41, 1.0, v41\n\tv_add_f32 v42, 1.0, v42\n\tv_add_f32 v43, 1.0, v43\n\tv_add_f32 v44, 1.0, v44\n\tv_add_f32 v45, 1.0, v45\n\t"
            "v_add_f32 v46, 1.0, v46\n\tv_add_f32 v47, 1.0, v47\n\tv_add_f32 v48, 1.0, v48\n\tv_add_f32 v49, 1.0, v49\n\tv_add_f32 v50, 1.0, v50\n\tv_add_f32 v51, 1.0, v51\n\t"
            "v_add_f32 v52, 1.0, v52\n\tv_add_f32 v53, 1.0, v53\n\tv_add_f32 v54, 1.0, v54\n\tv_add_f32 v55, 1.0, v55\n\tv_add_f32 v56, 1.0, v56\n\tv_add_f32 v57, 1.0, v57\n\t"
            "v_add_f32 v58, 1.0, v58\n\tv_add_f32 v59, 1.0, v59\n\tv_add_f32 v60, 1.0, v60\n\tv_add_f32 v61, 1.0, v61\n\tv_add_f32 v62, 1.0, v62\n\tv_add_f32 v63, 1.0, v63\n\t"
            "v_rcp_f32 v32, v32\n\tv_rcp_f32 v33, v33\n\tv_rcp_f32 v34, v34\n\tv_rcp_f32 v35, v35\n\tv_rcp_f32 v36, v36\n\tv_rcp_f32 v37, v37\n\t"
            "v_rcp_f32 v38, v38\n\tv_rcp_f32 v39, v39\n\tv_rcp_f32 v40, v40\n\tv_rcp_f32 v41, v41\n\tv_rcp_f32 v42, v42\n\tv_rcp_f32 v43, v43\n\t"
            "v_rcp_f32 v44, v44\n\tv_rcp_f32 v45, v45\n\tv_rcp_f32 v46, v46\n\tv_rcp_f32 v47, v47\n\tv_rcp_f32 v48, v48\n\tv_rcp_f32 v49, v49\n\t"
            "v_rcp_f32 v50, v50\n\tv_rcp_f32 v51, v51\n\tv_rcp_f32 v52, v52\n\tv_rcp_f32 v53, v53\n\tv_rcp_f32 v54, v54\n\tv_rcp_f32 v55, v55\n\t"
            "v_rcp_f32 v56, v56\n\tv_rcp_f32 v57, v57\n\tv_rcp_f32 v58, v58\n\tv_rcp_f32 v59, v59\n\tv_rcp_f32 v60, v60\n\tv_rcp_f32 v61, v61\n\t"
            "v_rcp_f32 v62, v62\n\tv_rcp_f32 v63, v63\n\tv_mul_f32 v32, v32, v96\n\tv_mul_f32 v33, v33, v97\n\tv_mul_f32 v34, v34, v98\n\tv_mul_f32 v35, v35, v99\n\t"
            "v_mul_f32 v36, v36, v100\n\tv_mul_f32 v37, v37, v101\n\tv_mul_f32 v38, v38, v102\n\tv_mul_f32 v39, v39, v103\n\tv_mul_f32 v40, v40, v104\n\tv_mul_f32 v41, v41, v105\n\t"
            "v_mul_f32 v42, v42, v106\n\tv_mul_f32 v43, v43, v107\n\tv_mul_f32 v44, v44, v108\n\tv_mul_f32 v45, v45, v109\n\tv_mul_f32 v46, v46, v110\n\tv_mul_f32 v47, v47, v111\n\t"
            "ds_read_b128 v[16:19], v8 offset:48\n\tds_read_b64_tr_b16 v[128:129], v4 offset:40960\n\tds_read_b64_tr_b16 v[130:131], v4 offset:41472\n\tds_read_b64_tr_b16 v[132:133], v5 offset:40960\n\tds_read_b64_tr_b16 v[134:135], v5 offset:41472\n\tds_read_b64_tr_b16 v[136:137], v6 offset:40960\n\t"
            "ds_read_b64_tr_b16 v[138:139], v6 offset:41472\n\tds_read_b64_tr_b16 v[140:141], v7 offset:40960\n\tds_read_b64_tr_b16 v[142:143], v7 offset:41472\n\tds_read_b64_tr_b16 v[144:145], v4 offset:41984\n\tds_read_b64_tr_b16 v[146:147], v4 offset:42496\n\tds_read_b64_tr_b16 v[148:149], v5 offset:41984\n\t"
            "ds_read_b64_tr_b16 v[150:151], v5 offset:42496\n\tds_read_b64_tr_b16 v[152:153], v6 offset:41984\n\tds_read_b64_tr_b16 v[154:155], v6 offset:42496\n\tds_read_b64_tr_b16 v[156:157], v7 offset:41984\n\tds_read_b64_tr_b16 v[158:159], v7 offset:42496\n\tv_mul_f32 v48, v48, v112\n\t"
            "v_mul_f32 v49, v49, v113\n\tv_mul_f32 v50, v50, v114\n\tv_mul_f32 v51, v51, v115\n\tv_mul_f32 v52, v52, v116\n\tv_mul_f32 v53, v53, v117\n\tv_mul_f32 v54, v54, v118\n\t"
            "v_mul_f32 v55, v55, v119\n\tv_mul_f32 v56, v56, v120\n\tv_mul_f32 v57, v57, v121\n\tv_mul_f32 v58, v58, v122\n\tv_mul_f32 v59, v59, v123\n\tv_mul_f32 v60, v60, v124\n\t"
            "v_mul_f32 v61, v61, v125\n\tv_mul_f32 v62, v62, v126\n\tv_mul_f32 v63, v63, v127\n\t"
            SO(3)
            "s_waitcnt lgkmcnt(15)\n\tv_min3_u32 v16, v16, v17, v18\n\tv_min_u32 v16, v16, v19\n\tv_cmp_eq_u32 vcc, 0, v16\n\ts_cbranch_vccnz 62f\n\t63:\n"
            PRIO
            "s_waitcnt vmcnt(8)\n\tv_max3_u32 v12, v176, v178, v180\n\tv_max_u32 v12, v12, v182\n\tv_cmp_eq_u32 vcc, -1, v12\n\ts_cbranch_vccnz 100f\n\t200:\n"
            SO(5)
            "s_waitcnt lgkmcnt(8)\n\tv_mfma_f32_32x32x16_bf16 v[96:111], v[128:131], v[176:179], 0\n\tds_read_b64_tr_b16 v[160:161], v4 offset:43008\n\tds_read_b64_tr_b16 v[162:163], v4 offset:43520\n\tv_mfma_f32_32x32x16_bf16 v[112:127], v[132:135], v[176:179], 0\n\tds_read_b64_tr_b16 v[164:165], v5 offset:43008\n\t"
            "ds_read_b64_tr_b16 v[166:167], v5 offset:43520\n\tbuffer_load_dwordx4 v[216:219], v2, s[44:47], s9 offen offset:1024 sc1\n\tv_mfma_f32_32x32x16_bf16 v[96:111], v[136:139], v[180:183], v[96:111]\n\tds_read_b64_tr_b16 v[168:169], v6 offset:43008\n\tds_read_b64_tr_b16 v[170:171], v6 offset:43520\n\tbuffer_load_dwordx4 v[220:223], v2, s[44:47], s9 offen offset:2048 sc1\n\t"
            "s_waitcnt vmcnt(8)\n\tv_max3_u32 v12, v184, v186, v188\n\tv_max_u32 v12, v12, v190\n\tv_cmp_eq_u32 vcc, -1, v12\n\tv_mfma_f32_32x32x16_bf16 v[112:127], v[140:143], v[180:183], v[112:127]\n\tds_read_b64_tr_b16 v[172:173], v7 offset:43008\n\t"
            "ds_read_b64_tr_b16 v[174:175], v7 offset:43520\n\ts_cbranch_vccnz 101f\n\t201:\ns_waitcnt lgkmcnt(8)\n\tv_mfma_f32_32x32x16_bf16 v[96:111], v[144:147], v[184:187], v[96:111]\n\tds_read_b64_tr_b16 v[128:129], v4 offset:44032\n\t"
            "ds_read_b64_tr_b16 v[130:131], v4 offset:44544\n\tv_mfma_f32_32x32x16_bf16 v[112:127], v[148:151], v[184:187], v[112:127]\n\tds_read_b64_tr_b16 v[132:133], v5 offset:44032\n\tds_read_b64_tr_b16 v[134:135], v5 offset:44544\n\tbuffer_load_dwordx4 v[224:227], v2, s[44:47], s10 offen offset:1024 sc1\n\tv_mfma_f32_32x32x16_bf16 v[96:111], v[152:155], v[188:191], v[96:111]\n\t"
            "ds_read_b64_tr_b16 v[136:137], v6 offset:44032\n\tds_read_b64_tr_b16 v[138:139], v6 offset:44544\n\tbuffer_load_dwordx4 v[228:231], v2, s[44:47], s10 offen offset:2048 sc1\n\ts_waitcnt vmcnt(8)\n\tv_max3_u32 v12, v192, v194, v196\n\tv_max_u32 v12, v12, v198\n\t"
            "v_cmp_eq_u32 vcc, -1, v12\n\tv_mfma_f32_32x32x16_bf16 v[112:127], v[156:159], v[188:191], v[112:127]\n\tds_read_b64_tr_b16 v[140:141], v7 offset:44032\n\tds_read_b64_tr_b16 v[142:143], v7 offset:44544\n\ts_cbranch_vccnz 102f\n\t202:\n"
            "s_waitcnt lgkmcnt(8)\n\tv_mfma_f32_32x32x16_bf16 v[96:111], v[160:163], v[192:195], v[96:111]\n\tds_read_b64_tr_b16 v[144:145], v4 offset:45056\n\tds_read_b64_tr_b16 v[146:147], v4 offset:45568\n\tv_mfma_f32_32x32x16_bf16 v[112:127], v[164:167], v[192:195], v[112:127]\n\tds_read_b64_tr_b16 v[148:149], v5 offset:45056\n\t"
            "ds_read_b64_tr_b16 v[150:151], v5 offset:45568\n\tbuffer_load_dwordx4 v[232:235], v2, s[44:47], s11 offen offset:1024 sc1\n\tv_mfma_f32_32x32x16_bf16 v[96:111], v[168:171], v[196:199], v[96:111]\n\tds_read_b64_tr_b16 v[152:153], v6 offset:45056\n\tds_read_b64_tr_b16 v[154:155], v6 offset:45568\n\tbuffer_load_dwordx4 v[236:239], v2, s[44:47], s11 offen offset:2048 sc1\n\t"
            "s_waitcnt vmcnt(8)\n\tv_max3_u32 v12, v200, v202, v204\n\tv_max_u32 v12, v12, v206\n\tv_cmp_eq_u32 vcc, -1, v12\n\tv_mfma_f32_32x32x16_bf16 v[112:127], v[172:175], v[196:199], v[112:127]\n\tds_read_b64_tr_b16 v[156:157], v7 offset:45056\n\t"
            "ds_read_b64_tr_b16 v[158:159], v7 offset:45568\n\ts_cbranch_vccnz 103f\n\t203:\ns_waitcnt lgkmcnt(8)\n\tv_mfma_f32_32x32x16_bf16 v[96:111], v[128:131], v[200:203], v[96:111]\n\tds_read_b64_tr_b16 v[160:161], v4 offset:46080\n\t"
            "ds_read_b64_tr_b16 v[162:163], v4 offset:46592\n\tv_mfma_f32_32x32x16_bf16 v[112:127], v[132:135], v[200:203], v[112:127]\n\tds_read_b64_tr_b16 v[164:165], v5 offset:46080\n\tds_read_b64_tr_b16 v[166:167], v5 offset:46592\n\tds_read_b64 v[14:15], v8 offset:40\n\tbuffer_load_dwordx4 v[176:179], v2, s[44:47], s12 offen offset:1024 sc1\n\t"
            "v_mfma_f32_32x32x16_bf16 v[96:111], v[136:139], v[204:207], v[96:111]\n\tds_read_b64_tr_b16 v[168:169], v6 offset:46080\n\tds_read_b64_tr_b16 v[170:171], v6 offset:46592\n\tbuffer_load_dwordx4 v[180:183], v2, s[44:47], s12 offen offset:2048 sc1\n\ts_waitcnt vmcnt(8)\n\tv_max3_u32 v12, v208, v210, v212\n\t"
            "v_max_u32 v12, v12, v214\n\tv_cmp_eq_u32 vcc, -1, v12\n\tv_mfma_f32_32x32x16_bf16 v[112:127], v[140:143], v[204:207], v[112:127]\n\tds_read_b64_tr_b16 v[172:173], v7 offset:46080\n\tds_read_b64_tr_b16 v[174:175], v7 offset:46592\n\ts_cbranch_vccnz 104f\n\t"
            "204:\ns_waitcnt lgkmcnt(9)\n\tv_mfma_f32_32x32x16_bf16 v[96:111], v[144:147], v[208:211], v[96:111]\n\tds_read_b64_tr_b16 v[128:129], v4 offset:47104\n\tds_read_b64_tr_b16 v[130:131], v4 offset:47616\n\tv_mfma_f32_32x32x16_bf16 v[112:127], v[148:151], v[208:211], v[112:127]\n\t"
            "ds_read_b64_tr_b16 v[132:133], v5 offset:47104\n\tds_read_b64_tr_b16 v[134:135], v5 offset:47616\n\tbuffer_load_dwordx4 v[184:187], v2, s[44:47], s13 offen offset:1024 sc1\n\tv_mfma_f32_32x32x16_bf16 v[96:111], v[152:155], v[212:215], v[96:111]\n\tds_read_b64_tr_b16 v[136:137], v6 offset:47104\n\tds_read_b64_tr_b16 v[138:139], v6 offset:47616\n\t"
            "buffer_load_dwordx4 v[188:191], v2, s[44:47], s13 offen offset:2048 sc1\n\ts_waitcnt vmcnt(8)\n\tv_max3_u32 v12, v216, v218, v220\n\tv_max_u32 v12, v12, v222\n\tv_cmp_eq_u32 vcc, -1, v12\n\tv_mfma_f32_32x32x16_bf16 v[112:127], v[156:159], v[212:215], v[112:127]\n\t"
            "ds_read_b64_tr_b16 v[140:141], v7 offset:47104\n\tds_read_b64_tr_b16 v[142:143], v7 offset:47616\n\ts_cbranch_vccnz 105f\n\t205:\ns_waitcnt lgkmcnt(8)\n\tv_mfma_f32_32x32x16_bf16 v[96:111], v[160:163], v[216:219], v[96:111]\n\t"
            "v_min_u32 v14, v14, v15\n\tv_cmp_ge_u32 vcc, 1, v14\n\tds_read_b64_tr_b16 v[144:145], v4 offset:48128\n\tds_read_b64_tr_b16 v[146:147], v4 offset:48640\n\tv_mfma_f32_32x32x16_bf16 v[112:127], v[164:167], v[216:219], v[112:127]\n\ts_cbranch_vccnz 5f\n\t"
            "6:\nds_read_b64_tr_b16 v[148:149], v5 offset:48128\n\tds_read_b64_tr_b16 v[150:151], v5 offset:48640\n\tbuffer_load_dwordx4 v[192:195], v2, s[44:47], s4 offen offset:0 sc1\n\tv_mfma_f32_32x32x16_bf16 v[96:111], v[168:171], v[220:223], v[96:111]\n\tds_read_b64_tr_b16 v[152:153], v6 offset:48128\n\t"
            "ds_read_b64_tr_b16 v[154:155], v6 offset:48640\n\tbuffer_load_dwordx4 v[196:199], v2, s[44:47], s4 offen offset:3072 sc1\n\ts_waitcnt vmcnt(8)\n\tv_max3_u32 v12, v224, v226, v228\n\tv_max_u32 v12, v12, v230\n\tv_cmp_eq_u32 vcc, -1, v12\n\t"
            "v_mfma_f32_32x32x16_bf16 v[112:127], v[172:175], v[220:223], v[112:127]\n\tds_read_b64_tr_b16 v[156:157], v7 offset:48128\n\tds_read_b64_tr_b16 v[158:159], v7 offset:48640\n\ts_cbranch_vccnz 106f\n\t206:\ns_waitcnt lgkmcnt(8)\n\t"
            "v_mfma_f32_32x32x16_bf16 v[96:111], v[128:131], v[224:227], v[96:111]\n\tds_read_b64_tr_b16 v[160:161], v4 offset:49152\n\tds_read_b64_tr_b16 v[162:163], v4 offset:49664\n\tv_mfma_f32_32x32x16_bf16 v[112:127], v[132:135], v[224:227], v[112:127]\n\tds_read_b64_tr_b16 v[164:165], v5 offset:49152\n\tds_read_b64_tr_b16 v[166:167], v5 offset:49664\n\t"
            "buffer_load_dwordx4 v[200:203], v2, s[44:47], s5 offen offset:0 sc1\n\tv_mfma_f32_32x32x16_bf16 v[96:111], v[136:139], v[228:231], v[96:111]\n\tds_read_b64_tr_b16 v[168:169], v6 offset:49152\n\tds_read_b64_tr_b16 v[170:171], v6 offset:49664\n\tbuffer_load_dwordx4 v[204:207], v2, s[44:47], s5 offen offset:3072 sc1\n\ts_waitcnt vmcnt(8)\n\t"
            "v_max3_u32 v12, v232, v234, v236\n\tv_max_u32 v12, v12, v238\n\tv_cmp_eq_u32 vcc, -1, v12\n\tv_mfma_f32_32x32x16_bf16 v[112:127], v[140:143], v[228:231], v[112:127]\n\tds_read_b64_tr_b16 v[172:173], v7 offset:49152\n\tds_read_b64_tr_b16 v[174:175], v7 offset:49664\n\t"
            "s_cbranch_vccnz 107f\n\t207:\ns_waitcnt lgkmcnt(8)\n\tv_mfma_f32_32x32x16_bf16 v[96:111], v[144:147], v[232:235], v[96:111]\n\tds_read_b64_tr_b16 v[128:129], v4 offset:50176\n\tds_read_b64_tr_b16 v[130:131], v4 offset:50688\n\t"
            "v_mfma_f32_32x32x16_bf16 v[112:127], v[148:151], v[232:235], v[112:127]\n\tds_read_b64_tr_b16 v[132:133], v5 offset:50176\n\tds_read_b64_tr_b16 v[134:135], v5 offset:50688\n\tbuffer_load_dwordx4 v[208:211], v2, s[44:47], s6 offen offset:0 sc1\n\tv_mfma_f32_32x32x16_bf16 v[96:111], v[152:155], v[236:239], v[96:111]\n\tds_read_b64_tr_b16 v[136:137], v6 offset:50176\n\t"
            "ds_read_b64_tr_b16 v[138:139], v6 offset:50688\n\tbuffer_load_dwordx4 v[212:215], v2, s[44:47], s6 offen offset:3072 sc1\n\ts_waitcnt vmcnt(8)\n\tv_max3_u32 v12, v176, v178, v180\n\tv_max_u32 v12, v12, v182\n\tv_cmp_eq_u32 vcc, -1, v12\n\t"
            "v_mfma_f32_32x32x16_bf16 v[112:127], v[156:159], v[236:239], v[112:127]\n\tds_read_b64_tr_b16 v[140:141], v7 offset:50176\n\tds_read_b64_tr_b16 v[142:143], v7 offset:50688\n\ts_cbranch_vccnz 108f\n\t208:\ns_waitcnt lgkmcnt(8)\n\t"
            "v_mfma_f32_32x32x16_bf16 v[96:111], v[160:163], v[176:179], v[96:111]\n\tds_read_b64_tr_b16 v[144:145], v4 offset:0\n\tds_read_b64_tr_b16 v[146:147], v4 offset:512\n\tv_mfma_f32_32x32x16_bf16 v[112:127], v[164:167], v[176:179], v[112:127]\n\tds_read_b64_tr_b16 v[148:149], v5 offset:0\n\tds_read_b64_tr_b16 v[150:151], v5 offset:512\n\t"
            "buffer_load_dwordx4 v[216:219], v2, s[44:47], s7 offen offset:0 sc1\n\tv_mfma_f32_32x32x16_bf16 v[96:111], v[168:171], v[180:183], v[96:111]\n\tds_read_b64_tr_b16 v[152:153], v6 offset:0\n\tds_read_b64_tr_b16 v[154:155], v6 offset:512\n\tbuffer_load_dwordx4 v[220:223], v2, s[44:47], s7 offen offset:3072 sc1\n\ts_waitcnt vmcnt(8)\n\t"
            "v_max3_u32 v12, v184, v186, v188\n\tv_max_u32 v12, v12, v190\n\tv_cmp_eq_u32 vcc, -1, v12\n\tv_mfma_f32_32x32x16_bf16 v[112:127], v[172:175], v[180:183], v[112:127]\n\tds_read_b64_tr_b16 v[156:157], v7 offset:0\n\tds_read_b64_tr_b16 v[158:159], v7 offset:512\n\t"
            "s_cbranch_vccnz 109f\n\t209:\ns_waitcnt lgkmcnt(8)\n\tv_mfma_f32_32x32x16_bf16 v[96:111], v[128:131], v[184:187], v[96:111]\n\tds_read_b64_tr_b16 v[160:161], v4 offset:1024\n\tds_read_b64_tr_b16 v[162:163], v4 offset:1536\n\t"
            "v_mfma_f32_32x32x16_bf16 v[112:127], v[132:135], v[184:187], v[112:127]\n\tds_read_b64_tr_b16 v[164:165], v5 offset:1024\n\tds_read_b64_tr_b16 v[166:167], v5 offset:1536\n\tbuffer_load_dwordx4 v[224:227], v2, s[44:47], s8 offen offset:0 sc1\n\tv_mfma_f32_32x32x16_bf16 v[96:111], v[136:139], v[188:191], v[96:111]\n\tds_read_b64_tr_b16 v[168:169], v6 offset:1024\n\t"
            "ds_read_b64_tr_b16 v[170:171], v6 offset:1536\n\tbuffer_load_dwordx4 v[228:231], v2, s[44:47], s8 offen offset:3072 sc1\n\ts_waitcnt vmcnt(8)\n\tv_max3_u32 v12, v192, v194, v196\n\tv_max_u32 v12, v12, v198\n\tv_cmp_eq_u32 vcc, -1, v12\n\t"
            "v_mfma_f32_32x32x16_bf16 v[112:127], v[140:143], v[188:191], v[112:127]\n\tds_read_b64_tr_b16 v[172:173], v7 offset:1024\n\tds_read_b64_tr_b16 v[174:175], v7 offset:1536\n\ts_cbranch_vccnz 110f\n\t210:\ns_waitcnt lgkmcnt(8)\n\t"
            "v_mfma_f32_32x32x16_bf16 v[96:111], v[144:147], v[192:195], v[96:111]\n\tds_read_b64_tr_b16 v[128:129], v4 offset:2048\n\tds_read_b64_tr_b16 v[130:131], v4 offset:2560\n\tv_mfma_f32_32x32x16_bf16 v[112:127], v[148:151], v[192:195], v[112:127]\n\tds_read_b64_tr_b16 v[132:133], v5 offset:2048\n\tds_read_b64_tr_b16 v[134:135], v5 offset:2560\n\t"
            "buffer_load_dwordx4 v[232:235], v2, s[44:47], s9 offen offset:0 sc1\n\tv_mfma_f32_32x32x16_bf16 v[96:111], v[152:155], v[196:199], v[96:111]\n\tds_read_b64_tr_b16 v[136:137], v6 offset:2048\n\tds_read_b64_tr_b16 v[138:139], v6 offset:2560\n\tbuffer_load_dwordx4 v[236:239], v2, s[44:47], s9 offen offset:3072 sc1\n\ts_waitcnt vmcnt(8)\n\t"
            "v_max3_u32 v12, v200, v202, v204\n\tv_max_u32 v12, v12, v206\n\tv_cmp_eq_u32 vcc, -1, v12\n\tv_mfma_f32_32x32x16_bf16 v[112:127], v[156:159], v[196:199], v[112:127]\n\tds_read_b64_tr_b16 v[140:141], v7 offset:2048\n\tds_read_b64_tr_b16 v[142:143], v7 offset:2560\n\t"
            "s_cbranch_vccnz 111f\n\t211:\ns_waitcnt lgkmcnt(8)\n\tv_mfma_f32_32x32x16_bf16 v[96:111], v[160:163], v[200:203], v[96:111]\n\tds_read_b64_tr_b16 v[144:145], v4 offset:3072\n\tds_read_b64_tr_b16 v[146:147], v4 offset:3584\n\t"
            "v_mfma_f32_32x32x16_bf16 v[112:127], v[164:167], v[200:203], v[112:127]\n\tds_read_b64_tr_b16 v[148:149], v5 offset:3072\n\tds_read_b64_tr_b16 v[150:151], v5 offset:3584\n\tbuffer_load_dwordx4 v[176:179], v2, s[44:47], s10 offen offset:0 sc1\n\tv_mfma_f32_32x32x16_bf16 v[96:111], v[168:171], v[204:207], v[96:111]\n\tds_read_b64_tr_b16 v[152:153], v6 offset:3072\n\t"
            "ds_read_b64_tr_b16 v[154:155], v6 offset:3584\n\tbuffer_load_dwordx4 v[180:183], v2, s[44:47], s10 offen offset:3072 sc1\n\ts_waitcnt vmcnt(8)\n\tv_max3_u32 v12, v208, v210, v212\n\tv_max_u32 v12, v12, v214\n\tv_cmp_eq_u32 vcc, -1, v12\n\t"
            "v_mfma_f32_32x32x16_bf16 v[112:127], v[172:175], v[204:207], v[112:127]\n\tds_read_b64_tr_b16 v[156:157], v7 offset:3072\n\tds_read_b64_tr_b16 v[158:159], v7 offset:3584\n\ts_cbranch_vccnz 112f\n\t212:\ns_waitcnt lgkmcnt(8)\n\t"
            "v_mfma_f32_32x32x16_bf16 v[96:111], v[128:131], v[208:211], v[96:111]\n\tds_read_b64_tr_b16 v[160:161], v4 offset:4096\n\tds_read_b64_tr_b16 v[162:163], v4 offset:4608\n\tv_mfma_f32_32x32x16_bf16 v[112:127], v[132:135], v[208:211], v[112:127]\n\tds_read_b64_tr_b16 v[164:165], v5 offset:4096\n\tds_read_b64_tr_b16 v[166:167], v5 offset:4608\n\t"
            "buffer_load_dwordx4 v[184:187], v2, s[44:47], s11 offen offset:0 sc1\n\tv_mfma_f32_32x32x16_bf16 v[96:111], v[136:139], v[212:215], v[96:111]\n\tds_read_b64_tr_b16 v[168:169], v6 offset:4096\n\tds_read_b64_tr_b16 v[170:171], v6 offset:4608\n\tbuffer_load_dwordx4 v[188:191], v2, s[44:47], s11 offen offset:3072 sc1\n\ts_waitcnt vmcnt(8)\n\t"
            "v_max3_u32 v12, v216, v218, v220\n\tv_max_u32 v12, v12, v222\n\tv_cmp_eq_u32 vcc, -1, v12\n\tv_mfma_f32_32x32x16_bf16 v[112:127], v[140:143], v[212:215], v[112:127]\n\tds_read_b64_tr_b16 v[172:173], v7 offset:4096\n\tds_read_b64_tr_b16 v[174:175], v7 offset:4608\n\t"
            "s_cbranch_vccnz 113f\n\t213:\ns_waitcnt lgkmcnt(8)\n\tv_mfma_f32_32x32x16_bf16 v[96:111], v[144:147], v[216:219], v[96:111]\n\tds_read_b64_tr_b16 v[128:129], v4 offset:5120\n\tds_read_b64_tr_b16 v[130:131], v4 offset:5632\n\t"
            "v_mfma_f32_32x32x16_bf16 v[112:127], v[148:151], v[216:219], v[112:127]\n\tds_read_b64_tr_b16 v[132:133], v5 offset:5120\n\tds_read_b64_tr_b16 v[134:135], v5 offset:5632\n\tbuffer_load_dwordx4 v[192:195], v2, s[44:47], s12 offen offset:0 sc1\n\tv_mfma_f32_32x32x16_bf16 v[96:111], v[152:155], v[220:223], v[96:111]\n\tds_read_b64_tr_b16 v[136:137], v6 offset:5120\n\t"
            "ds_read_b64_tr_b16 v[138:139], v6 offset:5632\n\tbuffer_load_dwordx4 v[196:199], v2, s[44:47], s12 offen offset:3072 sc1\n\ts_waitcnt vmcnt(8)\n\tv_max3_u32 v12, v224, v226, v228\n\tv_max_u32 v12, v12, v230\n\tv_cmp_eq_u32 vcc, -1, v12\n\t"
            "v_mfma_f32_32x32x16_bf16 v[112:127], v[156:159], v[220:223], v[112:127]\n\tds_read_b64_tr_b16 v[140:141], v7 offset:5120\n\tds_read_b64_tr_b16 v[142:143], v7 offset:5632\n\ts_cbranch_vccnz 114f\n\t214:\ns_waitcnt lgkmcnt(8)\n\t"
            "v_mfma_f32_32x32x16_bf16 v[96:111], v[160:163], v[224:227], v[96:111]\n\tds_read_b64_tr_b16 v[144:145], v4 offset:6144\n\tds_read_b64_tr_b16 v[146:147], v4 offset:6656\n\tv_mfma_f32_32x32x16_bf16 v[112:127], v[164:167], v[224:227], v[112:127]\n\tds_read_b64_tr_b16 v[148:149], v5 offset:6144\n\tds_read_b64_tr_b16 v[150:151], v5 offset:6656\n\t"
            "buffer_load_dwordx4 v[200:203], v2, s[44:47], s13 offen offset:0 sc1\n\tv_mfma_f32_32x32x16_bf16 v[96:111], v[168:171], v[228:231], v[96:111]\n\tds_read_b64_tr_b16 v[152:153], v6 offset:6144\n\tds_read_b64_tr_b16 v[154:155], v6 offset:6656\n\tbuffer_load_dwordx4 v[204:207], v2, s[44:47], s13 offen offset:3072 sc1\n\ts_waitcnt vmcnt(8)\n\t"
            "v_max3_u32 v12, v232, v234, v236\n\tv_max_u32 v12, v12, v238\n\tv_cmp_eq_u32 vcc, -1, v12\n\tv_mfma_f32_32x32x16_bf16 v[112:127], v[172:175], v[228:231], v[112:127]\n\tds_read_b64_tr_b16 v[156:157], v7 offset:6144\n\tds_read_b64_tr_b16 v[158:159], v7 offset:6656\n\t"
            "s_cbranch_vccnz 115f\n\t215:\ns_waitcnt lgkmcnt(8)\n\tv_mfma_f32_32x32x16_bf16 v[96:111], v[128:131], v[232:235], v[96:111]\n\tds_read_b64_tr_b16 v[160:161], v4 offset:7168\n\tds_read_b64_tr_b16 v[162:163], v4 offset:7680\n\t"
            "v_mfma_f32_32x32x16_bf16 v[112:127], v[132:135], v[232:235], v[112:127]\n\tds_read_b64_tr_b16 v[164:165], v5 offset:7168\n\tds_read_b64_tr_b16 v[166:167], v5 offset:7680\n\tv_mfma_f32_32x32x16_bf16 v[96:111], v[136:139], v[236:239], v[96:111]\n\tds_read_b64_tr_b16 v[168:169], v6 offset:7168\n\tds_read_b64_tr_b16 v[170:171], v6 offset:7680\n\t"
            "s_waitcnt vmcnt(6)\n\tv_max3_u32 v12, v176, v178, v180\n\tv_max_u32 v12, v12, v182\n\tv_cmp_eq_u32 vcc, -1, v12\n\tv_mfma_f32_32x32x16_bf16 v[112:127], v[140:143], v[236:239], v[112:127]\n\tds_read_b64_tr_b16 v[172:173], v7 offset:7168\n\t"
            "ds_read_b64_tr_b16 v[174:175], v7 offset:7680\n\ts_cbranch_vccnz 116f\n\t216:\ns_waitcnt lgkmcnt(8)\n\tv_mfma_f32_32x32x16_bf16 v[96:111], v[144:147], v[176:179], v[96:111]\n\tds_read_b64_tr_b16 v[128:129], v4 offset:8192\n\t"
            "ds_read_b64_tr_b16 v[130:131], v4 offset:8704\n\tv_mfma_f32_32x32x16_bf16 v[112:127], v[148:151], v[176:179], v[112:127]\n\tds_read_b64_tr_b16 v[132:133], v5 offset:8192\n\tds_read_b64_tr_b16 v[134:135], v5 offset:8704\n\tv_mfma_f32_32x32x16_bf16 v[96:111], v[152:155], v[180:183], v[96:111]\n\tds_read_b64_tr_b16 v[136:137], v6 offset:8192\n\t"
            "ds_read_b64_tr_b16 v[138:139], v6 offset:8704\n\ts_waitcnt vmcnt(4)\n\tv_max3_u32 v12, v184, v186, v188\n\tv_max_u32 v12, v12, v190\n\tv_cmp_eq_u32 vcc, -1, v12\n\tv_mfma_f32_32x32x16_bf16 v[112:127], v[156:159], v[180:183], v[112:127]\n\t"
            "ds_read_b64_tr_b16 v[140:141], v7 offset:8192\n\tds_read_b64_tr_b16 v[142:143], v7 offset:8704\n\ts_cbranch_vccnz 117f\n\t217:\ns_waitcnt lgkmcnt(8)\n\tv_mfma_f32_32x32x16_bf16 v[96:111], v[160:163], v[184:187], v[96:111]\n\t"
            "ds_read_b64_tr_b16 v[144:145], v4 offset:9216\n\tds_read_b64_tr_b16 v[146:147], v4 offset:9728\n\tv_mfma_f32_32x32x16_bf16 v[112:127], v[164:167], v[184:187], v[112:127]\n\tds_read_b64_tr_b16 v[148:149], v5 offset:9216\n\tds_read_b64_tr_b16 v[150:151], v5 offset:9728\n\tv_mfma_f32_32x32x16_bf16 v[96:111], v[168:171], v[188:191], v[96:111]\n\t"
            "ds_read_b64_tr_b16 v[152:153], v6 offset:9216\n\tds_read_b64_tr_b16 v[154:155], v6 offset:9728\n\ts_waitcnt vmcnt(2)\n\tv_max3_u32 v12, v192, v194, v196\n\tv_max_u32 v12, v12, v198\n\tv_cmp_eq_u32 vcc, -1, v12\n\t"
            "v_mfma_f32_32x32x16_bf16 v[112:127], v[172:175], v[188:191], v[112:127]\n\tds_read_b64_tr_b16 v[156:157], v7 offset:9216\n\tds_read_b64_tr_b16 v[158:159], v7 offset:9728\n\ts_cbranch_vccnz 118f\n\t218:\nds_read_b32 v16, v8 offset:64\n\t"
            "ds_read_b32 v17, v9 offset:1344\n\ts_waitcnt lgkmcnt(10)\n\tv_mfma_f32_32x32x16_bf16 v[96:111], v[128:131], v[192:195], v[96:111]\n\tv_mfma_f32_32x32x16_bf16 v[112:127], v[132:135], v[192:195], v[112:127]\n\tv_mfma_f32_32x32x16_bf16 v[96:111], v[136:139], v[196:199], v[96:111]\n\ts_waitcnt vmcnt(0)\n\t"
            "v_max3_u32 v12, v200, v202, v204\n\tv_max_u32 v12, v12, v206\n\tv_cmp_eq_u32 vcc, -1, v12\n\tv_mfma_f32_32x32x16_bf16 v[112:127], v[140:143], v[196:199], v[112:127]\n\ts_cbranch_vccnz 119f\n\t219:\n"
            "s_waitcnt lgkmcnt(2)\n\tv_mfma_f32_32x32x16_bf16 v[96:111], v[144:147], v[200:203], v[96:111]\n\ts_waitcnt lgkmcnt(0)\n\tv_cmp_ne_u32 s[74:75], 0, v17\n\tv_cmp_eq_u32 vcc, 0, v16\n\tv_mfma_f32_32x32x16_bf16 v[96:111], v[152:155], v[204:207], v[96:111]\n\t"
            "v_mfma_f32_32x32x16_bf16 v[112:127], v[148:151], v[200:203], v[112:127]\n\ts_or_b64 vcc, vcc, s[74:75]\n\tv_mfma_f32_32x32x16_bf16 v[112:127], v[156:159], v[204:207], v[112:127]\n\t"
            SO(6)
            "s_cbranch_scc1 7f\n\t8:\ns_nop 7\n\tv_fma_f32 v96, v96, v32, v64\n\tv_fma_f32 v97, v97, v33, v65\n\tv_fma_f32 v98, v98, v34, v66\n\t"
            "v_fma_f32 v99, v99, v35, v67\n\tv_fma_f32 v100, v100, v36, v68\n\tv_fma_f32 v101, v101, v37, v69\n\tv_fma_f32 v102, v102, v38, v70\n\tv_fma_f32 v103, v103, v39, v71\n\tv_fma_f32 v104, v104, v40, v72\n\t"
            "v_fma_f32 v105, v105, v41, v73\n\tv_fma_f32 v106, v106, v42, v74\n\tv_fma_f32 v107, v107, v43, v75\n\tv_fma_f32 v108, v108, v44, v76\n\tv_fma_f32 v109, v109, v45, v77\n\tv_fma_f32 v110, v110, v46, v78\n\t"
            "v_fma_f32 v111, v111, v47, v79\n\tv_cvt_pk_bf16_f32 v240, v96, v97\n\tv_cvt_pk_bf16_f32 v241, v98, v99\n\tv_cvt_pk_bf16_f32 v242, v100, v101\n\tv_cvt_pk_bf16_f32 v243, v102, v103\n\tv_cvt_pk_bf16_f32 v244, v104, v105\n\t"
            "v_cvt_pk_bf16_f32 v245, v106, v107\n\tv_cvt_pk_bf16_f32 v246, v108, v109\n\tv_cvt_pk_bf16_f32 v247, v110, v111\n\tv_permlane32_swap_b32 v240, v242\n\tv_permlane32_swap_b32 v241, v243\n\tv_permlane32_swap_b32 v244, v246\n\t"
            "v_permlane32_swap_b32 v245, v247\n\ts_nop 0\n\tv_permlane16_swap_b32 v240, v244\n\tv_permlane16_swap_b32 v241, v245\n\tv_permlane16_swap_b32 v242, v246\n\tv_permlane16_swap_b32 v243, v247\n\t"
            "buffer_store_dwordx4 v[240:243], v3, s[52:55], 0 offen offset:0\n\tbuffer_store_dwordx4 v[244:247], v11, s[52:55], 0 offen offset:0\n\tv_fma_f32 v112, v112, v48, v80\n\tv_fma_f32 v113, v113, v49, v81\n\tv_fma_f32 v114, v114, v50, v82\n\tv_fma_f32 v115, v115, v51, v83\n\t"
            "v_fma_f32 v116, v116, v52, v84\n\tv_fma_f32 v117, v117, v53, v85\n\tv_fma_f32 v118, v118, v54, v86\n\tv_fma_f32 v119, v119, v55, v87\n\tv_fma_f32 v120, v120, v56, v88\n\tv_fma_f32 v121, v121, v57, v89\n\t"
            "v_fma_f32 v122, v122, v58, v90\n\tv_fma_f32 v123, v123, v59, v91\n\tv_fma_f32 v124, v124, v60, v92\n\tv_fma_f32 v125, v125, v61, v93\n\tv_fma_f32 v126, v126, v62, v94\n\tv_fma_f32 v127, v127, v63, v95\n\t"
            "v_cvt_pk_bf16_f32 v248, v112, v113\n\tv_cvt_pk_bf16_f32 v249, v114, v115\n\tv_cvt_pk_bf16_f32 v250, v116, v117\n\tv_cvt_pk_bf16_f32 v251, v118, v119\n\tv_cvt_pk_bf16_f32 v252, v120, v121\n\tv_cvt_pk_bf16_f32 v253, v122, v123\n\t"
            "v_cvt_pk_bf16_f32 v254, v124, v125\n\tv_cvt_pk_bf16_f32 v255, v126, v127\n\tv_permlane32_swap_b32 v248, v250\n\tv_permlane32_swap_b32 v249, v251\n\tv_permlane32_swap_b32 v252, v254\n\tv_permlane32_swap_b32 v253, v255\n\t"
            "s_nop 0\n\tv_permlane16_swap_b32 v248, v252\n\tv_permlane16_swap_b32 v249, v253\n\tv_permlane16_swap_b32 v250, v254\n\tv_permlane16_swap_b32 v251, v255\n\tbuffer_store_dwordx4 v[248:251], v3, s[52:55], 0 offen offset:64\n\t"
            "buffer_store_dwordx4 v[252:255], v11, s[52:55], 0 offen offset:64\n\t9:\n"
            RCNT DUMP
            "s_endpgm\n\t.LG0:\nv_mul_f32 v20, v20, v21\n\tv_mul_f32 v23, 0xbfb8aa3b, v21\n\tv_mul_f32 v22, 0xbfb8aa3b, v20\n\tv_permlane16_swap_b32 v240, v244\n\t"
            "v_permlane16_swap_b32 v241, v245\n\tv_permlane16_swap_b32 v242, v246\n\tv_permlane16_swap_b32 v243, v247\n\tv_permlane16_swap_b32 v248, v252\n\tv_permlane16_swap_b32 v249, v253\n\tv_permlane16_swap_b32 v250, v254\n\t"
            "v_permlane16_swap_b32 v251, v255\n\tv_permlane32_swap_b32 v240, v242\n\tv_permlane32_swap_b32 v241, v243\n\tv_permlane32_swap_b32 v244, v246\n\tv_permlane32_swap_b32 v245, v247\n\tv_permlane32_swap_b32 v248, v250\n\t"
            "v_permlane32_swap_b32 v249, v251\n\tv_permlane32_swap_b32 v252, v254\n\tv_permlane32_swap_b32 v253, v255\n\tv_lshlrev_b32 v64, 16, v240\n\tv_and_b32 v65, 0xffff0000, v240\n\tv_lshlrev_b32 v66, 16, v241\n\t"
            "v_and_b32 v67, 0xffff0000, v241\n\tv_lshlrev_b32 v68, 16, v242\n\tv_and_b32 v69, 0xffff0000, v242\n\tv_lshlrev_b32 v70, 16, v243\n\tv_and_b32 v71, 0xffff0000, v243\n\tv_lshlrev_b32 v72, 16, v244\n\t"
            "v_and_b32 v73, 0xffff0000, v244\n\tv_lshlrev_b32 v74, 16, v245\n\tv_and_b32 v75, 0xffff0000, v245\n\tv_lshlrev_b32 v76, 16, v246\n\tv_and_b32 v77, 0xffff0000, v246\n\tv_lshlrev_b32 v78, 16, v247\n\t"
            "v_and_b32 v79, 0xffff0000, v247\n\tv_lshlrev_b32 v80, 16, v248\n\tv_and_b32 v81, 0xffff0000, v248\n\tv_lshlrev_b32 v82, 16, v249\n\tv_and_b32 v83, 0xffff0000, v249\n\tv_lshlrev_b32 v84, 16, v250\n\t"
            "v_and_b32 v85, 0xffff0000, v250\n\tv_lshlrev_b32 v86, 16, v251\n\tv_and_b32 v87, 0xffff0000, v251\n\tv_lshlrev_b32 v88, 16, v252\n\tv_and_b32 v89, 0xffff0000, v252\n\tv_lshlrev_b32 v90, 16, v253\n\t"
            "v_and_b32 v91, 0xffff0000, v253\n\tv_lshlrev_b32 v92, 16, v254\n\tv_and_b32 v93, 0xffff0000, v254\n\tv_lshlrev_b32 v94, 16, v255\n\tv_and_b32 v95, 0xffff0000, v255\n\tv_fma_f32 v32, v64, v23, v22\n\t"
            "v_fma_f32 v33, v65, v23, v22\n\tv_fma_f32 v34, v66, v23, v22\n\tv_fma_f32 v35, v67, v23, v22\n\tv_fma_f32 v36, v68, v23, v22\n\tv_fma_f32 v37, v69, v23, v22\n\tv_fma_f32 v38, v70, v23, v22\n\t"
            "v_fma_f32 v39, v71, v23, v22\n\tv_fma_f32 v40, v72, v23, v22\n\tv_fma_f32 v41, v73, v23, v22\n\tv_fma_f32 v42, v74, v23, v22\n\tv_fma_f32 v43, v75, v23, v22\n\tv_fma_f32 v44, v76, v23, v22\n\t"
            "v_fma_f32 v45, v77, v23, v22\n\tv_fma_f32 v46, v78, v23, v22\n\tv_fma_f32 v47, v79, v23, v22\n\tv_fma_f32 v48, v80, v23, v22\n\tv_fma_f32 v49, v81, v23, v22\n\tv_fma_f32 v50, v82, v23, v22\n\t"
            "v_fma_f32 v51, v83, v23, v22\n\tv_fma_f32 v52, v84, v23, v22\n\tv_fma_f32 v53, v85, v23, v22\n\tv_fma_f32 v54, v86, v23, v22\n\tv_fma_f32 v55, v87, v23, v22\n\tv_fma_f32 v56, v88, v23, v22\n\t"
            "v_fma_f32 v57, v89, v23, v22\n\tv_fma_f32 v58, v90, v23, v22\n\tv_fma_f32 v59, v91, v23, v22\n\tv_fma_f32 v60, v92, v23, v22\n\tv_fma_f32 v61, v93, v23, v22\n\tv_fma_f32 v62, v94, v23, v22\n\t"
            "v_fma_f32 v63, v95, v23, v22\n\ts_setpc_b64 s[76:77]\n\t7:\nds_read_b32 v16, v8 offset:64\n\tds_read_b32 v17, v9 offset:1344\n\ts_waitcnt lgkmcnt(0)\n\t"
            "v_cmp_ne_u32 s[74:75], 0, v17\n\tv_cmp_eq_u32 vcc, 0, v16\n\ts_cbranch_vccz 71f\n\ts_sleep 1\n\ts_sub_u32 s68, s68, 1\n\ts_cbranch_scc0 7b\n\t"
            "s_branch 9b\n\t71:\nv_lshrrev_b32 v12, 5, v2\n\tv_and_b32 v12, 16, v12\n\tv_add_u32 v12, v12, v17\n\ts_mov_b64 exec, s[74:75]\n\t"
            "s_cbranch_execz 72f\n\tds_read_b128 v[32:35], v12 offset:0\n\tds_read_b128 v[36:39], v12 offset:32\n\tds_read_b128 v[40:43], v12 offset:64\n\tds_read_b128 v[44:47], v12 offset:96\n\tds_read_b128 v[48:51], v12 offset:128\n\t"
            "ds_read_b128 v[52:55], v12 offset:160\n\tds_read_b128 v[56:59], v12 offset:192\n\tds_read_b128 v[60:63], v12 offset:224\n\t72:\ns_mov_b64 exec, -1\n\ts_waitcnt lgkmcnt(0)\n\t"
            "s_branch 8b\n\t5:\ns_sleep 1\n\tds_read_b64 v[14:15], v8 offset:40\n\ts_waitcnt lgkmcnt(0)\n\tv_min_u32 v14, v14, v15\n\t"
            "v_cmp_ge_u32 vcc, 1, v14\n\ts_cbranch_vccz 61f\n\ts_sub_u32 s68, s68, 1\n\ts_cbranch_scc0 5b\n\ts_branch 9b\n\t61:\n"
            TRG2D
            "s_branch 6b\n\t62:\nds_read_b128 v[16:19], v8 offset:48\n\tds_read_b64_tr_b16 v[128:129], v4 offset:40960\n\tds_read_b64_tr_b16 v[130:131], v4 offset:41472\n\tds_read_b64_tr_b16 v[132:133], v5 offset:40960\n\t"
            "ds_read_b64_tr_b16 v[134:135], v5 offset:41472\n\tds_read_b64_tr_b16 v[136:137], v6 offset:40960\n\tds_read_b64_tr_b16 v[138:139], v6 offset:41472\n\tds_read_b64_tr_b16 v[140:141], v7 offset:40960\n\tds_read_b64_tr_b16 v[142:143], v7 offset:41472\n\tds_read_b64_tr_b16 v[144:145], v4 offset:41984\n\t"
            "ds_read_b64_tr_b16 v[146:147], v4 offset:42496\n\tds_read_b64_tr_b16 v[148:149], v5 offset:41984\n\tds_read_b64_tr_b16 v[150:151], v5 offset:42496\n\tds_read_b64_tr_b16 v[152:153], v6 offset:41984\n\tds_read_b64_tr_b16 v[154:155], v6 offset:42496\n\tds_read_b64_tr_b16 v[156:157], v7 offset:41984\n\t"
            "ds_read_b64_tr_b16 v[158:159], v7 offset:42496\n\ts_waitcnt lgkmcnt(15)\n\tv_min3_u32 v16, v16, v17, v18\n\tv_min_u32 v16, v16, v19\n\tv_cmp_eq_u32 vcc, 0, v16\n\ts_cbranch_vccz 63b\n\t"
            "s_sub_u32 s68, s68, 1\n\ts_cbranch_scc0 62b\n\ts_branch 9b\n\t100:\ns_mov_b32 s70, 4\n\ts_call_b64 s[72:73], 300f\n\t"
            "v_max3_u32 v12, v176, v178, v180\n\tv_max_u32 v12, v12, v182\n\tv_cmp_eq_u32 vcc, -1, v12\n\ts_cbranch_vccnz 100b\n\ts_branch 200b\n\t101:\n"
            "s_mov_b32 s70, 5\n\ts_call_b64 s[72:73], 301f\n\tv_max3_u32 v12, v184, v186, v188\n\tv_max_u32 v12, v12, v190\n\tv_cmp_eq_u32 vcc, -1, v12\n\ts_cbranch_vccnz 101b\n\t"
            "s_branch 201b\n\t102:\ns_mov_b32 s70, 6\n\ts_call_b64 s[72:73], 302f\n\tv_max3_u32 v12, v192, v194, v196\n\tv_max_u32 v12, v12, v198\n\t"
            "v_cmp_eq_u32 vcc, -1, v12\n\ts_cbranch_vccnz 102b\n\ts_branch 202b\n\t103:\ns_mov_b32 s70, 7\n\ts_call_b64 s[72:73], 303f\n\t"
            "v_max3_u32 v12, v200, v202, v204\n\tv_max_u32 v12, v12, v206\n\tv_cmp_eq_u32 vcc, -1, v12\n\ts_cbranch_vccnz 103b\n\ts_branch 203b\n\t104:\n"
            "s_mov_b32 s70, 8\n\ts_call_b64 s[72:73], 304f\n\tv_max3_u32 v12, v208, v210, v212\n\tv_max_u32 v12, v12, v214\n\tv_cmp_eq_u32 vcc, -1, v12\n\ts_cbranch_vccnz 104b\n\t"
            "s_branch 204b\n\t105:\ns_mov_b32 s70, 9\n\ts_call_b64 s[72:73], 305f\n\tv_max3_u32 v12, v216, v218, v220\n\tv_max_u32 v12, v12, v222\n\t"
            "v_cmp_eq_u32 vcc, -1, v12\n\ts_cbranch_vccnz 105b\n\ts_branch 205b\n\t106:\ns_mov_b32 s70, 10\n\ts_call_b64 s[72:73], 306f\n\t"
            "v_max3_u32 v12, v224, v226, v228\n\tv_max_u32 v12, v12, v230\n\tv_cmp_eq_u32 vcc, -1, v12\n\ts_cbranch_vccnz 106b\n\ts_branch 206b\n\t107:\n"
            "s_mov_b32 s70, 11\n\ts_call_b64 s[72:73], 307f\n\tv_max3_u32 v12, v232, v234, v236\n\tv_max_u32 v12, v12, v238\n\tv_cmp_eq_u32 vcc, -1, v12\n\ts_cbranch_vccnz 107b\n\t"
            "s_branch 207b\n\t108:\ns_mov_b32 s70, 12\n\ts_call_b64 s[72:73], 308f\n\tv_max3_u32 v12, v176, v178, v180\n\tv_max_u32 v12, v12, v182\n\t"
            "v_cmp_eq_u32 vcc, -1, v12\n\ts_cbranch_vccnz 108b\n\ts_branch 208b\n\t109:\ns_mov_b32 s70, 13\n\ts_call_b64 s[72:73], 309f\n\t"
            "v_max3_u32 v12, v184, v186, v188\n\tv_max_u32 v12, v12, v190\n\tv_cmp_eq_u32 vcc, -1, v12\n\ts_cbranch_vccnz 109b\n\ts_branch 209b\n\t110:\n"
            "s_mov_b32 s70, 14\n\ts_call_b64 s[72:73], 310f\n\tv_max3_u32 v12, v192, v194, v196\n\tv_max_u32 v12, v12, v198\n\tv_cmp_eq_u32 vcc, -1, v12\n\ts_cbranch_vccnz 110b\n\t"
            "s_branch 210b\n\t111:\ns_mov_b32 s70, 15\n\ts_call_b64 s[72:73], 311f\n\tv_max3_u32 v12, v200, v202, v204\n\tv_max_u32 v12, v12, v206\n\t"
            "v_cmp_eq_u32 vcc, -1, v12\n\ts_cbranch_vccnz 111b\n\ts_branch 211b\n\t112:\ns_mov_b32 s70, 16\n\ts_call_b64 s[72:73], 312f\n\t"
            "v_max3_u32 v12, v208, v210, v212\n\tv_max_u32 v12, v12, v214\n\tv_cmp_eq_u32 vcc, -1, v12\n\ts_cbranch_vccnz 112b\n\ts_branch 212b\n\t113:\n"
            "s_mov_b32 s70, 17\n\ts_call_b64 s[72:73], 313f\n\tv_max3_u32 v12, v216, v218, v220\n\tv_max_u32 v12, v12, v222\n\tv_cmp_eq_u32 vcc, -1, v12\n\ts_cbranch_vccnz 113b\n\t"
            "s_branch 213b\n\t114:\ns_mov_b32 s70, 18\n\ts_call_b64 s[72:73], 314f\n\tv_max3_u32 v12, v224, v226, v228\n\tv_max_u32 v12, v12, v230\n\t"
            "v_cmp_eq_u32 vcc, -1, v12\n\ts_cbranch_vccnz 114b\n\ts_branch 214b\n\t115:\ns_mov_b32 s70, 19\n\ts_call_b64 s[72:73], 315f\n\t"
            "v_max3_u32 v12, v232, v234, v236\n\tv_max_u32 v12, v12, v238\n\tv_cmp_eq_u32 vcc, -1, v12\n\ts_cbranch_vccnz 115b\n\ts_branch 215b\n\t116:\n"
            "s_mov_b32 s70, 19\n\ts_call_b64 s[72:73], 316f\n\tv_max3_u32 v12, v176, v178, v180\n\tv_max_u32 v12, v12, v182\n\tv_cmp_eq_u32 vcc, -1, v12\n\ts_cbranch_vccnz 116b\n\t"
            "s_branch 216b\n\t117:\ns_mov_b32 s70, 19\n\ts_call_b64 s[72:73], 317f\n\tv_max3_u32 v12, v184, v186, v188\n\tv_max_u32 v12, v12, v190\n\t"
            "v_cmp_eq_u32 vcc, -1, v12\n\ts_cbranch_vccnz 117b\n\ts_branch 217b\n\t118:\ns_mov_b32 s70, 19\n\ts_call_b64 s[72:73], 318f\n\t"
            "v_max3_u32 v12, v192, v194, v196\n\tv_max_u32 v12, v12, v198\n\tv_cmp_eq_u32 vcc, -1, v12\n\ts_cbranch_vccnz 118b\n\ts_branch 218b\n\t119:\n"
            "s_mov_b32 s70, 19\n\ts_call_b64 s[72:73], 319f\n\tv_max3_u32 v12, v200, v202, v204\n\tv_max_u32 v12, v12, v206\n\tv_cmp_eq_u32 vcc, -1, v12\n\ts_cbranch_vccnz 119b\n\t"
            "s_branch 219b\n\t300:\ns_waitcnt vmcnt(0)\n\t400:\nv_max3_u32 v12, v176, v178, v180\n\tv_max_u32 v12, v12, v182\n\t"
            "v_cmp_eq_u32 vcc, -1, v12\n\ts_cbranch_vccz 11f\n\tbuffer_load_dwordx4 v[176:179], v2, s[44:47], s4 offen offset:1024 sc1\n\tbuffer_load_dwordx4 v[180:183], v2, s[44:47], s4 offen offset:2048 sc1\n\t11:\ns_cmp_eq_u32 s70, 0\n\t"
            "s_cbranch_scc0 401f\n\ts_branch 12f\n\t301:\ns_waitcnt vmcnt(0)\n\t401:\nv_max3_u32 v12, v184, v186, v188\n\t"
            "v_max_u32 v12, v12, v190\n\tv_cmp_eq_u32 vcc, -1, v12\n\ts_cbranch_vccz 11f\n\tbuffer_load_dwordx4 v[184:187], v2, s[44:47], s5 offen offset:1024 sc1\n\tbuffer_load_dwordx4 v[188:191], v2, s[44:47], s5 offen offset:2048 sc1\n\t11:\n"
            "s_cmp_eq_u32 s70, 1\n\ts_cbranch_scc0 402f\n\ts_branch 12f\n\t302:\ns_waitcnt vmcnt(0)\n\t402:\n"
            "v_max3_u32 v12, v192, v194, v196\n\tv_max_u32 v12, v12, v198\n\tv_cmp_eq_u32 vcc, -1, v12\n\ts_cbranch_vccz 11f\n\tbuffer_load_dwordx4 v[192:195], v2, s[44:47], s6 offen offset:1024 sc1\n\tbuffer_load_dwordx4 v[196:199], v2, s[44:47], s6 offen offset:2048 sc1\n\t"
            "11:\ns_cmp_eq_u32 s70, 2\n\ts_cbranch_scc0 403f\n\ts_branch 12f\n\t303:\ns_waitcnt vmcnt(0)\n\t"
            "403:\nv_max3_u32 v12, v200, v202, v204\n\tv_max_u32 v12, v12, v206\n\tv_cmp_eq_u32 vcc, -1, v12\n\ts_cbranch_vccz 11f\n\tbuffer_load_dwordx4 v[200:203], v2, s[44:47], s7 offen offset:1024 sc1\n\t"
            "buffer_load_dwordx4 v[204:207], v2, s[44:47], s7 offen offset:2048 sc1\n\t11:\ns_cmp_eq_u32 s70, 3\n\ts_cbranch_scc0 404f\n\ts_branch 12f\n\t304:\n"
            "s_waitcnt vmcnt(0)\n\t404:\nv_max3_u32 v12, v208, v210, v212\n\tv_max_u32 v12, v12, v214\n\tv_cmp_eq_u32 vcc, -1, v12\n\ts_cbranch_vccz 11f\n\t"
            "buffer_load_dwordx4 v[208:211], v2, s[44:47], s8 offen offset:1024 sc1\n\tbuffer_load_dwordx4 v[212:215], v2, s[44:47], s8 offen offset:2048 sc1\n\t11:\ns_cmp_eq_u32 s70, 4\n\ts_cbranch_scc0 405f\n\ts_branch 12f\n\t"
            "305:\ns_waitcnt vmcnt(0)\n\t405:\nv_max3_u32 v12, v216, v218, v220\n\tv_max_u32 v12, v12, v222\n\tv_cmp_eq_u32 vcc, -1, v12\n\t"
            "s_cbranch_vccz 11f\n\tbuffer_load_dwordx4 v[216:219], v2, s[44:47], s9 offen offset:1024 sc1\n\tbuffer_load_dwordx4 v[220:223], v2, s[44:47], s9 offen offset:2048 sc1\n\t11:\ns_cmp_eq_u32 s70, 5\n\ts_cbranch_scc0 406f\n\t"
            "s_branch 12f\n\t306:\ns_waitcnt vmcnt(0)\n\t406:\nv_max3_u32 v12, v224, v226, v228\n\tv_max_u32 v12, v12, v230\n\t"
            "v_cmp_eq_u32 vcc, -1, v12\n\ts_cbranch_vccz 11f\n\tbuffer_load_dwordx4 v[224:227], v2, s[44:47], s10 offen offset:1024 sc1\n\tbuffer_load_dwordx4 v[228:231], v2, s[44:47], s10 offen offset:2048 sc1\n\t11:\ns_cmp_eq_u32 s70, 6\n\t"
            "s_cbranch_scc0 407f\n\ts_branch 12f\n\t307:\ns_waitcnt vmcnt(0)\n\t407:\nv_max3_u32 v12, v232, v234, v236\n\t"
            "v_max_u32 v12, v12, v238\n\tv_cmp_eq_u32 vcc, -1, v12\n\ts_cbranch_vccz 11f\n\tbuffer_load_dwordx4 v[232:235], v2, s[44:47], s11 offen offset:1024 sc1\n\tbuffer_load_dwordx4 v[236:239], v2, s[44:47], s11 offen offset:2048 sc1\n\t11:\n"
            "s_cmp_eq_u32 s70, 7\n\ts_cbranch_scc0 408f\n\ts_branch 12f\n\t308:\ns_waitcnt vmcnt(0)\n\t408:\n"
            "v_max3_u32 v12, v176, v178, v180\n\tv_max_u32 v12, v12, v182\n\tv_cmp_eq_u32 vcc, -1, v12\n\ts_cbranch_vccz 11f\n\tbuffer_load_dwordx4 v[176:179], v2, s[44:47], s12 offen offset:1024 sc1\n\tbuffer_load_dwordx4 v[180:183], v2, s[44:47], s12 offen offset:2048 sc1\n\t"
            "11:\ns_cmp_eq_u32 s70, 8\n\ts_cbranch_scc0 409f\n\ts_branch 12f\n\t309:\ns_waitcnt vmcnt(0)\n\t"
            "409:\nv_max3_u32 v12, v184, v186, v188\n\tv_max_u32 v12, v12, v190\n\tv_cmp_eq_u32 vcc, -1, v12\n\ts_cbranch_vccz 11f\n\tbuffer_load_dwordx4 v[184:187], v2, s[44:47], s13 offen offset:1024 sc1\n\t"
            "buffer_load_dwordx4 v[188:191], v2, s[44:47], s13 offen offset:2048 sc1\n\t11:\ns_cmp_eq_u32 s70, 9\n\ts_cbranch_scc0 410f\n\ts_branch 12f\n\t310:\n"
            "s_waitcnt vmcnt(0)\n\t410:\nv_max3_u32 v12, v192, v194, v196\n\tv_max_u32 v12, v12, v198\n\tv_cmp_eq_u32 vcc, -1, v12\n\ts_cbranch_vccz 11f\n\t"
            "buffer_load_dwordx4 v[192:195], v2, s[44:47], s4 offen offset:0 sc1\n\tbuffer_load_dwordx4 v[196:199], v2, s[44:47], s4 offen offset:3072 sc1\n\t11:\ns_cmp_eq_u32 s70, 10\n\ts_cbranch_scc0 411f\n\ts_branch 12f\n\t"
            "311:\ns_waitcnt vmcnt(0)\n\t411:\nv_max3_u32 v12, v200, v202, v204\n\tv_max_u32 v12, v12, v206\n\tv_cmp_eq_u32 vcc, -1, v12\n\t"
            "s_cbranch_vccz 11f\n\tbuffer_load_dwordx4 v[200:203], v2, s[44:47], s5 offen offset:0 sc1\n\tbuffer_load_dwordx4 v[204:207], v2, s[44:47], s5 offen offset:3072 sc1\n\t11:\ns_cmp_eq_u32 s70, 11\n\ts_cbranch_scc0 412f\n\t"
            "s_branch 12f\n\t312:\ns_waitcnt vmcnt(0)\n\t412:\nv_max3_u32 v12, v208, v210, v212\n\tv_max_u32 v12, v12, v214\n\t"
            "v_cmp_eq_u32 vcc, -1, v12\n\ts_cbranch_vccz 11f\n\tbuffer_load_dwordx4 v[208:211], v2, s[44:47], s6 offen offset:0 sc1\n\tbuffer_load_dwordx4 v[212:215], v2, s[44:47], s6 offen offset:3072 sc1\n\t11:\ns_cmp_eq_u32 s70, 12\n\t"
            "s_cbranch_scc0 413f\n\ts_branch 12f\n\t313:\ns_waitcnt vmcnt(0)\n\t413:\nv_max3_u32 v12, v216, v218, v220\n\t"
            "v_max_u32 v12, v12, v222\n\tv_cmp_eq_u32 vcc, -1, v12\n\ts_cbranch_vccz 11f\n\tbuffer_load_dwordx4 v[216:219], v2, s[44:47], s7 offen offset:0 sc1\n\tbuffer_load_dwordx4 v[220:223], v2, s[44:47], s7 offen offset:3072 sc1\n\t11:\n"
            "s_cmp_eq_u32 s70, 13\n\ts_cbranch_scc0 414f\n\ts_branch 12f\n\t314:\ns_waitcnt vmcnt(0)\n\t414:\n"
            "v_max3_u32 v12, v224, v226, v228\n\tv_max_u32 v12, v12, v230\n\tv_cmp_eq_u32 vcc, -1, v12\n\ts_cbranch_vccz 11f\n\tbuffer_load_dwordx4 v[224:227], v2, s[44:47], s8 offen offset:0 sc1\n\tbuffer_load_dwordx4 v[228:231], v2, s[44:47], s8 offen offset:3072 sc1\n\t"
            "11:\ns_cmp_eq_u32 s70, 14\n\ts_cbranch_scc0 415f\n\ts_branch 12f\n\t315:\ns_waitcnt vmcnt(0)\n\t"
            "415:\nv_max3_u32 v12, v232, v234, v236\n\tv_max_u32 v12, v12, v238\n\tv_cmp_eq_u32 vcc, -1, v12\n\ts_cbranch_vccz 11f\n\tbuffer_load_dwordx4 v[232:235], v2, s[44:47], s9 offen offset:0 sc1\n\t"
            "buffer_load_dwordx4 v[236:239], v2, s[44:47], s9 offen offset:3072 sc1\n\t11:\ns_cmp_eq_u32 s70, 15\n\ts_cbranch_scc0 416f\n\ts_branch 12f\n\t316:\n"
            "s_waitcnt vmcnt(0)\n\t416:\nv_max3_u32 v12, v176, v178, v180\n\tv_max_u32 v12, v12, v182\n\tv_cmp_eq_u32 vcc, -1, v12\n\ts_cbranch_vccz 11f\n\t"
            "buffer_load_dwordx4 v[176:179], v2, s[44:47], s10 offen offset:0 sc1\n\tbuffer_load_dwordx4 v[180:183], v2, s[44:47], s10 offen offset:3072 sc1\n\t11:\ns_cmp_eq_u32 s70, 16\n\ts_cbranch_scc0 417f\n\ts_branch 12f\n\t"
            "317:\ns_waitcnt vmcnt(0)\n\t417:\nv_max3_u32 v12, v184, v186, v188\n\tv_max_u32 v12, v12, v190\n\tv_cmp_eq_u32 vcc, -1, v12\n\t"
            "s_cbranch_vccz 11f\n\tbuffer_load_dwordx4 v[184:187], v2, s[44:47], s11 offen offset:0 sc1\n\tbuffer_load_dwordx4 v[188:191], v2, s[44:47], s11 offen offset:3072 sc1\n\t11:\ns_cmp_eq_u32 s70, 17\n\ts_cbranch_scc0 418f\n\t"
            "s_branch 12f\n\t318:\ns_waitcnt vmcnt(0)\n\t418:\nv_max3_u32 v12, v192, v194, v196\n\tv_max_u32 v12, v12, v198\n\t"
            "v_cmp_eq_u32 vcc, -1, v12\n\ts_cbranch_vccz 11f\n\tbuffer_load_dwordx4 v[192:195], v2, s[44:47], s12 offen offset:0 sc1\n\tbuffer_load_dwordx4 v[196:199], v2, s[44:47], s12 offen offset:3072 sc1\n\t11:\ns_cmp_eq_u32 s70, 18\n\t"
            "s_cbranch_scc0 419f\n\ts_branch 12f\n\t319:\ns_waitcnt vmcnt(0)\n\t419:\nv_max3_u32 v12, v200, v202, v204\n\t"
            "v_max_u32 v12, v12, v206\n\tv_cmp_eq_u32 vcc, -1, v12\n\ts_cbranch_vccz 11f\n\tbuffer_load_dwordx4 v[200:203], v2, s[44:47], s13 offen offset:0 sc1\n\tbuffer_load_dwordx4 v[204:207], v2, s[44:47], s13 offen offset:3072 sc1\n\t11:\n"
            "12:\n"
            RINC
            "s_sub_u32 s68, s68, 1\n\ts_cbranch_scc1 9b\n\ts_waitcnt vmcnt(0)\n\ts_setpc_b64 s[72:73]\n\t.subsection 1\n\t.Lce:\n"
            ".subsection 0\n\t.Lout:\n"
            : "=&{s67}"(tag) : [t] "v"(tid), [b] "s"(bid), [k] "s"(ka)
            : "memory", "scc", "vcc", "s4", "s5", "s6", "s7", "s8", "s9", "s10", "s11", "s12", "s13", "s14", "s15", "s16", "s17", "s18", "s19", "s20", "s21", "s22", "s23", "s24", "s25", "s26", "s27", "s28", "s29", "s30", "s31", "s32", "s33", "s34", "s35", "s36", "s37", "s38", "s39", "s40", "s41", "s42", "s43", "s44", "s45", "s46", "s47", "s48", "s49", "s50", "s51", "s52", "s53", "s54", "s55", "s56", "s57", "s58", "s59", "s60", "s61", "s62", "s63", "s64", "s65", "s66", "s68", "s69", "s70", "s71", "s72", "s73", "s74", "s75", "s76", "s77", "s78", "s79", "s80", "s81", "s82", "s83", "s84", "s85", "s86", "s87", "s88", "s89", "s90", "s91", "s92", "s93", "s94", "s95", "s96", "s97", "s98", "s99", "s100", "s101", "v2", "v3", "v4", "v5", "v6", "v7", "v8", "v9", "v10", "v11", "v12", "v13", "v14", "v15", "v16", "v17", "v18", "v19", "v20", "v21", "v22", "v23", "v24", "v25", "v26", "v27", "v28", "v29", "v30", "v31", "v32", "v33", "v34", "v35", "v36", "v37", "v38", "v39", "v40", "v41", "v42", "v43", "v44", "v45", "v46", "v47", "v48", "v49", "v50", "v51", "v52", "v53", "v54", "v55", "v56", "v57", "v58", "v59", "v60", "v61", "v62", "v63", "v64", "v65", "v66", "v67", "v68", "v69", "v70", "v71", "v72", "v73", "v74", "v75", "v76", "v77", "v78", "v79", "v80", "v81", "v82", "v83", "v84", "v85", "v86", "v87", "v88", "v89", "v90", "v91", "v92", "v93", "v94", "v95", "v96", "v97", "v98", "v99", "v100", "v101", "v102", "v103", "v104", "v105", "v106", "v107", "v108", "v109", "v110", "v111", "v112", "v113", "v114", "v115", "v116", "v117", "v118", "v119", "v120", "v121", "v122", "v123", "v124", "v125", "v126", "v127", "v128", "v129", "v130", "v131", "v132", "v133", "v134", "v135", "v136", "v137", "v138", "v139", "v140", "v141", "v142", "v143", "v144", "v145", "v146", "v147", "v148", "v149", "v150", "v151", "v152", "v153", "v154", "v155", "v156", "v157", "v158", "v159", "v160", "v161", "v162", "v163", "v164", "v165", "v166", "v167", "v168", "v169", "v170", "v171", "v172", "v173", "v174", "v175", "v176", "v177", "v178", "v179", "v180", "v181", "v182", "v183", "v184", "v185", "v186", "v187", "v188", "v189", "v190", "v191", "v192", "v193", "v194", "v195", "v196", "v197", "v198", "v199", "v200", "v201", "v202", "v203", "v204", "v205", "v206", "v207", "v208", "v209", "v210", "v211", "v212", "v213", "v214", "v215", "v216", "v217", "v218", "v219", "v220", "v221", "v222", "v223", "v224", "v225", "v226", "v227", "v228", "v229", "v230", "v231", "v232", "v233", "v234", "v235", "v236", "v237", "v238", "v239", "v240", "v241", "v242", "v243", "v244", "v245", "v246", "v247", "v248", "v249", "v250", "v251", "v252", "v253", "v254", "v255");
    asm volatile("" : "+s"(bid), "+s"(ka)); asm volatile("" : "+v"(tid));
    const u16 *x = (const u16*)ka[0], *wo = (const u16*)ka[2], *w_r = (const u16*)ka[3], *wg = (const u16*)ka[4], *wu = (const u16*)ka[5], *wd = (const u16*)ka[6]; char* ws = (char*)ka[7];
    const int wv = __builtin_amdgcn_readfirstlane(tid >> 6);
    const unsigned lds0 = (unsigned)(size_t)(lchar*)smem;
#define LANE() const int lane = lane_id(); const int g = lane >> 4, li = lane & 15, jq = 4 * (li & 3) + (li >> 2); (void)jq
    const int xcd = bid & 7, j = ((bid >> 3) + 25) & 31;
    const bool gemm = j < 25;
    const int fp = xcd >> 1, bl = (j * 13) >> 6, cl = j - bl * 5;
    const int rb = fp * 5 + bl, cg = (xcd & 1) * 5 + cl;
    const int par = fp >> 1, I0 = ((fp & 1) * 5 + bl) * 104 - 8;
    const __amdgpu_buffer_rsrc_t rs = __builtin_amdgcn_make_buffer_rsrc((void*)ws, 0, W_END, 0x00020000);
    auto wait_flag = [&](unsigned a) { SPIN { if (RFL(ldsrv<unsigned>(a))) break; __builtin_amdgcn_s_sleep(1); } MEMB(); };
    auto all4 = [&](u32x4 c) { return RFL(min(min(c.x, c.y), min(c.z, c.w))); };
    unsigned ep = 0;
    auto epld = [&]() { asm volatile("s_load_dword %0, %1, 0x0" : "=s"(ep) : "s"(ws) : "memory"); };
    auto mktag = [&]() { asm volatile("s_waitcnt lgkmcnt(0)" : "+s"(ep) :: "memory"); tag = ep * 1664525u + 1013904223u; };
        if (gemm) {
            LANE();
            u32x4 kk;
            SPIN {
                MEMB();
                kk = __builtin_amdgcn_raw_buffer_load_b128(rs, W_KEY + min(lane, 51) * 16, 0, SC1);
                if (__builtin_amdgcn_ballot_w64(kk.y != tag || kk.w != tag) == 0) break;
            }
            const uint2 t2 = top2wave(lane < 52 ? kk.x : 0u, lane < 52 ? kk.z : 0u);
            _Pragma("nounroll") for (int k = 0; k < 2; ++k) {
                const unsigned key = k ? t2.y : t2.x, rr = (0xffffu - (key & 0xffffu)) & 2047u; const int pos = (int)(rr >> 1) - I0;
                if (__builtin_expect((int)(rr & 1) == par && pos >= 8 && pos < 112, 0)) {
                    const float sg = rbf(__builtin_amdgcn_rcpf(1.f + __builtin_amdgcn_exp2f(keyscore(key) * -1.4426950408889634f)));
                    const int m = (pos & ~15) + 4 * (pos & 3) + ((pos >> 2) & 3);
                    const float xr_ = lo2f(x[rr * 640 + cg * 64 + lane]), nm = ldsrv<float>(lds0 + L_NM + m * 4), rsj = ldsrv<float>(lds0 + L_RS + pos * 4);
                    u32x2 y[5];
                    SPIN {
                        MEMB();
                        bool old = false;
#pragma unroll
                        for (int i = 0; i < 5; ++i) y[i] = __builtin_amdgcn_raw_buffer_load_b64(rs, W_Y + (cg * 64 + lane) * 16 + k * 8, i * 10240, SC1);
#pragma unroll
                        for (int i = 0; i < 5; ++i) old = old || y[i].y != tag;
                        if (__builtin_amdgcn_ballot_w64(old) == 0) break;
                        __builtin_amdgcn_s_sleep(2);
                    }
                    const float ys = ((__uint_as_float(y[0].x) + __uint_as_float(y[1].x)) + (__uint_as_float(y[2].x) + __uint_as_float(y[3].x))) + __uint_as_float(y[4].x);
                    const float xn = rbf(rbf(xr_ + nm) * rsj), u = rbf(rbf(ys) * sg);
                    ldsw<float>(lds0 + L_RG + k * 256 + lane * 4, silu_b(rbf(xn + u)));
                    ldsw<unsigned>(lds0 + L_RM + m * 4, L_RG + k * 256);
                }
            }
            ldsw<unsigned>(lds0 + FL_R, 1u);
            END();
        }
    {
        TL(0);
        LANE();
        const int fq = (j - 25) * 2 + (xcd & 1), f = fp * 14 + fq;
        u32x4 sx[2][5], sw[5];
        const int n0 = fq * 40 + wv * 4 + g;
        unsigned a[2];
#pragma unroll
        for (int n = 0; n < 2; ++n) a[n] = (unsigned)(((fp & 1) * 512 + min(n0 + 32 * n, 511)) * 2560 + par * 1280 + li * 16);
#define LD5(r0, r1, r2, r3, r4, a, b) "global_load_dwordx4 %" #r0 ", %[" a "], %[" b "]\n\tglobal_load_dwordx4 %" #r1 ", %[" a "], %[" b "] offset:256\n\tglobal_load_dwordx4 %" #r2 ", %[" a "], %[" b "] offset:512\n\t" \
                                      "global_load_dwordx4 %" #r3 ", %[" a "], %[" b "] offset:768\n\tglobal_load_dwordx4 %" #r4 ", %[" a "], %[" b "] offset:1024\n\t"
        asm volatile("s_cmp_ge_i32 %[q], 2\n\ts_cbranch_scc1 1f\n\t" LD5(5, 6, 7, 8, 9, "a1", "x") "1:\n\t" LD5(0, 1, 2, 3, 4, "a0", "x") LD5(10, 11, 12, 13, 14, "aw", "wr")
                     : "=&v"(sx[0][0]), "=&v"(sx[0][1]), "=&v"(sx[0][2]), "=&v"(sx[0][3]), "=&v"(sx[0][4]), "=&v"(sx[1][0]), "=&v"(sx[1][1]), "=&v"(sx[1][2]), "=&v"(sx[1][3]), "=&v"(sx[1][4]),
                       "=&v"(sw[0]), "=&v"(sw[1]), "=&v"(sw[2]), "=&v"(sw[3]), "=&v"(sw[4])
                     : [a0] "v"(a[0]), [a1] "v"(a[1]), [aw] "v"(li * 16), [x] "s"(x), [wr] "s"(w_r), [q] "s"(wv)
                     : "memory", "scc");
        epld();
        if (wv == 7 && lane < 16) ldsw<u32x4>(lds0 + MISC + lane * 16, (u32x4){0u, 0u, 0u, 0u});
        const int u2 = max(f - 6, 0), kr = (u2 * 26) >> 8, fc = u2 - kr * 10, n1 = wv < 4 ? 3 : 2;
        const int d0 = kr * 11 + wv, d1 = kr * 11 + 4 + wv; const bool two = wv >= 4 && wv < 7 && d1 < 54;
        const char* w1 = (const char*)(f & 1 ? wu : wg) + min(f >> 1, 26) * 128; const char* w2 = (const char*)wd + fc * 128;
        unsigned fv[2][4];
#pragma unroll
        for (int i = 0; i < 4; ++i) { const unsigned c = (((lane & 7) ^ (2 * ((lane >> 4) & 1) + 4 * (i & 1))) * 16) - i * 1024; fv[0][i] = ((lane >> 3) + i * 8) * (F_ * 2) + c; fv[1][i] = ((lane >> 3) + i * 8) * 1280 + c; }
        auto issue = [&](int m, int ks, int slot) {
            asm volatile("s_mov_b32 m0, %[l]\n\ts_nop 0\n\tglobal_load_lds_dwordx4 %0, %[b] nt\n\tglobal_load_lds_dwordx4 %1, %[b] offset:1024 nt\n\tglobal_load_lds_dwordx4 %2, %[b] offset:2048 nt\n\tglobal_load_lds_dwordx4 %3, %[b] offset:3072 nt"
                         :: "v"(fv[m][0]), "v"(fv[m][1]), "v"(fv[m][2]), "v"(fv[m][3]), [b] "s"((m ? w2 : w1) + (unsigned)(ks * 32 * (m ? 1280 : F_ * 2))), [l] "s"(lds0 + wv * 16384 + slot * 4096) : "memory");
        };
        BARN();
        if (EARLYK) issue(0, wv, 0);
        asm volatile("s_waitcnt vmcnt(%15)" : "+v"(sx[0][0]), "+v"(sx[0][1]), "+v"(sx[0][2]), "+v"(sx[0][3]), "+v"(sx[0][4]), "+v"(sx[1][0]), "+v"(sx[1][1]), "+v"(sx[1][2]), "+v"(sx[1][3]), "+v"(sx[1][4]),
                     "+v"(sw[0]), "+v"(sw[1]), "+v"(sw[2]), "+v"(sw[3]), "+v"(sw[4]) : "n"(4 * EARLYK) : "memory");
        TL(1);
        mktag();
        {
            const unsigned one2 = 0x3F803F80u;
            float wsum = 0.f;
#pragma unroll
            for (int e = 0; e < 20; ++e) wsum = dot2(sw[e >> 2][e & 3], one2, wsum);
            wsum = sum16(wsum);
            auto stat = [&](int p) {
                float s1 = 0.f, s2 = 0.f, sr = 0.f;
#pragma unroll
                for (int e = 0; e < 20; ++e) { const unsigned u = sx[p][e >> 2][e & 3]; s1 = dot2(u, one2, s1); s2 = dot2(u, u, s2); sr = dot2(u, sw[e >> 2][e & 3], sr); }
                const float mean = sum16(s1) * (1.f / 640.f);
                const unsigned mb2 = cvtpk(mean, mean); const float mb = lo2f(mb2);
                const float q = fmaf(40.f * mb, mb, fmaf(-2.f * mb, s1, s2));
                const float dm = mean - mb;
                const float var = fmaxf(sum16(q) * (1.f / 640.f) - dm * dm, 0.f);
                const float sd = rbf(__builtin_amdgcn_sqrtf(rbf(rbf(var) + 1e-5f)));
                const float rsc = rbf(fmaf(-mb, wsum, sum16(sr)) / sd);
                const int n = n0 + 32 * p, i = (fp & 1) * 512 + n;
                if (li == 0) {
                    asm volatile("buffer_store_dwordx4 %0, %1, %2, 0 offen sc1\n\ts_nop 1" :: "v"((u32x4){(mb2 << 16) ^ 0x80000000u, tag, __float_as_uint(__builtin_amdgcn_rcpf(sd)), tag}), "v"(n < 512 ? W_ST + (par * 1024 + i) * 16 : 0x40000000), "s"(rs) : "memory");
                    ldsw<unsigned>(lds0 + LS_ROW + (p * 32 + wv * 4 + g) * 4, n < 512 ? rkey(rsc, 2 * i + par) : 0u);
                }
            };
            stat(0);
            if (wv < 2) stat(1);
        }
        BAR();
        if (wv == 6) asm volatile("buffer_store_dword %0, %1, %2, 0 offen sc1" :: "v"(tag), "v"(lane ? 0x40000000 : W_ACK + (f < 40 ? (f >> 1) * 128 + 40 + (f & 1) * 4 : 2560 + (f - 40) * 4)), "s"(rs) : "memory");
        if (wv == 7) {
            const unsigned k = ldsrv<unsigned>(lds0 + LS_ROW + min(lane, 39) * 4);
            const uint2 t2 = top2wave(lane < 40 ? k : 0u, 0u);
            if (lane == 0 && fq < 13) asm volatile("buffer_store_dwordx4 %0, %1, %2, 0 offen sc1\n\ts_nop 1" :: "v"((u32x4){t2.x, tag, t2.y, tag}), "v"(W_KEY + (fp * 13 + fq) * 16), "s"(rs) : "memory");
        }
        BARN();
        TL(2);
        if (!EARLYK) issue(0, wv, 0);
        issue(0, wv + 8, 1);
        if (wv < 4) issue(0, wv + 16, 2);
        else asm volatile("s_mov_b32 m0, %[l]\n\ts_mov_b32 exec_hi, 0\n\tglobal_load_lds_dword %0, %[b]\n\ts_mov_b32 exec_hi, -1" :: "v"(min(f * 128 + (wv - 4) * 32 + lane, 6399) * 128), [b] "s"(wo), [l] "s"(lds0 + L_DUM) : "memory");
        if (wv != 7) issue(1, d0, 3);
        if (two) issue(1, d1, 2);
        if (wv == 7) {
            u32x4 kk;
            SPIN {
                asm volatile("buffer_load_dwordx4 %0, %1, %2, 0 offen sc1\n\ts_waitcnt vmcnt(0)" : "=&v"(kk) : "v"(W_KEY + min(lane, 51) * 16), "s"(rs) : "memory");
                if (__builtin_amdgcn_ballot_w64(kk.y != tag || kk.w != tag) == 0) break;
            }
            const uint2 t2 = top2wave(lane < 52 ? kk.x : 0u, lane < 52 ? kk.z : 0u);
            if (lane < 2) ldsw<unsigned>(lds0 + ROUT + lane * 8, (0xffffu - ((lane ? t2.y : t2.x) & 0xffffu)) & 2047u);
        }
        BAR();
        TL(3);
        const unsigned row = ldsrv<unsigned>(lds0 + ROUT + (li & 1) * 8);
        u32x4 af[3], st;
        asm volatile("global_load_dwordx4 %0, %4, %5\n\tglobal_load_dwordx4 %1, %4, %5 offset:512\n\tglobal_load_dwordx4 %2, %6, %5 offset:1024\n\tbuffer_load_dwordx4 %3, %7, %8, 0 offen sc1"
                     : "=&v"(af[0]), "=&v"(af[1]), "=&v"(af[2]), "=&v"(st) : "v"(row * 1280u + wv * 64 + g * 16), "s"(x), "v"(row * 1280u + (wv & 3) * 64 + g * 16), "v"(W_ST + ((row & 1) * 1024 + (row >> 1)) * 16), "s"(rs) : "memory");
        bf16x8 wf[3][4];
        unsigned ra[4];
#pragma unroll
        for (int q = 0; q < 4; ++q) ra[q] = lds0 + wv * 16384 + (unsigned)((8 * g + (li >> 2)) * 128 + (((q ^ (((li >> 3) & 1) + 2 * (g & 1))) * 32) + (li & 3) * 8));
        auto rdf = [&](int k, int slot) {
#pragma unroll
            for (int q = 0; q < 4; ++q) wf[k][q] = cat8(trread(ra[q] + slot * 4096), trread(ra[q] + slot * 4096 + 512));
        };
        wvm<4>();
        rdf(0, 0); rdf(1, 1); rdf(2, 2);
        asm volatile("s_waitcnt vmcnt(0)" : "+v"(af[0]), "+v"(af[1]), "+v"(af[2]), "+v"(st) :: "memory");
        if (wv == 7) issue(1, d0, 3);
        f32x4 c[4];
#pragma unroll
        for (int q = 0; q < 4; ++q) c[q] = (f32x4){0.f, 0.f, 0.f, 0.f};
#pragma unroll
        for (int k = 0; k < 3; ++k) if (k < n1) {
#pragma unroll
            for (int e = 0; e < 4; ++e) af[k][e] = li & 2 ? 0x3F803F80u : af[k][e];
#pragma unroll
            for (int q = 0; q < 4; ++q) c[q] = mfma32(__builtin_bit_cast(bf16x8, af[k]), wf[k][q], c[q]);
        }
        auto total = [&]() {
            ldsw<f32x4>(lds0 + wv * 16384 + lane * 16, g == 0 ? c[0] : g == 1 ? c[1] : g == 2 ? c[2] : c[3]);
            BAR();
            f32x4 pr[8];
#pragma unroll
            for (int w = 0; w < 8; ++w) pr[w] = ldsrv<f32x4>(lds0 + w * 16384 + lane * 16);
            return ((pr[0] + pr[1]) + (pr[2] + pr[3])) + ((pr[4] + pr[5]) + (pr[6] + pr[7]));
        };
        TL(4);
        {
            const f32x4 s = total();
            if (wv == 0 && f < 54) {
                SPIN {
                    if (__builtin_amdgcn_ballot_w64(st.y != tag || st.w != tag) == 0) break;
                    asm volatile("buffer_load_dwordx4 %0, %1, %2, 0 offen sc1\n\ts_waitcnt vmcnt(0)" : "=&v"(st) : "v"(W_ST + ((row & 1) * 1024 + (row >> 1)) * 16), "s"(rs) : "memory");
                }
                const float n0_ = __uint_as_float(__builtin_amdgcn_readlane(st.x, 0)), n1_ = __uint_as_float(__builtin_amdgcn_readlane(st.x, 1)), i0 = __uint_as_float(__builtin_amdgcn_readlane(st.z, 0)), i1 = __uint_as_float(__builtin_amdgcn_readlane(st.z, 1));
                float v0 = rbf(fmaf(n0_, s.z, s.x) * i0), v1 = rbf(fmaf(n1_, s.z, s.y) * i1);
                if (!(f & 1)) { v0 = silu_b(v0); v1 = silu_b(v1); }
                const unsigned hb = cvtpk(v0, v1), nb = dppu<0xB1>(hb);
                const unsigned pay = (lane & 1) ? __builtin_amdgcn_perm(hb, nb, 0x07060302u) : __builtin_amdgcn_perm(nb, hb, 0x05040100u);
                asm volatile("buffer_store_dwordx2 %0, %1, %2, 0 offen sc1" :: "v"((u32x2){pay, tag}), "v"(W_H + ((f & 1) * 2 + (lane & 1)) * 6912 + ((f >> 1) * 32 + (lane >> 1)) * 8), "s"(rs) : "memory");
            }
        }
        TL(5);
        if (f == 0 && wv == 1) {
            SPIN {
                u32x4 a_;
                asm volatile("buffer_load_dwordx4 %0, %1, %2, 0 offen sc1\n\ts_waitcnt vmcnt(0)" : "=&v"(a_) : "v"(W_ACK + (lane < 60 ? ((lane * 43) >> 7) * 80 + lane * 16 : 1600 + lane * 16)), "s"(rs) : "memory");
                if (__builtin_amdgcn_ballot_w64(a_.x != tag || a_.y != tag || a_.z != tag || a_.w != tag) == 0) break;
                __builtin_amdgcn_s_sleep(8);
            }
            if (lane == 0) __builtin_amdgcn_raw_buffer_store_b32(tag, rs, W_EP, 0, SC1);
        }
        if (f >= 6) {
            wvm<0>();
            rdf(0, 3); rdf(1, 2);
            const int ho = W_H + ((lane >> 4) & 1) * 6912 + ((lane >> 5 && two ? d1 : d0) * 16 + li) * 8;
            u32x2 hg, hu;
            __builtin_amdgcn_s_sleep(HSL);
            SPIN {
                asm volatile("buffer_load_dwordx2 %0, %2, %3, 0 offen sc1\n\tbuffer_load_dwordx2 %1, %2, %3, %4 offen sc1\n\ts_waitcnt vmcnt(0)" : "=&v"(hg), "=&v"(hu) : "v"(ho), "s"(rs), "s"(13824) : "memory");
                if (__builtin_amdgcn_ballot_w64(hg.y != tag || hu.y != tag) == 0) break;
                __builtin_amdgcn_s_sleep(HS2);
            }
            ldsw<unsigned>(lds0 + wv * 16384 + 4096 + lane * 4, cvtpk(lo2f(hg.x) * lo2f(hu.x), hi2f(hg.x) * hi2f(hu.x)));
#pragma unroll
            for (int q = 0; q < 4; ++q) c[q] = (f32x4){0.f, 0.f, 0.f, 0.f};
#pragma unroll
            for (int k = 0; k < 2; ++k) if (k == 0 || two) {
                const u32x4 h = ldsrv<u32x4>(lds0 + wv * 16384 + 4096 + k * 128 + (li & 1) * 64 + g * 16);
#pragma unroll
                for (int q = 0; q < 4; ++q) c[q] = mfma32(__builtin_bit_cast(bf16x8, h), wf[k][q], c[q]);
            }
            BARN();
            const f32x4 ys = total();
            if (wv == 0) __builtin_amdgcn_raw_buffer_store_b128((u32x4){__float_as_uint(ys.x), tag, __float_as_uint(ys.y), tag}, rs, W_Y + kr * 10240 + (fc * 64 + lane) * 16, 0, SC1);
        }
        TL(6);
        TLEND();
        wvm<0>();
    }
}

extern "C" void kernel_launch(void* const* d_in, const int* in_sizes, int n_in, void* d_out, int out_size, void* d_ws,
                              size_t ws_size, hipStream_t stream) {
    (void)hipFuncSetAttribute((const void*)fused, hipFuncAttributeMaxDynamicSharedMemorySize, LDSZ);
    fused<<<256, 512, LDSZ, stream>>>((const u16*)d_in[0], (const u16*)d_in[1], (const u16*)d_in[2], (const u16*)d_in[4],
                                      (const u16*)d_in[5], (const u16*)d_in[6], (const u16*)d_in[7], (char*)d_ws, (u16*)d_out);
}
```

```cpp
#include <hip/hip_runtime.h>
#include <type_traits>

typedef __attribute__((ext_vector_type(8))) short bf16x8;
typedef __attribute__((ext_vector_type(4))) short bf16x4;
typedef __attribute__((ext_vector_type(4))) float f32x4;
typedef __attribute__((ext_vector_type(2))) float f32x2;
typedef __attribute__((ext_vector_type(4))) unsigned u32x4;
typedef __attribute__((ext_vector_type(2))) unsigned u32x2;
typedef __attribute__((ext_vector_type(2))) __bf16 bf16x2v;
typedef __attribute__((ext_vector_type(8))) unsigned u32x8;
typedef __attribute__((ext_vector_type(16))) unsigned u32x16;
typedef unsigned short u16;
typedef __attribute__((address_space(3))) char lchar;

#define F_ 1728
constexpr int W_EP = 0;
constexpr int W_KEY = 4096;
constexpr int W_ACK = 8192;
constexpr int W_H = 12288;
constexpr int W_ST = 65536;
constexpr int W_Y = 98304;
constexpr int W_O = 155648;
constexpr int W_END = W_O + 80 * 40960;
#define SENT 0xffffffffu
constexpr int XS = 14336, SLOT = XS + 24576;
constexpr int WO0 = 2 * SLOT;
constexpr int MISC = WO0 + 81920;
constexpr int FL_S = MISC, FL_P = MISC + 16, FL_E = MISC + 32, FL_W = MISC + 48, FL_R = MISC + 64, FL_AUX = MISC + 80, ONES = MISC + 128, ROUT = MISC + 144, LTAG = MISC + 160;
constexpr int L_NM = MISC + 256, L_RS = L_NM + 512 + 64, L_RM = L_RS + 512, L_RG = L_RM + 512, L_DUM = L_RG + 512, L_KEY = L_DUM + 512;
constexpr int LS_ROW = L_NM;
constexpr int LDSZ = 163840;
static_assert(L_KEY + 1024 <= LDSZ, "lds");
#define SPIN_MAX (1 << 20)
#define SPIN _Pragma("nounroll") for (int it = 0; it < SPIN_MAX; ++it)

__device__ __forceinline__ unsigned cvtpk(float lo, float hi) { return __builtin_bit_cast(unsigned, __builtin_convertvector((f32x2){lo, hi}, bf16x2v)); }
__device__ __forceinline__ unsigned nosent(unsigned u) { return u < 0xfffffffeu ? u : 0xfffffffeu; }
__device__ __forceinline__ float lo2f(unsigned u) { return __uint_as_float(u << 16); }
__device__ __forceinline__ float hi2f(unsigned u) { return __uint_as_float(u & 0xffff0000u); }
__device__ __forceinline__ float rbf(float f) { return lo2f(cvtpk(f, f)); }
template <int CTRL> __device__ __forceinline__ float dppf(float x) { return __builtin_bit_cast(float, __builtin_amdgcn_mov_dpp(__builtin_bit_cast(int, x), CTRL, 0xf, 0xf, true)); }
template <int CTRL> __device__ __forceinline__ unsigned dppu(unsigned x) { return (unsigned)__builtin_amdgcn_mov_dpp((int)x, CTRL, 0xf, 0xf, true); }
__device__ __forceinline__ void sw16(unsigned& a, unsigned& b) { const auto r = __builtin_amdgcn_permlane16_swap(a, b, false, false); a = r[0]; b = r[1]; }
__device__ __forceinline__ void sw32(unsigned& a, unsigned& b) { const auto r = __builtin_amdgcn_permlane32_swap(a, b, false, false); a = r[0]; b = r[1]; }
__device__ __forceinline__ void sw16f(float& a, float& b) { unsigned x = __float_as_uint(a), y = __float_as_uint(b); sw16(x, y); a = __uint_as_float(x); b = __uint_as_float(y); }
__device__ __forceinline__ void sw32f(float& a, float& b) { unsigned x = __float_as_uint(a), y = __float_as_uint(b); sw32(x, y); a = __uint_as_float(x); b = __uint_as_float(y); }
__device__ __forceinline__ float vmax(float a, float b) { return __builtin_elementwise_maximum(a, b); }
__device__ __forceinline__ float sum16(float x) { x += dppf<0xB1>(x); x += dppf<0x4E>(x); x += dppf<0x141>(x); x += dppf<0x140>(x); return x; }
__device__ __forceinline__ float dot2(unsigned a, unsigned b, float c) { return __builtin_amdgcn_fdot2_f32_bf16(__builtin_bit_cast(bf16x2v, a), __builtin_bit_cast(bf16x2v, b), c, false); }
__device__ __forceinline__ float silu_b(float v) { return v * __builtin_amdgcn_rcpf(__builtin_amdgcn_exp2f(v * -1.4426950408889634f) + 1.f); }
__device__ __forceinline__ unsigned rkey(float rb, int row) {
    unsigned b = __float_as_uint(rb) >> 16; b = (b & 0x8000u) ? (~b & 0xffffu) : (b | 0x8000u);
    return (b << 16) | (unsigned)(0xffff - row);
}
__device__ __forceinline__ float keyscore(unsigned k) { const unsigned b = k >> 16; return __uint_as_float(((b & 0x8000u) ? (b & 0x7fffu) : (~b & 0xffffu)) << 16); }
#define T2M(b0, b1) { const unsigned _b0 = (b0), _b1 = (b1); const unsigned _m = min(k0, _b0); k0 = max(k0, _b0); k1 = max(_m, max(k1, _b1)); }
__device__ __forceinline__ uint2 top2wave(unsigned k0, unsigned k1) {
    T2M(dppu<0xB1>(k0), dppu<0xB1>(k1)); T2M(dppu<0x4E>(k0), dppu<0x4E>(k1));
    T2M(dppu<0x141>(k0), dppu<0x141>(k1)); T2M(dppu<0x140>(k0), dppu<0x140>(k1));
    const unsigned a0 = __builtin_amdgcn_readlane(k0, 0), a1 = __builtin_amdgcn_readlane(k1, 0);
    const unsigned c0 = __builtin_amdgcn_readlane(k0, 16), c1 = __builtin_amdgcn_readlane(k1, 16);
    const unsigned d0 = __builtin_amdgcn_readlane(k0, 32), d1 = __builtin_amdgcn_readlane(k1, 32);
    const unsigned e0 = __builtin_amdgcn_readlane(k0, 48), e1 = __builtin_amdgcn_readlane(k1, 48);
    k0 = a0; k1 = a1; T2M(c0, c1); T2M(d0, d1); T2M(e0, e1);
    return make_uint2(k0, k1);
}
template <int N> __device__ __forceinline__ void wvm() { asm volatile("" ::: "memory"); __builtin_amdgcn_s_waitcnt((N & 15) | ((N >> 4) << 14) | 0x0F70); asm volatile("" ::: "memory"); }
__device__ __forceinline__ bf16x4 trread(unsigned a) { return __builtin_amdgcn_ds_read_tr16_b64_v4i16((__attribute__((address_space(3))) bf16x4*)(size_t)a); }
template <class T> __device__ __forceinline__ T ldsr(unsigned a) { return *(const __attribute__((address_space(3))) T*)(size_t)a; }
template <class T> __device__ __forceinline__ T ldsrv(unsigned a) { return *(volatile __attribute__((address_space(3))) T*)(size_t)a; }
template <class T> __device__ __forceinline__ void ldsw(unsigned a, T v) { *(volatile __attribute__((address_space(3))) T*)(size_t)a = v; }
__device__ __forceinline__ bf16x8 cat8(bf16x4 lo, bf16x4 hi) { return (bf16x8){lo[0], lo[1], lo[2], lo[3], hi[0], hi[1], hi[2], hi[3]}; }
__device__ __forceinline__ bf16x8 frag4(unsigned a, unsigned b, unsigned c, unsigned d) { return __builtin_bit_cast(bf16x8, (u32x4){a, b, c, d}); }
__device__ __forceinline__ f32x4 mfma32(bf16x8 a, bf16x8 b, f32x4 c) { return __builtin_amdgcn_mfma_f32_16x16x32_bf16(a, b, c, 0, 0, 0); }
__device__ __forceinline__ int lane_id() { int l; asm volatile("v_mbcnt_lo_u32_b32 %0, -1, 0\n\tv_mbcnt_hi_u32_b32 %0, -1, %0" : "=v"(l)); __builtin_assume(l >= 0 && l < 64); return l; }
#define SB() __builtin_amdgcn_sched_barrier(0)
#define BAR() asm volatile("s_waitcnt lgkmcnt(0)\n\ts_barrier" ::: "memory")
#define MEMB() asm volatile("" ::: "memory")
#define RFL(x) ((unsigned)__builtin_amdgcn_readfirstlane((int)(x)))
#define SC1 16

#define WOPF 1
#define DSEL "0"
#define TRG1D
#define TRG2D
#define PRIA "s_setprio 2\n\t"
#define TCHD
#define RSLP "s_sleep 3\n\t"
#define PRIR "s_setprio 0\n\t"
#define PRI1 "s_setprio 3\n\t"
#define PRIW
#define PRIX "s_setprio 3\n\t"
#define PRIO
#define RSL0 "s_sleep 4\n\t"
#define RSL1 "s_sleep 3\n\t"
#define ASLP "s_sleep 1\n\t"
#define PRIS
#define PRIG "s_setprio 0\n\t"
#define PSLP "s_sleep 1\n\t"
#define HSL 26
#define HS2 10
#define TCH 1
#define KSL "0"
#define E2 0
#define P2A 0
#define FPOL " nt"
#define EK2 7
#define YS2 2
#define WPF 0
#define EARLYK 0
#define NOPK(v) asm("" : "+v"(v))
#define OPQ(v) asm volatile("" : "+v"(v))
#define IB(m) __builtin_amdgcn_inverse_ballot_w64(m)
#define END() do { asm volatile("s_endpgm"); __builtin_amdgcn_s_waitcnt(0); return; } while (0)
#define BARN() asm volatile("s_barrier" ::: "memory")
#define TL(i)
#define TA(i)
#define TLEND()
__device__ __forceinline__ constexpr int phys(int S) { return (S + 1) % 3; }

__global__ __launch_bounds__(512, 2) void fused(const u16* x_, const u16* wqkv_, const u16* wo_, const u16* w_r_, const u16* wg_, const u16* wu_, const u16* wd_, char* ws_, u16* out_) {
    extern __shared__ __attribute__((aligned(16))) char smem[];
#define SL(k)
#define DUMP
#define SZ
#define SB_(k)
#define SO(k)
#define RINC
#define RZ
#define SA(k)
#define ST(h)
#define SP0
#define LINC
#define GINC
#define RCNT
    typedef __attribute__((ext_vector_type(32))) unsigned u32x32;
    u32x32 sa, sb; u32x8 sc;
    unsigned tag, bid = blockIdx.x; int tid = threadIdx.x;
    typedef __attribute__((address_space(1))) char gchar;
    typedef gchar* const __attribute__((address_space(4)))* karg_t;
    karg_t ka = (karg_t)__builtin_amdgcn_kernarg_segment_ptr();
    asm volatile(
            SL(0) SZ
            "s_load_dwordx2 s[40:41], %[k], 0x8\n\ts_load_dwordx2 s[36:37], %[k], 0x0\n\tv_readfirstlane_b32 s69, %[t]\n\ts_lshr_b32 s70, %[b], 3\n\ts_cmpk_lg_u32 s69, 0xc0\n\ts_cbranch_scc1 1f\n\t"
            "s_getpc_b64 s[72:73]\n\t.Lch:\ns_lshl3_add_u32 s71, s70, 0xffffff40\n\tv_add_lshl_u32 v36, s71, %[t], 7\n\tv_min_u32 v36, .Lce-.Lch-4, v36\n\ts_mov_b32 m0, 162368\n\t"
            "s_mov_b64 exec, 0xff\n\tglobal_load_lds_dword v36, s[72:73]\n\ts_mov_b64 exec, -1\n\t1:\ns_add_i32 s70, s70, 25\n\ts_and_b32 s70, s70, 31\n\t"
            "s_cmp_lt_u32 s70, 25\n\ts_cbranch_scc0 .LF\n\tv_bfe_u32 v36, %[t], 3, 3\n\tv_and_b32 v37, 7, %[t]\n\ts_mul_i32 s71, s70, 13\n\ts_lshr_b32 s71, s71, 6\n\t"
            "v_and_b32 v38, 2, v36\n\tv_xor_b32 v46, v38, v37\n\ts_lshl_b32 s64, s71, 2\n\ts_sub_i32 s64, s70, s64\n\ts_sub_i32 s57, s64, s71\n\tv_mul_u32_u24 v40, 0xf00, v36\n\t"
            "s_sub_i32 s72, s64, 5\n\ts_min_u32 s64, s64, s72\n\tv_lshl_add_u32 v40, v46, 4, v40\n\ts_bfe_u32 s56, s69, 0x20006\n\ts_and_b32 s72, %[b], 1\n\ts_mul_i32 s72, s72, 5\n\t"
            "s_add_i32 s58, s57, s72\n\ts_lshl_b32 s58, s58, 7\n\ts_mul_i32 s61, s64, 0x78000\n\ts_mul_i32 s73, s56, 0xf000\n\ts_add_i32 s73, s73, s58\n\ts_add_i32 s61, s61, s73\n\t"
            "s_mul_i32 s66, s56, 0x1800\n\ts_add_i32 m0, s66, 53248\n\ts_mov_b32 s42, 0x258000\n\ts_mov_b32 s43, 0x20000\n\tv_add_u32 v41, 0x100, v40\n\tv_add_u32 v42, 0x200, v40\n\t"
            "v_xor_b32 v43, 64, v40\n\tv_add_u32 v43, 0x6c00, v43\n\tv_add_u32 v44, 0x1100, v43\n\tv_add_u32 v45, 0x1200, v43\n\ts_cmpk_lt_u32 s69, 0x100\n\ts_cbranch_scc1 1f\n\t"
            "s_waitcnt lgkmcnt(0)\n\tbuffer_load_dwordx4 v40, s[40:43], s61 offen lds\n\tbuffer_load_dwordx4 v41, s[40:43], s61 offen offset:1024 lds\n\tbuffer_load_dwordx4 v42, s[40:43], s61 offen offset:2048 lds\n\tbuffer_load_dwordx4 v43, s[40:43], s61 offen offset:3072 lds\n\ts_addk_i32 m0, 0x1000\n\t"
            "s_nop 0\n\tbuffer_load_dwordx4 v44, s[40:43], s61 offen lds\n\tbuffer_load_dwordx4 v45, s[40:43], s61 offen offset:1024 lds\n\t1:\ns_mul_i32 s72, s56, 28\n\tv_add_u32 v39, s72, v36\n\t"
            "s_bfe_u32 s62, %[b], 0x10001\n\ts_mul_i32 s62, s62, 5\n\ts_add_i32 s62, s62, s71\n\ts_mul_i32 s62, s62, 104\n\ts_sub_i32 s62, s62, 8\n\tv_and_b32 v47, 3, v39\n\t"
            "v_lshl_add_u32 v47, v47, 2, s62\n\tv_and_b32 v48, 0x70, v39\n\tv_bfe_u32 v49, v39, 2, 2\n\tv_add3_u32 v48, v48, v49, v47\n\tv_bfe_u32 v49, v39, 1, 3\n\tv_xor_b32 v50, v49, v37\n\t"
            "v_mul_i32_i24 v48, 0xa00, v48\n\ts_bfe_u32 s63, %[b], 0x10002\n\ts_mul_i32 s63, s63, 0x500\n\ts_lshl_b32 s60, s64, 8\n\tv_mov_b32 v51, v36\n\tv_lshl_add_u32 v36, v50, 4, v48\n\t"
            "s_add_i32 s60, s60, s63\n\ts_mul_i32 s65, s56, 0xe00\n\ts_add_i32 m0, s65, 38912\n\ts_mov_b32 s38, 0x280000\n\ts_mov_b32 s39, 0x20000\n\tv_add_u32 v38, 0x9800, v36\n\t"
            "s_cmpk_lt_u32 s69, 0x100\n\ts_cbranch_scc1 1f\n\tbuffer_load_dwordx4 v36, s[36:39], s60 offen lds\n\tbuffer_load_dwordx4 v38, s[36:39], s60 offen offset:2048 lds\n\ts_barrier\n\t1:\n"
            "s_load_dwordx2 s[44:45], %[k], 0x38\n\ts_load_dwordx2 s[48:49], %[k], 0x10\n\ts_load_dwordx2 s[52:53], %[k], 0x40\n\tv_add_u32 v39, 8, v39\n\tv_and_b32 v48, 0x70, v39\n\tv_bfe_u32 v49, v39, 2, 2\n\t"
            "v_add3_u32 v48, v48, v49, v47\n\tv_xor_b32 v50, 4, v50\n\tv_mul_i32_i24 v48, 0xa00, v48\n\tv_lshl_add_u32 v37, v50, 4, v48\n\tv_subrev_u32 v37, 0x400, v37\n\tv_add_u32 v39, 0x9800, v37\n\t"
            "s_addk_i32 s66, 14336\n\ts_bfe_u32 s59, %[b], 0x20001\n\ts_mul_i32 s59, s59, 5\n\ts_add_i32 s59, s59, s71\n\ts_mov_b32 s46, 3432448\n\ts_mov_b32 s47, 0x20000\n\t"
            "s_mov_b32 s54, 0x280000\n\ts_mov_b32 s55, 0x20000\n\tv_and_b32 v52, 63, %[t]\n\tv_bfe_u32 v53, %[t], 4, 2\n\tv_and_b32 v54, 15, %[t]\n\tv_and_b32 v55, 3, %[t]\n\t"
            "v_bfe_u32 v56, %[t], 2, 2\n\tv_lshl_add_u32 v57, v55, 2, v56\n\ts_lshr_b32 s69, s69, 6\n\ts_cmp_lt_u32 s69, 4\n\ts_cbranch_scc0 .LB\n\ts_branch .LA\n\t"
            ".LF:\ns_load_dwordx2 s[72:73], %[k], 0x18\n\ts_and_b32 s71, %[b], 1\n\ts_lshl1_add_u32 s71, s70, s71\n\ts_mul_i32 s71, s71, 40\n\ts_sub_u32 s71, s71, 2000\n\t"
            "v_lshrrev_b32 v2, 4, %[t]\n\tv_add_u32 v2, s71, v2\n\tv_add_u32 v3, 32, v2\n\tv_min_u32 v2, 0x1ff, v2\n\tv_min_u32 v3, 0x1ff, v3\n\tv_and_b32 v4, 15, %[t]\n\t"
            "v_lshlrev_b32 v4, 4, v4\n\ts_bfe_u32 s71, %[b], 0x10001\n\ts_mul_i32 s71, s71, 0x140000\n\ts_bfe_u32 s74, %[b], 0x10002\n\ts_mul_i32 s74, s74, 0x500\n\ts_add_u32 s71, s71, s74\n\t"
            "v_add_u32 v5, s71, v4\n\ts_movk_i32 s71, 0xa00\n\tv_mad_u32_u24 v2, v2, s71, v5\n\tv_mad_u32_u24 v3, v3, s71, v5\n\ts_waitcnt lgkmcnt(0)\n\tglobal_load_dwordx4 v[104:107], v4, s[72:73] offset:0\n\t"
            "global_load_dwordx4 v[108:111], v4, s[72:73] offset:256\n\tglobal_load_dwordx4 v[112:115], v4, s[72:73] offset:512\n\tglobal_load_dwordx4 v[116:119], v4, s[72:73] offset:768\n\tglobal_load_dwordx4 v[120:123], v4, s[72:73] offset:1024\n\ts_cmpk_ge_u32 s69, 0x80\n\ts_cbranch_scc1 1f\n\t"
            "global_load_dwordx4 v[84:87], v3, s[36:37] offset:0\n\tglobal_load_dwordx4 v[88:91], v3, s[36:37] offset:256\n\tglobal_load_dwordx4 v[92:95], v3, s[36:37] offset:512\n\tglobal_load_dwordx4 v[96:99], v3, s[36:37] offset:768\n\tglobal_load_dwordx4 v[100:103], v3, s[36:37] offset:1024\n\t1:\n"
            "global_load_dwordx4 v[64:67], v2, s[36:37] offset:0\n\tglobal_load_dwordx4 v[68:71], v2, s[36:37] offset:256\n\tglobal_load_dwordx4 v[72:75], v2, s[36:37] offset:512\n\tglobal_load_dwordx4 v[76:79], v2, s[36:37] offset:768\n\tglobal_load_dwordx4 v[80:83], v2, s[36:37] offset:1024\n\ts_branch .Lout\n\t"
            ".LA:\nv_lshrrev_b32 v2, 1, v54\n\tv_xor_b32 v2, v2, v53\n\tv_lshlrev_b32 v3, 7, v54\n\tv_lshl_add_u32 v2, v2, 4, v3\n\tv_xor_b32 v3, 64, v2\n\t"
            "v_add_u32 v4, 38912, v2\n\tv_add_u32 v5, 38912, v3\n\tv_add_u32 v10, 77824, v2\n\tv_add_u32 v11, 77824, v3\n\tv_mov_b32 v12, 159872\n\tv_cmp_eq_u32 vcc, 0, v54\n\t"
            "v_mov_b32 v60, 0\n\tv_mov_b32 v61, 0\n\tv_mov_b32 v62, 0x3f803f80\n\tv_lshlrev_b32 v63, 3, v52\n\tv_cndmask_b32 v6, v2, v12, vcc\n\tv_cndmask_b32 v7, v3, v12, vcc\n\t"
            "v_cndmask_b32 v8, v4, v12, vcc\n\tv_cndmask_b32 v9, v5, v12, vcc\n\tv_cndmask_b32 v10, v10, v12, vcc\n\tv_cndmask_b32 v11, v11, v12, vcc\n\tv_lshlrev_b32 v64, 2, v52\n\tv_lshrrev_b32 v65, 2, v52\n\t"
            "v_cmp_eq_u32 vcc, 8, v65\n\tv_add_u32 v66, 161088, v63\n\tv_add_u32 v64, 159744, v64\n\tv_cndmask_b32 v62, 0, v62, vcc\n\ts_cmp_lg_u32 s69, 1\n\ts_cbranch_scc1 1f\n\t"
            "ds_write_b32 v64, v62\n\tds_write_b64 v66, v[60:61]\n\t1:\nv_bfe_u32 v12, v52, 3, 2\n\tv_xor_b32 v12, s69, v12\n\tv_lshlrev_b32 v13, 7, v56\n\t"
            "v_lshl_add_u32 v13, v55, 3, v13\n\tv_lshl_add_u32 v12, v12, 5, v13\n\tv_mul_u32_u24 v13, 0xc00, v53\n\ts_movk_i32 s72, 14336\n\tv_add3_u32 v12, v12, v13, s72\n\tv_add_u32 v13, 38912, v12\n\t"
            "s_mov_b32 s72, 160000\n\tv_lshl_add_u32 v14, v54, 2, s72\n\tv_lshl_add_u32 v15, v53, 4, s72\n\ts_cmp_eq_u32 s69, 3\n\ts_cselect_b32 s74, 1, 0\n\ts_cselect_b64 s[76:77], -1, 0\n\t"
            "s_cselect_b32 s75, 4, 0\n\ts_add_i32 s72, s69, 1\n\ts_bfe_u32 s72, s72, 0x10001\n\ts_xor_b32 s72, s72, 1\n\ts_lshr_b32 s73, s69, 1\n\ts_lshl1_add_u32 s78, s72, s73\n\t"
            "s_lshl2_add_u32 s78, s78, 159776\n\tv_mov_b32 v34, s78\n\ts_lshl2_add_u32 s78, s69, 159744\n\tv_mov_b32 v33, s78\n\ts_mul_i32 s78, s59, 163840\n\ts_lshl_b32 s79, s58, 5\n\t"
            "s_add_i32 s78, s78, s79\n\ts_lshl_b32 s79, s69, 10\n\ts_add_i32 s78, s78, s79\n\ts_add_i32 s78, s78, 155648\n\tv_lshl_add_u32 v16, v52, 4, s78\n\tv_and_b32 v215, 1, v53\n\t"
            "v_lshl_add_u32 v215, v215, 30, v16\n\ts_lshl_b32 s79, s74, 11\n\ts_add_i32 s78, s78, s79\n\ts_sub_i32 s78, s78, 0x400\n\ts_cmp_lg_u32 s57, s75\n\ts_cselect_b32 s79, s72, 0\n\t"
            "s_cmp_lg_u32 s79, 0\n\ts_cselect_b32 s78, s78, 0x40000000\n\tv_lshl_add_u32 v17, v52, 4, s78\n\ts_sub_i32 s78, s69, s57\n\ts_add_i32 s79, s78, 5\n\ts_min_u32 s78, s78, s79\n\t"
            "v_cmp_gt_u32 s[80:81], s78, v53\n\tv_cmp_gt_u32 s[82:83], s78, v56\n\ts_mov_b32 s14, 0x33363333\n\ts_mov_b32 s15, 0x36663366\n\ts_mov_b32 s16, 0x666c6666\n\ts_mov_b32 s17, 0x6ccc66cc\n\t"
            "s_mov_b32 s18, 0x33333333\n\ts_mov_b32 s19, 0x33333333\n\ts_xor_b64 s[80:81], s[80:81], s[76:77]\n\ts_xor_b64 s[82:83], s[82:83], s[76:77]\n\ts_andn2_b64 vcc, s[80:81], s[18:19]\n\tv_cndmask_b32 v18, 0, -1, vcc\n\t"
            "s_and_b64 vcc, s[80:81], s[18:19]\n\tv_cndmask_b32 v19, 0, -1, vcc\n\ts_nor_b64 vcc, s[80:81], s[18:19]\n\tv_cndmask_b32 v20, 0, -1, vcc\n\ts_andn2_b64 vcc, s[18:19], s[80:81]\n\tv_cndmask_b32 v21, 0, -1, vcc\n\t"
            "v_cndmask_b32 v22, 0, -1, s[82:83]\n\tv_cndmask_b32 v23, -1, 0, s[82:83]\n\tv_sub_u32 v58, v57, v53\n\tv_add_u32 v58, 4, v58\n\tv_and_b32 v58, 28, v58\n\tv_lshlrev_b32 v58, 1, v58\n\t"
            "v_mov_b32 v24, -1\n\tv_mov_b32 v25, 0\n\tv_lshlrev_b64 v[24:25], v58, v[24:25]\n\tv_lshlrev_b64 v[24:25], v58, v[24:25]\n\tv_sub_u32 v58, 32, v58\n\tv_min_u32 v58, 16, v58\n\t"
            "v_lshrrev_b32 v26, v58, -1\n\tv_lshrrev_b32 v26, v58, v26\n\ts_lshl1_add_u32 s78, s69, s74\n\ts_xor_b32 s79, s78, 1\n\ts_mul_i32 s78, s78, 0xc00\n\ts_mul_i32 s79, s79, 0xc00\n\t"
            "s_addk_i32 s78, 14336\n\ts_addk_i32 s79, 14336\n\tv_add_u32 v28, s78, v63\n\tv_add_u32 v29, s79, v63\n\ts_cmp_eq_u32 s69, s57\n\ts_addc_u32 s78, s69, 0\n\t"
            "s_add_i32 s79, s69, 1\n\ts_cmp_eq_u32 s79, s57\n\ts_subb_u32 s79, s79, 0\n\ts_cmp_eq_u32 s74, 1\n\ts_cselect_b32 s80, s79, s78\n\ts_cselect_b32 s81, s78, s79\n\t"
            "v_lshlrev_b32 v58, 2, v53\n\tv_lshl_add_u32 v58, v54, 4, v58\n\ts_cmp_gt_u32 s80, s57\n\ts_subb_u32 s82, s80, 0\n\ts_lshl2_add_u32 s82, s82, 159760\n\tv_mov_b32 v59, s82\n\t"
            "s_lshl_b32 s82, s80, 1\n\ts_max_u32 s82, s82, 1\n\ts_mul_i32 s82, s82, 0xc00\n\ts_addk_i32 s82, 11264\n\tv_add_u32 v30, s82, v58\n\ts_cmp_gt_u32 s81, s57\n\t"
            "s_subb_u32 s82, s81, 0\n\ts_lshl2_add_u32 s82, s82, 159760\n\tv_mov_b32 v60, s82\n\ts_lshl_b32 s82, s81, 1\n\ts_max_u32 s82, s82, 1\n\ts_mul_i32 s82, s82, 0xc00\n\t"
            "s_addk_i32 s82, 11264\n\tv_add_u32 v31, s82, v58\n\tv_cndmask_b32 v32, v59, v60, s[18:19]\n\tv_mov_b32 v35, 1\n\ts_waitcnt lgkmcnt(0)\n\ts_barrier\n\t"
            "s_add_u32 m0, s65, 38912\n\ts_load_dword s67, s[44:45], 0x0\n\tbuffer_load_dwordx4 v37, s[36:39], s60 offen offset:1024 lds\n\ts_mov_b32 exec_hi, 0\n\tbuffer_load_dwordx4 v39, s[36:39], s60 offen offset:3072 lds\n\ts_mov_b32 exec_hi, -1\n\t"
            "s_add_u32 m0, s66, 77824\n\ts_add_u32 s61, s61, 0x3c000\n\tbuffer_load_dwordx4 v40, s[40:43], s61 offen lds\n\tbuffer_load_dwordx4 v41, s[40:43], s61 offen offset:1024 lds\n\tbuffer_load_dwordx4 v42, s[40:43], s61 offen offset:2048 lds\n\tbuffer_load_dwordx4 v43, s[40:43], s61 offen offset:3072 lds\n\t"
            "s_addk_i32 m0, 0x1000\n\ts_add_u32 s60, s60, 128\n\tbuffer_load_dwordx4 v44, s[40:43], s61 offen lds\n\tbuffer_load_dwordx4 v45, s[40:43], s61 offen offset:1024 lds\n\ts_add_u32 m0, s65, 77824\n\ts_mov_b32 s20, 0\n\t"
            "s_mov_b32 s21, 40960\n\ts_mov_b32 s22, 81920\n\ts_mov_b32 s23, 122880\n\tbuffer_load_dwordx4 v36, s[36:39], s60 offen lds\n\tbuffer_load_dwordx4 v37, s[36:39], s60 offen offset:1024 lds\n\tbuffer_load_dwordx4 v38, s[36:39], s60 offen offset:2048 lds\n\t"
            "s_mov_b32 exec_hi, 0\n\tbuffer_load_dwordx4 v39, s[36:39], s60 offen offset:3072 lds\n\ts_mov_b32 exec_hi, -1\n\ts_mov_b32 s68, 0x40000\n\ts_waitcnt lgkmcnt(0)\n\ts_mul_i32 s67, s67, 0x19660d\n\t"
            "s_add_u32 s67, s67, 0x3c6ef35f\n\ts_waitcnt vmcnt(10)\n\ts_barrier\n\t"
            SA(1) ST(-1)
            "ds_read_b64_tr_b16 v[176:177], v13 offset:0\n\tds_read_b64_tr_b16 v[178:179], v13 offset:512\n\tds_read_b128 v[120:123], v8\n\tds_read_b128 v[124:127], v4 offset:2048\n\tds_read_b128 v[128:131], v4 offset:4096\n\tds_read_b128 v[132:135], v4 offset:6144\n\t"
            "ds_read_b128 v[136:139], v4 offset:8192\n\tds_read_b128 v[140:143], v4 offset:10240\n\tds_read_b128 v[144:147], v4 offset:12288\n\tds_read_b64_tr_b16 v[180:181], v13 offset:1024\n\tds_read_b64_tr_b16 v[182:183], v13 offset:1536\n\tds_read_b64_tr_b16 v[184:185], v13 offset:2048\n\t"
            "ds_read_b64_tr_b16 v[186:187], v13 offset:2560\n\ts_waitcnt lgkmcnt(10)\n\tv_mfma_f32_16x16x32_bf16 v[36:39], v[176:179], v[120:123], 0\n\ts_waitcnt lgkmcnt(9)\n\tv_mfma_f32_16x16x32_bf16 v[48:51], v[176:179], v[124:127], 0\n\ts_waitcnt lgkmcnt(8)\n\t"
            "v_mfma_f32_16x16x32_bf16 v[60:63], v[176:179], v[128:131], 0\n\tds_read_b64_tr_b16 v[188:189], v13 offset:12288\n\tds_read_b64_tr_b16 v[190:191], v13 offset:12800\n\ts_waitcnt lgkmcnt(9)\n\tv_mfma_f32_16x16x32_bf16 v[72:75], v[176:179], v[132:135], 0\n\tds_read_b128 v[148:151], v9\n\t"
            "s_waitcnt lgkmcnt(9)\n\tv_mfma_f32_16x16x32_bf16 v[84:87], v[176:179], v[136:139], 0\n\tds_read_b128 v[152:155], v5 offset:2048\n\ts_waitcnt lgkmcnt(9)\n\tv_mfma_f32_16x16x32_bf16 v[96:99], v[176:179], v[140:143], 0\n\tds_read_b128 v[156:159], v5 offset:4096\n\t"
            "s_waitcnt lgkmcnt(9)\n\tv_mfma_f32_16x16x32_bf16 v[108:111], v[176:179], v[144:147], 0\n\tds_read_b128 v[160:163], v5 offset:6144\n\ts_waitcnt lgkmcnt(8)\n\tv_mfma_f32_16x16x32_bf16 v[40:43], v[180:183], v[120:123], 0\n\tds_read_b128 v[164:167], v5 offset:8192\n\t"
            "v_mfma_f32_16x16x32_bf16 v[52:55], v[180:183], v[124:127], 0\n\tds_read_b128 v[168:171], v5 offset:10240\n\tv_mfma_f32_16x16x32_bf16 v[64:67], v[180:183], v[128:131], 0\n\tds_read_b128 v[172:175], v5 offset:12288\n\tv_mfma_f32_16x16x32_bf16 v[76:79], v[180:183], v[132:135], 0\n\tds_read_b64_tr_b16 v[192:193], v13 offset:13312\n\t"
            "ds_read_b64_tr_b16 v[194:195], v13 offset:13824\n\tv_mfma_f32_16x16x32_bf16 v[88:91], v[180:183], v[136:139], 0\n\tds_read_b64_tr_b16 v[196:197], v13 offset:14336\n\tds_read_b64_tr_b16 v[198:199], v13 offset:14848\n\tv_mfma_f32_16x16x32_bf16 v[100:103], v[180:183], v[140:143], 0\n\tv_mfma_f32_16x16x32_bf16 v[112:115], v[180:183], v[144:147], 0\n\t"
            "s_waitcnt lgkmcnt(13)\n\tv_mfma_f32_16x16x32_bf16 v[44:47], v[120:123], v[184:187], 0\n\tv_mfma_f32_16x16x32_bf16 v[56:59], v[124:127], v[184:187], 0\n\tv_mfma_f32_16x16x32_bf16 v[68:71], v[128:131], v[184:187], 0\n\tv_mfma_f32_16x16x32_bf16 v[80:83], v[132:135], v[184:187], 0\n\tv_mfma_f32_16x16x32_bf16 v[92:95], v[136:139], v[184:187], 0\n\t"
            "v_mfma_f32_16x16x32_bf16 v[104:107], v[140:143], v[184:187], 0\n\tv_mfma_f32_16x16x32_bf16 v[116:119], v[144:147], v[184:187], 0\n\ts_waitcnt lgkmcnt(10)\n\tv_mfma_f32_16x16x32_bf16 v[36:39], v[188:191], v[148:151], v[36:39]\n\ts_waitcnt lgkmcnt(9)\n\tv_mfma_f32_16x16x32_bf16 v[48:51], v[188:191], v[152:155], v[48:51]\n\t"
            "s_waitcnt lgkmcnt(8)\n\tv_mfma_f32_16x16x32_bf16 v[60:63], v[188:191], v[156:159], v[60:63]\n\ts_waitcnt lgkmcnt(7)\n\tv_mfma_f32_16x16x32_bf16 v[72:75], v[188:191], v[160:163], v[72:75]\n\ts_waitcnt lgkmcnt(6)\n\tv_mfma_f32_16x16x32_bf16 v[84:87], v[188:191], v[164:167], v[84:87]\n\t"
            "s_waitcnt lgkmcnt(5)\n\tv_mfma_f32_16x16x32_bf16 v[96:99], v[188:191], v[168:171], v[96:99]\n\ts_waitcnt lgkmcnt(4)\n\tv_mfma_f32_16x16x32_bf16 v[108:111], v[188:191], v[172:175], v[108:111]\n\ts_waitcnt lgkmcnt(2)\n\tv_mfma_f32_16x16x32_bf16 v[40:43], v[192:195], v[148:151], v[40:43]\n\t"
            "v_mfma_f32_16x16x32_bf16 v[52:55], v[192:195], v[152:155], v[52:55]\n\ts_waitcnt vmcnt(0) lgkmcnt(0)\n\ts_barrier\n\t"
            ST(1)
            "ds_read_b64_tr_b16 v[176:177], v13 offset:38912\n\tds_read_b64_tr_b16 v[178:179], v13 offset:39424\n\tv_mfma_f32_16x16x32_bf16 v[64:67], v[192:195], v[156:159], v[64:67]\n\tds_read_b128 v[120:123], v10\n\tv_mfma_f32_16x16x32_bf16 v[76:79], v[192:195], v[160:163], v[76:79]\n\tds_read_b128 v[124:127], v4 offset:40960\n\t"
            "v_mfma_f32_16x16x32_bf16 v[88:91], v[192:195], v[164:167], v[88:91]\n\tds_read_b128 v[128:131], v4 offset:43008\n\tv_mfma_f32_16x16x32_bf16 v[100:103], v[192:195], v[168:171], v[100:103]\n\tds_read_b128 v[132:135], v4 offset:45056\n\tv_mfma_f32_16x16x32_bf16 v[112:115], v[192:195], v[172:175], v[112:115]\n\tds_read_b128 v[136:139], v4 offset:47104\n\t"
            "v_mfma_f32_16x16x32_bf16 v[44:47], v[148:151], v[196:199], v[44:47]\n\tds_read_b128 v[140:143], v4 offset:49152\n\tv_mfma_f32_16x16x32_bf16 v[56:59], v[152:155], v[196:199], v[56:59]\n\tds_read_b128 v[144:147], v4 offset:51200\n\tv_mfma_f32_16x16x32_bf16 v[68:71], v[156:159], v[196:199], v[68:71]\n\tds_read_b64_tr_b16 v[180:181], v13 offset:39936\n\t"
            "ds_read_b64_tr_b16 v[182:183], v13 offset:40448\n\tv_mfma_f32_16x16x32_bf16 v[80:83], v[160:163], v[196:199], v[80:83]\n\tds_read_b64_tr_b16 v[184:185], v13 offset:40960\n\tds_read_b64_tr_b16 v[186:187], v13 offset:41472\n\tv_mfma_f32_16x16x32_bf16 v[92:95], v[164:167], v[196:199], v[92:95]\n\tv_mfma_f32_16x16x32_bf16 v[104:107], v[168:171], v[196:199], v[104:107]\n\t"
            "v_mfma_f32_16x16x32_bf16 v[116:119], v[172:175], v[196:199], v[116:119]\n\ts_waitcnt lgkmcnt(10)\n\tv_mfma_f32_16x16x32_bf16 v[36:39], v[176:179], v[120:123], v[36:39]\n\ts_waitcnt lgkmcnt(9)\n\tv_mfma_f32_16x16x32_bf16 v[48:51], v[176:179], v[124:127], v[48:51]\n\ts_waitcnt lgkmcnt(8)\n\t"
            "v_mfma_f32_16x16x32_bf16 v[60:63], v[176:179], v[128:131], v[60:63]\n\tds_read_b64_tr_b16 v[188:189], v13 offset:51200\n\tds_read_b64_tr_b16 v[190:191], v13 offset:51712\n\ts_waitcnt lgkmcnt(9)\n\tv_mfma_f32_16x16x32_bf16 v[72:75], v[176:179], v[132:135], v[72:75]\n\tds_read_b128 v[148:151], v11\n\t"
            "s_waitcnt lgkmcnt(9)\n\tv_mfma_f32_16x16x32_bf16 v[84:87], v[176:179], v[136:139], v[84:87]\n\tds_read_b128 v[152:155], v5 offset:40960\n\ts_waitcnt lgkmcnt(9)\n\tv_mfma_f32_16x16x32_bf16 v[96:99], v[176:179], v[140:143], v[96:99]\n\tds_read_b128 v[156:159], v5 offset:43008\n\t"
            "s_waitcnt lgkmcnt(9)\n\tv_mfma_f32_16x16x32_bf16 v[108:111], v[176:179], v[144:147], v[108:111]\n\tds_read_b128 v[160:163], v5 offset:45056\n\ts_waitcnt lgkmcnt(8)\n\tv_mfma_f32_16x16x32_bf16 v[40:43], v[180:183], v[120:123], v[40:43]\n\tds_read_b128 v[164:167], v5 offset:47104\n\t"
            "v_mfma_f32_16x16x32_bf16 v[52:55], v[180:183], v[124:127], v[52:55]\n\tds_read_b128 v[168:171], v5 offset:49152\n\tv_mfma_f32_16x16x32_bf16 v[64:67], v[180:183], v[128:131], v[64:67]\n\tds_read_b128 v[172:175], v5 offset:51200\n\tv_mfma_f32_16x16x32_bf16 v[76:79], v[180:183], v[132:135], v[76:79]\n\tds_read_b64_tr_b16 v[192:193], v13 offset:52224\n\t"
            "ds_read_b64_tr_b16 v[194:195], v13 offset:52736\n\tv_mfma_f32_16x16x32_bf16 v[88:91], v[180:183], v[136:139], v[88:91]\n\tds_read_b64_tr_b16 v[196:197], v13 offset:53248\n\tds_read_b64_tr_b16 v[198:199], v13 offset:53760\n\tv_mfma_f32_16x16x32_bf16 v[100:103], v[180:183], v[140:143], v[100:103]\n\tv_mfma_f32_16x16x32_bf16 v[112:115], v[180:183], v[144:147], v[112:115]\n\t"
            "s_waitcnt lgkmcnt(13)\n\tv_mfma_f32_16x16x32_bf16 v[44:47], v[120:123], v[184:187], v[44:47]\n\tv_mfma_f32_16x16x32_bf16 v[56:59], v[124:127], v[184:187], v[56:59]\n\tv_mfma_f32_16x16x32_bf16 v[68:71], v[128:131], v[184:187], v[68:71]\n\tv_mfma_f32_16x16x32_bf16 v[80:83], v[132:135], v[184:187], v[80:83]\n\tv_mfma_f32_16x16x32_bf16 v[92:95], v[136:139], v[184:187], v[92:95]\n\t"
            "v_mfma_f32_16x16x32_bf16 v[104:107], v[140:143], v[184:187], v[104:107]\n\tv_mfma_f32_16x16x32_bf16 v[116:119], v[144:147], v[184:187], v[116:119]\n\ts_waitcnt lgkmcnt(10)\n\tv_mfma_f32_16x16x32_bf16 v[36:39], v[188:191], v[148:151], v[36:39]\n\ts_waitcnt lgkmcnt(9)\n\tv_mfma_f32_16x16x32_bf16 v[48:51], v[188:191], v[152:155], v[48:51]\n\t"
            "s_waitcnt lgkmcnt(8)\n\tv_mfma_f32_16x16x32_bf16 v[60:63], v[188:191], v[156:159], v[60:63]\n\ts_waitcnt lgkmcnt(7)\n\tv_mfma_f32_16x16x32_bf16 v[72:75], v[188:191], v[160:163], v[72:75]\n\ts_waitcnt lgkmcnt(6)\n\tv_mfma_f32_16x16x32_bf16 v[84:87], v[188:191], v[164:167], v[84:87]\n\t"
            "s_waitcnt lgkmcnt(5)\n\tv_mfma_f32_16x16x32_bf16 v[96:99], v[188:191], v[168:171], v[96:99]\n\ts_waitcnt lgkmcnt(4)\n\tv_mfma_f32_16x16x32_bf16 v[108:111], v[188:191], v[172:175], v[108:111]\n\ts_waitcnt lgkmcnt(2)\n\tv_mfma_f32_16x16x32_bf16 v[40:43], v[192:195], v[148:151], v[40:43]\n\t"
            "v_mfma_f32_16x16x32_bf16 v[52:55], v[192:195], v[152:155], v[52:55]\n\ts_waitcnt lgkmcnt(0)\n\ts_barrier\n\t"
            ST(3)
            "ds_read_b64_tr_b16 v[176:177], v12 offset:0\n\tds_read_b64_tr_b16 v[178:179], v12 offset:512\n\tv_mfma_f32_16x16x32_bf16 v[64:67], v[192:195], v[156:159], v[64:67]\n\tds_read_b128 v[120:123], v6\n\tv_mfma_f32_16x16x32_bf16 v[76:79], v[192:195], v[160:163], v[76:79]\n\tds_read_b128 v[124:127], v2 offset:2048\n\t"
            "v_mfma_f32_16x16x32_bf16 v[88:91], v[192:195], v[164:167], v[88:91]\n\tds_read_b128 v[128:131], v2 offset:4096\n\tv_mfma_f32_16x16x32_bf16 v[100:103], v[192:195], v[168:171], v[100:103]\n\tds_read_b128 v[132:135], v2 offset:6144\n\tv_mfma_f32_16x16x32_bf16 v[112:115], v[192:195], v[172:175], v[112:115]\n\tds_read_b128 v[136:139], v2 offset:8192\n\t"
            "v_mfma_f32_16x16x32_bf16 v[44:47], v[148:151], v[196:199], v[44:47]\n\tds_read_b128 v[140:143], v2 offset:10240\n\tv_mfma_f32_16x16x32_bf16 v[56:59], v[152:155], v[196:199], v[56:59]\n\tds_read_b128 v[144:147], v2 offset:12288\n\tv_mfma_f32_16x16x32_bf16 v[68:71], v[156:159], v[196:199], v[68:71]\n\tds_read_b64_tr_b16 v[180:181], v12 offset:1024\n\t"
            "ds_read_b64_tr_b16 v[182:183], v12 offset:1536\n\tv_mfma_f32_16x16x32_bf16 v[80:83], v[160:163], v[196:199], v[80:83]\n\tds_read_b64_tr_b16 v[184:185], v12 offset:2048\n\tds_read_b64_tr_b16 v[186:187], v12 offset:2560\n\tv_mfma_f32_16x16x32_bf16 v[92:95], v[164:167], v[196:199], v[92:95]\n\tv_mfma_f32_16x16x32_bf16 v[104:107], v[168:171], v[196:199], v[104:107]\n\t"
            "v_mfma_f32_16x16x32_bf16 v[116:119], v[172:175], v[196:199], v[116:119]\n\ts_waitcnt lgkmcnt(10)\n\tv_mfma_f32_16x16x32_bf16 v[36:39], v[176:179], v[120:123], v[36:39]\n\ts_waitcnt lgkmcnt(9)\n\tv_mfma_f32_16x16x32_bf16 v[48:51], v[176:179], v[124:127], v[48:51]\n\ts_waitcnt lgkmcnt(8)\n\t"
            "v_mfma_f32_16x16x32_bf16 v[60:63], v[176:179], v[128:131], v[60:63]\n\tds_read_b64_tr_b16 v[188:189], v12 offset:12288\n\tds_read_b64_tr_b16 v[190:191], v12 offset:12800\n\ts_waitcnt lgkmcnt(9)\n\tv_mfma_f32_16x16x32_bf16 v[72:75], v[176:179], v[132:135], v[72:75]\n\tds_read_b128 v[148:151], v7\n\t"
            "s_waitcnt lgkmcnt(9)\n\tv_mfma_f32_16x16x32_bf16 v[84:87], v[176:179], v[136:139], v[84:87]\n\tds_read_b128 v[152:155], v3 offset:2048\n\ts_waitcnt lgkmcnt(9)\n\tv_mfma_f32_16x16x32_bf16 v[96:99], v[176:179], v[140:143], v[96:99]\n\tds_read_b128 v[156:159], v3 offset:4096\n\t"
            "s_waitcnt lgkmcnt(9)\n\tv_mfma_f32_16x16x32_bf16 v[108:111], v[176:179], v[144:147], v[108:111]\n\tds_read_b128 v[160:163], v3 offset:6144\n\ts_waitcnt lgkmcnt(8)\n\tv_mfma_f32_16x16x32_bf16 v[40:43], v[180:183], v[120:123], v[40:43]\n\tds_read_b128 v[164:167], v3 offset:8192\n\t"
            "v_mfma_f32_16x16x32_bf16 v[52:55], v[180:183], v[124:127], v[52:55]\n\tds_read_b128 v[168:171], v3 offset:10240\n\tv_mfma_f32_16x16x32_bf16 v[64:67], v[180:183], v[128:131], v[64:67]\n\tds_read_b128 v[172:175], v3 offset:12288\n\tv_mfma_f32_16x16x32_bf16 v[76:79], v[180:183], v[132:135], v[76:79]\n\tds_read_b64_tr_b16 v[192:193], v12 offset:13312\n\t"
            "ds_read_b64_tr_b16 v[194:195], v12 offset:13824\n\tv_mfma_f32_16x16x32_bf16 v[88:91], v[180:183], v[136:139], v[88:91]\n\tds_read_b64_tr_b16 v[196:197], v12 offset:14336\n\tds_read_b64_tr_b16 v[198:199], v12 offset:14848\n\tv_mfma_f32_16x16x32_bf16 v[100:103], v[180:183], v[140:143], v[100:103]\n\tv_mfma_f32_16x16x32_bf16 v[112:115], v[180:183], v[144:147], v[112:115]\n\t"
            "s_waitcnt lgkmcnt(13)\n\tv_mfma_f32_16x16x32_bf16 v[44:47], v[120:123], v[184:187], v[44:47]\n\tv_mfma_f32_16x16x32_bf16 v[56:59], v[124:127], v[184:187], v[56:59]\n\tv_mfma_f32_16x16x32_bf16 v[68:71], v[128:131], v[184:187], v[68:71]\n\tv_mfma_f32_16x16x32_bf16 v[80:83], v[132:135], v[184:187], v[80:83]\n\tv_mfma_f32_16x16x32_bf16 v[92:95], v[136:139], v[184:187], v[92:95]\n\t"
            "v_mfma_f32_16x16x32_bf16 v[104:107], v[140:143], v[184:187], v[104:107]\n\tv_mfma_f32_16x16x32_bf16 v[116:119], v[144:147], v[184:187], v[116:119]\n\ts_waitcnt lgkmcnt(10)\n\tv_mfma_f32_16x16x32_bf16 v[36:39], v[188:191], v[148:151], v[36:39]\n\ts_waitcnt lgkmcnt(9)\n\tv_mfma_f32_16x16x32_bf16 v[48:51], v[188:191], v[152:155], v[48:51]\n\t"
            "s_waitcnt lgkmcnt(8)\n\tv_mfma_f32_16x16x32_bf16 v[60:63], v[188:191], v[156:159], v[60:63]\n\ts_waitcnt lgkmcnt(7)\n\tv_mfma_f32_16x16x32_bf16 v[72:75], v[188:191], v[160:163], v[72:75]\n\ts_waitcnt lgkmcnt(6)\n\tv_mfma_f32_16x16x32_bf16 v[84:87], v[188:191], v[164:167], v[84:87]\n\t"
            "s_waitcnt lgkmcnt(5)\n\tv_mfma_f32_16x16x32_bf16 v[96:99], v[188:191], v[168:171], v[96:99]\n\ts_waitcnt lgkmcnt(4)\n\tv_mfma_f32_16x16x32_bf16 v[108:111], v[188:191], v[172:175], v[108:111]\n\ts_waitcnt lgkmcnt(2)\n\tv_mfma_f32_16x16x32_bf16 v[40:43], v[192:195], v[148:151], v[40:43]\n\t"
            "v_mfma_f32_16x16x32_bf16 v[52:55], v[192:195], v[152:155], v[52:55]\n\ts_waitcnt lgkmcnt(0)\n\ts_barrier\n\t"
            ST(5)
            "ds_read_b64_tr_b16 v[176:177], v13 offset:0\n\tds_read_b64_tr_b16 v[178:179], v13 offset:512\n\tv_mfma_f32_16x16x32_bf16 v[64:67], v[192:195], v[156:159], v[64:67]\n\tds_read_b128 v[120:123], v8\n\tv_mfma_f32_16x16x32_bf16 v[76:79], v[192:195], v[160:163], v[76:79]\n\tds_read_b128 v[124:127], v4 offset:2048\n\t"
            "v_mfma_f32_16x16x32_bf16 v[88:91], v[192:195], v[164:167], v[88:91]\n\tds_read_b128 v[128:131], v4 offset:4096\n\tv_mfma_f32_16x16x32_bf16 v[100:103], v[192:195], v[168:171], v[100:103]\n\tds_read_b128 v[132:135], v4 offset:6144\n\tv_mfma_f32_16x16x32_bf16 v[112:115], v[192:195], v[172:175], v[112:115]\n\tds_read_b128 v[136:139], v4 offset:8192\n\t"
            "v_mfma_f32_16x16x32_bf16 v[44:47], v[148:151], v[196:199], v[44:47]\n\tds_read_b128 v[140:143], v4 offset:10240\n\tv_mfma_f32_16x16x32_bf16 v[56:59], v[152:155], v[196:199], v[56:59]\n\tds_read_b128 v[144:147], v4 offset:12288\n\tv_mfma_f32_16x16x32_bf16 v[68:71], v[156:159], v[196:199], v[68:71]\n\tds_read_b64_tr_b16 v[180:181], v13 offset:1024\n\t"
            "ds_read_b64_tr_b16 v[182:183], v13 offset:1536\n\tv_mfma_f32_16x16x32_bf16 v[80:83], v[160:163], v[196:199], v[80:83]\n\tds_read_b64_tr_b16 v[184:185], v13 offset:2048\n\tds_read_b64_tr_b16 v[186:187], v13 offset:2560\n\tv_mfma_f32_16x16x32_bf16 v[92:95], v[164:167], v[196:199], v[92:95]\n\tv_mfma_f32_16x16x32_bf16 v[104:107], v[168:171], v[196:199], v[104:107]\n\t"
            "v_mfma_f32_16x16x32_bf16 v[116:119], v[172:175], v[196:199], v[116:119]\n\ts_waitcnt lgkmcnt(10)\n\tv_mfma_f32_16x16x32_bf16 v[36:39], v[176:179], v[120:123], v[36:39]\n\ts_waitcnt lgkmcnt(9)\n\tv_mfma_f32_16x16x32_bf16 v[48:51], v[176:179], v[124:127], v[48:51]\n\ts_waitcnt lgkmcnt(8)\n\t"
            "v_mfma_f32_16x16x32_bf16 v[60:63], v[176:179], v[128:131], v[60:63]\n\tds_read_b64_tr_b16 v[188:189], v13 offset:12288\n\tds_read_b64_tr_b16 v[190:191], v13 offset:12800\n\ts_waitcnt lgkmcnt(9)\n\tv_mfma_f32_16x16x32_bf16 v[72:75], v[176:179], v[132:135], v[72:75]\n\tds_read_b128 v[148:151], v9\n\t"
            "s_waitcnt lgkmcnt(9)\n\tv_mfma_f32_16x16x32_bf16 v[84:87], v[176:179], v[136:139], v[84:87]\n\tds_read_b128 v[152:155], v5 offset:2048\n\ts_waitcnt lgkmcnt(9)\n\tv_mfma_f32_16x16x32_bf16 v[96:99], v[176:179], v[140:143], v[96:99]\n\tds_read_b128 v[156:159], v5 offset:4096\n\t"
            "s_waitcnt lgkmcnt(9)\n\tv_mfma_f32_16x16x32_bf16 v[108:111], v[176:179], v[144:147], v[108:111]\n\tds_read_b128 v[160:163], v5 offset:6144\n\ts_waitcnt lgkmcnt(8)\n\tv_mfma_f32_16x16x32_bf16 v[40:43], v[180:183], v[120:123], v[40:43]\n\tds_read_b128 v[164:167], v5 offset:8192\n\t"
            "v_mfma_f32_16x16x32_bf16 v[52:55], v[180:183], v[124:127], v[52:55]\n\tds_read_b128 v[168:171], v5 offset:10240\n\tv_mfma_f32_16x16x32_bf16 v[64:67], v[180:183], v[128:131], v[64:67]\n\tds_read_b128 v[172:175], v5 offset:12288\n\tv_mfma_f32_16x16x32_bf16 v[76:79], v[180:183], v[132:135], v[76:79]\n\tds_read_b64_tr_b16 v[192:193], v13 offset:13312\n\t"
            "ds_read_b64_tr_b16 v[194:195], v13 offset:13824\n\tv_mfma_f32_16x16x32_bf16 v[88:91], v[180:183], v[136:139], v[88:91]\n\tds_read_b64_tr_b16 v[196:197], v13 offset:14336\n\tds_read_b64_tr_b16 v[198:199], v13 offset:14848\n\tv_mfma_f32_16x16x32_bf16 v[100:103], v[180:183], v[140:143], v[100:103]\n\tv_mfma_f32_16x16x32_bf16 v[112:115], v[180:183], v[144:147], v[112:115]\n\t"
            "s_waitcnt lgkmcnt(13)\n\tv_mfma_f32_16x16x32_bf16 v[44:47], v[120:123], v[184:187], v[44:47]\n\tv_mfma_f32_16x16x32_bf16 v[56:59], v[124:127], v[184:187], v[56:59]\n\tv_mfma_f32_16x16x32_bf16 v[68:71], v[128:131], v[184:187], v[68:71]\n\tv_mfma_f32_16x16x32_bf16 v[80:83], v[132:135], v[184:187], v[80:83]\n\tv_mfma_f32_16x16x32_bf16 v[92:95], v[136:139], v[184:187], v[92:95]\n\t"
            "v_mfma_f32_16x16x32_bf16 v[104:107], v[140:143], v[184:187], v[104:107]\n\tv_mfma_f32_16x16x32_bf16 v[116:119], v[144:147], v[184:187], v[116:119]\n\ts_waitcnt lgkmcnt(10)\n\tv_mfma_f32_16x16x32_bf16 v[36:39], v[188:191], v[148:151], v[36:39]\n\ts_waitcnt lgkmcnt(9)\n\tv_mfma_f32_16x16x32_bf16 v[48:51], v[188:191], v[152:155], v[48:51]\n\t"
            "s_waitcnt lgkmcnt(8)\n\tv_mfma_f32_16x16x32_bf16 v[60:63], v[188:191], v[156:159], v[60:63]\n\ts_waitcnt lgkmcnt(7)\n\tv_mfma_f32_16x16x32_bf16 v[72:75], v[188:191], v[160:163], v[72:75]\n\ts_waitcnt lgkmcnt(6)\n\tv_mfma_f32_16x16x32_bf16 v[84:87], v[188:191], v[164:167], v[84:87]\n\t"
            "s_waitcnt lgkmcnt(5)\n\tv_mfma_f32_16x16x32_bf16 v[96:99], v[188:191], v[168:171], v[96:99]\n\ts_waitcnt lgkmcnt(4)\n\tv_mfma_f32_16x16x32_bf16 v[108:111], v[188:191], v[172:175], v[108:111]\n\ts_waitcnt lgkmcnt(2)\n\tv_mfma_f32_16x16x32_bf16 v[40:43], v[192:195], v[148:151], v[40:43]\n\t"
            "v_mfma_f32_16x16x32_bf16 v[52:55], v[192:195], v[152:155], v[52:55]\n\ts_waitcnt lgkmcnt(0)\n\ts_barrier\n\t"
            ST(7)
            "ds_read_b64_tr_b16 v[176:177], v13 offset:38912\n\tds_read_b64_tr_b16 v[178:179], v13 offset:39424\n\tv_mfma_f32_16x16x32_bf16 v[64:67], v[192:195], v[156:159], v[64:67]\n\tds_read_b128 v[120:123], v10\n\tv_mfma_f32_16x16x32_bf16 v[76:79], v[192:195], v[160:163], v[76:79]\n\tds_read_b128 v[124:127], v4 offset:40960\n\t"
            "v_mfma_f32_16x16x32_bf16 v[88:91], v[192:195], v[164:167], v[88:91]\n\tds_read_b128 v[128:131], v4 offset:43008\n\tv_mfma_f32_16x16x32_bf16 v[100:103], v[192:195], v[168:171], v[100:103]\n\tds_read_b128 v[132:135], v4 offset:45056\n\tv_mfma_f32_16x16x32_bf16 v[112:115], v[192:195], v[172:175], v[112:115]\n\tds_read_b128 v[136:139], v4 offset:47104\n\t"
            "v_mfma_f32_16x16x32_bf16 v[44:47], v[148:151], v[196:199], v[44:47]\n\tds_read_b128 v[140:143], v4 offset:49152\n\tv_mfma_f32_16x16x32_bf16 v[56:59], v[152:155], v[196:199], v[56:59]\n\tds_read_b128 v[144:147], v4 offset:51200\n\tv_mfma_f32_16x16x32_bf16 v[68:71], v[156:159], v[196:199], v[68:71]\n\tds_read_b64_tr_b16 v[180:181], v13 offset:39936\n\t"
            "ds_read_b64_tr_b16 v[182:183], v13 offset:40448\n\tv_mfma_f32_16x16x32_bf16 v[80:83], v[160:163], v[196:199], v[80:83]\n\tds_read_b64_tr_b16 v[184:185], v13 offset:40960\n\tds_read_b64_tr_b16 v[186:187], v13 offset:41472\n\tv_mfma_f32_16x16x32_bf16 v[92:95], v[164:167], v[196:199], v[92:95]\n\tv_mfma_f32_16x16x32_bf16 v[104:107], v[168:171], v[196:199], v[104:107]\n\t"
            "v_mfma_f32_16x16x32_bf16 v[116:119], v[172:175], v[196:199], v[116:119]\n\ts_waitcnt lgkmcnt(10)\n\tv_mfma_f32_16x16x32_bf16 v[36:39], v[176:179], v[120:123], v[36:39]\n\ts_waitcnt lgkmcnt(9)\n\tv_mfma_f32_16x16x32_bf16 v[48:51], v[176:179], v[124:127], v[48:51]\n\ts_waitcnt lgkmcnt(8)\n\t"
            "v_mfma_f32_16x16x32_bf16 v[60:63], v[176:179], v[128:131], v[60:63]\n\tds_read_b64_tr_b16 v[188:189], v13 offset:51200\n\tds_read_b64_tr_b16 v[190:191], v13 offset:51712\n\ts_waitcnt lgkmcnt(9)\n\tv_mfma_f32_16x16x32_bf16 v[72:75], v[176:179], v[132:135], v[72:75]\n\tds_read_b128 v[148:151], v11\n\t"
            "s_waitcnt lgkmcnt(9)\n\tv_mfma_f32_16x16x32_bf16 v[84:87], v[176:179], v[136:139], v[84:87]\n\tds_read_b128 v[152:155], v5 offset:40960\n\ts_waitcnt lgkmcnt(9)\n\tv_mfma_f32_16x16x32_bf16 v[96:99], v[176:179], v[140:143], v[96:99]\n\tds_read_b128 v[156:159], v5 offset:43008\n\t"
            "s_waitcnt lgkmcnt(9)\n\tv_mfma_f32_16x16x32_bf16 v[108:111], v[176:179], v[144:147], v[108:111]\n\tds_read_b128 v[160:163], v5 offset:45056\n\ts_waitcnt lgkmcnt(8)\n\tv_mfma_f32_16x16x32_bf16 v[40:43], v[180:183], v[120:123], v[40:43]\n\tds_read_b128 v[164:167], v5 offset:47104\n\t"
            "v_mfma_f32_16x16x32_bf16 v[52:55], v[180:183], v[124:127], v[52:55]\n\tds_read_b128 v[168:171], v5 offset:49152\n\tv_mfma_f32_16x16x32_bf16 v[64:67], v[180:183], v[128:131], v[64:67]\n\tds_read_b128 v[172:175], v5 offset:51200\n\tv_mfma_f32_16x16x32_bf16 v[76:79], v[180:183], v[132:135], v[76:79]\n\tds_read_b64_tr_b16 v[192:193], v13 offset:52224\n\t"
            "ds_read_b64_tr_b16 v[194:195], v13 offset:52736\n\tv_mfma_f32_16x16x32_bf16 v[88:91], v[180:183], v[136:139], v[88:91]\n\tds_read_b64_tr_b16 v[196:197], v13 offset:53248\n\tds_read_b64_tr_b16 v[198:199], v13 offset:53760\n\tv_mfma_f32_16x16x32_bf16 v[100:103], v[180:183], v[140:143], v[100:103]\n\tv_mfma_f32_16x16x32_bf16 v[112:115], v[180:183], v[144:147], v[112:115]\n\t"
            "s_waitcnt lgkmcnt(13)\n\tv_mfma_f32_16x16x32_bf16 v[44:47], v[120:123], v[184:187], v[44:47]\n\tv_mfma_f32_16x16x32_bf16 v[56:59], v[124:127], v[184:187], v[56:59]\n\tv_mfma_f32_16x16x32_bf16 v[68:71], v[128:131], v[184:187], v[68:71]\n\tv_mfma_f32_16x16x32_bf16 v[80:83], v[132:135], v[184:187], v[80:83]\n\tv_mfma_f32_16x16x32_bf16 v[92:95], v[136:139], v[184:187], v[92:95]\n\t"
            "v_mfma_f32_16x16x32_bf16 v[104:107], v[140:143], v[184:187], v[104:107]\n\tv_mfma_f32_16x16x32_bf16 v[116:119], v[144:147], v[184:187], v[116:119]\n\ts_waitcnt lgkmcnt(10)\n\tv_mfma_f32_16x16x32_bf16 v[36:39], v[188:191], v[148:151], v[36:39]\n\ts_waitcnt lgkmcnt(9)\n\tv_mfma_f32_16x16x32_bf16 v[48:51], v[188:191], v[152:155], v[48:51]\n\t"
            "s_waitcnt lgkmcnt(8)\n\tv_mfma_f32_16x16x32_bf16 v[60:63], v[188:191], v[156:159], v[60:63]\n\ts_waitcnt lgkmcnt(7)\n\tv_mfma_f32_16x16x32_bf16 v[72:75], v[188:191], v[160:163], v[72:75]\n\ts_waitcnt lgkmcnt(6)\n\tv_mfma_f32_16x16x32_bf16 v[84:87], v[188:191], v[164:167], v[84:87]\n\t"
            "s_waitcnt lgkmcnt(5)\n\tv_mfma_f32_16x16x32_bf16 v[96:99], v[188:191], v[168:171], v[96:99]\n\ts_waitcnt lgkmcnt(4)\n\tv_mfma_f32_16x16x32_bf16 v[108:111], v[188:191], v[172:175], v[108:111]\n\ts_waitcnt lgkmcnt(2)\n\tv_mfma_f32_16x16x32_bf16 v[40:43], v[192:195], v[148:151], v[40:43]\n\t"
            "v_mfma_f32_16x16x32_bf16 v[52:55], v[192:195], v[152:155], v[52:55]\n\ts_waitcnt lgkmcnt(0)\n\ts_barrier\n\t"
            ST(9)
            "ds_read_b64_tr_b16 v[176:177], v12 offset:0\n\tds_read_b64_tr_b16 v[178:179], v12 offset:512\n\tv_mfma_f32_16x16x32_bf16 v[64:67], v[192:195], v[156:159], v[64:67]\n\tds_read_b128 v[120:123], v6\n\tv_mfma_f32_16x16x32_bf16 v[76:79], v[192:195], v[160:163], v[76:79]\n\tds_read_b128 v[124:127], v2 offset:2048\n\t"
            "v_mfma_f32_16x16x32_bf16 v[88:91], v[192:195], v[164:167], v[88:91]\n\tds_read_b128 v[128:131], v2 offset:4096\n\tv_mfma_f32_16x16x32_bf16 v[100:103], v[192:195], v[168:171], v[100:103]\n\tds_read_b128 v[132:135], v2 offset:6144\n\tv_mfma_f32_16x16x32_bf16 v[112:115], v[192:195], v[172:175], v[112:115]\n\tds_read_b128 v[136:139], v2 offset:8192\n\t"
            "v_mfma_f32_16x16x32_bf16 v[44:47], v[148:151], v[196:199], v[44:47]\n\tds_read_b128 v[140:143], v2 offset:10240\n\tv_mfma_f32_16x16x32_bf16 v[56:59], v[152:155], v[196:199], v[56:59]\n\tds_read_b128 v[144:147], v2 offset:12288\n\tv_mfma_f32_16x16x32_bf16 v[68:71], v[156:159], v[196:199], v[68:71]\n\tds_read_b64_tr_b16 v[180:181], v12 offset:1024\n\t"
            "ds_read_b64_tr_b16 v[182:183], v12 offset:1536\n\tv_mfma_f32_16x16x32_bf16 v[80:83], v[160:163], v[196:199], v[80:83]\n\tds_read_b64_tr_b16 v[184:185], v12 offset:2048\n\tds_read_b64_tr_b16 v[186:187], v12 offset:2560\n\tv_mfma_f32_16x16x32_bf16 v[92:95], v[164:167], v[196:199], v[92:95]\n\tv_mfma_f32_16x16x32_bf16 v[104:107], v[168:171], v[196:199], v[104:107]\n\t"
            "v_mfma_f32_16x16x32_bf16 v[116:119], v[172:175], v[196:199], v[116:119]\n\ts_waitcnt lgkmcnt(10)\n\tv_mfma_f32_16x16x32_bf16 v[36:39], v[176:179], v[120:123], v[36:39]\n\ts_waitcnt lgkmcnt(9)\n\tv_mfma_f32_16x16x32_bf16 v[48:51], v[176:179], v[124:127], v[48:51]\n\ts_waitcnt lgkmcnt(8)\n\t"
            "v_mfma_f32_16x16x32_bf16 v[60:63], v[176:179], v[128:131], v[60:63]\n\tds_read_b64_tr_b16 v[188:189], v12 offset:12288\n\tds_read_b64_tr_b16 v[190:191], v12 offset:12800\n\ts_waitcnt lgkmcnt(9)\n\tv_mfma_f32_16x16x32_bf16 v[72:75], v[176:179], v[132:135], v[72:75]\n\tds_read_b128 v[148:151], v7\n\t"
            "s_waitcnt lgkmcnt(9)\n\tv_mfma_f32_16x16x32_bf16 v[84:87], v[176:179], v[136:139], v[84:87]\n\tds_read_b128 v[152:155], v3 offset:2048\n\ts_waitcnt lgkmcnt(9)\n\tv_mfma_f32_16x16x32_bf16 v[96:99], v[176:179], v[140:143], v[96:99]\n\tds_read_b128 v[156:159], v3 offset:4096\n\t"
            "s_waitcnt lgkmcnt(9)\n\tv_mfma_f32_16x16x32_bf16 v[108:111], v[176:179], v[144:147], v[108:111]\n\tds_read_b128 v[160:163], v3 offset:6144\n\ts_waitcnt lgkmcnt(8)\n\tv_mfma_f32_16x16x32_bf16 v[40:43], v[180:183], v[120:123], v[40:43]\n\tds_read_b128 v[164:167], v3 offset:8192\n\t"
            "v_mfma_f32_16x16x32_bf16 v[52:55], v[180:183], v[124:127], v[52:55]\n\tds_read_b128 v[168:171], v3 offset:10240\n\tv_mfma_f32_16x16x32_bf16 v[64:67], v[180:183], v[128:131], v[64:67]\n\tds_read_b128 v[172:175], v3 offset:12288\n\tv_mfma_f32_16x16x32_bf16 v[76:79], v[180:183], v[132:135], v[76:79]\n\tds_read_b64_tr_b16 v[192:193], v12 offset:13312\n\t"
            "ds_read_b64_tr_b16 v[194:195], v12 offset:13824\n\tv_mfma_f32_16x16x32_bf16 v[88:91], v[180:183], v[136:139], v[88:91]\n\tds_read_b64_tr_b16 v[196:197], v12 offset:14336\n\tds_read_b64_tr_b16 v[198:199], v12 offset:14848\n\tv_mfma_f32_16x16x32_bf16 v[100:103], v[180:183], v[140:143], v[100:103]\n\tv_mfma_f32_16x16x32_bf16 v[112:115], v[180:183], v[144:147], v[112:115]\n\t"
            "s_waitcnt lgkmcnt(13)\n\tv_mfma_f32_16x16x32_bf16 v[44:47], v[120:123], v[184:187], v[44:47]\n\tv_mfma_f32_16x16x32_bf16 v[56:59], v[124:127], v[184:187], v[56:59]\n\tv_mfma_f32_16x16x32_bf16 v[68:71], v[128:131], v[184:187], v[68:71]\n\tv_mfma_f32_16x16x32_bf16 v[80:83], v[132:135], v[184:187], v[80:83]\n\tv_mfma_f32_16x16x32_bf16 v[92:95], v[136:139], v[184:187], v[92:95]\n\t"
            "v_mfma_f32_16x16x32_bf16 v[104:107], v[140:143], v[184:187], v[104:107]\n\tv_mfma_f32_16x16x32_bf16 v[116:119], v[144:147], v[184:187], v[116:119]\n\ts_waitcnt lgkmcnt(10)\n\tv_mfma_f32_16x16x32_bf16 v[36:39], v[188:191], v[148:151], v[36:39]\n\ts_waitcnt lgkmcnt(9)\n\tv_mfma_f32_16x16x32_bf16 v[48:51], v[188:191], v[152:155], v[48:51]\n\t"
            "s_waitcnt lgkmcnt(8)\n\tv_mfma_f32_16x16x32_bf16 v[60:63], v[188:191], v[156:159], v[60:63]\n\ts_waitcnt lgkmcnt(7)\n\tv_mfma_f32_16x16x32_bf16 v[72:75], v[188:191], v[160:163], v[72:75]\n\ts_waitcnt lgkmcnt(6)\n\tv_mfma_f32_16x16x32_bf16 v[84:87], v[188:191], v[164:167], v[84:87]\n\t"
            "s_waitcnt lgkmcnt(5)\n\tv_mfma_f32_16x16x32_bf16 v[96:99], v[188:191], v[168:171], v[96:99]\n\ts_waitcnt lgkmcnt(4)\n\tv_mfma_f32_16x16x32_bf16 v[108:111], v[188:191], v[172:175], v[108:111]\n\ts_waitcnt lgkmcnt(2)\n\tv_mfma_f32_16x16x32_bf16 v[40:43], v[192:195], v[148:151], v[40:43]\n\t"
            "v_mfma_f32_16x16x32_bf16 v[52:55], v[192:195], v[152:155], v[52:55]\n\ts_waitcnt lgkmcnt(0)\n\ts_barrier\n\t"
            ST(11)
            "ds_read_b64_tr_b16 v[176:177], v13 offset:0\n\tds_read_b64_tr_b16 v[178:179], v13 offset:512\n\tv_mfma_f32_16x16x32_bf16 v[64:67], v[192:195], v[156:159], v[64:67]\n\tds_read_b128 v[120:123], v8\n\tv_mfma_f32_16x16x32_bf16 v[76:79], v[192:195], v[160:163], v[76:79]\n\tds_read_b128 v[124:127], v4 offset:2048\n\t"
            "v_mfma_f32_16x16x32_bf16 v[88:91], v[192:195], v[164:167], v[88:91]\n\tds_read_b128 v[128:131], v4 offset:4096\n\tv_mfma_f32_16x16x32_bf16 v[100:103], v[192:195], v[168:171], v[100:103]\n\tds_read_b128 v[132:135], v4 offset:6144\n\tv_mfma_f32_16x16x32_bf16 v[112:115], v[192:195], v[172:175], v[112:115]\n\tds_read_b128 v[136:139], v4 offset:8192\n\t"
            "v_mfma_f32_16x16x32_bf16 v[44:47], v[148:151], v[196:199], v[44:47]\n\tds_read_b128 v[140:143], v4 offset:10240\n\tv_mfma_f32_16x16x32_bf16 v[56:59], v[152:155], v[196:199], v[56:59]\n\tds_read_b128 v[144:147], v4 offset:12288\n\tv_mfma_f32_16x16x32_bf16 v[68:71], v[156:159], v[196:199], v[68:71]\n\tds_read_b64_tr_b16 v[180:181], v13 offset:1024\n\t"
            "ds_read_b64_tr_b16 v[182:183], v13 offset:1536\n\tv_mfma_f32_16x16x32_bf16 v[80:83], v[160:163], v[196:199], v[80:83]\n\tds_read_b64_tr_b16 v[184:185], v13 offset:2048\n\tds_read_b64_tr_b16 v[186:187], v13 offset:2560\n\tv_mfma_f32_16x16x32_bf16 v[92:95], v[164:167], v[196:199], v[92:95]\n\tv_mfma_f32_16x16x32_bf16 v[104:107], v[168:171], v[196:199], v[104:107]\n\t"
            "v_mfma_f32_16x16x32_bf16 v[116:119], v[172:175], v[196:199], v[116:119]\n\ts_waitcnt lgkmcnt(10)\n\tv_mfma_f32_16x16x32_bf16 v[36:39], v[176:179], v[120:123], v[36:39]\n\ts_waitcnt lgkmcnt(9)\n\tv_mfma_f32_16x16x32_bf16 v[48:51], v[176:179], v[124:127], v[48:51]\n\ts_waitcnt lgkmcnt(8)\n\t"
            "v_mfma_f32_16x16x32_bf16 v[60:63], v[176:179], v[128:131], v[60:63]\n\tds_read_b64_tr_b16 v[188:189], v13 offset:12288\n\tds_read_b64_tr_b16 v[190:191], v13 offset:12800\n\ts_waitcnt lgkmcnt(9)\n\tv_mfma_f32_16x16x32_bf16 v[72:75], v[176:179], v[132:135], v[72:75]\n\tds_read_b128 v[148:151], v9\n\t"
            "s_waitcnt lgkmcnt(9)\n\tv_mfma_f32_16x16x32_bf16 v[84:87], v[176:179], v[136:139], v[84:87]\n\tds_read_b128 v[152:155], v5 offset:2048\n\ts_waitcnt lgkmcnt(9)\n\tv_mfma_f32_16x16x32_bf16 v[96:99], v[176:179], v[140:143], v[96:99]\n\tds_read_b128 v[156:159], v5 offset:4096\n\t"
            "s_waitcnt lgkmcnt(9)\n\tv_mfma_f32_16x16x32_bf16 v[108:111], v[176:179], v[144:147], v[108:111]\n\tds_read_b128 v[160:163], v5 offset:6144\n\ts_waitcnt lgkmcnt(8)\n\tv_mfma_f32_16x16x32_bf16 v[40:43], v[180:183], v[120:123], v[40:43]\n\tds_read_b128 v[164:167], v5 offset:8192\n\t"
            "v_mfma_f32_16x16x32_bf16 v[52:55], v[180:183], v[124:127], v[52:55]\n\tds_read_b128 v[168:171], v5 offset:10240\n\tv_mfma_f32_16x16x32_bf16 v[64:67], v[180:183], v[128:131], v[64:67]\n\tds_read_b128 v[172:175], v5 offset:12288\n\tv_mfma_f32_16x16x32_bf16 v[76:79], v[180:183], v[132:135], v[76:79]\n\tds_read_b64_tr_b16 v[192:193], v13 offset:13312\n\t"
            "ds_read_b64_tr_b16 v[194:195], v13 offset:13824\n\tv_mfma_f32_16x16x32_bf16 v[88:91], v[180:183], v[136:139], v[88:91]\n\tds_read_b64_tr_b16 v[196:197], v13 offset:14336\n\tds_read_b64_tr_b16 v[198:199], v13 offset:14848\n\tv_mfma_f32_16x16x32_bf16 v[100:103], v[180:183], v[140:143], v[100:103]\n\tv_mfma_f32_16x16x32_bf16 v[112:115], v[180:183], v[144:147], v[112:115]\n\t"
            "s_waitcnt lgkmcnt(13)\n\tv_mfma_f32_16x16x32_bf16 v[44:47], v[120:123], v[184:187], v[44:47]\n\tv_mfma_f32_16x16x32_bf16 v[56:59], v[124:127], v[184:187], v[56:59]\n\tv_mfma_f32_16x16x32_bf16 v[68:71], v[128:131], v[184:187], v[68:71]\n\tv_mfma_f32_16x16x32_bf16 v[80:83], v[132:135], v[184:187], v[80:83]\n\tv_mfma_f32_16x16x32_bf16 v[92:95], v[136:139], v[184:187], v[92:95]\n\t"
            "v_mfma_f32_16x16x32_bf16 v[104:107], v[140:143], v[184:187], v[104:107]\n\tv_mfma_f32_16x16x32_bf16 v[116:119], v[144:147], v[184:187], v[116:119]\n\ts_waitcnt lgkmcnt(10)\n\tv_mfma_f32_16x16x32_bf16 v[36:39], v[188:191], v[148:151], v[36:39]\n\ts_waitcnt lgkmcnt(9)\n\tv_mfma_f32_16x16x32_bf16 v[48:51], v[188:191], v[152:155], v[48:51]\n\t"
            "s_waitcnt lgkmcnt(8)\n\tv_mfma_f32_16x16x32_bf16 v[60:63], v[188:191], v[156:159], v[60:63]\n\ts_waitcnt lgkmcnt(7)\n\tv_mfma_f32_16x16x32_bf16 v[72:75], v[188:191], v[160:163], v[72:75]\n\ts_waitcnt lgkmcnt(6)\n\tv_mfma_f32_16x16x32_bf16 v[84:87], v[188:191], v[164:167], v[84:87]\n\t"
            "s_waitcnt lgkmcnt(5)\n\tv_mfma_f32_16x16x32_bf16 v[96:99], v[188:191], v[168:171], v[96:99]\n\ts_waitcnt lgkmcnt(4)\n\tv_mfma_f32_16x16x32_bf16 v[108:111], v[188:191], v[172:175], v[108:111]\n\ts_waitcnt lgkmcnt(2)\n\tv_mfma_f32_16x16x32_bf16 v[40:43], v[192:195], v[148:151], v[40:43]\n\t"
            "v_mfma_f32_16x16x32_bf16 v[52:55], v[192:195], v[152:155], v[52:55]\n\ts_waitcnt lgkmcnt(0)\n\ts_barrier\n\t"
            ST(13)
            "ds_read_b64_tr_b16 v[176:177], v13 offset:38912\n\tds_read_b64_tr_b16 v[178:179], v13 offset:39424\n\tv_mfma_f32_16x16x32_bf16 v[64:67], v[192:195], v[156:159], v[64:67]\n\tds_read_b128 v[120:123], v10\n\tv_mfma_f32_16x16x32_bf16 v[76:79], v[192:195], v[160:163], v[76:79]\n\tds_read_b128 v[124:127], v4 offset:40960\n\t"
            "v_mfma_f32_16x16x32_bf16 v[88:91], v[192:195], v[164:167], v[88:91]\n\tds_read_b128 v[128:131], v4 offset:43008\n\tv_mfma_f32_16x16x32_bf16 v[100:103], v[192:195], v[168:171], v[100:103]\n\tds_read_b128 v[132:135], v4 offset:45056\n\tv_mfma_f32_16x16x32_bf16 v[112:115], v[192:195], v[172:175], v[112:115]\n\tds_read_b128 v[136:139], v4 offset:47104\n\t"
            "v_mfma_f32_16x16x32_bf16 v[44:47], v[148:151], v[196:199], v[44:47]\n\tds_read_b128 v[140:143], v4 offset:49152\n\tv_mfma_f32_16x16x32_bf16 v[56:59], v[152:155], v[196:199], v[56:59]\n\tds_read_b128 v[144:147], v4 offset:51200\n\tv_mfma_f32_16x16x32_bf16 v[68:71], v[156:159], v[196:199], v[68:71]\n\tds_read_b64_tr_b16 v[180:181], v13 offset:39936\n\t"
            "ds_read_b64_tr_b16 v[182:183], v13 offset:40448\n\tv_mfma_f32_16x16x32_bf16 v[80:83], v[160:163], v[196:199], v[80:83]\n\tds_read_b64_tr_b16 v[184:185], v13 offset:40960\n\tds_read_b64_tr_b16 v[186:187], v13 offset:41472\n\tv_mfma_f32_16x16x32_bf16 v[92:95], v[164:167], v[196:199], v[92:95]\n\tv_mfma_f32_16x16x32_bf16 v[104:107], v[168:171], v[196:199], v[104:107]\n\t"
            "v_mfma_f32_16x16x32_bf16 v[116:119], v[172:175], v[196:199], v[116:119]\n\ts_waitcnt lgkmcnt(10)\n\tv_mfma_f32_16x16x32_bf16 v[36:39], v[176:179], v[120:123], v[36:39]\n\ts_waitcnt lgkmcnt(9)\n\tv_mfma_f32_16x16x32_bf16 v[48:51], v[176:179], v[124:127], v[48:51]\n\ts_waitcnt lgkmcnt(8)\n\t"
            "v_mfma_f32_16x16x32_bf16 v[60:63], v[176:179], v[128:131], v[60:63]\n\tds_read_b64_tr_b16 v[188:189], v13 offset:51200\n\tds_read_b64_tr_b16 v[190:191], v13 offset:51712\n\ts_waitcnt lgkmcnt(9)\n\tv_mfma_f32_16x16x32_bf16 v[72:75], v[176:179], v[132:135], v[72:75]\n\tds_read_b128 v[148:151], v11\n\t"
            "s_waitcnt lgkmcnt(9)\n\tv_mfma_f32_16x16x32_bf16 v[84:87], v[176:179], v[136:139], v[84:87]\n\tds_read_b128 v[152:155], v5 offset:40960\n\ts_waitcnt lgkmcnt(9)\n\tv_mfma_f32_16x16x32_bf16 v[96:99], v[176:179], v[140:143], v[96:99]\n\tds_read_b128 v[156:159], v5 offset:43008\n\t"
            "s_waitcnt lgkmcnt(9)\n\tv_mfma_f32_16x16x32_bf16 v[108:111], v[176:179], v[144:147], v[108:111]\n\tds_read_b128 v[160:163], v5 offset:45056\n\ts_waitcnt lgkmcnt(8)\n\tv_mfma_f32_16x16x32_bf16 v[40:43], v[180:183], v[120:123], v[40:43]\n\tds_read_b128 v[164:167], v5 offset:47104\n\t"
            "v_mfma_f32_16x16x32_bf16 v[52:55], v[180:183], v[124:127], v[52:55]\n\tds_read_b128 v[168:171], v5 offset:49152\n\tv_mfma_f32_16x16x32_bf16 v[64:67], v[180:183], v[128:131], v[64:67]\n\tds_read_b128 v[172:175], v5 offset:51200\n\tv_mfma_f32_16x16x32_bf16 v[76:79], v[180:183], v[132:135], v[76:79]\n\tds_read_b64_tr_b16 v[192:193], v13 offset:52224\n\t"
            "ds_read_b64_tr_b16 v[194:195], v13 offset:52736\n\tv_mfma_f32_16x16x32_bf16 v[88:91], v[180:183], v[136:139], v[88:91]\n\tds_read_b64_tr_b16 v[196:197], v13 offset:53248\n\tds_read_b64_tr_b16 v[198:199], v13 offset:53760\n\tv_mfma_f32_16x16x32_bf16 v[100:103], v[180:183], v[140:143], v[100:103]\n\tv_mfma_f32_16x16x32_bf16 v[112:115], v[180:183], v[144:147], v[112:115]\n\t"
            "s_waitcnt lgkmcnt(13)\n\tv_mfma_f32_16x16x32_bf16 v[44:47], v[120:123], v[184:187], v[44:47]\n\tv_mfma_f32_16x16x32_bf16 v[56:59], v[124:127], v[184:187], v[56:59]\n\tv_mfma_f32_16x16x32_bf16 v[68:71], v[128:131], v[184:187], v[68:71]\n\tv_mfma_f32_16x16x32_bf16 v[80:83], v[132:135], v[184:187], v[80:83]\n\tv_mfma_f32_16x16x32_bf16 v[92:95], v[136:139], v[184:187], v[92:95]\n\t"
            "v_mfma_f32_16x16x32_bf16 v[104:107], v[140:143], v[184:187], v[104:107]\n\tv_mfma_f32_16x16x32_bf16 v[116:119], v[144:147], v[184:187], v[116:119]\n\ts_waitcnt lgkmcnt(10)\n\tv_mfma_f32_16x16x32_bf16 v[36:39], v[188:191], v[148:151], v[36:39]\n\ts_waitcnt lgkmcnt(9)\n\tv_mfma_f32_16x16x32_bf16 v[48:51], v[188:191], v[152:155], v[48:51]\n\t"
            "s_waitcnt lgkmcnt(8)\n\tv_mfma_f32_16x16x32_bf16 v[60:63], v[188:191], v[156:159], v[60:63]\n\ts_waitcnt lgkmcnt(7)\n\tv_mfma_f32_16x16x32_bf16 v[72:75], v[188:191], v[160:163], v[72:75]\n\ts_waitcnt lgkmcnt(6)\n\tv_mfma_f32_16x16x32_bf16 v[84:87], v[188:191], v[164:167], v[84:87]\n\t"
            "s_waitcnt lgkmcnt(5)\n\tv_mfma_f32_16x16x32_bf16 v[96:99], v[188:191], v[168:171], v[96:99]\n\ts_waitcnt lgkmcnt(4)\n\tv_mfma_f32_16x16x32_bf16 v[108:111], v[188:191], v[172:175], v[108:111]\n\ts_waitcnt lgkmcnt(2)\n\tv_mfma_f32_16x16x32_bf16 v[40:43], v[192:195], v[148:151], v[40:43]\n\t"
            "v_mfma_f32_16x16x32_bf16 v[52:55], v[192:195], v[152:155], v[52:55]\n\ts_waitcnt lgkmcnt(0)\n\ts_barrier\n\t"
            ST(15)
            "ds_read_b64_tr_b16 v[176:177], v12 offset:0\n\tds_read_b64_tr_b16 v[178:179], v12 offset:512\n\tv_mfma_f32_16x16x32_bf16 v[64:67], v[192:195], v[156:159], v[64:67]\n\tds_read_b128 v[120:123], v6\n\tv_mfma_f32_16x16x32_bf16 v[76:79], v[192:195], v[160:163], v[76:79]\n\tds_read_b128 v[124:127], v2 offset:2048\n\t"
            "v_mfma_f32_16x16x32_bf16 v[88:91], v[192:195], v[164:167], v[88:91]\n\tds_read_b128 v[128:131], v2 offset:4096\n\tv_mfma_f32_16x16x32_bf16 v[100:103], v[192:195], v[168:171], v[100:103]\n\tds_read_b128 v[132:135], v2 offset:6144\n\tv_mfma_f32_16x16x32_bf16 v[112:115], v[192:195], v[172:175], v[112:115]\n\tds_read_b128 v[136:139], v2 offset:8192\n\t"
            "v_mfma_f32_16x16x32_bf16 v[44:47], v[148:151], v[196:199], v[44:47]\n\tds_read_b128 v[140:143], v2 offset:10240\n\tv_mfma_f32_16x16x32_bf16 v[56:59], v[152:155], v[196:199], v[56:59]\n\tds_read_b128 v[144:147], v2 offset:12288\n\tv_mfma_f32_16x16x32_bf16 v[68:71], v[156:159], v[196:199], v[68:71]\n\tds_read_b64_tr_b16 v[180:181], v12 offset:1024\n\t"
            "ds_read_b64_tr_b16 v[182:183], v12 offset:1536\n\tv_mfma_f32_16x16x32_bf16 v[80:83], v[160:163], v[196:199], v[80:83]\n\tv_mfma_f32_16x16x32_bf16 v[92:95], v[164:167], v[196:199], v[92:95]\n\tv_mfma_f32_16x16x32_bf16 v[104:107], v[168:171], v[196:199], v[104:107]\n\tv_mfma_f32_16x16x32_bf16 v[116:119], v[172:175], v[196:199], v[116:119]\n\ts_waitcnt lgkmcnt(8)\n\t"
            "v_mfma_f32_16x16x32_bf16 v[36:39], v[176:179], v[120:123], v[36:39]\n\ts_waitcnt lgkmcnt(7)\n\tv_mfma_f32_16x16x32_bf16 v[48:51], v[176:179], v[124:127], v[48:51]\n\ts_waitcnt lgkmcnt(6)\n\tv_mfma_f32_16x16x32_bf16 v[60:63], v[176:179], v[128:131], v[60:63]\n\tds_read_b64_tr_b16 v[188:189], v12 offset:12288\n\t"
            "ds_read_b64_tr_b16 v[190:191], v12 offset:12800\n\ts_waitcnt lgkmcnt(7)\n\tv_mfma_f32_16x16x32_bf16 v[72:75], v[176:179], v[132:135], v[72:75]\n\tds_read_b128 v[148:151], v7\n\ts_waitcnt lgkmcnt(7)\n\tv_mfma_f32_16x16x32_bf16 v[84:87], v[176:179], v[136:139], v[84:87]\n\t"
            "ds_read_b128 v[152:155], v3 offset:2048\n\ts_waitcnt lgkmcnt(7)\n\tv_mfma_f32_16x16x32_bf16 v[96:99], v[176:179], v[140:143], v[96:99]\n\tds_read_b128 v[156:159], v3 offset:4096\n\ts_waitcnt lgkmcnt(7)\n\tv_mfma_f32_16x16x32_bf16 v[108:111], v[176:179], v[144:147], v[108:111]\n\t"
            "ds_read_b128 v[160:163], v3 offset:6144\n\ts_waitcnt lgkmcnt(6)\n\tv_mfma_f32_16x16x32_bf16 v[40:43], v[180:183], v[120:123], v[40:43]\n\tds_read_b128 v[164:167], v3 offset:8192\n\tv_mfma_f32_16x16x32_bf16 v[52:55], v[180:183], v[124:127], v[52:55]\n\tds_read_b128 v[168:171], v3 offset:10240\n\t"
            "v_mfma_f32_16x16x32_bf16 v[64:67], v[180:183], v[128:131], v[64:67]\n\tds_read_b128 v[172:175], v3 offset:12288\n\tv_mfma_f32_16x16x32_bf16 v[76:79], v[180:183], v[132:135], v[76:79]\n\tds_read_b64_tr_b16 v[192:193], v12 offset:13312\n\tds_read_b64_tr_b16 v[194:195], v12 offset:13824\n\tv_mfma_f32_16x16x32_bf16 v[88:91], v[180:183], v[136:139], v[88:91]\n\t"
            "v_mfma_f32_16x16x32_bf16 v[100:103], v[180:183], v[140:143], v[100:103]\n\tv_mfma_f32_16x16x32_bf16 v[112:115], v[180:183], v[144:147], v[112:115]\n\ts_waitcnt lgkmcnt(8)\n\tv_mfma_f32_16x16x32_bf16 v[36:39], v[188:191], v[148:151], v[36:39]\n\ts_waitcnt lgkmcnt(7)\n\tv_mfma_f32_16x16x32_bf16 v[48:51], v[188:191], v[152:155], v[48:51]\n\t"
            "s_waitcnt lgkmcnt(6)\n\tv_mfma_f32_16x16x32_bf16 v[60:63], v[188:191], v[156:159], v[60:63]\n\ts_waitcnt lgkmcnt(5)\n\tv_mfma_f32_16x16x32_bf16 v[72:75], v[188:191], v[160:163], v[72:75]\n\ts_waitcnt lgkmcnt(0)\n\ts_barrier\n\t"
            ST(17)
            "ds_read_b64_tr_b16 v[176:177], v13 offset:0\n\tds_read_b64_tr_b16 v[178:179], v13 offset:512\n\tv_mfma_f32_16x16x32_bf16 v[84:87], v[188:191], v[164:167], v[84:87]\n\tds_read_b128 v[120:123], v8\n\tv_mfma_f32_16x16x32_bf16 v[96:99], v[188:191], v[168:171], v[96:99]\n\tds_read_b128 v[124:127], v4 offset:2048\n\t"
            "v_mfma_f32_16x16x32_bf16 v[108:111], v[188:191], v[172:175], v[108:111]\n\tds_read_b128 v[128:131], v4 offset:4096\n\tv_mfma_f32_16x16x32_bf16 v[40:43], v[192:195], v[148:151], v[40:43]\n\tds_read_b128 v[132:135], v4 offset:6144\n\tv_mfma_f32_16x16x32_bf16 v[52:55], v[192:195], v[152:155], v[52:55]\n\tds_read_b128 v[136:139], v4 offset:8192\n\t"
            "v_mfma_f32_16x16x32_bf16 v[64:67], v[192:195], v[156:159], v[64:67]\n\tds_read_b128 v[140:143], v4 offset:10240\n\tv_mfma_f32_16x16x32_bf16 v[76:79], v[192:195], v[160:163], v[76:79]\n\tds_read_b128 v[144:147], v4 offset:12288\n\tv_mfma_f32_16x16x32_bf16 v[88:91], v[192:195], v[164:167], v[88:91]\n\tds_read_b64_tr_b16 v[180:181], v13 offset:1024\n\t"
            "ds_read_b64_tr_b16 v[182:183], v13 offset:1536\n\tv_mfma_f32_16x16x32_bf16 v[100:103], v[192:195], v[168:171], v[100:103]\n\tds_read_b64_tr_b16 v[184:185], v13 offset:2048\n\tds_read_b64_tr_b16 v[186:187], v13 offset:2560\n\tv_mfma_f32_16x16x32_bf16 v[112:115], v[192:195], v[172:175], v[112:115]\n\tds_read2_b32 v[208:209], v14 offset1:16\n\t"
            SA(2) PRIA
            "s_mov_b64 vcc, s[18:19]\n\ts_bitcmp1_b32 9, s69\n\tds_read_b64_tr_b16 v[188:189], v13 offset:12288\n\tv_mov_b32 v252, -1\n\tds_read_b64_tr_b16 v[190:191], v13 offset:12800\n\tv_mov_b32 v254, -1\n\t"
            "ds_read_b128 v[148:151], v9\n\ts_waitcnt lgkmcnt(6)\n\tv_mfma_f32_16x16x32_bf16 v[40:43], v[180:183], v[120:123], v[40:43]\n\tds_read_b128 v[152:155], v5 offset:2048\n\tv_mfma_f32_16x16x32_bf16 v[36:39], v[176:179], v[120:123], v[36:39]\n\tds_read_b128 v[156:159], v5 offset:4096\n\t"
            "v_mfma_f32_16x16x32_bf16 v[52:55], v[180:183], v[124:127], v[52:55]\n\tds_read_b128 v[160:163], v5 offset:6144\n\tv_mfma_f32_16x16x32_bf16 v[48:51], v[176:179], v[124:127], v[48:51]\n\tds_read_b128 v[164:167], v5 offset:8192\n\tv_mfma_f32_16x16x32_bf16 v[64:67], v[180:183], v[128:131], v[64:67]\n\tds_read_b128 v[168:171], v5 offset:10240\n\t"
            "v_mfma_f32_16x16x32_bf16 v[60:63], v[176:179], v[128:131], v[60:63]\n\tds_read_b128 v[172:175], v5 offset:12288\n\tv_mfma_f32_16x16x32_bf16 v[76:79], v[180:183], v[132:135], v[76:79]\n\tds_read_b64_tr_b16 v[192:193], v13 offset:13312\n\ts_waitcnt lgkmcnt(7)\n\tv_mfma_f32_16x16x32_bf16 v[36:39], v[188:191], v[148:151], v[36:39]\n\t"
            "ds_read_b64_tr_b16 v[194:195], v13 offset:13824\n\tds_read_b64_tr_b16 v[196:197], v13 offset:14336\n\ts_waitcnt lgkmcnt(8)\n\tv_mfma_f32_16x16x32_bf16 v[48:51], v[188:191], v[152:155], v[48:51]\n\tds_read_b64_tr_b16 v[198:199], v13 offset:14848\n\tds_read2_b32 v[210:211], v14 offset0:32 offset1:48\n\t"
            "s_waitcnt lgkmcnt(9)\n\tv_mfma_f32_16x16x32_bf16 v[60:63], v[188:191], v[156:159], v[60:63]\n\tv_mov_b32_dpp v200, v36 row_newbcast:0 row_mask:0xf bank_mask:0xf bound_ctrl:1\n\tds_read2_b32 v[212:213], v14 offset0:64 offset1:80\n\tv_mfma_f32_16x16x32_bf16 v[72:75], v[176:179], v[132:135], v[72:75]\n\tv_mov_b32_dpp v201, v37 row_newbcast:0 row_mask:0xf bank_mask:0xf bound_ctrl:1\n\t"
            "ds_read_b32 v214, v14 offset:384\n\ts_waitcnt lgkmcnt(8)\n\tv_mfma_f32_16x16x32_bf16 v[72:75], v[188:191], v[160:163], v[72:75]\n\tv_mov_b32_dpp v202, v38 row_newbcast:0 row_mask:0xf bank_mask:0xf bound_ctrl:1\n\tv_mov_b32_dpp v203, v39 row_newbcast:0 row_mask:0xf bank_mask:0xf bound_ctrl:1\n\tv_mfma_f32_16x16x32_bf16 v[88:91], v[180:183], v[136:139], v[88:91]\n\t"
            "v_fmac_f32 v36, v208, v200\n\tv_fmac_f32 v37, v208, v201\n\tv_mfma_f32_16x16x32_bf16 v[84:87], v[176:179], v[136:139], v[84:87]\n\tv_fmac_f32 v38, v208, v202\n\tv_fmac_f32 v39, v208, v203\n\ts_waitcnt lgkmcnt(4)\n\t"
            "v_mfma_f32_16x16x32_bf16 v[40:43], v[192:195], v[148:151], v[40:43]\n\tv_cvt_pk_bf16_f32 v250, v36, v37\n\tv_cvt_pk_bf16_f32 v251, v38, v39\n\tv_mfma_f32_16x16x32_bf16 v[52:55], v[192:195], v[152:155], v[52:55]\n\tv_and_b32 v36, v250, v18\n\tv_and_b32 v37, v251, v18\n\t"
            "v_mfma_f32_16x16x32_bf16 v[64:67], v[192:195], v[156:159], v[64:67]\n\ts_nop 1\n\tv_mov_b32_dpp v204, v40 row_newbcast:0 row_mask:0xf bank_mask:0xf bound_ctrl:1\n\tv_mov_b32_dpp v205, v41 row_newbcast:0 row_mask:0xf bank_mask:0xf bound_ctrl:1\n\tv_mfma_f32_16x16x32_bf16 v[76:79], v[192:195], v[160:163], v[76:79]\n\tv_mov_b32_dpp v206, v42 row_newbcast:0 row_mask:0xf bank_mask:0xf bound_ctrl:1\n\t"
            "v_mov_b32_dpp v207, v43 row_newbcast:0 row_mask:0xf bank_mask:0xf bound_ctrl:1\n\tv_mfma_f32_16x16x32_bf16 v[88:91], v[192:195], v[164:167], v[88:91]\n\tv_fmac_f32 v40, v208, v204\n\tv_fmac_f32 v41, v208, v205\n\tv_mfma_f32_16x16x32_bf16 v[84:87], v[188:191], v[164:167], v[84:87]\n\tv_fmac_f32 v42, v208, v206\n\t"
            "v_fmac_f32 v43, v208, v207\n\tv_mfma_f32_16x16x32_bf16 v[100:103], v[180:183], v[140:143], v[100:103]\n\tv_cvt_pk_bf16_f32 v40, v40, v41\n\tv_cvt_pk_bf16_f32 v41, v42, v43\n\tv_mfma_f32_16x16x32_bf16 v[96:99], v[176:179], v[140:143], v[96:99]\n\tv_cndmask_b32 v42, v40, v40, vcc\n\t"
            "v_cndmask_b32 v43, v41, v41, vcc\n\tv_mfma_f32_16x16x32_bf16 v[100:103], v[192:195], v[168:171], v[100:103]\n\tv_and_b32 v38, v250, v19\n\tv_and_b32 v39, v251, v19\n\tv_mfma_f32_16x16x32_bf16 v[96:99], v[188:191], v[168:171], v[96:99]\n\tv_and_b32 v222, v250, v20\n\t"
            "v_and_b32 v223, v251, v20\n\tv_mfma_f32_16x16x32_bf16 v[218:221], v[40:43], v[36:39], 0\n\tv_and_b32 v224, v250, v21\n\tv_and_b32 v225, v251, v21\n\tv_mfma_f32_16x16x32_bf16 v[112:115], v[180:183], v[144:147], v[112:115]\n\tv_fmac_f32 v52, v209, v204\n\t"
            "v_fmac_f32 v53, v209, v205\n\tv_mfma_f32_16x16x32_bf16 v[226:229], v[40:43], v[222:225], 0\n\ts_nop 1\n\tv_cndmask_b32 v36, v220, v218, s[14:15]\n\tv_cndmask_b32 v37, v221, v219, s[16:17]\n\tv_mfma_f32_16x16x32_bf16 v[108:111], v[176:179], v[144:147], v[108:111]\n\t"
            "v_fmac_f32 v54, v209, v206\n\tv_fmac_f32 v55, v209, v207\n\tv_mfma_f32_16x16x32_bf16 v[112:115], v[192:195], v[172:175], v[112:115]\n\tv_cndmask_b32 v38, v228, v226, s[14:15]\n\tv_cndmask_b32 v39, v229, v227, s[16:17]\n\tv_mfma_f32_16x16x32_bf16 v[108:111], v[188:191], v[172:175], v[108:111]\n\t"
            "v_fmac_f32 v48, v209, v200\n\tv_fmac_f32 v49, v209, v201\n\tv_fmac_f32 v50, v209, v202\n\tv_fmac_f32 v51, v209, v203\n\tv_cvt_pk_bf16_f32 v52, v52, v53\n\tv_cvt_pk_bf16_f32 v53, v54, v55\n\t"
            "v_cvt_pk_bf16_f32 v250, v48, v49\n\tv_cvt_pk_bf16_f32 v251, v50, v51\n\tv_cndmask_b32 v54, v40, v52, vcc\n\tv_cndmask_b32 v55, v41, v53, vcc\n\tv_and_b32 v48, v250, v18\n\tv_and_b32 v49, v251, v18\n\t"
            "v_and_b32 v50, v250, v19\n\tv_and_b32 v51, v251, v19\n\tv_and_b32 v234, v250, v20\n\tv_and_b32 v235, v251, v20\n\tv_mfma_f32_16x16x32_bf16 v[230:233], v[52:55], v[48:51], 0\n\tv_and_b32 v236, v250, v21\n\t"
            "v_and_b32 v237, v251, v21\n\ts_waitcnt lgkmcnt(0)\n\tv_fmac_f32 v64, v210, v204\n\tv_fmac_f32 v65, v210, v205\n\tv_mfma_f32_16x16x32_bf16 v[238:241], v[52:55], v[234:237], 0\n\tv_fmac_f32 v66, v210, v206\n\t"
            "s_nop 0\n\tv_cndmask_b32 v48, v232, v230, s[14:15]\n\tv_cndmask_b32 v49, v233, v231, s[16:17]\n\tv_fmac_f32 v67, v210, v207\n\tv_fmac_f32 v60, v210, v200\n\tv_fmac_f32 v61, v210, v201\n\t"
            "v_fmac_f32 v62, v210, v202\n\tv_cndmask_b32 v50, v240, v238, s[14:15]\n\tv_cndmask_b32 v51, v241, v239, s[16:17]\n\tv_fmac_f32 v63, v210, v203\n\tv_cvt_pk_bf16_f32 v64, v64, v65\n\tv_cvt_pk_bf16_f32 v65, v66, v67\n\t"
            "v_cvt_pk_bf16_f32 v250, v60, v61\n\tv_cvt_pk_bf16_f32 v251, v62, v63\n\tv_cndmask_b32 v66, v52, v64, vcc\n\tv_cndmask_b32 v67, v53, v65, vcc\n\tv_and_b32 v60, v250, v18\n\tv_and_b32 v61, v251, v18\n\t"
            "v_and_b32 v62, v250, v19\n\tv_and_b32 v63, v251, v19\n\tv_and_b32 v246, v250, v20\n\tv_and_b32 v247, v251, v20\n\tv_mfma_f32_16x16x32_bf16 v[242:245], v[64:67], v[60:63], 0\n\tv_and_b32 v248, v250, v21\n\t"
            "v_and_b32 v249, v251, v21\n\tv_fmac_f32 v76, v211, v204\n\tv_fmac_f32 v77, v211, v205\n\tv_mfma_f32_16x16x32_bf16 v[218:221], v[64:67], v[246:249], 0\n\tv_fmac_f32 v78, v211, v206\n\ts_nop 1\n\t"
            "v_cndmask_b32 v60, v244, v242, s[14:15]\n\tv_cndmask_b32 v61, v245, v243, s[16:17]\n\tv_fmac_f32 v79, v211, v207\n\tv_fmac_f32 v72, v211, v200\n\tv_fmac_f32 v73, v211, v201\n\tv_fmac_f32 v74, v211, v202\n\t"
            "v_cndmask_b32 v62, v220, v218, s[14:15]\n\tv_cndmask_b32 v63, v221, v219, s[16:17]\n\tv_fmac_f32 v75, v211, v203\n\tv_cvt_pk_bf16_f32 v76, v76, v77\n\tv_cvt_pk_bf16_f32 v77, v78, v79\n\tv_cvt_pk_bf16_f32 v250, v72, v73\n\t"
            "v_cvt_pk_bf16_f32 v251, v74, v75\n\tv_cndmask_b32 v78, v64, v76, vcc\n\tv_cndmask_b32 v79, v65, v77, vcc\n\tv_and_b32 v72, v250, v18\n\tv_and_b32 v73, v251, v18\n\tv_and_b32 v74, v250, v19\n\t"
            "v_and_b32 v75, v251, v19\n\tv_mfma_f32_16x16x32_bf16 v[44:47], v[120:123], v[184:187], v[44:47]\n\tds_read_b128 v[120:123], v6\n\tv_and_b32 v226, v250, v20\n\tv_mfma_f32_16x16x32_bf16 v[222:225], v[76:79], v[72:75], 0\n\tv_and_b32 v227, v251, v20\n\t"
            "v_and_b32 v228, v250, v21\n\tv_mfma_f32_16x16x32_bf16 v[56:59], v[124:127], v[184:187], v[56:59]\n\tds_read_b128 v[124:127], v2 offset:2048\n\tv_and_b32 v229, v251, v21\n\tv_mfma_f32_16x16x32_bf16 v[68:71], v[128:131], v[184:187], v[68:71]\n\tds_read_b128 v[128:131], v2 offset:4096\n\t"
            "s_nop 0\n\tv_cndmask_b32 v72, v224, v222, s[14:15]\n\tv_mfma_f32_16x16x32_bf16 v[230:233], v[76:79], v[226:229], 0\n\tv_cndmask_b32 v73, v225, v223, s[16:17]\n\ts_cbranch_scc0 1f\n\tv_cvt_pk_bf16_f32 v253, v36, v37\n\t"
            "v_min_u32 v253, -2, v253\n\tv_cvt_pk_bf16_f32 v255, v48, v49\n\tv_min_u32 v255, -2, v255\n\tbuffer_store_dwordx4 v[252:255], v17, s[44:47], s20 offen sc1\n\ts_nop 1\n\tv_cvt_pk_bf16_f32 v253, v60, v61\n\t"
            "v_min_u32 v253, -2, v253\n\tv_cvt_pk_bf16_f32 v255, v72, v73\n\tv_min_u32 v255, -2, v255\n\tbuffer_store_dwordx4 v[252:255], v17, s[44:47], s21 offen sc1\n\t1:\nv_mfma_f32_16x16x32_bf16 v[80:83], v[132:135], v[184:187], v[80:83]\n\t"
            "ds_read_b128 v[132:135], v2 offset:6144\n\tv_fmac_f32 v88, v212, v204\n\tv_mfma_f32_16x16x32_bf16 v[92:95], v[136:139], v[184:187], v[92:95]\n\tds_read_b128 v[136:139], v2 offset:8192\n\ts_nop 0\n\tv_cndmask_b32 v74, v232, v230, s[14:15]\n\t"
            "v_mfma_f32_16x16x32_bf16 v[104:107], v[140:143], v[184:187], v[104:107]\n\tds_read_b128 v[140:143], v2 offset:10240\n\tv_cndmask_b32 v75, v233, v231, s[16:17]\n\tv_mfma_f32_16x16x32_bf16 v[116:119], v[144:147], v[184:187], v[116:119]\n\tds_read_b64_tr_b16 v[184:185], v12 offset:2048\n\tv_fmac_f32 v89, v212, v205\n\t"
            "ds_read_b64_tr_b16 v[186:187], v12 offset:2560\n\tv_mfma_f32_16x16x32_bf16 v[44:47], v[148:151], v[196:199], v[44:47]\n\tds_read_b128 v[148:151], v7\n\tv_fmac_f32 v90, v212, v206\n\tv_mfma_f32_16x16x32_bf16 v[56:59], v[152:155], v[196:199], v[56:59]\n\tds_write_b64 v28, v[36:37] offset:0\n\t"
            "v_fmac_f32 v91, v212, v207\n\tv_mfma_f32_16x16x32_bf16 v[68:71], v[156:159], v[196:199], v[68:71]\n\tv_fmac_f32 v84, v212, v200\n\tv_fmac_f32 v85, v212, v201\n\tds_write_b64 v29, v[38:39] offset:0\n\tv_mfma_f32_16x16x32_bf16 v[80:83], v[160:163], v[196:199], v[80:83]\n\t"
            "v_fmac_f32 v86, v212, v202\n\tv_fmac_f32 v87, v212, v203\n\tv_mfma_f32_16x16x32_bf16 v[92:95], v[164:167], v[196:199], v[92:95]\n\tds_write_b64 v28, v[48:49] offset:512\n\tv_cvt_pk_bf16_f32 v88, v88, v89\n\tv_mfma_f32_16x16x32_bf16 v[104:107], v[168:171], v[196:199], v[104:107]\n\t"
            "v_cvt_pk_bf16_f32 v89, v90, v91\n\tv_cvt_pk_bf16_f32 v250, v84, v85\n\tds_write_b64 v29, v[50:51] offset:512\n\tv_mfma_f32_16x16x32_bf16 v[116:119], v[172:175], v[196:199], v[116:119]\n\tv_cvt_pk_bf16_f32 v251, v86, v87\n\tv_cndmask_b32 v90, v76, v88, vcc\n\t"
            "s_waitcnt lgkmcnt(4)\n\tv_mfma_f32_16x16x32_bf16 v[44:47], v[120:123], v[184:187], v[44:47]\n\tds_write_b64 v28, v[60:61] offset:1024\n\tv_cndmask_b32 v91, v77, v89, vcc\n\tv_mfma_f32_16x16x32_bf16 v[56:59], v[124:127], v[184:187], v[56:59]\n\tv_and_b32 v84, v250, v18\n\t"
            "v_and_b32 v85, v251, v18\n\tds_write_b64 v29, v[62:63] offset:1024\n\tv_mfma_f32_16x16x32_bf16 v[68:71], v[128:131], v[184:187], v[68:71]\n\tv_and_b32 v86, v250, v19\n\tv_and_b32 v87, v251, v19\n\tv_mfma_f32_16x16x32_bf16 v[80:83], v[132:135], v[184:187], v[80:83]\n\t"
            "ds_write_b64 v28, v[72:73] offset:1536\n\tv_and_b32 v238, v250, v20\n\tv_mfma_f32_16x16x32_bf16 v[234:237], v[88:91], v[84:87], 0\n\tv_and_b32 v239, v251, v20\n\tv_and_b32 v240, v250, v21\n\tds_write_b64 v29, v[74:75] offset:1536\n\t"
            "v_and_b32 v241, v251, v21\n\tv_fmac_f32 v100, v213, v204\n\tv_fmac_f32 v101, v213, v205\n\tv_mfma_f32_16x16x32_bf16 v[242:245], v[88:91], v[238:241], 0\n\tds_read_b64_tr_b16 v[196:197], v12 offset:14336\n\tv_cndmask_b32 v84, v236, v234, s[14:15]\n\t"
            "ds_read_b64_tr_b16 v[198:199], v12 offset:14848\n\tv_cndmask_b32 v85, v237, v235, s[16:17]\n\tds_read_b128 v[152:155], v3 offset:2048\n\tv_fmac_f32 v102, v213, v206\n\ts_waitcnt lgkmcnt(0)\n\ts_barrier\n\t"
            "v_cndmask_b32 v86, v244, v242, s[14:15]\n\tds_write_b64 v28, v[84:85] offset:38912\n\tv_cndmask_b32 v87, v245, v243, s[16:17]\n\tv_fmac_f32 v103, v213, v207\n\tv_fmac_f32 v96, v213, v200\n\tv_fmac_f32 v97, v213, v201\n\t"
            "v_fmac_f32 v98, v213, v202\n\tds_write_b64 v29, v[86:87] offset:38912\n\tv_fmac_f32 v99, v213, v203\n\tv_cvt_pk_bf16_f32 v100, v100, v101\n\tv_cvt_pk_bf16_f32 v101, v102, v103\n\tv_cvt_pk_bf16_f32 v250, v96, v97\n\t"
            "v_cvt_pk_bf16_f32 v251, v98, v99\n\tds_read_b128 v[156:159], v3 offset:4096\n\tv_cndmask_b32 v102, v88, v100, vcc\n\tv_cndmask_b32 v103, v89, v101, vcc\n\tv_and_b32 v96, v250, v18\n\tds_read_b128 v[36:39], v15 offset:0\n\t"
            "v_and_b32 v97, v251, v18\n\tv_and_b32 v98, v250, v19\n\tv_and_b32 v99, v251, v19\n\tds_read_b128 v[160:163], v3 offset:6144\n\tv_and_b32 v218, v250, v20\n\tv_mfma_f32_16x16x32_bf16 v[246:249], v[100:103], v[96:99], 0\n\t"
            "ds_read_b128 v[48:51], v15 offset:64\n\tv_and_b32 v219, v251, v20\n\tv_mfma_f32_16x16x32_bf16 v[44:47], v[148:151], v[196:199], v[44:47]\n\tds_read_b128 v[60:63], v15 offset:128\n\tv_and_b32 v220, v250, v21\n\tv_mfma_f32_16x16x32_bf16 v[56:59], v[152:155], v[196:199], v[56:59]\n\t"
            "ds_read_b128 v[72:75], v15 offset:192\n\tv_and_b32 v221, v251, v21\n\tv_cndmask_b32 v96, v248, v246, s[14:15]\n\tv_cndmask_b32 v97, v249, v247, s[16:17]\n\tds_write_b64 v28, v[96:97] offset:39424\n\tv_mfma_f32_16x16x32_bf16 v[222:225], v[100:103], v[218:221], 0\n\t"
            "v_fmac_f32 v112, v214, v204\n\tv_fmac_f32 v113, v214, v205\n\ts_waitcnt lgkmcnt(5)\n\tv_mfma_f32_16x16x32_bf16 v[68:71], v[156:159], v[196:199], v[68:71]\n\tds_read_b128 v[144:147], v2 offset:12288\n\tv_fmac_f32 v114, v214, v206\n\t"
            "v_fmac_f32 v115, v214, v207\n\ts_nop 0\n\tv_cndmask_b32 v98, v224, v222, s[14:15]\n\tds_read_b128 v[164:167], v3 offset:8192\n\ts_waitcnt lgkmcnt(5)\n\tv_mfma_f32_16x16x32_bf16 v[80:83], v[160:163], v[196:199], v[80:83]\n\t"
            "v_cndmask_b32 v99, v225, v223, s[16:17]\n\tds_write_b64 v29, v[98:99] offset:39424\n\tv_fmac_f32 v108, v214, v200\n\tv_fmac_f32 v109, v214, v201\n\tv_fmac_f32 v110, v214, v202\n\tv_fmac_f32 v111, v214, v203\n\t"
            "v_cvt_pk_bf16_f32 v112, v112, v113\n\tds_read_b128 v[168:171], v3 offset:10240\n\tv_cvt_pk_bf16_f32 v113, v114, v115\n\tv_cvt_pk_bf16_f32 v250, v108, v109\n\tv_cvt_pk_bf16_f32 v251, v110, v111\n\tds_read_b128 v[172:175], v3 offset:12288\n\t"
            "v_cndmask_b32 v114, v100, v112, vcc\n\tv_cndmask_b32 v115, v101, v113, vcc\n\tv_and_b32 v108, v250, v18\n\tv_and_b32 v109, v251, v18\n\tv_and_b32 v110, v250, v19\n\tv_and_b32 v111, v251, v19\n\t"
            "v_and_b32 v230, v250, v20\n\tv_and_b32 v231, v251, v20\n\tv_mfma_f32_16x16x32_bf16 v[226:229], v[112:115], v[108:111], 0\n\tv_and_b32 v232, v250, v21\n\tv_and_b32 v233, v251, v21\n\tv_mfma_f32_16x16x32_bf16 v[92:95], v[136:139], v[184:187], v[92:95]\n\t"
            "v_mov_b32 v216, v44\n\tv_mov_b32 v217, v44\n\tv_mfma_f32_16x16x32_bf16 v[234:237], v[112:115], v[230:233], 0\n\ts_nop 1\n\tv_cndmask_b32 v108, v228, v226, s[14:15]\n\tv_cndmask_b32 v109, v229, v227, s[16:17]\n\t"
            "ds_write_b64 v28, v[108:109] offset:39936\n\ts_cbranch_scc0 1f\n\tv_cvt_pk_bf16_f32 v253, v84, v85\n\tv_min_u32 v253, -2, v253\n\tv_cvt_pk_bf16_f32 v255, v96, v97\n\tv_min_u32 v255, -2, v255\n\t"
            "buffer_store_dwordx4 v[252:255], v17, s[44:47], s22 offen sc1\n\ts_nop 1\n\tv_cvt_pk_bf16_f32 v253, v108, v109\n\tv_min_u32 v253, -2, v253\n\tbuffer_store_dwordx4 v[252:255], v17, s[44:47], s23 offen sc1\n\t1:\n"
            "v_mfma_f32_16x16x32_bf16 v[104:107], v[140:143], v[184:187], v[104:107]\n\tv_permlane32_swap_b32 v216, v217\n\tv_cndmask_b32 v110, v236, v234, s[14:15]\n\ts_waitcnt lgkmcnt(1)\n\tv_mfma_f32_16x16x32_bf16 v[116:119], v[144:147], v[184:187], v[116:119]\n\tv_cndmask_b32 v111, v237, v235, s[16:17]\n\t"
            "ds_write_b64 v29, v[110:111] offset:39936\n\tv_mfma_f32_16x16x32_bf16 v[92:95], v[164:167], v[196:199], v[92:95]\n\tv_mov_b32 v217, v216\n\tv_mov_b32 v218, 0\n\tv_mfma_f32_16x16x32_bf16 v[104:107], v[168:171], v[196:199], v[104:107]\n\tds_write_b32 v33, v33\n\t"
            SA(3)
            "v_permlane16_swap_b32 v216, v217\n\tv_mfma_f32_16x16x32_bf16 v[116:119], v[172:175], v[196:199], v[116:119]\n\tv_fmac_f32 v44, v36, v216\n\tv_fmac_f32 v45, v37, v216\n\tv_fmac_f32 v46, v38, v216\n\tv_fmac_f32 v47, v39, v216\n\t"
            "v_cvt_pk_bf16_f32 v44, v44, v45\n\tv_cvt_pk_bf16_f32 v45, v46, v47\n\tv_and_b32 v220, v44, v22\n\tv_and_b32 v221, v45, v22\n\tv_and_b32 v236, v44, v23\n\tv_and_b32 v237, v45, v23\n\t"
            "v_fmac_f32 v56, v48, v216\n\tv_fmac_f32 v57, v49, v216\n\tv_fmac_f32 v58, v50, v216\n\tv_fmac_f32 v59, v51, v216\n\tv_cvt_pk_bf16_f32 v56, v56, v57\n\tv_cvt_pk_bf16_f32 v57, v58, v59\n\t"
            "v_and_b32 v222, v56, v22\n\tv_and_b32 v223, v57, v22\n\tv_and_b32 v238, v56, v23\n\tv_and_b32 v239, v57, v23\n\tv_fmac_f32 v68, v60, v216\n\tv_fmac_f32 v69, v61, v216\n\t"
            "v_fmac_f32 v70, v62, v216\n\tv_fmac_f32 v71, v63, v216\n\tv_cvt_pk_bf16_f32 v68, v68, v69\n\tv_cvt_pk_bf16_f32 v69, v70, v71\n\tv_and_b32 v224, v68, v22\n\tv_and_b32 v225, v69, v22\n\t"
            "v_and_b32 v240, v68, v23\n\tv_and_b32 v241, v69, v23\n\tv_fmac_f32 v80, v72, v216\n\tv_fmac_f32 v81, v73, v216\n\tv_fmac_f32 v82, v74, v216\n\tv_fmac_f32 v83, v75, v216\n\t"
            "ds_read_b32 v27, v32\n\tds_read2st64_b32 v[42:43], v31 offset0:0 offset1:1\n\tds_read2st64_b32 v[40:41], v30 offset0:0 offset1:1\n\tds_read2st64_b32 v[66:67], v31 offset0:2 offset1:3\n\tds_read2st64_b32 v[64:65], v30 offset0:2 offset1:3\n\tv_cvt_pk_bf16_f32 v80, v80, v81\n\t"
            "v_cvt_pk_bf16_f32 v81, v82, v83\n\tv_and_b32 v226, v80, v22\n\tv_and_b32 v227, v81, v22\n\tv_and_b32 v242, v80, v23\n\tv_and_b32 v243, v81, v23\n\tds_read_b128 v[84:87], v15 offset:256\n\t"
            "v_mov_b32 v219, 0\n\tv_mov_b32 v234, 0\n\tv_mov_b32 v235, 0\n\tds_read_b128 v[96:99], v15 offset:320\n\tv_mov_b32 v176, 0\n\tv_mov_b32 v180, 0\n\t"
            "v_mov_b32 v188, 0\n\tds_read_b128 v[108:111], v15 offset:384\n\tv_mov_b32 v192, 0\n\t"
            SA(6)
            "s_waitcnt lgkmcnt(5)\n\tv_cmp_ge_u32 vcc, 0, v27\n\ts_cbranch_vccnz 10f\n\t20:\nv_and_b32 v177, v42, v24\n\tv_and_b32 v178, v42, v25\n\t"
            "v_and_b32 v179, v42, v26\n\tv_and_b32 v189, v43, v24\n\tv_and_b32 v190, v43, v25\n\tv_and_b32 v191, v43, v26\n\tv_mfma_f32_16x16x32_bf16 v[44:47], v[234:237], v[176:179], 0\n\tv_and_b32 v181, v40, v24\n\t"
            "v_and_b32 v182, v40, v25\n\tv_mfma_f32_16x16x32_bf16 v[56:59], v[236:239], v[188:191], 0\n\tv_and_b32 v183, v40, v26\n\tv_and_b32 v193, v41, v24\n\tv_and_b32 v194, v41, v25\n\tv_and_b32 v195, v41, v26\n\t"
            "v_mfma_f32_16x16x32_bf16 v[44:47], v[218:221], v[180:183], v[44:47]\n\ts_waitcnt lgkmcnt(0)\n\tv_and_b32 v177, v66, v24\n\tv_and_b32 v178, v66, v25\n\tv_mfma_f32_16x16x32_bf16 v[56:59], v[220:223], v[192:195], v[56:59]\n\tv_and_b32 v179, v66, v26\n\t"
            "v_and_b32 v189, v67, v24\n\tv_and_b32 v190, v67, v25\n\ts_nop 0\n\tv_cvt_pk_bf16_f32 v36, v44, v45\n\tv_cvt_pk_bf16_f32 v37, v46, v47\n\tv_min_u32 v36, -2, v36\n\t"
            "v_and_b32 v191, v67, v26\n\tv_cvt_pk_bf16_f32 v38, v56, v57\n\tv_cvt_pk_bf16_f32 v39, v58, v59\n\tv_min_u32 v38, -2, v38\n\tv_mfma_f32_16x16x32_bf16 v[68:71], v[238:241], v[176:179], 0\n\ts_nop 0\n\t"
            "v_permlane16_swap_b32 v36, v38\n\tv_permlane16_swap_b32 v37, v39\n\tbuffer_store_dwordx4 v[36:39], v16, s[44:47], s20 offen sc1\n\tv_mfma_f32_16x16x32_bf16 v[80:83], v[240:243], v[188:191], 0\n\tv_and_b32 v181, v64, v24\n\tv_and_b32 v182, v64, v25\n\t"
            "v_and_b32 v183, v64, v26\n\tv_and_b32 v193, v65, v24\n\tv_and_b32 v194, v65, v25\n\tv_and_b32 v195, v65, v26\n\tv_mfma_f32_16x16x32_bf16 v[68:71], v[222:225], v[180:183], v[68:71]\n\tv_fmac_f32 v92, v84, v216\n\t"
            "v_fmac_f32 v93, v85, v216\n\tv_mfma_f32_16x16x32_bf16 v[80:83], v[224:227], v[192:195], v[80:83]\n\tv_fmac_f32 v94, v86, v216\n\tv_fmac_f32 v95, v87, v216\n\tv_cvt_pk_bf16_f32 v92, v92, v93\n\ts_nop 1\n\t"
            "v_cvt_pk_bf16_f32 v60, v68, v69\n\tv_cvt_pk_bf16_f32 v61, v70, v71\n\tv_min_u32 v60, -2, v60\n\tv_fmac_f32 v104, v96, v216\n\tv_cvt_pk_bf16_f32 v62, v80, v81\n\tv_cvt_pk_bf16_f32 v63, v82, v83\n\t"
            "v_min_u32 v62, -2, v62\n\tv_fmac_f32 v105, v97, v216\n\tv_permlane16_swap_b32 v61, v63\n\tv_permlane16_swap_b32 v60, v62\n\tbuffer_store_dwordx4 v[60:63], v16, s[44:47], s21 offen sc1\n\tds_write_b32 v34, v35\n\t"
            SA(4)
            "v_cvt_pk_bf16_f32 v93, v94, v95\n\tv_fmac_f32 v106, v98, v216\n\tv_fmac_f32 v107, v99, v216\n\tv_cvt_pk_bf16_f32 v104, v104, v105\n\tv_fmac_f32 v116, v108, v216\n\tv_fmac_f32 v117, v109, v216\n\t"
            "v_and_b32 v228, v92, v22\n\tds_read_b32 v27, v32\n\tds_read2st64_b32 v[102:103], v31 offset0:152 offset1:153\n\tds_read2st64_b32 v[100:101], v30 offset0:152 offset1:153\n\tds_read2st64_b32 v[78:79], v31 offset0:154 offset1:155\n\tds_read2st64_b32 v[76:77], v30 offset0:154 offset1:155\n\t"
            "v_and_b32 v229, v93, v22\n\tv_and_b32 v244, v92, v23\n\tv_and_b32 v245, v93, v23\n\tv_cvt_pk_bf16_f32 v105, v106, v107\n\tv_fmac_f32 v118, v110, v216\n\tv_fmac_f32 v119, v111, v216\n\t"
            "v_cvt_pk_bf16_f32 v116, v116, v117\n\tv_and_b32 v230, v104, v22\n\tv_and_b32 v231, v105, v22\n\tv_and_b32 v246, v104, v23\n\tv_and_b32 v247, v105, v23\n\tv_cvt_pk_bf16_f32 v117, v118, v119\n\t"
            "v_and_b32 v232, v116, v22\n\tv_and_b32 v233, v117, v22\n\tv_and_b32 v248, v116, v23\n\tv_and_b32 v249, v117, v23\n\ts_waitcnt lgkmcnt(3)\n\tv_cmp_ge_u32 vcc, 1, v27\n\t"
            "s_cbranch_vccnz 11f\n\t21:\nv_and_b32 v177, v102, v24\n\tv_and_b32 v178, v102, v25\n\tv_and_b32 v179, v102, v26\n\tv_and_b32 v189, v103, v24\n\t"
            "v_and_b32 v190, v103, v25\n\tv_and_b32 v191, v103, v26\n\tv_mfma_f32_16x16x32_bf16 v[92:95], v[242:245], v[176:179], 0\n\ts_waitcnt lgkmcnt(0)\n\tv_and_b32 v181, v100, v24\n\tv_and_b32 v182, v100, v25\n\t"
            "v_mfma_f32_16x16x32_bf16 v[104:107], v[244:247], v[188:191], 0\n\tv_and_b32 v183, v100, v26\n\tv_and_b32 v193, v101, v24\n\tv_and_b32 v194, v101, v25\n\tv_and_b32 v195, v101, v26\n\tv_mfma_f32_16x16x32_bf16 v[92:95], v[226:229], v[180:183], v[92:95]\n\t"
            "v_and_b32 v177, v78, v24\n\tv_and_b32 v178, v78, v25\n\tv_mfma_f32_16x16x32_bf16 v[104:107], v[228:231], v[192:195], v[104:107]\n\tv_and_b32 v179, v78, v26\n\tv_and_b32 v181, v76, v24\n\tv_and_b32 v182, v76, v25\n\t"
            "v_mfma_f32_16x16x32_bf16 v[116:119], v[246:249], v[176:179], 0\n\tv_and_b32 v183, v76, v26\n\tv_cvt_pk_bf16_f32 v84, v92, v93\n\ts_nop 1\n\tv_cvt_pk_bf16_f32 v86, v104, v105\n\tv_mfma_f32_16x16x32_bf16 v[116:119], v[230:233], v[180:183], v[116:119]\n\t"
            "v_cvt_pk_bf16_f32 v85, v94, v95\n\tv_min_u32 v84, -2, v84\n\tv_cvt_pk_bf16_f32 v87, v106, v107\n\tv_min_u32 v86, -2, v86\n\ts_nop 0\n\tv_permlane16_swap_b32 v85, v87\n\t"
            "v_permlane16_swap_b32 v84, v86\n\tbuffer_store_dwordx4 v[84:87], v16, s[44:47], s22 offen sc1\n\tv_cvt_pk_bf16_f32 v108, v116, v117\n\tv_cvt_pk_bf16_f32 v109, v118, v119\n\tv_min_u32 v108, -2, v108\n\ts_nop 0\n\t"
            "v_permlane16_swap_b32 v109, v111\n\tv_permlane16_swap_b32 v108, v110\n\tbuffer_store_dwordx4 v[108:111], v215, s[44:47], s23 offen sc1\n\tds_write_b32 v34, v34\n\t"
            SA(5)
            "s_setprio 0\n\t"
            DUMP
            "s_cmp_eq_u32 s69, 1\n\ts_cbranch_scc1 .Lout\n\ts_endpgm\n\t10:\nds_read_b32 v27, v32\n\tds_read2st64_b32 v[42:43], v31 offset0:0 offset1:1\n\t"
            "ds_read2st64_b32 v[40:41], v30 offset0:0 offset1:1\n\tds_read2st64_b32 v[66:67], v31 offset0:2 offset1:3\n\tds_read2st64_b32 v[64:65], v30 offset0:2 offset1:3\n\ts_waitcnt lgkmcnt(0)\n\tv_cmp_ge_u32 vcc, 0, v27\n\ts_cbranch_vccz 20b\n\t"
            ASLP
            "s_sub_u32 s68, s68, 1\n\ts_cbranch_scc0 10b\n\ts_branch 20b\n\t11:\nds_read_b32 v27, v32\n\tds_read2st64_b32 v[102:103], v31 offset0:152 offset1:153\n\t"
            "ds_read2st64_b32 v[100:101], v30 offset0:152 offset1:153\n\tds_read2st64_b32 v[78:79], v31 offset0:154 offset1:155\n\tds_read2st64_b32 v[76:77], v30 offset0:154 offset1:155\n\ts_waitcnt lgkmcnt(0)\n\tv_cmp_ge_u32 vcc, 1, v27\n\ts_cbranch_vccz 21b\n\t"
            ASLP
            "s_sub_u32 s68, s68, 1\n\ts_cbranch_scc0 11b\n\ts_branch 21b\n\t.LB:\ns_and_b32 s72, s56, 1\n\ts_lshr_b32 s73, s56, 1\n\t"
            "v_and_b32 v156, 2, v51\n\tv_lshlrev_b32 v156, 1, v156\n\tv_and_b32 v157, 7, v52\n\tv_xor_b32 v157, v156, v157\n\tv_mul_u32_u24 v156, 0x500, v51\n\tv_lshl_add_u32 v156, v157, 4, v156\n\t"
            "s_mul_i32 s74, s72, 0x2800\n\ts_mul_i32 s75, s73, 0xf000\n\ts_add_i32 s75, s75, s74\n\tv_add_u32 v154, s75, v156\n\ts_mul_i32 s75, s73, 0x5000\n\ts_add_i32 s75, s75, s74\n\t"
            "s_addk_i32 s75, 0x5000\n\tv_add_u32 v156, s75, v156\n\ts_cmp_lg_u32 s63, 0\n\ts_cselect_b32 s74, 81920, 0\n\ts_max_u32 s74, s74, 65536\n\tv_add_u32 v158, s62, v52\n\t"
            "v_min_u32 v159, 47, v52\n\ts_add_i32 s75, s62, 64\n\tv_add_u32 v159, s75, v159\n\tv_max_i32 v158, 0, v158\n\tv_min_i32 v159, 0x3ff, v159\n\tv_lshl_add_u32 v158, v158, 4, s74\n\t"
            "v_lshl_add_u32 v159, v159, 4, s74\n\tv_lshl_add_u32 v59, v53, 4, v57\n\ts_cmp_lg_u32 s73, 0\n\ts_cselect_b64 vcc, -1, 0\n\tv_cndmask_b32 v97, v59, v52, vcc\n\ts_lshl_b32 s75, s72, 6\n\t"
            "s_add_i32 s75, s75, s62\n\tv_add_u32 v97, s75, v97\n\tv_mov_b32 v60, s62\n\tv_add_u32 v60, 0x6f, v60\n\tv_min_i32 v60, 0x3ff, v60\n\tv_med3_i32 v97, v97, 0, v60\n\t"
            "s_lshl3_add_u32 s74, s73, s74\n\tv_lshl_add_u32 v97, v97, 4, s74\n\ts_mul_i32 s30, s73, 576\n\ts_lshl_b32 s75, s72, 8\n\ts_add_i32 s30, s30, s75\n\ts_add_i32 s30, s30, 160000\n\t"
            "s_lshl_b32 s74, s59, 7\n\ts_add_i32 s74, s74, 8192\n\tv_min_u32 v96, 9, v52\n\tv_lshl_add_u32 v96, v96, 2, s74\n\ts_lshr_b32 s75, s58, 5\n\ts_add_i32 s74, s74, s75\n\t"
            "s_cmp_eq_u32 s56, 3\n\ts_cselect_b32 s74, s74, 0x40000000\n\tv_lshl_add_u32 v98, v52, 24, s74\n\ts_cselect_b32 s75, 1, 0\n\ts_lshl2_add_u32 s74, s59, s56\n\ts_mul_i32 s74, s74, 0xa000\n\t"
            "s_add_i32 s74, s74, 155648\n\tv_lshl_add_u32 v101, v52, 4, s74\n\tv_and_b32 v60, s75, v53\n\tv_lshl_add_u32 v2, v60, 30, v101\n\ts_cmp_ge_u32 s56, s57\n\ts_addc_u32 s76, s56, 0\n\t"
            "s_and_b32 s16, s76, 3\n\ts_lshl_b32 s77, s76, 1\n\ts_max_u32 s78, s77, 1\n\ts_sub_u32 s78, s78, 1\n\ts_min_u32 s79, s77, 7\n\tv_lshlrev_b32 v60, 9, v53\n\t"
            "v_lshl_add_u32 v60, v54, 3, v60\n\ts_mul_i32 s80, s78, 0xc00\n\ts_mul_i32 s81, s79, 0xc00\n\ts_addk_i32 s80, 14336\n\ts_addk_i32 s81, 14336\n\tv_add_u32 v24, s80, v60\n\t"
            "v_add_u32 v26, s81, v60\n\tv_add_u32 v25, 38912, v24\n\tv_add_u32 v27, 38912, v26\n\tv_lshl_add_u32 v29, v52, 4, s80\n\ts_lshr_b32 s78, s78, 1\n\ts_lshr_b32 s79, s79, 1\n\t"
            "s_sub_u32 s79, s79, s78\n\ts_lshl2_add_u32 s78, s78, 159744\n\tv_and_b32 v28, s79, v52\n\tv_lshl_add_u32 v28, v28, 2, s78\n\ts_lshl2_add_u32 s78, s56, 159744\n\tv_mov_b32 v30, s78\n\t"
            "s_mov_b32 s78, 160576\n\tv_lshl_add_u32 v153, v59, 2, s78\n\ts_mul_i32 s79, s59, 163840\n\ts_lshl_b32 s80, s58, 5\n\ts_add_i32 s79, s79, s80\n\ts_cmp_eq_u32 s76, 0\n\t"
            "s_cselect_b32 s80, 0, 0xc00\n\ts_add_i32 s79, s79, s80\n\ts_add_i32 s79, s79, 155652\n\tv_lshl_add_u32 v31, v54, 4, s79\n\tv_lshrrev_b32 v60, 1, v53\n\tv_mul_u32_u24 v60, 0xa000, v60\n\t"
            "v_add_u32 v31, v60, v31\n\tv_and_b32 v60, 1, v53\n\tv_lshl_add_u32 v31, v60, 3, v31\n\ts_mov_b32 s31, 0x14000\n\ts_cmp_lt_i32 s62, 0\n\ts_cselect_b32 s79, 8, -16\n\t"
            "v_mov_b32 v61, 0xff800000\n\tv_mov_b32 v142, 1\n\tv_add_u32 v60, 0, v59\n\tv_and_b32 v60, -8, v60\n\tv_add_u32 v60, 0, v60\n\tv_cmp_gt_i32 vcc, s79, v60\n\t"
            "v_lshl_add_u32 v160, v60, 2, s78\n\ts_nop 0\n\tv_cndmask_b32 v128, 0, v61, vcc\n\tv_add_u32 v60, 4, v59\n\tv_and_b32 v60, -8, v60\n\tv_add_u32 v60, -4, v60\n\t"
            "v_cmp_gt_i32 vcc, s79, v60\n\tv_lshl_add_u32 v161, v60, 2, s78\n\ts_nop 0\n\tv_cndmask_b32 v129, 0, v61, vcc\n\tv_add_u32 v60, -1, v59\n\tv_and_b32 v60, -8, v60\n\t"
            "v_add_u32 v60, 1, v60\n\tv_cmp_gt_i32 vcc, s79, v60\n\tv_lshl_add_u32 v162, v60, 2, s78\n\ts_nop 0\n\tv_cndmask_b32 v130, 0, v61, vcc\n\tv_add_u32 v60, 3, v59\n\t"
            "v_and_b32 v60, -8, v60\n\tv_add_u32 v60, -3, v60\n\tv_cmp_gt_i32 vcc, s79, v60\n\tv_lshl_add_u32 v163, v60, 2, s78\n\ts_nop 0\n\tv_cndmask_b32 v131, 0, v61, vcc\n\t"
            "v_add_u32 v60, -2, v59\n\tv_and_b32 v60, -8, v60\n\tv_add_u32 v60, 2, v60\n\tv_cmp_gt_i32 vcc, s79, v60\n\tv_lshl_add_u32 v164, v60, 2, s78\n\ts_nop 0\n\t"
            "v_cndmask_b32 v132, 0, v61, vcc\n\tv_add_u32 v60, 2, v59\n\tv_and_b32 v60, -8, v60\n\tv_add_u32 v60, -2, v60\n\tv_cmp_gt_i32 vcc, s79, v60\n\tv_lshl_add_u32 v165, v60, 2, s78\n\t"
            "s_nop 0\n\tv_cndmask_b32 v133, 0, v61, vcc\n\tv_add_u32 v60, -3, v59\n\tv_and_b32 v60, -8, v60\n\tv_add_u32 v60, 3, v60\n\tv_cmp_gt_i32 vcc, s79, v60\n\t"
            "v_lshl_add_u32 v166, v60, 2, s78\n\ts_nop 0\n\tv_cndmask_b32 v134, 0, v61, vcc\n\tv_add_u32 v60, 1, v59\n\tv_and_b32 v60, -8, v60\n\tv_add_u32 v60, -1, v60\n\t"
            "v_cmp_gt_i32 vcc, s79, v60\n\tv_lshl_add_u32 v167, v60, 2, s78\n\ts_nop 0\n\tv_cndmask_b32 v135, 0, v61, vcc\n\ts_lshl2_add_u32 s80, s71, s56\n\ts_lshl_b32 s80, s80, 5\n\t"
            "v_add_u32 v100, s80, v52\n\tv_mul_u32_u24 v100, 0x500, v100\n\ts_add_i32 s80, s58, 3072\n\tv_add_u32 v100, s80, v100\n\tv_min_u32 v104, 51, v52\n\tv_lshlrev_b32 v104, 4, v104\n\t"
            "v_add_u32 v104, 0x1000, v104\n\ts_lshl_b32 s4, s64, 13\n\ts_add_u32 s5, s4, 0x1000\n\ts_add_u32 s6, s5, 0x1000\n\ts_cmpk_eq_u32 s6, 0xa000\n\ts_cselect_b32 s6, 0, s6\n\t"
            "s_add_u32 s7, s6, 0x1000\n\ts_add_u32 s8, s7, 0x1000\n\ts_cmpk_eq_u32 s8, 0xa000\n\ts_cselect_b32 s8, 0, s8\n\ts_add_u32 s9, s8, 0x1000\n\ts_add_u32 s10, s9, 0x1000\n\t"
            "s_cmpk_eq_u32 s10, 0xa000\n\ts_cselect_b32 s10, 0, s10\n\ts_add_u32 s11, s10, 0x1000\n\ts_add_u32 s12, s11, 0x1000\n\ts_cmpk_eq_u32 s12, 0xa000\n\ts_cselect_b32 s12, 0, s12\n\t"
            "s_add_u32 s13, s12, 0x1000\n\ts_mul_i32 s20, s4, 20\n\ts_add_i32 s20, s20, s58\n\ts_addk_i32 s20, 3072\n\ts_mul_i32 s21, s5, 20\n\ts_add_i32 s21, s21, s58\n\t"
            "s_addk_i32 s21, 2048\n\ts_mul_i32 s22, s6, 20\n\ts_add_i32 s22, s22, s58\n\ts_addk_i32 s22, 1024\n\ts_mul_i32 s23, s7, 20\n\ts_add_i32 s23, s23, s58\n\t"
            "s_mul_i32 s24, s8, 20\n\ts_add_i32 s24, s24, s58\n\ts_addk_i32 s24, 3072\n\ts_mul_i32 s25, s9, 20\n\ts_add_i32 s25, s25, s58\n\ts_addk_i32 s25, 2048\n\t"
            "s_mul_i32 s26, s10, 20\n\ts_add_i32 s26, s26, s58\n\ts_addk_i32 s26, 1024\n\ts_mul_i32 s27, s11, 20\n\ts_add_i32 s27, s27, s58\n\ts_mul_i32 s28, s12, 20\n\t"
            "s_add_i32 s28, s28, s58\n\ts_addk_i32 s28, 3072\n\ts_mul_i32 s29, s13, 20\n\ts_add_i32 s29, s29, s58\n\ts_addk_i32 s29, 2048\n\ts_mul_i32 s72, s60, 1920\n\t"
            "s_sub_u32 s14, s61, s72\n\ts_add_u32 s17, s63, 0x500\n\ts_add_u32 s60, s60, 256\n\ts_cmp_eq_u32 s60, s17\n\ts_cselect_b32 s60, s63, s60\n\ts_mul_i32 s15, s56, 0x2800\n\t"
            "s_add_u32 s15, s15, 77824\n\tv_lshl_add_u32 v152, v52, 4, s15\n\ts_lshl_b32 s18, s58, 5\n\ts_mov_b32 s50, 822272\n\ts_mov_b32 s51, 0x20000\n\ts_waitcnt lgkmcnt(0)\n\t"
            "s_load_dword s67, s[44:45], 0x0\n\ts_sub_u32 s48, s48, 3072\n\ts_subb_u32 s49, s49, 0\n\ts_lshl_b32 s78, s56, 5\n\tv_add_u32 v12, s78, v57\n\tv_add_u32 v3, s62, v12\n\t"
            "v_and_b32 v13, 1, v53\n\tv_lshrrev_b32 v14, 1, v53\n\tv_lshl_add_u32 v15, v13, 1, v14\n\ts_add_u32 s79, s63, s58\n\tv_lshl_add_u32 v15, v15, 4, s79\n\ts_movk_i32 s79, 0xa00\n\t"
            "v_mad_i32_i24 v3, v3, s79, v15\n\tv_cmp_gt_u32 vcc, 8, v12\n\ts_cmp_eq_u32 s56, 3\n\ts_cselect_b32 s79, 2.0, 0xa000\n\tv_add_u32 v11, s79, v3\n\tv_cndmask_b32 v3, v3, 2.0, vcc\n\t"
            "v_mov_b32 v8, 159744\n\tv_and_b32 v9, 31, v52\n\tv_add_u32 v9, s78, v9\n\tv_lshl_add_u32 v9, v9, 2, v8\n\tv_lshl_add_u32 v10, v13, 4, v12\n\tv_lshl_add_u32 v10, v10, 2, v8\n\t"
            "v_mul_u32_u24 v4, 0x2800, v14\n\tv_lshl_add_u32 v4, v56, 7, v4\n\tv_lshl_add_u32 v4, v55, 3, v4\n\tv_lshl_add_u32 v4, v13, 5, v4\n\tv_bfe_u32 v5, v52, 3, 1\n\tv_lshl_add_u32 v4, v5, 6, v4\n\t"
            "v_add_u32 v4, 77824, v4\n\tv_xor_b32 v5, 64, v4\n\tv_add_u32 v6, 0x5000, v4\n\tv_add_u32 v7, 0x5000, v5\n\ts_waitcnt vmcnt(0) lgkmcnt(0)\n\ts_barrier\n\t"
            "v_mov_b32 v136, -1\n\tv_mov_b32 v137, -1\n\tv_mov_b32 v138, -1\n\tv_mov_b32 v139, -1\n\ts_add_u32 m0, s66, 0\n\ts_mul_i32 s61, s60, 1920\n\t"
            "s_add_u32 s61, s61, s14\n\tbuffer_load_dwordx4 v40, s[40:43], s61 offen lds\n\tbuffer_load_dwordx4 v41, s[40:43], s61 offen offset:1024 lds\n\tbuffer_load_dwordx4 v42, s[40:43], s61 offen offset:2048 lds\n\tbuffer_load_dwordx4 v43, s[40:43], s61 offen offset:3072 lds\n\ts_addk_i32 m0, 0x1000\n\t"
            "s_add_u32 s68, s60, 128\n\tbuffer_load_dwordx4 v44, s[40:43], s61 offen lds\n\tbuffer_load_dwordx4 v45, s[40:43], s61 offen offset:1024 lds\n\ts_add_u32 m0, s65, 0\n\ts_cmp_eq_u32 s68, s17\n\tbuffer_load_dwordx4 v36, s[36:39], s60 offen lds\n\t"
            "buffer_load_dwordx4 v37, s[36:39], s60 offen offset:1024 lds\n\tbuffer_load_dwordx4 v38, s[36:39], s60 offen offset:2048 lds\n\ts_mov_b32 exec_hi, 0\n\tbuffer_load_dwordx4 v39, s[36:39], s60 offen offset:3072 lds\n\ts_mov_b32 exec_hi, -1\n\ts_cselect_b32 s60, s63, s68\n\t"
            "s_mul_i32 s67, s67, 0x19660d\n\ts_add_u32 s67, s67, 0x3c6ef35f\n\tbuffer_store_dwordx4 v[136:139], v101, s[44:47], s18 offen offset:0 sc1\n\tbuffer_store_dwordx4 v[136:139], v2, s[44:47], s18 offen offset:1024 sc1\n\tbuffer_store_dwordx4 v[136:139], v2, s[44:47], s18 offen offset:2048 sc1\n\tbuffer_store_dwordx4 v[136:139], v101, s[44:47], s18 offen offset:3072 sc1\n\t"
            "s_barrier\n\ts_add_u32 m0, s66, 38912\n\ts_mul_i32 s61, s60, 1920\n\ts_add_u32 s61, s61, s14\n\tbuffer_load_dwordx4 v40, s[40:43], s61 offen lds\n\tbuffer_load_dwordx4 v41, s[40:43], s61 offen offset:1024 lds\n\t"
            "buffer_load_dwordx4 v42, s[40:43], s61 offen offset:2048 lds\n\tbuffer_load_dwordx4 v43, s[40:43], s61 offen offset:3072 lds\n\ts_addk_i32 m0, 0x1000\n\ts_add_u32 s68, s60, 128\n\tbuffer_load_dwordx4 v44, s[40:43], s61 offen lds\n\tbuffer_load_dwordx4 v45, s[40:43], s61 offen offset:1024 lds\n\t"
            "s_add_u32 m0, s65, 38912\n\ts_cmp_eq_u32 s68, s17\n\tbuffer_load_dwordx4 v36, s[36:39], s60 offen lds\n\tbuffer_load_dwordx4 v37, s[36:39], s60 offen offset:1024 lds\n\tbuffer_load_dwordx4 v38, s[36:39], s60 offen offset:2048 lds\n\ts_mov_b32 exec_hi, 0\n\t"
            "buffer_load_dwordx4 v39, s[36:39], s60 offen offset:3072 lds\n\ts_mov_b32 exec_hi, -1\n\ts_cselect_b32 s60, s63, s68\n\ts_waitcnt vmcnt(14)\n\ts_barrier\n\ts_add_u32 m0, s66, 77824\n\t"
            "s_mul_i32 s61, s60, 1920\n\ts_add_u32 s61, s61, s14\n\tbuffer_load_dwordx4 v40, s[40:43], s61 offen lds\n\tbuffer_load_dwordx4 v41, s[40:43], s61 offen offset:1024 lds\n\tbuffer_load_dwordx4 v42, s[40:43], s61 offen offset:2048 lds\n\tbuffer_load_dwordx4 v43, s[40:43], s61 offen offset:3072 lds\n\t"
            "s_addk_i32 m0, 0x1000\n\ts_add_u32 s68, s60, 128\n\tbuffer_load_dwordx4 v44, s[40:43], s61 offen lds\n\tbuffer_load_dwordx4 v45, s[40:43], s61 offen offset:1024 lds\n\ts_add_u32 m0, s65, 77824\n\ts_cmp_eq_u32 s68, s17\n\t"
            "buffer_load_dwordx4 v36, s[36:39], s60 offen lds\n\tbuffer_load_dwordx4 v37, s[36:39], s60 offen offset:1024 lds\n\tbuffer_load_dwordx4 v38, s[36:39], s60 offen offset:2048 lds\n\ts_mov_b32 exec_hi, 0\n\tbuffer_load_dwordx4 v39, s[36:39], s60 offen offset:3072 lds\n\ts_mov_b32 exec_hi, -1\n\t"
            "s_cselect_b32 s60, s63, s68\n\ts_waitcnt vmcnt(10)\n\ts_barrier\n\ts_add_u32 m0, s66, 0\n\ts_mul_i32 s61, s60, 1920\n\ts_add_u32 s61, s61, s14\n\t"
            "buffer_load_dwordx4 v40, s[40:43], s61 offen lds\n\tbuffer_load_dwordx4 v41, s[40:43], s61 offen offset:1024 lds\n\tbuffer_load_dwordx4 v42, s[40:43], s61 offen offset:2048 lds\n\tbuffer_load_dwordx4 v43, s[40:43], s61 offen offset:3072 lds\n\ts_addk_i32 m0, 0x1000\n\ts_add_u32 s68, s60, 128\n\t"
            "buffer_load_dwordx4 v44, s[40:43], s61 offen lds\n\tbuffer_load_dwordx4 v45, s[40:43], s61 offen offset:1024 lds\n\ts_add_u32 m0, s65, 0\n\ts_cmp_eq_u32 s68, s17\n\tbuffer_load_dwordx4 v36, s[36:39], s60 offen lds\n\tbuffer_load_dwordx4 v37, s[36:39], s60 offen offset:1024 lds\n\t"
            "buffer_load_dwordx4 v38, s[36:39], s60 offen offset:2048 lds\n\ts_mov_b32 exec_hi, 0\n\tbuffer_load_dwordx4 v39, s[36:39], s60 offen offset:3072 lds\n\ts_mov_b32 exec_hi, -1\n\ts_cselect_b32 s60, s63, s68\n\tv_mov_b32 v140, s67\n\t"
            "buffer_store_dword v140, v98, s[44:47], 0 offen sc1\n\ts_waitcnt vmcnt(11)\n\ts_barrier\n\ts_add_u32 m0, s66, 38912\n\ts_mul_i32 s61, s60, 1920\n\ts_add_u32 s61, s61, s14\n\t"
            "buffer_load_dwordx4 v40, s[40:43], s61 offen lds\n\tbuffer_load_dwordx4 v41, s[40:43], s61 offen offset:1024 lds\n\tbuffer_load_dwordx4 v42, s[40:43], s61 offen offset:2048 lds\n\tbuffer_load_dwordx4 v43, s[40:43], s61 offen offset:3072 lds\n\ts_addk_i32 m0, 0x1000\n\ts_add_u32 s68, s60, 128\n\t"
            "buffer_load_dwordx4 v44, s[40:43], s61 offen lds\n\tbuffer_load_dwordx4 v45, s[40:43], s61 offen offset:1024 lds\n\ts_add_u32 m0, s65, 38912\n\ts_cmp_eq_u32 s68, s17\n\tbuffer_load_dwordx4 v36, s[36:39], s60 offen lds\n\tbuffer_load_dwordx4 v37, s[36:39], s60 offen offset:1024 lds\n\t"
            "buffer_load_dwordx4 v38, s[36:39], s60 offen offset:2048 lds\n\ts_mov_b32 exec_hi, 0\n\tbuffer_load_dwordx4 v39, s[36:39], s60 offen offset:3072 lds\n\ts_mov_b32 exec_hi, -1\n\ts_cselect_b32 s60, s63, s68\n\ts_waitcnt vmcnt(11)\n\t"
            "s_barrier\n\ts_add_u32 m0, s66, 77824\n\ts_mul_i32 s61, s60, 1920\n\ts_add_u32 s61, s61, s14\n\tbuffer_load_dwordx4 v40, s[40:43], s61 offen lds\n\tbuffer_load_dwordx4 v41, s[40:43], s61 offen offset:1024 lds\n\t"
            "buffer_load_dwordx4 v42, s[40:43], s61 offen offset:2048 lds\n\tbuffer_load_dwordx4 v43, s[40:43], s61 offen offset:3072 lds\n\ts_addk_i32 m0, 0x1000\n\ts_add_u32 s68, s60, 128\n\tbuffer_load_dwordx4 v44, s[40:43], s61 offen lds\n\tbuffer_load_dwordx4 v45, s[40:43], s61 offen offset:1024 lds\n\t"
            "s_add_u32 m0, s65, 77824\n\ts_cmp_eq_u32 s68, s17\n\tbuffer_load_dwordx4 v36, s[36:39], s60 offen lds\n\tbuffer_load_dwordx4 v37, s[36:39], s60 offen offset:1024 lds\n\tbuffer_load_dwordx4 v38, s[36:39], s60 offen offset:2048 lds\n\ts_mov_b32 exec_hi, 0\n\t"
            "buffer_load_dwordx4 v39, s[36:39], s60 offen offset:3072 lds\n\ts_mov_b32 exec_hi, -1\n\ts_cselect_b32 s60, s63, s68\n\ts_waitcnt vmcnt(10)\n\ts_barrier\n\ts_cmp_lg_u32 s56, 3\n\t"
            "s_cbranch_scc1 1f\n\tbuffer_load_dwordx4 v[106:109], v158, s[44:47], 0 offen sc1\n\tbuffer_load_dwordx4 v[110:113], v159, s[44:47], 0 offen sc1\n\tbuffer_load_dwordx4 v[114:117], v104, s[44:47], 0 offen sc1\n\tbuffer_load_dword v118, v96, s[44:47], 0 offen sc1\n\t1:\n"
            "s_add_u32 m0, s66, 0\n\ts_mul_i32 s61, s60, 1920\n\ts_add_u32 s61, s61, s14\n\tbuffer_load_dwordx4 v40, s[40:43], s61 offen lds\n\tbuffer_load_dwordx4 v41, s[40:43], s61 offen offset:1024 lds\n\tbuffer_load_dwordx4 v42, s[40:43], s61 offen offset:2048 lds\n\t"
            "buffer_load_dwordx4 v43, s[40:43], s61 offen offset:3072 lds\n\ts_addk_i32 m0, 0x1000\n\ts_add_u32 s68, s60, 128\n\tbuffer_load_dwordx4 v44, s[40:43], s61 offen lds\n\tbuffer_load_dwordx4 v45, s[40:43], s61 offen offset:1024 lds\n\ts_add_u32 m0, s65, 0\n\t"
            "s_cmp_eq_u32 s68, s17\n\tbuffer_load_dwordx4 v36, s[36:39], s60 offen lds\n\tbuffer_load_dwordx4 v37, s[36:39], s60 offen offset:1024 lds\n\tbuffer_load_dwordx4 v38, s[36:39], s60 offen offset:2048 lds\n\ts_mov_b32 exec_hi, 0\n\tbuffer_load_dwordx4 v39, s[36:39], s60 offen offset:3072 lds\n\t"
            "s_mov_b32 exec_hi, -1\n\ts_cselect_b32 s60, s63, s68\n\ts_waitcnt vmcnt(10)\n\ts_cmp_lg_u32 s56, 3\n\ts_cbranch_scc1 2f\n\ts_mov_b32 s68, 0x40000\n\t"
            "v_xor_b32 v106, s67, v107\n\tv_xor_b32 v108, s67, v109\n\tv_xor_b32 v110, s67, v111\n\tv_xor_b32 v112, s67, v113\n\tv_xor_b32 v114, s67, v115\n\tv_xor_b32 v116, s67, v117\n\t"
            "v_xor_b32 v118, s67, v118\n\tv_or3_b32 v106, v106, v108, v110\n\tv_or3_b32 v118, v118, v114, v116\n\tv_or3_b32 v106, v106, v112, v118\n\tv_cmp_ne_u32 vcc, 0, v106\n\ts_cbranch_vccnz 30f\n\t"
            "31:\n2:\ns_waitcnt vmcnt(10)\n\ts_barrier\n\ts_add_u32 m0, s66, 38912\n\ts_mul_i32 s61, s60, 1920\n\t"
            "s_add_u32 s61, s61, s14\n\tbuffer_load_dwordx4 v40, s[40:43], s61 offen lds\n\tbuffer_load_dwordx4 v41, s[40:43], s61 offen offset:1024 lds\n\tbuffer_load_dwordx4 v42, s[40:43], s61 offen offset:2048 lds\n\tbuffer_load_dwordx4 v43, s[40:43], s61 offen offset:3072 lds\n\ts_addk_i32 m0, 0x1000\n\t"
            "s_add_u32 s68, s60, 128\n\tbuffer_load_dwordx4 v44, s[40:43], s61 offen lds\n\tbuffer_load_dwordx4 v45, s[40:43], s61 offen offset:1024 lds\n\ts_add_u32 m0, s65, 38912\n\ts_cmp_eq_u32 s68, s17\n\tbuffer_load_dwordx4 v36, s[36:39], s60 offen lds\n\t"
            "buffer_load_dwordx4 v37, s[36:39], s60 offen offset:1024 lds\n\tbuffer_load_dwordx4 v38, s[36:39], s60 offen offset:2048 lds\n\ts_mov_b32 exec_hi, 0\n\tbuffer_load_dwordx4 v39, s[36:39], s60 offen offset:3072 lds\n\ts_mov_b32 exec_hi, -1\n\ts_cselect_b32 s60, s63, s68\n\t"
            "s_mov_b32 m0, s30\n\ts_nop 0\n\tbuffer_load_dword v97, s[44:47], 0 offen sc1 lds\n\ts_waitcnt vmcnt(11)\n\ts_barrier\n\ts_add_u32 m0, s15, 40960\n\t"
            "s_nop 0\n\tbuffer_load_dwordx4 v156, s[48:51], s20 offen lds\n\tbuffer_load_dwordx4 v156, s[48:51], s21 offen offset:1024 lds\n\tbuffer_load_dwordx4 v156, s[48:51], s22 offen offset:2048 lds\n\tbuffer_load_dwordx4 v156, s[48:51], s23 offen offset:3072 lds\n\ts_add_u32 m0, s15, 45056\n\t"
            "s_nop 0\n\tbuffer_load_dwordx4 v156, s[48:51], s24 offen lds\n\tbuffer_load_dwordx4 v156, s[48:51], s25 offen offset:1024 lds\n\tbuffer_load_dwordx4 v156, s[48:51], s26 offen offset:2048 lds\n\ts_waitcnt vmcnt(7)\n\ts_barrier\n\t"
            "s_add_u32 m0, s15, 45056\n\ts_nop 0\n\tbuffer_load_dwordx4 v156, s[48:51], s27 offen offset:3072 lds\n\ts_add_u32 m0, s15, 49152\n\ts_nop 0\n\tbuffer_load_dwordx4 v156, s[48:51], s28 offen lds\n\t"
            "buffer_load_dwordx4 v156, s[48:51], s29 offen offset:1024 lds\n\ts_add_u32 m0, s15, 0\n\ts_nop 0\n\tbuffer_load_dwordx4 v154, s[48:51], s20 offen lds\n\tbuffer_load_dwordx4 v154, s[48:51], s21 offen offset:1024 lds\n\tbuffer_load_dwordx4 v154, s[48:51], s22 offen offset:2048 lds\n\t"
            "buffer_load_dwordx4 v154, s[48:51], s23 offen offset:3072 lds\n\ts_add_u32 m0, s15, 4096\n\ts_nop 0\n\tbuffer_load_dwordx4 v154, s[48:51], s24 offen lds\n\tbuffer_load_dwordx4 v154, s[48:51], s25 offen offset:1024 lds\n\tbuffer_load_dwordx4 v154, s[48:51], s26 offen offset:2048 lds\n\t"
            "buffer_load_dwordx4 v154, s[48:51], s27 offen offset:3072 lds\n\ts_add_u32 m0, s15, 8192\n\ts_nop 0\n\tbuffer_load_dwordx4 v154, s[48:51], s28 offen lds\n\tbuffer_load_dwordx4 v154, s[48:51], s29 offen offset:1024 lds\n\ts_cmp_lg_u32 s56, 0\n\t"
            "s_cbranch_scc1 1f\n\ts_mov_b32 m0, 162624\n\ts_nop 0\n\tbuffer_load_dwordx4 v104, s[44:47], 0 offen sc1 lds\n\t1:\nbuffer_load_dwordx4 v[240:243], v3, s[36:39], 0 offen offset:0\n\t"
            "buffer_load_dwordx4 v[244:247], v11, s[36:39], 0 offen offset:0\n\tbuffer_load_dwordx4 v[248:251], v3, s[36:39], 0 offen offset:64\n\tbuffer_load_dwordx4 v[252:255], v11, s[36:39], 0 offen offset:64\n\tds_read_b32 v20, v9 offset:256\n\tds_read_b32 v21, v10 offset:832\n\tds_read2st64_b32 v[138:139], v153 offset1:1\n\t"
            "ds_read2st64_b32 v[176:177], v160 offset1:1\n\tds_read2st64_b32 v[178:179], v161 offset1:1\n\tds_read2st64_b32 v[180:181], v162 offset1:1\n\tds_read2st64_b32 v[182:183], v163 offset1:1\n\tds_read2st64_b32 v[184:185], v164 offset1:1\n\tds_read2st64_b32 v[186:187], v165 offset1:1\n\t"
            "ds_read2st64_b32 v[188:189], v166 offset1:1\n\tds_read2st64_b32 v[190:191], v167 offset1:1\n\ts_mov_b32 s68, 0x40000\n\ts_waitcnt lgkmcnt(8)\n\tv_mul_f32 v138, 0x3ea52b5c, v138\n\tv_mul_f32 v139, 0x3ea52b5c, v139\n\t"
            "s_waitcnt lgkmcnt(7)\n\tv_mul_f32 v192, v176, v138\n\tv_mul_f32 v200, v177, v139\n\ts_waitcnt lgkmcnt(6)\n\tv_mul_f32 v193, v178, v138\n\tv_mul_f32 v201, v179, v139\n\t"
            "s_waitcnt lgkmcnt(5)\n\tv_mul_f32 v194, v180, v138\n\tv_mul_f32 v202, v181, v139\n\ts_waitcnt lgkmcnt(4)\n\tv_mul_f32 v195, v182, v138\n\tv_mul_f32 v203, v183, v139\n\t"
            "s_waitcnt lgkmcnt(3)\n\tv_mul_f32 v196, v184, v138\n\tv_mul_f32 v204, v185, v139\n\ts_waitcnt lgkmcnt(2)\n\tv_mul_f32 v197, v186, v138\n\tv_mul_f32 v205, v187, v139\n\t"
            "s_waitcnt lgkmcnt(1)\n\tv_mul_f32 v198, v188, v138\n\tv_mul_f32 v206, v189, v139\n\ts_waitcnt lgkmcnt(0)\n\tv_mul_f32 v199, v190, v138\n\tv_mul_f32 v207, v191, v139\n\t"
            "s_barrier\n\t"
            SB_(1) PRIS
            "50:\nds_read2_b64 v[208:211], v24 offset0:0 offset1:16\n\tds_read2_b64 v[212:215], v24 offset0:32 offset1:48\n\tds_read2_b64 v[216:219], v26 offset0:0 offset1:16\n\tds_read2_b64 v[220:223], v26 offset0:32 offset1:48\n\ts_cmp_eq_u32 s16, 0\n\t"
            "s_cbranch_scc1 56f\n\t58:\ns_cbranch_scc0 1f\n\tbuffer_load_dword v232, v31, s[44:47], s31 offen offset:0 sc1\n\tbuffer_load_dword v233, v31, s[44:47], s31 offen offset:256 sc1\n\tbuffer_load_dword v234, v31, s[44:47], s31 offen offset:512 sc1\n\t"
            "buffer_load_dword v235, v31, s[44:47], s31 offen offset:768 sc1\n\t1:\ns_waitcnt lgkmcnt(1)\n\tv_add_f32 v208, v208, v216\n\tv_add_f32 v209, v209, v217\n\tv_add_f32 v210, v210, v218\n\t"
            "v_add_f32 v211, v211, v219\n\ts_waitcnt lgkmcnt(0)\n\tv_add_f32 v212, v212, v220\n\tv_add_f32 v213, v213, v221\n\tv_add_f32 v214, v214, v222\n\tv_add_f32 v215, v215, v223\n\t"
            "v_fma_f32 v208, v208, v192, v128\n\tv_fma_f32 v209, v209, v193, v129\n\tv_fma_f32 v210, v210, v194, v130\n\tv_fma_f32 v211, v211, v195, v131\n\tv_fma_f32 v212, v212, v196, v132\n\tv_fma_f32 v213, v213, v197, v133\n\t"
            "v_fma_f32 v214, v214, v198, v134\n\tv_fma_f32 v215, v215, v199, v135\n\tv_max3_f32 v136, v208, v209, v210\n\tv_max3_f32 v137, v211, v212, v213\n\tv_max3_f32 v136, v136, v214, v215\n\tv_max_f32 v136, v136, v137\n\t"
            "v_sub_f32 v208, v208, v136\n\tv_sub_f32 v209, v209, v136\n\tv_sub_f32 v210, v210, v136\n\tv_sub_f32 v211, v211, v136\n\tv_sub_f32 v212, v212, v136\n\tv_sub_f32 v213, v213, v136\n\t"
            "v_sub_f32 v214, v214, v136\n\tv_sub_f32 v215, v215, v136\n\tv_exp_f32 v208, v208\n\tv_exp_f32 v209, v209\n\tv_exp_f32 v210, v210\n\tv_exp_f32 v211, v211\n\t"
            "v_exp_f32 v212, v212\n\tv_exp_f32 v213, v213\n\tv_exp_f32 v214, v214\n\tv_exp_f32 v215, v215\n\tv_add_f32 v216, v208, v209\n\tv_add_f32 v217, v210, v211\n\t"
            "v_add_f32 v218, v212, v213\n\tv_add_f32 v219, v214, v215\n\tv_add_f32 v216, v216, v217\n\tv_add_f32 v218, v218, v219\n\tv_mul_f32 v208, v208, v176\n\tv_add_f32 v216, v216, v218\n\t"
            "v_mul_f32 v216, 0xbfb8aa3b, v216\n\tv_rcp_f32 v216, v216\n\tv_mul_f32 v209, v209, v178\n\tv_mul_f32 v210, v210, v180\n\tv_mul_f32 v211, v211, v182\n\tv_mul_f32 v212, v212, v184\n\t"
            "v_mul_f32 v213, v213, v186\n\tv_mul_f32 v214, v214, v188\n\tv_mul_f32 v215, v215, v190\n\tv_mul_f32 v208, v208, v216\n\tv_mul_f32 v209, v209, v216\n\tv_mul_f32 v210, v210, v216\n\t"
            "v_mul_f32 v211, v211, v216\n\tv_mul_f32 v212, v212, v216\n\tv_mul_f32 v213, v213, v216\n\tv_mul_f32 v214, v214, v216\n\tv_mul_f32 v215, v215, v216\n\tv_cvt_pk_bf16_f32 v220, v208, v209\n\t"
            "v_cvt_pk_bf16_f32 v221, v210, v211\n\tv_cvt_pk_bf16_f32 v222, v212, v213\n\tv_cvt_pk_bf16_f32 v223, v214, v215\n\tds_write_b128 v29, v[220:223] offset:0\n\tds_write_b32 v30, v142 offset:16\n\t"
            SP0 PRIW
            "51:\nds_read_b32 v136, v28\n\tds_read2_b64 v[208:211], v25 offset0:0 offset1:16\n\tds_read2_b64 v[212:215], v25 offset0:32 offset1:48\n\tds_read2_b64 v[216:219], v27 offset0:0 offset1:16\n\tds_read2_b64 v[220:223], v27 offset0:32 offset1:48\n\t"
            "s_waitcnt lgkmcnt(4)\n\tv_cmp_eq_u32 vcc, 0, v136\n\ts_cbranch_vccnz 53f\n\t55:\n"
            PRI1
            "s_cmp_eq_u32 s16, 0\n\ts_cbranch_scc1 57f\n\t59:\ns_waitcnt lgkmcnt(1)\n\tv_add_f32 v208, v208, v216\n\tv_add_f32 v209, v209, v217\n\t"
            "v_add_f32 v210, v210, v218\n\tv_add_f32 v211, v211, v219\n\ts_waitcnt lgkmcnt(0)\n\tv_add_f32 v212, v212, v220\n\tv_add_f32 v213, v213, v221\n\tv_add_f32 v214, v214, v222\n\t"
            "v_add_f32 v215, v215, v223\n\tv_mul_f32 v208, v208, v200\n\tv_mul_f32 v209, v209, v201\n\tv_mul_f32 v210, v210, v202\n\tv_mul_f32 v211, v211, v203\n\tv_mul_f32 v212, v212, v204\n\t"
            "v_mul_f32 v213, v213, v205\n\tv_mul_f32 v214, v214, v206\n\tv_mul_f32 v215, v215, v207\n\tv_max3_f32 v136, v208, v209, v210\n\tv_max3_f32 v137, v211, v212, v213\n\tv_max3_f32 v136, v136, v214, v215\n\t"
            "v_max_f32 v136, v136, v137\n\tv_sub_f32 v208, v208, v136\n\tv_sub_f32 v209, v209, v136\n\tv_sub_f32 v210, v210, v136\n\tv_sub_f32 v211, v211, v136\n\tv_sub_f32 v212, v212, v136\n\t"
            "v_sub_f32 v213, v213, v136\n\tv_sub_f32 v214, v214, v136\n\tv_sub_f32 v215, v215, v136\n\tv_exp_f32 v208, v208\n\tv_exp_f32 v209, v209\n\tv_exp_f32 v210, v210\n\t"
            "v_exp_f32 v211, v211\n\tv_exp_f32 v212, v212\n\tv_exp_f32 v213, v213\n\tv_exp_f32 v214, v214\n\tv_exp_f32 v215, v215\n\tv_add_f32 v216, v208, v209\n\t"
            "v_add_f32 v217, v210, v211\n\tv_add_f32 v218, v212, v213\n\tv_add_f32 v219, v214, v215\n\tv_add_f32 v216, v216, v217\n\tv_add_f32 v218, v218, v219\n\tv_mul_f32 v208, v208, v177\n\t"
            "v_add_f32 v216, v216, v218\n\tv_mul_f32 v216, 0xbfb8aa3b, v216\n\tv_rcp_f32 v216, v216\n\tv_mul_f32 v209, v209, v179\n\tv_mul_f32 v210, v210, v181\n\tv_mul_f32 v211, v211, v183\n\t"
            "v_mul_f32 v212, v212, v185\n\tv_mul_f32 v213, v213, v187\n\tv_mul_f32 v214, v214, v189\n\tv_mul_f32 v215, v215, v191\n\tv_mul_f32 v208, v208, v216\n\tv_mul_f32 v209, v209, v216\n\t"
            "v_mul_f32 v210, v210, v216\n\tv_mul_f32 v211, v211, v216\n\tv_mul_f32 v212, v212, v216\n\tv_mul_f32 v213, v213, v216\n\tv_mul_f32 v214, v214, v216\n\tv_mul_f32 v215, v215, v216\n\t"
            "v_cvt_pk_bf16_f32 v220, v208, v209\n\tv_cvt_pk_bf16_f32 v221, v210, v211\n\tv_cvt_pk_bf16_f32 v222, v212, v213\n\tv_cvt_pk_bf16_f32 v223, v214, v215\n\tds_write_b128 v29, v[220:223] offset:38912\n\tds_write_b32 v30, v30 offset:16\n\t"
            SB_(2) PRIG
            "s_waitcnt vmcnt(0)\n\tds_write_b32 v30, v30 offset:48\n\ts_branch .LOL\n\t30:\nbuffer_load_dwordx4 v[106:109], v158, s[44:47], 0 offen sc1\n\tbuffer_load_dwordx4 v[110:113], v159, s[44:47], 0 offen sc1\n\t"
            "buffer_load_dwordx4 v[114:117], v104, s[44:47], 0 offen sc1\n\tbuffer_load_dword v118, v96, s[44:47], 0 offen sc1\n\t"
            GINC
            "s_waitcnt vmcnt(3)\n\tv_xor_b32 v106, s67, v107\n\tv_xor_b32 v108, s67, v109\n\ts_waitcnt vmcnt(2)\n\tv_xor_b32 v110, s67, v111\n\tv_xor_b32 v112, s67, v113\n\t"
            "s_waitcnt vmcnt(1)\n\tv_xor_b32 v114, s67, v115\n\tv_xor_b32 v116, s67, v117\n\ts_waitcnt vmcnt(0)\n\tv_xor_b32 v118, s67, v118\n\tv_or3_b32 v106, v106, v108, v110\n\t"
            "v_or3_b32 v118, v118, v114, v116\n\tv_or3_b32 v106, v106, v112, v118\n\tv_cmp_ne_u32 vcc, 0, v106\n\ts_cbranch_vccz 31b\n\ts_sub_u32 s68, s68, 1\n\ts_cbranch_scc0 30b\n\t"
            "s_branch 31b\n\t53:\n"
            PSLP
            "s_sub_u32 s68, s68, 1\n\ts_cbranch_scc0 51b\n\ts_branch 55b\n\t56:\n"
            RSL0
            "buffer_load_dword v224, v31, s[44:47], 0 offen offset:0 sc1\n\tbuffer_load_dword v225, v31, s[44:47], 0 offen offset:256 sc1\n\tbuffer_load_dword v226, v31, s[44:47], 0 offen offset:512 sc1\n\tbuffer_load_dword v227, v31, s[44:47], 0 offen offset:768 sc1\n\ts_waitcnt vmcnt(4)\n\t"
            PRIR
            "s_call_b64 s[76:77], .LG0\n\t"
            PRIX
            "s_branch 2f\n\t1:\ns_waitcnt vmcnt(3)\n\tbuffer_load_dword v224, v31, s[44:47], 0 offen offset:0 sc1\n\ts_waitcnt vmcnt(3)\n\tbuffer_load_dword v225, v31, s[44:47], 0 offen offset:256 sc1\n\t"
            "s_waitcnt vmcnt(3)\n\tbuffer_load_dword v226, v31, s[44:47], 0 offen offset:512 sc1\n\ts_waitcnt vmcnt(3)\n\tbuffer_load_dword v227, v31, s[44:47], 0 offen offset:768 sc1\n\t2:\ns_waitcnt vmcnt(0) lgkmcnt(0)\n\t"
            "ds_write_b32 v30, v30 offset:48\n\tv_max3_u32 v136, v224, v225, v226\n\tv_max_u32 v136, v136, v227\n\tv_cmp_eq_u32 vcc, -1, v136\n\tv_lshlrev_b32 v216, 16, v224\n\tv_and_b32 v217, 0xffff0000, v224\n\t"
            "v_lshlrev_b32 v218, 16, v225\n\tv_and_b32 v219, 0xffff0000, v225\n\tv_lshlrev_b32 v220, 16, v226\n\tv_and_b32 v221, 0xffff0000, v226\n\tv_lshlrev_b32 v222, 16, v227\n\tv_and_b32 v223, 0xffff0000, v227\n\t"
            LINC
            "s_cmp_eq_u32 0, 0\n\ts_cbranch_vccz 58b\n\ts_sub_u32 s68, s68, 1\n\ts_cbranch_scc0 1b\n\ts_branch 58b\n\t57:\n"
            "s_branch 2f\n\t1:\nbuffer_load_dword v232, v31, s[44:47], s31 offen offset:0 sc1\n\tbuffer_load_dword v233, v31, s[44:47], s31 offen offset:256 sc1\n\tbuffer_load_dword v234, v31, s[44:47], s31 offen offset:512 sc1\n\tbuffer_load_dword v235, v31, s[44:47], s31 offen offset:768 sc1\n\t"
            "2:\ns_waitcnt vmcnt(0) lgkmcnt(0)\n\tv_max3_u32 v136, v232, v233, v234\n\tv_max_u32 v136, v136, v235\n\tv_cmp_eq_u32 vcc, -1, v136\n\tv_lshlrev_b32 v216, 16, v232\n\t"
            "v_and_b32 v217, 0xffff0000, v232\n\tv_lshlrev_b32 v218, 16, v233\n\tv_and_b32 v219, 0xffff0000, v233\n\tv_lshlrev_b32 v220, 16, v234\n\tv_and_b32 v221, 0xffff0000, v234\n\tv_lshlrev_b32 v222, 16, v235\n\t"
            "v_and_b32 v223, 0xffff0000, v235\n\t"
            LINC
            "s_cmp_eq_u32 0, 0\n\ts_cbranch_vccz 59b\n\ts_sub_u32 s68, s68, 1\n\ts_cbranch_scc0 1b\n\ts_branch 59b\n\t.LOL:\n"
            "s_mov_b32 s68, 0x40000\n\ts_cmp_eq_u32 s16, 0\n\ts_cbranch_scc1 3f\n\ts_call_b64 s[76:77], .LG0\n\t3:\nds_read_b64 v[14:15], v8 offset:32\n\t"
            "s_waitcnt lgkmcnt(0)\n\tv_min_u32 v12, v14, v15\n\tv_cmp_lt_u32 vcc, 1, v12\n\ts_cbranch_vccnz 4f\n\ts_sleep 1\n\ts_sub_u32 s68, s68, 1\n\t"
            "s_cbranch_scc0 3b\n\t4:\ns_cmp_eq_u32 s16, 0\n\ts_cbranch_scc1 1f\n\ts_call_b64 s[76:77], .LG1\n\t1:\n"
            SO(4) RZ
            "buffer_load_dwordx4 v[176:179], v2, s[44:47], s4 offen offset:1024 sc1\n\tbuffer_load_dwordx4 v[180:183], v2, s[44:47], s4 offen offset:2048 sc1\n\tbuffer_load_dwordx4 v[184:187], v2, s[44:47], s5 offen offset:1024 sc1\n\tbuffer_load_dwordx4 v[188:191], v2, s[44:47], s5 offen offset:2048 sc1\n\tbuffer_load_dwordx4 v[192:195], v2, s[44:47], s6 offen offset:1024 sc1\n\tbuffer_load_dwordx4 v[196:199], v2, s[44:47], s6 offen offset:2048 sc1\n\t"
            "s_cbranch_scc0 1f\n\ts_call_b64 s[76:77], .LG1\n\t1:\nv_rcp_f32 v36, v36\n\tv_rcp_f32 v37, v37\n\tv_rcp_f32 v38, v38\n\t"
            "v_rcp_f32 v39, v39\n\tv_rcp_f32 v40, v40\n\tv_rcp_f32 v41, v41\n\tv_rcp_f32 v42, v42\n\tv_rcp_f32 v43, v43\n\tv_rcp_f32 v44, v44\n\t"
            "v_rcp_f32 v45, v45\n\tv_rcp_f32 v46, v46\n\tv_rcp_f32 v47, v47\n\tv_rcp_f32 v48, v48\n\tv_rcp_f32 v49, v49\n\tv_rcp_f32 v50, v50\n\t"
            "v_rcp_f32 v51, v51\n\tv_rcp_f32 v52, v52\n\tv_rcp_f32 v53, v53\n\tv_rcp_f32 v54, v54\n\tv_rcp_f32 v55, v55\n\tv_rcp_f32 v56, v56\n\t"
            "v_rcp_f32 v57, v57\n\tv_rcp_f32 v58, v58\n\tv_rcp_f32 v59, v59\n\tv_rcp_f32 v60, v60\n\tv_rcp_f32 v61, v61\n\tv_rcp_f32 v62, v62\n\t"
            "v_rcp_f32 v63, v63\n\tv_mul_f32 v32, v32, v96\n\tv_mul_f32 v33, v33, v97\n\tv_mul_f32 v34, v34, v98\n\tv_mul_f32 v35, v35, v99\n\tv_mul_f32 v36, v36, v100\n\t"
            "v_mul_f32 v37, v37, v101\n\tv_mul_f32 v38, v38, v102\n\tv_mul_f32 v39, v39, v103\n\tv_mul_f32 v40, v40, v104\n\tv_mul_f32 v41, v41, v105\n\tv_mul_f32 v42, v42, v106\n\t"
            "v_mul_f32 v43, v43, v107\n\tv_mul_f32 v44, v44, v108\n\tv_mul_f32 v45, v45, v109\n\tv_mul_f32 v46, v46, v110\n\tv_mul_f32 v47, v47, v111\n\tbuffer_load_dwordx4 v[200:203], v2, s[44:47], s7 offen offset:1024 sc1\n\t"
            "buffer_load_dwordx4 v[204:207], v2, s[44:47], s7 offen offset:2048 sc1\n\tbuffer_load_dwordx4 v[208:211], v2, s[44:47], s8 offen offset:1024 sc1\n\tbuffer_load_dwordx4 v[212:215], v2, s[44:47], s8 offen offset:2048 sc1\n\tds_read_b128 v[16:19], v8 offset:48\n\tds_read_b64_tr_b16 v[128:129], v4 offset:40960\n\tds_read_b64_tr_b16 v[130:131], v4 offset:41472\n\t"
            "ds_read_b64_tr_b16 v[132:133], v5 offset:40960\n\tds_read_b64_tr_b16 v[134:135], v5 offset:41472\n\tds_read_b64_tr_b16 v[136:137], v6 offset:40960\n\tds_read_b64_tr_b16 v[138:139], v6 offset:41472\n\tds_read_b64_tr_b16 v[140:141], v7 offset:40960\n\tds_read_b64_tr_b16 v[142:143], v7 offset:41472\n\t"
            "ds_read_b64_tr_b16 v[144:145], v4 offset:41984\n\tds_read_b64_tr_b16 v[146:147], v4 offset:42496\n\tds_read_b64_tr_b16 v[148:149], v5 offset:41984\n\tds_read_b64_tr_b16 v[150:151], v5 offset:42496\n\tds_read_b64_tr_b16 v[152:153], v6 offset:41984\n\tds_read_b64_tr_b16 v[154:155], v6 offset:42496\n\t"
            "ds_read_b64_tr_b16 v[156:157], v7 offset:41984\n\tds_read_b64_tr_b16 v[158:159], v7 offset:42496\n\tv_mul_f32 v48, v48, v112\n\tv_mul_f32 v49, v49, v113\n\tv_mul_f32 v50, v50, v114\n\tv_mul_f32 v51, v51, v115\n\t"
            "v_mul_f32 v52, v52, v116\n\tv_mul_f32 v53, v53, v117\n\tv_mul_f32 v54, v54, v118\n\tv_mul_f32 v55, v55, v119\n\tv_mul_f32 v56, v56, v120\n\tv_mul_f32 v57, v57, v121\n\t"
            "v_mul_f32 v58, v58, v122\n\tv_mul_f32 v59, v59, v123\n\tv_mul_f32 v60, v60, v124\n\tv_mul_f32 v61, v61, v125\n\tv_mul_f32 v62, v62, v126\n\tv_mul_f32 v63, v63, v127\n\t"
            SO(3)
            "s_waitcnt lgkmcnt(15)\n\tv_min3_u32 v16, v16, v17, v18\n\tv_min_u32 v16, v16, v19\n\tv_cmp_eq_u32 vcc, 0, v16\n\ts_cbranch_vccnz 62f\n\t63:\n"
            PRIO
            "s_waitcnt vmcnt(8)\n\tv_max3_u32 v12, v176, v178, v180\n\tv_max_u32 v12, v12, v182\n\tv_cmp_eq_u32 vcc, -1, v12\n\ts_cbranch_vccnz 100f\n\t200:\n"
            SO(5)
            "s_waitcnt lgkmcnt(8)\n\tv_mfma_f32_32x32x16_bf16 v[96:111], v[128:131], v[176:179], 0\n\tds_read_b64_tr_b16 v[160:161], v4 offset:43008\n\tds_read_b64_tr_b16 v[162:163], v4 offset:43520\n\tv_mfma_f32_32x32x16_bf16 v[112:127], v[132:135], v[176:179], 0\n\tds_read_b64_tr_b16 v[164:165], v5 offset:43008\n\t"
            "ds_read_b64_tr_b16 v[166:167], v5 offset:43520\n\tbuffer_load_dwordx4 v[216:219], v2, s[44:47], s9 offen offset:1024 sc1\n\tv_mfma_f32_32x32x16_bf16 v[96:111], v[136:139], v[180:183], v[96:111]\n\tds_read_b64_tr_b16 v[168:169], v6 offset:43008\n\tds_read_b64_tr_b16 v[170:171], v6 offset:43520\n\tbuffer_load_dwordx4 v[220:223], v2, s[44:47], s9 offen offset:2048 sc1\n\t"
            "s_waitcnt vmcnt(8)\n\tv_max3_u32 v12, v184, v186, v188\n\tv_max_u32 v12, v12, v190\n\tv_cmp_eq_u32 vcc, -1, v12\n\tv_mfma_f32_32x32x16_bf16 v[112:127], v[140:143], v[180:183], v[112:127]\n\tds_read_b64_tr_b16 v[172:173], v7 offset:43008\n\t"
            "ds_read_b64_tr_b16 v[174:175], v7 offset:43520\n\ts_cbranch_vccnz 101f\n\t201:\ns_waitcnt lgkmcnt(8)\n\tv_mfma_f32_32x32x16_bf16 v[96:111], v[144:147], v[184:187], v[96:111]\n\tds_read_b64_tr_b16 v[128:129], v4 offset:44032\n\t"
            "ds_read_b64_tr_b16 v[130:131], v4 offset:44544\n\tv_mfma_f32_32x32x16_bf16 v[112:127], v[148:151], v[184:187], v[112:127]\n\tds_read_b64_tr_b16 v[132:133], v5 offset:44032\n\tds_read_b64_tr_b16 v[134:135], v5 offset:44544\n\tbuffer_load_dwordx4 v[224:227], v2, s[44:47], s10 offen offset:1024 sc1\n\tv_mfma_f32_32x32x16_bf16 v[96:111], v[152:155], v[188:191], v[96:111]\n\t"
            "ds_read_b64_tr_b16 v[136:137], v6 offset:44032\n\tds_read_b64_tr_b16 v[138:139], v6 offset:44544\n\tbuffer_load_dwordx4 v[228:231], v2, s[44:47], s10 offen offset:2048 sc1\n\ts_waitcnt vmcnt(8)\n\tv_max3_u32 v12, v192, v194, v196\n\tv_max_u32 v12, v12, v198\n\t"
            "v_cmp_eq_u32 vcc, -1, v12\n\tv_mfma_f32_32x32x16_bf16 v[112:127], v[156:159], v[188:191], v[112:127]\n\tds_read_b64_tr_b16 v[140:141], v7 offset:44032\n\tds_read_b64_tr_b16 v[142:143], v7 offset:44544\n\ts_cbranch_vccnz 102f\n\t202:\n"
            "s_waitcnt lgkmcnt(8)\n\tv_mfma_f32_32x32x16_bf16 v[96:111], v[160:163], v[192:195], v[96:111]\n\tds_read_b64_tr_b16 v[144:145], v4 offset:45056\n\tds_read_b64_tr_b16 v[146:147], v4 offset:45568\n\tv_mfma_f32_32x32x16_bf16 v[112:127], v[164:167], v[192:195], v[112:127]\n\tds_read_b64_tr_b16 v[148:149], v5 offset:45056\n\t"
            "ds_read_b64_tr_b16 v[150:151], v5 offset:45568\n\tbuffer_load_dwordx4 v[232:235], v2, s[44:47], s11 offen offset:1024 sc1\n\tv_mfma_f32_32x32x16_bf16 v[96:111], v[168:171], v[196:199], v[96:111]\n\tds_read_b64_tr_b16 v[152:153], v6 offset:45056\n\tds_read_b64_tr_b16 v[154:155], v6 offset:45568\n\tbuffer_load_dwordx4 v[236:239], v2, s[44:47], s11 offen offset:2048 sc1\n\t"
            "s_waitcnt vmcnt(8)\n\tv_max3_u32 v12, v200, v202, v204\n\tv_max_u32 v12, v12, v206\n\tv_cmp_eq_u32 vcc, -1, v12\n\tv_mfma_f32_32x32x16_bf16 v[112:127], v[172:175], v[196:199], v[112:127]\n\tds_read_b64_tr_b16 v[156:157], v7 offset:45056\n\t"
            "ds_read_b64_tr_b16 v[158:159], v7 offset:45568\n\ts_cbranch_vccnz 103f\n\t203:\ns_waitcnt lgkmcnt(8)\n\tv_mfma_f32_32x32x16_bf16 v[96:111], v[128:131], v[200:203], v[96:111]\n\tds_read_b64_tr_b16 v[160:161], v4 offset:46080\n\t"
            "ds_read_b64_tr_b16 v[162:163], v4 offset:46592\n\tv_mfma_f32_32x32x16_bf16 v[112:127], v[132:135], v[200:203], v[112:127]\n\tds_read_b64_tr_b16 v[164:165], v5 offset:46080\n\tds_read_b64_tr_b16 v[166:167], v5 offset:46592\n\tds_read_b64 v[14:15], v8 offset:40\n\tbuffer_load_dwordx4 v[176:179], v2, s[44:47], s12 offen offset:1024 sc1\n\t"
            "v_mfma_f32_32x32x16_bf16 v[96:111], v[136:139], v[204:207], v[96:111]\n\tds_read_b64_tr_b16 v[168:169], v6 offset:46080\n\tds_read_b64_tr_b16 v[170:171], v6 offset:46592\n\tbuffer_load_dwordx4 v[180:183], v2, s[44:47], s12 offen offset:2048 sc1\n\ts_waitcnt vmcnt(8)\n\tv_max3_u32 v12, v208, v210, v212\n\t"
            "v_max_u32 v12, v12, v214\n\tv_cmp_eq_u32 vcc, -1, v12\n\tv_mfma_f32_32x32x16_bf16 v[112:127], v[140:143], v[204:207], v[112:127]\n\tds_read_b64_tr_b16 v[172:173], v7 offset:46080\n\tds_read_b64_tr_b16 v[174:175], v7 offset:46592\n\ts_cbranch_vccnz 104f\n\t"
            "204:\ns_waitcnt lgkmcnt(9)\n\tv_mfma_f32_32x32x16_bf16 v[96:111], v[144:147], v[208:211], v[96:111]\n\tds_read_b64_tr_b16 v[128:129], v4 offset:47104\n\tds_read_b64_tr_b16 v[130:131], v4 offset:47616\n\tv_mfma_f32_32x32x16_bf16 v[112:127], v[148:151], v[208:211], v[112:127]\n\t"
            "ds_read_b64_tr_b16 v[132:133], v5 offset:47104\n\tds_read_b64_tr_b16 v[134:135], v5 offset:47616\n\tbuffer_load_dwordx4 v[184:187], v2, s[44:47], s13 offen offset:1024 sc1\n\tv_mfma_f32_32x32x16_bf16 v[96:111], v[152:155], v[212:215], v[96:111]\n\tds_read_b64_tr_b16 v[136:137], v6 offset:47104\n\tds_read_b64_tr_b16 v[138:139], v6 offset:47616\n\t"
            "buffer_load_dwordx4 v[188:191], v2, s[44:47], s13 offen offset:2048 sc1\n\ts_waitcnt vmcnt(8)\n\tv_max3_u32 v12, v216, v218, v220\n\tv_max_u32 v12, v12, v222\n\tv_cmp_eq_u32 vcc, -1, v12\n\tv_mfma_f32_32x32x16_bf16 v[112:127], v[156:159], v[212:215], v[112:127]\n\t"
            "ds_read_b64_tr_b16 v[140:141], v7 offset:47104\n\tds_read_b64_tr_b16 v[142:143], v7 offset:47616\n\ts_cbranch_vccnz 105f\n\t205:\ns_waitcnt lgkmcnt(8)\n\tv_mfma_f32_32x32x16_bf16 v[96:111], v[160:163], v[216:219], v[96:111]\n\t"
            "v_min_u32 v14, v14, v15\n\tv_cmp_ge_u32 vcc, 1, v14\n\tds_read_b64_tr_b16 v[144:145], v4 offset:48128\n\tds_read_b64_tr_b16 v[146:147], v4 offset:48640\n\tv_mfma_f32_32x32x16_bf16 v[112:127], v[164:167], v[216:219], v[112:127]\n\ts_cbranch_vccnz 5f\n\t"
            "6:\nds_read_b64_tr_b16 v[148:149], v5 offset:48128\n\tds_read_b64_tr_b16 v[150:151], v5 offset:48640\n\tbuffer_load_dwordx4 v[192:195], v2, s[44:47], s4 offen offset:0 sc1\n\tv_mfma_f32_32x32x16_bf16 v[96:111], v[168:171], v[220:223], v[96:111]\n\tds_read_b64_tr_b16 v[152:153], v6 offset:48128\n\t"
            "ds_read_b64_tr_b16 v[154:155], v6 offset:48640\n\tbuffer_load_dwordx4 v[196:199], v2, s[44:47], s4 offen offset:3072 sc1\n\ts_waitcnt vmcnt(8)\n\tv_max3_u32 v12, v224, v226, v228\n\tv_max_u32 v12, v12, v230\n\tv_cmp_eq_u32 vcc, -1, v12\n\t"
            "v_mfma_f32_32x32x16_bf16 v[112:127], v[172:175], v[220:223], v[112:127]\n\tds_read_b64_tr_b16 v[156:157], v7 offset:48128\n\tds_read_b64_tr_b16 v[158:159], v7 offset:48640\n\ts_cbranch_vccnz 106f\n\t206:\ns_waitcnt lgkmcnt(8)\n\t"
            "v_mfma_f32_32x32x16_bf16 v[96:111], v[128:131], v[224:227], v[96:111]\n\tds_read_b64_tr_b16 v[160:161], v4 offset:49152\n\tds_read_b64_tr_b16 v[162:163], v4 offset:49664\n\tv_mfma_f32_32x32x16_bf16 v[112:127], v[132:135], v[224:227], v[112:127]\n\tds_read_b64_tr_b16 v[164:165], v5 offset:49152\n\tds_read_b64_tr_b16 v[166:167], v5 offset:49664\n\t"
            "buffer_load_dwordx4 v[200:203], v2, s[44:47], s5 offen offset:0 sc1\n\tv_mfma_f32_32x32x16_bf16 v[96:111], v[136:139], v[228:231], v[96:111]\n\tds_read_b64_tr_b16 v[168:169], v6 offset:49152\n\tds_read_b64_tr_b16 v[170:171], v6 offset:49664\n\tbuffer_load_dwordx4 v[204:207], v2, s[44:47], s5 offen offset:3072 sc1\n\ts_waitcnt vmcnt(8)\n\t"
            "v_max3_u32 v12, v232, v234, v236\n\tv_max_u32 v12, v12, v238\n\tv_cmp_eq_u32 vcc, -1, v12\n\tv_mfma_f32_32x32x16_bf16 v[112:127], v[140:143], v[228:231], v[112:127]\n\tds_read_b64_tr_b16 v[172:173], v7 offset:49152\n\tds_read_b64_tr_b16 v[174:175], v7 offset:49664\n\t"
            "s_cbranch_vccnz 107f\n\t207:\ns_waitcnt lgkmcnt(8)\n\tv_mfma_f32_32x32x16_bf16 v[96:111], v[144:147], v[232:235], v[96:111]\n\tds_read_b64_tr_b16 v[128:129], v4 offset:50176\n\tds_read_b64_tr_b16 v[130:131], v4 offset:50688\n\t"
            "v_mfma_f32_32x32x16_bf16 v[112:127], v[148:151], v[232:235], v[112:127]\n\tds_read_b64_tr_b16 v[132:133], v5 offset:50176\n\tds_read_b64_tr_b16 v[134:135], v5 offset:50688\n\tbuffer_load_dwordx4 v[208:211], v2, s[44:47], s6 offen offset:0 sc1\n\tv_mfma_f32_32x32x16_bf16 v[96:111], v[152:155], v[236:239], v[96:111]\n\tds_read_b64_tr_b16 v[136:137], v6 offset:50176\n\t"
            "ds_read_b64_tr_b16 v[138:139], v6 offset:50688\n\tbuffer_load_dwordx4 v[212:215], v2, s[44:47], s6 offen offset:3072 sc1\n\ts_waitcnt vmcnt(8)\n\tv_max3_u32 v12, v176, v178, v180\n\tv_max_u32 v12, v12, v182\n\tv_cmp_eq_u32 vcc, -1, v12\n\t"
            "v_mfma_f32_32x32x16_bf16 v[112:127], v[156:159], v[236:239], v[112:127]\n\tds_read_b64_tr_b16 v[140:141], v7 offset:50176\n\tds_read_b64_tr_b16 v[142:143], v7 offset:50688\n\ts_cbranch_vccnz 108f\n\t208:\ns_waitcnt lgkmcnt(8)\n\t"
            "v_mfma_f32_32x32x16_bf16 v[96:111], v[160:163], v[176:179], v[96:111]\n\tds_read_b64_tr_b16 v[144:145], v4 offset:0\n\tds_read_b64_tr_b16 v[146:147], v4 offset:512\n\tv_mfma_f32_32x32x16_bf16 v[112:127], v[164:167], v[176:179], v[112:127]\n\tds_read_b64_tr_b16 v[148:149], v5 offset:0\n\tds_read_b64_tr_b16 v[150:151], v5 offset:512\n\t"
            "buffer_load_dwordx4 v[216:219], v2, s[44:47], s7 offen offset:0 sc1\n\tv_mfma_f32_32x32x16_bf16 v[96:111], v[168:171], v[180:183], v[96:111]\n\tds_read_b64_tr_b16 v[152:153], v6 offset:0\n\tds_read_b64_tr_b16 v[154:155], v6 offset:512\n\tbuffer_load_dwordx4 v[220:223], v2, s[44:47], s7 offen offset:3072 sc1\n\ts_waitcnt vmcnt(8)\n\t"
            "v_max3_u32 v12, v184, v186, v188\n\tv_max_u32 v12, v12, v190\n\tv_cmp_eq_u32 vcc, -1, v12\n\tv_mfma_f32_32x32x16_bf16 v[112:127], v[172:175], v[180:183], v[112:127]\n\tds_read_b64_tr_b16 v[156:157], v7 offset:0\n\tds_read_b64_tr_b16 v[158:159], v7 offset:512\n\t"
            "s_cbranch_vccnz 109f\n\t209:\ns_waitcnt lgkmcnt(8)\n\tv_mfma_f32_32x32x16_bf16 v[96:111], v[128:131], v[184:187], v[96:111]\n\tds_read_b64_tr_b16 v[160:161], v4 offset:1024\n\tds_read_b64_tr_b16 v[162:163], v4 offset:1536\n\t"
            "v_mfma_f32_32x32x16_bf16 v[112:127], v[132:135], v[184:187], v[112:127]\n\tds_read_b64_tr_b16 v[164:165], v5 offset:1024\n\tds_read_b64_tr_b16 v[166:167], v5 offset:1536\n\tbuffer_load_dwordx4 v[224:227], v2, s[44:47], s8 offen offset:0 sc1\n\tv_mfma_f32_32x32x16_bf16 v[96:111], v[136:139], v[188:191], v[96:111]\n\tds_read_b64_tr_b16 v[168:169], v6 offset:1024\n\t"
            "ds_read_b64_tr_b16 v[170:171], v6 offset:1536\n\tbuffer_load_dwordx4 v[228:231], v2, s[44:47], s8 offen offset:3072 sc1\n\ts_waitcnt vmcnt(8)\n\tv_max3_u32 v12, v192, v194, v196\n\tv_max_u32 v12, v12, v198\n\tv_cmp_eq_u32 vcc, -1, v12\n\t"
            "v_mfma_f32_32x32x16_bf16 v[112:127], v[140:143], v[188:191], v[112:127]\n\tds_read_b64_tr_b16 v[172:173], v7 offset:1024\n\tds_read_b64_tr_b16 v[174:175], v7 offset:1536\n\ts_cbranch_vccnz 110f\n\t210:\ns_waitcnt lgkmcnt(8)\n\t"
            "v_mfma_f32_32x32x16_bf16 v[96:111], v[144:147], v[192:195], v[96:111]\n\tds_read_b64_tr_b16 v[128:129], v4 offset:2048\n\tds_read_b64_tr_b16 v[130:131], v4 offset:2560\n\tv_mfma_f32_32x32x16_bf16 v[112:127], v[148:151], v[192:195], v[112:127]\n\tds_read_b64_tr_b16 v[132:133], v5 offset:2048\n\tds_read_b64_tr_b16 v[134:135], v5 offset:2560\n\t"
            "buffer_load_dwordx4 v[232:235], v2, s[44:47], s9 offen offset:0 sc1\n\tv_mfma_f32_32x32x16_bf16 v[96:111], v[152:155], v[196:199], v[96:111]\n\tds_read_b64_tr_b16 v[136:137], v6 offset:2048\n\tds_read_b64_tr_b16 v[138:139], v6 offset:2560\n\tbuffer_load_dwordx4 v[236:239], v2, s[44:47], s9 offen offset:3072 sc1\n\ts_waitcnt vmcnt(8)\n\t"
            "v_max3_u32 v12, v200, v202, v204\n\tv_max_u32 v12, v12, v206\n\tv_cmp_eq_u32 vcc, -1, v12\n\tv_mfma_f32_32x32x16_bf16 v[112:127], v[156:159], v[196:199], v[112:127]\n\tds_read_b64_tr_b16 v[140:141], v7 offset:2048\n\tds_read_b64_tr_b16 v[142:143], v7 offset:2560\n\t"
            "s_cbranch_vccnz 111f\n\t211:\ns_waitcnt lgkmcnt(8)\n\tv_mfma_f32_32x32x16_bf16 v[96:111], v[160:163], v[200:203], v[96:111]\n\tds_read_b64_tr_b16 v[144:145], v4 offset:3072\n\tds_read_b64_tr_b16 v[146:147], v4 offset:3584\n\t"
            "v_mfma_f32_32x32x16_bf16 v[112:127], v[164:167], v[200:203], v[112:127]\n\tds_read_b64_tr_b16 v[148:149], v5 offset:3072\n\tds_read_b64_tr_b16 v[150:151], v5 offset:3584\n\tbuffer_load_dwordx4 v[176:179], v2, s[44:47], s10 offen offset:0 sc1\n\tv_mfma_f32_32x32x16_bf16 v[96:111], v[168:171], v[204:207], v[96:111]\n\tds_read_b64_tr_b16 v[152:153], v6 offset:3072\n\t"
            "ds_read_b64_tr_b16 v[154:155], v6 offset:3584\n\tbuffer_load_dwordx4 v[180:183], v2, s[44:47], s10 offen offset:3072 sc1\n\ts_waitcnt vmcnt(8)\n\tv_max3_u32 v12, v208, v210, v212\n\tv_max_u32 v12, v12, v214\n\tv_cmp_eq_u32 vcc, -1, v12\n\t"
            "v_mfma_f32_32x32x16_bf16 v[112:127], v[172:175], v[204:207], v[112:127]\n\tds_read_b64_tr_b16 v[156:157], v7 offset:3072\n\tds_read_b64_tr_b16 v[158:159], v7 offset:3584\n\ts_cbranch_vccnz 112f\n\t212:\ns_waitcnt lgkmcnt(8)\n\t"
            "v_mfma_f32_32x32x16_bf16 v[96:111], v[128:131], v[208:211], v[96:111]\n\tds_read_b64_tr_b16 v[160:161], v4 offset:4096\n\tds_read_b64_tr_b16 v[162:163], v4 offset:4608\n\tv_mfma_f32_32x32x16_bf16 v[112:127], v[132:135], v[208:211], v[112:127]\n\tds_read_b64_tr_b16 v[164:165], v5 offset:4096\n\tds_read_b64_tr_b16 v[166:167], v5 offset:4608\n\t"
            "buffer_load_dwordx4 v[184:187], v2, s[44:47], s11 offen offset:0 sc1\n\tv_mfma_f32_32x32x16_bf16 v[96:111], v[136:139], v[212:215], v[96:111]\n\tds_read_b64_tr_b16 v[168:169], v6 offset:4096\n\tds_read_b64_tr_b16 v[170:171], v6 offset:4608\n\tbuffer_load_dwordx4 v[188:191], v2, s[44:47], s11 offen offset:3072 sc1\n\ts_waitcnt vmcnt(8)\n\t"
            "v_max3_u32 v12, v216, v218, v220\n\tv_max_u32 v12, v12, v222\n\tv_cmp_eq_u32 vcc, -1, v12\n\tv_mfma_f32_32x32x16_bf16 v[112:127], v[140:143], v[212:215], v[112:127]\n\tds_read_b64_tr_b16 v[172:173], v7 offset:4096\n\tds_read_b64_tr_b16 v[174:175], v7 offset:4608\n\t"
            "s_cbranch_vccnz 113f\n\t213:\ns_waitcnt lgkmcnt(8)\n\tv_mfma_f32_32x32x16_bf16 v[96:111], v[144:147], v[216:219], v[96:111]\n\tds_read_b64_tr_b16 v[128:129], v4 offset:5120\n\tds_read_b64_tr_b16 v[130:131], v4 offset:5632\n\t"
            "v_mfma_f32_32x32x16_bf16 v[112:127], v[148:151], v[216:219], v[112:127]\n\tds_read_b64_tr_b16 v[132:133], v5 offset:5120\n\tds_read_b64_tr_b16 v[134:135], v5 offset:5632\n\tbuffer_load_dwordx4 v[192:195], v2, s[44:47], s12 offen offset:0 sc1\n\tv_mfma_f32_32x32x16_bf16 v[96:111], v[152:155], v[220:223], v[96:111]\n\tds_read_b64_tr_b16 v[136:137], v6 offset:5120\n\t"
            "ds_read_b64_tr_b16 v[138:139], v6 offset:5632\n\tbuffer_load_dwordx4 v[196:199], v2, s[44:47], s12 offen offset:3072 sc1\n\ts_waitcnt vmcnt(8)\n\tv_max3_u32 v12, v224, v226, v228\n\tv_max_u32 v12, v12, v230\n\tv_cmp_eq_u32 vcc, -1, v12\n\t"
            "v_mfma_f32_32x32x16_bf16 v[112:127], v[156:159], v[220:223], v[112:127]\n\tds_read_b64_tr_b16 v[140:141], v7 offset:5120\n\tds_read_b64_tr_b16 v[142:143], v7 offset:5632\n\ts_cbranch_vccnz 114f\n\t214:\ns_waitcnt lgkmcnt(8)\n\t"
            "v_mfma_f32_32x32x16_bf16 v[96:111], v[160:163], v[224:227], v[96:111]\n\tds_read_b64_tr_b16 v[144:145], v4 offset:6144\n\tds_read_b64_tr_b16 v[146:147], v4 offset:6656\n\tv_mfma_f32_32x32x16_bf16 v[112:127], v[164:167], v[224:227], v[112:127]\n\tds_read_b64_tr_b16 v[148:149], v5 offset:6144\n\tds_read_b64_tr_b16 v[150:151], v5 offset:6656\n\t"
            "buffer_load_dwordx4 v[200:203], v2, s[44:47], s13 offen offset:0 sc1\n\tv_mfma_f32_32x32x16_bf16 v[96:111], v[168:171], v[228:231], v[96:111]\n\tds_read_b64_tr_b16 v[152:153], v6 offset:6144\n\tds_read_b64_tr_b16 v[154:155], v6 offset:6656\n\tbuffer_load_dwordx4 v[204:207], v2, s[44:47], s13 offen offset:3072 sc1\n\ts_waitcnt vmcnt(8)\n\t"
            "v_max3_u32 v12, v232, v234, v236\n\tv_max_u32 v12, v12, v238\n\tv_cmp_eq_u32 vcc, -1, v12\n\tv_mfma_f32_32x32x16_bf16 v[112:127], v[172:175], v[228:231], v[112:127]\n\tds_read_b64_tr_b16 v[156:157], v7 offset:6144\n\tds_read_b64_tr_b16 v[158:159], v7 offset:6656\n\t"
            "s_cbranch_vccnz 115f\n\t215:\ns_waitcnt lgkmcnt(8)\n\tv_mfma_f32_32x32x16_bf16 v[96:111], v[128:131], v[232:235], v[96:111]\n\tds_read_b64_tr_b16 v[160:161], v4 offset:7168\n\tds_read_b64_tr_b16 v[162:163], v4 offset:7680\n\t"
            "v_mfma_f32_32x32x16_bf16 v[112:127], v[132:135], v[232:235], v[112:127]\n\tds_read_b64_tr_b16 v[164:165], v5 offset:7168\n\tds_read_b64_tr_b16 v[166:167], v5 offset:7680\n\tv_mfma_f32_32x32x16_bf16 v[96:111], v[136:139], v[236:239], v[96:111]\n\tds_read_b64_tr_b16 v[168:169], v6 offset:7168\n\tds_read_b64_tr_b16 v[170:171], v6 offset:7680\n\t"
            "s_waitcnt vmcnt(6)\n\tv_max3_u32 v12, v176, v178, v180\n\tv_max_u32 v12, v12, v182\n\tv_cmp_eq_u32 vcc, -1, v12\n\tv_mfma_f32_32x32x16_bf16 v[112:127], v[140:143], v[236:239], v[112:127]\n\tds_read_b64_tr_b16 v[172:173], v7 offset:7168\n\t"
            "ds_read_b64_tr_b16 v[174:175], v7 offset:7680\n\ts_cbranch_vccnz 116f\n\t216:\ns_waitcnt lgkmcnt(8)\n\tv_mfma_f32_32x32x16_bf16 v[96:111], v[144:147], v[176:179], v[96:111]\n\tds_read_b64_tr_b16 v[128:129], v4 offset:8192\n\t"
            "ds_read_b64_tr_b16 v[130:131], v4 offset:8704\n\tv_mfma_f32_32x32x16_bf16 v[112:127], v[148:151], v[176:179], v[112:127]\n\tds_read_b64_tr_b16 v[132:133], v5 offset:8192\n\tds_read_b64_tr_b16 v[134:135], v5 offset:8704\n\tv_mfma_f32_32x32x16_bf16 v[96:111], v[152:155], v[180:183], v[96:111]\n\tds_read_b64_tr_b16 v[136:137], v6 offset:8192\n\t"
            "ds_read_b64_tr_b16 v[138:139], v6 offset:8704\n\ts_waitcnt vmcnt(4)\n\tv_max3_u32 v12, v184, v186, v188\n\tv_max_u32 v12, v12, v190\n\tv_cmp_eq_u32 vcc, -1, v12\n\tv_mfma_f32_32x32x16_bf16 v[112:127], v[156:159], v[180:183], v[112:127]\n\t"
            "ds_read_b64_tr_b16 v[140:141], v7 offset:8192\n\tds_read_b64_tr_b16 v[142:143], v7 offset:8704\n\ts_cbranch_vccnz 117f\n\t217:\nds_read_b32 v16, v8 offset:64\n\tds_read_b32 v17, v9 offset:1344\n\t"
            "s_waitcnt lgkmcnt(10)\n\tv_mfma_f32_32x32x16_bf16 v[96:111], v[160:163], v[184:187], v[96:111]\n\tds_read_b64_tr_b16 v[144:145], v4 offset:9216\n\tds_read_b64_tr_b16 v[146:147], v4 offset:9728\n\tds_read_b64_tr_b16 v[148:149], v5 offset:9216\n\tds_read_b64_tr_b16 v[150:151], v5 offset:9728\n\t"
            "ds_read_b64_tr_b16 v[152:153], v6 offset:9216\n\tds_read_b64_tr_b16 v[154:155], v6 offset:9728\n\ts_waitcnt vmcnt(2)\n\tv_max3_u32 v12, v192, v194, v196\n\tv_max_u32 v12, v12, v198\n\tv_cmp_eq_u32 vcc, -1, v12\n\t"
            "v_mfma_f32_32x32x16_bf16 v[96:111], v[168:171], v[188:191], v[96:111]\n\tds_read_b64_tr_b16 v[156:157], v7 offset:9216\n\tds_read_b64_tr_b16 v[158:159], v7 offset:9728\n\ts_cbranch_vccnz 118f\n\t218:\ns_waitcnt lgkmcnt(10)\n\t"
            "v_mfma_f32_32x32x16_bf16 v[96:111], v[128:131], v[192:195], v[96:111]\n\ts_waitcnt vmcnt(0)\n\tv_max3_u32 v12, v200, v202, v204\n\tv_max_u32 v12, v12, v206\n\tv_cmp_eq_u32 vcc, -1, v12\n\tv_mfma_f32_32x32x16_bf16 v[96:111], v[136:139], v[196:199], v[96:111]\n\t"
            "s_cbranch_vccnz 119f\n\t219:\ns_waitcnt lgkmcnt(0)\n\tv_mfma_f32_32x32x16_bf16 v[96:111], v[144:147], v[200:203], v[96:111]\n\tv_cmp_ne_u32 s[74:75], 0, v17\n\tv_cmp_eq_u32 vcc, 0, v16\n\t"
            "v_mfma_f32_32x32x16_bf16 v[96:111], v[152:155], v[204:207], v[96:111]\n\ts_or_b64 vcc, vcc, s[74:75]\n\tv_mfma_f32_32x32x16_bf16 v[112:127], v[164:167], v[184:187], v[112:127]\n\ts_cbranch_scc1 7f\n\t8:\ns_nop 5\n\t"
            "v_mfma_f32_32x32x16_bf16 v[112:127], v[172:175], v[188:191], v[112:127]\n\ts_nop 1\n\tv_fmac_f32 v64, v96, v32\n\tv_fmac_f32 v65, v97, v33\n\tv_fmac_f32 v66, v98, v34\n\tv_fmac_f32 v67, v99, v35\n\t"
            "v_fmac_f32 v68, v100, v36\n\tv_fmac_f32 v69, v101, v37\n\tv_fmac_f32 v70, v102, v38\n\tv_mfma_f32_32x32x16_bf16 v[112:127], v[132:135], v[192:195], v[112:127]\n\tv_fmac_f32 v71, v103, v39\n\tv_fmac_f32 v72, v104, v40\n\t"
            "v_fmac_f32 v73, v105, v41\n\tv_fmac_f32 v74, v106, v42\n\tv_fmac_f32 v75, v107, v43\n\tv_fmac_f32 v76, v108, v44\n\tv_fmac_f32 v77, v109, v45\n\tv_mfma_f32_32x32x16_bf16 v[112:127], v[140:143], v[196:199], v[112:127]\n\t"
            "v_fmac_f32 v78, v110, v46\n\tv_fmac_f32 v79, v111, v47\n\tv_cvt_pk_bf16_f32 v240, v64, v65\n\tv_cvt_pk_bf16_f32 v242, v68, v69\n\tv_cvt_pk_bf16_f32 v241, v66, v67\n\tv_cvt_pk_bf16_f32 v243, v70, v71\n\t"
            "v_cvt_pk_bf16_f32 v244, v72, v73\n\tv_mfma_f32_32x32x16_bf16 v[112:127], v[148:151], v[200:203], v[112:127]\n\tv_cvt_pk_bf16_f32 v246, v76, v77\n\tv_cvt_pk_bf16_f32 v245, v74, v75\n\tv_cvt_pk_bf16_f32 v247, v78, v79\n\tv_permlane32_swap_b32 v240, v242\n\t"
            "v_permlane32_swap_b32 v241, v243\n\tv_permlane32_swap_b32 v244, v246\n\tv_permlane32_swap_b32 v245, v247\n\tv_mfma_f32_32x32x16_bf16 v[112:127], v[156:159], v[204:207], v[112:127]\n\tv_permlane16_swap_b32 v240, v244\n\tv_permlane16_swap_b32 v242, v246\n\t"
            "v_permlane16_swap_b32 v241, v245\n\tv_permlane16_swap_b32 v243, v247\n\tbuffer_store_dwordx4 v[240:243], v3, s[52:55], 0 offen offset:0\n\tbuffer_store_dwordx4 v[244:247], v11, s[52:55], 0 offen offset:0\n\t"
            SO(6)
            "s_nop 5\n\tv_fmac_f32 v80, v112, v48\n\tv_fmac_f32 v81, v113, v49\n\tv_fmac_f32 v82, v114, v50\n\tv_fmac_f32 v83, v115, v51\n\tv_fmac_f32 v84, v116, v52\n\t"
            "v_fmac_f32 v85, v117, v53\n\tv_fmac_f32 v86, v118, v54\n\tv_fmac_f32 v87, v119, v55\n\tv_fmac_f32 v88, v120, v56\n\tv_fmac_f32 v89, v121, v57\n\tv_fmac_f32 v90, v122, v58\n\t"
            "v_fmac_f32 v91, v123, v59\n\tv_fmac_f32 v92, v124, v60\n\tv_fmac_f32 v93, v125, v61\n\tv_fmac_f32 v94, v126, v62\n\tv_fmac_f32 v95, v127, v63\n\tv_cvt_pk_bf16_f32 v248, v80, v81\n\t"
            "v_cvt_pk_bf16_f32 v250, v84, v85\n\tv_cvt_pk_bf16_f32 v249, v82, v83\n\tv_cvt_pk_bf16_f32 v251, v86, v87\n\tv_cvt_pk_bf16_f32 v252, v88, v89\n\tv_cvt_pk_bf16_f32 v254, v92, v93\n\tv_cvt_pk_bf16_f32 v253, v90, v91\n\t"
            "v_cvt_pk_bf16_f32 v255, v94, v95\n\tv_permlane32_swap_b32 v248, v250\n\tv_permlane32_swap_b32 v249, v251\n\tv_permlane32_swap_b32 v252, v254\n\tv_permlane32_swap_b32 v253, v255\n\ts_nop 0\n\t"
            "v_permlane16_swap_b32 v248, v252\n\tv_permlane16_swap_b32 v250, v254\n\tv_permlane16_swap_b32 v249, v253\n\tv_permlane16_swap_b32 v251, v255\n\tbuffer_store_dwordx4 v[248:251], v3, s[52:55], 0 offen offset:64\n\tbuffer_store_dwordx4 v[252:255], v11, s[52:55], 0 offen offset:64\n\t"
            "9:\n"
            RCNT DUMP
            "s_endpgm\n\t.LG0:\nv_mul_f32 v21, 0xbfb8aa3b, v21\n\tv_mul_f32 v20, v20, v21\n\tv_permlane16_swap_b32 v240, v244\n\tv_permlane16_swap_b32 v241, v245\n\t"
            "v_permlane16_swap_b32 v242, v246\n\tv_permlane16_swap_b32 v243, v247\n\tv_permlane16_swap_b32 v248, v252\n\tv_permlane16_swap_b32 v249, v253\n\tv_permlane16_swap_b32 v250, v254\n\tv_permlane16_swap_b32 v251, v255\n\t"
            "v_permlane32_swap_b32 v240, v242\n\tv_permlane32_swap_b32 v241, v243\n\tv_permlane32_swap_b32 v244, v246\n\tv_permlane32_swap_b32 v245, v247\n\tv_permlane32_swap_b32 v248, v250\n\tv_permlane32_swap_b32 v249, v251\n\t"
            "v_permlane32_swap_b32 v252, v254\n\tv_permlane32_swap_b32 v253, v255\n\tv_lshlrev_b32 v64, 16, v240\n\tv_and_b32 v65, 0xffff0000, v240\n\tv_lshlrev_b32 v66, 16, v241\n\tv_and_b32 v67, 0xffff0000, v241\n\t"
            "v_lshlrev_b32 v68, 16, v242\n\tv_and_b32 v69, 0xffff0000, v242\n\tv_lshlrev_b32 v70, 16, v243\n\tv_and_b32 v71, 0xffff0000, v243\n\tv_lshlrev_b32 v72, 16, v244\n\tv_and_b32 v73, 0xffff0000, v244\n\t"
            "v_lshlrev_b32 v74, 16, v245\n\tv_and_b32 v75, 0xffff0000, v245\n\tv_lshlrev_b32 v76, 16, v246\n\tv_and_b32 v77, 0xffff0000, v246\n\tv_lshlrev_b32 v78, 16, v247\n\tv_and_b32 v79, 0xffff0000, v247\n\t"
            "v_lshlrev_b32 v80, 16, v248\n\tv_and_b32 v81, 0xffff0000, v248\n\tv_lshlrev_b32 v82, 16, v249\n\tv_and_b32 v83, 0xffff0000, v249\n\tv_lshlrev_b32 v84, 16, v250\n\tv_and_b32 v85, 0xffff0000, v250\n\t"
            "v_lshlrev_b32 v86, 16, v251\n\tv_and_b32 v87, 0xffff0000, v251\n\tv_lshlrev_b32 v88, 16, v252\n\tv_and_b32 v89, 0xffff0000, v252\n\tv_lshlrev_b32 v90, 16, v253\n\tv_and_b32 v91, 0xffff0000, v253\n\t"
            "v_lshlrev_b32 v92, 16, v254\n\tv_and_b32 v93, 0xffff0000, v254\n\tv_lshlrev_b32 v94, 16, v255\n\tv_and_b32 v95, 0xffff0000, v255\n\tv_fma_f32 v96, v64, v21, v20\n\tv_fma_f32 v97, v65, v21, v20\n\t"
            "v_fma_f32 v98, v66, v21, v20\n\tv_fma_f32 v99, v67, v21, v20\n\tv_fma_f32 v100, v68, v21, v20\n\tv_fma_f32 v101, v69, v21, v20\n\tv_fma_f32 v102, v70, v21, v20\n\tv_fma_f32 v103, v71, v21, v20\n\t"
            "v_fma_f32 v104, v72, v21, v20\n\tv_fma_f32 v105, v73, v21, v20\n\tv_fma_f32 v106, v74, v21, v20\n\tv_fma_f32 v107, v75, v21, v20\n\tv_fma_f32 v108, v76, v21, v20\n\tv_fma_f32 v109, v77, v21, v20\n\t"
            "v_fma_f32 v110, v78, v21, v20\n\tv_fma_f32 v111, v79, v21, v20\n\tv_fma_f32 v112, v80, v21, v20\n\tv_fma_f32 v113, v81, v21, v20\n\tv_fma_f32 v114, v82, v21, v20\n\tv_fma_f32 v115, v83, v21, v20\n\t"
            "v_fma_f32 v116, v84, v21, v20\n\tv_fma_f32 v117, v85, v21, v20\n\tv_fma_f32 v118, v86, v21, v20\n\tv_fma_f32 v119, v87, v21, v20\n\tv_fma_f32 v120, v88, v21, v20\n\tv_fma_f32 v121, v89, v21, v20\n\t"
            "v_fma_f32 v122, v90, v21, v20\n\tv_fma_f32 v123, v91, v21, v20\n\tv_fma_f32 v124, v92, v21, v20\n\tv_fma_f32 v125, v93, v21, v20\n\tv_fma_f32 v126, v94, v21, v20\n\tv_fma_f32 v127, v95, v21, v20\n\t"
            "s_setpc_b64 s[76:77]\n\t.LG1:\nv_exp_f32 v32, v96\n\tv_exp_f32 v33, v97\n\tv_exp_f32 v34, v98\n\tv_exp_f32 v35, v99\n\t"
            "v_exp_f32 v36, v100\n\tv_exp_f32 v37, v101\n\tv_exp_f32 v38, v102\n\tv_exp_f32 v39, v103\n\tv_exp_f32 v40, v104\n\tv_exp_f32 v41, v105\n\t"
            "v_exp_f32 v42, v106\n\tv_exp_f32 v43, v107\n\tv_exp_f32 v44, v108\n\tv_exp_f32 v45, v109\n\tv_exp_f32 v46, v110\n\tv_exp_f32 v47, v111\n\t"
            "v_exp_f32 v48, v112\n\tv_exp_f32 v49, v113\n\tv_exp_f32 v50, v114\n\tv_exp_f32 v51, v115\n\tv_exp_f32 v52, v116\n\tv_exp_f32 v53, v117\n\t"
            "v_exp_f32 v54, v118\n\tv_exp_f32 v55, v119\n\tv_exp_f32 v56, v120\n\tv_exp_f32 v57, v121\n\tv_exp_f32 v58, v122\n\tv_exp_f32 v59, v123\n\t"
            "v_exp_f32 v60, v124\n\tv_exp_f32 v61, v125\n\tv_exp_f32 v62, v126\n\tv_exp_f32 v63, v127\n\tv_add_f32 v32, 1.0, v32\n\tv_add_f32 v33, 1.0, v33\n\t"
            "v_add_f32 v34, 1.0, v34\n\tv_add_f32 v35, 1.0, v35\n\tv_add_f32 v36, 1.0, v36\n\tv_add_f32 v37, 1.0, v37\n\tv_add_f32 v38, 1.0, v38\n\tv_add_f32 v39, 1.0, v39\n\t"
            "v_add_f32 v40, 1.0, v40\n\tv_add_f32 v41, 1.0, v41\n\tv_add_f32 v42, 1.0, v42\n\tv_add_f32 v43, 1.0, v43\n\tv_add_f32 v44, 1.0, v44\n\tv_add_f32 v45, 1.0, v45\n\t"
            "v_add_f32 v46, 1.0, v46\n\tv_add_f32 v47, 1.0, v47\n\tv_add_f32 v48, 1.0, v48\n\tv_add_f32 v49, 1.0, v49\n\tv_add_f32 v50, 1.0, v50\n\tv_add_f32 v51, 1.0, v51\n\t"
            "v_add_f32 v52, 1.0, v52\n\tv_add_f32 v53, 1.0, v53\n\tv_add_f32 v54, 1.0, v54\n\tv_add_f32 v55, 1.0, v55\n\tv_add_f32 v56, 1.0, v56\n\tv_add_f32 v57, 1.0, v57\n\t"
            "v_add_f32 v58, 1.0, v58\n\tv_add_f32 v59, 1.0, v59\n\tv_add_f32 v60, 1.0, v60\n\tv_add_f32 v61, 1.0, v61\n\tv_add_f32 v62, 1.0, v62\n\tv_add_f32 v63, 1.0, v63\n\t"
            "v_rcp_f32 v32, v32\n\tv_rcp_f32 v33, v33\n\tv_rcp_f32 v34, v34\n\tv_rcp_f32 v35, v35\n\ts_setpc_b64 s[76:77]\n\t7:\n"
            "ds_read_b32 v16, v8 offset:64\n\tds_read_b32 v17, v9 offset:1344\n\ts_waitcnt lgkmcnt(0)\n\tv_cmp_ne_u32 s[74:75], 0, v17\n\tv_cmp_eq_u32 vcc, 0, v16\n\ts_cbranch_vccz 71f\n\t"
            "s_sleep 1\n\ts_sub_u32 s68, s68, 1\n\ts_cbranch_scc0 7b\n\ts_branch 9b\n\t71:\nv_lshrrev_b32 v12, 5, v2\n\t"
            "v_and_b32 v12, 16, v12\n\tv_add_u32 v12, v12, v17\n\ts_mov_b64 exec, s[74:75]\n\ts_cbranch_execz 72f\n\tds_read_b128 v[32:35], v12 offset:0\n\tds_read_b128 v[36:39], v12 offset:32\n\t"
            "ds_read_b128 v[40:43], v12 offset:64\n\tds_read_b128 v[44:47], v12 offset:96\n\tds_read_b128 v[48:51], v12 offset:128\n\tds_read_b128 v[52:55], v12 offset:160\n\tds_read_b128 v[56:59], v12 offset:192\n\tds_read_b128 v[60:63], v12 offset:224\n\t"
            "72:\ns_mov_b64 exec, -1\n\ts_waitcnt lgkmcnt(0)\n\ts_branch 8b\n\t5:\ns_sleep 1\n\t"
            "ds_read_b64 v[14:15], v8 offset:40\n\ts_waitcnt lgkmcnt(0)\n\tv_min_u32 v14, v14, v15\n\tv_cmp_ge_u32 vcc, 1, v14\n\ts_cbranch_vccz 61f\n\ts_sub_u32 s68, s68, 1\n\t"
            "s_cbranch_scc0 5b\n\ts_branch 9b\n\t61:\n"
            TRG2D
            "s_branch 6b\n\t62:\nds_read_b128 v[16:19], v8 offset:48\n\tds_read_b64_tr_b16 v[128:129], v4 offset:40960\n\tds_read_b64_tr_b16 v[130:131], v4 offset:41472\n\tds_read_b64_tr_b16 v[132:133], v5 offset:40960\n\t"
            "ds_read_b64_tr_b16 v[134:135], v5 offset:41472\n\tds_read_b64_tr_b16 v[136:137], v6 offset:40960\n\tds_read_b64_tr_b16 v[138:139], v6 offset:41472\n\tds_read_b64_tr_b16 v[140:141], v7 offset:40960\n\tds_read_b64_tr_b16 v[142:143], v7 offset:41472\n\tds_read_b64_tr_b16 v[144:145], v4 offset:41984\n\t"
            "ds_read_b64_tr_b16 v[146:147], v4 offset:42496\n\tds_read_b64_tr_b16 v[148:149], v5 offset:41984\n\tds_read_b64_tr_b16 v[150:151], v5 offset:42496\n\tds_read_b64_tr_b16 v[152:153], v6 offset:41984\n\tds_read_b64_tr_b16 v[154:155], v6 offset:42496\n\tds_read_b64_tr_b16 v[156:157], v7 offset:41984\n\t"
            "ds_read_b64_tr_b16 v[158:159], v7 offset:42496\n\ts_waitcnt lgkmcnt(15)\n\tv_min3_u32 v16, v16, v17, v18\n\tv_min_u32 v16, v16, v19\n\tv_cmp_eq_u32 vcc, 0, v16\n\ts_cbranch_vccz 63b\n\t"
            "s_sub_u32 s68, s68, 1\n\ts_cbranch_scc0 62b\n\ts_branch 9b\n\t100:\ns_mov_b32 s70, 4\n\ts_call_b64 s[72:73], 300f\n\t"
            "v_max3_u32 v12, v176, v178, v180\n\tv_max_u32 v12, v12, v182\n\tv_cmp_eq_u32 vcc, -1, v12\n\ts_cbranch_vccnz 100b\n\ts_branch 200b\n\t101:\n"
            "s_mov_b32 s70, 5\n\ts_call_b64 s[72:73], 301f\n\tv_max3_u32 v12, v184, v186, v188\n\tv_max_u32 v12, v12, v190\n\tv_cmp_eq_u32 vcc, -1, v12\n\ts_cbranch_vccnz 101b\n\t"
            "s_branch 201b\n\t102:\ns_mov_b32 s70, 6\n\ts_call_b64 s[72:73], 302f\n\tv_max3_u32 v12, v192, v194, v196\n\tv_max_u32 v12, v12, v198\n\t"
            "v_cmp_eq_u32 vcc, -1, v12\n\ts_cbranch_vccnz 102b\n\ts_branch 202b\n\t103:\ns_mov_b32 s70, 7\n\ts_call_b64 s[72:73], 303f\n\t"
            "v_max3_u32 v12, v200, v202, v204\n\tv_max_u32 v12, v12, v206\n\tv_cmp_eq_u32 vcc, -1, v12\n\ts_cbranch_vccnz 103b\n\ts_branch 203b\n\t104:\n"
            "s_mov_b32 s70, 8\n\ts_call_b64 s[72:73], 304f\n\tv_max3_u32 v12, v208, v210, v212\n\tv_max_u32 v12, v12, v214\n\tv_cmp_eq_u32 vcc, -1, v12\n\ts_cbranch_vccnz 104b\n\t"
            "s_branch 204b\n\t105:\ns_mov_b32 s70, 9\n\ts_call_b64 s[72:73], 305f\n\tv_max3_u32 v12, v216, v218, v220\n\tv_max_u32 v12, v12, v222\n\t"
            "v_cmp_eq_u32 vcc, -1, v12\n\ts_cbranch_vccnz 105b\n\ts_branch 205b\n\t106:\ns_mov_b32 s70, 10\n\ts_call_b64 s[72:73], 306f\n\t"
            "v_max3_u32 v12, v224, v226, v228\n\tv_max_u32 v12, v12, v230\n\tv_cmp_eq_u32 vcc, -1, v12\n\ts_cbranch_vccnz 106b\n\ts_branch 206b\n\t107:\n"
            "s_mov_b32 s70, 11\n\ts_call_b64 s[72:73], 307f\n\tv_max3_u32 v12, v232, v234, v236\n\tv_max_u32 v12, v12, v238\n\tv_cmp_eq_u32 vcc, -1, v12\n\ts_cbranch_vccnz 107b\n\t"
            "s_branch 207b\n\t108:\ns_mov_b32 s70, 12\n\ts_call_b64 s[72:73], 308f\n\tv_max3_u32 v12, v176, v178, v180\n\tv_max_u32 v12, v12, v182\n\t"
            "v_cmp_eq_u32 vcc, -1, v12\n\ts_cbranch_vccnz 108b\n\ts_branch 208b\n\t109:\ns_mov_b32 s70, 13\n\ts_call_b64 s[72:73], 309f\n\t"
            "v_max3_u32 v12, v184, v186, v188\n\tv_max_u32 v12, v12, v190\n\tv_cmp_eq_u32 vcc, -1, v12\n\ts_cbranch_vccnz 109b\n\ts_branch 209b\n\t110:\n"
            "s_mov_b32 s70, 14\n\ts_call_b64 s[72:73], 310f\n\tv_max3_u32 v12, v192, v194, v196\n\tv_max_u32 v12, v12, v198\n\tv_cmp_eq_u32 vcc, -1, v12\n\ts_cbranch_vccnz 110b\n\t"
            "s_branch 210b\n\t111:\ns_mov_b32 s70, 15\n\ts_call_b64 s[72:73], 311f\n\tv_max3_u32 v12, v200, v202, v204\n\tv_max_u32 v12, v12, v206\n\t"
            "v_cmp_eq_u32 vcc, -1, v12\n\ts_cbranch_vccnz 111b\n\ts_branch 211b\n\t112:\ns_mov_b32 s70, 16\n\ts_call_b64 s[72:73], 312f\n\t"
            "v_max3_u32 v12, v208, v210, v212\n\tv_max_u32 v12, v12, v214\n\tv_cmp_eq_u32 vcc, -1, v12\n\ts_cbranch_vccnz 112b\n\ts_branch 212b\n\t113:\n"
            "s_mov_b32 s70, 17\n\ts_call_b64 s[72:73], 313f\n\tv_max3_u32 v12, v216, v218, v220\n\tv_max_u32 v12, v12, v222\n\tv_cmp_eq_u32 vcc, -1, v12\n\ts_cbranch_vccnz 113b\n\t"
            "s_branch 213b\n\t114:\ns_mov_b32 s70, 18\n\ts_call_b64 s[72:73], 314f\n\tv_max3_u32 v12, v224, v226, v228\n\tv_max_u32 v12, v12, v230\n\t"
            "v_cmp_eq_u32 vcc, -1, v12\n\ts_cbranch_vccnz 114b\n\ts_branch 214b\n\t115:\ns_mov_b32 s70, 19\n\ts_call_b64 s[72:73], 315f\n\t"
            "v_max3_u32 v12, v232, v234, v236\n\tv_max_u32 v12, v12, v238\n\tv_cmp_eq_u32 vcc, -1, v12\n\ts_cbranch_vccnz 115b\n\ts_branch 215b\n\t116:\n"
            "s_mov_b32 s70, 19\n\ts_call_b64 s[72:73], 316f\n\tv_max3_u32 v12, v176, v178, v180\n\tv_max_u32 v12, v12, v182\n\tv_cmp_eq_u32 vcc, -1, v12\n\ts_cbranch_vccnz 116b\n\t"
            "s_branch 216b\n\t117:\ns_mov_b32 s70, 19\n\ts_call_b64 s[72:73], 317f\n\tv_max3_u32 v12, v184, v186, v188\n\tv_max_u32 v12, v12, v190\n\t"
            "v_cmp_eq_u32 vcc, -1, v12\n\ts_cbranch_vccnz 117b\n\ts_branch 217b\n\t118:\ns_mov_b32 s70, 19\n\ts_call_b64 s[72:73], 318f\n\t"
            "v_max3_u32 v12, v192, v194, v196\n\tv_max_u32 v12, v12, v198\n\tv_cmp_eq_u32 vcc, -1, v12\n\ts_cbranch_vccnz 118b\n\ts_branch 218b\n\t119:\n"
            "s_mov_b32 s70, 19\n\ts_call_b64 s[72:73], 319f\n\tv_max3_u32 v12, v200, v202, v204\n\tv_max_u32 v12, v12, v206\n\tv_cmp_eq_u32 vcc, -1, v12\n\ts_cbranch_vccnz 119b\n\t"
            "s_branch 219b\n\t300:\ns_waitcnt vmcnt(0)\n\t400:\nv_max3_u32 v12, v176, v178, v180\n\tv_max_u32 v12, v12, v182\n\t"
            "v_cmp_eq_u32 vcc, -1, v12\n\ts_cbranch_vccz 11f\n\tbuffer_load_dwordx4 v[176:179], v2, s[44:47], s4 offen offset:1024 sc1\n\tbuffer_load_dwordx4 v[180:183], v2, s[44:47], s4 offen offset:2048 sc1\n\t11:\ns_cmp_eq_u32 s70, 0\n\t"
            "s_cbranch_scc0 401f\n\ts_branch 12f\n\t301:\ns_waitcnt vmcnt(0)\n\t401:\nv_max3_u32 v12, v184, v186, v188\n\t"
            "v_max_u32 v12, v12, v190\n\tv_cmp_eq_u32 vcc, -1, v12\n\ts_cbranch_vccz 11f\n\tbuffer_load_dwordx4 v[184:187], v2, s[44:47], s5 offen offset:1024 sc1\n\tbuffer_load_dwordx4 v[188:191], v2, s[44:47], s5 offen offset:2048 sc1\n\t11:\n"
            "s_cmp_eq_u32 s70, 1\n\ts_cbranch_scc0 402f\n\ts_branch 12f\n\t302:\ns_waitcnt vmcnt(0)\n\t402:\n"
            "v_max3_u32 v12, v192, v194, v196\n\tv_max_u32 v12, v12, v198\n\tv_cmp_eq_u32 vcc, -1, v12\n\ts_cbranch_vccz 11f\n\tbuffer_load_dwordx4 v[192:195], v2, s[44:47], s6 offen offset:1024 sc1\n\tbuffer_load_dwordx4 v[196:199], v2, s[44:47], s6 offen offset:2048 sc1\n\t"
            "11:\ns_cmp_eq_u32 s70, 2\n\ts_cbranch_scc0 403f\n\ts_branch 12f\n\t303:\ns_waitcnt vmcnt(0)\n\t"
            "403:\nv_max3_u32 v12, v200, v202, v204\n\tv_max_u32 v12, v12, v206\n\tv_cmp_eq_u32 vcc, -1, v12\n\ts_cbranch_vccz 11f\n\tbuffer_load_dwordx4 v[200:203], v2, s[44:47], s7 offen offset:1024 sc1\n\t"
            "buffer_load_dwordx4 v[204:207], v2, s[44:47], s7 offen offset:2048 sc1\n\t11:\ns_cmp_eq_u32 s70, 3\n\ts_cbranch_scc0 404f\n\ts_branch 12f\n\t304:\n"
            "s_waitcnt vmcnt(0)\n\t404:\nv_max3_u32 v12, v208, v210, v212\n\tv_max_u32 v12, v12, v214\n\tv_cmp_eq_u32 vcc, -1, v12\n\ts_cbranch_vccz 11f\n\t"
            "buffer_load_dwordx4 v[208:211], v2, s[44:47], s8 offen offset:1024 sc1\n\tbuffer_load_dwordx4 v[212:215], v2, s[44:47], s8 offen offset:2048 sc1\n\t11:\ns_cmp_eq_u32 s70, 4\n\ts_cbranch_scc0 405f\n\ts_branch 12f\n\t"
            "305:\ns_waitcnt vmcnt(0)\n\t405:\nv_max3_u32 v12, v216, v218, v220\n\tv_max_u32 v12, v12, v222\n\tv_cmp_eq_u32 vcc, -1, v12\n\t"
            "s_cbranch_vccz 11f\n\tbuffer_load_dwordx4 v[216:219], v2, s[44:47], s9 offen offset:1024 sc1\n\tbuffer_load_dwordx4 v[220:223], v2, s[44:47], s9 offen offset:2048 sc1\n\t11:\ns_cmp_eq_u32 s70, 5\n\ts_cbranch_scc0 406f\n\t"
            "s_branch 12f\n\t306:\ns_waitcnt vmcnt(0)\n\t406:\nv_max3_u32 v12, v224, v226, v228\n\tv_max_u32 v12, v12, v230\n\t"
            "v_cmp_eq_u32 vcc, -1, v12\n\ts_cbranch_vccz 11f\n\tbuffer_load_dwordx4 v[224:227], v2, s[44:47], s10 offen offset:1024 sc1\n\tbuffer_load_dwordx4 v[228:231], v2, s[44:47], s10 offen offset:2048 sc1\n\t11:\ns_cmp_eq_u32 s70, 6\n\t"
            "s_cbranch_scc0 407f\n\ts_branch 12f\n\t307:\ns_waitcnt vmcnt(0)\n\t407:\nv_max3_u32 v12, v232, v234, v236\n\t"
            "v_max_u32 v12, v12, v238\n\tv_cmp_eq_u32 vcc, -1, v12\n\ts_cbranch_vccz 11f\n\tbuffer_load_dwordx4 v[232:235], v2, s[44:47], s11 offen offset:1024 sc1\n\tbuffer_load_dwordx4 v[236:239], v2, s[44:47], s11 offen offset:2048 sc1\n\t11:\n"
            "s_cmp_eq_u32 s70, 7\n\ts_cbranch_scc0 408f\n\ts_branch 12f\n\t308:\ns_waitcnt vmcnt(0)\n\t408:\n"
            "v_max3_u32 v12, v176, v178, v180\n\tv_max_u32 v12, v12, v182\n\tv_cmp_eq_u32 vcc, -1, v12\n\ts_cbranch_vccz 11f\n\tbuffer_load_dwordx4 v[176:179], v2, s[44:47], s12 offen offset:1024 sc1\n\tbuffer_load_dwordx4 v[180:183], v2, s[44:47], s12 offen offset:2048 sc1\n\t"
            "11:\ns_cmp_eq_u32 s70, 8\n\ts_cbranch_scc0 409f\n\ts_branch 12f\n\t309:\ns_waitcnt vmcnt(0)\n\t"
            "409:\nv_max3_u32 v12, v184, v186, v188\n\tv_max_u32 v12, v12, v190\n\tv_cmp_eq_u32 vcc, -1, v12\n\ts_cbranch_vccz 11f\n\tbuffer_load_dwordx4 v[184:187], v2, s[44:47], s13 offen offset:1024 sc1\n\t"
            "buffer_load_dwordx4 v[188:191], v2, s[44:47], s13 offen offset:2048 sc1\n\t11:\ns_cmp_eq_u32 s70, 9\n\ts_cbranch_scc0 410f\n\ts_branch 12f\n\t310:\n"
            "s_waitcnt vmcnt(0)\n\t410:\nv_max3_u32 v12, v192, v194, v196\n\tv_max_u32 v12, v12, v198\n\tv_cmp_eq_u32 vcc, -1, v12\n\ts_cbranch_vccz 11f\n\t"
            "buffer_load_dwordx4 v[192:195], v2, s[44:47], s4 offen offset:0 sc1\n\tbuffer_load_dwordx4 v[196:199], v2, s[44:47], s4 offen offset:3072 sc1\n\t11:\ns_cmp_eq_u32 s70, 10\n\ts_cbranch_scc0 411f\n\ts_branch 12f\n\t"
            "311:\ns_waitcnt vmcnt(0)\n\t411:\nv_max3_u32 v12, v200, v202, v204\n\tv_max_u32 v12, v12, v206\n\tv_cmp_eq_u32 vcc, -1, v12\n\t"
            "s_cbranch_vccz 11f\n\tbuffer_load_dwordx4 v[200:203], v2, s[44:47], s5 offen offset:0 sc1\n\tbuffer_load_dwordx4 v[204:207], v2, s[44:47], s5 offen offset:3072 sc1\n\t11:\ns_cmp_eq_u32 s70, 11\n\ts_cbranch_scc0 412f\n\t"
            "s_branch 12f\n\t312:\ns_waitcnt vmcnt(0)\n\t412:\nv_max3_u32 v12, v208, v210, v212\n\tv_max_u32 v12, v12, v214\n\t"
            "v_cmp_eq_u32 vcc, -1, v12\n\ts_cbranch_vccz 11f\n\tbuffer_load_dwordx4 v[208:211], v2, s[44:47], s6 offen offset:0 sc1\n\tbuffer_load_dwordx4 v[212:215], v2, s[44:47], s6 offen offset:3072 sc1\n\t11:\ns_cmp_eq_u32 s70, 12\n\t"
            "s_cbranch_scc0 413f\n\ts_branch 12f\n\t313:\ns_waitcnt vmcnt(0)\n\t413:\nv_max3_u32 v12, v216, v218, v220\n\t"
            "v_max_u32 v12, v12, v222\n\tv_cmp_eq_u32 vcc, -1, v12\n\ts_cbranch_vccz 11f\n\tbuffer_load_dwordx4 v[216:219], v2, s[44:47], s7 offen offset:0 sc1\n\tbuffer_load_dwordx4 v[220:223], v2, s[44:47], s7 offen offset:3072 sc1\n\t11:\n"
            "s_cmp_eq_u32 s70, 13\n\ts_cbranch_scc0 414f\n\ts_branch 12f\n\t314:\ns_waitcnt vmcnt(0)\n\t414:\n"
            "v_max3_u32 v12, v224, v226, v228\n\tv_max_u32 v12, v12, v230\n\tv_cmp_eq_u32 vcc, -1, v12\n\ts_cbranch_vccz 11f\n\tbuffer_load_dwordx4 v[224:227], v2, s[44:47], s8 offen offset:0 sc1\n\tbuffer_load_dwordx4 v[228:231], v2, s[44:47], s8 offen offset:3072 sc1\n\t"
            "11:\ns_cmp_eq_u32 s70, 14\n\ts_cbranch_scc0 415f\n\ts_branch 12f\n\t315:\ns_waitcnt vmcnt(0)\n\t"
            "415:\nv_max3_u32 v12, v232, v234, v236\n\tv_max_u32 v12, v12, v238\n\tv_cmp_eq_u32 vcc, -1, v12\n\ts_cbranch_vccz 11f\n\tbuffer_load_dwordx4 v[232:235], v2, s[44:47], s9 offen offset:0 sc1\n\t"
            "buffer_load_dwordx4 v[236:239], v2, s[44:47], s9 offen offset:3072 sc1\n\t11:\ns_cmp_eq_u32 s70, 15\n\ts_cbranch_scc0 416f\n\ts_branch 12f\n\t316:\n"
            "s_waitcnt vmcnt(0)\n\t416:\nv_max3_u32 v12, v176, v178, v180\n\tv_max_u32 v12, v12, v182\n\tv_cmp_eq_u32 vcc, -1, v12\n\ts_cbranch_vccz 11f\n\t"
            "buffer_load_dwordx4 v[176:179], v2, s[44:47], s10 offen offset:0 sc1\n\tbuffer_load_dwordx4 v[180:183], v2, s[44:47], s10 offen offset:3072 sc1\n\t11:\ns_cmp_eq_u32 s70, 16\n\ts_cbranch_scc0 417f\n\ts_branch 12f\n\t"
            "317:\ns_waitcnt vmcnt(0)\n\t417:\nv_max3_u32 v12, v184, v186, v188\n\tv_max_u32 v12, v12, v190\n\tv_cmp_eq_u32 vcc, -1, v12\n\t"
            "s_cbranch_vccz 11f\n\tbuffer_load_dwordx4 v[184:187], v2, s[44:47], s11 offen offset:0 sc1\n\tbuffer_load_dwordx4 v[188:191], v2, s[44:47], s11 offen offset:3072 sc1\n\t11:\ns_cmp_eq_u32 s70, 17\n\ts_cbranch_scc0 418f\n\t"
            "s_branch 12f\n\t318:\ns_waitcnt vmcnt(0)\n\t418:\nv_max3_u32 v12, v192, v194, v196\n\tv_max_u32 v12, v12, v198\n\t"
            "v_cmp_eq_u32 vcc, -1, v12\n\ts_cbranch_vccz 11f\n\tbuffer_load_dwordx4 v[192:195], v2, s[44:47], s12 offen offset:0 sc1\n\tbuffer_load_dwordx4 v[196:199], v2, s[44:47], s12 offen offset:3072 sc1\n\t11:\ns_cmp_eq_u32 s70, 18\n\t"
            "s_cbranch_scc0 419f\n\ts_branch 12f\n\t319:\ns_waitcnt vmcnt(0)\n\t419:\nv_max3_u32 v12, v200, v202, v204\n\t"
            "v_max_u32 v12, v12, v206\n\tv_cmp_eq_u32 vcc, -1, v12\n\ts_cbranch_vccz 11f\n\tbuffer_load_dwordx4 v[200:203], v2, s[44:47], s13 offen offset:0 sc1\n\tbuffer_load_dwordx4 v[204:207], v2, s[44:47], s13 offen offset:3072 sc1\n\t11:\n"
            "12:\n"
            RINC
            "s_sub_u32 s68, s68, 1\n\ts_cbranch_scc1 9b\n\ts_waitcnt vmcnt(0)\n\ts_setpc_b64 s[72:73]\n\t.subsection 1\n\t.Lce:\n"
            ".subsection 0\n\t.Lout:\ns_waitcnt lgkmcnt(0)\n\t"
            : "=&{s67}"(tag), "=&{v[52:83]}"(sa), "=&{v[84:115]}"(sb), "=&{v[116:123]}"(sc) : [t] "v"(tid), [b] "s"(bid), [k] "s"(ka)
            : "memory", "scc", "vcc", "s4", "s5", "s6", "s7", "s8", "s9", "s10", "s11", "s12", "s13", "s14", "s15", "s16", "s17", "s18", "s19", "s20", "s21", "s22", "s23", "s24", "s25", "s26", "s27", "s28", "s29", "s30", "s31", "s32", "s33", "s34", "s35", "s36", "s37", "s38", "s39", "s40", "s41", "s42", "s43", "s44", "s45", "s46", "s47", "s48", "s49", "s50", "s51", "s52", "s53", "s54", "s55", "s56", "s57", "s58", "s59", "s60", "s61", "s62", "s63", "s64", "s65", "s66", "s68", "s69", "s70", "s71", "s72", "s73", "s74", "s75", "s76", "s77", "s78", "s79", "s80", "s81", "s82", "s83", "s84", "s85", "s86", "s87", "s88", "s89", "s90", "s91", "s92", "s93", "s94", "s95", "s96", "s97", "s98", "s99", "s100", "s101", "v2", "v3", "v4", "v5", "v6", "v7", "v8", "v9", "v10", "v11", "v12", "v13", "v14", "v15", "v16", "v17", "v18", "v19", "v20", "v21", "v22", "v23", "v24", "v25", "v26", "v27", "v28", "v29", "v30", "v31", "v32", "v33", "v34", "v35", "v36", "v37", "v38", "v39", "v40", "v41", "v42", "v43", "v44", "v45", "v46", "v47", "v48", "v49", "v50", "v51", "v124", "v125", "v126", "v127", "v128", "v129", "v130", "v131", "v132", "v133", "v134", "v135", "v136", "v137", "v138", "v139", "v140", "v141", "v142", "v143", "v144", "v145", "v146", "v147", "v148", "v149", "v150", "v151", "v152", "v153", "v154", "v155", "v156", "v157", "v158", "v159", "v160", "v161", "v162", "v163", "v164", "v165", "v166", "v167", "v168", "v169", "v170", "v171", "v172", "v173", "v174", "v175", "v176", "v177", "v178", "v179", "v180", "v181", "v182", "v183", "v184", "v185", "v186", "v187", "v188", "v189", "v190", "v191", "v192", "v193", "v194", "v195", "v196", "v197", "v198", "v199", "v200", "v201", "v202", "v203", "v204", "v205", "v206", "v207", "v208", "v209", "v210", "v211", "v212", "v213", "v214", "v215", "v216", "v217", "v218", "v219", "v220", "v221", "v222", "v223", "v224", "v225", "v226", "v227", "v228", "v229", "v230", "v231", "v232", "v233", "v234", "v235", "v236", "v237", "v238", "v239", "v240", "v241", "v242", "v243", "v244", "v245", "v246", "v247", "v248", "v249", "v250", "v251", "v252", "v253", "v254", "v255");
    tag = RFL(tag);
    asm volatile("" : "+s"(bid), "+s"(ka)); asm volatile("" : "+v"(tid));
    const u16 *x = (const u16*)ka[0], *wo = (const u16*)ka[2], *w_r = (const u16*)ka[3], *wg = (const u16*)ka[4], *wu = (const u16*)ka[5], *wd = (const u16*)ka[6]; char* ws = (char*)ka[7];
    const int wv = __builtin_amdgcn_readfirstlane(tid >> 6);
    const unsigned lds0 = (unsigned)(size_t)(lchar*)smem;
#define LANE() const int lane = lane_id(); const int g = lane >> 4, li = lane & 15, jq = 4 * (li & 3) + (li >> 2); (void)jq
    const int xcd = bid & 7, j = ((bid >> 3) + 25) & 31;
    const bool gemm = j < 25;
    const int fp = xcd >> 1, bl = (j * 13) >> 6, cl = j - bl * 5;
    const int rb = fp * 5 + bl, cg = (xcd & 1) * 5 + cl;
    const int par = fp >> 1, I0 = ((fp & 1) * 5 + bl) * 104 - 8;
    const __amdgpu_buffer_rsrc_t rs = __builtin_amdgcn_make_buffer_rsrc((void*)ws, 0, W_END, 0x00020000);
    auto wait_flag = [&](unsigned a) { SPIN { if (RFL(ldsrv<unsigned>(a))) break; __builtin_amdgcn_s_sleep(1); } MEMB(); };
    auto all4 = [&](u32x4 c) { return RFL(min(min(c.x, c.y), min(c.z, c.w))); };
    unsigned ep = 0;
    auto epld = [&]() { asm volatile("s_load_dword %0, %1, 0x0" : "=s"(ep) : "s"(ws) : "memory"); };
    auto mktag = [&]() { asm volatile("s_waitcnt lgkmcnt(0)" : "+s"(ep) :: "memory"); tag = ep * 1664525u + 1013904223u; };
        if (gemm) {
            LANE();
            wait_flag(lds0 + FL_W);
            const u32x4 kk = ldsrv<u32x4>(lds0 + L_KEY + lane * 16);
            const uint2 t2 = top2wave(lane < 52 ? kk.x : 0u, lane < 52 ? kk.z : 0u);
            _Pragma("nounroll") for (int k = 0; k < 2; ++k) {
                const unsigned key = k ? t2.y : t2.x, rr = (0xffffu - (key & 0xffffu)) & 2047u; const int pos = (int)(rr >> 1) - I0;
                if (__builtin_expect((int)(rr & 1) == par && pos >= 8 && pos < 112, 0)) {
                    const float sg = rbf(__builtin_amdgcn_rcpf(1.f + __builtin_amdgcn_exp2f(keyscore(key) * -1.4426950408889634f)));
                    const int m = (pos & ~15) + 4 * (pos & 3) + ((pos >> 2) & 3);
                    const float xr_ = lo2f(x[rr * 640 + cg * 64 + lane]), nm = ldsrv<float>(lds0 + L_NM + m * 4), rsj = ldsrv<float>(lds0 + L_RS + pos * 4);
                    u32x2 y[5];
                    SPIN {
                        MEMB();
                        bool old = false;
#pragma unroll
                        for (int i = 0; i < 5; ++i) y[i] = __builtin_amdgcn_raw_buffer_load_b64(rs, W_Y + (cg * 64 + lane) * 16 + k * 8, i * 10240, SC1);
#pragma unroll
                        for (int i = 0; i < 5; ++i) old = old || y[i].y != tag;
                        if (__builtin_amdgcn_ballot_w64(old) == 0) break;
                        __builtin_amdgcn_s_sleep(YS2);
                    }
                    const float ys = ((__uint_as_float(y[0].x) + __uint_as_float(y[1].x)) + (__uint_as_float(y[2].x) + __uint_as_float(y[3].x))) + __uint_as_float(y[4].x);
                    const float xn = rbf(rbf(xr_ + nm) * rsj), u = rbf(rbf(ys) * sg);
                    ldsw<float>(lds0 + L_RG + k * 256 + lane * 4, silu_b(rbf(xn + u)) * -1.4426950408889634f);
                    ldsw<unsigned>(lds0 + L_RM + m * 4, L_RG + k * 256);
                }
            }
            ldsw<unsigned>(lds0 + FL_R, 1u);
            END();
        }
    {
        TL(0);
        LANE();
        const int fq = (j - 25) * 2 + (xcd & 1), f = fp * 14 + fq;
        const int n0 = fq * 40 + wv * 4 + g;
        epld();
        if (wv == 7 && lane < 16) ldsw<u32x4>(lds0 + MISC + lane * 16, (u32x4){0u, 0u, 0u, 0u});
        const int u2 = max(f - 6, 0), kr = (u2 * 26) >> 8, fc = u2 - kr * 10, n1 = wv < 4 ? 3 : 2;
        const int d0 = kr * 11 + wv, d1 = kr * 11 + 4 + wv; const bool two = wv >= 4 && wv < 7 && d1 < 54;
        const char* w1 = (const char*)(f & 1 ? wu : wg) + min(f >> 1, 26) * 128; const char* w2 = (const char*)wd + fc * 128;
        unsigned fv[2][4];
#pragma unroll
        for (int i = 0; i < 4; ++i) { const unsigned c = (((lane & 7) ^ (2 * ((lane >> 4) & 1) + 4 * (i & 1))) * 16) - i * 1024; fv[0][i] = ((lane >> 3) + i * 8) * (F_ * 2) + c; fv[1][i] = ((lane >> 3) + i * 8) * 1280 + c; }
        auto issue = [&](int m, int ks, int slot) {
            asm volatile("s_mov_b32 m0, %[l]\n\ts_nop 0\n\tglobal_load_lds_dwordx4 %0, %[b]" FPOL "\n\tglobal_load_lds_dwordx4 %1, %[b] offset:1024" FPOL "\n\tglobal_load_lds_dwordx4 %2, %[b] offset:2048" FPOL "\n\tglobal_load_lds_dwordx4 %3, %[b] offset:3072" FPOL
                         :: "v"(fv[m][0]), "v"(fv[m][1]), "v"(fv[m][2]), "v"(fv[m][3]), [b] "s"((m ? w2 : w1) + (unsigned)(ks * 32 * (m ? 1280 : F_ * 2))), [l] "s"(lds0 + wv * 16384 + slot * 4096) : "memory");
        };
        if (TCH && wv == 5) {
            const int l = min(f * 14 + lane, 770), o = l < 280 ? W_KEY + l * 128 : l < 536 ? W_ST + (l - 280) * 128 : W_Y + (l - 536) * 256;
            if (lane < 14) asm volatile("s_mov_b32 m0, %[l]\n\ts_nop 0\n\tbuffer_load_dword %0, %1, 0 offen lds" :: "v"(o), "s"(rs), [l] "s"(lds0 + L_DUM) : "memory");
        }
        BARN();
        auto wopf = [&]() {
            const int i = min(((j - 25) * 8 + wv) * 58 + lane, 3199), rw = (i * 13108) >> 16;
            asm volatile("s_mov_b32 m0, %[l]\n\ts_nop 0\n\tglobal_load_lds_dword %0, %[b]" :: "v"(rw * 640 + i * 128 + (xcd & 1) * 640), [b] "s"(wo), [l] "s"(lds0 + L_DUM) : "memory"); };
        if (WPF == 1) wopf();
        if (EARLYK) issue(0, wv, 0);
        asm volatile("s_waitcnt vmcnt(%2)" : "+{v[84:115]}"(sb), "+{v[116:123]}"(sc) : "n"(5 + 4 * EARLYK + (WPF == 1)) : "memory");
#define SXR(i) ((i) < 20 ? sa[(12 + (i)) & 31] : (i) < 52 ? sb[((i) - 20) & 31] : sc[((i) - 52) & 7])
        mktag();
        {
            const unsigned one2 = 0x3F803F80u;
            float wsum = 0.f;
#pragma unroll
            for (int e = 0; e < 20; ++e) wsum = dot2(SXR(40 + e), one2, wsum);
            wsum = sum16(wsum);
            auto stat = [&](int p) {
                float s1 = 0.f, s2 = 0.f, sr = 0.f;
#pragma unroll
                for (int e = 0; e < 20; ++e) { const unsigned u = SXR(20 * p + e); s1 = dot2(u, one2, s1); s2 = dot2(u, u, s2); sr = dot2(u, SXR(40 + e), sr); }
                const float mean = sum16(s1) * (1.f / 640.f);
                const unsigned mb2 = cvtpk(mean, mean); const float mb = lo2f(mb2);
                const float q = fmaf(40.f * mb, mb, fmaf(-2.f * mb, s1, s2));
                const float dm = mean - mb;
                const float var = fmaxf(sum16(q) * (1.f / 640.f) - dm * dm, 0.f);
                const float sd = rbf(__builtin_amdgcn_sqrtf(rbf(rbf(var) + 1e-5f)));
                const float rsc = rbf(fmaf(-mb, wsum, sum16(sr)) / sd);
                const int n = n0 + 32 * p, i = (fp & 1) * 512 + n;
                if (li == 0) {
                    asm volatile("buffer_store_dwordx4 %0, %1, %2, 0 offen sc1\n\ts_nop 1" :: "v"((u32x4){(mb2 << 16) ^ 0x80000000u, tag, __float_as_uint(__builtin_amdgcn_rcpf(sd)), tag}), "v"(n < 512 ? W_ST + (par * 1024 + i) * 16 : 0x40000000), "s"(rs) : "memory");
                    ldsw<unsigned>(lds0 + LS_ROW + (p * 32 + wv * 4 + g) * 4, n < 512 ? rkey(rsc, 2 * i + par) : 0u);
                }
            };
            asm volatile("" : "+v"(wsum));
            if (wv < 2) stat(1);
            asm volatile("s_cmp_lt_u32 %2, 2\n\ts_cbranch_scc1 1f\n\ts_waitcnt vmcnt(%1)\n1:\n\ts_waitcnt vmcnt(%1 + 1)" : "+{v[52:83]}"(sa) : "n"(4 * EARLYK + (WPF == 1)), "s"(wv) : "memory", "scc");
            TL(1);
            if (EK2 && wv < EK2) issue(0, wv, 0);
            stat(0);
            if (E2 && wv < 7) issue(0, wv + 8, 1);
        }
        BAR();
        if (wv == 6) asm volatile("buffer_store_dword %0, %1, %2, 0 offen sc1" :: "v"(tag), "v"(lane ? 0x40000000 : W_ACK + (f < 40 ? (f >> 1) * 128 + 40 + (f & 1) * 4 : 2560 + (f - 40) * 4)), "s"(rs) : "memory");
        if (wv == 7) {
            const unsigned k = ldsrv<unsigned>(lds0 + LS_ROW + min(lane, 39) * 4);
            const uint2 t2 = top2wave(lane < 40 ? k : 0u, 0u);
            if (lane == 0 && fq < 13) asm volatile("buffer_store_dwordx4 %0, %1, %2, 0 offen sc1\n\ts_nop 1" :: "v"((u32x4){t2.x, tag, t2.y, tag}), "v"(W_KEY + (fp * 13 + fq) * 16), "s"(rs) : "memory");
        }
        BARN();
        TL(2);
        if (WPF == 2) wopf();
        if (!EARLYK && wv >= EK2) issue(0, wv, 0);
        if (!E2 || wv == 7) issue(0, wv + 8, 1);
        if (wv < 4) issue(0, wv + 16, 2);
        else if (!WPF) asm volatile("s_mov_b32 m0, %[l]\n\ts_mov_b32 exec_hi, 0\n\tglobal_load_lds_dword %0, %[b]\n\ts_mov_b32 exec_hi, -1" :: "v"(min(f * 128 + (wv - 4) * 32 + lane, 6399) * 128), [b] "s"(wo), [l] "s"(lds0 + L_DUM) : "memory");
        if (wv >= 4 && wv < 4 + P2A) issue(1, d0, 3);
        u32x4 kk;
        BARN();
        asm volatile("s_cmp_lg_u32 %3, 7\n\ts_cbranch_scc1 1f\n\ts_sleep " KSL "\n\tbuffer_load_dwordx4 %0, %1, %2, 0 offen sc1\n1:\n\ts_barrier" : "=&v"(kk) : "v"(W_KEY + min(lane, 51) * 16), "s"(rs), "s"(wv) : "memory", "scc");
        if (wv != 7 && !(wv >= 4 && wv < 4 + P2A)) issue(1, d0, 3);
        if (P2A && two) issue(1, d1, 2);
        asm volatile("s_cmp_lg_u32 %1, 7\n\ts_cbranch_scc1 1f\n\ts_waitcnt vmcnt(0)\n1:" : "+v"(kk) : "s"(wv) : "memory", "scc");
        if (wv == 7) {
            SPIN {
                if (__builtin_amdgcn_ballot_w64(kk.y != tag || kk.w != tag) == 0) break;
                asm volatile("buffer_load_dwordx4 %0, %1, %2, 0 offen sc1\n\ts_waitcnt vmcnt(0)" : "=&v"(kk) : "v"(W_KEY + min(lane, 51) * 16), "s"(rs) : "memory");
            }
            const uint2 t2 = top2wave(lane < 52 ? kk.x : 0u, lane < 52 ? kk.z : 0u);
            if (lane < 2) ldsw<unsigned>(lds0 + ROUT + lane * 8, (0xffffu - ((lane ? t2.y : t2.x) & 0xffffu)) & 2047u);
        }
        BAR();
        TL(3);
        const unsigned row = ldsrv<unsigned>(lds0 + ROUT + (li & 1) * 8);
        u32x4 af[3], st;
        asm volatile("global_load_dwordx4 %0, %4, %5\n\tglobal_load_dwordx4 %1, %4, %5 offset:512\n\tglobal_load_dwordx4 %2, %6, %5 offset:1024\n\tbuffer_load_dwordx4 %3, %7, %8, 0 offen sc1"
                     : "=&v"(af[0]), "=&v"(af[1]), "=&v"(af[2]), "=&v"(st) : "v"(row * 1280u + wv * 64 + g * 16), "s"(x), "v"(row * 1280u + (wv & 3) * 64 + g * 16), "v"(wv ? 0x40000000 : W_ST + ((row & 1) * 1024 + (row >> 1)) * 16), "s"(rs) : "memory");
        bf16x8 wf[3][4];
        unsigned ra[4];
#pragma unroll
        for (int q = 0; q < 4; ++q) ra[q] = lds0 + wv * 16384 + (unsigned)((8 * g + (li >> 2)) * 128 + (((q ^ (((li >> 3) & 1) + 2 * (g & 1))) * 32) + (li & 3) * 8));
        auto rdf = [&](int k, int slot) {
#pragma unroll
            for (int q = 0; q < 4; ++q) wf[k][q] = cat8(trread(ra[q] + slot * 4096), trread(ra[q] + slot * 4096 + 512));
        };
        if (wv == 7) issue(1, d0, 3);
        if (!P2A && two) issue(1, d1, 2);
        if (two) wvm<12>(); else wvm<8>();
        rdf(0, 0); rdf(1, 1); if (wv < 4) rdf(2, 2);
        asm volatile("s_cmp_eq_u32 %4, 0\n\ts_cbranch_scc0 1f\n\ts_waitcnt vmcnt(0)\n1:\n\ts_waitcnt vmcnt(4)" : "+v"(af[0]), "+v"(af[1]), "+v"(af[2]), "+v"(st) : "s"(RFL((!P2A && two) || wv == 7)) : "memory", "scc");
        f32x4 c[4];
#pragma unroll
        for (int q = 0; q < 4; ++q) c[q] = (f32x4){0.f, 0.f, 0.f, 0.f};
#pragma unroll
        for (int k = 0; k < 3; ++k) if (k < n1) {
#pragma unroll
            for (int e = 0; e < 4; ++e) af[k][e] = li & 2 ? 0x3F803F80u : af[k][e];
#pragma unroll
            for (int q = 0; q < 4; ++q) c[q] = mfma32(__builtin_bit_cast(bf16x8, af[k]), wf[k][q], c[q]);
        }
        auto total = [&](int off) {
            ldsw<f32x4>(lds0 + wv * 16384 + off + lane * 16, g == 0 ? c[0] : g == 1 ? c[1] : g == 2 ? c[2] : c[3]);
            BAR();
            if (wv) return c[0];
            f32x4 pr[8];
#pragma unroll
            for (int w = 0; w < 8; ++w) pr[w] = ldsrv<f32x4>(lds0 + w * 16384 + off + lane * 16);
            return ((pr[0] + pr[1]) + (pr[2] + pr[3])) + ((pr[4] + pr[5]) + (pr[6] + pr[7]));
        };
        TL(4);
        {
            const f32x4 s = total(0);
            if (wv == 0 && f < 54) {
                SPIN {
                    if (__builtin_amdgcn_ballot_w64(st.y != tag || st.w != tag) == 0) break;
                    asm volatile("buffer_load_dwordx4 %0, %1, %2, 0 offen sc1\n\ts_waitcnt vmcnt(0)" : "=&v"(st) : "v"(W_ST + ((row & 1) * 1024 + (row >> 1)) * 16), "s"(rs) : "memory");
                }
                const float n0_ = __uint_as_float(__builtin_amdgcn_readlane(st.x, 0)), n1_ = __uint_as_float(__builtin_amdgcn_readlane(st.x, 1)), i0 = __uint_as_float(__builtin_amdgcn_readlane(st.z, 0)), i1 = __uint_as_float(__builtin_amdgcn_readlane(st.z, 1));
                float v0 = rbf(fmaf(n0_, s.z, s.x) * i0), v1 = rbf(fmaf(n1_, s.z, s.y) * i1);
                if (!(f & 1)) { v0 = silu_b(v0); v1 = silu_b(v1); }
                const unsigned hb = cvtpk(v0, v1), nb = dppu<0xB1>(hb);
                const unsigned pay = (lane & 1) ? __builtin_amdgcn_perm(hb, nb, 0x07060302u) : __builtin_amdgcn_perm(nb, hb, 0x05040100u);
                asm volatile("buffer_store_dwordx2 %0, %1, %2, 0 offen sc1" :: "v"((u32x2){pay, tag}), "v"(W_H + ((f & 1) * 2 + (lane & 1)) * 6912 + ((f >> 1) * 32 + (lane >> 1)) * 8), "s"(rs) : "memory");
            }
        }
        TL(5);
        if (f == 0 && wv == 1) {
            SPIN {
                u32x4 a_;
                asm volatile("buffer_load_dwordx4 %0, %1, %2, 0 offen sc1\n\ts_waitcnt vmcnt(0)" : "=&v"(a_) : "v"(W_ACK + (lane < 60 ? ((lane * 43) >> 7) * 80 + lane * 16 : 1600 + lane * 16)), "s"(rs) : "memory");
                if (__builtin_amdgcn_ballot_w64(a_.x != tag || a_.y != tag || a_.z != tag || a_.w != tag) == 0) break;
                __builtin_amdgcn_s_sleep(8);
            }
            if (lane == 0) __builtin_amdgcn_raw_buffer_store_b32(tag, rs, W_EP, 0, SC1);
        }
        if (f >= 6) {
            wvm<0>();
            rdf(0, 3); rdf(1, 2);
            const int ho = W_H + ((lane >> 4) & 1) * 6912 + ((lane >> 5 && two ? d1 : d0) * 16 + li) * 8;
            u32x2 hg, hu;
            __builtin_amdgcn_s_sleep(HSL);
            SPIN {
                asm volatile("buffer_load_dwordx2 %0, %2, %3, 0 offen sc1\n\tbuffer_load_dwordx2 %1, %2, %3, %4 offen sc1\n\ts_waitcnt vmcnt(0)" : "=&v"(hg), "=&v"(hu) : "v"(ho), "s"(rs), "s"(13824) : "memory");
                if (__builtin_amdgcn_ballot_w64(hg.y != tag || hu.y != tag) == 0) break;
                __builtin_amdgcn_s_sleep(HS2);
            }
            ldsw<unsigned>(lds0 + wv * 16384 + 4096 + lane * 4, cvtpk(lo2f(hg.x) * lo2f(hu.x), hi2f(hg.x) * hi2f(hu.x)));
#pragma unroll
            for (int q = 0; q < 4; ++q) c[q] = (f32x4){0.f, 0.f, 0.f, 0.f};
#pragma unroll
            for (int k = 0; k < 2; ++k) if (k == 0 || two) {
                const u32x4 h = ldsrv<u32x4>(lds0 + wv * 16384 + 4096 + k * 128 + (li & 1) * 64 + g * 16);
#pragma unroll
                for (int q = 0; q < 4; ++q) c[q] = mfma32(__builtin_bit_cast(bf16x8, h), wf[k][q], c[q]);
            }
            const f32x4 ys = total(1024);
            if (wv == 0) __builtin_amdgcn_raw_buffer_store_b128((u32x4){__float_as_uint(ys.x), tag, __float_as_uint(ys.y), tag}, rs, W_Y + kr * 10240 + (fc * 64 + lane) * 16, 0, SC1);
        }
        TL(6);
        TLEND();
        wvm<0>();
    }
}

extern "C" void kernel_launch(void* const* d_in, const int* in_sizes, int n_in, void* d_out, int out_size, void* d_ws,
                              size_t ws_size, hipStream_t stream) {
    (void)hipFuncSetAttribute((const void*)fused, hipFuncAttributeMaxDynamicSharedMemorySize, LDSZ);
    fused<<<256, 512, LDSZ, stream>>>((const u16*)d_in[0], (const u16*)d_in[1], (const u16*)d_in[2], (const u16*)d_in[4],
                                      (const u16*)d_in[5], (const u16*)d_in[6], (const u16*)d_in[7], (char*)d_ws, (u16*)d_out);
}
```

```cpp
#include <hip/hip_runtime.h>
#include <type_traits>

typedef __attribute__((ext_vector_type(8))) short bf16x8;
typedef __attribute__((ext_vector_type(4))) short bf16x4;
typedef __attribute__((ext_vector_type(4))) float f32x4;
typedef __attribute__((ext_vector_type(2))) float f32x2;
typedef __attribute__((ext_vector_type(4))) unsigned u32x4;
typedef __attribute__((ext_vector_type(2))) unsigned u32x2;
typedef __attribute__((ext_vector_type(2))) __bf16 bf16x2v;
typedef __attribute__((ext_vector_type(8))) unsigned u32x8;
typedef __attribute__((ext_vector_type(16))) unsigned u32x16;
typedef unsigned short u16;
typedef __attribute__((address_space(3))) char lchar;

#define F_ 1728
constexpr int W_EP = 0;
constexpr int W_KEY = 4096;
constexpr int W_ACK = 8192;
constexpr int W_H = 12288;
constexpr int W_ST = 65536;
constexpr int W_Y = 98304;
constexpr int W_O = 155648;
constexpr int W_END = W_O + 80 * 40960;
#define SENT 0xffffffffu
constexpr int XS = 14336, SLOT = XS + 24576;
constexpr int WO0 = 2 * SLOT;
constexpr int MISC = WO0 + 81920;
constexpr int FL_S = MISC, FL_P = MISC + 16, FL_E = MISC + 32, FL_W = MISC + 48, FL_R = MISC + 64, FL_AUX = MISC + 80, ONES = MISC + 128, ROUT = MISC + 144, LTAG = MISC + 160;
constexpr int L_NM = MISC + 256, L_RS = L_NM + 512 + 64, L_RM = L_RS + 512, L_RG = L_RM + 512, L_DUM = L_RG + 512, L_KEY = L_DUM + 512;
constexpr int LS_ROW = L_NM;
constexpr int LDSZ = 163840;
static_assert(L_KEY + 1024 <= LDSZ, "lds");
#define SPIN_MAX (1 << 20)
#define SPIN _Pragma("nounroll") for (int it = 0; it < SPIN_MAX; ++it)

__device__ __forceinline__ unsigned cvtpk(float lo, float hi) { return __builtin_bit_cast(unsigned, __builtin_convertvector((f32x2){lo, hi}, bf16x2v)); }
__device__ __forceinline__ unsigned nosent(unsigned u) { return u < 0xfffffffeu ? u : 0xfffffffeu; }
__device__ __forceinline__ float lo2f(unsigned u) { return __uint_as_float(u << 16); }
__device__ __forceinline__ float hi2f(unsigned u) { return __uint_as_float(u & 0xffff0000u); }
__device__ __forceinline__ float rbf(float f) { return lo2f(cvtpk(f, f)); }
template <int CTRL> __device__ __forceinline__ float dppf(float x) { return __builtin_bit_cast(float, __builtin_amdgcn_mov_dpp(__builtin_bit_cast(int, x), CTRL, 0xf, 0xf, true)); }
template <int CTRL> __device__ __forceinline__ unsigned dppu(unsigned x) { return (unsigned)__builtin_amdgcn_mov_dpp((int)x, CTRL, 0xf, 0xf, true); }
__device__ __forceinline__ void sw16(unsigned& a, unsigned& b) { const auto r = __builtin_amdgcn_permlane16_swap(a, b, false, false); a = r[0]; b = r[1]; }
__device__ __forceinline__ void sw32(unsigned& a, unsigned& b) { const auto r = __builtin_amdgcn_permlane32_swap(a, b, false, false); a = r[0]; b = r[1]; }
__device__ __forceinline__ void sw16f(float& a, float& b) { unsigned x = __float_as_uint(a), y = __float_as_uint(b); sw16(x, y); a = __uint_as_float(x); b = __uint_as_float(y); }
__device__ __forceinline__ void sw32f(float& a, float& b) { unsigned x = __float_as_uint(a), y = __float_as_uint(b); sw32(x, y); a = __uint_as_float(x); b = __uint_as_float(y); }
__device__ __forceinline__ float vmax(float a, float b) { return __builtin_elementwise_maximum(a, b); }
__device__ __forceinline__ float sum16(float x) { x += dppf<0xB1>(x); x += dppf<0x4E>(x); x += dppf<0x141>(x); x += dppf<0x140>(x); return x; }
__device__ __forceinline__ float dot2(unsigned a, unsigned b, float c) { return __builtin_amdgcn_fdot2_f32_bf16(__builtin_bit_cast(bf16x2v, a), __builtin_bit_cast(bf16x2v, b), c, false); }
__device__ __forceinline__ float silu_b(float v) { return v * __builtin_amdgcn_rcpf(__builtin_amdgcn_exp2f(v * -1.4426950408889634f) + 1.f); }
__device__ __forceinline__ unsigned rkey(float rb, int row) {
    unsigned b = __float_as_uint(rb) >> 16; b = (b & 0x8000u) ? (~b & 0xffffu) : (b | 0x8000u);
    return (b << 16) | (unsigned)(0xffff - row);
}
__device__ __forceinline__ float keyscore(unsigned k) { const unsigned b = k >> 16; return __uint_as_float(((b & 0x8000u) ? (b & 0x7fffu) : (~b & 0xffffu)) << 16); }
#define T2M(b0, b1) { const unsigned _b0 = (b0), _b1 = (b1); const unsigned _m = min(k0, _b0); k0 = max(k0, _b0); k1 = max(_m, max(k1, _b1)); }
__device__ __forceinline__ uint2 top2wave(unsigned k0, unsigned k1) {
    T2M(dppu<0xB1>(k0), dppu<0xB1>(k1)); T2M(dppu<0x4E>(k0), dppu<0x4E>(k1));
    T2M(dppu<0x141>(k0), dppu<0x141>(k1)); T2M(dppu<0x140>(k0), dppu<0x140>(k1));
    const unsigned a0 = __builtin_amdgcn_readlane(k0, 0), a1 = __builtin_amdgcn_readlane(k1, 0);
    const unsigned c0 = __builtin_amdgcn_readlane(k0, 16), c1 = __builtin_amdgcn_readlane(k1, 16);
    const unsigned d0 = __builtin_amdgcn_readlane(k0, 32), d1 = __builtin_amdgcn_readlane(k1, 32);
    const unsigned e0 = __builtin_amdgcn_readlane(k0, 48), e1 = __builtin_amdgcn_readlane(k1, 48);
    k0 = a0; k1 = a1; T2M(c0, c1); T2M(d0, d1); T2M(e0, e1);
    return make_uint2(k0, k1);
}
template <int N> __device__ __forceinline__ void wvm() { asm volatile("" ::: "memory"); __builtin_amdgcn_s_waitcnt((N & 15) | ((N >> 4) << 14) | 0x0F70); asm volatile("" ::: "memory"); }
__device__ __forceinline__ bf16x4 trread(unsigned a) { return __builtin_amdgcn_ds_read_tr16_b64_v4i16((__attribute__((address_space(3))) bf16x4*)(size_t)a); }
template <class T> __device__ __forceinline__ T ldsr(unsigned a) { return *(const __attribute__((address_space(3))) T*)(size_t)a; }
template <class T> __device__ __forceinline__ T ldsrv(unsigned a) { return *(volatile __attribute__((address_space(3))) T*)(size_t)a; }
template <class T> __device__ __forceinline__ void ldsw(unsigned a, T v) { *(volatile __attribute__((address_space(3))) T*)(size_t)a = v; }
__device__ __forceinline__ bf16x8 cat8(bf16x4 lo, bf16x4 hi) { return (bf16x8){lo[0], lo[1], lo[2], lo[3], hi[0], hi[1], hi[2], hi[3]}; }
__device__ __forceinline__ bf16x8 frag4(unsigned a, unsigned b, unsigned c, unsigned d) { return __builtin_bit_cast(bf16x8, (u32x4){a, b, c, d}); }
__device__ __forceinline__ f32x4 mfma32(bf16x8 a, bf16x8 b, f32x4 c) { return __builtin_amdgcn_mfma_f32_16x16x32_bf16(a, b, c, 0, 0, 0); }
__device__ __forceinline__ int lane_id() { int l; asm volatile("v_mbcnt_lo_u32_b32 %0, -1, 0\n\tv_mbcnt_hi_u32_b32 %0, -1, %0" : "=v"(l)); __builtin_assume(l >= 0 && l < 64); return l; }
#define SB() __builtin_amdgcn_sched_barrier(0)
#define BAR() asm volatile("s_waitcnt lgkmcnt(0)\n\ts_barrier" ::: "memory")
#define MEMB() asm volatile("" ::: "memory")
#define RFL(x) ((unsigned)__builtin_amdgcn_readfirstlane((int)(x)))
#define SC1 16

#define WOPF 1
#define DSEL "0"
#define TRG1D
#define TRG2D
#define PRIA "s_setprio 2\n\t"
#define TCHD
#define RSLP "s_sleep 3\n\t"
#define PRIR "s_setprio 0\n\t"
#define PRI1 "s_setprio 3\n\t"
#define PRIW
#define PRIX "s_setprio 3\n\t"
#define PRIO
#define RSL0 "s_sleep 4\n\t"
#define RSL1 "s_sleep 3\n\t"
#define ASLP "s_sleep 1\n\t"
#define PRIS
#define PRIG "s_setprio 0\n\t"
#define PSLP "s_sleep 1\n\t"
#define HSL 26
#define HS2 10
#define TCH 1
#define KSL "0"
#define E2 0
#define P2A 0
#define FPOL " nt"
#define EK2 7
#define YS2 2
#define WPF 0
#define EARLYK 0
#define NOPK(v) asm("" : "+v"(v))
#define OPQ(v) asm volatile("" : "+v"(v))
#define IB(m) __builtin_amdgcn_inverse_ballot_w64(m)
#define END() do { asm volatile("s_endpgm"); __builtin_amdgcn_s_waitcnt(0); return; } while (0)
#define BARN() asm volatile("s_barrier" ::: "memory")
#define TL(i)
#define TA(i)
#define TLEND()
__device__ __forceinline__ constexpr int phys(int S) { return (S + 1) % 3; }

__global__ __launch_bounds__(512, 2) void fused(const u16* x_, const u16* wqkv_, const u16* wo_, const u16* w_r_, const u16* wg_, const u16* wu_, const u16* wd_, char* ws_, u16* out_) {
    extern __shared__ __attribute__((aligned(16))) char smem[];
#define SL(k)
#define DUMP
#define SZ
#define SB_(k)
#define SO(k)
#define RINC
#define RZ
#define SA(k)
#define ST(h)
#define SP0
#define LINC
#define GINC
#define RCNT
    typedef __attribute__((ext_vector_type(32))) unsigned u32x32;
    u32x32 sa, sb; u32x8 sc;
    unsigned tag, bid = blockIdx.x; int tid = threadIdx.x;
    typedef __attribute__((address_space(1))) char gchar;
    typedef gchar* const __attribute__((address_space(4)))* karg_t;
    karg_t ka = (karg_t)__builtin_amdgcn_kernarg_segment_ptr();
    asm volatile(
            SL(0) SZ
            "s_load_dwordx2 s[40:41], %[k], 0x8\n\ts_load_dwordx2 s[36:37], %[k], 0x0\n\tv_readfirstlane_b32 s69, %[t]\n\ts_lshr_b32 s70, %[b], 3\n\ts_cmpk_lg_u32 s69, 0xc0\n\ts_cbranch_scc1 1f\n\t"
            "s_getpc_b64 s[72:73]\n\t.Lch:\ns_lshl3_add_u32 s71, s70, 0xffffff40\n\tv_add_lshl_u32 v36, s71, %[t], 7\n\tv_min_u32 v36, .Lce-.Lch-4, v36\n\ts_mov_b32 m0, 162368\n\t"
            "s_mov_b64 exec, 0xff\n\tglobal_load_lds_dword v36, s[72:73]\n\ts_mov_b64 exec, -1\n\t1:\ns_add_i32 s70, s70, 25\n\ts_and_b32 s70, s70, 31\n\t"
            "s_cmp_lt_u32 s70, 25\n\ts_cbranch_scc0 .LF\n\tv_bfe_u32 v36, %[t], 3, 3\n\tv_and_b32 v37, 7, %[t]\n\ts_mul_i32 s71, s70, 13\n\ts_lshr_b32 s71, s71, 6\n\t"
            "v_and_b32 v38, 2, v36\n\tv_xor_b32 v46, v38, v37\n\ts_lshl_b32 s64, s71, 2\n\ts_sub_i32 s64, s70, s64\n\ts_sub_i32 s57, s64, s71\n\tv_mul_u32_u24 v40, 0xf00, v36\n\t"
            "s_sub_i32 s72, s64, 5\n\ts_min_u32 s64, s64, s72\n\tv_lshl_add_u32 v40, v46, 4, v40\n\ts_bfe_u32 s56, s69, 0x20006\n\ts_and_b32 s72, %[b], 1\n\ts_mul_i32 s72, s72, 5\n\t"
            "s_add_i32 s58, s57, s72\n\ts_lshl_b32 s58, s58, 7\n\ts_mul_i32 s61, s64, 0x78000\n\ts_mul_i32 s73, s56, 0xf000\n\ts_add_i32 s73, s73, s58\n\ts_add_i32 s61, s61, s73\n\t"
            "s_mul_i32 s66, s56, 0x1800\n\ts_add_i32 m0, s66, 53248\n\ts_mov_b32 s42, 0x258000\n\ts_mov_b32 s43, 0x20000\n\tv_add_u32 v41, 0x100, v40\n\tv_add_u32 v42, 0x200, v40\n\t"
            "v_xor_b32 v43, 64, v40\n\tv_add_u32 v43, 0x6c00, v43\n\tv_add_u32 v44, 0x1100, v43\n\tv_add_u32 v45, 0x1200, v43\n\ts_cmpk_lt_u32 s69, 0x100\n\ts_cbranch_scc1 1f\n\t"
            "s_waitcnt lgkmcnt(0)\n\tbuffer_load_dwordx4 v40, s[40:43], s61 offen lds\n\tbuffer_load_dwordx4 v41, s[40:43], s61 offen offset:1024 lds\n\tbuffer_load_dwordx4 v42, s[40:43], s61 offen offset:2048 lds\n\tbuffer_load_dwordx4 v43, s[40:43], s61 offen offset:3072 lds\n\ts_addk_i32 m0, 0x1000\n\t"
            "s_nop 0\n\tbuffer_load_dwordx4 v44, s[40:43], s61 offen lds\n\tbuffer_load_dwordx4 v45, s[40:43], s61 offen offset:1024 lds\n\t1:\ns_mul_i32 s72, s56, 28\n\tv_add_u32 v39, s72, v36\n\t"
            "s_bfe_u32 s62, %[b], 0x10001\n\ts_mul_i32 s62, s62, 5\n\ts_add_i32 s62, s62, s71\n\ts_mul_i32 s62, s62, 104\n\ts_sub_i32 s62, s62, 8\n\tv_and_b32 v47, 3, v39\n\t"
            "v_lshl_add_u32 v47, v47, 2, s62\n\tv_and_b32 v48, 0x70, v39\n\tv_bfe_u32 v49, v39, 2, 2\n\tv_add3_u32 v48, v48, v49, v47\n\tv_bfe_u32 v49, v39, 1, 3\n\tv_xor_b32 v50, v49, v37\n\t"
            "v_mul_i32_i24 v48, 0xa00, v48\n\ts_bfe_u32 s63, %[b], 0x10002\n\ts_mul_i32 s63, s63, 0x500\n\ts_lshl_b32 s60, s64, 8\n\tv_mov_b32 v51, v36\n\tv_lshl_add_u32 v36, v50, 4, v48\n\t"
            "s_add_i32 s60, s60, s63\n\ts_mul_i32 s65, s56, 0xe00\n\ts_add_i32 m0, s65, 38912\n\ts_mov_b32 s38, 0x280000\n\ts_mov_b32 s39, 0x20000\n\tv_add_u32 v38, 0x9800, v36\n\t"
            "s_cmpk_lt_u32 s69, 0x100\n\ts_cbranch_scc1 1f\n\tbuffer_load_dwordx4 v36, s[36:39], s60 offen lds\n\tbuffer_load_dwordx4 v38, s[36:39], s60 offen offset:2048 lds\n\ts_barrier\n\t1:\n"
            "s_load_dwordx2 s[44:45], %[k], 0x38\n\ts_load_dwordx2 s[48:49], %[k], 0x10\n\ts_load_dwordx2 s[52:53], %[k], 0x40\n\tv_add_u32 v39, 8, v39\n\tv_and_b32 v48, 0x70, v39\n\tv_bfe_u32 v49, v39, 2, 2\n\t"
            "v_add3_u32 v48, v48, v49, v47\n\tv_xor_b32 v50, 4, v50\n\tv_mul_i32_i24 v48, 0xa00, v48\n\tv_lshl_add_u32 v37, v50, 4, v48\n\tv_subrev_u32 v37, 0x400, v37\n\tv_add_u32 v39, 0x9800, v37\n\t"
            "s_addk_i32 s66, 14336\n\ts_bfe_u32 s59, %[b], 0x20001\n\ts_mul_i32 s59, s59, 5\n\ts_add_i32 s59, s59, s71\n\ts_mov_b32 s46, 3432448\n\ts_mov_b32 s47, 0x20000\n\t"
            "s_mov_b32 s54, 0x280000\n\ts_mov_b32 s55, 0x20000\n\tv_and_b32 v52, 63, %[t]\n\tv_bfe_u32 v53, %[t], 4, 2\n\tv_and_b32 v54, 15, %[t]\n\tv_and_b32 v55, 3, %[t]\n\t"
            "v_bfe_u32 v56, %[t], 2, 2\n\tv_lshl_add_u32 v57, v55, 2, v56\n\ts_lshr_b32 s69, s69, 6\n\ts_cmp_lt_u32 s69, 4\n\ts_cbranch_scc0 .LB\n\ts_branch .LA\n\t"
            ".LF:\ns_load_dwordx2 s[72:73], %[k], 0x18\n\ts_and_b32 s71, %[b], 1\n\ts_lshl1_add_u32 s71, s70, s71\n\ts_mul_i32 s71, s71, 40\n\ts_sub_u32 s71, s71, 2000\n\t"
            "v_lshrrev_b32 v2, 4, %[t]\n\tv_add_u32 v2, s71, v2\n\tv_add_u32 v3, 32, v2\n\tv_min_u32 v2, 0x1ff, v2\n\tv_min_u32 v3, 0x1ff, v3\n\tv_and_b32 v4, 15, %[t]\n\t"
            "v_lshlrev_b32 v4, 4, v4\n\ts_bfe_u32 s71, %[b], 0x10001\n\ts_mul_i32 s71, s71, 0x140000\n\ts_bfe_u32 s74, %[b], 0x10002\n\ts_mul_i32 s74, s74, 0x500\n\ts_add_u32 s71, s71, s74\n\t"
            "v_add_u32 v5, s71, v4\n\ts_movk_i32 s71, 0xa00\n\tv_mad_u32_u24 v2, v2, s71, v5\n\tv_mad_u32_u24 v3, v3, s71, v5\n\ts_waitcnt lgkmcnt(0)\n\tglobal_load_dwordx4 v[104:107], v4, s[72:73] offset:0\n\t"
            "global_load_dwordx4 v[108:111], v4, s[72:73] offset:256\n\tglobal_load_dwordx4 v[112:115], v4, s[72:73] offset:512\n\tglobal_load_dwordx4 v[116:119], v4, s[72:73] offset:768\n\tglobal_load_dwordx4 v[120:123], v4, s[72:73] offset:1024\n\ts_cmpk_ge_u32 s69, 0x80\n\ts_cbranch_scc1 1f\n\t"
            "global_load_dwordx4 v[84:87], v3, s[36:37] offset:0\n\tglobal_load_dwordx4 v[88:91], v3, s[36:37] offset:256\n\tglobal_load_dwordx4 v[92:95], v3, s[36:37] offset:512\n\tglobal_load_dwordx4 v[96:99], v3, s[36:37] offset:768\n\tglobal_load_dwordx4 v[100:103], v3, s[36:37] offset:1024\n\t1:\n"
            "global_load_dwordx4 v[64:67], v2, s[36:37] offset:0\n\tglobal_load_dwordx4 v[68:71], v2, s[36:37] offset:256\n\tglobal_load_dwordx4 v[72:75], v2, s[36:37] offset:512\n\tglobal_load_dwordx4 v[76:79], v2, s[36:37] offset:768\n\tglobal_load_dwordx4 v[80:83], v2, s[36:37] offset:1024\n\ts_branch .Lout\n\t"
            ".LA:\nv_lshrrev_b32 v2, 1, v54\n\tv_xor_b32 v2, v2, v53\n\tv_lshlrev_b32 v3, 7, v54\n\tv_lshl_add_u32 v2, v2, 4, v3\n\tv_xor_b32 v3, 64, v2\n\t"
            "v_add_u32 v4, 38912, v2\n\tv_add_u32 v5, 38912, v3\n\tv_add_u32 v10, 77824, v2\n\tv_add_u32 v11, 77824, v3\n\tv_mov_b32 v12, 159872\n\tv_cmp_eq_u32 vcc, 0, v54\n\t"
            "v_mov_b32 v60, 0\n\tv_mov_b32 v61, 0\n\tv_mov_b32 v62, 0x3f803f80\n\tv_lshlrev_b32 v63, 3, v52\n\tv_cndmask_b32 v6, v2, v12, vcc\n\tv_cndmask_b32 v7, v3, v12, vcc\n\t"
            "v_cndmask_b32 v8, v4, v12, vcc\n\tv_cndmask_b32 v9, v5, v12, vcc\n\tv_cndmask_b32 v10, v10, v12, vcc\n\tv_cndmask_b32 v11, v11, v12, vcc\n\tv_lshlrev_b32 v64, 2, v52\n\tv_lshrrev_b32 v65, 2, v52\n\t"
            "v_cmp_eq_u32 vcc, 8, v65\n\tv_add_u32 v66, 161088, v63\n\tv_add_u32 v64, 159744, v64\n\tv_cndmask_b32 v62, 0, v62, vcc\n\ts_cmp_lg_u32 s69, 1\n\ts_cbranch_scc1 1f\n\t"
            "ds_write_b32 v64, v62\n\tds_write_b64 v66, v[60:61]\n\t1:\nv_bfe_u32 v12, v52, 3, 2\n\tv_xor_b32 v12, s69, v12\n\tv_lshlrev_b32 v13, 7, v56\n\t"
            "v_lshl_add_u32 v13, v55, 3, v13\n\tv_lshl_add_u32 v12, v12, 5, v13\n\tv_mul_u32_u24 v13, 0xc00, v53\n\ts_movk_i32 s72, 14336\n\tv_add3_u32 v12, v12, v13, s72\n\tv_add_u32 v13, 38912, v12\n\t"
            "s_mov_b32 s72, 160000\n\tv_lshl_add_u32 v14, v54, 2, s72\n\tv_lshl_add_u32 v15, v53, 4, s72\n\ts_cmp_eq_u32 s69, 3\n\ts_cselect_b32 s74, 1, 0\n\ts_cselect_b64 s[76:77], -1, 0\n\t"
            "s_cselect_b32 s75, 4, 0\n\ts_add_i32 s72, s69, 1\n\ts_bfe_u32 s72, s72, 0x10001\n\ts_xor_b32 s72, s72, 1\n\ts_lshr_b32 s73, s69, 1\n\ts_lshl1_add_u32 s78, s72, s73\n\t"
            "s_lshl2_add_u32 s78, s78, 159776\n\tv_mov_b32 v34, s78\n\ts_lshl2_add_u32 s78, s69, 159744\n\tv_mov_b32 v33, s78\n\ts_mul_i32 s78, s59, 163840\n\ts_lshl_b32 s79, s58, 5\n\t"
            "s_add_i32 s78, s78, s79\n\ts_lshl_b32 s79, s69, 10\n\ts_add_i32 s78, s78, s79\n\ts_add_i32 s78, s78, 155648\n\tv_lshl_add_u32 v16, v52, 4, s78\n\tv_and_b32 v215, 1, v53\n\t"
            "v_lshl_add_u32 v215, v215, 30, v16\n\ts_lshl_b32 s79, s74, 11\n\ts_add_i32 s78, s78, s79\n\ts_sub_i32 s78, s78, 0x400\n\ts_cmp_lg_u32 s57, s75\n\ts_cselect_b32 s79, s72, 0\n\t"
            "s_cmp_lg_u32 s79, 0\n\ts_cselect_b32 s78, s78, 0x40000000\n\tv_lshl_add_u32 v17, v52, 4, s78\n\ts_sub_i32 s78, s69, s57\n\ts_add_i32 s79, s78, 5\n\ts_min_u32 s78, s78, s79\n\t"
            "v_cmp_gt_u32 s[80:81], s78, v53\n\tv_cmp_gt_u32 s[82:83], s78, v56\n\ts_mov_b32 s14, 0x33363333\n\ts_mov_b32 s15, 0x36663366\n\ts_mov_b32 s16, 0x666c6666\n\ts_mov_b32 s17, 0x6ccc66cc\n\t"
            "s_mov_b32 s18, 0x33333333\n\ts_mov_b32 s19, 0x33333333\n\ts_xor_b64 s[80:81], s[80:81], s[76:77]\n\ts_xor_b64 s[82:83], s[82:83], s[76:77]\n\ts_andn2_b64 vcc, s[80:81], s[18:19]\n\tv_cndmask_b32 v18, 0, -1, vcc\n\t"
            "s_and_b64 vcc, s[80:81], s[18:19]\n\tv_cndmask_b32 v19, 0, -1, vcc\n\ts_nor_b64 vcc, s[80:81], s[18:19]\n\tv_cndmask_b32 v20, 0, -1, vcc\n\ts_andn2_b64 vcc, s[18:19], s[80:81]\n\tv_cndmask_b32 v21, 0, -1, vcc\n\t"
            "v_cndmask_b32 v22, 0, -1, s[82:83]\n\tv_cndmask_b32 v23, -1, 0, s[82:83]\n\tv_sub_u32 v58, v57, v53\n\tv_add_u32 v58, 4, v58\n\tv_and_b32 v58, 28, v58\n\tv_lshlrev_b32 v58, 1, v58\n\t"
            "v_mov_b32 v24, -1\n\tv_mov_b32 v25, 0\n\tv_lshlrev_b64 v[24:25], v58, v[24:25]\n\tv_lshlrev_b64 v[24:25], v58, v[24:25]\n\tv_sub_u32 v58, 32, v58\n\tv_min_u32 v58, 16, v58\n\t"
            "v_lshrrev_b32 v26, v58, -1\n\tv_lshrrev_b32 v26, v58, v26\n\ts_lshl1_add_u32 s78, s69, s74\n\ts_xor_b32 s79, s78, 1\n\ts_mul_i32 s78, s78, 0xc00\n\ts_mul_i32 s79, s79, 0xc00\n\t"
            "s_addk_i32 s78, 14336\n\ts_addk_i32 s79, 14336\n\tv_add_u32 v28, s78, v63\n\tv_add_u32 v29, s79, v63\n\ts_cmp_eq_u32 s69, s57\n\ts_addc_u32 s78, s69, 0\n\t"
            "s_add_i32 s79, s69, 1\n\ts_cmp_eq_u32 s79, s57\n\ts_subb_u32 s79, s79, 0\n\ts_cmp_eq_u32 s74, 1\n\ts_cselect_b32 s80, s79, s78\n\ts_cselect_b32 s81, s78, s79\n\t"
            "v_lshlrev_b32 v58, 2, v53\n\tv_lshl_add_u32 v58, v54, 4, v58\n\ts_cmp_gt_u32 s80, s57\n\ts_subb_u32 s82, s80, 0\n\ts_lshl2_add_u32 s82, s82, 159760\n\tv_mov_b32 v59, s82\n\t"
            "s_lshl_b32 s82, s80, 1\n\ts_max_u32 s82, s82, 1\n\ts_mul_i32 s82, s82, 0xc00\n\ts_addk_i32 s82, 11264\n\tv_add_u32 v30, s82, v58\n\ts_cmp_gt_u32 s81, s57\n\t"
            "s_subb_u32 s82, s81, 0\n\ts_lshl2_add_u32 s82, s82, 159760\n\tv_mov_b32 v60, s82\n\ts_lshl_b32 s82, s81, 1\n\ts_max_u32 s82, s82, 1\n\ts_mul_i32 s82, s82, 0xc00\n\t"
            "s_addk_i32 s82, 11264\n\tv_add_u32 v31, s82, v58\n\tv_cndmask_b32 v32, v59, v60, s[18:19]\n\tv_mov_b32 v35, 1\n\ts_waitcnt lgkmcnt(0)\n\ts_barrier\n\t"
            "s_add_u32 m0, s65, 38912\n\ts_load_dword s67, s[44:45], 0x0\n\tbuffer_load_dwordx4 v37, s[36:39], s60 offen offset:1024 lds\n\ts_mov_b32 exec_hi, 0\n\tbuffer_load_dwordx4 v39, s[36:39], s60 offen offset:3072 lds\n\ts_mov_b32 exec_hi, -1\n\t"
            "s_add_u32 m0, s66, 77824\n\ts_add_u32 s61, s61, 0x3c000\n\tbuffer_load_dwordx4 v40, s[40:43], s61 offen lds\n\tbuffer_load_dwordx4 v41, s[40:43], s61 offen offset:1024 lds\n\tbuffer_load_dwordx4 v42, s[40:43], s61 offen offset:2048 lds\n\tbuffer_load_dwordx4 v43, s[40:43], s61 offen offset:3072 lds\n\t"
            "s_addk_i32 m0, 0x1000\n\ts_add_u32 s60, s60, 128\n\tbuffer_load_dwordx4 v44, s[40:43], s61 offen lds\n\tbuffer_load_dwordx4 v45, s[40:43], s61 offen offset:1024 lds\n\ts_add_u32 m0, s65, 77824\n\ts_mov_b32 s20, 0\n\t"
            "s_mov_b32 s21, 40960\n\ts_mov_b32 s22, 81920\n\ts_mov_b32 s23, 122880\n\tbuffer_load_dwordx4 v36, s[36:39], s60 offen lds\n\tbuffer_load_dwordx4 v37, s[36:39], s60 offen offset:1024 lds\n\tbuffer_load_dwordx4 v38, s[36:39], s60 offen offset:2048 lds\n\t"
            "s_mov_b32 exec_hi, 0\n\tbuffer_load_dwordx4 v39, s[36:39], s60 offen offset:3072 lds\n\ts_mov_b32 exec_hi, -1\n\ts_mov_b32 s68, 0x40000\n\ts_waitcnt lgkmcnt(0)\n\ts_mul_i32 s67, s67, 0x19660d\n\t"
            "s_add_u32 s67, s67, 0x3c6ef35f\n\ts_waitcnt vmcnt(10)\n\ts_barrier\n\t"
            SA(1) ST(-1)
            "ds_read_b64_tr_b16 v[176:177], v13 offset:0\n\tds_read_b64_tr_b16 v[178:179], v13 offset:512\n\tds_read_b128 v[120:123], v8\n\tds_read_b128 v[124:127], v4 offset:2048\n\tds_read_b128 v[128:131], v4 offset:4096\n\tds_read_b128 v[132:135], v4 offset:6144\n\t"
            "ds_read_b128 v[136:139], v4 offset:8192\n\tds_read_b128 v[140:143], v4 offset:10240\n\tds_read_b128 v[144:147], v4 offset:12288\n\tds_read_b64_tr_b16 v[180:181], v13 offset:1024\n\tds_read_b64_tr_b16 v[182:183], v13 offset:1536\n\tds_read_b64_tr_b16 v[184:185], v13 offset:2048\n\t"
            "ds_read_b64_tr_b16 v[186:187], v13 offset:2560\n\ts_waitcnt lgkmcnt(10)\n\tv_mfma_f32_16x16x32_bf16 v[36:39], v[176:179], v[120:123], 0\n\ts_waitcnt lgkmcnt(9)\n\tv_mfma_f32_16x16x32_bf16 v[48:51], v[176:179], v[124:127], 0\n\ts_waitcnt lgkmcnt(8)\n\t"
            "v_mfma_f32_16x16x32_bf16 v[60:63], v[176:179], v[128:131], 0\n\tds_read_b64_tr_b16 v[188:189], v13 offset:12288\n\tds_read_b64_tr_b16 v[190:191], v13 offset:12800\n\ts_waitcnt lgkmcnt(9)\n\tv_mfma_f32_16x16x32_bf16 v[72:75], v[176:179], v[132:135], 0\n\tds_read_b128 v[148:151], v9\n\t"
            "s_waitcnt lgkmcnt(9)\n\tv_mfma_f32_16x16x32_bf16 v[84:87], v[176:179], v[136:139], 0\n\tds_read_b128 v[152:155], v5 offset:2048\n\ts_waitcnt lgkmcnt(9)\n\tv_mfma_f32_16x16x32_bf16 v[96:99], v[176:179], v[140:143], 0\n\tds_read_b128 v[156:159], v5 offset:4096\n\t"
            "s_waitcnt lgkmcnt(9)\n\tv_mfma_f32_16x16x32_bf16 v[108:111], v[176:179], v[144:147], 0\n\tds_read_b128 v[160:163], v5 offset:6144\n\ts_waitcnt lgkmcnt(8)\n\tv_mfma_f32_16x16x32_bf16 v[40:43], v[180:183], v[120:123], 0\n\tds_read_b128 v[164:167], v5 offset:8192\n\t"
            "v_mfma_f32_16x16x32_bf16 v[52:55], v[180:183], v[124:127], 0\n\tds_read_b128 v[168:171], v5 offset:10240\n\tv_mfma_f32_16x16x32_bf16 v[64:67], v[180:183], v[128:131], 0\n\tds_read_b128 v[172:175], v5 offset:12288\n\tv_mfma_f32_16x16x32_bf16 v[76:79], v[180:183], v[132:135], 0\n\tds_read_b64_tr_b16 v[192:193], v13 offset:13312\n\t"
            "ds_read_b64_tr_b16 v[194:195], v13 offset:13824\n\tv_mfma_f32_16x16x32_bf16 v[88:91], v[180:183], v[136:139], 0\n\tds_read_b64_tr_b16 v[196:197], v13 offset:14336\n\tds_read_b64_tr_b16 v[198:199], v13 offset:14848\n\tv_mfma_f32_16x16x32_bf16 v[100:103], v[180:183], v[140:143], 0\n\tv_mfma_f32_16x16x32_bf16 v[112:115], v[180:183], v[144:147], 0\n\t"
            "s_waitcnt lgkmcnt(13)\n\tv_mfma_f32_16x16x32_bf16 v[44:47], v[120:123], v[184:187], 0\n\tv_mfma_f32_16x16x32_bf16 v[56:59], v[124:127], v[184:187], 0\n\tv_mfma_f32_16x16x32_bf16 v[68:71], v[128:131], v[184:187], 0\n\tv_mfma_f32_16x16x32_bf16 v[80:83], v[132:135], v[184:187], 0\n\tv_mfma_f32_16x16x32_bf16 v[92:95], v[136:139], v[184:187], 0\n\t"
            "v_mfma_f32_16x16x32_bf16 v[104:107], v[140:143], v[184:187], 0\n\tv_mfma_f32_16x16x32_bf16 v[116:119], v[144:147], v[184:187], 0\n\ts_waitcnt lgkmcnt(10)\n\tv_mfma_f32_16x16x32_bf16 v[36:39], v[188:191], v[148:151], v[36:39]\n\ts_waitcnt lgkmcnt(9)\n\tv_mfma_f32_16x16x32_bf16 v[48:51], v[188:191], v[152:155], v[48:51]\n\t"
            "s_waitcnt lgkmcnt(8)\n\tv_mfma_f32_16x16x32_bf16 v[60:63], v[188:191], v[156:159], v[60:63]\n\ts_waitcnt lgkmcnt(7)\n\tv_mfma_f32_16x16x32_bf16 v[72:75], v[188:191], v[160:163], v[72:75]\n\ts_waitcnt lgkmcnt(6)\n\tv_mfma_f32_16x16x32_bf16 v[84:87], v[188:191], v[164:167], v[84:87]\n\t"
            "s_waitcnt lgkmcnt(5)\n\tv_mfma_f32_16x16x32_bf16 v[96:99], v[188:191], v[168:171], v[96:99]\n\ts_waitcnt lgkmcnt(4)\n\tv_mfma_f32_16x16x32_bf16 v[108:111], v[188:191], v[172:175], v[108:111]\n\ts_waitcnt lgkmcnt(2)\n\tv_mfma_f32_16x16x32_bf16 v[40:43], v[192:195], v[148:151], v[40:43]\n\t"
            "v_mfma_f32_16x16x32_bf16 v[52:55], v[192:195], v[152:155], v[52:55]\n\tv_mfma_f32_16x16x32_bf16 v[64:67], v[192:195], v[156:159], v[64:67]\n\tv_mfma_f32_16x16x32_bf16 v[76:79], v[192:195], v[160:163], v[76:79]\n\ts_waitcnt vmcnt(0) lgkmcnt(0)\n\ts_barrier\n\t"
            ST(1)
            "ds_read_b64_tr_b16 v[176:177], v13 offset:38912\n\tds_read_b64_tr_b16 v[178:179], v13 offset:39424\n\tv_mfma_f32_16x16x32_bf16 v[88:91], v[192:195], v[164:167], v[88:91]\n\tds_read_b128 v[120:123], v10\n\tv_mfma_f32_16x16x32_bf16 v[100:103], v[192:195], v[168:171], v[100:103]\n\tds_read_b128 v[124:127], v4 offset:40960\n\t"
            "v_mfma_f32_16x16x32_bf16 v[112:115], v[192:195], v[172:175], v[112:115]\n\tds_read_b128 v[128:131], v4 offset:43008\n\tv_mfma_f32_16x16x32_bf16 v[44:47], v[148:151], v[196:199], v[44:47]\n\tds_read_b128 v[132:135], v4 offset:45056\n\tv_mfma_f32_16x16x32_bf16 v[56:59], v[152:155], v[196:199], v[56:59]\n\tds_read_b128 v[136:139], v4 offset:47104\n\t"
            "v_mfma_f32_16x16x32_bf16 v[68:71], v[156:159], v[196:199], v[68:71]\n\tds_read_b128 v[140:143], v4 offset:49152\n\tv_mfma_f32_16x16x32_bf16 v[80:83], v[160:163], v[196:199], v[80:83]\n\tds_read_b128 v[144:147], v4 offset:51200\n\tv_mfma_f32_16x16x32_bf16 v[92:95], v[164:167], v[196:199], v[92:95]\n\tds_read_b64_tr_b16 v[180:181], v13 offset:39936\n\t"
            "ds_read_b64_tr_b16 v[182:183], v13 offset:40448\n\tv_mfma_f32_16x16x32_bf16 v[104:107], v[168:171], v[196:199], v[104:107]\n\tds_read_b64_tr_b16 v[184:185], v13 offset:40960\n\tds_read_b64_tr_b16 v[186:187], v13 offset:41472\n\tv_mfma_f32_16x16x32_bf16 v[116:119], v[172:175], v[196:199], v[116:119]\n\ts_waitcnt lgkmcnt(10)\n\t"
            "v_mfma_f32_16x16x32_bf16 v[36:39], v[176:179], v[120:123], v[36:39]\n\ts_waitcnt lgkmcnt(9)\n\tv_mfma_f32_16x16x32_bf16 v[48:51], v[176:179], v[124:127], v[48:51]\n\ts_waitcnt lgkmcnt(8)\n\tv_mfma_f32_16x16x32_bf16 v[60:63], v[176:179], v[128:131], v[60:63]\n\tds_read_b64_tr_b16 v[188:189], v13 offset:51200\n\t"
            "ds_read_b64_tr_b16 v[190:191], v13 offset:51712\n\ts_waitcnt lgkmcnt(9)\n\tv_mfma_f32_16x16x32_bf16 v[72:75], v[176:179], v[132:135], v[72:75]\n\tds_read_b128 v[148:151], v11\n\ts_waitcnt lgkmcnt(9)\n\tv_mfma_f32_16x16x32_bf16 v[84:87], v[176:179], v[136:139], v[84:87]\n\t"
            "ds_read_b128 v[152:155], v5 offset:40960\n\ts_waitcnt lgkmcnt(9)\n\tv_mfma_f32_16x16x32_bf16 v[96:99], v[176:179], v[140:143], v[96:99]\n\tds_read_b128 v[156:159], v5 offset:43008\n\ts_waitcnt lgkmcnt(9)\n\tv_mfma_f32_16x16x32_bf16 v[108:111], v[176:179], v[144:147], v[108:111]\n\t"
            "ds_read_b128 v[160:163], v5 offset:45056\n\ts_waitcnt lgkmcnt(8)\n\tv_mfma_f32_16x16x32_bf16 v[40:43], v[180:183], v[120:123], v[40:43]\n\tds_read_b128 v[164:167], v5 offset:47104\n\tv_mfma_f32_16x16x32_bf16 v[52:55], v[180:183], v[124:127], v[52:55]\n\tds_read_b128 v[168:171], v5 offset:49152\n\t"
            "v_mfma_f32_16x16x32_bf16 v[64:67], v[180:183], v[128:131], v[64:67]\n\tds_read_b128 v[172:175], v5 offset:51200\n\tv_mfma_f32_16x16x32_bf16 v[76:79], v[180:183], v[132:135], v[76:79]\n\tds_read_b64_tr_b16 v[192:193], v13 offset:52224\n\tds_read_b64_tr_b16 v[194:195], v13 offset:52736\n\tv_mfma_f32_16x16x32_bf16 v[88:91], v[180:183], v[136:139], v[88:91]\n\t"
            "ds_read_b64_tr_b16 v[196:197], v13 offset:53248\n\tds_read_b64_tr_b16 v[198:199], v13 offset:53760\n\tv_mfma_f32_16x16x32_bf16 v[100:103], v[180:183], v[140:143], v[100:103]\n\tv_mfma_f32_16x16x32_bf16 v[112:115], v[180:183], v[144:147], v[112:115]\n\ts_waitcnt lgkmcnt(13)\n\tv_mfma_f32_16x16x32_bf16 v[44:47], v[120:123], v[184:187], v[44:47]\n\t"
            "v_mfma_f32_16x16x32_bf16 v[56:59], v[124:127], v[184:187], v[56:59]\n\tv_mfma_f32_16x16x32_bf16 v[68:71], v[128:131], v[184:187], v[68:71]\n\tv_mfma_f32_16x16x32_bf16 v[80:83], v[132:135], v[184:187], v[80:83]\n\tv_mfma_f32_16x16x32_bf16 v[92:95], v[136:139], v[184:187], v[92:95]\n\tv_mfma_f32_16x16x32_bf16 v[104:107], v[140:143], v[184:187], v[104:107]\n\tv_mfma_f32_16x16x32_bf16 v[116:119], v[144:147], v[184:187], v[116:119]\n\t"
            "s_waitcnt lgkmcnt(10)\n\tv_mfma_f32_16x16x32_bf16 v[36:39], v[188:191], v[148:151], v[36:39]\n\ts_waitcnt lgkmcnt(9)\n\tv_mfma_f32_16x16x32_bf16 v[48:51], v[188:191], v[152:155], v[48:51]\n\ts_waitcnt lgkmcnt(8)\n\tv_mfma_f32_16x16x32_bf16 v[60:63], v[188:191], v[156:159], v[60:63]\n\t"
            "s_waitcnt lgkmcnt(7)\n\tv_mfma_f32_16x16x32_bf16 v[72:75], v[188:191], v[160:163], v[72:75]\n\ts_waitcnt lgkmcnt(6)\n\tv_mfma_f32_16x16x32_bf16 v[84:87], v[188:191], v[164:167], v[84:87]\n\ts_waitcnt lgkmcnt(5)\n\tv_mfma_f32_16x16x32_bf16 v[96:99], v[188:191], v[168:171], v[96:99]\n\t"
            "s_waitcnt lgkmcnt(4)\n\tv_mfma_f32_16x16x32_bf16 v[108:111], v[188:191], v[172:175], v[108:111]\n\ts_waitcnt lgkmcnt(2)\n\tv_mfma_f32_16x16x32_bf16 v[40:43], v[192:195], v[148:151], v[40:43]\n\tv_mfma_f32_16x16x32_bf16 v[52:55], v[192:195], v[152:155], v[52:55]\n\tv_mfma_f32_16x16x32_bf16 v[64:67], v[192:195], v[156:159], v[64:67]\n\t"
            "v_mfma_f32_16x16x32_bf16 v[76:79], v[192:195], v[160:163], v[76:79]\n\ts_waitcnt lgkmcnt(0)\n\ts_barrier\n\t"
            ST(3)
            "ds_read_b64_tr_b16 v[176:177], v12 offset:0\n\tds_read_b64_tr_b16 v[178:179], v12 offset:512\n\tv_mfma_f32_16x16x32_bf16 v[88:91], v[192:195], v[164:167], v[88:91]\n\tds_read_b128 v[120:123], v6\n\tv_mfma_f32_16x16x32_bf16 v[100:103], v[192:195], v[168:171], v[100:103]\n\tds_read_b128 v[124:127], v2 offset:2048\n\t"
            "v_mfma_f32_16x16x32_bf16 v[112:115], v[192:195], v[172:175], v[112:115]\n\tds_read_b128 v[128:131], v2 offset:4096\n\tv_mfma_f32_16x16x32_bf16 v[44:47], v[148:151], v[196:199], v[44:47]\n\tds_read_b128 v[132:135], v2 offset:6144\n\tv_mfma_f32_16x16x32_bf16 v[56:59], v[152:155], v[196:199], v[56:59]\n\tds_read_b128 v[136:139], v2 offset:8192\n\t"
            "v_mfma_f32_16x16x32_bf16 v[68:71], v[156:159], v[196:199], v[68:71]\n\tds_read_b128 v[140:143], v2 offset:10240\n\tv_mfma_f32_16x16x32_bf16 v[80:83], v[160:163], v[196:199], v[80:83]\n\tds_read_b128 v[144:147], v2 offset:12288\n\tv_mfma_f32_16x16x32_bf16 v[92:95], v[164:167], v[196:199], v[92:95]\n\tds_read_b64_tr_b16 v[180:181], v12 offset:1024\n\t"
            "ds_read_b64_tr_b16 v[182:183], v12 offset:1536\n\tv_mfma_f32_16x16x32_bf16 v[104:107], v[168:171], v[196:199], v[104:107]\n\tds_read_b64_tr_b16 v[184:185], v12 offset:2048\n\tds_read_b64_tr_b16 v[186:187], v12 offset:2560\n\tv_mfma_f32_16x16x32_bf16 v[116:119], v[172:175], v[196:199], v[116:119]\n\ts_waitcnt lgkmcnt(10)\n\t"
            "v_mfma_f32_16x16x32_bf16 v[36:39], v[176:179], v[120:123], v[36:39]\n\ts_waitcnt lgkmcnt(9)\n\tv_mfma_f32_16x16x32_bf16 v[48:51], v[176:179], v[124:127], v[48:51]\n\ts_waitcnt lgkmcnt(8)\n\tv_mfma_f32_16x16x32_bf16 v[60:63], v[176:179], v[128:131], v[60:63]\n\tds_read_b64_tr_b16 v[188:189], v12 offset:12288\n\t"
            "ds_read_b64_tr_b16 v[190:191], v12 offset:12800\n\ts_waitcnt lgkmcnt(9)\n\tv_mfma_f32_16x16x32_bf16 v[72:75], v[176:179], v[132:135], v[72:75]\n\tds_read_b128 v[148:151], v7\n\ts_waitcnt lgkmcnt(9)\n\tv_mfma_f32_16x16x32_bf16 v[84:87], v[176:179], v[136:139], v[84:87]\n\t"
            "ds_read_b128 v[152:155], v3 offset:2048\n\ts_waitcnt lgkmcnt(9)\n\tv_mfma_f32_16x16x32_bf16 v[96:99], v[176:179], v[140:143], v[96:99]\n\tds_read_b128 v[156:159], v3 offset:4096\n\ts_waitcnt lgkmcnt(9)\n\tv_mfma_f32_16x16x32_bf16 v[108:111], v[176:179], v[144:147], v[108:111]\n\t"
            "ds_read_b128 v[160:163], v3 offset:6144\n\ts_waitcnt lgkmcnt(8)\n\tv_mfma_f32_16x16x32_bf16 v[40:43], v[180:183], v[120:123], v[40:43]\n\tds_read_b128 v[164:167], v3 offset:8192\n\tv_mfma_f32_16x16x32_bf16 v[52:55], v[180:183], v[124:127], v[52:55]\n\tds_read_b128 v[168:171], v3 offset:10240\n\t"
            "v_mfma_f32_16x16x32_bf16 v[64:67], v[180:183], v[128:131], v[64:67]\n\tds_read_b128 v[172:175], v3 offset:12288\n\tv_mfma_f32_16x16x32_bf16 v[76:79], v[180:183], v[132:135], v[76:79]\n\tds_read_b64_tr_b16 v[192:193], v12 offset:13312\n\tds_read_b64_tr_b16 v[194:195], v12 offset:13824\n\tv_mfma_f32_16x16x32_bf16 v[88:91], v[180:183], v[136:139], v[88:91]\n\t"
            "ds_read_b64_tr_b16 v[196:197], v12 offset:14336\n\tds_read_b64_tr_b16 v[198:199], v12 offset:14848\n\tv_mfma_f32_16x16x32_bf16 v[100:103], v[180:183], v[140:143], v[100:103]\n\tv_mfma_f32_16x16x32_bf16 v[112:115], v[180:183], v[144:147], v[112:115]\n\ts_waitcnt lgkmcnt(13)\n\tv_mfma_f32_16x16x32_bf16 v[44:47], v[120:123], v[184:187], v[44:47]\n\t"
            "v_mfma_f32_16x16x32_bf16 v[56:59], v[124:127], v[184:187], v[56:59]\n\tv_mfma_f32_16x16x32_bf16 v[68:71], v[128:131], v[184:187], v[68:71]\n\tv_mfma_f32_16x16x32_bf16 v[80:83], v[132:135], v[184:187], v[80:83]\n\tv_mfma_f32_16x16x32_bf16 v[92:95], v[136:139], v[184:187], v[92:95]\n\tv_mfma_f32_16x16x32_bf16 v[104:107], v[140:143], v[184:187], v[104:107]\n\tv_mfma_f32_16x16x32_bf16 v[116:119], v[144:147], v[184:187], v[116:119]\n\t"
            "s_waitcnt lgkmcnt(10)\n\tv_mfma_f32_16x16x32_bf16 v[36:39], v[188:191], v[148:151], v[36:39]\n\ts_waitcnt lgkmcnt(9)\n\tv_mfma_f32_16x16x32_bf16 v[48:51], v[188:191], v[152:155], v[48:51]\n\ts_waitcnt lgkmcnt(8)\n\tv_mfma_f32_16x16x32_bf16 v[60:63], v[188:191], v[156:159], v[60:63]\n\t"
            "s_waitcnt lgkmcnt(7)\n\tv_mfma_f32_16x16x32_bf16 v[72:75], v[188:191], v[160:163], v[72:75]\n\ts_waitcnt lgkmcnt(6)\n\tv_mfma_f32_16x16x32_bf16 v[84:87], v[188:191], v[164:167], v[84:87]\n\ts_waitcnt lgkmcnt(5)\n\tv_mfma_f32_16x16x32_bf16 v[96:99], v[188:191], v[168:171], v[96:99]\n\t"
            "s_waitcnt lgkmcnt(4)\n\tv_mfma_f32_16x16x32_bf16 v[108:111], v[188:191], v[172:175], v[108:111]\n\ts_waitcnt lgkmcnt(2)\n\tv_mfma_f32_16x16x32_bf16 v[40:43], v[192:195], v[148:151], v[40:43]\n\tv_mfma_f32_16x16x32_bf16 v[52:55], v[192:195], v[152:155], v[52:55]\n\tv_mfma_f32_16x16x32_bf16 v[64:67], v[192:195], v[156:159], v[64:67]\n\t"
            "v_mfma_f32_16x16x32_bf16 v[76:79], v[192:195], v[160:163], v[76:79]\n\ts_waitcnt lgkmcnt(0)\n\ts_barrier\n\t"
            ST(5)
            "ds_read_b64_tr_b16 v[176:177], v13 offset:0\n\tds_read_b64_tr_b16 v[178:179], v13 offset:512\n\tv_mfma_f32_16x16x32_bf16 v[88:91], v[192:195], v[164:167], v[88:91]\n\tds_read_b128 v[120:123], v8\n\tv_mfma_f32_16x16x32_bf16 v[100:103], v[192:195], v[168:171], v[100:103]\n\tds_read_b128 v[124:127], v4 offset:2048\n\t"
            "v_mfma_f32_16x16x32_bf16 v[112:115], v[192:195], v[172:175], v[112:115]\n\tds_read_b128 v[128:131], v4 offset:4096\n\tv_mfma_f32_16x16x32_bf16 v[44:47], v[148:151], v[196:199], v[44:47]\n\tds_read_b128 v[132:135], v4 offset:6144\n\tv_mfma_f32_16x16x32_bf16 v[56:59], v[152:155], v[196:199], v[56:59]\n\tds_read_b128 v[136:139], v4 offset:8192\n\t"
            "v_mfma_f32_16x16x32_bf16 v[68:71], v[156:159], v[196:199], v[68:71]\n\tds_read_b128 v[140:143], v4 offset:10240\n\tv_mfma_f32_16x16x32_bf16 v[80:83], v[160:163], v[196:199], v[80:83]\n\tds_read_b128 v[144:147], v4 offset:12288\n\tv_mfma_f32_16x16x32_bf16 v[92:95], v[164:167], v[196:199], v[92:95]\n\tds_read_b64_tr_b16 v[180:181], v13 offset:1024\n\t"
            "ds_read_b64_tr_b16 v[182:183], v13 offset:1536\n\tv_mfma_f32_16x16x32_bf16 v[104:107], v[168:171], v[196:199], v[104:107]\n\tds_read_b64_tr_b16 v[184:185], v13 offset:2048\n\tds_read_b64_tr_b16 v[186:187], v13 offset:2560\n\tv_mfma_f32_16x16x32_bf16 v[116:119], v[172:175], v[196:199], v[116:119]\n\ts_waitcnt lgkmcnt(10)\n\t"
            "v_mfma_f32_16x16x32_bf16 v[36:39], v[176:179], v[120:123], v[36:39]\n\ts_waitcnt lgkmcnt(9)\n\tv_mfma_f32_16x16x32_bf16 v[48:51], v[176:179], v[124:127], v[48:51]\n\ts_waitcnt lgkmcnt(8)\n\tv_mfma_f32_16x16x32_bf16 v[60:63], v[176:179], v[128:131], v[60:63]\n\tds_read_b64_tr_b16 v[188:189], v13 offset:12288\n\t"
            "ds_read_b64_tr_b16 v[190:191], v13 offset:12800\n\ts_waitcnt lgkmcnt(9)\n\tv_mfma_f32_16x16x32_bf16 v[72:75], v[176:179], v[132:135], v[72:75]\n\tds_read_b128 v[148:151], v9\n\ts_waitcnt lgkmcnt(9)\n\tv_mfma_f32_16x16x32_bf16 v[84:87], v[176:179], v[136:139], v[84:87]\n\t"
            "ds_read_b128 v[152:155], v5 offset:2048\n\ts_waitcnt lgkmcnt(9)\n\tv_mfma_f32_16x16x32_bf16 v[96:99], v[176:179], v[140:143], v[96:99]\n\tds_read_b128 v[156:159], v5 offset:4096\n\ts_waitcnt lgkmcnt(9)\n\tv_mfma_f32_16x16x32_bf16 v[108:111], v[176:179], v[144:147], v[108:111]\n\t"
            "ds_read_b128 v[160:163], v5 offset:6144\n\ts_waitcnt lgkmcnt(8)\n\tv_mfma_f32_16x16x32_bf16 v[40:43], v[180:183], v[120:123], v[40:43]\n\tds_read_b128 v[164:167], v5 offset:8192\n\tv_mfma_f32_16x16x32_bf16 v[52:55], v[180:183], v[124:127], v[52:55]\n\tds_read_b128 v[168:171], v5 offset:10240\n\t"
            "v_mfma_f32_16x16x32_bf16 v[64:67], v[180:183], v[128:131], v[64:67]\n\tds_read_b128 v[172:175], v5 offset:12288\n\tv_mfma_f32_16x16x32_bf16 v[76:79], v[180:183], v[132:135], v[76:79]\n\tds_read_b64_tr_b16 v[192:193], v13 offset:13312\n\tds_read_b64_tr_b16 v[194:195], v13 offset:13824\n\tv_mfma_f32_16x16x32_bf16 v[88:91], v[180:183], v[136:139], v[88:91]\n\t"
            "ds_read_b64_tr_b16 v[196:197], v13 offset:14336\n\tds_read_b64_tr_b16 v[198:199], v13 offset:14848\n\tv_mfma_f32_16x16x32_bf16 v[100:103], v[180:183], v[140:143], v[100:103]\n\tv_mfma_f32_16x16x32_bf16 v[112:115], v[180:183], v[144:147], v[112:115]\n\ts_waitcnt lgkmcnt(13)\n\tv_mfma_f32_16x16x32_bf16 v[44:47], v[120:123], v[184:187], v[44:47]\n\t"
            "v_mfma_f32_16x16x32_bf16 v[56:59], v[124:127], v[184:187], v[56:59]\n\tv_mfma_f32_16x16x32_bf16 v[68:71], v[128:131], v[184:187], v[68:71]\n\tv_mfma_f32_16x16x32_bf16 v[80:83], v[132:135], v[184:187], v[80:83]\n\tv_mfma_f32_16x16x32_bf16 v[92:95], v[136:139], v[184:187], v[92:95]\n\tv_mfma_f32_16x16x32_bf16 v[104:107], v[140:143], v[184:187], v[104:107]\n\tv_mfma_f32_16x16x32_bf16 v[116:119], v[144:147], v[184:187], v[116:119]\n\t"
            "s_waitcnt lgkmcnt(10)\n\tv_mfma_f32_16x16x32_bf16 v[36:39], v[188:191], v[148:151], v[36:39]\n\ts_waitcnt lgkmcnt(9)\n\tv_mfma_f32_16x16x32_bf16 v[48:51], v[188:191], v[152:155], v[48:51]\n\ts_waitcnt lgkmcnt(8)\n\tv_mfma_f32_16x16x32_bf16 v[60:63], v[188:191], v[156:159], v[60:63]\n\t"
            "s_waitcnt lgkmcnt(7)\n\tv_mfma_f32_16x16x32_bf16 v[72:75], v[188:191], v[160:163], v[72:75]\n\ts_waitcnt lgkmcnt(6)\n\tv_mfma_f32_16x16x32_bf16 v[84:87], v[188:191], v[164:167], v[84:87]\n\ts_waitcnt lgkmcnt(5)\n\tv_mfma_f32_16x16x32_bf16 v[96:99], v[188:191], v[168:171], v[96:99]\n\t"
            "s_waitcnt lgkmcnt(4)\n\tv_mfma_f32_16x16x32_bf16 v[108:111], v[188:191], v[172:175], v[108:111]\n\ts_waitcnt lgkmcnt(2)\n\tv_mfma_f32_16x16x32_bf16 v[40:43], v[192:195], v[148:151], v[40:43]\n\tv_mfma_f32_16x16x32_bf16 v[52:55], v[192:195], v[152:155], v[52:55]\n\tv_mfma_f32_16x16x32_bf16 v[64:67], v[192:195], v[156:159], v[64:67]\n\t"
            "v_mfma_f32_16x16x32_bf16 v[76:79], v[192:195], v[160:163], v[76:79]\n\ts_waitcnt lgkmcnt(0)\n\ts_barrier\n\t"
            ST(7)
            "ds_read_b64_tr_b16 v[176:177], v13 offset:38912\n\tds_read_b64_tr_b16 v[178:179], v13 offset:39424\n\tv_mfma_f32_16x16x32_bf16 v[88:91], v[192:195], v[164:167], v[88:91]\n\tds_read_b128 v[120:123], v10\n\tv_mfma_f32_16x16x32_bf16 v[100:103], v[192:195], v[168:171], v[100:103]\n\tds_read_b128 v[124:127], v4 offset:40960\n\t"
            "v_mfma_f32_16x16x32_bf16 v[112:115], v[192:195], v[172:175], v[112:115]\n\tds_read_b128 v[128:131], v4 offset:43008\n\tv_mfma_f32_16x16x32_bf16 v[44:47], v[148:151], v[196:199], v[44:47]\n\tds_read_b128 v[132:135], v4 offset:45056\n\tv_mfma_f32_16x16x32_bf16 v[56:59], v[152:155], v[196:199], v[56:59]\n\tds_read_b128 v[136:139], v4 offset:47104\n\t"
            "v_mfma_f32_16x16x32_bf16 v[68:71], v[156:159], v[196:199], v[68:71]\n\tds_read_b128 v[140:143], v4 offset:49152\n\tv_mfma_f32_16x16x32_bf16 v[80:83], v[160:163], v[196:199], v[80:83]\n\tds_read_b128 v[144:147], v4 offset:51200\n\tv_mfma_f32_16x16x32_bf16 v[92:95], v[164:167], v[196:199], v[92:95]\n\tds_read_b64_tr_b16 v[180:181], v13 offset:39936\n\t"
            "ds_read_b64_tr_b16 v[182:183], v13 offset:40448\n\tv_mfma_f32_16x16x32_bf16 v[104:107], v[168:171], v[196:199], v[104:107]\n\tds_read_b64_tr_b16 v[184:185], v13 offset:40960\n\tds_read_b64_tr_b16 v[186:187], v13 offset:41472\n\tv_mfma_f32_16x16x32_bf16 v[116:119], v[172:175], v[196:199], v[116:119]\n\ts_waitcnt lgkmcnt(10)\n\t"
            "v_mfma_f32_16x16x32_bf16 v[36:39], v[176:179], v[120:123], v[36:39]\n\ts_waitcnt lgkmcnt(9)\n\tv_mfma_f32_16x16x32_bf16 v[48:51], v[176:179], v[124:127], v[48:51]\n\ts_waitcnt lgkmcnt(8)\n\tv_mfma_f32_16x16x32_bf16 v[60:63], v[176:179], v[128:131], v[60:63]\n\tds_read_b64_tr_b16 v[188:189], v13 offset:51200\n\t"
            "ds_read_b64_tr_b16 v[190:191], v13 offset:51712\n\ts_waitcnt lgkmcnt(9)\n\tv_mfma_f32_16x16x32_bf16 v[72:75], v[176:179], v[132:135], v[72:75]\n\tds_read_b128 v[148:151], v11\n\ts_waitcnt lgkmcnt(9)\n\tv_mfma_f32_16x16x32_bf16 v[84:87], v[176:179], v[136:139], v[84:87]\n\t"
            "ds_read_b128 v[152:155], v5 offset:40960\n\ts_waitcnt lgkmcnt(9)\n\tv_mfma_f32_16x16x32_bf16 v[96:99], v[176:179], v[140:143], v[96:99]\n\tds_read_b128 v[156:159], v5 offset:43008\n\ts_waitcnt lgkmcnt(9)\n\tv_mfma_f32_16x16x32_bf16 v[108:111], v[176:179], v[144:147], v[108:111]\n\t"
            "ds_read_b128 v[160:163], v5 offset:45056\n\ts_waitcnt lgkmcnt(8)\n\tv_mfma_f32_16x16x32_bf16 v[40:43], v[180:183], v[120:123], v[40:43]\n\tds_read_b128 v[164:167], v5 offset:47104\n\tv_mfma_f32_16x16x32_bf16 v[52:55], v[180:183], v[124:127], v[52:55]\n\tds_read_b128 v[168:171], v5 offset:49152\n\t"
            "v_mfma_f32_16x16x32_bf16 v[64:67], v[180:183], v[128:131], v[64:67]\n\tds_read_b128 v[172:175], v5 offset:51200\n\tv_mfma_f32_16x16x32_bf16 v[76:79], v[180:183], v[132:135], v[76:79]\n\tds_read_b64_tr_b16 v[192:193], v13 offset:52224\n\tds_read_b64_tr_b16 v[194:195], v13 offset:52736\n\tv_mfma_f32_16x16x32_bf16 v[88:91], v[180:183], v[136:139], v[88:91]\n\t"
            "ds_read_b64_tr_b16 v[196:197], v13 offset:53248\n\tds_read_b64_tr_b16 v[198:199], v13 offset:53760\n\tv_mfma_f32_16x16x32_bf16 v[100:103], v[180:183], v[140:143], v[100:103]\n\tv_mfma_f32_16x16x32_bf16 v[112:115], v[180:183], v[144:147], v[112:115]\n\ts_waitcnt lgkmcnt(13)\n\tv_mfma_f32_16x16x32_bf16 v[44:47], v[120:123], v[184:187], v[44:47]\n\t"
            "v_mfma_f32_16x16x32_bf16 v[56:59], v[124:127], v[184:187], v[56:59]\n\tv_mfma_f32_16x16x32_bf16 v[68:71], v[128:131], v[184:187], v[68:71]\n\tv_mfma_f32_16x16x32_bf16 v[80:83], v[132:135], v[184:187], v[80:83]\n\tv_mfma_f32_16x16x32_bf16 v[92:95], v[136:139], v[184:187], v[92:95]\n\tv_mfma_f32_16x16x32_bf16 v[104:107], v[140:143], v[184:187], v[104:107]\n\tv_mfma_f32_16x16x32_bf16 v[116:119], v[144:147], v[184:187], v[116:119]\n\t"
            "s_waitcnt lgkmcnt(10)\n\tv_mfma_f32_16x16x32_bf16 v[36:39], v[188:191], v[148:151], v[36:39]\n\ts_waitcnt lgkmcnt(9)\n\tv_mfma_f32_16x16x32_bf16 v[48:51], v[188:191], v[152:155], v[48:51]\n\ts_waitcnt lgkmcnt(8)\n\tv_mfma_f32_16x16x32_bf16 v[60:63], v[188:191], v[156:159], v[60:63]\n\t"
            "s_waitcnt lgkmcnt(7)\n\tv_mfma_f32_16x16x32_bf16 v[72:75], v[188:191], v[160:163], v[72:75]\n\ts_waitcnt lgkmcnt(6)\n\tv_mfma_f32_16x16x32_bf16 v[84:87], v[188:191], v[164:167], v[84:87]\n\ts_waitcnt lgkmcnt(5)\n\tv_mfma_f32_16x16x32_bf16 v[96:99], v[188:191], v[168:171], v[96:99]\n\t"
            "s_waitcnt lgkmcnt(4)\n\tv_mfma_f32_16x16x32_bf16 v[108:111], v[188:191], v[172:175], v[108:111]\n\ts_waitcnt lgkmcnt(2)\n\tv_mfma_f32_16x16x32_bf16 v[40:43], v[192:195], v[148:151], v[40:43]\n\tv_mfma_f32_16x16x32_bf16 v[52:55], v[192:195], v[152:155], v[52:55]\n\tv_mfma_f32_16x16x32_bf16 v[64:67], v[192:195], v[156:159], v[64:67]\n\t"
            "v_mfma_f32_16x16x32_bf16 v[76:79], v[192:195], v[160:163], v[76:79]\n\ts_waitcnt lgkmcnt(0)\n\ts_barrier\n\t"
            ST(9)
            "ds_read_b64_tr_b16 v[176:177], v12 offset:0\n\tds_read_b64_tr_b16 v[178:179], v12 offset:512\n\tv_mfma_f32_16x16x32_bf16 v[88:91], v[192:195], v[164:167], v[88:91]\n\tds_read_b128 v[120:123], v6\n\tv_mfma_f32_16x16x32_bf16 v[100:103], v[192:195], v[168:171], v[100:103]\n\tds_read_b128 v[124:127], v2 offset:2048\n\t"
            "v_mfma_f32_16x16x32_bf16 v[112:115], v[192:195], v[172:175], v[112:115]\n\tds_read_b128 v[128:131], v2 offset:4096\n\tv_mfma_f32_16x16x32_bf16 v[44:47], v[148:151], v[196:199], v[44:47]\n\tds_read_b128 v[132:135], v2 offset:6144\n\tv_mfma_f32_16x16x32_bf16 v[56:59], v[152:155], v[196:199], v[56:59]\n\tds_read_b128 v[136:139], v2 offset:8192\n\t"
            "v_mfma_f32_16x16x32_bf16 v[68:71], v[156:159], v[196:199], v[68:71]\n\tds_read_b128 v[140:143], v2 offset:10240\n\tv_mfma_f32_16x16x32_bf16 v[80:83], v[160:163], v[196:199], v[80:83]\n\tds_read_b128 v[144:147], v2 offset:12288\n\tv_mfma_f32_16x16x32_bf16 v[92:95], v[164:167], v[196:199], v[92:95]\n\tds_read_b64_tr_b16 v[180:181], v12 offset:1024\n\t"
            "ds_read_b64_tr_b16 v[182:183], v12 offset:1536\n\tv_mfma_f32_16x16x32_bf16 v[104:107], v[168:171], v[196:199], v[104:107]\n\tds_read_b64_tr_b16 v[184:185], v12 offset:2048\n\tds_read_b64_tr_b16 v[186:187], v12 offset:2560\n\tv_mfma_f32_16x16x32_bf16 v[116:119], v[172:175], v[196:199], v[116:119]\n\ts_waitcnt lgkmcnt(10)\n\t"
            "v_mfma_f32_16x16x32_bf16 v[36:39], v[176:179], v[120:123], v[36:39]\n\ts_waitcnt lgkmcnt(9)\n\tv_mfma_f32_16x16x32_bf16 v[48:51], v[176:179], v[124:127], v[48:51]\n\ts_waitcnt lgkmcnt(8)\n\tv_mfma_f32_16x16x32_bf16 v[60:63], v[176:179], v[128:131], v[60:63]\n\tds_read_b64_tr_b16 v[188:189], v12 offset:12288\n\t"
            "ds_read_b64_tr_b16 v[190:191], v12 offset:12800\n\ts_waitcnt lgkmcnt(9)\n\tv_mfma_f32_16x16x32_bf16 v[72:75], v[176:179], v[132:135], v[72:75]\n\tds_read_b128 v[148:151], v7\n\ts_waitcnt lgkmcnt(9)\n\tv_mfma_f32_16x16x32_bf16 v[84:87], v[176:179], v[136:139], v[84:87]\n\t"
            "ds_read_b128 v[152:155], v3 offset:2048\n\ts_waitcnt lgkmcnt(9)\n\tv_mfma_f32_16x16x32_bf16 v[96:99], v[176:179], v[140:143], v[96:99]\n\tds_read_b128 v[156:159], v3 offset:4096\n\ts_waitcnt lgkmcnt(9)\n\tv_mfma_f32_16x16x32_bf16 v[108:111], v[176:179], v[144:147], v[108:111]\n\t"
            "ds_read_b128 v[160:163], v3 offset:6144\n\ts_waitcnt lgkmcnt(8)\n\tv_mfma_f32_16x16x32_bf16 v[40:43], v[180:183], v[120:123], v[40:43]\n\tds_read_b128 v[164:167], v3 offset:8192\n\tv_mfma_f32_16x16x32_bf16 v[52:55], v[180:183], v[124:127], v[52:55]\n\tds_read_b128 v[168:171], v3 offset:10240\n\t"
            "v_mfma_f32_16x16x32_bf16 v[64:67], v[180:183], v[128:131], v[64:67]\n\tds_read_b128 v[172:175], v3 offset:12288\n\tv_mfma_f32_16x16x32_bf16 v[76:79], v[180:183], v[132:135], v[76:79]\n\tds_read_b64_tr_b16 v[192:193], v12 offset:13312\n\tds_read_b64_tr_b16 v[194:195], v12 offset:13824\n\tv_mfma_f32_16x16x32_bf16 v[88:91], v[180:183], v[136:139], v[88:91]\n\t"
            "ds_read_b64_tr_b16 v[196:197], v12 offset:14336\n\tds_read_b64_tr_b16 v[198:199], v12 offset:14848\n\tv_mfma_f32_16x16x32_bf16 v[100:103], v[180:183], v[140:143], v[100:103]\n\tv_mfma_f32_16x16x32_bf16 v[112:115], v[180:183], v[144:147], v[112:115]\n\ts_waitcnt lgkmcnt(13)\n\tv_mfma_f32_16x16x32_bf16 v[44:47], v[120:123], v[184:187], v[44:47]\n\t"
            "v_mfma_f32_16x16x32_bf16 v[56:59], v[124:127], v[184:187], v[56:59]\n\tv_mfma_f32_16x16x32_bf16 v[68:71], v[128:131], v[184:187], v[68:71]\n\tv_mfma_f32_16x16x32_bf16 v[80:83], v[132:135], v[184:187], v[80:83]\n\tv_mfma_f32_16x16x32_bf16 v[92:95], v[136:139], v[184:187], v[92:95]\n\tv_mfma_f32_16x16x32_bf16 v[104:107], v[140:143], v[184:187], v[104:107]\n\tv_mfma_f32_16x16x32_bf16 v[116:119], v[144:147], v[184:187], v[116:119]\n\t"
            "s_waitcnt lgkmcnt(10)\n\tv_mfma_f32_16x16x32_bf16 v[36:39], v[188:191], v[148:151], v[36:39]\n\ts_waitcnt lgkmcnt(9)\n\tv_mfma_f32_16x16x32_bf16 v[48:51], v[188:191], v[152:155], v[48:51]\n\ts_waitcnt lgkmcnt(8)\n\tv_mfma_f32_16x16x32_bf16 v[60:63], v[188:191], v[156:159], v[60:63]\n\t"
            "s_waitcnt lgkmcnt(7)\n\tv_mfma_f32_16x16x32_bf16 v[72:75], v[188:191], v[160:163], v[72:75]\n\ts_waitcnt lgkmcnt(6)\n\tv_mfma_f32_16x16x32_bf16 v[84:87], v[188:191], v[164:167], v[84:87]\n\ts_waitcnt lgkmcnt(5)\n\tv_mfma_f32_16x16x32_bf16 v[96:99], v[188:191], v[168:171], v[96:99]\n\t"
            "s_waitcnt lgkmcnt(4)\n\tv_mfma_f32_16x16x32_bf16 v[108:111], v[188:191], v[172:175], v[108:111]\n\ts_waitcnt lgkmcnt(2)\n\tv_mfma_f32_16x16x32_bf16 v[40:43], v[192:195], v[148:151], v[40:43]\n\tv_mfma_f32_16x16x32_bf16 v[52:55], v[192:195], v[152:155], v[52:55]\n\tv_mfma_f32_16x16x32_bf16 v[64:67], v[192:195], v[156:159], v[64:67]\n\t"
            "v_mfma_f32_16x16x32_bf16 v[76:79], v[192:195], v[160:163], v[76:79]\n\ts_waitcnt lgkmcnt(0)\n\ts_barrier\n\t"
            ST(11)
            "ds_read_b64_tr_b16 v[176:177], v13 offset:0\n\tds_read_b64_tr_b16 v[178:179], v13 offset:512\n\tv_mfma_f32_16x16x32_bf16 v[88:91], v[192:195], v[164:167], v[88:91]\n\tds_read_b128 v[120:123], v8\n\tv_mfma_f32_16x16x32_bf16 v[100:103], v[192:195], v[168:171], v[100:103]\n\tds_read_b128 v[124:127], v4 offset:2048\n\t"
            "v_mfma_f32_16x16x32_bf16 v[112:115], v[192:195], v[172:175], v[112:115]\n\tds_read_b128 v[128:131], v4 offset:4096\n\tv_mfma_f32_16x16x32_bf16 v[44:47], v[148:151], v[196:199], v[44:47]\n\tds_read_b128 v[132:135], v4 offset:6144\n\tv_mfma_f32_16x16x32_bf16 v[56:59], v[152:155], v[196:199], v[56:59]\n\tds_read_b128 v[136:139], v4 offset:8192\n\t"
            "v_mfma_f32_16x16x32_bf16 v[68:71], v[156:159], v[196:199], v[68:71]\n\tds_read_b128 v[140:143], v4 offset:10240\n\tv_mfma_f32_16x16x32_bf16 v[80:83], v[160:163], v[196:199], v[80:83]\n\tds_read_b128 v[144:147], v4 offset:12288\n\tv_mfma_f32_16x16x32_bf16 v[92:95], v[164:167], v[196:199], v[92:95]\n\tds_read_b64_tr_b16 v[180:181], v13 offset:1024\n\t"
            "ds_read_b64_tr_b16 v[182:183], v13 offset:1536\n\tv_mfma_f32_16x16x32_bf16 v[104:107], v[168:171], v[196:199], v[104:107]\n\tds_read_b64_tr_b16 v[184:185], v13 offset:2048\n\tds_read_b64_tr_b16 v[186:187], v13 offset:2560\n\tv_mfma_f32_16x16x32_bf16 v[116:119], v[172:175], v[196:199], v[116:119]\n\ts_waitcnt lgkmcnt(10)\n\t"
            "v_mfma_f32_16x16x32_bf16 v[36:39], v[176:179], v[120:123], v[36:39]\n\ts_waitcnt lgkmcnt(9)\n\tv_mfma_f32_16x16x32_bf16 v[48:51], v[176:179], v[124:127], v[48:51]\n\ts_waitcnt lgkmcnt(8)\n\tv_mfma_f32_16x16x32_bf16 v[60:63], v[176:179], v[128:131], v[60:63]\n\tds_read_b64_tr_b16 v[188:189], v13 offset:12288\n\t"
            "ds_read_b64_tr_b16 v[190:191], v13 offset:12800\n\ts_waitcnt lgkmcnt(9)\n\tv_mfma_f32_16x16x32_bf16 v[72:75], v[176:179], v[132:135], v[72:75]\n\tds_read_b128 v[148:151], v9\n\ts_waitcnt lgkmcnt(9)\n\tv_mfma_f32_16x16x32_bf16 v[84:87], v[176:179], v[136:139], v[84:87]\n\t"
            "ds_read_b128 v[152:155], v5 offset:2048\n\ts_waitcnt lgkmcnt(9)\n\tv_mfma_f32_16x16x32_bf16 v[96:99], v[176:179], v[140:143], v[96:99]\n\tds_read_b128 v[156:159], v5 offset:4096\n\ts_waitcnt lgkmcnt(9)\n\tv_mfma_f32_16x16x32_bf16 v[108:111], v[176:179], v[144:147], v[108:111]\n\t"
            "ds_read_b128 v[160:163], v5 offset:6144\n\ts_waitcnt lgkmcnt(8)\n\tv_mfma_f32_16x16x32_bf16 v[40:43], v[180:183], v[120:123], v[40:43]\n\tds_read_b128 v[164:167], v5 offset:8192\n\tv_mfma_f32_16x16x32_bf16 v[52:55], v[180:183], v[124:127], v[52:55]\n\tds_read_b128 v[168:171], v5 offset:10240\n\t"
            "v_mfma_f32_16x16x32_bf16 v[64:67], v[180:183], v[128:131], v[64:67]\n\tds_read_b128 v[172:175], v5 offset:12288\n\tv_mfma_f32_16x16x32_bf16 v[76:79], v[180:183], v[132:135], v[76:79]\n\tds_read_b64_tr_b16 v[192:193], v13 offset:13312\n\tds_read_b64_tr_b16 v[194:195], v13 offset:13824\n\tv_mfma_f32_16x16x32_bf16 v[88:91], v[180:183], v[136:139], v[88:91]\n\t"
            "ds_read_b64_tr_b16 v[196:197], v13 offset:14336\n\tds_read_b64_tr_b16 v[198:199], v13 offset:14848\n\tv_mfma_f32_16x16x32_bf16 v[100:103], v[180:183], v[140:143], v[100:103]\n\tv_mfma_f32_16x16x32_bf16 v[112:115], v[180:183], v[144:147], v[112:115]\n\ts_waitcnt lgkmcnt(13)\n\tv_mfma_f32_16x16x32_bf16 v[44:47], v[120:123], v[184:187], v[44:47]\n\t"
            "v_mfma_f32_16x16x32_bf16 v[56:59], v[124:127], v[184:187], v[56:59]\n\tv_mfma_f32_16x16x32_bf16 v[68:71], v[128:131], v[184:187], v[68:71]\n\tv_mfma_f32_16x16x32_bf16 v[80:83], v[132:135], v[184:187], v[80:83]\n\tv_mfma_f32_16x16x32_bf16 v[92:95], v[136:139], v[184:187], v[92:95]\n\tv_mfma_f32_16x16x32_bf16 v[104:107], v[140:143], v[184:187], v[104:107]\n\tv_mfma_f32_16x16x32_bf16 v[116:119], v[144:147], v[184:187], v[116:119]\n\t"
            "s_waitcnt lgkmcnt(10)\n\tv_mfma_f32_16x16x32_bf16 v[36:39], v[188:191], v[148:151], v[36:39]\n\ts_waitcnt lgkmcnt(9)\n\tv_mfma_f32_16x16x32_bf16 v[48:51], v[188:191], v[152:155], v[48:51]\n\ts_waitcnt lgkmcnt(8)\n\tv_mfma_f32_16x16x32_bf16 v[60:63], v[188:191], v[156:159], v[60:63]\n\t"
            "s_waitcnt lgkmcnt(7)\n\tv_mfma_f32_16x16x32_bf16 v[72:75], v[188:191], v[160:163], v[72:75]\n\ts_waitcnt lgkmcnt(6)\n\tv_mfma_f32_16x16x32_bf16 v[84:87], v[188:191], v[164:167], v[84:87]\n\ts_waitcnt lgkmcnt(5)\n\tv_mfma_f32_16x16x32_bf16 v[96:99], v[188:191], v[168:171], v[96:99]\n\t"
            "s_waitcnt lgkmcnt(4)\n\tv_mfma_f32_16x16x32_bf16 v[108:111], v[188:191], v[172:175], v[108:111]\n\ts_waitcnt lgkmcnt(2)\n\tv_mfma_f32_16x16x32_bf16 v[40:43], v[192:195], v[148:151], v[40:43]\n\tv_mfma_f32_16x16x32_bf16 v[52:55], v[192:195], v[152:155], v[52:55]\n\tv_mfma_f32_16x16x32_bf16 v[64:67], v[192:195], v[156:159], v[64:67]\n\t"
            "v_mfma_f32_16x16x32_bf16 v[76:79], v[192:195], v[160:163], v[76:79]\n\ts_waitcnt lgkmcnt(0)\n\ts_barrier\n\t"
            ST(13)
            "ds_read_b64_tr_b16 v[176:177], v13 offset:38912\n\tds_read_b64_tr_b16 v[178:179], v13 offset:39424\n\tv_mfma_f32_16x16x32_bf16 v[88:91], v[192:195], v[164:167], v[88:91]\n\tds_read_b128 v[120:123], v10\n\tv_mfma_f32_16x16x32_bf16 v[100:103], v[192:195], v[168:171], v[100:103]\n\tds_read_b128 v[124:127], v4 offset:40960\n\t"
            "v_mfma_f32_16x16x32_bf16 v[112:115], v[192:195], v[172:175], v[112:115]\n\tds_read_b128 v[128:131], v4 offset:43008\n\tv_mfma_f32_16x16x32_bf16 v[44:47], v[148:151], v[196:199], v[44:47]\n\tds_read_b128 v[132:135], v4 offset:45056\n\tv_mfma_f32_16x16x32_bf16 v[56:59], v[152:155], v[196:199], v[56:59]\n\tds_read_b128 v[136:139], v4 offset:47104\n\t"
            "v_mfma_f32_16x16x32_bf16 v[68:71], v[156:159], v[196:199], v[68:71]\n\tds_read_b128 v[140:143], v4 offset:49152\n\tv_mfma_f32_16x16x32_bf16 v[80:83], v[160:163], v[196:199], v[80:83]\n\tds_read_b128 v[144:147], v4 offset:51200\n\tv_mfma_f32_16x16x32_bf16 v[92:95], v[164:167], v[196:199], v[92:95]\n\tds_read_b64_tr_b16 v[180:181], v13 offset:39936\n\t"
            "ds_read_b64_tr_b16 v[182:183], v13 offset:40448\n\tv_mfma_f32_16x16x32_bf16 v[104:107], v[168:171], v[196:199], v[104:107]\n\tds_read_b64_tr_b16 v[184:185], v13 offset:40960\n\tds_read_b64_tr_b16 v[186:187], v13 offset:41472\n\tv_mfma_f32_16x16x32_bf16 v[116:119], v[172:175], v[196:199], v[116:119]\n\ts_waitcnt lgkmcnt(10)\n\t"
            "v_mfma_f32_16x16x32_bf16 v[36:39], v[176:179], v[120:123], v[36:39]\n\ts_waitcnt lgkmcnt(9)\n\tv_mfma_f32_16x16x32_bf16 v[48:51], v[176:179], v[124:127], v[48:51]\n\ts_waitcnt lgkmcnt(8)\n\tv_mfma_f32_16x16x32_bf16 v[60:63], v[176:179], v[128:131], v[60:63]\n\tds_read_b64_tr_b16 v[188:189], v13 offset:51200\n\t"
            "ds_read_b64_tr_b16 v[190:191], v13 offset:51712\n\ts_waitcnt lgkmcnt(9)\n\tv_mfma_f32_16x16x32_bf16 v[72:75], v[176:179], v[132:135], v[72:75]\n\tds_read_b128 v[148:151], v11\n\ts_waitcnt lgkmcnt(9)\n\tv_mfma_f32_16x16x32_bf16 v[84:87], v[176:179], v[136:139], v[84:87]\n\t"
            "ds_read_b128 v[152:155], v5 offset:40960\n\ts_waitcnt lgkmcnt(9)\n\tv_mfma_f32_16x16x32_bf16 v[96:99], v[176:179], v[140:143], v[96:99]\n\tds_read_b128 v[156:159], v5 offset:43008\n\ts_waitcnt lgkmcnt(9)\n\tv_mfma_f32_16x16x32_bf16 v[108:111], v[176:179], v[144:147], v[108:111]\n\t"
            "ds_read_b128 v[160:163], v5 offset:45056\n\ts_waitcnt lgkmcnt(8)\n\tv_mfma_f32_16x16x32_bf16 v[40:43], v[180:183], v[120:123], v[40:43]\n\tds_read_b128 v[164:167], v5 offset:47104\n\tv_mfma_f32_16x16x32_bf16 v[52:55], v[180:183], v[124:127], v[52:55]\n\tds_read_b128 v[168:171], v5 offset:49152\n\t"
            "v_mfma_f32_16x16x32_bf16 v[64:67], v[180:183], v[128:131], v[64:67]\n\tds_read_b128 v[172:175], v5 offset:51200\n\tv_mfma_f32_16x16x32_bf16 v[76:79], v[180:183], v[132:135], v[76:79]\n\tds_read_b64_tr_b16 v[192:193], v13 offset:52224\n\tds_read_b64_tr_b16 v[194:195], v13 offset:52736\n\tv_mfma_f32_16x16x32_bf16 v[88:91], v[180:183], v[136:139], v[88:91]\n\t"
            "ds_read_b64_tr_b16 v[196:197], v13 offset:53248\n\tds_read_b64_tr_b16 v[198:199], v13 offset:53760\n\tv_mfma_f32_16x16x32_bf16 v[100:103], v[180:183], v[140:143], v[100:103]\n\tv_mfma_f32_16x16x32_bf16 v[112:115], v[180:183], v[144:147], v[112:115]\n\ts_waitcnt lgkmcnt(13)\n\tv_mfma_f32_16x16x32_bf16 v[44:47], v[120:123], v[184:187], v[44:47]\n\t"
            "v_mfma_f32_16x16x32_bf16 v[56:59], v[124:127], v[184:187], v[56:59]\n\tv_mfma_f32_16x16x32_bf16 v[68:71], v[128:131], v[184:187], v[68:71]\n\tv_mfma_f32_16x16x32_bf16 v[80:83], v[132:135], v[184:187], v[80:83]\n\tv_mfma_f32_16x16x32_bf16 v[92:95], v[136:139], v[184:187], v[92:95]\n\tv_mfma_f32_16x16x32_bf16 v[104:107], v[140:143], v[184:187], v[104:107]\n\tv_mfma_f32_16x16x32_bf16 v[116:119], v[144:147], v[184:187], v[116:119]\n\t"
            "s_waitcnt lgkmcnt(10)\n\tv_mfma_f32_16x16x32_bf16 v[36:39], v[188:191], v[148:151], v[36:39]\n\ts_waitcnt lgkmcnt(9)\n\tv_mfma_f32_16x16x32_bf16 v[48:51], v[188:191], v[152:155], v[48:51]\n\ts_waitcnt lgkmcnt(8)\n\tv_mfma_f32_16x16x32_bf16 v[60:63], v[188:191], v[156:159], v[60:63]\n\t"
            "s_waitcnt lgkmcnt(7)\n\tv_mfma_f32_16x16x32_bf16 v[72:75], v[188:191], v[160:163], v[72:75]\n\ts_waitcnt lgkmcnt(6)\n\tv_mfma_f32_16x16x32_bf16 v[84:87], v[188:191], v[164:167], v[84:87]\n\ts_waitcnt lgkmcnt(5)\n\tv_mfma_f32_16x16x32_bf16 v[96:99], v[188:191], v[168:171], v[96:99]\n\t"
            "s_waitcnt lgkmcnt(4)\n\tv_mfma_f32_16x16x32_bf16 v[108:111], v[188:191], v[172:175], v[108:111]\n\ts_waitcnt lgkmcnt(2)\n\tv_mfma_f32_16x16x32_bf16 v[40:43], v[192:195], v[148:151], v[40:43]\n\tv_mfma_f32_16x16x32_bf16 v[52:55], v[192:195], v[152:155], v[52:55]\n\tv_mfma_f32_16x16x32_bf16 v[64:67], v[192:195], v[156:159], v[64:67]\n\t"
            "v_mfma_f32_16x16x32_bf16 v[76:79], v[192:195], v[160:163], v[76:79]\n\ts_waitcnt lgkmcnt(0)\n\ts_barrier\n\t"
            ST(15)
            "ds_read_b64_tr_b16 v[176:177], v12 offset:0\n\tds_read_b64_tr_b16 v[178:179], v12 offset:512\n\tv_mfma_f32_16x16x32_bf16 v[88:91], v[192:195], v[164:167], v[88:91]\n\tds_read_b128 v[120:123], v6\n\tv_mfma_f32_16x16x32_bf16 v[100:103], v[192:195], v[168:171], v[100:103]\n\tds_read_b128 v[124:127], v2 offset:2048\n\t"
            "v_mfma_f32_16x16x32_bf16 v[112:115], v[192:195], v[172:175], v[112:115]\n\tds_read_b128 v[128:131], v2 offset:4096\n\tv_mfma_f32_16x16x32_bf16 v[44:47], v[148:151], v[196:199], v[44:47]\n\tds_read_b128 v[132:135], v2 offset:6144\n\tv_mfma_f32_16x16x32_bf16 v[56:59], v[152:155], v[196:199], v[56:59]\n\tds_read_b128 v[136:139], v2 offset:8192\n\t"
            "v_mfma_f32_16x16x32_bf16 v[68:71], v[156:159], v[196:199], v[68:71]\n\tds_read_b128 v[140:143], v2 offset:10240\n\tv_mfma_f32_16x16x32_bf16 v[80:83], v[160:163], v[196:199], v[80:83]\n\tds_read_b128 v[144:147], v2 offset:12288\n\tv_mfma_f32_16x16x32_bf16 v[92:95], v[164:167], v[196:199], v[92:95]\n\tds_read_b64_tr_b16 v[180:181], v12 offset:1024\n\t"
            "ds_read_b64_tr_b16 v[182:183], v12 offset:1536\n\tv_mfma_f32_16x16x32_bf16 v[104:107], v[168:171], v[196:199], v[104:107]\n\tv_mfma_f32_16x16x32_bf16 v[116:119], v[172:175], v[196:199], v[116:119]\n\ts_waitcnt lgkmcnt(8)\n\tv_mfma_f32_16x16x32_bf16 v[36:39], v[176:179], v[120:123], v[36:39]\n\ts_waitcnt lgkmcnt(7)\n\t"
            "v_mfma_f32_16x16x32_bf16 v[48:51], v[176:179], v[124:127], v[48:51]\n\ts_waitcnt lgkmcnt(6)\n\tv_mfma_f32_16x16x32_bf16 v[60:63], v[176:179], v[128:131], v[60:63]\n\tds_read_b64_tr_b16 v[188:189], v12 offset:12288\n\tds_read_b64_tr_b16 v[190:191], v12 offset:12800\n\ts_waitcnt lgkmcnt(7)\n\t"
            "v_mfma_f32_16x16x32_bf16 v[72:75], v[176:179], v[132:135], v[72:75]\n\tds_read_b128 v[148:151], v7\n\ts_waitcnt lgkmcnt(7)\n\tv_mfma_f32_16x16x32_bf16 v[84:87], v[176:179], v[136:139], v[84:87]\n\tds_read_b128 v[152:155], v3 offset:2048\n\ts_waitcnt lgkmcnt(7)\n\t"
            "v_mfma_f32_16x16x32_bf16 v[96:99], v[176:179], v[140:143], v[96:99]\n\tds_read_b128 v[156:159], v3 offset:4096\n\ts_waitcnt lgkmcnt(7)\n\tv_mfma_f32_16x16x32_bf16 v[108:111], v[176:179], v[144:147], v[108:111]\n\tds_read_b128 v[160:163], v3 offset:6144\n\ts_waitcnt lgkmcnt(6)\n\t"
            "v_mfma_f32_16x16x32_bf16 v[40:43], v[180:183], v[120:123], v[40:43]\n\tds_read_b128 v[164:167], v3 offset:8192\n\tv_mfma_f32_16x16x32_bf16 v[52:55], v[180:183], v[124:127], v[52:55]\n\tds_read_b128 v[168:171], v3 offset:10240\n\tv_mfma_f32_16x16x32_bf16 v[64:67], v[180:183], v[128:131], v[64:67]\n\tds_read_b128 v[172:175], v3 offset:12288\n\t"
            "v_mfma_f32_16x16x32_bf16 v[76:79], v[180:183], v[132:135], v[76:79]\n\tds_read_b64_tr_b16 v[192:193], v12 offset:13312\n\tds_read_b64_tr_b16 v[194:195], v12 offset:13824\n\tv_mfma_f32_16x16x32_bf16 v[88:91], v[180:183], v[136:139], v[88:91]\n\tv_mfma_f32_16x16x32_bf16 v[100:103], v[180:183], v[140:143], v[100:103]\n\tv_mfma_f32_16x16x32_bf16 v[112:115], v[180:183], v[144:147], v[112:115]\n\t"
            "s_waitcnt lgkmcnt(8)\n\tv_mfma_f32_16x16x32_bf16 v[36:39], v[188:191], v[148:151], v[36:39]\n\ts_waitcnt lgkmcnt(7)\n\tv_mfma_f32_16x16x32_bf16 v[48:51], v[188:191], v[152:155], v[48:51]\n\ts_waitcnt lgkmcnt(6)\n\tv_mfma_f32_16x16x32_bf16 v[60:63], v[188:191], v[156:159], v[60:63]\n\t"
            "s_waitcnt lgkmcnt(5)\n\tv_mfma_f32_16x16x32_bf16 v[72:75], v[188:191], v[160:163], v[72:75]\n\ts_waitcnt lgkmcnt(0)\n\ts_barrier\n\t"
            ST(17)
            "ds_read_b64_tr_b16 v[176:177], v13 offset:0\n\tds_read_b64_tr_b16 v[178:179], v13 offset:512\n\tv_mfma_f32_16x16x32_bf16 v[84:87], v[188:191], v[164:167], v[84:87]\n\tds_read_b128 v[120:123], v8\n\tv_mfma_f32_16x16x32_bf16 v[96:99], v[188:191], v[168:171], v[96:99]\n\tds_read_b128 v[124:127], v4 offset:2048\n\t"
            "v_mfma_f32_16x16x32_bf16 v[108:111], v[188:191], v[172:175], v[108:111]\n\tds_read_b128 v[128:131], v4 offset:4096\n\tv_mfma_f32_16x16x32_bf16 v[40:43], v[192:195], v[148:151], v[40:43]\n\tds_read_b128 v[132:135], v4 offset:6144\n\tv_mfma_f32_16x16x32_bf16 v[52:55], v[192:195], v[152:155], v[52:55]\n\tds_read_b128 v[136:139], v4 offset:8192\n\t"
            "v_mfma_f32_16x16x32_bf16 v[64:67], v[192:195], v[156:159], v[64:67]\n\tds_read_b128 v[140:143], v4 offset:10240\n\tv_mfma_f32_16x16x32_bf16 v[76:79], v[192:195], v[160:163], v[76:79]\n\tds_read_b128 v[144:147], v4 offset:12288\n\tv_mfma_f32_16x16x32_bf16 v[88:91], v[192:195], v[164:167], v[88:91]\n\tds_read_b64_tr_b16 v[180:181], v13 offset:1024\n\t"
            "ds_read_b64_tr_b16 v[182:183], v13 offset:1536\n\tv_mfma_f32_16x16x32_bf16 v[100:103], v[192:195], v[168:171], v[100:103]\n\tds_read_b64_tr_b16 v[184:185], v13 offset:2048\n\tds_read_b64_tr_b16 v[186:187], v13 offset:2560\n\tv_mfma_f32_16x16x32_bf16 v[112:115], v[192:195], v[172:175], v[112:115]\n\tds_read2_b32 v[208:209], v14 offset1:16\n\t"
            SA(2) PRIA
            "s_mov_b64 vcc, s[18:19]\n\ts_bitcmp1_b32 9, s69\n\tds_read_b64_tr_b16 v[188:189], v13 offset:12288\n\tv_mov_b32 v252, -1\n\tds_read_b64_tr_b16 v[190:191], v13 offset:12800\n\tv_mov_b32 v254, -1\n\t"
            "ds_read_b128 v[148:151], v9\n\ts_waitcnt lgkmcnt(6)\n\tv_mfma_f32_16x16x32_bf16 v[40:43], v[180:183], v[120:123], v[40:43]\n\tds_read_b128 v[152:155], v5 offset:2048\n\tv_mfma_f32_16x16x32_bf16 v[36:39], v[176:179], v[120:123], v[36:39]\n\tds_read_b128 v[156:159], v5 offset:4096\n\t"
            "v_mfma_f32_16x16x32_bf16 v[52:55], v[180:183], v[124:127], v[52:55]\n\tds_read_b128 v[160:163], v5 offset:6144\n\tv_mfma_f32_16x16x32_bf16 v[48:51], v[176:179], v[124:127], v[48:51]\n\tds_read_b128 v[164:167], v5 offset:8192\n\tv_mfma_f32_16x16x32_bf16 v[64:67], v[180:183], v[128:131], v[64:67]\n\tds_read_b128 v[168:171], v5 offset:10240\n\t"
            "v_mfma_f32_16x16x32_bf16 v[60:63], v[176:179], v[128:131], v[60:63]\n\tds_read_b128 v[172:175], v5 offset:12288\n\tv_mfma_f32_16x16x32_bf16 v[76:79], v[180:183], v[132:135], v[76:79]\n\tds_read_b64_tr_b16 v[192:193], v13 offset:13312\n\ts_waitcnt lgkmcnt(7)\n\tv_mfma_f32_16x16x32_bf16 v[36:39], v[188:191], v[148:151], v[36:39]\n\t"
            "ds_read_b64_tr_b16 v[194:195], v13 offset:13824\n\tds_read_b64_tr_b16 v[196:197], v13 offset:14336\n\ts_waitcnt lgkmcnt(8)\n\tv_mfma_f32_16x16x32_bf16 v[48:51], v[188:191], v[152:155], v[48:51]\n\tds_read_b64_tr_b16 v[198:199], v13 offset:14848\n\tds_read2_b32 v[210:211], v14 offset0:32 offset1:48\n\t"
            "s_waitcnt lgkmcnt(9)\n\tv_mfma_f32_16x16x32_bf16 v[60:63], v[188:191], v[156:159], v[60:63]\n\tv_mov_b32_dpp v200, v36 row_newbcast:0 row_mask:0xf bank_mask:0xf bound_ctrl:1\n\tds_read2_b32 v[212:213], v14 offset0:64 offset1:80\n\tv_mfma_f32_16x16x32_bf16 v[72:75], v[176:179], v[132:135], v[72:75]\n\tv_mov_b32_dpp v201, v37 row_newbcast:0 row_mask:0xf bank_mask:0xf bound_ctrl:1\n\t"
            "ds_read_b32 v214, v14 offset:384\n\ts_waitcnt lgkmcnt(8)\n\tv_mfma_f32_16x16x32_bf16 v[72:75], v[188:191], v[160:163], v[72:75]\n\tv_mov_b32_dpp v202, v38 row_newbcast:0 row_mask:0xf bank_mask:0xf bound_ctrl:1\n\tv_mov_b32_dpp v203, v39 row_newbcast:0 row_mask:0xf bank_mask:0xf bound_ctrl:1\n\tv_mfma_f32_16x16x32_bf16 v[88:91], v[180:183], v[136:139], v[88:91]\n\t"
            "v_fmac_f32 v36, v208, v200\n\tv_fmac_f32 v37, v208, v201\n\tv_mfma_f32_16x16x32_bf16 v[84:87], v[176:179], v[136:139], v[84:87]\n\tv_fmac_f32 v38, v208, v202\n\tv_fmac_f32 v39, v208, v203\n\ts_waitcnt lgkmcnt(4)\n\t"
            "v_mfma_f32_16x16x32_bf16 v[40:43], v[192:195], v[148:151], v[40:43]\n\tv_cvt_pk_bf16_f32 v250, v36, v37\n\tv_cvt_pk_bf16_f32 v251, v38, v39\n\tv_mfma_f32_16x16x32_bf16 v[52:55], v[192:195], v[152:155], v[52:55]\n\tv_and_b32 v36, v250, v18\n\tv_and_b32 v37, v251, v18\n\t"
            "v_mfma_f32_16x16x32_bf16 v[64:67], v[192:195], v[156:159], v[64:67]\n\ts_nop 1\n\tv_mov_b32_dpp v204, v40 row_newbcast:0 row_mask:0xf bank_mask:0xf bound_ctrl:1\n\tv_mov_b32_dpp v205, v41 row_newbcast:0 row_mask:0xf bank_mask:0xf bound_ctrl:1\n\tv_mfma_f32_16x16x32_bf16 v[76:79], v[192:195], v[160:163], v[76:79]\n\tv_mov_b32_dpp v206, v42 row_newbcast:0 row_mask:0xf bank_mask:0xf bound_ctrl:1\n\t"
            "v_mov_b32_dpp v207, v43 row_newbcast:0 row_mask:0xf bank_mask:0xf bound_ctrl:1\n\tv_mfma_f32_16x16x32_bf16 v[88:91], v[192:195], v[164:167], v[88:91]\n\tv_fmac_f32 v40, v208, v204\n\tv_fmac_f32 v41, v208, v205\n\tv_mfma_f32_16x16x32_bf16 v[84:87], v[188:191], v[164:167], v[84:87]\n\tv_fmac_f32 v42, v208, v206\n\t"
            "v_fmac_f32 v43, v208, v207\n\tv_mfma_f32_16x16x32_bf16 v[100:103], v[180:183], v[140:143], v[100:103]\n\tv_cvt_pk_bf16_f32 v40, v40, v41\n\tv_cvt_pk_bf16_f32 v41, v42, v43\n\tv_mfma_f32_16x16x32_bf16 v[96:99], v[176:179], v[140:143], v[96:99]\n\tv_cndmask_b32 v42, v40, v40, vcc\n\t"
            "v_cndmask_b32 v43, v41, v41, vcc\n\tv_mfma_f32_16x16x32_bf16 v[100:103], v[192:195], v[168:171], v[100:103]\n\tv_and_b32 v38, v250, v19\n\tv_and_b32 v39, v251, v19\n\tv_mfma_f32_16x16x32_bf16 v[96:99], v[188:191], v[168:171], v[96:99]\n\tv_and_b32 v222, v250, v20\n\t"
            "v_and_b32 v223, v251, v20\n\tv_mfma_f32_16x16x32_bf16 v[218:221], v[40:43], v[36:39], 0\n\tv_and_b32 v224, v250, v21\n\tv_and_b32 v225, v251, v21\n\tv_mfma_f32_16x16x32_bf16 v[112:115], v[180:183], v[144:147], v[112:115]\n\tv_fmac_f32 v52, v209, v204\n\t"
            "v_fmac_f32 v53, v209, v205\n\tv_mfma_f32_16x16x32_bf16 v[226:229], v[40:43], v[222:225], 0\n\ts_nop 1\n\tv_cndmask_b32 v36, v220, v218, s[14:15]\n\tv_cndmask_b32 v37, v221, v219, s[16:17]\n\tv_mfma_f32_16x16x32_bf16 v[108:111], v[176:179], v[144:147], v[108:111]\n\t"
            "v_fmac_f32 v54, v209, v206\n\tv_fmac_f32 v55, v209, v207\n\tv_mfma_f32_16x16x32_bf16 v[112:115], v[192:195], v[172:175], v[112:115]\n\tv_cndmask_b32 v38, v228, v226, s[14:15]\n\tv_cndmask_b32 v39, v229, v227, s[16:17]\n\tv_mfma_f32_16x16x32_bf16 v[108:111], v[188:191], v[172:175], v[108:111]\n\t"
            "v_fmac_f32 v48, v209, v200\n\tv_fmac_f32 v49, v209, v201\n\tv_fmac_f32 v50, v209, v202\n\tv_fmac_f32 v51, v209, v203\n\tv_cvt_pk_bf16_f32 v52, v52, v53\n\tv_cvt_pk_bf16_f32 v53, v54, v55\n\t"
            "v_cvt_pk_bf16_f32 v250, v48, v49\n\tv_cvt_pk_bf16_f32 v251, v50, v51\n\tv_cndmask_b32 v54, v40, v52, vcc\n\tv_cndmask_b32 v55, v41, v53, vcc\n\tv_and_b32 v48, v250, v18\n\tv_and_b32 v49, v251, v18\n\t"
            "v_and_b32 v50, v250, v19\n\tv_and_b32 v51, v251, v19\n\tv_and_b32 v234, v250, v20\n\tv_and_b32 v235, v251, v20\n\tv_mfma_f32_16x16x32_bf16 v[230:233], v[52:55], v[48:51], 0\n\tv_and_b32 v236, v250, v21\n\t"
            "v_and_b32 v237, v251, v21\n\ts_waitcnt lgkmcnt(0)\n\tv_fmac_f32 v64, v210, v204\n\tv_fmac_f32 v65, v210, v205\n\tv_mfma_f32_16x16x32_bf16 v[238:241], v[52:55], v[234:237], 0\n\tv_fmac_f32 v66, v210, v206\n\t"
            "s_nop 0\n\tv_cndmask_b32 v48, v232, v230, s[14:15]\n\tv_cndmask_b32 v49, v233, v231, s[16:17]\n\tv_fmac_f32 v67, v210, v207\n\tv_fmac_f32 v60, v210, v200\n\tv_fmac_f32 v61, v210, v201\n\t"
            "v_fmac_f32 v62, v210, v202\n\tv_cndmask_b32 v50, v240, v238, s[14:15]\n\tv_cndmask_b32 v51, v241, v239, s[16:17]\n\tv_fmac_f32 v63, v210, v203\n\tv_cvt_pk_bf16_f32 v64, v64, v65\n\tv_cvt_pk_bf16_f32 v65, v66, v67\n\t"
            "v_cvt_pk_bf16_f32 v250, v60, v61\n\tv_cvt_pk_bf16_f32 v251, v62, v63\n\tv_cndmask_b32 v66, v52, v64, vcc\n\tv_cndmask_b32 v67, v53, v65, vcc\n\tv_and_b32 v60, v250, v18\n\tv_and_b32 v61, v251, v18\n\t"
            "v_and_b32 v62, v250, v19\n\tv_and_b32 v63, v251, v19\n\tv_and_b32 v246, v250, v20\n\tv_and_b32 v247, v251, v20\n\tv_mfma_f32_16x16x32_bf16 v[242:245], v[64:67], v[60:63], 0\n\tv_and_b32 v248, v250, v21\n\t"
            "v_and_b32 v249, v251, v21\n\tv_fmac_f32 v76, v211, v204\n\tv_fmac_f32 v77, v211, v205\n\tv_mfma_f32_16x16x32_bf16 v[218:221], v[64:67], v[246:249], 0\n\tv_fmac_f32 v78, v211, v206\n\ts_nop 1\n\t"
            "v_cndmask_b32 v60, v244, v242, s[14:15]\n\tv_cndmask_b32 v61, v245, v243, s[16:17]\n\tv_fmac_f32 v79, v211, v207\n\tv_fmac_f32 v72, v211, v200\n\tv_fmac_f32 v73, v211, v201\n\tv_fmac_f32 v74, v211, v202\n\t"
            "v_cndmask_b32 v62, v220, v218, s[14:15]\n\tv_cndmask_b32 v63, v221, v219, s[16:17]\n\tv_fmac_f32 v75, v211, v203\n\tv_cvt_pk_bf16_f32 v76, v76, v77\n\tv_cvt_pk_bf16_f32 v77, v78, v79\n\tv_cvt_pk_bf16_f32 v250, v72, v73\n\t"
            "v_cvt_pk_bf16_f32 v251, v74, v75\n\tv_cndmask_b32 v78, v64, v76, vcc\n\tv_cndmask_b32 v79, v65, v77, vcc\n\tv_and_b32 v72, v250, v18\n\tv_and_b32 v73, v251, v18\n\tv_and_b32 v74, v250, v19\n\t"
            "v_and_b32 v75, v251, v19\n\tv_mfma_f32_16x16x32_bf16 v[44:47], v[120:123], v[184:187], v[44:47]\n\tds_read_b128 v[120:123], v6\n\tv_and_b32 v226, v250, v20\n\tv_mfma_f32_16x16x32_bf16 v[222:225], v[76:79], v[72:75], 0\n\tv_and_b32 v227, v251, v20\n\t"
            "v_and_b32 v228, v250, v21\n\tv_mfma_f32_16x16x32_bf16 v[56:59], v[124:127], v[184:187], v[56:59]\n\tds_read_b128 v[124:127], v2 offset:2048\n\tv_and_b32 v229, v251, v21\n\tv_mfma_f32_16x16x32_bf16 v[68:71], v[128:131], v[184:187], v[68:71]\n\tds_read_b128 v[128:131], v2 offset:4096\n\t"
            "s_nop 0\n\tv_cndmask_b32 v72, v224, v222, s[14:15]\n\tv_mfma_f32_16x16x32_bf16 v[230:233], v[76:79], v[226:229], 0\n\tv_cndmask_b32 v73, v225, v223, s[16:17]\n\ts_cbranch_scc0 1f\n\tv_cvt_pk_bf16_f32 v253, v36, v37\n\t"
            "v_min_u32 v253, -2, v253\n\tv_cvt_pk_bf16_f32 v255, v48, v49\n\tv_min_u32 v255, -2, v255\n\tbuffer_store_dwordx4 v[252:255], v17, s[44:47], s20 offen sc1\n\ts_nop 1\n\tv_cvt_pk_bf16_f32 v253, v60, v61\n\t"
            "v_min_u32 v253, -2, v253\n\tv_cvt_pk_bf16_f32 v255, v72, v73\n\tv_min_u32 v255, -2, v255\n\tbuffer_store_dwordx4 v[252:255], v17, s[44:47], s21 offen sc1\n\t1:\nv_mfma_f32_16x16x32_bf16 v[80:83], v[132:135], v[184:187], v[80:83]\n\t"
            "ds_read_b128 v[132:135], v2 offset:6144\n\tv_fmac_f32 v88, v212, v204\n\tv_mfma_f32_16x16x32_bf16 v[92:95], v[136:139], v[184:187], v[92:95]\n\tds_read_b128 v[136:139], v2 offset:8192\n\ts_nop 0\n\tv_cndmask_b32 v74, v232, v230, s[14:15]\n\t"
            "v_mfma_f32_16x16x32_bf16 v[104:107], v[140:143], v[184:187], v[104:107]\n\tds_read_b128 v[140:143], v2 offset:10240\n\tv_cndmask_b32 v75, v233, v231, s[16:17]\n\tv_mfma_f32_16x16x32_bf16 v[116:119], v[144:147], v[184:187], v[116:119]\n\tds_read_b64_tr_b16 v[184:185], v12 offset:2048\n\tv_fmac_f32 v89, v212, v205\n\t"
            "ds_read_b64_tr_b16 v[186:187], v12 offset:2560\n\tv_mfma_f32_16x16x32_bf16 v[44:47], v[148:151], v[196:199], v[44:47]\n\tds_read_b128 v[148:151], v7\n\tv_fmac_f32 v90, v212, v206\n\tv_mfma_f32_16x16x32_bf16 v[56:59], v[152:155], v[196:199], v[56:59]\n\tds_write_b64 v28, v[36:37] offset:0\n\t"
            "v_fmac_f32 v91, v212, v207\n\tv_mfma_f32_16x16x32_bf16 v[68:71], v[156:159], v[196:199], v[68:71]\n\tv_fmac_f32 v84, v212, v200\n\tv_fmac_f32 v85, v212, v201\n\tds_write_b64 v29, v[38:39] offset:0\n\tv_mfma_f32_16x16x32_bf16 v[80:83], v[160:163], v[196:199], v[80:83]\n\t"
            "v_fmac_f32 v86, v212, v202\n\tv_fmac_f32 v87, v212, v203\n\tv_mfma_f32_16x16x32_bf16 v[92:95], v[164:167], v[196:199], v[92:95]\n\tds_write_b64 v28, v[48:49] offset:512\n\tv_cvt_pk_bf16_f32 v88, v88, v89\n\tv_mfma_f32_16x16x32_bf16 v[104:107], v[168:171], v[196:199], v[104:107]\n\t"
            "v_cvt_pk_bf16_f32 v89, v90, v91\n\tv_cvt_pk_bf16_f32 v250, v84, v85\n\tds_write_b64 v29, v[50:51] offset:512\n\tv_mfma_f32_16x16x32_bf16 v[116:119], v[172:175], v[196:199], v[116:119]\n\tv_cvt_pk_bf16_f32 v251, v86, v87\n\tv_cndmask_b32 v90, v76, v88, vcc\n\t"
            "s_waitcnt lgkmcnt(4)\n\tv_mfma_f32_16x16x32_bf16 v[44:47], v[120:123], v[184:187], v[44:47]\n\tds_write_b64 v28, v[60:61] offset:1024\n\tv_cndmask_b32 v91, v77, v89, vcc\n\tv_mfma_f32_16x16x32_bf16 v[56:59], v[124:127], v[184:187], v[56:59]\n\tv_and_b32 v84, v250, v18\n\t"
            "v_and_b32 v85, v251, v18\n\tds_write_b64 v29, v[62:63] offset:1024\n\tv_mfma_f32_16x16x32_bf16 v[68:71], v[128:131], v[184:187], v[68:71]\n\tv_and_b32 v86, v250, v19\n\tv_and_b32 v87, v251, v19\n\tv_mfma_f32_16x16x32_bf16 v[80:83], v[132:135], v[184:187], v[80:83]\n\t"
            "ds_write_b64 v28, v[72:73] offset:1536\n\tv_and_b32 v238, v250, v20\n\tv_mfma_f32_16x16x32_bf16 v[234:237], v[88:91], v[84:87], 0\n\tv_and_b32 v239, v251, v20\n\tv_and_b32 v240, v250, v21\n\tds_write_b64 v29, v[74:75] offset:1536\n\t"
            "v_and_b32 v241, v251, v21\n\tv_fmac_f32 v100, v213, v204\n\tv_fmac_f32 v101, v213, v205\n\tv_mfma_f32_16x16x32_bf16 v[242:245], v[88:91], v[238:241], 0\n\tds_read_b64_tr_b16 v[196:197], v12 offset:14336\n\tv_cndmask_b32 v84, v236, v234, s[14:15]\n\t"
            "ds_read_b64_tr_b16 v[198:199], v12 offset:14848\n\tv_cndmask_b32 v85, v237, v235, s[16:17]\n\tds_read_b128 v[152:155], v3 offset:2048\n\tv_fmac_f32 v102, v213, v206\n\ts_waitcnt lgkmcnt(0)\n\ts_barrier\n\t"
            "v_cndmask_b32 v86, v244, v242, s[14:15]\n\tds_write_b64 v28, v[84:85] offset:38912\n\tv_cndmask_b32 v87, v245, v243, s[16:17]\n\tv_fmac_f32 v103, v213, v207\n\tv_fmac_f32 v96, v213, v200\n\tv_fmac_f32 v97, v213, v201\n\t"
            "v_fmac_f32 v98, v213, v202\n\tds_write_b64 v29, v[86:87] offset:38912\n\tv_fmac_f32 v99, v213, v203\n\tv_cvt_pk_bf16_f32 v100, v100, v101\n\tv_cvt_pk_bf16_f32 v101, v102, v103\n\tv_cvt_pk_bf16_f32 v250, v96, v97\n\t"
            "v_cvt_pk_bf16_f32 v251, v98, v99\n\tds_read_b128 v[156:159], v3 offset:4096\n\tv_cndmask_b32 v102, v88, v100, vcc\n\tv_cndmask_b32 v103, v89, v101, vcc\n\tv_and_b32 v96, v250, v18\n\tds_read_b128 v[36:39], v15 offset:0\n\t"
            "v_and_b32 v97, v251, v18\n\tv_and_b32 v98, v250, v19\n\tv_and_b32 v99, v251, v19\n\tds_read_b128 v[160:163], v3 offset:6144\n\tv_and_b32 v218, v250, v20\n\tv_mfma_f32_16x16x32_bf16 v[246:249], v[100:103], v[96:99], 0\n\t"
            "ds_read_b128 v[48:51], v15 offset:64\n\tv_and_b32 v219, v251, v20\n\tv_mfma_f32_16x16x32_bf16 v[44:47], v[148:151], v[196:199], v[44:47]\n\tds_read_b128 v[60:63], v15 offset:128\n\tv_and_b32 v220, v250, v21\n\tv_mfma_f32_16x16x32_bf16 v[56:59], v[152:155], v[196:199], v[56:59]\n\t"
            "ds_read_b128 v[72:75], v15 offset:192\n\tv_and_b32 v221, v251, v21\n\tv_cndmask_b32 v96, v248, v246, s[14:15]\n\tv_cndmask_b32 v97, v249, v247, s[16:17]\n\tds_write_b64 v28, v[96:97] offset:39424\n\tv_mfma_f32_16x16x32_bf16 v[222:225], v[100:103], v[218:221], 0\n\t"
            "v_fmac_f32 v112, v214, v204\n\tv_fmac_f32 v113, v214, v205\n\ts_waitcnt lgkmcnt(5)\n\tv_mfma_f32_16x16x32_bf16 v[68:71], v[156:159], v[196:199], v[68:71]\n\tds_read_b128 v[144:147], v2 offset:12288\n\tv_fmac_f32 v114, v214, v206\n\t"
            "v_fmac_f32 v115, v214, v207\n\ts_nop 0\n\tv_cndmask_b32 v98, v224, v222, s[14:15]\n\tds_read_b128 v[164:167], v3 offset:8192\n\ts_waitcnt lgkmcnt(5)\n\tv_mfma_f32_16x16x32_bf16 v[80:83], v[160:163], v[196:199], v[80:83]\n\t"
            "v_cndmask_b32 v99, v225, v223, s[16:17]\n\tds_write_b64 v29, v[98:99] offset:39424\n\tv_fmac_f32 v108, v214, v200\n\tv_fmac_f32 v109, v214, v201\n\tv_fmac_f32 v110, v214, v202\n\tv_fmac_f32 v111, v214, v203\n\t"
            "v_cvt_pk_bf16_f32 v112, v112, v113\n\tds_read_b128 v[168:171], v3 offset:10240\n\tv_cvt_pk_bf16_f32 v113, v114, v115\n\tv_cvt_pk_bf16_f32 v250, v108, v109\n\tv_cvt_pk_bf16_f32 v251, v110, v111\n\tds_read_b128 v[172:175], v3 offset:12288\n\t"
            "v_cndmask_b32 v114, v100, v112, vcc\n\tv_cndmask_b32 v115, v101, v113, vcc\n\tv_and_b32 v108, v250, v18\n\tv_and_b32 v109, v251, v18\n\tv_and_b32 v110, v250, v19\n\tv_and_b32 v111, v251, v19\n\t"
            "v_and_b32 v230, v250, v20\n\tv_and_b32 v231, v251, v20\n\tv_mfma_f32_16x16x32_bf16 v[226:229], v[112:115], v[108:111], 0\n\tv_and_b32 v232, v250, v21\n\tv_and_b32 v233, v251, v21\n\tv_mfma_f32_16x16x32_bf16 v[92:95], v[136:139], v[184:187], v[92:95]\n\t"
            "v_mov_b32 v216, v44\n\tv_mov_b32 v217, v44\n\tv_mfma_f32_16x16x32_bf16 v[234:237], v[112:115], v[230:233], 0\n\ts_nop 1\n\tv_cndmask_b32 v108, v228, v226, s[14:15]\n\tv_cndmask_b32 v109, v229, v227, s[16:17]\n\t"
            "ds_write_b64 v28, v[108:109] offset:39936\n\ts_cbranch_scc0 1f\n\tv_cvt_pk_bf16_f32 v253, v84, v85\n\tv_min_u32 v253, -2, v253\n\tv_cvt_pk_bf16_f32 v255, v96, v97\n\tv_min_u32 v255, -2, v255\n\t"
            "buffer_store_dwordx4 v[252:255], v17, s[44:47], s22 offen sc1\n\ts_nop 1\n\tv_cvt_pk_bf16_f32 v253, v108, v109\n\tv_min_u32 v253, -2, v253\n\tbuffer_store_dwordx4 v[252:255], v17, s[44:47], s23 offen sc1\n\t1:\n"
            "v_mfma_f32_16x16x32_bf16 v[104:107], v[140:143], v[184:187], v[104:107]\n\tv_permlane32_swap_b32 v216, v217\n\tv_cndmask_b32 v110, v236, v234, s[14:15]\n\ts_waitcnt lgkmcnt(1)\n\tv_mfma_f32_16x16x32_bf16 v[116:119], v[144:147], v[184:187], v[116:119]\n\tv_cndmask_b32 v111, v237, v235, s[16:17]\n\t"
            "ds_write_b64 v29, v[110:111] offset:39936\n\tv_mfma_f32_16x16x32_bf16 v[92:95], v[164:167], v[196:199], v[92:95]\n\tv_mov_b32 v217, v216\n\tv_mov_b32 v218, 0\n\tv_mfma_f32_16x16x32_bf16 v[104:107], v[168:171], v[196:199], v[104:107]\n\tds_write_b32 v33, v33\n\t"
            SA(3)
            "v_permlane16_swap_b32 v216, v217\n\tv_mfma_f32_16x16x32_bf16 v[116:119], v[172:175], v[196:199], v[116:119]\n\tv_fmac_f32 v44, v36, v216\n\tv_fmac_f32 v45, v37, v216\n\tv_fmac_f32 v46, v38, v216\n\tv_fmac_f32 v47, v39, v216\n\t"
            "v_cvt_pk_bf16_f32 v44, v44, v45\n\tv_cvt_pk_bf16_f32 v45, v46, v47\n\tv_and_b32 v220, v44, v22\n\tv_and_b32 v221, v45, v22\n\tv_and_b32 v236, v44, v23\n\tv_and_b32 v237, v45, v23\n\t"
            "v_fmac_f32 v56, v48, v216\n\tv_fmac_f32 v57, v49, v216\n\tv_fmac_f32 v58, v50, v216\n\tv_fmac_f32 v59, v51, v216\n\tv_cvt_pk_bf16_f32 v56, v56, v57\n\tv_cvt_pk_bf16_f32 v57, v58, v59\n\t"
            "v_and_b32 v222, v56, v22\n\tv_and_b32 v223, v57, v22\n\tv_and_b32 v238, v56, v23\n\tv_and_b32 v239, v57, v23\n\tv_fmac_f32 v68, v60, v216\n\tv_fmac_f32 v69, v61, v216\n\t"
            "v_fmac_f32 v70, v62, v216\n\tv_fmac_f32 v71, v63, v216\n\tv_cvt_pk_bf16_f32 v68, v68, v69\n\tv_cvt_pk_bf16_f32 v69, v70, v71\n\tv_and_b32 v224, v68, v22\n\tv_and_b32 v225, v69, v22\n\t"
            "v_and_b32 v240, v68, v23\n\tv_and_b32 v241, v69, v23\n\tv_fmac_f32 v80, v72, v216\n\tv_fmac_f32 v81, v73, v216\n\tv_fmac_f32 v82, v74, v216\n\tv_fmac_f32 v83, v75, v216\n\t"
            "ds_read_b32 v27, v32\n\tds_read2st64_b32 v[42:43], v31 offset0:0 offset1:1\n\tds_read2st64_b32 v[40:41], v30 offset0:0 offset1:1\n\tds_read2st64_b32 v[66:67], v31 offset0:2 offset1:3\n\tds_read2st64_b32 v[64:65], v30 offset0:2 offset1:3\n\tv_cvt_pk_bf16_f32 v80, v80, v81\n\t"
            "v_cvt_pk_bf16_f32 v81, v82, v83\n\tv_and_b32 v226, v80, v22\n\tv_and_b32 v227, v81, v22\n\tv_and_b32 v242, v80, v23\n\tv_and_b32 v243, v81, v23\n\tds_read_b128 v[84:87], v15 offset:256\n\t"
            "v_mov_b32 v219, 0\n\tv_mov_b32 v234, 0\n\tv_mov_b32 v235, 0\n\tds_read_b128 v[96:99], v15 offset:320\n\tv_mov_b32 v176, 0\n\tv_mov_b32 v180, 0\n\t"
            "v_mov_b32 v188, 0\n\tds_read_b128 v[108:111], v15 offset:384\n\tv_mov_b32 v192, 0\n\t"
            SA(6)
            "s_waitcnt lgkmcnt(5)\n\tv_cmp_ge_u32 vcc, 0, v27\n\ts_cbranch_vccnz 10f\n\t20:\nv_and_b32 v177, v42, v24\n\tv_and_b32 v178, v42, v25\n\t"
            "v_and_b32 v179, v42, v26\n\tv_and_b32 v189, v43, v24\n\tv_and_b32 v190, v43, v25\n\tv_and_b32 v191, v43, v26\n\tv_mfma_f32_16x16x32_bf16 v[44:47], v[234:237], v[176:179], 0\n\tv_and_b32 v181, v40, v24\n\t"
            "v_and_b32 v182, v40, v25\n\tv_mfma_f32_16x16x32_bf16 v[56:59], v[236:239], v[188:191], 0\n\tv_and_b32 v183, v40, v26\n\tv_and_b32 v193, v41, v24\n\tv_and_b32 v194, v41, v25\n\tv_and_b32 v195, v41, v26\n\t"
            "v_mfma_f32_16x16x32_bf16 v[44:47], v[218:221], v[180:183], v[44:47]\n\ts_waitcnt lgkmcnt(0)\n\tv_and_b32 v177, v66, v24\n\tv_and_b32 v178, v66, v25\n\tv_mfma_f32_16x16x32_bf16 v[56:59], v[220:223], v[192:195], v[56:59]\n\tv_and_b32 v179, v66, v26\n\t"
            "v_and_b32 v189, v67, v24\n\tv_and_b32 v190, v67, v25\n\ts_nop 0\n\tv_cvt_pk_bf16_f32 v36, v44, v45\n\tv_cvt_pk_bf16_f32 v37, v46, v47\n\tv_min_u32 v36, -2, v36\n\t"
            "v_and_b32 v191, v67, v26\n\tv_cvt_pk_bf16_f32 v38, v56, v57\n\tv_cvt_pk_bf16_f32 v39, v58, v59\n\tv_min_u32 v38, -2, v38\n\tv_mfma_f32_16x16x32_bf16 v[68:71], v[238:241], v[176:179], 0\n\ts_nop 0\n\t"
            "v_permlane16_swap_b32 v36, v38\n\tv_permlane16_swap_b32 v37, v39\n\tbuffer_store_dwordx4 v[36:39], v16, s[44:47], s20 offen sc1\n\tv_mfma_f32_16x16x32_bf16 v[80:83], v[240:243], v[188:191], 0\n\tv_and_b32 v181, v64, v24\n\tv_and_b32 v182, v64, v25\n\t"
            "v_and_b32 v183, v64, v26\n\tv_and_b32 v193, v65, v24\n\tv_and_b32 v194, v65, v25\n\tv_and_b32 v195, v65, v26\n\tv_mfma_f32_16x16x32_bf16 v[68:71], v[222:225], v[180:183], v[68:71]\n\tv_fmac_f32 v92, v84, v216\n\t"
            "v_fmac_f32 v93, v85, v216\n\tv_mfma_f32_16x16x32_bf16 v[80:83], v[224:227], v[192:195], v[80:83]\n\tv_fmac_f32 v94, v86, v216\n\tv_fmac_f32 v95, v87, v216\n\tv_cvt_pk_bf16_f32 v92, v92, v93\n\ts_nop 1\n\t"
            "v_cvt_pk_bf16_f32 v60, v68, v69\n\tv_cvt_pk_bf16_f32 v61, v70, v71\n\tv_min_u32 v60, -2, v60\n\tv_fmac_f32 v104, v96, v216\n\tv_cvt_pk_bf16_f32 v62, v80, v81\n\tv_cvt_pk_bf16_f32 v63, v82, v83\n\t"
            "v_min_u32 v62, -2, v62\n\tv_fmac_f32 v105, v97, v216\n\tv_permlane16_swap_b32 v61, v63\n\tv_permlane16_swap_b32 v60, v62\n\tbuffer_store_dwordx4 v[60:63], v16, s[44:47], s21 offen sc1\n\tds_write_b32 v34, v35\n\t"
            SA(4)
            "v_cvt_pk_bf16_f32 v93, v94, v95\n\tv_fmac_f32 v106, v98, v216\n\tv_fmac_f32 v107, v99, v216\n\tv_cvt_pk_bf16_f32 v104, v104, v105\n\tv_fmac_f32 v116, v108, v216\n\tv_fmac_f32 v117, v109, v216\n\t"
            "v_and_b32 v228, v92, v22\n\tds_read_b32 v27, v32\n\tds_read2st64_b32 v[102:103], v31 offset0:152 offset1:153\n\tds_read2st64_b32 v[100:101], v30 offset0:152 offset1:153\n\tds_read2st64_b32 v[78:79], v31 offset0:154 offset1:155\n\tds_read2st64_b32 v[76:77], v30 offset0:154 offset1:155\n\t"
            "v_and_b32 v229, v93, v22\n\tv_and_b32 v244, v92, v23\n\tv_and_b32 v245, v93, v23\n\tv_cvt_pk_bf16_f32 v105, v106, v107\n\tv_fmac_f32 v118, v110, v216\n\tv_fmac_f32 v119, v111, v216\n\t"
            "v_cvt_pk_bf16_f32 v116, v116, v117\n\tv_and_b32 v230, v104, v22\n\tv_and_b32 v231, v105, v22\n\tv_and_b32 v246, v104, v23\n\tv_and_b32 v247, v105, v23\n\tv_cvt_pk_bf16_f32 v117, v118, v119\n\t"
            "v_and_b32 v232, v116, v22\n\tv_and_b32 v233, v117, v22\n\tv_and_b32 v248, v116, v23\n\tv_and_b32 v249, v117, v23\n\ts_waitcnt lgkmcnt(3)\n\tv_cmp_ge_u32 vcc, 1, v27\n\t"
            "s_cbranch_vccnz 11f\n\t21:\nv_and_b32 v177, v102, v24\n\tv_and_b32 v178, v102, v25\n\tv_and_b32 v179, v102, v26\n\tv_and_b32 v189, v103, v24\n\t"
            "v_and_b32 v190, v103, v25\n\tv_and_b32 v191, v103, v26\n\tv_mfma_f32_16x16x32_bf16 v[92:95], v[242:245], v[176:179], 0\n\ts_waitcnt lgkmcnt(0)\n\tv_and_b32 v181, v100, v24\n\tv_and_b32 v182, v100, v25\n\t"
            "v_mfma_f32_16x16x32_bf16 v[104:107], v[244:247], v[188:191], 0\n\tv_and_b32 v183, v100, v26\n\tv_and_b32 v193, v101, v24\n\tv_and_b32 v194, v101, v25\n\tv_and_b32 v195, v101, v26\n\tv_mfma_f32_16x16x32_bf16 v[92:95], v[226:229], v[180:183], v[92:95]\n\t"
            "v_and_b32 v177, v78, v24\n\tv_and_b32 v178, v78, v25\n\tv_mfma_f32_16x16x32_bf16 v[104:107], v[228:231], v[192:195], v[104:107]\n\tv_and_b32 v179, v78, v26\n\tv_and_b32 v181, v76, v24\n\tv_and_b32 v182, v76, v25\n\t"
            "v_mfma_f32_16x16x32_bf16 v[116:119], v[246:249], v[176:179], 0\n\tv_and_b32 v183, v76, v26\n\tv_cvt_pk_bf16_f32 v84, v92, v93\n\ts_nop 1\n\tv_cvt_pk_bf16_f32 v86, v104, v105\n\tv_mfma_f32_16x16x32_bf16 v[116:119], v[230:233], v[180:183], v[116:119]\n\t"
            "v_cvt_pk_bf16_f32 v85, v94, v95\n\tv_min_u32 v84, -2, v84\n\tv_cvt_pk_bf16_f32 v87, v106, v107\n\tv_min_u32 v86, -2, v86\n\ts_nop 0\n\tv_permlane16_swap_b32 v85, v87\n\t"
            "v_permlane16_swap_b32 v84, v86\n\tbuffer_store_dwordx4 v[84:87], v16, s[44:47], s22 offen sc1\n\tv_cvt_pk_bf16_f32 v108, v116, v117\n\tv_cvt_pk_bf16_f32 v109, v118, v119\n\tv_min_u32 v108, -2, v108\n\ts_nop 0\n\t"
            "v_permlane16_swap_b32 v109, v111\n\tv_permlane16_swap_b32 v108, v110\n\tbuffer_store_dwordx4 v[108:111], v215, s[44:47], s23 offen sc1\n\tds_write_b32 v34, v34\n\t"
            SA(5)
            "s_setprio 0\n\t"
            DUMP
            "s_cmp_eq_u32 s69, 1\n\ts_cbranch_scc1 .Lout\n\ts_endpgm\n\t10:\nds_read_b32 v27, v32\n\tds_read2st64_b32 v[42:43], v31 offset0:0 offset1:1\n\t"
            "ds_read2st64_b32 v[40:41], v30 offset0:0 offset1:1\n\tds_read2st64_b32 v[66:67], v31 offset0:2 offset1:3\n\tds_read2st64_b32 v[64:65], v30 offset0:2 offset1:3\n\ts_waitcnt lgkmcnt(0)\n\tv_cmp_ge_u32 vcc, 0, v27\n\ts_cbranch_vccz 20b\n\t"
            ASLP
            "s_sub_u32 s68, s68, 1\n\ts_cbranch_scc0 10b\n\ts_branch 20b\n\t11:\nds_read_b32 v27, v32\n\tds_read2st64_b32 v[102:103], v31 offset0:152 offset1:153\n\t"
            "ds_read2st64_b32 v[100:101], v30 offset0:152 offset1:153\n\tds_read2st64_b32 v[78:79], v31 offset0:154 offset1:155\n\tds_read2st64_b32 v[76:77], v30 offset0:154 offset1:155\n\ts_waitcnt lgkmcnt(0)\n\tv_cmp_ge_u32 vcc, 1, v27\n\ts_cbranch_vccz 21b\n\t"
            ASLP
            "s_sub_u32 s68, s68, 1\n\ts_cbranch_scc0 11b\n\ts_branch 21b\n\t.LB:\ns_and_b32 s72, s56, 1\n\ts_lshr_b32 s73, s56, 1\n\t"
            "v_and_b32 v156, 2, v51\n\tv_lshlrev_b32 v156, 1, v156\n\tv_and_b32 v157, 7, v52\n\tv_xor_b32 v157, v156, v157\n\tv_mul_u32_u24 v156, 0x500, v51\n\tv_lshl_add_u32 v156, v157, 4, v156\n\t"
            "s_mul_i32 s74, s72, 0x2800\n\ts_mul_i32 s75, s73, 0xf000\n\ts_add_i32 s75, s75, s74\n\tv_add_u32 v154, s75, v156\n\ts_mul_i32 s75, s73, 0x5000\n\ts_add_i32 s75, s75, s74\n\t"
            "s_addk_i32 s75, 0x5000\n\tv_add_u32 v156, s75, v156\n\ts_cmp_lg_u32 s63, 0\n\ts_cselect_b32 s74, 81920, 0\n\ts_max_u32 s74, s74, 65536\n\tv_add_u32 v158, s62, v52\n\t"
            "v_min_u32 v159, 47, v52\n\ts_add_i32 s75, s62, 64\n\tv_add_u32 v159, s75, v159\n\tv_max_i32 v158, 0, v158\n\tv_min_i32 v159, 0x3ff, v159\n\tv_lshl_add_u32 v158, v158, 4, s74\n\t"
            "v_lshl_add_u32 v159, v159, 4, s74\n\tv_lshl_add_u32 v59, v53, 4, v57\n\ts_cmp_lg_u32 s73, 0\n\ts_cselect_b64 vcc, -1, 0\n\tv_cndmask_b32 v97, v59, v52, vcc\n\ts_lshl_b32 s75, s72, 6\n\t"
            "s_add_i32 s75, s75, s62\n\tv_add_u32 v97, s75, v97\n\tv_mov_b32 v60, s62\n\tv_add_u32 v60, 0x6f, v60\n\tv_min_i32 v60, 0x3ff, v60\n\tv_med3_i32 v97, v97, 0, v60\n\t"
            "s_lshl3_add_u32 s74, s73, s74\n\tv_lshl_add_u32 v97, v97, 4, s74\n\ts_mul_i32 s30, s73, 576\n\ts_lshl_b32 s75, s72, 8\n\ts_add_i32 s30, s30, s75\n\ts_add_i32 s30, s30, 160000\n\t"
            "s_lshl_b32 s74, s59, 7\n\ts_add_i32 s74, s74, 8192\n\tv_min_u32 v96, 9, v52\n\tv_lshl_add_u32 v96, v96, 2, s74\n\ts_lshr_b32 s75, s58, 5\n\ts_add_i32 s74, s74, s75\n\t"
            "s_cmp_eq_u32 s56, 3\n\ts_cselect_b32 s74, s74, 0x40000000\n\tv_lshl_add_u32 v98, v52, 24, s74\n\ts_cselect_b32 s75, 1, 0\n\ts_lshl2_add_u32 s74, s59, s56\n\ts_mul_i32 s74, s74, 0xa000\n\t"
            "s_add_i32 s74, s74, 155648\n\tv_lshl_add_u32 v101, v52, 4, s74\n\tv_and_b32 v60, s75, v53\n\tv_lshl_add_u32 v2, v60, 30, v101\n\ts_cmp_ge_u32 s56, s57\n\ts_addc_u32 s76, s56, 0\n\t"
            "s_and_b32 s16, s76, 3\n\ts_lshl_b32 s77, s76, 1\n\ts_max_u32 s78, s77, 1\n\ts_sub_u32 s78, s78, 1\n\ts_min_u32 s79, s77, 7\n\tv_lshlrev_b32 v60, 9, v53\n\t"
            "v_lshl_add_u32 v60, v54, 3, v60\n\ts_mul_i32 s80, s78, 0xc00\n\ts_mul_i32 s81, s79, 0xc00\n\ts_addk_i32 s80, 14336\n\ts_addk_i32 s81, 14336\n\tv_add_u32 v24, s80, v60\n\t"
            "v_add_u32 v26, s81, v60\n\tv_add_u32 v25, 38912, v24\n\tv_add_u32 v27, 38912, v26\n\tv_lshl_add_u32 v29, v52, 4, s80\n\ts_lshr_b32 s78, s78, 1\n\ts_lshr_b32 s79, s79, 1\n\t"
            "s_sub_u32 s79, s79, s78\n\ts_lshl2_add_u32 s78, s78, 159744\n\tv_and_b32 v28, s79, v52\n\tv_lshl_add_u32 v28, v28, 2, s78\n\ts_lshl2_add_u32 s78, s56, 159744\n\tv_mov_b32 v30, s78\n\t"
            "s_mov_b32 s78, 160576\n\tv_lshl_add_u32 v153, v59, 2, s78\n\ts_mul_i32 s79, s59, 163840\n\ts_lshl_b32 s80, s58, 5\n\ts_add_i32 s79, s79, s80\n\ts_cmp_eq_u32 s76, 0\n\t"
            "s_cselect_b32 s80, 0, 0xc00\n\ts_add_i32 s79, s79, s80\n\ts_add_i32 s79, s79, 155652\n\tv_lshl_add_u32 v31, v54, 4, s79\n\tv_lshrrev_b32 v60, 1, v53\n\tv_mul_u32_u24 v60, 0xa000, v60\n\t"
            "v_add_u32 v31, v60, v31\n\tv_and_b32 v60, 1, v53\n\tv_lshl_add_u32 v31, v60, 3, v31\n\ts_mov_b32 s31, 0x14000\n\ts_cmp_lt_i32 s62, 0\n\ts_cselect_b32 s79, 8, -16\n\t"
            "v_mov_b32 v61, 0xff800000\n\tv_mov_b32 v142, 1\n\tv_add_u32 v60, 0, v59\n\tv_and_b32 v60, -8, v60\n\tv_add_u32 v60, 0, v60\n\tv_cmp_gt_i32 vcc, s79, v60\n\t"
            "v_lshl_add_u32 v160, v60, 2, s78\n\ts_nop 0\n\tv_cndmask_b32 v128, 0, v61, vcc\n\tv_add_u32 v60, 4, v59\n\tv_and_b32 v60, -8, v60\n\tv_add_u32 v60, -4, v60\n\t"
            "v_cmp_gt_i32 vcc, s79, v60\n\tv_lshl_add_u32 v161, v60, 2, s78\n\ts_nop 0\n\tv_cndmask_b32 v129, 0, v61, vcc\n\tv_add_u32 v60, -1, v59\n\tv_and_b32 v60, -8, v60\n\t"
            "v_add_u32 v60, 1, v60\n\tv_cmp_gt_i32 vcc, s79, v60\n\tv_lshl_add_u32 v162, v60, 2, s78\n\ts_nop 0\n\tv_cndmask_b32 v130, 0, v61, vcc\n\tv_add_u32 v60, 3, v59\n\t"
            "v_and_b32 v60, -8, v60\n\tv_add_u32 v60, -3, v60\n\tv_cmp_gt_i32 vcc, s79, v60\n\tv_lshl_add_u32 v163, v60, 2, s78\n\ts_nop 0\n\tv_cndmask_b32 v131, 0, v61, vcc\n\t"
            "v_add_u32 v60, -2, v59\n\tv_and_b32 v60, -8, v60\n\tv_add_u32 v60, 2, v60\n\tv_cmp_gt_i32 vcc, s79, v60\n\tv_lshl_add_u32 v164, v60, 2, s78\n\ts_nop 0\n\t"
            "v_cndmask_b32 v132, 0, v61, vcc\n\tv_add_u32 v60, 2, v59\n\tv_and_b32 v60, -8, v60\n\tv_add_u32 v60, -2, v60\n\tv_cmp_gt_i32 vcc, s79, v60\n\tv_lshl_add_u32 v165, v60, 2, s78\n\t"
            "s_nop 0\n\tv_cndmask_b32 v133, 0, v61, vcc\n\tv_add_u32 v60, -3, v59\n\tv_and_b32 v60, -8, v60\n\tv_add_u32 v60, 3, v60\n\tv_cmp_gt_i32 vcc, s79, v60\n\t"
            "v_lshl_add_u32 v166, v60, 2, s78\n\ts_nop 0\n\tv_cndmask_b32 v134, 0, v61, vcc\n\tv_add_u32 v60, 1, v59\n\tv_and_b32 v60, -8, v60\n\tv_add_u32 v60, -1, v60\n\t"
            "v_cmp_gt_i32 vcc, s79, v60\n\tv_lshl_add_u32 v167, v60, 2, s78\n\ts_nop 0\n\tv_cndmask_b32 v135, 0, v61, vcc\n\ts_lshl2_add_u32 s80, s71, s56\n\ts_lshl_b32 s80, s80, 5\n\t"
            "v_add_u32 v100, s80, v52\n\tv_mul_u32_u24 v100, 0x500, v100\n\ts_add_i32 s80, s58, 3072\n\tv_add_u32 v100, s80, v100\n\tv_min_u32 v104, 51, v52\n\tv_lshlrev_b32 v104, 4, v104\n\t"
            "v_add_u32 v104, 0x1000, v104\n\ts_lshl_b32 s4, s64, 13\n\ts_add_u32 s5, s4, 0x1000\n\ts_add_u32 s6, s5, 0x1000\n\ts_cmpk_eq_u32 s6, 0xa000\n\ts_cselect_b32 s6, 0, s6\n\t"
            "s_add_u32 s7, s6, 0x1000\n\ts_add_u32 s8, s7, 0x1000\n\ts_cmpk_eq_u32 s8, 0xa000\n\ts_cselect_b32 s8, 0, s8\n\ts_add_u32 s9, s8, 0x1000\n\ts_add_u32 s10, s9, 0x1000\n\t"
            "s_cmpk_eq_u32 s10, 0xa000\n\ts_cselect_b32 s10, 0, s10\n\ts_add_u32 s11, s10, 0x1000\n\ts_add_u32 s12, s11, 0x1000\n\ts_cmpk_eq_u32 s12, 0xa000\n\ts_cselect_b32 s12, 0, s12\n\t"
            "s_add_u32 s13, s12, 0x1000\n\ts_mul_i32 s20, s4, 20\n\ts_add_i32 s20, s20, s58\n\ts_addk_i32 s20, 3072\n\ts_mul_i32 s21, s5, 20\n\ts_add_i32 s21, s21, s58\n\t"
            "s_addk_i32 s21, 2048\n\ts_mul_i32 s22, s6, 20\n\ts_add_i32 s22, s22, s58\n\ts_addk_i32 s22, 1024\n\ts_mul_i32 s23, s7, 20\n\ts_add_i32 s23, s23, s58\n\t"
            "s_mul_i32 s24, s8, 20\n\ts_add_i32 s24, s24, s58\n\ts_addk_i32 s24, 3072\n\ts_mul_i32 s25, s9, 20\n\ts_add_i32 s25, s25, s58\n\ts_addk_i32 s25, 2048\n\t"
            "s_mul_i32 s26, s10, 20\n\ts_add_i32 s26, s26, s58\n\ts_addk_i32 s26, 1024\n\ts_mul_i32 s27, s11, 20\n\ts_add_i32 s27, s27, s58\n\ts_mul_i32 s28, s12, 20\n\t"
            "s_add_i32 s28, s28, s58\n\ts_addk_i32 s28, 3072\n\ts_mul_i32 s29, s13, 20\n\ts_add_i32 s29, s29, s58\n\ts_addk_i32 s29, 2048\n\ts_mul_i32 s72, s60, 1920\n\t"
            "s_sub_u32 s14, s61, s72\n\ts_add_u32 s17, s63, 0x500\n\ts_add_u32 s60, s60, 256\n\ts_cmp_eq_u32 s60, s17\n\ts_cselect_b32 s60, s63, s60\n\ts_mul_i32 s15, s56, 0x2800\n\t"
            "s_add_u32 s15, s15, 77824\n\tv_lshl_add_u32 v152, v52, 4, s15\n\ts_lshl_b32 s18, s58, 5\n\ts_mov_b32 s50, 822272\n\ts_mov_b32 s51, 0x20000\n\ts_waitcnt lgkmcnt(0)\n\t"
            "s_load_dword s67, s[44:45], 0x0\n\ts_sub_u32 s48, s48, 3072\n\ts_subb_u32 s49, s49, 0\n\ts_lshl_b32 s78, s56, 5\n\tv_add_u32 v12, s78, v57\n\tv_add_u32 v3, s62, v12\n\t"
            "v_and_b32 v13, 1, v53\n\tv_lshrrev_b32 v14, 1, v53\n\tv_lshl_add_u32 v15, v13, 1, v14\n\ts_add_u32 s79, s63, s58\n\tv_lshl_add_u32 v15, v15, 4, s79\n\ts_movk_i32 s79, 0xa00\n\t"
            "v_mad_i32_i24 v3, v3, s79, v15\n\tv_cmp_gt_u32 vcc, 8, v12\n\ts_cmp_eq_u32 s56, 3\n\ts_cselect_b32 s79, 2.0, 0xa000\n\tv_add_u32 v11, s79, v3\n\tv_cndmask_b32 v3, v3, 2.0, vcc\n\t"
            "v_mov_b32 v8, 159744\n\tv_and_b32 v9, 31, v52\n\tv_add_u32 v9, s78, v9\n\tv_lshl_add_u32 v9, v9, 2, v8\n\tv_lshl_add_u32 v10, v13, 4, v12\n\tv_lshl_add_u32 v10, v10, 2, v8\n\t"
            "v_mul_u32_u24 v4, 0x2800, v14\n\tv_lshl_add_u32 v4, v56, 7, v4\n\tv_lshl_add_u32 v4, v55, 3, v4\n\tv_lshl_add_u32 v4, v13, 5, v4\n\tv_bfe_u32 v5, v52, 3, 1\n\tv_lshl_add_u32 v4, v5, 6, v4\n\t"
            "v_add_u32 v4, 77824, v4\n\tv_xor_b32 v5, 64, v4\n\tv_add_u32 v6, 0x5000, v4\n\tv_add_u32 v7, 0x5000, v5\n\ts_waitcnt vmcnt(0) lgkmcnt(0)\n\ts_barrier\n\t"
            "v_mov_b32 v136, -1\n\tv_mov_b32 v137, -1\n\tv_mov_b32 v138, -1\n\tv_mov_b32 v139, -1\n\ts_add_u32 m0, s66, 0\n\ts_mul_i32 s61, s60, 1920\n\t"
            "s_add_u32 s61, s61, s14\n\tbuffer_load_dwordx4 v40, s[40:43], s61 offen lds\n\tbuffer_load_dwordx4 v41, s[40:43], s61 offen offset:1024 lds\n\tbuffer_load_dwordx4 v42, s[40:43], s61 offen offset:2048 lds\n\tbuffer_load_dwordx4 v43, s[40:43], s61 offen offset:3072 lds\n\ts_addk_i32 m0, 0x1000\n\t"
            "s_add_u32 s68, s60, 128\n\tbuffer_load_dwordx4 v44, s[40:43], s61 offen lds\n\tbuffer_load_dwordx4 v45, s[40:43], s61 offen offset:1024 lds\n\ts_add_u32 m0, s65, 0\n\ts_cmp_eq_u32 s68, s17\n\tbuffer_load_dwordx4 v36, s[36:39], s60 offen lds\n\t"
            "buffer_load_dwordx4 v37, s[36:39], s60 offen offset:1024 lds\n\tbuffer_load_dwordx4 v38, s[36:39], s60 offen offset:2048 lds\n\ts_mov_b32 exec_hi, 0\n\tbuffer_load_dwordx4 v39, s[36:39], s60 offen offset:3072 lds\n\ts_mov_b32 exec_hi, -1\n\ts_cselect_b32 s60, s63, s68\n\t"
            "s_mul_i32 s67, s67, 0x19660d\n\ts_add_u32 s67, s67, 0x3c6ef35f\n\tbuffer_store_dwordx4 v[136:139], v101, s[44:47], s18 offen offset:0 sc1\n\tbuffer_store_dwordx4 v[136:139], v2, s[44:47], s18 offen offset:1024 sc1\n\tbuffer_store_dwordx4 v[136:139], v2, s[44:47], s18 offen offset:2048 sc1\n\tbuffer_store_dwordx4 v[136:139], v101, s[44:47], s18 offen offset:3072 sc1\n\t"
            "s_barrier\n\ts_add_u32 m0, s66, 38912\n\ts_mul_i32 s61, s60, 1920\n\ts_add_u32 s61, s61, s14\n\tbuffer_load_dwordx4 v40, s[40:43], s61 offen lds\n\tbuffer_load_dwordx4 v41, s[40:43], s61 offen offset:1024 lds\n\t"
            "buffer_load_dwordx4 v42, s[40:43], s61 offen offset:2048 lds\n\tbuffer_load_dwordx4 v43, s[40:43], s61 offen offset:3072 lds\n\ts_addk_i32 m0, 0x1000\n\ts_add_u32 s68, s60, 128\n\tbuffer_load_dwordx4 v44, s[40:43], s61 offen lds\n\tbuffer_load_dwordx4 v45, s[40:43], s61 offen offset:1024 lds\n\t"
            "s_add_u32 m0, s65, 38912\n\ts_cmp_eq_u32 s68, s17\n\tbuffer_load_dwordx4 v36, s[36:39], s60 offen lds\n\tbuffer_load_dwordx4 v37, s[36:39], s60 offen offset:1024 lds\n\tbuffer_load_dwordx4 v38, s[36:39], s60 offen offset:2048 lds\n\ts_mov_b32 exec_hi, 0\n\t"
            "buffer_load_dwordx4 v39, s[36:39], s60 offen offset:3072 lds\n\ts_mov_b32 exec_hi, -1\n\ts_cselect_b32 s60, s63, s68\n\ts_waitcnt vmcnt(14)\n\ts_barrier\n\ts_add_u32 m0, s66, 77824\n\t"
            "s_mul_i32 s61, s60, 1920\n\ts_add_u32 s61, s61, s14\n\tbuffer_load_dwordx4 v40, s[40:43], s61 offen lds\n\tbuffer_load_dwordx4 v41, s[40:43], s61 offen offset:1024 lds\n\tbuffer_load_dwordx4 v42, s[40:43], s61 offen offset:2048 lds\n\tbuffer_load_dwordx4 v43, s[40:43], s61 offen offset:3072 lds\n\t"
            "s_addk_i32 m0, 0x1000\n\ts_add_u32 s68, s60, 128\n\tbuffer_load_dwordx4 v44, s[40:43], s61 offen lds\n\tbuffer_load_dwordx4 v45, s[40:43], s61 offen offset:1024 lds\n\ts_add_u32 m0, s65, 77824\n\ts_cmp_eq_u32 s68, s17\n\t"
            "buffer_load_dwordx4 v36, s[36:39], s60 offen lds\n\tbuffer_load_dwordx4 v37, s[36:39], s60 offen offset:1024 lds\n\tbuffer_load_dwordx4 v38, s[36:39], s60 offen offset:2048 lds\n\ts_mov_b32 exec_hi, 0\n\tbuffer_load_dwordx4 v39, s[36:39], s60 offen offset:3072 lds\n\ts_mov_b32 exec_hi, -1\n\t"
            "s_cselect_b32 s60, s63, s68\n\ts_waitcnt vmcnt(10)\n\ts_barrier\n\ts_add_u32 m0, s66, 0\n\ts_mul_i32 s61, s60, 1920\n\ts_add_u32 s61, s61, s14\n\t"
            "buffer_load_dwordx4 v40, s[40:43], s61 offen lds\n\tbuffer_load_dwordx4 v41, s[40:43], s61 offen offset:1024 lds\n\tbuffer_load_dwordx4 v42, s[40:43], s61 offen offset:2048 lds\n\tbuffer_load_dwordx4 v43, s[40:43], s61 offen offset:3072 lds\n\ts_addk_i32 m0, 0x1000\n\ts_add_u32 s68, s60, 128\n\t"
            "buffer_load_dwordx4 v44, s[40:43], s61 offen lds\n\tbuffer_load_dwordx4 v45, s[40:43], s61 offen offset:1024 lds\n\ts_add_u32 m0, s65, 0\n\ts_cmp_eq_u32 s68, s17\n\tbuffer_load_dwordx4 v36, s[36:39], s60 offen lds\n\tbuffer_load_dwordx4 v37, s[36:39], s60 offen offset:1024 lds\n\t"
            "buffer_load_dwordx4 v38, s[36:39], s60 offen offset:2048 lds\n\ts_mov_b32 exec_hi, 0\n\tbuffer_load_dwordx4 v39, s[36:39], s60 offen offset:3072 lds\n\ts_mov_b32 exec_hi, -1\n\ts_cselect_b32 s60, s63, s68\n\tv_mov_b32 v140, s67\n\t"
            "buffer_store_dword v140, v98, s[44:47], 0 offen sc1\n\ts_waitcnt vmcnt(11)\n\ts_barrier\n\ts_add_u32 m0, s66, 38912\n\ts_mul_i32 s61, s60, 1920\n\ts_add_u32 s61, s61, s14\n\t"
            "buffer_load_dwordx4 v40, s[40:43], s61 offen lds\n\tbuffer_load_dwordx4 v41, s[40:43], s61 offen offset:1024 lds\n\tbuffer_load_dwordx4 v42, s[40:43], s61 offen offset:2048 lds\n\tbuffer_load_dwordx4 v43, s[40:43], s61 offen offset:3072 lds\n\ts_addk_i32 m0, 0x1000\n\ts_add_u32 s68, s60, 128\n\t"
            "buffer_load_dwordx4 v44, s[40:43], s61 offen lds\n\tbuffer_load_dwordx4 v45, s[40:43], s61 offen offset:1024 lds\n\ts_add_u32 m0, s65, 38912\n\ts_cmp_eq_u32 s68, s17\n\tbuffer_load_dwordx4 v36, s[36:39], s60 offen lds\n\tbuffer_load_dwordx4 v37, s[36:39], s60 offen offset:1024 lds\n\t"
            "buffer_load_dwordx4 v38, s[36:39], s60 offen offset:2048 lds\n\ts_mov_b32 exec_hi, 0\n\tbuffer_load_dwordx4 v39, s[36:39], s60 offen offset:3072 lds\n\ts_mov_b32 exec_hi, -1\n\ts_cselect_b32 s60, s63, s68\n\ts_waitcnt vmcnt(11)\n\t"
            "s_barrier\n\ts_add_u32 m0, s66, 77824\n\ts_mul_i32 s61, s60, 1920\n\ts_add_u32 s61, s61, s14\n\tbuffer_load_dwordx4 v40, s[40:43], s61 offen lds\n\tbuffer_load_dwordx4 v41, s[40:43], s61 offen offset:1024 lds\n\t"
            "buffer_load_dwordx4 v42, s[40:43], s61 offen offset:2048 lds\n\tbuffer_load_dwordx4 v43, s[40:43], s61 offen offset:3072 lds\n\ts_addk_i32 m0, 0x1000\n\ts_add_u32 s68, s60, 128\n\tbuffer_load_dwordx4 v44, s[40:43], s61 offen lds\n\tbuffer_load_dwordx4 v45, s[40:43], s61 offen offset:1024 lds\n\t"
            "s_add_u32 m0, s65, 77824\n\ts_cmp_eq_u32 s68, s17\n\tbuffer_load_dwordx4 v36, s[36:39], s60 offen lds\n\tbuffer_load_dwordx4 v37, s[36:39], s60 offen offset:1024 lds\n\tbuffer_load_dwordx4 v38, s[36:39], s60 offen offset:2048 lds\n\ts_mov_b32 exec_hi, 0\n\t"
            "buffer_load_dwordx4 v39, s[36:39], s60 offen offset:3072 lds\n\ts_mov_b32 exec_hi, -1\n\ts_cselect_b32 s60, s63, s68\n\ts_waitcnt vmcnt(10)\n\ts_barrier\n\ts_cmp_lg_u32 s56, 3\n\t"
            "s_cbranch_scc1 1f\n\tbuffer_load_dwordx4 v[106:109], v158, s[44:47], 0 offen sc1\n\tbuffer_load_dwordx4 v[110:113], v159, s[44:47], 0 offen sc1\n\tbuffer_load_dwordx4 v[114:117], v104, s[44:47], 0 offen sc1\n\tbuffer_load_dword v118, v96, s[44:47], 0 offen sc1\n\t1:\n"
            "s_add_u32 m0, s66, 0\n\ts_mul_i32 s61, s60, 1920\n\ts_add_u32 s61, s61, s14\n\tbuffer_load_dwordx4 v40, s[40:43], s61 offen lds\n\tbuffer_load_dwordx4 v41, s[40:43], s61 offen offset:1024 lds\n\tbuffer_load_dwordx4 v42, s[40:43], s61 offen offset:2048 lds\n\t"
            "buffer_load_dwordx4 v43, s[40:43], s61 offen offset:3072 lds\n\ts_addk_i32 m0, 0x1000\n\ts_add_u32 s68, s60, 128\n\tbuffer_load_dwordx4 v44, s[40:43], s61 offen lds\n\tbuffer_load_dwordx4 v45, s[40:43], s61 offen offset:1024 lds\n\ts_add_u32 m0, s65, 0\n\t"
            "s_cmp_eq_u32 s68, s17\n\tbuffer_load_dwordx4 v36, s[36:39], s60 offen lds\n\tbuffer_load_dwordx4 v37, s[36:39], s60 offen offset:1024 lds\n\tbuffer_load_dwordx4 v38, s[36:39], s60 offen offset:2048 lds\n\ts_mov_b32 exec_hi, 0\n\tbuffer_load_dwordx4 v39, s[36:39], s60 offen offset:3072 lds\n\t"
            "s_mov_b32 exec_hi, -1\n\ts_cselect_b32 s60, s63, s68\n\ts_waitcnt vmcnt(10)\n\ts_cmp_lg_u32 s56, 3\n\ts_cbranch_scc1 2f\n\ts_mov_b32 s68, 0x40000\n\t"
            "v_xor_b32 v106, s67, v107\n\tv_xor_b32 v108, s67, v109\n\tv_xor_b32 v110, s67, v111\n\tv_xor_b32 v112, s67, v113\n\tv_xor_b32 v114, s67, v115\n\tv_xor_b32 v116, s67, v117\n\t"
            "v_xor_b32 v118, s67, v118\n\tv_or3_b32 v106, v106, v108, v110\n\tv_or3_b32 v118, v118, v114, v116\n\tv_or3_b32 v106, v106, v112, v118\n\tv_cmp_ne_u32 vcc, 0, v106\n\ts_cbranch_vccnz 30f\n\t"
            "31:\n2:\ns_waitcnt vmcnt(10)\n\ts_barrier\n\ts_add_u32 m0, s66, 38912\n\ts_mul_i32 s61, s60, 1920\n\t"
            "s_add_u32 s61, s61, s14\n\tbuffer_load_dwordx4 v40, s[40:43], s61 offen lds\n\tbuffer_load_dwordx4 v41, s[40:43], s61 offen offset:1024 lds\n\tbuffer_load_dwordx4 v42, s[40:43], s61 offen offset:2048 lds\n\tbuffer_load_dwordx4 v43, s[40:43], s61 offen offset:3072 lds\n\ts_addk_i32 m0, 0x1000\n\t"
            "s_add_u32 s68, s60, 128\n\tbuffer_load_dwordx4 v44, s[40:43], s61 offen lds\n\tbuffer_load_dwordx4 v45, s[40:43], s61 offen offset:1024 lds\n\ts_add_u32 m0, s65, 38912\n\ts_cmp_eq_u32 s68, s17\n\tbuffer_load_dwordx4 v36, s[36:39], s60 offen lds\n\t"
            "buffer_load_dwordx4 v37, s[36:39], s60 offen offset:1024 lds\n\tbuffer_load_dwordx4 v38, s[36:39], s60 offen offset:2048 lds\n\ts_mov_b32 exec_hi, 0\n\tbuffer_load_dwordx4 v39, s[36:39], s60 offen offset:3072 lds\n\ts_mov_b32 exec_hi, -1\n\ts_cselect_b32 s60, s63, s68\n\t"
            "s_mov_b32 m0, s30\n\ts_nop 0\n\tbuffer_load_dword v97, s[44:47], 0 offen sc1 lds\n\ts_waitcnt vmcnt(11)\n\ts_barrier\n\ts_add_u32 m0, s15, 40960\n\t"
            "s_nop 0\n\tbuffer_load_dwordx4 v156, s[48:51], s20 offen lds\n\tbuffer_load_dwordx4 v156, s[48:51], s21 offen offset:1024 lds\n\tbuffer_load_dwordx4 v156, s[48:51], s22 offen offset:2048 lds\n\tbuffer_load_dwordx4 v156, s[48:51], s23 offen offset:3072 lds\n\ts_add_u32 m0, s15, 45056\n\t"
            "s_nop 0\n\tbuffer_load_dwordx4 v156, s[48:51], s24 offen lds\n\tbuffer_load_dwordx4 v156, s[48:51], s25 offen offset:1024 lds\n\tbuffer_load_dwordx4 v156, s[48:51], s26 offen offset:2048 lds\n\ts_waitcnt vmcnt(7)\n\ts_barrier\n\t"
            "s_add_u32 m0, s15, 45056\n\ts_nop 0\n\tbuffer_load_dwordx4 v156, s[48:51], s27 offen offset:3072 lds\n\ts_add_u32 m0, s15, 49152\n\ts_nop 0\n\tbuffer_load_dwordx4 v156, s[48:51], s28 offen lds\n\t"
            "buffer_load_dwordx4 v156, s[48:51], s29 offen offset:1024 lds\n\ts_add_u32 m0, s15, 0\n\ts_nop 0\n\tbuffer_load_dwordx4 v154, s[48:51], s20 offen lds\n\tbuffer_load_dwordx4 v154, s[48:51], s21 offen offset:1024 lds\n\tbuffer_load_dwordx4 v154, s[48:51], s22 offen offset:2048 lds\n\t"
            "buffer_load_dwordx4 v154, s[48:51], s23 offen offset:3072 lds\n\ts_add_u32 m0, s15, 4096\n\ts_nop 0\n\tbuffer_load_dwordx4 v154, s[48:51], s24 offen lds\n\tbuffer_load_dwordx4 v154, s[48:51], s25 offen offset:1024 lds\n\tbuffer_load_dwordx4 v154, s[48:51], s26 offen offset:2048 lds\n\t"
            "buffer_load_dwordx4 v154, s[48:51], s27 offen offset:3072 lds\n\ts_add_u32 m0, s15, 8192\n\ts_nop 0\n\tbuffer_load_dwordx4 v154, s[48:51], s28 offen lds\n\tbuffer_load_dwordx4 v154, s[48:51], s29 offen offset:1024 lds\n\ts_cmp_lg_u32 s56, 0\n\t"
            "s_cbranch_scc1 1f\n\ts_mov_b32 m0, 162624\n\ts_nop 0\n\tbuffer_load_dwordx4 v104, s[44:47], 0 offen sc1 lds\n\t1:\nbuffer_load_dwordx4 v[240:243], v3, s[36:39], 0 offen offset:0\n\t"
            "buffer_load_dwordx4 v[244:247], v11, s[36:39], 0 offen offset:0\n\tbuffer_load_dwordx4 v[248:251], v3, s[36:39], 0 offen offset:64\n\tbuffer_load_dwordx4 v[252:255], v11, s[36:39], 0 offen offset:64\n\tds_read_b32 v20, v9 offset:256\n\tds_read_b32 v21, v10 offset:832\n\tds_read2st64_b32 v[138:139], v153 offset1:1\n\t"
            "ds_read2st64_b32 v[176:177], v160 offset1:1\n\tds_read2st64_b32 v[178:179], v161 offset1:1\n\tds_read2st64_b32 v[180:181], v162 offset1:1\n\tds_read2st64_b32 v[182:183], v163 offset1:1\n\tds_read2st64_b32 v[184:185], v164 offset1:1\n\tds_read2st64_b32 v[186:187], v165 offset1:1\n\t"
            "ds_read2st64_b32 v[188:189], v166 offset1:1\n\tds_read2st64_b32 v[190:191], v167 offset1:1\n\ts_mov_b32 s68, 0x40000\n\ts_waitcnt lgkmcnt(8)\n\tv_mul_f32 v138, 0x3ea52b5c, v138\n\tv_mul_f32 v139, 0x3ea52b5c, v139\n\t"
            "s_waitcnt lgkmcnt(7)\n\tv_mul_f32 v192, v176, v138\n\tv_mul_f32 v200, v177, v139\n\ts_waitcnt lgkmcnt(6)\n\tv_mul_f32 v193, v178, v138\n\tv_mul_f32 v201, v179, v139\n\t"
            "s_waitcnt lgkmcnt(5)\n\tv_mul_f32 v194, v180, v138\n\tv_mul_f32 v202, v181, v139\n\ts_waitcnt lgkmcnt(4)\n\tv_mul_f32 v195, v182, v138\n\tv_mul_f32 v203, v183, v139\n\t"
            "s_waitcnt lgkmcnt(3)\n\tv_mul_f32 v196, v184, v138\n\tv_mul_f32 v204, v185, v139\n\ts_waitcnt lgkmcnt(2)\n\tv_mul_f32 v197, v186, v138\n\tv_mul_f32 v205, v187, v139\n\t"
            "s_waitcnt lgkmcnt(1)\n\tv_mul_f32 v198, v188, v138\n\tv_mul_f32 v206, v189, v139\n\ts_waitcnt lgkmcnt(0)\n\tv_mul_f32 v199, v190, v138\n\tv_mul_f32 v207, v191, v139\n\t"
            "s_mov_b32 s19, 0\n\ts_barrier\n\t"
            SB_(1) PRIS
            "50:\nds_read2_b64 v[208:211], v24 offset0:0 offset1:16\n\tds_read2_b64 v[212:215], v24 offset0:32 offset1:48\n\tds_read2_b64 v[216:219], v26 offset0:0 offset1:16\n\tds_read2_b64 v[220:223], v26 offset0:32 offset1:48\n\ts_cmp_eq_u32 s16, 0\n\t"
            "s_cbranch_scc1 56f\n\t58:\ns_cbranch_scc0 1f\n\tbuffer_load_dword v232, v31, s[44:47], s31 offen offset:0 sc1\n\tbuffer_load_dword v233, v31, s[44:47], s31 offen offset:256 sc1\n\tbuffer_load_dword v234, v31, s[44:47], s31 offen offset:512 sc1\n\t"
            "buffer_load_dword v235, v31, s[44:47], s31 offen offset:768 sc1\n\t1:\ns_waitcnt lgkmcnt(1)\n\tv_add_f32 v208, v208, v216\n\tv_add_f32 v209, v209, v217\n\tv_add_f32 v210, v210, v218\n\t"
            "v_add_f32 v211, v211, v219\n\ts_waitcnt lgkmcnt(0)\n\tv_add_f32 v212, v212, v220\n\tv_add_f32 v213, v213, v221\n\tv_add_f32 v214, v214, v222\n\tv_add_f32 v215, v215, v223\n\t"
            "v_fma_f32 v208, v208, v192, v128\n\tv_fma_f32 v209, v209, v193, v129\n\tv_fma_f32 v210, v210, v194, v130\n\tv_fma_f32 v211, v211, v195, v131\n\tv_fma_f32 v212, v212, v196, v132\n\tv_fma_f32 v213, v213, v197, v133\n\t"
            "v_fma_f32 v214, v214, v198, v134\n\tv_fma_f32 v215, v215, v199, v135\n\tv_max3_f32 v136, v208, v209, v210\n\tv_max3_f32 v137, v211, v212, v213\n\tv_max3_f32 v136, v136, v214, v215\n\tv_max_f32 v136, v136, v137\n\t"
            "v_sub_f32 v208, v208, v136\n\tv_sub_f32 v209, v209, v136\n\tv_sub_f32 v210, v210, v136\n\tv_sub_f32 v211, v211, v136\n\tv_sub_f32 v212, v212, v136\n\tv_sub_f32 v213, v213, v136\n\t"
            "v_sub_f32 v214, v214, v136\n\tv_sub_f32 v215, v215, v136\n\tv_exp_f32 v208, v208\n\tv_exp_f32 v209, v209\n\tv_exp_f32 v210, v210\n\tv_exp_f32 v211, v211\n\t"
            "v_exp_f32 v212, v212\n\tv_exp_f32 v213, v213\n\tv_exp_f32 v214, v214\n\tv_exp_f32 v215, v215\n\tv_add_f32 v216, v208, v209\n\tv_add_f32 v217, v210, v211\n\t"
            "v_add_f32 v218, v212, v213\n\tv_add_f32 v219, v214, v215\n\tv_add_f32 v216, v216, v217\n\tv_add_f32 v218, v218, v219\n\tv_mul_f32 v208, v208, v176\n\tv_add_f32 v216, v216, v218\n\t"
            "v_mul_f32 v216, 0xbfb8aa3b, v216\n\tv_rcp_f32 v216, v216\n\tv_mul_f32 v209, v209, v178\n\tv_mul_f32 v210, v210, v180\n\tv_mul_f32 v211, v211, v182\n\tv_mul_f32 v212, v212, v184\n\t"
            "v_mul_f32 v213, v213, v186\n\tv_mul_f32 v214, v214, v188\n\tv_mul_f32 v215, v215, v190\n\tv_mul_f32 v208, v208, v216\n\tv_mul_f32 v209, v209, v216\n\tv_mul_f32 v210, v210, v216\n\t"
            "v_mul_f32 v211, v211, v216\n\tv_mul_f32 v212, v212, v216\n\tv_mul_f32 v213, v213, v216\n\tv_mul_f32 v214, v214, v216\n\tv_mul_f32 v215, v215, v216\n\tv_cvt_pk_bf16_f32 v220, v208, v209\n\t"
            "v_cvt_pk_bf16_f32 v221, v210, v211\n\tv_cvt_pk_bf16_f32 v222, v212, v213\n\tv_cvt_pk_bf16_f32 v223, v214, v215\n\tds_write_b128 v29, v[220:223] offset:0\n\tds_write_b32 v30, v142 offset:16\n\t"
            SP0 PRIW
            "51:\nds_read_b32 v136, v28\n\tds_read2_b64 v[208:211], v25 offset0:0 offset1:16\n\tds_read2_b64 v[212:215], v25 offset0:32 offset1:48\n\tds_read2_b64 v[216:219], v27 offset0:0 offset1:16\n\tds_read2_b64 v[220:223], v27 offset0:32 offset1:48\n\t"
            "s_waitcnt lgkmcnt(4)\n\tv_cmp_eq_u32 vcc, 0, v136\n\ts_cbranch_vccnz 53f\n\t55:\n"
            PRI1
            "s_cmp_eq_u32 s16, 0\n\ts_cbranch_scc1 57f\n\t59:\ns_waitcnt lgkmcnt(1)\n\tv_add_f32 v208, v208, v216\n\tv_add_f32 v209, v209, v217\n\t"
            "v_add_f32 v210, v210, v218\n\tv_add_f32 v211, v211, v219\n\ts_waitcnt lgkmcnt(0)\n\tv_add_f32 v212, v212, v220\n\tv_add_f32 v213, v213, v221\n\tv_add_f32 v214, v214, v222\n\t"
            "v_add_f32 v215, v215, v223\n\tv_mul_f32 v208, v208, v200\n\tv_mul_f32 v209, v209, v201\n\tv_mul_f32 v210, v210, v202\n\tv_mul_f32 v211, v211, v203\n\tv_mul_f32 v212, v212, v204\n\t"
            "v_mul_f32 v213, v213, v205\n\tv_mul_f32 v214, v214, v206\n\tv_mul_f32 v215, v215, v207\n\tv_max3_f32 v136, v208, v209, v210\n\tv_max3_f32 v137, v211, v212, v213\n\tv_max3_f32 v136, v136, v214, v215\n\t"
            "v_max_f32 v136, v136, v137\n\tv_sub_f32 v208, v208, v136\n\tv_sub_f32 v209, v209, v136\n\tv_sub_f32 v210, v210, v136\n\tv_sub_f32 v211, v211, v136\n\tv_sub_f32 v212, v212, v136\n\t"
            "v_sub_f32 v213, v213, v136\n\tv_sub_f32 v214, v214, v136\n\tv_sub_f32 v215, v215, v136\n\tv_exp_f32 v208, v208\n\tv_exp_f32 v209, v209\n\tv_exp_f32 v210, v210\n\t"
            "v_exp_f32 v211, v211\n\tv_exp_f32 v212, v212\n\tv_exp_f32 v213, v213\n\tv_exp_f32 v214, v214\n\tv_exp_f32 v215, v215\n\tv_add_f32 v216, v208, v209\n\t"
            "v_add_f32 v217, v210, v211\n\tv_add_f32 v218, v212, v213\n\tv_add_f32 v219, v214, v215\n\tv_add_f32 v216, v216, v217\n\tv_add_f32 v218, v218, v219\n\tv_mul_f32 v208, v208, v177\n\t"
            "v_add_f32 v216, v216, v218\n\tv_mul_f32 v216, 0xbfb8aa3b, v216\n\tv_rcp_f32 v216, v216\n\tv_mul_f32 v209, v209, v179\n\tv_mul_f32 v210, v210, v181\n\tv_mul_f32 v211, v211, v183\n\t"
            "v_mul_f32 v212, v212, v185\n\tv_mul_f32 v213, v213, v187\n\tv_mul_f32 v214, v214, v189\n\tv_mul_f32 v215, v215, v191\n\tv_mul_f32 v208, v208, v216\n\tv_mul_f32 v209, v209, v216\n\t"
            "v_mul_f32 v210, v210, v216\n\tv_mul_f32 v211, v211, v216\n\tv_mul_f32 v212, v212, v216\n\tv_mul_f32 v213, v213, v216\n\tv_mul_f32 v214, v214, v216\n\tv_mul_f32 v215, v215, v216\n\t"
            "v_cvt_pk_bf16_f32 v220, v208, v209\n\tv_cvt_pk_bf16_f32 v221, v210, v211\n\tv_cvt_pk_bf16_f32 v222, v212, v213\n\tv_cvt_pk_bf16_f32 v223, v214, v215\n\tds_write_b128 v29, v[220:223] offset:38912\n\tds_write_b32 v30, v30 offset:16\n\t"
            SB_(2) PRIG
            "s_waitcnt vmcnt(0)\n\tds_write_b32 v30, v30 offset:48\n\ts_branch .LOL\n\t30:\nbuffer_load_dwordx4 v[106:109], v158, s[44:47], 0 offen sc1\n\tbuffer_load_dwordx4 v[110:113], v159, s[44:47], 0 offen sc1\n\t"
            "buffer_load_dwordx4 v[114:117], v104, s[44:47], 0 offen sc1\n\tbuffer_load_dword v118, v96, s[44:47], 0 offen sc1\n\t"
            GINC
            "s_waitcnt vmcnt(3)\n\tv_xor_b32 v106, s67, v107\n\tv_xor_b32 v108, s67, v109\n\ts_waitcnt vmcnt(2)\n\tv_xor_b32 v110, s67, v111\n\tv_xor_b32 v112, s67, v113\n\t"
            "s_waitcnt vmcnt(1)\n\tv_xor_b32 v114, s67, v115\n\tv_xor_b32 v116, s67, v117\n\ts_waitcnt vmcnt(0)\n\tv_xor_b32 v118, s67, v118\n\tv_or3_b32 v106, v106, v108, v110\n\t"
            "v_or3_b32 v118, v118, v114, v116\n\tv_or3_b32 v106, v106, v112, v118\n\tv_cmp_ne_u32 vcc, 0, v106\n\ts_cbranch_vccz 31b\n\ts_sub_u32 s68, s68, 1\n\ts_cbranch_scc0 30b\n\t"
            "s_branch 31b\n\t53:\n"
            PSLP
            "s_sub_u32 s68, s68, 1\n\ts_cbranch_scc0 51b\n\ts_branch 55b\n\t56:\n"
            RSL0
            "buffer_load_dword v224, v31, s[44:47], 0 offen offset:0 sc1\n\tbuffer_load_dword v225, v31, s[44:47], 0 offen offset:256 sc1\n\tbuffer_load_dword v226, v31, s[44:47], 0 offen offset:512 sc1\n\tbuffer_load_dword v227, v31, s[44:47], 0 offen offset:768 sc1\n\ts_waitcnt vmcnt(4)\n\t"
            PRIR
            "s_call_b64 s[76:77], .LG0\n\t"
            PRIX
            "s_branch 2f\n\t1:\ns_waitcnt vmcnt(3)\n\tbuffer_load_dword v224, v31, s[44:47], 0 offen offset:0 sc1\n\ts_waitcnt vmcnt(3)\n\tbuffer_load_dword v225, v31, s[44:47], 0 offen offset:256 sc1\n\t"
            "s_waitcnt vmcnt(3)\n\tbuffer_load_dword v226, v31, s[44:47], 0 offen offset:512 sc1\n\ts_waitcnt vmcnt(3)\n\tbuffer_load_dword v227, v31, s[44:47], 0 offen offset:768 sc1\n\ts_cmp_lg_u32 s19, 0\n\ts_cbranch_scc1 3f\n\t"
            "s_call_b64 s[76:77], .LG1\n\t3:\ns_cmp_eq_u32 s19, 2\n\ts_cbranch_scc1 2f\n\ts_call_b64 s[76:77], .LG2\n\t2:\n"
            "s_waitcnt vmcnt(0) lgkmcnt(0)\n\tds_write_b32 v30, v30 offset:48\n\tv_max3_u32 v136, v224, v225, v226\n\tv_max_u32 v136, v136, v227\n\tv_cmp_eq_u32 vcc, -1, v136\n\tv_lshlrev_b32 v216, 16, v224\n\t"
            "v_and_b32 v217, 0xffff0000, v224\n\tv_lshlrev_b32 v218, 16, v225\n\tv_and_b32 v219, 0xffff0000, v225\n\tv_lshlrev_b32 v220, 16, v226\n\tv_and_b32 v221, 0xffff0000, v226\n\tv_lshlrev_b32 v222, 16, v227\n\t"
            "v_and_b32 v223, 0xffff0000, v227\n\t"
            LINC
            "s_cmp_eq_u32 0, 0\n\ts_cbranch_vccz 58b\n\ts_sub_u32 s68, s68, 1\n\ts_cbranch_scc0 1b\n\ts_branch 58b\n\t57:\n"
            "s_branch 2f\n\t1:\nbuffer_load_dword v232, v31, s[44:47], s31 offen offset:0 sc1\n\tbuffer_load_dword v233, v31, s[44:47], s31 offen offset:256 sc1\n\tbuffer_load_dword v234, v31, s[44:47], s31 offen offset:512 sc1\n\tbuffer_load_dword v235, v31, s[44:47], s31 offen offset:768 sc1\n\t"
            "s_cmp_lg_u32 s19, 0\n\ts_cbranch_scc1 3f\n\ts_call_b64 s[76:77], .LG1\n\t3:\ns_cmp_eq_u32 s19, 2\n\ts_cbranch_scc1 2f\n\t"
            "s_call_b64 s[76:77], .LG2\n\t2:\ns_waitcnt vmcnt(0) lgkmcnt(0)\n\tv_max3_u32 v136, v232, v233, v234\n\tv_max_u32 v136, v136, v235\n\tv_cmp_eq_u32 vcc, -1, v136\n\t"
            "v_lshlrev_b32 v216, 16, v232\n\tv_and_b32 v217, 0xffff0000, v232\n\tv_lshlrev_b32 v218, 16, v233\n\tv_and_b32 v219, 0xffff0000, v233\n\tv_lshlrev_b32 v220, 16, v234\n\tv_and_b32 v221, 0xffff0000, v234\n\t"
            "v_lshlrev_b32 v222, 16, v235\n\tv_and_b32 v223, 0xffff0000, v235\n\t"
            LINC
            "s_cmp_eq_u32 0, 0\n\ts_cbranch_vccz 59b\n\ts_sub_u32 s68, s68, 1\n\ts_cbranch_scc0 1b\n\ts_branch 59b\n\t.LOL:\n"
            "s_mov_b32 s71, 1\n\ts_mov_b32 s68, 0x40000\n\ts_cmp_eq_u32 s16, 0\n\ts_cbranch_scc1 3f\n\ts_call_b64 s[76:77], .LG0\n\t3:\n"
            "ds_read_b64 v[14:15], v8 offset:32\n\ts_waitcnt lgkmcnt(0)\n\tv_min_u32 v12, v14, v15\n\tv_cmp_lt_u32 vcc, s71, v12\n\ts_cbranch_vccnz 4f\n\ts_sleep 1\n\t"
            "s_sub_u32 s68, s68, 1\n\ts_cbranch_scc0 3b\n\t4:\ns_cmp_eq_u32 s16, 0\n\ts_cbranch_scc1 1f\n\ts_call_b64 s[76:77], .LG1\n\t"
            "1:\ns_cmp_lt_u32 s56, 2\n\ts_cbranch_scc1 1f\n\ts_sleep 3\n\t1:\n"
            SO(4) RZ
            "buffer_load_dwordx4 v[176:179], v2, s[44:47], s4 offen offset:1024 sc1\n\tbuffer_load_dwordx4 v[180:183], v2, s[44:47], s4 offen offset:2048 sc1\n\tbuffer_load_dwordx4 v[184:187], v2, s[44:47], s5 offen offset:1024 sc1\n\tbuffer_load_dwordx4 v[188:191], v2, s[44:47], s5 offen offset:2048 sc1\n\tbuffer_load_dwordx4 v[192:195], v2, s[44:47], s6 offen offset:1024 sc1\n\tbuffer_load_dwordx4 v[196:199], v2, s[44:47], s6 offen offset:2048 sc1\n\t"
            "s_cmp_lg_u32 s19, 0\n\ts_cbranch_scc1 1f\n\ts_call_b64 s[76:77], .LG1\n\t1:\ns_cmp_eq_u32 s19, 2\n\ts_cbranch_scc1 1f\n\t"
            "s_call_b64 s[76:77], .LG2\n\t1:\nbuffer_load_dwordx4 v[200:203], v2, s[44:47], s7 offen offset:1024 sc1\n\tbuffer_load_dwordx4 v[204:207], v2, s[44:47], s7 offen offset:2048 sc1\n\tbuffer_load_dwordx4 v[208:211], v2, s[44:47], s8 offen offset:1024 sc1\n\tbuffer_load_dwordx4 v[212:215], v2, s[44:47], s8 offen offset:2048 sc1\n\t"
            "ds_read_b128 v[16:19], v8 offset:48\n\tds_read_b64_tr_b16 v[128:129], v4 offset:40960\n\tds_read_b64_tr_b16 v[130:131], v4 offset:41472\n\tds_read_b64_tr_b16 v[132:133], v5 offset:40960\n\tds_read_b64_tr_b16 v[134:135], v5 offset:41472\n\tds_read_b64_tr_b16 v[136:137], v6 offset:40960\n\t"
            "ds_read_b64_tr_b16 v[138:139], v6 offset:41472\n\tds_read_b64_tr_b16 v[140:141], v7 offset:40960\n\tds_read_b64_tr_b16 v[142:143], v7 offset:41472\n\tds_read_b64_tr_b16 v[144:145], v4 offset:41984\n\tds_read_b64_tr_b16 v[146:147], v4 offset:42496\n\tds_read_b64_tr_b16 v[148:149], v5 offset:41984\n\t"
            "ds_read_b64_tr_b16 v[150:151], v5 offset:42496\n\tds_read_b64_tr_b16 v[152:153], v6 offset:41984\n\tds_read_b64_tr_b16 v[154:155], v6 offset:42496\n\tds_read_b64_tr_b16 v[156:157], v7 offset:41984\n\tds_read_b64_tr_b16 v[158:159], v7 offset:42496\n\tv_mul_f32 v48, v48, v112\n\t"
            "v_mul_f32 v49, v49, v113\n\tv_mul_f32 v50, v50, v114\n\tv_mul_f32 v51, v51, v115\n\tv_mul_f32 v52, v52, v116\n\tv_mul_f32 v53, v53, v117\n\tv_mul_f32 v54, v54, v118\n\t"
            "v_mul_f32 v55, v55, v119\n\tv_mul_f32 v56, v56, v120\n\tv_mul_f32 v57, v57, v121\n\tv_mul_f32 v58, v58, v122\n\tv_mul_f32 v59, v59, v123\n\tv_mul_f32 v60, v60, v124\n\t"
            "v_mul_f32 v61, v61, v125\n\tv_mul_f32 v62, v62, v126\n\tv_mul_f32 v63, v63, v127\n\t"
            SO(3)
            "s_waitcnt lgkmcnt(15)\n\tv_min3_u32 v16, v16, v17, v18\n\tv_min_u32 v16, v16, v19\n\tv_cmp_eq_u32 vcc, 0, v16\n\ts_cbranch_vccnz 62f\n\t63:\n"
            PRIO
            "s_waitcnt vmcnt(8)\n\tv_max3_u32 v12, v176, v178, v180\n\tv_max_u32 v12, v12, v182\n\tv_cmp_eq_u32 vcc, -1, v12\n\ts_cbranch_vccnz 100f\n\t200:\n"
            SO(5)
            "s_waitcnt lgkmcnt(8)\n\tv_mfma_f32_32x32x16_bf16 v[96:111], v[128:131], v[176:179], 0\n\tds_read_b64_tr_b16 v[160:161], v4 offset:43008\n\tds_read_b64_tr_b16 v[162:163], v4 offset:43520\n\tv_mfma_f32_32x32x16_bf16 v[112:127], v[132:135], v[176:179], 0\n\tds_read_b64_tr_b16 v[164:165], v5 offset:43008\n\t"
            "ds_read_b64_tr_b16 v[166:167], v5 offset:43520\n\tbuffer_load_dwordx4 v[216:219], v2, s[44:47], s9 offen offset:1024 sc1\n\tv_mfma_f32_32x32x16_bf16 v[96:111], v[136:139], v[180:183], v[96:111]\n\tds_read_b64_tr_b16 v[168:169], v6 offset:43008\n\tds_read_b64_tr_b16 v[170:171], v6 offset:43520\n\tbuffer_load_dwordx4 v[220:223], v2, s[44:47], s9 offen offset:2048 sc1\n\t"
            "s_waitcnt vmcnt(8)\n\tv_max3_u32 v12, v184, v186, v188\n\tv_max_u32 v12, v12, v190\n\tv_cmp_eq_u32 vcc, -1, v12\n\tv_mfma_f32_32x32x16_bf16 v[112:127], v[140:143], v[180:183], v[112:127]\n\tds_read_b64_tr_b16 v[172:173], v7 offset:43008\n\t"
            "ds_read_b64_tr_b16 v[174:175], v7 offset:43520\n\ts_cbranch_vccnz 101f\n\t201:\ns_waitcnt lgkmcnt(8)\n\tv_mfma_f32_32x32x16_bf16 v[96:111], v[144:147], v[184:187], v[96:111]\n\tds_read_b64_tr_b16 v[128:129], v4 offset:44032\n\t"
            "ds_read_b64_tr_b16 v[130:131], v4 offset:44544\n\tv_mfma_f32_32x32x16_bf16 v[112:127], v[148:151], v[184:187], v[112:127]\n\tds_read_b64_tr_b16 v[132:133], v5 offset:44032\n\tds_read_b64_tr_b16 v[134:135], v5 offset:44544\n\tbuffer_load_dwordx4 v[224:227], v2, s[44:47], s10 offen offset:1024 sc1\n\tv_mfma_f32_32x32x16_bf16 v[96:111], v[152:155], v[188:191], v[96:111]\n\t"
            "ds_read_b64_tr_b16 v[136:137], v6 offset:44032\n\tds_read_b64_tr_b16 v[138:139], v6 offset:44544\n\tbuffer_load_dwordx4 v[228:231], v2, s[44:47], s10 offen offset:2048 sc1\n\ts_waitcnt vmcnt(8)\n\tv_max3_u32 v12, v192, v194, v196\n\tv_max_u32 v12, v12, v198\n\t"
            "v_cmp_eq_u32 vcc, -1, v12\n\tv_mfma_f32_32x32x16_bf16 v[112:127], v[156:159], v[188:191], v[112:127]\n\tds_read_b64_tr_b16 v[140:141], v7 offset:44032\n\tds_read_b64_tr_b16 v[142:143], v7 offset:44544\n\ts_cbranch_vccnz 102f\n\t202:\n"
            "s_waitcnt lgkmcnt(8)\n\tv_mfma_f32_32x32x16_bf16 v[96:111], v[160:163], v[192:195], v[96:111]\n\tds_read_b64_tr_b16 v[144:145], v4 offset:45056\n\tds_read_b64_tr_b16 v[146:147], v4 offset:45568\n\tv_mfma_f32_32x32x16_bf16 v[112:127], v[164:167], v[192:195], v[112:127]\n\tds_read_b64_tr_b16 v[148:149], v5 offset:45056\n\t"
            "ds_read_b64_tr_b16 v[150:151], v5 offset:45568\n\tbuffer_load_dwordx4 v[232:235], v2, s[44:47], s11 offen offset:1024 sc1\n\tv_mfma_f32_32x32x16_bf16 v[96:111], v[168:171], v[196:199], v[96:111]\n\tds_read_b64_tr_b16 v[152:153], v6 offset:45056\n\tds_read_b64_tr_b16 v[154:155], v6 offset:45568\n\tbuffer_load_dwordx4 v[236:239], v2, s[44:47], s11 offen offset:2048 sc1\n\t"
            "s_waitcnt vmcnt(8)\n\tv_max3_u32 v12, v200, v202, v204\n\tv_max_u32 v12, v12, v206\n\tv_cmp_eq_u32 vcc, -1, v12\n\tv_mfma_f32_32x32x16_bf16 v[112:127], v[172:175], v[196:199], v[112:127]\n\tds_read_b64_tr_b16 v[156:157], v7 offset:45056\n\t"
            "ds_read_b64_tr_b16 v[158:159], v7 offset:45568\n\ts_cbranch_vccnz 103f\n\t203:\ns_waitcnt lgkmcnt(8)\n\tv_mfma_f32_32x32x16_bf16 v[96:111], v[128:131], v[200:203], v[96:111]\n\tds_read_b64_tr_b16 v[160:161], v4 offset:46080\n\t"
            "ds_read_b64_tr_b16 v[162:163], v4 offset:46592\n\tv_mfma_f32_32x32x16_bf16 v[112:127], v[132:135], v[200:203], v[112:127]\n\tds_read_b64_tr_b16 v[164:165], v5 offset:46080\n\tds_read_b64_tr_b16 v[166:167], v5 offset:46592\n\tds_read_b64 v[14:15], v8 offset:40\n\tbuffer_load_dwordx4 v[176:179], v2, s[44:47], s12 offen offset:1024 sc1\n\t"
            "v_mfma_f32_32x32x16_bf16 v[96:111], v[136:139], v[204:207], v[96:111]\n\tds_read_b64_tr_b16 v[168:169], v6 offset:46080\n\tds_read_b64_tr_b16 v[170:171], v6 offset:46592\n\tbuffer_load_dwordx4 v[180:183], v2, s[44:47], s12 offen offset:2048 sc1\n\ts_waitcnt vmcnt(8)\n\tv_max3_u32 v12, v208, v210, v212\n\t"
            "v_max_u32 v12, v12, v214\n\tv_cmp_eq_u32 vcc, -1, v12\n\tv_mfma_f32_32x32x16_bf16 v[112:127], v[140:143], v[204:207], v[112:127]\n\tds_read_b64_tr_b16 v[172:173], v7 offset:46080\n\tds_read_b64_tr_b16 v[174:175], v7 offset:46592\n\ts_cbranch_vccnz 104f\n\t"
            "204:\ns_waitcnt lgkmcnt(9)\n\tv_mfma_f32_32x32x16_bf16 v[96:111], v[144:147], v[208:211], v[96:111]\n\tds_read_b64_tr_b16 v[128:129], v4 offset:47104\n\tds_read_b64_tr_b16 v[130:131], v4 offset:47616\n\tv_mfma_f32_32x32x16_bf16 v[112:127], v[148:151], v[208:211], v[112:127]\n\t"
            "ds_read_b64_tr_b16 v[132:133], v5 offset:47104\n\tds_read_b64_tr_b16 v[134:135], v5 offset:47616\n\tbuffer_load_dwordx4 v[184:187], v2, s[44:47], s13 offen offset:1024 sc1\n\tv_mfma_f32_32x32x16_bf16 v[96:111], v[152:155], v[212:215], v[96:111]\n\tds_read_b64_tr_b16 v[136:137], v6 offset:47104\n\tds_read_b64_tr_b16 v[138:139], v6 offset:47616\n\t"
            "buffer_load_dwordx4 v[188:191], v2, s[44:47], s13 offen offset:2048 sc1\n\ts_waitcnt vmcnt(8)\n\tv_max3_u32 v12, v216, v218, v220\n\tv_max_u32 v12, v12, v222\n\tv_cmp_eq_u32 vcc, -1, v12\n\tv_mfma_f32_32x32x16_bf16 v[112:127], v[156:159], v[212:215], v[112:127]\n\t"
            "ds_read_b64_tr_b16 v[140:141], v7 offset:47104\n\tds_read_b64_tr_b16 v[142:143], v7 offset:47616\n\ts_cbranch_vccnz 105f\n\t205:\ns_waitcnt lgkmcnt(8)\n\tv_mfma_f32_32x32x16_bf16 v[96:111], v[160:163], v[216:219], v[96:111]\n\t"
            "v_min_u32 v14, v14, v15\n\tv_cmp_ge_u32 vcc, s71, v14\n\tds_read_b64_tr_b16 v[144:145], v4 offset:48128\n\tds_read_b64_tr_b16 v[146:147], v4 offset:48640\n\tv_mfma_f32_32x32x16_bf16 v[112:127], v[164:167], v[216:219], v[112:127]\n\ts_cbranch_vccnz 5f\n\t"
            "6:\nds_read_b64_tr_b16 v[148:149], v5 offset:48128\n\tds_read_b64_tr_b16 v[150:151], v5 offset:48640\n\tbuffer_load_dwordx4 v[192:195], v2, s[44:47], s4 offen offset:0 sc1\n\tv_mfma_f32_32x32x16_bf16 v[96:111], v[168:171], v[220:223], v[96:111]\n\tds_read_b64_tr_b16 v[152:153], v6 offset:48128\n\t"
            "ds_read_b64_tr_b16 v[154:155], v6 offset:48640\n\tbuffer_load_dwordx4 v[196:199], v2, s[44:47], s4 offen offset:3072 sc1\n\ts_waitcnt vmcnt(8)\n\tv_max3_u32 v12, v224, v226, v228\n\tv_max_u32 v12, v12, v230\n\tv_cmp_eq_u32 vcc, -1, v12\n\t"
            "v_mfma_f32_32x32x16_bf16 v[112:127], v[172:175], v[220:223], v[112:127]\n\tds_read_b64_tr_b16 v[156:157], v7 offset:48128\n\tds_read_b64_tr_b16 v[158:159], v7 offset:48640\n\ts_cbranch_vccnz 106f\n\t206:\ns_waitcnt lgkmcnt(8)\n\t"
            "v_mfma_f32_32x32x16_bf16 v[96:111], v[128:131], v[224:227], v[96:111]\n\tds_read_b64_tr_b16 v[160:161], v4 offset:49152\n\tds_read_b64_tr_b16 v[162:163], v4 offset:49664\n\tv_mfma_f32_32x32x16_bf16 v[112:127], v[132:135], v[224:227], v[112:127]\n\tds_read_b64_tr_b16 v[164:165], v5 offset:49152\n\tds_read_b64_tr_b16 v[166:167], v5 offset:49664\n\t"
            "buffer_load_dwordx4 v[200:203], v2, s[44:47], s5 offen offset:0 sc1\n\tv_mfma_f32_32x32x16_bf16 v[96:111], v[136:139], v[228:231], v[96:111]\n\tds_read_b64_tr_b16 v[168:169], v6 offset:49152\n\tds_read_b64_tr_b16 v[170:171], v6 offset:49664\n\tbuffer_load_dwordx4 v[204:207], v2, s[44:47], s5 offen offset:3072 sc1\n\ts_waitcnt vmcnt(8)\n\t"
            "v_max3_u32 v12, v232, v234, v236\n\tv_max_u32 v12, v12, v238\n\tv_cmp_eq_u32 vcc, -1, v12\n\tv_mfma_f32_32x32x16_bf16 v[112:127], v[140:143], v[228:231], v[112:127]\n\tds_read_b64_tr_b16 v[172:173], v7 offset:49152\n\tds_read_b64_tr_b16 v[174:175], v7 offset:49664\n\t"
            "s_cbranch_vccnz 107f\n\t207:\ns_waitcnt lgkmcnt(8)\n\tv_mfma_f32_32x32x16_bf16 v[96:111], v[144:147], v[232:235], v[96:111]\n\tds_read_b64_tr_b16 v[128:129], v4 offset:50176\n\tds_read_b64_tr_b16 v[130:131], v4 offset:50688\n\t"
            "v_mfma_f32_32x32x16_bf16 v[112:127], v[148:151], v[232:235], v[112:127]\n\tds_read_b64_tr_b16 v[132:133], v5 offset:50176\n\tds_read_b64_tr_b16 v[134:135], v5 offset:50688\n\tbuffer_load_dwordx4 v[208:211], v2, s[44:47], s6 offen offset:0 sc1\n\tv_mfma_f32_32x32x16_bf16 v[96:111], v[152:155], v[236:239], v[96:111]\n\tds_read_b64_tr_b16 v[136:137], v6 offset:50176\n\t"
            "ds_read_b64_tr_b16 v[138:139], v6 offset:50688\n\tbuffer_load_dwordx4 v[212:215], v2, s[44:47], s6 offen offset:3072 sc1\n\ts_waitcnt vmcnt(8)\n\tv_max3_u32 v12, v176, v178, v180\n\tv_max_u32 v12, v12, v182\n\tv_cmp_eq_u32 vcc, -1, v12\n\t"
            "v_mfma_f32_32x32x16_bf16 v[112:127], v[156:159], v[236:239], v[112:127]\n\tds_read_b64_tr_b16 v[140:141], v7 offset:50176\n\tds_read_b64_tr_b16 v[142:143], v7 offset:50688\n\ts_cbranch_vccnz 108f\n\t208:\ns_waitcnt lgkmcnt(8)\n\t"
            "v_mfma_f32_32x32x16_bf16 v[96:111], v[160:163], v[176:179], v[96:111]\n\tds_read_b64_tr_b16 v[144:145], v4 offset:0\n\tds_read_b64_tr_b16 v[146:147], v4 offset:512\n\tv_mfma_f32_32x32x16_bf16 v[112:127], v[164:167], v[176:179], v[112:127]\n\tds_read_b64_tr_b16 v[148:149], v5 offset:0\n\tds_read_b64_tr_b16 v[150:151], v5 offset:512\n\t"
            "buffer_load_dwordx4 v[216:219], v2, s[44:47], s7 offen offset:0 sc1\n\tv_mfma_f32_32x32x16_bf16 v[96:111], v[168:171], v[180:183], v[96:111]\n\tds_read_b64_tr_b16 v[152:153], v6 offset:0\n\tds_read_b64_tr_b16 v[154:155], v6 offset:512\n\tbuffer_load_dwordx4 v[220:223], v2, s[44:47], s7 offen offset:3072 sc1\n\ts_waitcnt vmcnt(8)\n\t"
            "v_max3_u32 v12, v184, v186, v188\n\tv_max_u32 v12, v12, v190\n\tv_cmp_eq_u32 vcc, -1, v12\n\tv_mfma_f32_32x32x16_bf16 v[112:127], v[172:175], v[180:183], v[112:127]\n\tds_read_b64_tr_b16 v[156:157], v7 offset:0\n\tds_read_b64_tr_b16 v[158:159], v7 offset:512\n\t"
            "s_cbranch_vccnz 109f\n\t209:\ns_waitcnt lgkmcnt(8)\n\tv_mfma_f32_32x32x16_bf16 v[96:111], v[128:131], v[184:187], v[96:111]\n\tds_read_b64_tr_b16 v[160:161], v4 offset:1024\n\tds_read_b64_tr_b16 v[162:163], v4 offset:1536\n\t"
            "v_mfma_f32_32x32x16_bf16 v[112:127], v[132:135], v[184:187], v[112:127]\n\tds_read_b64_tr_b16 v[164:165], v5 offset:1024\n\tds_read_b64_tr_b16 v[166:167], v5 offset:1536\n\tbuffer_load_dwordx4 v[224:227], v2, s[44:47], s8 offen offset:0 sc1\n\tv_mfma_f32_32x32x16_bf16 v[96:111], v[136:139], v[188:191], v[96:111]\n\tds_read_b64_tr_b16 v[168:169], v6 offset:1024\n\t"
            "ds_read_b64_tr_b16 v[170:171], v6 offset:1536\n\tbuffer_load_dwordx4 v[228:231], v2, s[44:47], s8 offen offset:3072 sc1\n\ts_waitcnt vmcnt(8)\n\tv_max3_u32 v12, v192, v194, v196\n\tv_max_u32 v12, v12, v198\n\tv_cmp_eq_u32 vcc, -1, v12\n\t"
            "v_mfma_f32_32x32x16_bf16 v[112:127], v[140:143], v[188:191], v[112:127]\n\tds_read_b64_tr_b16 v[172:173], v7 offset:1024\n\tds_read_b64_tr_b16 v[174:175], v7 offset:1536\n\ts_cbranch_vccnz 110f\n\t210:\ns_waitcnt lgkmcnt(8)\n\t"
            "v_mfma_f32_32x32x16_bf16 v[96:111], v[144:147], v[192:195], v[96:111]\n\tds_read_b64_tr_b16 v[128:129], v4 offset:2048\n\tds_read_b64_tr_b16 v[130:131], v4 offset:2560\n\tv_mfma_f32_32x32x16_bf16 v[112:127], v[148:151], v[192:195], v[112:127]\n\tds_read_b64_tr_b16 v[132:133], v5 offset:2048\n\tds_read_b64_tr_b16 v[134:135], v5 offset:2560\n\t"
            "buffer_load_dwordx4 v[232:235], v2, s[44:47], s9 offen offset:0 sc1\n\tv_mfma_f32_32x32x16_bf16 v[96:111], v[152:155], v[196:199], v[96:111]\n\tds_read_b64_tr_b16 v[136:137], v6 offset:2048\n\tds_read_b64_tr_b16 v[138:139], v6 offset:2560\n\tbuffer_load_dwordx4 v[236:239], v2, s[44:47], s9 offen offset:3072 sc1\n\ts_waitcnt vmcnt(8)\n\t"
            "v_max3_u32 v12, v200, v202, v204\n\tv_max_u32 v12, v12, v206\n\tv_cmp_eq_u32 vcc, -1, v12\n\tv_mfma_f32_32x32x16_bf16 v[112:127], v[156:159], v[196:199], v[112:127]\n\tds_read_b64_tr_b16 v[140:141], v7 offset:2048\n\tds_read_b64_tr_b16 v[142:143], v7 offset:2560\n\t"
            "s_cbranch_vccnz 111f\n\t211:\ns_waitcnt lgkmcnt(8)\n\tv_mfma_f32_32x32x16_bf16 v[96:111], v[160:163], v[200:203], v[96:111]\n\tds_read_b64_tr_b16 v[144:145], v4 offset:3072\n\tds_read_b64_tr_b16 v[146:147], v4 offset:3584\n\t"
            "v_mfma_f32_32x32x16_bf16 v[112:127], v[164:167], v[200:203], v[112:127]\n\tds_read_b64_tr_b16 v[148:149], v5 offset:3072\n\tds_read_b64_tr_b16 v[150:151], v5 offset:3584\n\tbuffer_load_dwordx4 v[176:179], v2, s[44:47], s10 offen offset:0 sc1\n\tv_mfma_f32_32x32x16_bf16 v[96:111], v[168:171], v[204:207], v[96:111]\n\tds_read_b64_tr_b16 v[152:153], v6 offset:3072\n\t"
            "ds_read_b64_tr_b16 v[154:155], v6 offset:3584\n\tbuffer_load_dwordx4 v[180:183], v2, s[44:47], s10 offen offset:3072 sc1\n\ts_waitcnt vmcnt(8)\n\tv_max3_u32 v12, v208, v210, v212\n\tv_max_u32 v12, v12, v214\n\tv_cmp_eq_u32 vcc, -1, v12\n\t"
            "v_mfma_f32_32x32x16_bf16 v[112:127], v[172:175], v[204:207], v[112:127]\n\tds_read_b64_tr_b16 v[156:157], v7 offset:3072\n\tds_read_b64_tr_b16 v[158:159], v7 offset:3584\n\ts_cbranch_vccnz 112f\n\t212:\ns_waitcnt lgkmcnt(8)\n\t"
            "v_mfma_f32_32x32x16_bf16 v[96:111], v[128:131], v[208:211], v[96:111]\n\tds_read_b64_tr_b16 v[160:161], v4 offset:4096\n\tds_read_b64_tr_b16 v[162:163], v4 offset:4608\n\tv_mfma_f32_32x32x16_bf16 v[112:127], v[132:135], v[208:211], v[112:127]\n\tds_read_b64_tr_b16 v[164:165], v5 offset:4096\n\tds_read_b64_tr_b16 v[166:167], v5 offset:4608\n\t"
            "buffer_load_dwordx4 v[184:187], v2, s[44:47], s11 offen offset:0 sc1\n\tv_mfma_f32_32x32x16_bf16 v[96:111], v[136:139], v[212:215], v[96:111]\n\tds_read_b64_tr_b16 v[168:169], v6 offset:4096\n\tds_read_b64_tr_b16 v[170:171], v6 offset:4608\n\tbuffer_load_dwordx4 v[188:191], v2, s[44:47], s11 offen offset:3072 sc1\n\ts_waitcnt vmcnt(8)\n\t"
            "v_max3_u32 v12, v216, v218, v220\n\tv_max_u32 v12, v12, v222\n\tv_cmp_eq_u32 vcc, -1, v12\n\tv_mfma_f32_32x32x16_bf16 v[112:127], v[140:143], v[212:215], v[112:127]\n\tds_read_b64_tr_b16 v[172:173], v7 offset:4096\n\tds_read_b64_tr_b16 v[174:175], v7 offset:4608\n\t"
            "s_cbranch_vccnz 113f\n\t213:\ns_waitcnt lgkmcnt(8)\n\tv_mfma_f32_32x32x16_bf16 v[96:111], v[144:147], v[216:219], v[96:111]\n\tds_read_b64_tr_b16 v[128:129], v4 offset:5120\n\tds_read_b64_tr_b16 v[130:131], v4 offset:5632\n\t"
            "v_mfma_f32_32x32x16_bf16 v[112:127], v[148:151], v[216:219], v[112:127]\n\tds_read_b64_tr_b16 v[132:133], v5 offset:5120\n\tds_read_b64_tr_b16 v[134:135], v5 offset:5632\n\tbuffer_load_dwordx4 v[192:195], v2, s[44:47], s12 offen offset:0 sc1\n\tv_mfma_f32_32x32x16_bf16 v[96:111], v[152:155], v[220:223], v[96:111]\n\tds_read_b64_tr_b16 v[136:137], v6 offset:5120\n\t"
            "ds_read_b64_tr_b16 v[138:139], v6 offset:5632\n\tbuffer_load_dwordx4 v[196:199], v2, s[44:47], s12 offen offset:3072 sc1\n\ts_waitcnt vmcnt(8)\n\tv_max3_u32 v12, v224, v226, v228\n\tv_max_u32 v12, v12, v230\n\tv_cmp_eq_u32 vcc, -1, v12\n\t"
            "v_mfma_f32_32x32x16_bf16 v[112:127], v[156:159], v[220:223], v[112:127]\n\tds_read_b64_tr_b16 v[140:141], v7 offset:5120\n\tds_read_b64_tr_b16 v[142:143], v7 offset:5632\n\ts_cbranch_vccnz 114f\n\t214:\ns_waitcnt lgkmcnt(8)\n\t"
            "v_mfma_f32_32x32x16_bf16 v[96:111], v[160:163], v[224:227], v[96:111]\n\tds_read_b64_tr_b16 v[144:145], v4 offset:6144\n\tds_read_b64_tr_b16 v[146:147], v4 offset:6656\n\tv_mfma_f32_32x32x16_bf16 v[112:127], v[164:167], v[224:227], v[112:127]\n\tds_read_b64_tr_b16 v[148:149], v5 offset:6144\n\tds_read_b64_tr_b16 v[150:151], v5 offset:6656\n\t"
            "buffer_load_dwordx4 v[200:203], v2, s[44:47], s13 offen offset:0 sc1\n\tv_mfma_f32_32x32x16_bf16 v[96:111], v[168:171], v[228:231], v[96:111]\n\tds_read_b64_tr_b16 v[152:153], v6 offset:6144\n\tds_read_b64_tr_b16 v[154:155], v6 offset:6656\n\tbuffer_load_dwordx4 v[204:207], v2, s[44:47], s13 offen offset:3072 sc1\n\ts_waitcnt vmcnt(8)\n\t"
            "v_max3_u32 v12, v232, v234, v236\n\tv_max_u32 v12, v12, v238\n\tv_cmp_eq_u32 vcc, -1, v12\n\tv_mfma_f32_32x32x16_bf16 v[112:127], v[172:175], v[228:231], v[112:127]\n\tds_read_b64_tr_b16 v[156:157], v7 offset:6144\n\tds_read_b64_tr_b16 v[158:159], v7 offset:6656\n\t"
            "s_cbranch_vccnz 115f\n\t215:\ns_waitcnt lgkmcnt(8)\n\tv_mfma_f32_32x32x16_bf16 v[96:111], v[128:131], v[232:235], v[96:111]\n\tds_read_b64_tr_b16 v[160:161], v4 offset:7168\n\tds_read_b64_tr_b16 v[162:163], v4 offset:7680\n\t"
            "v_mfma_f32_32x32x16_bf16 v[112:127], v[132:135], v[232:235], v[112:127]\n\tds_read_b64_tr_b16 v[164:165], v5 offset:7168\n\tds_read_b64_tr_b16 v[166:167], v5 offset:7680\n\tv_mfma_f32_32x32x16_bf16 v[96:111], v[136:139], v[236:239], v[96:111]\n\tds_read_b64_tr_b16 v[168:169], v6 offset:7168\n\tds_read_b64_tr_b16 v[170:171], v6 offset:7680\n\t"
            "s_waitcnt vmcnt(6)\n\tv_max3_u32 v12, v176, v178, v180\n\tv_max_u32 v12, v12, v182\n\tv_cmp_eq_u32 vcc, -1, v12\n\tv_mfma_f32_32x32x16_bf16 v[112:127], v[140:143], v[236:239], v[112:127]\n\tds_read_b64_tr_b16 v[172:173], v7 offset:7168\n\t"
            "ds_read_b64_tr_b16 v[174:175], v7 offset:7680\n\ts_cbranch_vccnz 116f\n\t216:\ns_waitcnt lgkmcnt(8)\n\tv_mfma_f32_32x32x16_bf16 v[96:111], v[144:147], v[176:179], v[96:111]\n\tds_read_b64_tr_b16 v[128:129], v4 offset:8192\n\t"
            "ds_read_b64_tr_b16 v[130:131], v4 offset:8704\n\tv_mfma_f32_32x32x16_bf16 v[112:127], v[148:151], v[176:179], v[112:127]\n\tds_read_b64_tr_b16 v[132:133], v5 offset:8192\n\tds_read_b64_tr_b16 v[134:135], v5 offset:8704\n\tv_mfma_f32_32x32x16_bf16 v[96:111], v[152:155], v[180:183], v[96:111]\n\tds_read_b64_tr_b16 v[136:137], v6 offset:8192\n\t"
            "ds_read_b64_tr_b16 v[138:139], v6 offset:8704\n\ts_waitcnt vmcnt(4)\n\tv_max3_u32 v12, v184, v186, v188\n\tv_max_u32 v12, v12, v190\n\tv_cmp_eq_u32 vcc, -1, v12\n\tv_mfma_f32_32x32x16_bf16 v[112:127], v[156:159], v[180:183], v[112:127]\n\t"
            "ds_read_b64_tr_b16 v[140:141], v7 offset:8192\n\tds_read_b64_tr_b16 v[142:143], v7 offset:8704\n\ts_cbranch_vccnz 117f\n\t217:\nds_read_b32 v16, v8 offset:64\n\tds_read_b32 v17, v9 offset:1344\n\t"
            "s_waitcnt lgkmcnt(10)\n\tv_mfma_f32_32x32x16_bf16 v[96:111], v[160:163], v[184:187], v[96:111]\n\tds_read_b64_tr_b16 v[144:145], v4 offset:9216\n\tds_read_b64_tr_b16 v[146:147], v4 offset:9728\n\tds_read_b64_tr_b16 v[148:149], v5 offset:9216\n\tds_read_b64_tr_b16 v[150:151], v5 offset:9728\n\t"
            "ds_read_b64_tr_b16 v[152:153], v6 offset:9216\n\tds_read_b64_tr_b16 v[154:155], v6 offset:9728\n\ts_waitcnt vmcnt(2)\n\tv_max3_u32 v12, v192, v194, v196\n\tv_max_u32 v12, v12, v198\n\tv_cmp_eq_u32 vcc, -1, v12\n\t"
            "v_mfma_f32_32x32x16_bf16 v[96:111], v[168:171], v[188:191], v[96:111]\n\tds_read_b64_tr_b16 v[156:157], v7 offset:9216\n\tds_read_b64_tr_b16 v[158:159], v7 offset:9728\n\ts_cbranch_vccnz 118f\n\t218:\ns_waitcnt lgkmcnt(10)\n\t"
            "v_mfma_f32_32x32x16_bf16 v[96:111], v[128:131], v[192:195], v[96:111]\n\ts_waitcnt vmcnt(0)\n\tv_max3_u32 v12, v200, v202, v204\n\tv_max_u32 v12, v12, v206\n\tv_cmp_eq_u32 vcc, -1, v12\n\tv_mfma_f32_32x32x16_bf16 v[96:111], v[136:139], v[196:199], v[96:111]\n\t"
            "s_cbranch_vccnz 119f\n\t219:\ns_waitcnt lgkmcnt(0)\n\tv_mfma_f32_32x32x16_bf16 v[96:111], v[144:147], v[200:203], v[96:111]\n\tv_cmp_ne_u32 s[74:75], 0, v17\n\tv_cmp_eq_u32 vcc, 0, v16\n\t"
            "v_mfma_f32_32x32x16_bf16 v[96:111], v[152:155], v[204:207], v[96:111]\n\ts_or_b64 vcc, vcc, s[74:75]\n\tv_mfma_f32_32x32x16_bf16 v[112:127], v[164:167], v[184:187], v[112:127]\n\ts_cbranch_scc1 7f\n\t8:\ns_nop 5\n\t"
            "v_mfma_f32_32x32x16_bf16 v[112:127], v[172:175], v[188:191], v[112:127]\n\ts_nop 1\n\tv_fmac_f32 v64, v96, v32\n\tv_fmac_f32 v65, v97, v33\n\tv_fmac_f32 v66, v98, v34\n\tv_fmac_f32 v67, v99, v35\n\t"
            "v_fmac_f32 v68, v100, v36\n\tv_fmac_f32 v69, v101, v37\n\tv_fmac_f32 v70, v102, v38\n\tv_mfma_f32_32x32x16_bf16 v[112:127], v[132:135], v[192:195], v[112:127]\n\tv_fmac_f32 v71, v103, v39\n\tv_fmac_f32 v72, v104, v40\n\t"
            "v_fmac_f32 v73, v105, v41\n\tv_fmac_f32 v74, v106, v42\n\tv_fmac_f32 v75, v107, v43\n\tv_fmac_f32 v76, v108, v44\n\tv_fmac_f32 v77, v109, v45\n\tv_mfma_f32_32x32x16_bf16 v[112:127], v[140:143], v[196:199], v[112:127]\n\t"
            "v_fmac_f32 v78, v110, v46\n\tv_fmac_f32 v79, v111, v47\n\tv_cvt_pk_bf16_f32 v240, v64, v65\n\tv_cvt_pk_bf16_f32 v242, v68, v69\n\tv_cvt_pk_bf16_f32 v241, v66, v67\n\tv_cvt_pk_bf16_f32 v243, v70, v71\n\t"
            "v_cvt_pk_bf16_f32 v244, v72, v73\n\tv_mfma_f32_32x32x16_bf16 v[112:127], v[148:151], v[200:203], v[112:127]\n\tv_cvt_pk_bf16_f32 v246, v76, v77\n\tv_cvt_pk_bf16_f32 v245, v74, v75\n\tv_cvt_pk_bf16_f32 v247, v78, v79\n\tv_permlane32_swap_b32 v240, v242\n\t"
            "v_permlane32_swap_b32 v241, v243\n\tv_permlane32_swap_b32 v244, v246\n\tv_permlane32_swap_b32 v245, v247\n\tv_mfma_f32_32x32x16_bf16 v[112:127], v[156:159], v[204:207], v[112:127]\n\tv_permlane16_swap_b32 v240, v244\n\tv_permlane16_swap_b32 v242, v246\n\t"
            "v_permlane16_swap_b32 v241, v245\n\tv_permlane16_swap_b32 v243, v247\n\tbuffer_store_dwordx4 v[240:243], v3, s[52:55], 0 offen offset:0 sc1\n\tbuffer_store_dwordx4 v[244:247], v11, s[52:55], 0 offen offset:0 sc1\n\t"
            SO(6)
            "s_nop 5\n\tv_fmac_f32 v80, v112, v48\n\tv_fmac_f32 v81, v113, v49\n\tv_fmac_f32 v82, v114, v50\n\tv_fmac_f32 v83, v115, v51\n\tv_fmac_f32 v84, v116, v52\n\t"
            "v_fmac_f32 v85, v117, v53\n\tv_fmac_f32 v86, v118, v54\n\tv_fmac_f32 v87, v119, v55\n\tv_fmac_f32 v88, v120, v56\n\tv_fmac_f32 v89, v121, v57\n\tv_fmac_f32 v90, v122, v58\n\t"
            "v_fmac_f32 v91, v123, v59\n\tv_fmac_f32 v92, v124, v60\n\tv_fmac_f32 v93, v125, v61\n\tv_fmac_f32 v94, v126, v62\n\tv_fmac_f32 v95, v127, v63\n\tv_cvt_pk_bf16_f32 v248, v80, v81\n\t"
            "v_cvt_pk_bf16_f32 v250, v84, v85\n\tv_cvt_pk_bf16_f32 v249, v82, v83\n\tv_cvt_pk_bf16_f32 v251, v86, v87\n\tv_cvt_pk_bf16_f32 v252, v88, v89\n\tv_cvt_pk_bf16_f32 v254, v92, v93\n\tv_cvt_pk_bf16_f32 v253, v90, v91\n\t"
            "v_cvt_pk_bf16_f32 v255, v94, v95\n\tv_permlane32_swap_b32 v248, v250\n\tv_permlane32_swap_b32 v249, v251\n\tv_permlane32_swap_b32 v252, v254\n\tv_permlane32_swap_b32 v253, v255\n\ts_nop 0\n\t"
            "v_permlane16_swap_b32 v248, v252\n\tv_permlane16_swap_b32 v250, v254\n\tv_permlane16_swap_b32 v249, v253\n\tv_permlane16_swap_b32 v251, v255\n\tbuffer_store_dwordx4 v[248:251], v3, s[52:55], 0 offen offset:64 sc1\n\tbuffer_store_dwordx4 v[252:255], v11, s[52:55], 0 offen offset:64 sc1\n\t"
            "9:\n"
            RCNT DUMP
            "s_endpgm\n\t.LG0:\nv_mul_f32 v21, 0xbfb8aa3b, v21\n\tv_mul_f32 v20, v20, v21\n\tv_permlane16_swap_b32 v240, v244\n\tv_permlane16_swap_b32 v241, v245\n\t"
            "v_permlane16_swap_b32 v242, v246\n\tv_permlane16_swap_b32 v243, v247\n\tv_permlane16_swap_b32 v248, v252\n\tv_permlane16_swap_b32 v249, v253\n\tv_permlane16_swap_b32 v250, v254\n\tv_permlane16_swap_b32 v251, v255\n\t"
            "v_permlane32_swap_b32 v240, v242\n\tv_permlane32_swap_b32 v241, v243\n\tv_permlane32_swap_b32 v244, v246\n\tv_permlane32_swap_b32 v245, v247\n\tv_permlane32_swap_b32 v248, v250\n\tv_permlane32_swap_b32 v249, v251\n\t"
            "v_permlane32_swap_b32 v252, v254\n\tv_permlane32_swap_b32 v253, v255\n\tv_lshlrev_b32 v64, 16, v240\n\tv_and_b32 v65, 0xffff0000, v240\n\tv_lshlrev_b32 v66, 16, v241\n\tv_and_b32 v67, 0xffff0000, v241\n\t"
            "v_lshlrev_b32 v68, 16, v242\n\tv_and_b32 v69, 0xffff0000, v242\n\tv_lshlrev_b32 v70, 16, v243\n\tv_and_b32 v71, 0xffff0000, v243\n\tv_lshlrev_b32 v72, 16, v244\n\tv_and_b32 v73, 0xffff0000, v244\n\t"
            "v_lshlrev_b32 v74, 16, v245\n\tv_and_b32 v75, 0xffff0000, v245\n\tv_lshlrev_b32 v76, 16, v246\n\tv_and_b32 v77, 0xffff0000, v246\n\tv_lshlrev_b32 v78, 16, v247\n\tv_and_b32 v79, 0xffff0000, v247\n\t"
            "v_lshlrev_b32 v80, 16, v248\n\tv_and_b32 v81, 0xffff0000, v248\n\tv_lshlrev_b32 v82, 16, v249\n\tv_and_b32 v83, 0xffff0000, v249\n\tv_lshlrev_b32 v84, 16, v250\n\tv_and_b32 v85, 0xffff0000, v250\n\t"
            "v_lshlrev_b32 v86, 16, v251\n\tv_and_b32 v87, 0xffff0000, v251\n\tv_lshlrev_b32 v88, 16, v252\n\tv_and_b32 v89, 0xffff0000, v252\n\tv_lshlrev_b32 v90, 16, v253\n\tv_and_b32 v91, 0xffff0000, v253\n\t"
            "v_lshlrev_b32 v92, 16, v254\n\tv_and_b32 v93, 0xffff0000, v254\n\tv_lshlrev_b32 v94, 16, v255\n\tv_and_b32 v95, 0xffff0000, v255\n\tv_fma_f32 v96, v64, v21, v20\n\tv_fma_f32 v97, v65, v21, v20\n\t"
            "v_fma_f32 v98, v66, v21, v20\n\tv_fma_f32 v99, v67, v21, v20\n\tv_fma_f32 v100, v68, v21, v20\n\tv_fma_f32 v101, v69, v21, v20\n\tv_fma_f32 v102, v70, v21, v20\n\tv_fma_f32 v103, v71, v21, v20\n\t"
            "v_fma_f32 v104, v72, v21, v20\n\tv_fma_f32 v105, v73, v21, v20\n\tv_fma_f32 v106, v74, v21, v20\n\tv_fma_f32 v107, v75, v21, v20\n\tv_fma_f32 v108, v76, v21, v20\n\tv_fma_f32 v109, v77, v21, v20\n\t"
            "v_fma_f32 v110, v78, v21, v20\n\tv_fma_f32 v111, v79, v21, v20\n\tv_fma_f32 v112, v80, v21, v20\n\tv_fma_f32 v113, v81, v21, v20\n\tv_fma_f32 v114, v82, v21, v20\n\tv_fma_f32 v115, v83, v21, v20\n\t"
            "v_fma_f32 v116, v84, v21, v20\n\tv_fma_f32 v117, v85, v21, v20\n\tv_fma_f32 v118, v86, v21, v20\n\tv_fma_f32 v119, v87, v21, v20\n\tv_fma_f32 v120, v88, v21, v20\n\tv_fma_f32 v121, v89, v21, v20\n\t"
            "v_fma_f32 v122, v90, v21, v20\n\tv_fma_f32 v123, v91, v21, v20\n\tv_fma_f32 v124, v92, v21, v20\n\tv_fma_f32 v125, v93, v21, v20\n\tv_fma_f32 v126, v94, v21, v20\n\tv_fma_f32 v127, v95, v21, v20\n\t"
            "s_setpc_b64 s[76:77]\n\t.LG1:\nv_exp_f32 v32, v96\n\tv_exp_f32 v33, v97\n\tv_exp_f32 v34, v98\n\tv_exp_f32 v35, v99\n\t"
            "v_exp_f32 v36, v100\n\tv_exp_f32 v37, v101\n\tv_exp_f32 v38, v102\n\tv_exp_f32 v39, v103\n\tv_exp_f32 v40, v104\n\tv_exp_f32 v41, v105\n\t"
            "v_exp_f32 v42, v106\n\tv_exp_f32 v43, v107\n\tv_exp_f32 v44, v108\n\tv_exp_f32 v45, v109\n\tv_exp_f32 v46, v110\n\tv_exp_f32 v47, v111\n\t"
            "v_exp_f32 v48, v112\n\tv_exp_f32 v49, v113\n\tv_exp_f32 v50, v114\n\tv_exp_f32 v51, v115\n\tv_exp_f32 v52, v116\n\tv_exp_f32 v53, v117\n\t"
            "v_exp_f32 v54, v118\n\tv_exp_f32 v55, v119\n\tv_exp_f32 v56, v120\n\tv_exp_f32 v57, v121\n\tv_exp_f32 v58, v122\n\tv_exp_f32 v59, v123\n\t"
            "v_exp_f32 v60, v124\n\tv_exp_f32 v61, v125\n\tv_exp_f32 v62, v126\n\tv_exp_f32 v63, v127\n\tv_add_f32 v32, 1.0, v32\n\tv_add_f32 v33, 1.0, v33\n\t"
            "v_add_f32 v34, 1.0, v34\n\tv_add_f32 v35, 1.0, v35\n\tv_add_f32 v36, 1.0, v36\n\tv_add_f32 v37, 1.0, v37\n\tv_add_f32 v38, 1.0, v38\n\tv_add_f32 v39, 1.0, v39\n\t"
            "v_add_f32 v40, 1.0, v40\n\tv_add_f32 v41, 1.0, v41\n\tv_add_f32 v42, 1.0, v42\n\tv_add_f32 v43, 1.0, v43\n\tv_add_f32 v44, 1.0, v44\n\tv_add_f32 v45, 1.0, v45\n\t"
            "v_add_f32 v46, 1.0, v46\n\tv_add_f32 v47, 1.0, v47\n\tv_add_f32 v48, 1.0, v48\n\tv_add_f32 v49, 1.0, v49\n\tv_add_f32 v50, 1.0, v50\n\tv_add_f32 v51, 1.0, v51\n\t"
            "v_add_f32 v52, 1.0, v52\n\tv_add_f32 v53, 1.0, v53\n\tv_add_f32 v54, 1.0, v54\n\tv_add_f32 v55, 1.0, v55\n\tv_add_f32 v56, 1.0, v56\n\tv_add_f32 v57, 1.0, v57\n\t"
            "v_add_f32 v58, 1.0, v58\n\tv_add_f32 v59, 1.0, v59\n\tv_add_f32 v60, 1.0, v60\n\tv_add_f32 v61, 1.0, v61\n\tv_add_f32 v62, 1.0, v62\n\tv_add_f32 v63, 1.0, v63\n\t"
            "v_rcp_f32 v32, v32\n\tv_rcp_f32 v33, v33\n\tv_rcp_f32 v34, v34\n\tv_rcp_f32 v35, v35\n\ts_mov_b32 s19, 1\n\ts_setpc_b64 s[76:77]\n\t"
            ".LG2:\nv_rcp_f32 v36, v36\n\tv_rcp_f32 v37, v37\n\tv_rcp_f32 v38, v38\n\tv_rcp_f32 v39, v39\n\tv_rcp_f32 v40, v40\n\t"
            "v_rcp_f32 v41, v41\n\tv_rcp_f32 v42, v42\n\tv_rcp_f32 v43, v43\n\tv_rcp_f32 v44, v44\n\tv_rcp_f32 v45, v45\n\tv_rcp_f32 v46, v46\n\t"
            "v_rcp_f32 v47, v47\n\tv_rcp_f32 v48, v48\n\tv_rcp_f32 v49, v49\n\tv_rcp_f32 v50, v50\n\tv_rcp_f32 v51, v51\n\tv_rcp_f32 v52, v52\n\t"
            "v_rcp_f32 v53, v53\n\tv_rcp_f32 v54, v54\n\tv_rcp_f32 v55, v55\n\tv_rcp_f32 v56, v56\n\tv_rcp_f32 v57, v57\n\tv_rcp_f32 v58, v58\n\t"
            "v_rcp_f32 v59, v59\n\tv_rcp_f32 v60, v60\n\tv_rcp_f32 v61, v61\n\tv_rcp_f32 v62, v62\n\tv_rcp_f32 v63, v63\n\tv_mul_f32 v32, v32, v96\n\t"
            "v_mul_f32 v33, v33, v97\n\tv_mul_f32 v34, v34, v98\n\tv_mul_f32 v35, v35, v99\n\tv_mul_f32 v36, v36, v100\n\tv_mul_f32 v37, v37, v101\n\tv_mul_f32 v38, v38, v102\n\t"
            "v_mul_f32 v39, v39, v103\n\tv_mul_f32 v40, v40, v104\n\tv_mul_f32 v41, v41, v105\n\tv_mul_f32 v42, v42, v106\n\tv_mul_f32 v43, v43, v107\n\tv_mul_f32 v44, v44, v108\n\t"
            "v_mul_f32 v45, v45, v109\n\tv_mul_f32 v46, v46, v110\n\tv_mul_f32 v47, v47, v111\n\ts_mov_b32 s19, 2\n\ts_setpc_b64 s[76:77]\n\t7:\n"
            "ds_read_b32 v16, v8 offset:64\n\tds_read_b32 v17, v9 offset:1344\n\ts_waitcnt lgkmcnt(0)\n\tv_cmp_ne_u32 s[74:75], 0, v17\n\tv_cmp_eq_u32 vcc, 0, v16\n\ts_cbranch_vccz 71f\n\t"
            "s_sleep 1\n\ts_sub_u32 s68, s68, 1\n\ts_cbranch_scc0 7b\n\ts_branch 9b\n\t71:\nv_lshrrev_b32 v12, 5, v2\n\t"
            "v_and_b32 v12, 16, v12\n\tv_add_u32 v12, v12, v17\n\ts_mov_b64 exec, s[74:75]\n\ts_cbranch_execz 72f\n\tds_read_b128 v[32:35], v12 offset:0\n\tds_read_b128 v[36:39], v12 offset:32\n\t"
            "ds_read_b128 v[40:43], v12 offset:64\n\tds_read_b128 v[44:47], v12 offset:96\n\tds_read_b128 v[48:51], v12 offset:128\n\tds_read_b128 v[52:55], v12 offset:160\n\tds_read_b128 v[56:59], v12 offset:192\n\tds_read_b128 v[60:63], v12 offset:224\n\t"
            "72:\ns_mov_b64 exec, -1\n\ts_waitcnt lgkmcnt(0)\n\ts_branch 8b\n\t5:\ns_sleep 1\n\t"
            "ds_read_b64 v[14:15], v8 offset:40\n\ts_waitcnt lgkmcnt(0)\n\tv_min_u32 v14, v14, v15\n\tv_cmp_ge_u32 vcc, s71, v14\n\ts_cbranch_vccz 61f\n\ts_sub_u32 s68, s68, 1\n\t"
            "s_cbranch_scc0 5b\n\ts_branch 9b\n\t61:\n"
            TRG2D
            "s_branch 6b\n\t62:\nds_read_b128 v[16:19], v8 offset:48\n\tds_read_b64_tr_b16 v[128:129], v4 offset:40960\n\tds_read_b64_tr_b16 v[130:131], v4 offset:41472\n\tds_read_b64_tr_b16 v[132:133], v5 offset:40960\n\t"
            "ds_read_b64_tr_b16 v[134:135], v5 offset:41472\n\tds_read_b64_tr_b16 v[136:137], v6 offset:40960\n\tds_read_b64_tr_b16 v[138:139], v6 offset:41472\n\tds_read_b64_tr_b16 v[140:141], v7 offset:40960\n\tds_read_b64_tr_b16 v[142:143], v7 offset:41472\n\tds_read_b64_tr_b16 v[144:145], v4 offset:41984\n\t"
            "ds_read_b64_tr_b16 v[146:147], v4 offset:42496\n\tds_read_b64_tr_b16 v[148:149], v5 offset:41984\n\tds_read_b64_tr_b16 v[150:151], v5 offset:42496\n\tds_read_b64_tr_b16 v[152:153], v6 offset:41984\n\tds_read_b64_tr_b16 v[154:155], v6 offset:42496\n\tds_read_b64_tr_b16 v[156:157], v7 offset:41984\n\t"
            "ds_read_b64_tr_b16 v[158:159], v7 offset:42496\n\ts_waitcnt lgkmcnt(15)\n\tv_min3_u32 v16, v16, v17, v18\n\tv_min_u32 v16, v16, v19\n\tv_cmp_eq_u32 vcc, 0, v16\n\ts_cbranch_vccz 63b\n\t"
            "s_sub_u32 s68, s68, 1\n\ts_cbranch_scc0 62b\n\ts_branch 9b\n\t100:\ns_mov_b32 s70, 4\n\ts_call_b64 s[72:73], 300f\n\t"
            "v_max3_u32 v12, v176, v178, v180\n\tv_max_u32 v12, v12, v182\n\tv_cmp_eq_u32 vcc, -1, v12\n\ts_cbranch_vccnz 100b\n\ts_branch 200b\n\t101:\n"
            "s_mov_b32 s70, 5\n\ts_call_b64 s[72:73], 301f\n\tv_max3_u32 v12, v184, v186, v188\n\tv_max_u32 v12, v12, v190\n\tv_cmp_eq_u32 vcc, -1, v12\n\ts_cbranch_vccnz 101b\n\t"
            "s_branch 201b\n\t102:\ns_mov_b32 s70, 6\n\ts_call_b64 s[72:73], 302f\n\tv_max3_u32 v12, v192, v194, v196\n\tv_max_u32 v12, v12, v198\n\t"
            "v_cmp_eq_u32 vcc, -1, v12\n\ts_cbranch_vccnz 102b\n\ts_branch 202b\n\t103:\ns_mov_b32 s70, 7\n\ts_call_b64 s[72:73], 303f\n\t"
            "v_max3_u32 v12, v200, v202, v204\n\tv_max_u32 v12, v12, v206\n\tv_cmp_eq_u32 vcc, -1, v12\n\ts_cbranch_vccnz 103b\n\ts_branch 203b\n\t104:\n"
            "s_mov_b32 s70, 8\n\ts_call_b64 s[72:73], 304f\n\tv_max3_u32 v12, v208, v210, v212\n\tv_max_u32 v12, v12, v214\n\tv_cmp_eq_u32 vcc, -1, v12\n\ts_cbranch_vccnz 104b\n\t"
            "s_branch 204b\n\t105:\ns_mov_b32 s70, 9\n\ts_call_b64 s[72:73], 305f\n\tv_max3_u32 v12, v216, v218, v220\n\tv_max_u32 v12, v12, v222\n\t"
            "v_cmp_eq_u32 vcc, -1, v12\n\ts_cbranch_vccnz 105b\n\ts_branch 205b\n\t106:\ns_mov_b32 s70, 10\n\ts_call_b64 s[72:73], 306f\n\t"
            "v_max3_u32 v12, v224, v226, v228\n\tv_max_u32 v12, v12, v230\n\tv_cmp_eq_u32 vcc, -1, v12\n\ts_cbranch_vccnz 106b\n\ts_branch 206b\n\t107:\n"
            "s_mov_b32 s70, 11\n\ts_call_b64 s[72:73], 307f\n\tv_max3_u32 v12, v232, v234, v236\n\tv_max_u32 v12, v12, v238\n\tv_cmp_eq_u32 vcc, -1, v12\n\ts_cbranch_vccnz 107b\n\t"
            "s_branch 207b\n\t108:\ns_mov_b32 s70, 12\n\ts_call_b64 s[72:73], 308f\n\tv_max3_u32 v12, v176, v178, v180\n\tv_max_u32 v12, v12, v182\n\t"
            "v_cmp_eq_u32 vcc, -1, v12\n\ts_cbranch_vccnz 108b\n\ts_branch 208b\n\t109:\ns_mov_b32 s70, 13\n\ts_call_b64 s[72:73], 309f\n\t"
            "v_max3_u32 v12, v184, v186, v188\n\tv_max_u32 v12, v12, v190\n\tv_cmp_eq_u32 vcc, -1, v12\n\ts_cbranch_vccnz 109b\n\ts_branch 209b\n\t110:\n"
            "s_mov_b32 s70, 14\n\ts_call_b64 s[72:73], 310f\n\tv_max3_u32 v12, v192, v194, v196\n\tv_max_u32 v12, v12, v198\n\tv_cmp_eq_u32 vcc, -1, v12\n\ts_cbranch_vccnz 110b\n\t"
            "s_branch 210b\n\t111:\ns_mov_b32 s70, 15\n\ts_call_b64 s[72:73], 311f\n\tv_max3_u32 v12, v200, v202, v204\n\tv_max_u32 v12, v12, v206\n\t"
            "v_cmp_eq_u32 vcc, -1, v12\n\ts_cbranch_vccnz 111b\n\ts_branch 211b\n\t112:\ns_mov_b32 s70, 16\n\ts_call_b64 s[72:73], 312f\n\t"
            "v_max3_u32 v12, v208, v210, v212\n\tv_max_u32 v12, v12, v214\n\tv_cmp_eq_u32 vcc, -1, v12\n\ts_cbranch_vccnz 112b\n\ts_branch 212b\n\t113:\n"
            "s_mov_b32 s70, 17\n\ts_call_b64 s[72:73], 313f\n\tv_max3_u32 v12, v216, v218, v220\n\tv_max_u32 v12, v12, v222\n\tv_cmp_eq_u32 vcc, -1, v12\n\ts_cbranch_vccnz 113b\n\t"
            "s_branch 213b\n\t114:\ns_mov_b32 s70, 18\n\ts_call_b64 s[72:73], 314f\n\tv_max3_u32 v12, v224, v226, v228\n\tv_max_u32 v12, v12, v230\n\t"
            "v_cmp_eq_u32 vcc, -1, v12\n\ts_cbranch_vccnz 114b\n\ts_branch 214b\n\t115:\ns_mov_b32 s70, 19\n\ts_call_b64 s[72:73], 315f\n\t"
            "v_max3_u32 v12, v232, v234, v236\n\tv_max_u32 v12, v12, v238\n\tv_cmp_eq_u32 vcc, -1, v12\n\ts_cbranch_vccnz 115b\n\ts_branch 215b\n\t116:\n"
            "s_mov_b32 s70, 19\n\ts_call_b64 s[72:73], 316f\n\tv_max3_u32 v12, v176, v178, v180\n\tv_max_u32 v12, v12, v182\n\tv_cmp_eq_u32 vcc, -1, v12\n\ts_cbranch_vccnz 116b\n\t"
            "s_branch 216b\n\t117:\ns_mov_b32 s70, 19\n\ts_call_b64 s[72:73], 317f\n\tv_max3_u32 v12, v184, v186, v188\n\tv_max_u32 v12, v12, v190\n\t"
            "v_cmp_eq_u32 vcc, -1, v12\n\ts_cbranch_vccnz 117b\n\ts_branch 217b\n\t118:\ns_mov_b32 s70, 19\n\ts_call_b64 s[72:73], 318f\n\t"
            "v_max3_u32 v12, v192, v194, v196\n\tv_max_u32 v12, v12, v198\n\tv_cmp_eq_u32 vcc, -1, v12\n\ts_cbranch_vccnz 118b\n\ts_branch 218b\n\t119:\n"
            "s_mov_b32 s70, 19\n\ts_call_b64 s[72:73], 319f\n\tv_max3_u32 v12, v200, v202, v204\n\tv_max_u32 v12, v12, v206\n\tv_cmp_eq_u32 vcc, -1, v12\n\ts_cbranch_vccnz 119b\n\t"
            "s_branch 219b\n\t300:\ns_waitcnt vmcnt(0)\n\t400:\nv_max3_u32 v12, v176, v178, v180\n\tv_max_u32 v12, v12, v182\n\t"
            "v_cmp_eq_u32 vcc, -1, v12\n\ts_cbranch_vccz 11f\n\tbuffer_load_dwordx4 v[176:179], v2, s[44:47], s4 offen offset:1024 sc1\n\tbuffer_load_dwordx4 v[180:183], v2, s[44:47], s4 offen offset:2048 sc1\n\t11:\ns_cmp_eq_u32 s70, 0\n\t"
            "s_cbranch_scc0 401f\n\ts_branch 12f\n\t301:\ns_waitcnt vmcnt(0)\n\t401:\nv_max3_u32 v12, v184, v186, v188\n\t"
            "v_max_u32 v12, v12, v190\n\tv_cmp_eq_u32 vcc, -1, v12\n\ts_cbranch_vccz 11f\n\tbuffer_load_dwordx4 v[184:187], v2, s[44:47], s5 offen offset:1024 sc1\n\tbuffer_load_dwordx4 v[188:191], v2, s[44:47], s5 offen offset:2048 sc1\n\t11:\n"
            "s_cmp_eq_u32 s70, 1\n\ts_cbranch_scc0 402f\n\ts_branch 12f\n\t302:\ns_waitcnt vmcnt(0)\n\t402:\n"
            "v_max3_u32 v12, v192, v194, v196\n\tv_max_u32 v12, v12, v198\n\tv_cmp_eq_u32 vcc, -1, v12\n\ts_cbranch_vccz 11f\n\tbuffer_load_dwordx4 v[192:195], v2, s[44:47], s6 offen offset:1024 sc1\n\tbuffer_load_dwordx4 v[196:199], v2, s[44:47], s6 offen offset:2048 sc1\n\t"
            "11:\ns_cmp_eq_u32 s70, 2\n\ts_cbranch_scc0 403f\n\ts_branch 12f\n\t303:\ns_waitcnt vmcnt(0)\n\t"
            "403:\nv_max3_u32 v12, v200, v202, v204\n\tv_max_u32 v12, v12, v206\n\tv_cmp_eq_u32 vcc, -1, v12\n\ts_cbranch_vccz 11f\n\tbuffer_load_dwordx4 v[200:203], v2, s[44:47], s7 offen offset:1024 sc1\n\t"
            "buffer_load_dwordx4 v[204:207], v2, s[44:47], s7 offen offset:2048 sc1\n\t11:\ns_cmp_eq_u32 s70, 3\n\ts_cbranch_scc0 404f\n\ts_branch 12f\n\t304:\n"
            "s_waitcnt vmcnt(0)\n\t404:\nv_max3_u32 v12, v208, v210, v212\n\tv_max_u32 v12, v12, v214\n\tv_cmp_eq_u32 vcc, -1, v12\n\ts_cbranch_vccz 11f\n\t"
            "buffer_load_dwordx4 v[208:211], v2, s[44:47], s8 offen offset:1024 sc1\n\tbuffer_load_dwordx4 v[212:215], v2, s[44:47], s8 offen offset:2048 sc1\n\t11:\ns_cmp_eq_u32 s70, 4\n\ts_cbranch_scc0 405f\n\ts_branch 12f\n\t"
            "305:\ns_waitcnt vmcnt(0)\n\t405:\nv_max3_u32 v12, v216, v218, v220\n\tv_max_u32 v12, v12, v222\n\tv_cmp_eq_u32 vcc, -1, v12\n\t"
            "s_cbranch_vccz 11f\n\tbuffer_load_dwordx4 v[216:219], v2, s[44:47], s9 offen offset:1024 sc1\n\tbuffer_load_dwordx4 v[220:223], v2, s[44:47], s9 offen offset:2048 sc1\n\t11:\ns_cmp_eq_u32 s70, 5\n\ts_cbranch_scc0 406f\n\t"
            "s_branch 12f\n\t306:\ns_waitcnt vmcnt(0)\n\t406:\nv_max3_u32 v12, v224, v226, v228\n\tv_max_u32 v12, v12, v230\n\t"
            "v_cmp_eq_u32 vcc, -1, v12\n\ts_cbranch_vccz 11f\n\tbuffer_load_dwordx4 v[224:227], v2, s[44:47], s10 offen offset:1024 sc1\n\tbuffer_load_dwordx4 v[228:231], v2, s[44:47], s10 offen offset:2048 sc1\n\t11:\ns_cmp_eq_u32 s70, 6\n\t"
            "s_cbranch_scc0 407f\n\ts_branch 12f\n\t307:\ns_waitcnt vmcnt(0)\n\t407:\nv_max3_u32 v12, v232, v234, v236\n\t"
            "v_max_u32 v12, v12, v238\n\tv_cmp_eq_u32 vcc, -1, v12\n\ts_cbranch_vccz 11f\n\tbuffer_load_dwordx4 v[232:235], v2, s[44:47], s11 offen offset:1024 sc1\n\tbuffer_load_dwordx4 v[236:239], v2, s[44:47], s11 offen offset:2048 sc1\n\t11:\n"
            "s_cmp_eq_u32 s70, 7\n\ts_cbranch_scc0 408f\n\ts_branch 12f\n\t308:\ns_waitcnt vmcnt(0)\n\t408:\n"
            "v_max3_u32 v12, v176, v178, v180\n\tv_max_u32 v12, v12, v182\n\tv_cmp_eq_u32 vcc, -1, v12\n\ts_cbranch_vccz 11f\n\tbuffer_load_dwordx4 v[176:179], v2, s[44:47], s12 offen offset:1024 sc1\n\tbuffer_load_dwordx4 v[180:183], v2, s[44:47], s12 offen offset:2048 sc1\n\t"
            "11:\ns_cmp_eq_u32 s70, 8\n\ts_cbranch_scc0 409f\n\ts_branch 12f\n\t309:\ns_waitcnt vmcnt(0)\n\t"
            "409:\nv_max3_u32 v12, v184, v186, v188\n\tv_max_u32 v12, v12, v190\n\tv_cmp_eq_u32 vcc, -1, v12\n\ts_cbranch_vccz 11f\n\tbuffer_load_dwordx4 v[184:187], v2, s[44:47], s13 offen offset:1024 sc1\n\t"
            "buffer_load_dwordx4 v[188:191], v2, s[44:47], s13 offen offset:2048 sc1\n\t11:\ns_cmp_eq_u32 s70, 9\n\ts_cbranch_scc0 410f\n\ts_branch 12f\n\t310:\n"
            "s_waitcnt vmcnt(0)\n\t410:\nv_max3_u32 v12, v192, v194, v196\n\tv_max_u32 v12, v12, v198\n\tv_cmp_eq_u32 vcc, -1, v12\n\ts_cbranch_vccz 11f\n\t"
            "buffer_load_dwordx4 v[192:195], v2, s[44:47], s4 offen offset:0 sc1\n\tbuffer_load_dwordx4 v[196:199], v2, s[44:47], s4 offen offset:3072 sc1\n\t11:\ns_cmp_eq_u32 s70, 10\n\ts_cbranch_scc0 411f\n\ts_branch 12f\n\t"
            "311:\ns_waitcnt vmcnt(0)\n\t411:\nv_max3_u32 v12, v200, v202, v204\n\tv_max_u32 v12, v12, v206\n\tv_cmp_eq_u32 vcc, -1, v12\n\t"
            "s_cbranch_vccz 11f\n\tbuffer_load_dwordx4 v[200:203], v2, s[44:47], s5 offen offset:0 sc1\n\tbuffer_load_dwordx4 v[204:207], v2, s[44:47], s5 offen offset:3072 sc1\n\t11:\ns_cmp_eq_u32 s70, 11\n\ts_cbranch_scc0 412f\n\t"
            "s_branch 12f\n\t312:\ns_waitcnt vmcnt(0)\n\t412:\nv_max3_u32 v12, v208, v210, v212\n\tv_max_u32 v12, v12, v214\n\t"
            "v_cmp_eq_u32 vcc, -1, v12\n\ts_cbranch_vccz 11f\n\tbuffer_load_dwordx4 v[208:211], v2, s[44:47], s6 offen offset:0 sc1\n\tbuffer_load_dwordx4 v[212:215], v2, s[44:47], s6 offen offset:3072 sc1\n\t11:\ns_cmp_eq_u32 s70, 12\n\t"
            "s_cbranch_scc0 413f\n\ts_branch 12f\n\t313:\ns_waitcnt vmcnt(0)\n\t413:\nv_max3_u32 v12, v216, v218, v220\n\t"
            "v_max_u32 v12, v12, v222\n\tv_cmp_eq_u32 vcc, -1, v12\n\ts_cbranch_vccz 11f\n\tbuffer_load_dwordx4 v[216:219], v2, s[44:47], s7 offen offset:0 sc1\n\tbuffer_load_dwordx4 v[220:223], v2, s[44:47], s7 offen offset:3072 sc1\n\t11:\n"
            "s_cmp_eq_u32 s70, 13\n\ts_cbranch_scc0 414f\n\ts_branch 12f\n\t314:\ns_waitcnt vmcnt(0)\n\t414:\n"
            "v_max3_u32 v12, v224, v226, v228\n\tv_max_u32 v12, v12, v230\n\tv_cmp_eq_u32 vcc, -1, v12\n\ts_cbranch_vccz 11f\n\tbuffer_load_dwordx4 v[224:227], v2, s[44:47], s8 offen offset:0 sc1\n\tbuffer_load_dwordx4 v[228:231], v2, s[44:47], s8 offen offset:3072 sc1\n\t"
            "11:\ns_cmp_eq_u32 s70, 14\n\ts_cbranch_scc0 415f\n\ts_branch 12f\n\t315:\ns_waitcnt vmcnt(0)\n\t"
            "415:\nv_max3_u32 v12, v232, v234, v236\n\tv_max_u32 v12, v12, v238\n\tv_cmp_eq_u32 vcc, -1, v12\n\ts_cbranch_vccz 11f\n\tbuffer_load_dwordx4 v[232:235], v2, s[44:47], s9 offen offset:0 sc1\n\t"
            "buffer_load_dwordx4 v[236:239], v2, s[44:47], s9 offen offset:3072 sc1\n\t11:\ns_cmp_eq_u32 s70, 15\n\ts_cbranch_scc0 416f\n\ts_branch 12f\n\t316:\n"
            "s_waitcnt vmcnt(0)\n\t416:\nv_max3_u32 v12, v176, v178, v180\n\tv_max_u32 v12, v12, v182\n\tv_cmp_eq_u32 vcc, -1, v12\n\ts_cbranch_vccz 11f\n\t"
            "buffer_load_dwordx4 v[176:179], v2, s[44:47], s10 offen offset:0 sc1\n\tbuffer_load_dwordx4 v[180:183], v2, s[44:47], s10 offen offset:3072 sc1\n\t11:\ns_cmp_eq_u32 s70, 16\n\ts_cbranch_scc0 417f\n\ts_branch 12f\n\t"
            "317:\ns_waitcnt vmcnt(0)\n\t417:\nv_max3_u32 v12, v184, v186, v188\n\tv_max_u32 v12, v12, v190\n\tv_cmp_eq_u32 vcc, -1, v12\n\t"
            "s_cbranch_vccz 11f\n\tbuffer_load_dwordx4 v[184:187], v2, s[44:47], s11 offen offset:0 sc1\n\tbuffer_load_dwordx4 v[188:191], v2, s[44:47], s11 offen offset:3072 sc1\n\t11:\ns_cmp_eq_u32 s70, 17\n\ts_cbranch_scc0 418f\n\t"
            "s_branch 12f\n\t318:\ns_waitcnt vmcnt(0)\n\t418:\nv_max3_u32 v12, v192, v194, v196\n\tv_max_u32 v12, v12, v198\n\t"
            "v_cmp_eq_u32 vcc, -1, v12\n\ts_cbranch_vccz 11f\n\tbuffer_load_dwordx4 v[192:195], v2, s[44:47], s12 offen offset:0 sc1\n\tbuffer_load_dwordx4 v[196:199], v2, s[44:47], s12 offen offset:3072 sc1\n\t11:\ns_cmp_eq_u32 s70, 18\n\t"
            "s_cbranch_scc0 419f\n\ts_branch 12f\n\t319:\ns_waitcnt vmcnt(0)\n\t419:\nv_max3_u32 v12, v200, v202, v204\n\t"
            "v_max_u32 v12, v12, v206\n\tv_cmp_eq_u32 vcc, -1, v12\n\ts_cbranch_vccz 11f\n\tbuffer_load_dwordx4 v[200:203], v2, s[44:47], s13 offen offset:0 sc1\n\tbuffer_load_dwordx4 v[204:207], v2, s[44:47], s13 offen offset:3072 sc1\n\t11:\n"
            "12:\n"
            RINC
            "s_sub_u32 s68, s68, 1\n\ts_cbranch_scc1 9b\n\ts_waitcnt vmcnt(0)\n\ts_setpc_b64 s[72:73]\n\t.subsection 1\n\t.Lce:\n"
            ".subsection 0\n\t.Lout:\ns_waitcnt lgkmcnt(0)\n\t"
            : "=&{s67}"(tag), "=&{v[52:83]}"(sa), "=&{v[84:115]}"(sb), "=&{v[116:123]}"(sc) : [t] "v"(tid), [b] "s"(bid), [k] "s"(ka)
            : "memory", "scc", "vcc", "s4", "s5", "s6", "s7", "s8", "s9", "s10", "s11", "s12", "s13", "s14", "s15", "s16", "s17", "s18", "s19", "s20", "s21", "s22", "s23", "s24", "s25", "s26", "s27", "s28", "s29", "s30", "s31", "s32", "s33", "s34", "s35", "s36", "s37", "s38", "s39", "s40", "s41", "s42", "s43", "s44", "s45", "s46", "s47", "s48", "s49", "s50", "s51", "s52", "s53", "s54", "s55", "s56", "s57", "s58", "s59", "s60", "s61", "s62", "s63", "s64", "s65", "s66", "s68", "s69", "s70", "s71", "s72", "s73", "s74", "s75", "s76", "s77", "s78", "s79", "s80", "s81", "s82", "s83", "s84", "s85", "s86", "s87", "s88", "s89", "s90", "s91", "s92", "s93", "s94", "s95", "s96", "s97", "s98", "s99", "s100", "s101", "v2", "v3", "v4", "v5", "v6", "v7", "v8", "v9", "v10", "v11", "v12", "v13", "v14", "v15", "v16", "v17", "v18", "v19", "v20", "v21", "v22", "v23", "v24", "v25", "v26", "v27", "v28", "v29", "v30", "v31", "v32", "v33", "v34", "v35", "v36", "v37", "v38", "v39", "v40", "v41", "v42", "v43", "v44", "v45", "v46", "v47", "v48", "v49", "v50", "v51", "v124", "v125", "v126", "v127", "v128", "v129", "v130", "v131", "v132", "v133", "v134", "v135", "v136", "v137", "v138", "v139", "v140", "v141", "v142", "v143", "v144", "v145", "v146", "v147", "v148", "v149", "v150", "v151", "v152", "v153", "v154", "v155", "v156", "v157", "v158", "v159", "v160", "v161", "v162", "v163", "v164", "v165", "v166", "v167", "v168", "v169", "v170", "v171", "v172", "v173", "v174", "v175", "v176", "v177", "v178", "v179", "v180", "v181", "v182", "v183", "v184", "v185", "v186", "v187", "v188", "v189", "v190", "v191", "v192", "v193", "v194", "v195", "v196", "v197", "v198", "v199", "v200", "v201", "v202", "v203", "v204", "v205", "v206", "v207", "v208", "v209", "v210", "v211", "v212", "v213", "v214", "v215", "v216", "v217", "v218", "v219", "v220", "v221", "v222", "v223", "v224", "v225", "v226", "v227", "v228", "v229", "v230", "v231", "v232", "v233", "v234", "v235", "v236", "v237", "v238", "v239", "v240", "v241", "v242", "v243", "v244", "v245", "v246", "v247", "v248", "v249", "v250", "v251", "v252", "v253", "v254", "v255");
    tag = RFL(tag);
    asm volatile("" : "+s"(bid), "+s"(ka)); asm volatile("" : "+v"(tid));
    const u16 *x = (const u16*)ka[0], *wo = (const u16*)ka[2], *w_r = (const u16*)ka[3], *wg = (const u16*)ka[4], *wu = (const u16*)ka[5], *wd = (const u16*)ka[6]; char* ws = (char*)ka[7];
    const int wv = __builtin_amdgcn_readfirstlane(tid >> 6);
    const unsigned lds0 = (unsigned)(size_t)(lchar*)smem;
#define LANE() const int lane = lane_id(); const int g = lane >> 4, li = lane & 15, jq = 4 * (li & 3) + (li >> 2); (void)jq
    const int xcd = bid & 7, j = ((bid >> 3) + 25) & 31;
    const bool gemm = j < 25;
    const int fp = xcd >> 1, bl = (j * 13) >> 6, cl = j - bl * 5;
    const int rb = fp * 5 + bl, cg = (xcd & 1) * 5 + cl;
    const int par = fp >> 1, I0 = ((fp & 1) * 5 + bl) * 104 - 8;
    const __amdgpu_buffer_rsrc_t rs = __builtin_amdgcn_make_buffer_rsrc((void*)ws, 0, W_END, 0x00020000);
    auto wait_flag = [&](unsigned a) { SPIN { if (RFL(ldsrv<unsigned>(a))) break; __builtin_amdgcn_s_sleep(1); } MEMB(); };
    auto all4 = [&](u32x4 c) { return RFL(min(min(c.x, c.y), min(c.z, c.w))); };
    unsigned ep = 0;
    auto epld = [&]() { asm volatile("s_load_dword %0, %1, 0x0" : "=s"(ep) : "s"(ws) : "memory"); };
    auto mktag = [&]() { asm volatile("s_waitcnt lgkmcnt(0)" : "+s"(ep) :: "memory"); tag = ep * 1664525u + 1013904223u; };
        if (gemm) {
            LANE();
            wait_flag(lds0 + FL_W);
            const u32x4 kk = ldsrv<u32x4>(lds0 + L_KEY + lane * 16);
            const uint2 t2 = top2wave(lane < 52 ? kk.x : 0u, lane < 52 ? kk.z : 0u);
            _Pragma("nounroll") for (int k = 0; k < 2; ++k) {
                const unsigned key = k ? t2.y : t2.x, rr = (0xffffu - (key & 0xffffu)) & 2047u; const int pos = (int)(rr >> 1) - I0;
                if (__builtin_expect((int)(rr & 1) == par && pos >= 8 && pos < 112, 0)) {
                    const float sg = rbf(__builtin_amdgcn_rcpf(1.f + __builtin_amdgcn_exp2f(keyscore(key) * -1.4426950408889634f)));
                    const int m = (pos & ~15) + 4 * (pos & 3) + ((pos >> 2) & 3);
                    const float xr_ = lo2f(x[rr * 640 + cg * 64 + lane]), nm = ldsrv<float>(lds0 + L_NM + m * 4), rsj = ldsrv<float>(lds0 + L_RS + pos * 4);
                    u32x2 y[5];
                    SPIN {
                        MEMB();
                        bool old = false;
#pragma unroll
                        for (int i = 0; i < 5; ++i) y[i] = __builtin_amdgcn_raw_buffer_load_b64(rs, W_Y + (cg * 64 + lane) * 16 + k * 8, i * 10240, SC1);
#pragma unroll
                        for (int i = 0; i < 5; ++i) old = old || y[i].y != tag;
                        if (__builtin_amdgcn_ballot_w64(old) == 0) break;
                        __builtin_amdgcn_s_sleep(YS2);
                    }
                    const float ys = ((__uint_as_float(y[0].x) + __uint_as_float(y[1].x)) + (__uint_as_float(y[2].x) + __uint_as_float(y[3].x))) + __uint_as_float(y[4].x);
                    const float xn = rbf(rbf(xr_ + nm) * rsj), u = rbf(rbf(ys) * sg);
                    ldsw<float>(lds0 + L_RG + k * 256 + lane * 4, silu_b(rbf(xn + u)) * -1.4426950408889634f);
                    ldsw<unsigned>(lds0 + L_RM + m * 4, L_RG + k * 256);
                }
            }
            ldsw<unsigned>(lds0 + FL_R, 1u);
            END();
        }
    {
        TL(0);
        LANE();
        const int fq = (j - 25) * 2 + (xcd & 1), f = fp * 14 + fq;
        const int n0 = fq * 40 + wv * 4 + g;
        epld();
        if (wv == 7 && lane < 16) ldsw<u32x4>(lds0 + MISC + lane * 16, (u32x4){0u, 0u, 0u, 0u});
        const int u2 = max(f - 6, 0), kr = (u2 * 26) >> 8, fc = u2 - kr * 10, n1 = wv < 4 ? 3 : 2;
        const int d0 = kr * 11 + wv, d1 = kr * 11 + 4 + wv; const bool two = wv >= 4 && wv < 7 && d1 < 54;
        const char* w1 = (const char*)(f & 1 ? wu : wg) + min(f >> 1, 26) * 128; const char* w2 = (const char*)wd + fc * 128;
        unsigned fv[2][4];
#pragma unroll
        for (int i = 0; i < 4; ++i) { const unsigned c = (((lane & 7) ^ (2 * ((lane >> 4) & 1) + 4 * (i & 1))) * 16) - i * 1024; fv[0][i] = ((lane >> 3) + i * 8) * (F_ * 2) + c; fv[1][i] = ((lane >> 3) + i * 8) * 1280 + c; }
        auto issue = [&](int m, int ks, int slot) {
            asm volatile("s_mov_b32 m0, %[l]\n\ts_nop 0\n\tglobal_load_lds_dwordx4 %0, %[b]" FPOL "\n\tglobal_load_lds_dwordx4 %1, %[b] offset:1024" FPOL "\n\tglobal_load_lds_dwordx4 %2, %[b] offset:2048" FPOL "\n\tglobal_load_lds_dwordx4 %3, %[b] offset:3072" FPOL
                         :: "v"(fv[m][0]), "v"(fv[m][1]), "v"(fv[m][2]), "v"(fv[m][3]), [b] "s"((m ? w2 : w1) + (unsigned)(ks * 32 * (m ? 1280 : F_ * 2))), [l] "s"(lds0 + wv * 16384 + slot * 4096) : "memory");
        };
        if (TCH && wv == 5) {
            const int l = min(f * 14 + lane, 770), o = l < 280 ? W_KEY + l * 128 : l < 536 ? W_ST + (l - 280) * 128 : W_Y + (l - 536) * 256;
            if (lane < 14) asm volatile("s_mov_b32 m0, %[l]\n\ts_nop 0\n\tbuffer_load_dword %0, %1, 0 offen lds" :: "v"(o), "s"(rs), [l] "s"(lds0 + L_DUM) : "memory");
        }
        BARN();
        auto wopf = [&]() {
            const int i = min(((j - 25) * 8 + wv) * 58 + lane, 3199), rw = (i * 13108) >> 16;
            asm volatile("s_mov_b32 m0, %[l]\n\ts_nop 0\n\tglobal_load_lds_dword %0, %[b]" :: "v"(rw * 640 + i * 128 + (xcd & 1) * 640), [b] "s"(wo), [l] "s"(lds0 + L_DUM) : "memory"); };
        if (WPF == 1) wopf();
        if (EARLYK) issue(0, wv, 0);
        asm volatile("s_waitcnt vmcnt(%2)" : "+{v[84:115]}"(sb), "+{v[116:123]}"(sc) : "n"(5 + 4 * EARLYK + (WPF == 1)) : "memory");
#define SXR(i) ((i) < 20 ? sa[(12 + (i)) & 31] : (i) < 52 ? sb[((i) - 20) & 31] : sc[((i) - 52) & 7])
        mktag();
        {
            const unsigned one2 = 0x3F803F80u;
            float wsum = 0.f;
#pragma unroll
            for (int e = 0; e < 20; ++e) wsum = dot2(SXR(40 + e), one2, wsum);
            wsum = sum16(wsum);
            auto stat = [&](int p) {
                float s1 = 0.f, s2 = 0.f, sr = 0.f;
#pragma unroll
                for (int e = 0; e < 20; ++e) { const unsigned u = SXR(20 * p + e); s1 = dot2(u, one2, s1); s2 = dot2(u, u, s2); sr = dot2(u, SXR(40 + e), sr); }
                const float mean = sum16(s1) * (1.f / 640.f);
                const unsigned mb2 = cvtpk(mean, mean); const float mb = lo2f(mb2);
                const float q = fmaf(40.f * mb, mb, fmaf(-2.f * mb, s1, s2));
                const float dm = mean - mb;
                const float var = fmaxf(sum16(q) * (1.f / 640.f) - dm * dm, 0.f);
                const float sd = rbf(__builtin_amdgcn_sqrtf(rbf(rbf(var) + 1e-5f)));
                const float rsc = rbf(fmaf(-mb, wsum, sum16(sr)) / sd);
                const int n = n0 + 32 * p, i = (fp & 1) * 512 + n;
                if (li == 0) {
                    asm volatile("buffer_store_dwordx4 %0, %1, %2, 0 offen sc1\n\ts_nop 1" :: "v"((u32x4){(mb2 << 16) ^ 0x80000000u, tag, __float_as_uint(__builtin_amdgcn_rcpf(sd)), tag}), "v"(n < 512 ? W_ST + (par * 1024 + i) * 16 : 0x40000000), "s"(rs) : "memory");
                    ldsw<unsigned>(lds0 + LS_ROW + (p * 32 + wv * 4 + g) * 4, n < 512 ? rkey(rsc, 2 * i + par) : 0u);
                }
            };
            asm volatile("" : "+v"(wsum));
            if (wv < 2) stat(1);
            asm volatile("s_cmp_lt_u32 %2, 2\n\ts_cbranch_scc1 1f\n\ts_waitcnt vmcnt(%1)\n1:\n\ts_waitcnt vmcnt(%1 + 1)" : "+{v[52:83]}"(sa) : "n"(4 * EARLYK + (WPF == 1)), "s"(wv) : "memory", "scc");
            TL(1);
            if (EK2 && wv < EK2) issue(0, wv, 0);
            stat(0);
            if (E2 && wv < 7) issue(0, wv + 8, 1);
        }
        BAR();
        if (wv == 6) asm volatile("buffer_store_dword %0, %1, %2, 0 offen sc1" :: "v"(tag), "v"(lane ? 0x40000000 : W_ACK + (f < 40 ? (f >> 1) * 128 + 40 + (f & 1) * 4 : 2560 + (f - 40) * 4)), "s"(rs) : "memory");
        if (wv == 7) {
            const unsigned k = ldsrv<unsigned>(lds0 + LS_ROW + min(lane, 39) * 4);
            const uint2 t2 = top2wave(lane < 40 ? k : 0u, 0u);
            if (lane == 0 && fq < 13) asm volatile("buffer_store_dwordx4 %0, %1, %2, 0 offen sc1\n\ts_nop 1" :: "v"((u32x4){t2.x, tag, t2.y, tag}), "v"(W_KEY + (fp * 13 + fq) * 16), "s"(rs) : "memory");
        }
        BARN();
        TL(2);
        if (WPF == 2) wopf();
        if (!EARLYK && wv >= EK2) issue(0, wv, 0);
        if (!E2 || wv == 7) issue(0, wv + 8, 1);
        if (wv < 4) issue(0, wv + 16, 2);
        else if (!WPF) asm volatile("s_mov_b32 m0, %[l]\n\ts_mov_b32 exec_hi, 0\n\tglobal_load_lds_dword %0, %[b]\n\ts_mov_b32 exec_hi, -1" :: "v"(min(f * 128 + (wv - 4) * 32 + lane, 6399) * 128), [b] "s"(wo), [l] "s"(lds0 + L_DUM) : "memory");
        if (wv >= 4 && wv < 4 + P2A) issue(1, d0, 3);
        u32x4 kk;
        BARN();
        asm volatile("s_cmp_lg_u32 %3, 7\n\ts_cbranch_scc1 1f\n\ts_sleep " KSL "\n\tbuffer_load_dwordx4 %0, %1, %2, 0 offen sc1\n1:\n\ts_barrier" : "=&v"(kk) : "v"(W_KEY + min(lane, 51) * 16), "s"(rs), "s"(wv) : "memory", "scc");
        if (wv != 7 && !(wv >= 4 && wv < 4 + P2A)) issue(1, d0, 3);
        if (P2A && two) issue(1, d1, 2);
        asm volatile("s_cmp_lg_u32 %1, 7\n\ts_cbranch_scc1 1f\n\ts_waitcnt vmcnt(0)\n1:" : "+v"(kk) : "s"(wv) : "memory", "scc");
        if (wv == 7) {
            SPIN {
                if (__builtin_amdgcn_ballot_w64(kk.y != tag || kk.w != tag) == 0) break;
                asm volatile("buffer_load_dwordx4 %0, %1, %2, 0 offen sc1\n\ts_waitcnt vmcnt(0)" : "=&v"(kk) : "v"(W_KEY + min(lane, 51) * 16), "s"(rs) : "memory");
            }
            const uint2 t2 = top2wave(lane < 52 ? kk.x : 0u, lane < 52 ? kk.z : 0u);
            if (lane < 2) ldsw<unsigned>(lds0 + ROUT + lane * 8, (0xffffu - ((lane ? t2.y : t2.x) & 0xffffu)) & 2047u);
        }
        BAR();
        TL(3);
        const unsigned row = ldsrv<unsigned>(lds0 + ROUT + (li & 1) * 8);
        u32x4 af[3], st;
        asm volatile("global_load_dwordx4 %0, %4, %5\n\tglobal_load_dwordx4 %1, %4, %5 offset:512\n\tglobal_load_dwordx4 %2, %6, %5 offset:1024\n\tbuffer_load_dwordx4 %3, %7, %8, 0 offen sc1"
                     : "=&v"(af[0]), "=&v"(af[1]), "=&v"(af[2]), "=&v"(st) : "v"(row * 1280u + wv * 64 + g * 16), "s"(x), "v"(row * 1280u + (wv & 3) * 64 + g * 16), "v"(wv ? 0x40000000 : W_ST + ((row & 1) * 1024 + (row >> 1)) * 16), "s"(rs) : "memory");
        bf16x8 wf[3][4];
        unsigned ra[4];
#pragma unroll
        for (int q = 0; q < 4; ++q) ra[q] = lds0 + wv * 16384 + (unsigned)((8 * g + (li >> 2)) * 128 + (((q ^ (((li >> 3) & 1) + 2 * (g & 1))) * 32) + (li & 3) * 8));
        auto rdf = [&](int k, int slot) {
#pragma unroll
            for (int q = 0; q < 4; ++q) wf[k][q] = cat8(trread(ra[q] + slot * 4096), trread(ra[q] + slot * 4096 + 512));
        };
        if (wv == 7) issue(1, d0, 3);
        if (!P2A && two) issue(1, d1, 2);
        if (two) wvm<12>(); else wvm<8>();
        rdf(0, 0); rdf(1, 1); if (wv < 4) rdf(2, 2);
        asm volatile("s_cmp_eq_u32 %4, 0\n\ts_cbranch_scc0 1f\n\ts_waitcnt vmcnt(0)\n1:\n\ts_waitcnt vmcnt(4)" : "+v"(af[0]), "+v"(af[1]), "+v"(af[2]), "+v"(st) : "s"(RFL((!P2A && two) || wv == 7)) : "memory", "scc");
        f32x4 c[4];
#pragma unroll
        for (int q = 0; q < 4; ++q) c[q] = (f32x4){0.f, 0.f, 0.f, 0.f};
#pragma unroll
        for (int k = 0; k < 3; ++k) if (k < n1) {
#pragma unroll
            for (int e = 0; e < 4; ++e) af[k][e] = li & 2 ? 0x3F803F80u : af[k][e];
#pragma unroll
            for (int q = 0; q < 4; ++q) c[q] = mfma32(__builtin_bit_cast(bf16x8, af[k]), wf[k][q], c[q]);
        }
        auto total = [&](int off) {
            ldsw<f32x4>(lds0 + wv * 16384 + off + lane * 16, g == 0 ? c[0] : g == 1 ? c[1] : g == 2 ? c[2] : c[3]);
            BAR();
            if (wv) return c[0];
            f32x4 pr[8];
#pragma unroll
            for (int w = 0; w < 8; ++w) pr[w] = ldsrv<f32x4>(lds0 + w * 16384 + off + lane * 16);
            return ((pr[0] + pr[1]) + (pr[2] + pr[3])) + ((pr[4] + pr[5]) + (pr[6] + pr[7]));
        };
        TL(4);
        {
            const f32x4 s = total(0);
            if (wv == 0 && f < 54) {
                SPIN {
                    if (__builtin_amdgcn_ballot_w64(st.y != tag || st.w != tag) == 0) break;
                    asm volatile("buffer_load_dwordx4 %0, %1, %2, 0 offen sc1\n\ts_waitcnt vmcnt(0)" : "=&v"(st) : "v"(W_ST + ((row & 1) * 1024 + (row >> 1)) * 16), "s"(rs) : "memory");
                }
                const float n0_ = __uint_as_float(__builtin_amdgcn_readlane(st.x, 0)), n1_ = __uint_as_float(__builtin_amdgcn_readlane(st.x, 1)), i0 = __uint_as_float(__builtin_amdgcn_readlane(st.z, 0)), i1 = __uint_as_float(__builtin_amdgcn_readlane(st.z, 1));
                float v0 = rbf(fmaf(n0_, s.z, s.x) * i0), v1 = rbf(fmaf(n1_, s.z, s.y) * i1);
                if (!(f & 1)) { v0 = silu_b(v0); v1 = silu_b(v1); }
                const unsigned hb = cvtpk(v0, v1), nb = dppu<0xB1>(hb);
                const unsigned pay = (lane & 1) ? __builtin_amdgcn_perm(hb, nb, 0x07060302u) : __builtin_amdgcn_perm(nb, hb, 0x05040100u);
                asm volatile("buffer_store_dwordx2 %0, %1, %2, 0 offen sc1" :: "v"((u32x2){pay, tag}), "v"(W_H + ((f & 1) * 2 + (lane & 1)) * 6912 + ((f >> 1) * 32 + (lane >> 1)) * 8), "s"(rs) : "memory");
            }
        }
        TL(5);
        if (f == 0 && wv == 1) {
            SPIN {
                u32x4 a_;
                asm volatile("buffer_load_dwordx4 %0, %1, %2, 0 offen sc1\n\ts_waitcnt vmcnt(0)" : "=&v"(a_) : "v"(W_ACK + (lane < 60 ? ((lane * 43) >> 7) * 80 + lane * 16 : 1600 + lane * 16)), "s"(rs) : "memory");
                if (__builtin_amdgcn_ballot_w64(a_.x != tag || a_.y != tag || a_.z != tag || a_.w != tag) == 0) break;
                __builtin_amdgcn_s_sleep(8);
            }
            if (lane == 0) __builtin_amdgcn_raw_buffer_store_b32(tag, rs, W_EP, 0, SC1);
        }
        if (f >= 6) {
            wvm<0>();
            rdf(0, 3); rdf(1, 2);
            const int ho = W_H + ((lane >> 4) & 1) * 6912 + ((lane >> 5 && two ? d1 : d0) * 16 + li) * 8;
            u32x2 hg, hu;
            __builtin_amdgcn_s_sleep(HSL);
            SPIN {
                asm volatile("buffer_load_dwordx2 %0, %2, %3, 0 offen sc1\n\tbuffer_load_dwordx2 %1, %2, %3, %4 offen sc1\n\ts_waitcnt vmcnt(0)" : "=&v"(hg), "=&v"(hu) : "v"(ho), "s"(rs), "s"(13824) : "memory");
                if (__builtin_amdgcn_ballot_w64(hg.y != tag || hu.y != tag) == 0) break;
                __builtin_amdgcn_s_sleep(HS2);
            }
            ldsw<unsigned>(lds0 + wv * 16384 + 4096 + lane * 4, cvtpk(lo2f(hg.x) * lo2f(hu.x), hi2f(hg.x) * hi2f(hu.x)));
#pragma unroll
            for (int q = 0; q < 4; ++q) c[q] = (f32x4){0.f, 0.f, 0.f, 0.f};
#pragma unroll
            for (int k = 0; k < 2; ++k) if (k == 0 || two) {
                const u32x4 h = ldsrv<u32x4>(lds0 + wv * 16384 + 4096 + k * 128 + (li & 1) * 64 + g * 16);
#pragma unroll
                for (int q = 0; q < 4; ++q) c[q] = mfma32(__builtin_bit_cast(bf16x8, h), wf[k][q], c[q]);
            }
            const f32x4 ys = total(1024);
            if (wv == 0) __builtin_amdgcn_raw_buffer_store_b128((u32x4){__float_as_uint(ys.x), tag, __float_as_uint(ys.y), tag}, rs, W_Y + kr * 10240 + (fc * 64 + lane) * 16, 0, SC1);
        }
        TL(6);
        TLEND();
        wvm<0>();
    }
}

extern "C" void kernel_launch(void* const* d_in, const int* in_sizes, int n_in, void* d_out, int out_size, void* d_ws,
                              size_t ws_size, hipStream_t stream) {
    (void)hipFuncSetAttribute((const void*)fused, hipFuncAttributeMaxDynamicSharedMemorySize, LDSZ);
    fused<<<256, 512, LDSZ, stream>>>((const u16*)d_in[0], (const u16*)d_in[1], (const u16*)d_in[2], (const u16*)d_in[4],
                                      (const u16*)d_in[5], (const u16*)d_in[6], (const u16*)d_in[7], (char*)d_ws, (u16*)d_out);
}
```

```cpp
#include <hip/hip_runtime.h>
#include <type_traits>

typedef __attribute__((ext_vector_type(8))) short bf16x8;
typedef __attribute__((ext_vector_type(4))) short bf16x4;
typedef __attribute__((ext_vector_type(4))) float f32x4;
typedef __attribute__((ext_vector_type(2))) float f32x2;
typedef __attribute__((ext_vector_type(4))) unsigned u32x4;
typedef __attribute__((ext_vector_type(2))) unsigned u32x2;
typedef __attribute__((ext_vector_type(2))) __bf16 bf16x2v;
typedef __attribute__((ext_vector_type(8))) unsigned u32x8;
typedef __attribute__((ext_vector_type(16))) unsigned u32x16;
typedef unsigned short u16;
typedef __attribute__((address_space(3))) char lchar;

#define F_ 1728
constexpr int W_EP = 0;
constexpr int W_KEY = 4096;
constexpr int W_ACK = 8192;
constexpr int W_H = 12288;
constexpr int W_ST = 65536;
constexpr int W_Y = 98304;
constexpr int W_O = 155648;
constexpr int W_END = W_O + 80 * 40960;
#define SENT 0xffffffffu
constexpr int XS = 14336, SLOT = XS + 24576;
constexpr int WO0 = 2 * SLOT;
constexpr int MISC = WO0 + 81920;
constexpr int FL_S = MISC, FL_P = MISC + 16, FL_E = MISC + 32, FL_W = MISC + 48, FL_R = MISC + 64, FL_AUX = MISC + 80, ONES = MISC + 128, ROUT = MISC + 144, LTAG = MISC + 160;
constexpr int L_NM = MISC + 256, L_RS = L_NM + 512 + 64, L_RM = L_RS + 512, L_RG = L_RM + 512, L_DUM = L_RG + 512, L_KEY = L_DUM + 512;
constexpr int LS_ROW = L_NM;
constexpr int LDSZ = 163840;
static_assert(L_KEY + 1024 <= LDSZ, "lds");
#define SPIN_MAX (1 << 20)
#define SPIN _Pragma("nounroll") for (int it = 0; it < SPIN_MAX; ++it)

__device__ __forceinline__ unsigned cvtpk(float lo, float hi) { return __builtin_bit_cast(unsigned, __builtin_convertvector((f32x2){lo, hi}, bf16x2v)); }
__device__ __forceinline__ unsigned nosent(unsigned u) { return u < 0xfffffffeu ? u : 0xfffffffeu; }
__device__ __forceinline__ float lo2f(unsigned u) { return __uint_as_float(u << 16); }
__device__ __forceinline__ float hi2f(unsigned u) { return __uint_as_float(u & 0xffff0000u); }
__device__ __forceinline__ float rbf(float f) { return lo2f(cvtpk(f, f)); }
template <int CTRL> __device__ __forceinline__ float dppf(float x) { return __builtin_bit_cast(float, __builtin_amdgcn_mov_dpp(__builtin_bit_cast(int, x), CTRL, 0xf, 0xf, true)); }
template <int CTRL> __device__ __forceinline__ unsigned dppu(unsigned x) { return (unsigned)__builtin_amdgcn_mov_dpp((int)x, CTRL, 0xf, 0xf, true); }
__device__ __forceinline__ void sw16(unsigned& a, unsigned& b) { const auto r = __builtin_amdgcn_permlane16_swap(a, b, false, false); a = r[0]; b = r[1]; }
__device__ __forceinline__ void sw32(unsigned& a, unsigned& b) { const auto r = __builtin_amdgcn_permlane32_swap(a, b, false, false); a = r[0]; b = r[1]; }
__device__ __forceinline__ void sw16f(float& a, float& b) { unsigned x = __float_as_uint(a), y = __float_as_uint(b); sw16(x, y); a = __uint_as_float(x); b = __uint_as_float(y); }
__device__ __forceinline__ void sw32f(float& a, float& b) { unsigned x = __float_as_uint(a), y = __float_as_uint(b); sw32(x, y); a = __uint_as_float(x); b = __uint_as_float(y); }
__device__ __forceinline__ float vmax(float a, float b) { return __builtin_elementwise_maximum(a, b); }
__device__ __forceinline__ float sum16(float x) { x += dppf<0xB1>(x); x += dppf<0x4E>(x); x += dppf<0x141>(x); x += dppf<0x140>(x); return x; }
__device__ __forceinline__ float dot2(unsigned a, unsigned b, float c) { return __builtin_amdgcn_fdot2_f32_bf16(__builtin_bit_cast(bf16x2v, a), __builtin_bit_cast(bf16x2v, b), c, false); }
__device__ __forceinline__ float silu_b(float v) { return v * __builtin_amdgcn_rcpf(__builtin_amdgcn_exp2f(v * -1.4426950408889634f) + 1.f); }
__device__ __forceinline__ unsigned rkey(float rb, int row) {
    unsigned b = __float_as_uint(rb) >> 16; b = (b & 0x8000u) ? (~b & 0xffffu) : (b | 0x8000u);
    return (b << 16) | (unsigned)(0xffff - row);
}
__device__ __forceinline__ float keyscore(unsigned k) { const unsigned b = k >> 16; return __uint_as_float(((b & 0x8000u) ? (b & 0x7fffu) : (~b & 0xffffu)) << 16); }
#define T2M(b0, b1) { const unsigned _b0 = (b0), _b1 = (b1); const unsigned _m = min(k0, _b0); k0 = max(k0, _b0); k1 = max(_m, max(k1, _b1)); }
__device__ __forceinline__ uint2 top2wave(unsigned k0, unsigned k1) {
    T2M(dppu<0xB1>(k0), dppu<0xB1>(k1)); T2M(dppu<0x4E>(k0), dppu<0x4E>(k1));
    T2M(dppu<0x141>(k0), dppu<0x141>(k1)); T2M(dppu<0x140>(k0), dppu<0x140>(k1));
    const unsigned a0 = __builtin_amdgcn_readlane(k0, 0), a1 = __builtin_amdgcn_readlane(k1, 0);
    const unsigned c0 = __builtin_amdgcn_readlane(k0, 16), c1 = __builtin_amdgcn_readlane(k1, 16);
    const unsigned d0 = __builtin_amdgcn_readlane(k0, 32), d1 = __builtin_amdgcn_readlane(k1, 32);
    const unsigned e0 = __builtin_amdgcn_readlane(k0, 48), e1 = __builtin_amdgcn_readlane(k1, 48);
    k0 = a0; k1 = a1; T2M(c0, c1); T2M(d0, d1); T2M(e0, e1);
    return make_uint2(k0, k1);
}
template <int N> __device__ __forceinline__ void wvm() { asm volatile("" ::: "memory"); __builtin_amdgcn_s_waitcnt((N & 15) | ((N >> 4) << 14) | 0x0F70); asm volatile("" ::: "memory"); }
__device__ __forceinline__ bf16x4 trread(unsigned a) { return __builtin_amdgcn_ds_read_tr16_b64_v4i16((__attribute__((address_space(3))) bf16x4*)(size_t)a); }
template <class T> __device__ __forceinline__ T ldsr(unsigned a) { return *(const __attribute__((address_space(3))) T*)(size_t)a; }
template <class T> __device__ __forceinline__ T ldsrv(unsigned a) { return *(volatile __attribute__((address_space(3))) T*)(size_t)a; }
template <class T> __device__ __forceinline__ void ldsw(unsigned a, T v) { *(volatile __attribute__((address_space(3))) T*)(size_t)a = v; }
__device__ __forceinline__ bf16x8 cat8(bf16x4 lo, bf16x4 hi) { return (bf16x8){lo[0], lo[1], lo[2], lo[3], hi[0], hi[1], hi[2], hi[3]}; }
__device__ __forceinline__ bf16x8 frag4(unsigned a, unsigned b, unsigned c, unsigned d) { return __builtin_bit_cast(bf16x8, (u32x4){a, b, c, d}); }
__device__ __forceinline__ f32x4 mfma32(bf16x8 a, bf16x8 b, f32x4 c) { return __builtin_amdgcn_mfma_f32_16x16x32_bf16(a, b, c, 0, 0, 0); }
__device__ __forceinline__ int lane_id() { int l; asm volatile("v_mbcnt_lo_u32_b32 %0, -1, 0\n\tv_mbcnt_hi_u32_b32 %0, -1, %0" : "=v"(l)); __builtin_assume(l >= 0 && l < 64); return l; }
#define SB() __builtin_amdgcn_sched_barrier(0)
#define BAR() asm volatile("s_waitcnt lgkmcnt(0)\n\ts_barrier" ::: "memory")
#define MEMB() asm volatile("" ::: "memory")
#define RFL(x) ((unsigned)__builtin_amdgcn_readfirstlane((int)(x)))
#define SC1 16

#define WOPF 1
#define DSEL "0"
#define TRG1D
#define TRG2D
#define PRIA "s_setprio 2\n\t"
#define TCHD
#define RSLP "s_sleep 3\n\t"
#define PRIR "s_setprio 0\n\t"
#define PRI1 "s_setprio 3\n\t"
#define PRIW
#define PRIX "s_setprio 3\n\t"
#define PRIO
#define RSL0 "s_sleep 4\n\t"
#define RSL1 "s_sleep 3\n\t"
#define ASLP "s_sleep 1\n\t"
#define PRIS
#define PRIG "s_setprio 0\n\t"
#define PSLP "s_sleep 1\n\t"
#define HSL 26
#define HS2 10
#define TCH 1
#define KSL "0"
#define E2 0
#define P2A 0
#define FPOL " nt"
#define EK2 7
#define YS2 2
#define WPF 0
#define EARLYK 0
#define NOPK(v) asm("" : "+v"(v))
#define OPQ(v) asm volatile("" : "+v"(v))
#define IB(m) __builtin_amdgcn_inverse_ballot_w64(m)
#define END() do { asm volatile("s_endpgm"); __builtin_amdgcn_s_waitcnt(0); return; } while (0)
#define BARN() asm volatile("s_barrier" ::: "memory")
#define TL(i)
#define TA(i)
#define TLEND()
__device__ __forceinline__ constexpr int phys(int S) { return (S + 1) % 3; }

__global__ __launch_bounds__(512, 2) void fused(const u16* x_, const u16* wqkv_, const u16* wo_, const u16* w_r_, const u16* wg_, const u16* wu_, const u16* wd_, char* ws_, u16* out_) {
    extern __shared__ __attribute__((aligned(16))) char smem[];
#define SL(k)
#define DUMP
#define SZ
#define SB_(k)
#define SO(k)
#define RINC
#define RZ
#define SA(k)
#define ST(h)
#define SP0
#define LINC
#define GINC
#define RCNT
    typedef __attribute__((ext_vector_type(32))) unsigned u32x32;
    u32x32 sa, sb; u32x8 sc;
    unsigned tag, bid = blockIdx.x; int tid = threadIdx.x;
    typedef __attribute__((address_space(1))) char gchar;
    typedef gchar* const __attribute__((address_space(4)))* karg_t;
    karg_t ka = (karg_t)__builtin_amdgcn_kernarg_segment_ptr();
    asm volatile(
            SL(0) SZ
            "s_load_dwordx2 s[40:41], %[k], 0x8\n\ts_load_dwordx2 s[36:37], %[k], 0x0\n\tv_readfirstlane_b32 s69, %[t]\n\ts_lshr_b32 s70, %[b], 3\n\ts_cmpk_lg_u32 s69, 0xc0\n\ts_cbranch_scc1 1f\n\t"
            "s_getpc_b64 s[72:73]\n\t.Lch:\ns_lshl3_add_u32 s71, s70, 0xffffff40\n\tv_add_lshl_u32 v36, s71, %[t], 7\n\tv_min_u32 v36, .Lce-.Lch-4, v36\n\ts_mov_b32 m0, 162368\n\t"
            "s_mov_b64 exec, 0xff\n\tglobal_load_lds_dword v36, s[72:73]\n\ts_mov_b64 exec, -1\n\t1:\ns_add_i32 s70, s70, 25\n\ts_and_b32 s70, s70, 31\n\t"
            "s_cmp_lt_u32 s70, 25\n\ts_cbranch_scc0 .LF\n\tv_bfe_u32 v36, %[t], 3, 3\n\tv_and_b32 v37, 7, %[t]\n\ts_mul_i32 s71, s70, 13\n\ts_lshr_b32 s71, s71, 6\n\t"
            "v_and_b32 v38, 2, v36\n\tv_xor_b32 v46, v38, v37\n\ts_lshl_b32 s64, s71, 2\n\ts_sub_i32 s64, s70, s64\n\ts_sub_i32 s57, s64, s71\n\tv_mul_u32_u24 v40, 0xf00, v36\n\t"
            "s_sub_i32 s72, s64, 5\n\ts_min_u32 s64, s64, s72\n\tv_lshl_add_u32 v40, v46, 4, v40\n\ts_bfe_u32 s56, s69, 0x20006\n\ts_and_b32 s72, %[b], 1\n\ts_mul_i32 s72, s72, 5\n\t"
            "s_add_i32 s58, s57, s72\n\ts_lshl_b32 s58, s58, 7\n\ts_mul_i32 s61, s64, 0x78000\n\ts_mul_i32 s73, s56, 0xf000\n\ts_add_i32 s73, s73, s58\n\ts_add_i32 s61, s61, s73\n\t"
            "s_mul_i32 s66, s56, 0x1800\n\ts_add_i32 m0, s66, 53248\n\ts_mov_b32 s42, 0x258000\n\ts_mov_b32 s43, 0x20000\n\tv_add_u32 v41, 0x100, v40\n\tv_add_u32 v42, 0x200, v40\n\t"
            "v_xor_b32 v43, 64, v40\n\tv_add_u32 v43, 0x6c00, v43\n\tv_add_u32 v44, 0x1100, v43\n\tv_add_u32 v45, 0x1200, v43\n\ts_cmpk_lt_u32 s69, 0x100\n\ts_cbranch_scc1 1f\n\t"
            "s_waitcnt lgkmcnt(0)\n\tbuffer_load_dwordx4 v40, s[40:43], s61 offen lds\n\tbuffer_load_dwordx4 v41, s[40:43], s61 offen offset:1024 lds\n\tbuffer_load_dwordx4 v42, s[40:43], s61 offen offset:2048 lds\n\tbuffer_load_dwordx4 v43, s[40:43], s61 offen offset:3072 lds\n\ts_addk_i32 m0, 0x1000\n\t"
            "s_nop 0\n\tbuffer_load_dwordx4 v44, s[40:43], s61 offen lds\n\tbuffer_load_dwordx4 v45, s[40:43], s61 offen offset:1024 lds\n\t1:\ns_mul_i32 s72, s56, 28\n\tv_add_u32 v39, s72, v36\n\t"
            "s_bfe_u32 s62, %[b], 0x10001\n\ts_mul_i32 s62, s62, 5\n\ts_add_i32 s62, s62, s71\n\ts_mul_i32 s62, s62, 104\n\ts_sub_i32 s62, s62, 8\n\tv_and_b32 v47, 3, v39\n\t"
            "v_lshl_add_u32 v47, v47, 2, s62\n\tv_and_b32 v48, 0x70, v39\n\tv_bfe_u32 v49, v39, 2, 2\n\tv_add3_u32 v48, v48, v49, v47\n\tv_bfe_u32 v49, v39, 1, 3\n\tv_xor_b32 v50, v49, v37\n\t"
            "v_mul_i32_i24 v48, 0xa00, v48\n\ts_bfe_u32 s63, %[b], 0x10002\n\ts_mul_i32 s63, s63, 0x500\n\ts_lshl_b32 s60, s64, 8\n\tv_mov_b32 v51, v36\n\tv_lshl_add_u32 v36, v50, 4, v48\n\t"
            "s_add_i32 s60, s60, s63\n\ts_mul_i32 s65, s56, 0xe00\n\ts_add_i32 m0, s65, 38912\n\ts_mov_b32 s38, 0x280000\n\ts_mov_b32 s39, 0x20000\n\tv_add_u32 v38, 0x9800, v36\n\t"
            "s_cmpk_lt_u32 s69, 0x100\n\ts_cbranch_scc1 1f\n\tbuffer_load_dwordx4 v36, s[36:39], s60 offen lds\n\tbuffer_load_dwordx4 v38, s[36:39], s60 offen offset:2048 lds\n\ts_barrier\n\t1:\n"
            "s_load_dwordx2 s[44:45], %[k], 0x38\n\ts_load_dwordx2 s[48:49], %[k], 0x10\n\ts_load_dwordx2 s[52:53], %[k], 0x40\n\tv_add_u32 v39, 8, v39\n\tv_and_b32 v48, 0x70, v39\n\tv_bfe_u32 v49, v39, 2, 2\n\t"
            "v_add3_u32 v48, v48, v49, v47\n\tv_xor_b32 v50, 4, v50\n\tv_mul_i32_i24 v48, 0xa00, v48\n\tv_lshl_add_u32 v37, v50, 4, v48\n\tv_subrev_u32 v37, 0x400, v37\n\tv_add_u32 v39, 0x9800, v37\n\t"
            "s_addk_i32 s66, 14336\n\ts_bfe_u32 s59, %[b], 0x20001\n\ts_mul_i32 s59, s59, 5\n\ts_add_i32 s59, s59, s71\n\ts_mov_b32 s46, 3432448\n\ts_mov_b32 s47, 0x20000\n\t"
            "s_mov_b32 s54, 0x280000\n\ts_mov_b32 s55, 0x20000\n\tv_and_b32 v52, 63, %[t]\n\tv_bfe_u32 v53, %[t], 4, 2\n\tv_and_b32 v54, 15, %[t]\n\tv_and_b32 v55, 3, %[t]\n\t"
            "v_bfe_u32 v56, %[t], 2, 2\n\tv_lshl_add_u32 v57, v55, 2, v56\n\ts_lshr_b32 s69, s69, 6\n\ts_cmp_lt_u32 s69, 4\n\ts_cbranch_scc0 .LB\n\ts_branch .LA\n\t"
            ".LF:\ns_load_dwordx2 s[72:73], %[k], 0x18\n\ts_and_b32 s71, %[b], 1\n\ts_lshl1_add_u32 s71, s70, s71\n\ts_mul_i32 s71, s71, 40\n\ts_sub_u32 s71, s71, 2000\n\t"
            "v_lshrrev_b32 v2, 4, %[t]\n\tv_add_u32 v2, s71, v2\n\tv_add_u32 v3, 32, v2\n\tv_min_u32 v2, 0x1ff, v2\n\tv_min_u32 v3, 0x1ff, v3\n\tv_and_b32 v4, 15, %[t]\n\t"
            "v_lshlrev_b32 v4, 4, v4\n\ts_bfe_u32 s71, %[b], 0x10001\n\ts_mul_i32 s71, s71, 0x140000\n\ts_bfe_u32 s74, %[b], 0x10002\n\ts_mul_i32 s74, s74, 0x500\n\ts_add_u32 s71, s71, s74\n\t"
            "v_add_u32 v5, s71, v4\n\ts_movk_i32 s71, 0xa00\n\tv_mad_u32_u24 v2, v2, s71, v5\n\tv_mad_u32_u24 v3, v3, s71, v5\n\ts_waitcnt lgkmcnt(0)\n\tglobal_load_dwordx4 v[104:107], v4, s[72:73] offset:0\n\t"
            "global_load_dwordx4 v[108:111], v4, s[72:73] offset:256\n\tglobal_load_dwordx4 v[112:115], v4, s[72:73] offset:512\n\tglobal_load_dwordx4 v[116:119], v4, s[72:73] offset:768\n\tglobal_load_dwordx4 v[120:123], v4, s[72:73] offset:1024\n\ts_cmpk_ge_u32 s69, 0x80\n\ts_cbranch_scc1 1f\n\t"
            "global_load_dwordx4 v[84:87], v3, s[36:37] offset:0\n\tglobal_load_dwordx4 v[88:91], v3, s[36:37] offset:256\n\tglobal_load_dwordx4 v[92:95], v3, s[36:37] offset:512\n\tglobal_load_dwordx4 v[96:99], v3, s[36:37] offset:768\n\tglobal_load_dwordx4 v[100:103], v3, s[36:37] offset:1024\n\t1:\n"
            "global_load_dwordx4 v[64:67], v2, s[36:37] offset:0\n\tglobal_load_dwordx4 v[68:71], v2, s[36:37] offset:256\n\tglobal_load_dwordx4 v[72:75], v2, s[36:37] offset:512\n\tglobal_load_dwordx4 v[76:79], v2, s[36:37] offset:768\n\tglobal_load_dwordx4 v[80:83], v2, s[36:37] offset:1024\n\ts_branch .Lout\n\t"
            ".LA:\nv_lshrrev_b32 v2, 1, v54\n\tv_xor_b32 v2, v2, v53\n\tv_lshlrev_b32 v3, 7, v54\n\tv_lshl_add_u32 v2, v2, 4, v3\n\tv_xor_b32 v3, 64, v2\n\t"
            "v_add_u32 v4, 38912, v2\n\tv_add_u32 v5, 38912, v3\n\tv_add_u32 v10, 77824, v2\n\tv_add_u32 v11, 77824, v3\n\tv_mov_b32 v12, 159872\n\tv_cmp_eq_u32 vcc, 0, v54\n\t"
            "v_mov_b32 v60, 0\n\tv_mov_b32 v61, 0\n\tv_mov_b32 v62, 0x3f803f80\n\tv_lshlrev_b32 v63, 3, v52\n\tv_cndmask_b32 v6, v2, v12, vcc\n\tv_cndmask_b32 v7, v3, v12, vcc\n\t"
            "v_cndmask_b32 v8, v4, v12, vcc\n\tv_cndmask_b32 v9, v5, v12, vcc\n\tv_cndmask_b32 v10, v10, v12, vcc\n\tv_cndmask_b32 v11, v11, v12, vcc\n\tv_lshlrev_b32 v64, 2, v52\n\tv_lshrrev_b32 v65, 2, v52\n\t"
            "v_cmp_eq_u32 vcc, 8, v65\n\tv_add_u32 v66, 161088, v63\n\tv_add_u32 v64, 159744, v64\n\tv_cndmask_b32 v62, 0, v62, vcc\n\ts_cmp_lg_u32 s69, 1\n\ts_cbranch_scc1 1f\n\t"
            "ds_write_b32 v64, v62\n\tds_write_b64 v66, v[60:61]\n\t1:\nv_bfe_u32 v12, v52, 3, 2\n\tv_xor_b32 v12, s69, v12\n\tv_lshlrev_b32 v13, 7, v56\n\t"
            "v_lshl_add_u32 v13, v55, 3, v13\n\tv_lshl_add_u32 v12, v12, 5, v13\n\tv_mul_u32_u24 v13, 0xc00, v53\n\ts_movk_i32 s72, 14336\n\tv_add3_u32 v12, v12, v13, s72\n\tv_add_u32 v13, 38912, v12\n\t"
            "s_mov_b32 s72, 160000\n\tv_lshl_add_u32 v14, v54, 2, s72\n\tv_lshl_add_u32 v15, v53, 4, s72\n\ts_cmp_eq_u32 s69, 3\n\ts_cselect_b32 s74, 1, 0\n\ts_cselect_b64 s[76:77], -1, 0\n\t"
            "s_cselect_b32 s75, 4, 0\n\ts_add_i32 s72, s69, 1\n\ts_bfe_u32 s72, s72, 0x10001\n\ts_xor_b32 s72, s72, 1\n\ts_lshr_b32 s73, s69, 1\n\ts_lshl1_add_u32 s78, s72, s73\n\t"
            "s_lshl2_add_u32 s78, s78, 159776\n\tv_mov_b32 v34, s78\n\ts_lshl2_add_u32 s78, s69, 159744\n\tv_mov_b32 v33, s78\n\ts_mul_i32 s78, s59, 163840\n\ts_lshl_b32 s79, s58, 5\n\t"
            "s_add_i32 s78, s78, s79\n\ts_lshl_b32 s79, s69, 10\n\ts_add_i32 s78, s78, s79\n\ts_add_i32 s78, s78, 155648\n\tv_lshl_add_u32 v16, v52, 4, s78\n\tv_and_b32 v215, 1, v53\n\t"
            "v_lshl_add_u32 v215, v215, 30, v16\n\ts_lshl_b32 s79, s74, 11\n\ts_add_i32 s78, s78, s79\n\ts_sub_i32 s78, s78, 0x400\n\ts_cmp_lg_u32 s57, s75\n\ts_cselect_b32 s79, s72, 0\n\t"
            "s_cmp_lg_u32 s79, 0\n\ts_cselect_b32 s78, s78, 0x40000000\n\tv_lshl_add_u32 v17, v52, 4, s78\n\ts_sub_i32 s78, s69, s57\n\ts_add_i32 s79, s78, 5\n\ts_min_u32 s78, s78, s79\n\t"
            "v_cmp_gt_u32 s[80:81], s78, v53\n\tv_cmp_gt_u32 s[82:83], s78, v56\n\ts_mov_b32 s14, 0x33363333\n\ts_mov_b32 s15, 0x36663366\n\ts_mov_b32 s16, 0x666c6666\n\ts_mov_b32 s17, 0x6ccc66cc\n\t"
            "s_mov_b32 s18, 0x33333333\n\ts_mov_b32 s19, 0x33333333\n\ts_xor_b64 s[80:81], s[80:81], s[76:77]\n\ts_xor_b64 s[82:83], s[82:83], s[76:77]\n\ts_andn2_b64 vcc, s[80:81], s[18:19]\n\tv_cndmask_b32 v18, 0, -1, vcc\n\t"
            "s_and_b64 vcc, s[80:81], s[18:19]\n\tv_cndmask_b32 v19, 0, -1, vcc\n\ts_nor_b64 vcc, s[80:81], s[18:19]\n\tv_cndmask_b32 v20, 0, -1, vcc\n\ts_andn2_b64 vcc, s[18:19], s[80:81]\n\tv_cndmask_b32 v21, 0, -1, vcc\n\t"
            "v_cndmask_b32 v22, 0, -1, s[82:83]\n\tv_cndmask_b32 v23, -1, 0, s[82:83]\n\tv_sub_u32 v58, v57, v53\n\tv_add_u32 v58, 4, v58\n\tv_and_b32 v58, 28, v58\n\tv_lshlrev_b32 v58, 1, v58\n\t"
            "v_mov_b32 v24, -1\n\tv_mov_b32 v25, 0\n\tv_lshlrev_b64 v[24:25], v58, v[24:25]\n\tv_lshlrev_b64 v[24:25], v58, v[24:25]\n\tv_sub_u32 v58, 32, v58\n\tv_min_u32 v58, 16, v58\n\t"
            "v_lshrrev_b32 v26, v58, -1\n\tv_lshrrev_b32 v26, v58, v26\n\ts_lshl1_add_u32 s78, s69, s74\n\ts_xor_b32 s79, s78, 1\n\ts_mul_i32 s78, s78, 0xc00\n\ts_mul_i32 s79, s79, 0xc00\n\t"
            "s_addk_i32 s78, 14336\n\ts_addk_i32 s79, 14336\n\tv_add_u32 v28, s78, v63\n\tv_add_u32 v29, s79, v63\n\ts_cmp_eq_u32 s69, s57\n\ts_addc_u32 s78, s69, 0\n\t"
            "s_add_i32 s79, s69, 1\n\ts_cmp_eq_u32 s79, s57\n\ts_subb_u32 s79, s79, 0\n\ts_cmp_eq_u32 s74, 1\n\ts_cselect_b32 s80, s79, s78\n\ts_cselect_b32 s81, s78, s79\n\t"
            "v_lshlrev_b32 v58, 2, v53\n\tv_lshl_add_u32 v58, v54, 4, v58\n\ts_cmp_gt_u32 s80, s57\n\ts_subb_u32 s82, s80, 0\n\ts_lshl2_add_u32 s82, s82, 159760\n\tv_mov_b32 v59, s82\n\t"
            "s_lshl_b32 s82, s80, 1\n\ts_max_u32 s82, s82, 1\n\ts_mul_i32 s82, s82, 0xc00\n\ts_addk_i32 s82, 11264\n\tv_add_u32 v30, s82, v58\n\ts_cmp_gt_u32 s81, s57\n\t"
            "s_subb_u32 s82, s81, 0\n\ts_lshl2_add_u32 s82, s82, 159760\n\tv_mov_b32 v60, s82\n\ts_lshl_b32 s82, s81, 1\n\ts_max_u32 s82, s82, 1\n\ts_mul_i32 s82, s82, 0xc00\n\t"
            "s_addk_i32 s82, 11264\n\tv_add_u32 v31, s82, v58\n\tv_cndmask_b32 v32, v59, v60, s[18:19]\n\tv_mov_b32 v35, 1\n\ts_waitcnt lgkmcnt(0)\n\ts_barrier\n\t"
            "s_add_u32 m0, s65, 38912\n\ts_load_dword s67, s[44:45], 0x0\n\tbuffer_load_dwordx4 v37, s[36:39], s60 offen offset:1024 lds\n\ts_mov_b32 exec_hi, 0\n\tbuffer_load_dwordx4 v39, s[36:39], s60 offen offset:3072 lds\n\ts_mov_b32 exec_hi, -1\n\t"
            "s_add_u32 m0, s66, 77824\n\ts_add_u32 s61, s61, 0x3c000\n\tbuffer_load_dwordx4 v40, s[40:43], s61 offen lds\n\tbuffer_load_dwordx4 v41, s[40:43], s61 offen offset:1024 lds\n\tbuffer_load_dwordx4 v42, s[40:43], s61 offen offset:2048 lds\n\tbuffer_load_dwordx4 v43, s[40:43], s61 offen offset:3072 lds\n\t"
            "s_addk_i32 m0, 0x1000\n\ts_add_u32 s60, s60, 128\n\tbuffer_load_dwordx4 v44, s[40:43], s61 offen lds\n\tbuffer_load_dwordx4 v45, s[40:43], s61 offen offset:1024 lds\n\ts_add_u32 m0, s65, 77824\n\ts_mov_b32 s20, 0\n\t"
            "s_mov_b32 s21, 40960\n\ts_mov_b32 s22, 81920\n\ts_mov_b32 s23, 122880\n\tbuffer_load_dwordx4 v36, s[36:39], s60 offen lds\n\tbuffer_load_dwordx4 v37, s[36:39], s60 offen offset:1024 lds\n\tbuffer_load_dwordx4 v38, s[36:39], s60 offen offset:2048 lds\n\t"
            "s_mov_b32 exec_hi, 0\n\tbuffer_load_dwordx4 v39, s[36:39], s60 offen offset:3072 lds\n\ts_mov_b32 exec_hi, -1\n\ts_mov_b32 s68, 0x40000\n\ts_waitcnt lgkmcnt(0)\n\ts_mul_i32 s67, s67, 0x19660d\n\t"
            "s_add_u32 s67, s67, 0x3c6ef35f\n\ts_waitcnt vmcnt(10)\n\ts_barrier\n\t"
            SA(1) ST(-1)
            "ds_read_b64_tr_b16 v[176:177], v13 offset:0\n\tds_read_b64_tr_b16 v[178:179], v13 offset:512\n\tds_read_b128 v[120:123], v8\n\tds_read_b128 v[124:127], v4 offset:2048\n\tds_read_b128 v[128:131], v4 offset:4096\n\tds_read_b128 v[132:135], v4 offset:6144\n\t"
            "ds_read_b128 v[136:139], v4 offset:8192\n\tds_read_b128 v[140:143], v4 offset:10240\n\tds_read_b128 v[144:147], v4 offset:12288\n\tds_read_b64_tr_b16 v[180:181], v13 offset:1024\n\tds_read_b64_tr_b16 v[182:183], v13 offset:1536\n\tds_read_b64_tr_b16 v[184:185], v13 offset:2048\n\t"
            "ds_read_b64_tr_b16 v[186:187], v13 offset:2560\n\ts_waitcnt lgkmcnt(10)\n\tv_mfma_f32_16x16x32_bf16 v[36:39], v[176:179], v[120:123], 0\n\ts_waitcnt lgkmcnt(9)\n\tv_mfma_f32_16x16x32_bf16 v[48:51], v[176:179], v[124:127], 0\n\ts_waitcnt lgkmcnt(8)\n\t"
            "v_mfma_f32_16x16x32_bf16 v[60:63], v[176:179], v[128:131], 0\n\tds_read_b64_tr_b16 v[188:189], v13 offset:12288\n\tds_read_b64_tr_b16 v[190:191], v13 offset:12800\n\ts_waitcnt lgkmcnt(9)\n\tv_mfma_f32_16x16x32_bf16 v[72:75], v[176:179], v[132:135], 0\n\tds_read_b128 v[148:151], v9\n\t"
            "s_waitcnt lgkmcnt(9)\n\tv_mfma_f32_16x16x32_bf16 v[84:87], v[176:179], v[136:139], 0\n\tds_read_b128 v[152:155], v5 offset:2048\n\ts_waitcnt lgkmcnt(9)\n\tv_mfma_f32_16x16x32_bf16 v[96:99], v[176:179], v[140:143], 0\n\tds_read_b128 v[156:159], v5 offset:4096\n\t"
            "s_waitcnt lgkmcnt(9)\n\tv_mfma_f32_16x16x32_bf16 v[108:111], v[176:179], v[144:147], 0\n\tds_read_b128 v[160:163], v5 offset:6144\n\ts_waitcnt lgkmcnt(8)\n\tv_mfma_f32_16x16x32_bf16 v[40:43], v[180:183], v[120:123], 0\n\tds_read_b128 v[164:167], v5 offset:8192\n\t"
            "v_mfma_f32_16x16x32_bf16 v[52:55], v[180:183], v[124:127], 0\n\tds_read_b128 v[168:171], v5 offset:10240\n\tv_mfma_f32_16x16x32_bf16 v[64:67], v[180:183], v[128:131], 0\n\tds_read_b128 v[172:175], v5 offset:12288\n\tv_mfma_f32_16x16x32_bf16 v[76:79], v[180:183], v[132:135], 0\n\tds_read_b64_tr_b16 v[192:193], v13 offset:13312\n\t"
            "ds_read_b64_tr_b16 v[194:195], v13 offset:13824\n\tv_mfma_f32_16x16x32_bf16 v[88:91], v[180:183], v[136:139], 0\n\tds_read_b64_tr_b16 v[196:197], v13 offset:14336\n\tds_read_b64_tr_b16 v[198:199], v13 offset:14848\n\tv_mfma_f32_16x16x32_bf16 v[100:103], v[180:183], v[140:143], 0\n\tv_mfma_f32_16x16x32_bf16 v[112:115], v[180:183], v[144:147], 0\n\t"
            "s_waitcnt lgkmcnt(13)\n\tv_mfma_f32_16x16x32_bf16 v[44:47], v[120:123], v[184:187], 0\n\tv_mfma_f32_16x16x32_bf16 v[56:59], v[124:127], v[184:187], 0\n\tv_mfma_f32_16x16x32_bf16 v[68:71], v[128:131], v[184:187], 0\n\tv_mfma_f32_16x16x32_bf16 v[80:83], v[132:135], v[184:187], 0\n\tv_mfma_f32_16x16x32_bf16 v[92:95], v[136:139], v[184:187], 0\n\t"
            "v_mfma_f32_16x16x32_bf16 v[104:107], v[140:143], v[184:187], 0\n\tv_mfma_f32_16x16x32_bf16 v[116:119], v[144:147], v[184:187], 0\n\ts_waitcnt lgkmcnt(10)\n\tv_mfma_f32_16x16x32_bf16 v[36:39], v[188:191], v[148:151], v[36:39]\n\ts_waitcnt lgkmcnt(9)\n\tv_mfma_f32_16x16x32_bf16 v[48:51], v[188:191], v[152:155], v[48:51]\n\t"
            "s_waitcnt lgkmcnt(8)\n\tv_mfma_f32_16x16x32_bf16 v[60:63], v[188:191], v[156:159], v[60:63]\n\ts_waitcnt lgkmcnt(7)\n\tv_mfma_f32_16x16x32_bf16 v[72:75], v[188:191], v[160:163], v[72:75]\n\ts_waitcnt lgkmcnt(6)\n\tv_mfma_f32_16x16x32_bf16 v[84:87], v[188:191], v[164:167], v[84:87]\n\t"
            "s_waitcnt lgkmcnt(5)\n\tv_mfma_f32_16x16x32_bf16 v[96:99], v[188:191], v[168:171], v[96:99]\n\ts_waitcnt lgkmcnt(4)\n\tv_mfma_f32_16x16x32_bf16 v[108:111], v[188:191], v[172:175], v[108:111]\n\ts_waitcnt lgkmcnt(2)\n\tv_mfma_f32_16x16x32_bf16 v[40:43], v[192:195], v[148:151], v[40:43]\n\t"
            "v_mfma_f32_16x16x32_bf16 v[52:55], v[192:195], v[152:155], v[52:55]\n\tv_mfma_f32_16x16x32_bf16 v[64:67], v[192:195], v[156:159], v[64:67]\n\tv_mfma_f32_16x16x32_bf16 v[76:79], v[192:195], v[160:163], v[76:79]\n\ts_waitcnt vmcnt(0) lgkmcnt(0)\n\ts_barrier\n\t"
            ST(1)
            "ds_read_b64_tr_b16 v[176:177], v13 offset:38912\n\tds_read_b64_tr_b16 v[178:179], v13 offset:39424\n\tv_mfma_f32_16x16x32_bf16 v[88:91], v[192:195], v[164:167], v[88:91]\n\tds_read_b128 v[120:123], v10\n\tv_mfma_f32_16x16x32_bf16 v[100:103], v[192:195], v[168:171], v[100:103]\n\tds_read_b128 v[124:127], v4 offset:40960\n\t"
            "v_mfma_f32_16x16x32_bf16 v[112:115], v[192:195], v[172:175], v[112:115]\n\tds_read_b128 v[128:131], v4 offset:43008\n\tv_mfma_f32_16x16x32_bf16 v[44:47], v[148:151], v[196:199], v[44:47]\n\tds_read_b128 v[132:135], v4 offset:45056\n\tv_mfma_f32_16x16x32_bf16 v[56:59], v[152:155], v[196:199], v[56:59]\n\tds_read_b128 v[136:139], v4 offset:47104\n\t"
            "v_mfma_f32_16x16x32_bf16 v[68:71], v[156:159], v[196:199], v[68:71]\n\tds_read_b128 v[140:143], v4 offset:49152\n\tv_mfma_f32_16x16x32_bf16 v[80:83], v[160:163], v[196:199], v[80:83]\n\tds_read_b128 v[144:147], v4 offset:51200\n\tv_mfma_f32_16x16x32_bf16 v[92:95], v[164:167], v[196:199], v[92:95]\n\tds_read_b64_tr_b16 v[180:181], v13 offset:39936\n\t"
            "ds_read_b64_tr_b16 v[182:183], v13 offset:40448\n\tv_mfma_f32_16x16x32_bf16 v[104:107], v[168:171], v[196:199], v[104:107]\n\tds_read_b64_tr_b16 v[184:185], v13 offset:40960\n\tds_read_b64_tr_b16 v[186:187], v13 offset:41472\n\tv_mfma_f32_16x16x32_bf16 v[116:119], v[172:175], v[196:199], v[116:119]\n\ts_waitcnt lgkmcnt(10)\n\t"
            "v_mfma_f32_16x16x32_bf16 v[36:39], v[176:179], v[120:123], v[36:39]\n\ts_waitcnt lgkmcnt(9)\n\tv_mfma_f32_16x16x32_bf16 v[48:51], v[176:179], v[124:127], v[48:51]\n\ts_waitcnt lgkmcnt(8)\n\tv_mfma_f32_16x16x32_bf16 v[60:63], v[176:179], v[128:131], v[60:63]\n\tds_read_b64_tr_b16 v[188:189], v13 offset:51200\n\t"
            "ds_read_b64_tr_b16 v[190:191], v13 offset:51712\n\ts_waitcnt lgkmcnt(9)\n\tv_mfma_f32_16x16x32_bf16 v[72:75], v[176:179], v[132:135], v[72:75]\n\tds_read_b128 v[148:151], v11\n\ts_waitcnt lgkmcnt(9)\n\tv_mfma_f32_16x16x32_bf16 v[84:87], v[176:179], v[136:139], v[84:87]\n\t"
            "ds_read_b128 v[152:155], v5 offset:40960\n\ts_waitcnt lgkmcnt(9)\n\tv_mfma_f32_16x16x32_bf16 v[96:99], v[176:179], v[140:143], v[96:99]\n\tds_read_b128 v[156:159], v5 offset:43008\n\ts_waitcnt lgkmcnt(9)\n\tv_mfma_f32_16x16x32_bf16 v[108:111], v[176:179], v[144:147], v[108:111]\n\t"
            "ds_read_b128 v[160:163], v5 offset:45056\n\ts_waitcnt lgkmcnt(8)\n\tv_mfma_f32_16x16x32_bf16 v[40:43], v[180:183], v[120:123], v[40:43]\n\tds_read_b128 v[164:167], v5 offset:47104\n\tv_mfma_f32_16x16x32_bf16 v[52:55], v[180:183], v[124:127], v[52:55]\n\tds_read_b128 v[168:171], v5 offset:49152\n\t"
            "v_mfma_f32_16x16x32_bf16 v[64:67], v[180:183], v[128:131], v[64:67]\n\tds_read_b128 v[172:175], v5 offset:51200\n\tv_mfma_f32_16x16x32_bf16 v[76:79], v[180:183], v[132:135], v[76:79]\n\tds_read_b64_tr_b16 v[192:193], v13 offset:52224\n\tds_read_b64_tr_b16 v[194:195], v13 offset:52736\n\tv_mfma_f32_16x16x32_bf16 v[88:91], v[180:183], v[136:139], v[88:91]\n\t"
            "ds_read_b64_tr_b16 v[196:197], v13 offset:53248\n\tds_read_b64_tr_b16 v[198:199], v13 offset:53760\n\tv_mfma_f32_16x16x32_bf16 v[100:103], v[180:183], v[140:143], v[100:103]\n\tv_mfma_f32_16x16x32_bf16 v[112:115], v[180:183], v[144:147], v[112:115]\n\ts_waitcnt lgkmcnt(13)\n\tv_mfma_f32_16x16x32_bf16 v[44:47], v[120:123], v[184:187], v[44:47]\n\t"
            "v_mfma_f32_16x16x32_bf16 v[56:59], v[124:127], v[184:187], v[56:59]\n\tv_mfma_f32_16x16x32_bf16 v[68:71], v[128:131], v[184:187], v[68:71]\n\tv_mfma_f32_16x16x32_bf16 v[80:83], v[132:135], v[184:187], v[80:83]\n\tv_mfma_f32_16x16x32_bf16 v[92:95], v[136:139], v[184:187], v[92:95]\n\tv_mfma_f32_16x16x32_bf16 v[104:107], v[140:143], v[184:187], v[104:107]\n\tv_mfma_f32_16x16x32_bf16 v[116:119], v[144:147], v[184:187], v[116:119]\n\t"
            "s_waitcnt lgkmcnt(10)\n\tv_mfma_f32_16x16x32_bf16 v[36:39], v[188:191], v[148:151], v[36:39]\n\ts_waitcnt lgkmcnt(9)\n\tv_mfma_f32_16x16x32_bf16 v[48:51], v[188:191], v[152:155], v[48:51]\n\ts_waitcnt lgkmcnt(8)\n\tv_mfma_f32_16x16x32_bf16 v[60:63], v[188:191], v[156:159], v[60:63]\n\t"
            "s_waitcnt lgkmcnt(7)\n\tv_mfma_f32_16x16x32_bf16 v[72:75], v[188:191], v[160:163], v[72:75]\n\ts_waitcnt lgkmcnt(6)\n\tv_mfma_f32_16x16x32_bf16 v[84:87], v[188:191], v[164:167], v[84:87]\n\ts_waitcnt lgkmcnt(5)\n\tv_mfma_f32_16x16x32_bf16 v[96:99], v[188:191], v[168:171], v[96:99]\n\t"
            "s_waitcnt lgkmcnt(4)\n\tv_mfma_f32_16x16x32_bf16 v[108:111], v[188:191], v[172:175], v[108:111]\n\ts_waitcnt lgkmcnt(2)\n\tv_mfma_f32_16x16x32_bf16 v[40:43], v[192:195], v[148:151], v[40:43]\n\tv_mfma_f32_16x16x32_bf16 v[52:55], v[192:195], v[152:155], v[52:55]\n\tv_mfma_f32_16x16x32_bf16 v[64:67], v[192:195], v[156:159], v[64:67]\n\t"
            "v_mfma_f32_16x16x32_bf16 v[76:79], v[192:195], v[160:163], v[76:79]\n\ts_waitcnt lgkmcnt(0)\n\ts_barrier\n\t"
            ST(3)
            "ds_read_b64_tr_b16 v[176:177], v12 offset:0\n\tds_read_b64_tr_b16 v[178:179], v12 offset:512\n\tv_mfma_f32_16x16x32_bf16 v[88:91], v[192:195], v[164:167], v[88:91]\n\tds_read_b128 v[120:123], v6\n\tv_mfma_f32_16x16x32_bf16 v[100:103], v[192:195], v[168:171], v[100:103]\n\tds_read_b128 v[124:127], v2 offset:2048\n\t"
            "v_mfma_f32_16x16x32_bf16 v[112:115], v[192:195], v[172:175], v[112:115]\n\tds_read_b128 v[128:131], v2 offset:4096\n\tv_mfma_f32_16x16x32_bf16 v[44:47], v[148:151], v[196:199], v[44:47]\n\tds_read_b128 v[132:135], v2 offset:6144\n\tv_mfma_f32_16x16x32_bf16 v[56:59], v[152:155], v[196:199], v[56:59]\n\tds_read_b128 v[136:139], v2 offset:8192\n\t"
            "v_mfma_f32_16x16x32_bf16 v[68:71], v[156:159], v[196:199], v[68:71]\n\tds_read_b128 v[140:143], v2 offset:10240\n\tv_mfma_f32_16x16x32_bf16 v[80:83], v[160:163], v[196:199], v[80:83]\n\tds_read_b128 v[144:147], v2 offset:12288\n\tv_mfma_f32_16x16x32_bf16 v[92:95], v[164:167], v[196:199], v[92:95]\n\tds_read_b64_tr_b16 v[180:181], v12 offset:1024\n\t"
            "ds_read_b64_tr_b16 v[182:183], v12 offset:1536\n\tv_mfma_f32_16x16x32_bf16 v[104:107], v[168:171], v[196:199], v[104:107]\n\tds_read_b64_tr_b16 v[184:185], v12 offset:2048\n\tds_read_b64_tr_b16 v[186:187], v12 offset:2560\n\tv_mfma_f32_16x16x32_bf16 v[116:119], v[172:175], v[196:199], v[116:119]\n\ts_waitcnt lgkmcnt(10)\n\t"
            "v_mfma_f32_16x16x32_bf16 v[36:39], v[176:179], v[120:123], v[36:39]\n\ts_waitcnt lgkmcnt(9)\n\tv_mfma_f32_16x16x32_bf16 v[48:51], v[176:179], v[124:127], v[48:51]\n\ts_waitcnt lgkmcnt(8)\n\tv_mfma_f32_16x16x32_bf16 v[60:63], v[176:179], v[128:131], v[60:63]\n\tds_read_b64_tr_b16 v[188:189], v12 offset:12288\n\t"
            "ds_read_b64_tr_b16 v[190:191], v12 offset:12800\n\ts_waitcnt lgkmcnt(9)\n\tv_mfma_f32_16x16x32_bf16 v[72:75], v[176:179], v[132:135], v[72:75]\n\tds_read_b128 v[148:151], v7\n\ts_waitcnt lgkmcnt(9)\n\tv_mfma_f32_16x16x32_bf16 v[84:87], v[176:179], v[136:139], v[84:87]\n\t"
            "ds_read_b128 v[152:155], v3 offset:2048\n\ts_waitcnt lgkmcnt(9)\n\tv_mfma_f32_16x16x32_bf16 v[96:99], v[176:179], v[140:143], v[96:99]\n\tds_read_b128 v[156:159], v3 offset:4096\n\ts_waitcnt lgkmcnt(9)\n\tv_mfma_f32_16x16x32_bf16 v[108:111], v[176:179], v[144:147], v[108:111]\n\t"
            "ds_read_b128 v[160:163], v3 offset:6144\n\ts_waitcnt lgkmcnt(8)\n\tv_mfma_f32_16x16x32_bf16 v[40:43], v[180:183], v[120:123], v[40:43]\n\tds_read_b128 v[164:167], v3 offset:8192\n\tv_mfma_f32_16x16x32_bf16 v[52:55], v[180:183], v[124:127], v[52:55]\n\tds_read_b128 v[168:171], v3 offset:10240\n\t"
            "v_mfma_f32_16x16x32_bf16 v[64:67], v[180:183], v[128:131], v[64:67]\n\tds_read_b128 v[172:175], v3 offset:12288\n\tv_mfma_f32_16x16x32_bf16 v[76:79], v[180:183], v[132:135], v[76:79]\n\tds_read_b64_tr_b16 v[192:193], v12 offset:13312\n\tds_read_b64_tr_b16 v[194:195], v12 offset:13824\n\tv_mfma_f32_16x16x32_bf16 v[88:91], v[180:183], v[136:139], v[88:91]\n\t"
            "ds_read_b64_tr_b16 v[196:197], v12 offset:14336\n\tds_read_b64_tr_b16 v[198:199], v12 offset:14848\n\tv_mfma_f32_16x16x32_bf16 v[100:103], v[180:183], v[140:143], v[100:103]\n\tv_mfma_f32_16x16x32_bf16 v[112:115], v[180:183], v[144:147], v[112:115]\n\ts_waitcnt lgkmcnt(13)\n\tv_mfma_f32_16x16x32_bf16 v[44:47], v[120:123], v[184:187], v[44:47]\n\t"
            "v_mfma_f32_16x16x32_bf16 v[56:59], v[124:127], v[184:187], v[56:59]\n\tv_mfma_f32_16x16x32_bf16 v[68:71], v[128:131], v[184:187], v[68:71]\n\tv_mfma_f32_16x16x32_bf16 v[80:83], v[132:135], v[184:187], v[80:83]\n\tv_mfma_f32_16x16x32_bf16 v[92:95], v[136:139], v[184:187], v[92:95]\n\tv_mfma_f32_16x16x32_bf16 v[104:107], v[140:143], v[184:187], v[104:107]\n\tv_mfma_f32_16x16x32_bf16 v[116:119], v[144:147], v[184:187], v[116:119]\n\t"
            "s_waitcnt lgkmcnt(10)\n\tv_mfma_f32_16x16x32_bf16 v[36:39], v[188:191], v[148:151], v[36:39]\n\ts_waitcnt lgkmcnt(9)\n\tv_mfma_f32_16x16x32_bf16 v[48:51], v[188:191], v[152:155], v[48:51]\n\ts_waitcnt lgkmcnt(8)\n\tv_mfma_f32_16x16x32_bf16 v[60:63], v[188:191], v[156:159], v[60:63]\n\t"
            "s_waitcnt lgkmcnt(7)\n\tv_mfma_f32_16x16x32_bf16 v[72:75], v[188:191], v[160:163], v[72:75]\n\ts_waitcnt lgkmcnt(6)\n\tv_mfma_f32_16x16x32_bf16 v[84:87], v[188:191], v[164:167], v[84:87]\n\ts_waitcnt lgkmcnt(5)\n\tv_mfma_f32_16x16x32_bf16 v[96:99], v[188:191], v[168:171], v[96:99]\n\t"
            "s_waitcnt lgkmcnt(4)\n\tv_mfma_f32_16x16x32_bf16 v[108:111], v[188:191], v[172:175], v[108:111]\n\ts_waitcnt lgkmcnt(2)\n\tv_mfma_f32_16x16x32_bf16 v[40:43], v[192:195], v[148:151], v[40:43]\n\tv_mfma_f32_16x16x32_bf16 v[52:55], v[192:195], v[152:155], v[52:55]\n\tv_mfma_f32_16x16x32_bf16 v[64:67], v[192:195], v[156:159], v[64:67]\n\t"
            "v_mfma_f32_16x16x32_bf16 v[76:79], v[192:195], v[160:163], v[76:79]\n\ts_waitcnt lgkmcnt(0)\n\ts_barrier\n\t"
            ST(5)
            "ds_read_b64_tr_b16 v[176:177], v13 offset:0\n\tds_read_b64_tr_b16 v[178:179], v13 offset:512\n\tv_mfma_f32_16x16x32_bf16 v[88:91], v[192:195], v[164:167], v[88:91]\n\tds_read_b128 v[120:123], v8\n\tv_mfma_f32_16x16x32_bf16 v[100:103], v[192:195], v[168:171], v[100:103]\n\tds_read_b128 v[124:127], v4 offset:2048\n\t"
            "v_mfma_f32_16x16x32_bf16 v[112:115], v[192:195], v[172:175], v[112:115]\n\tds_read_b128 v[128:131], v4 offset:4096\n\tv_mfma_f32_16x16x32_bf16 v[44:47], v[148:151], v[196:199], v[44:47]\n\tds_read_b128 v[132:135], v4 offset:6144\n\tv_mfma_f32_16x16x32_bf16 v[56:59], v[152:155], v[196:199], v[56:59]\n\tds_read_b128 v[136:139], v4 offset:8192\n\t"
            "v_mfma_f32_16x16x32_bf16 v[68:71], v[156:159], v[196:199], v[68:71]\n\tds_read_b128 v[140:143], v4 offset:10240\n\tv_mfma_f32_16x16x32_bf16 v[80:83], v[160:163], v[196:199], v[80:83]\n\tds_read_b128 v[144:147], v4 offset:12288\n\tv_mfma_f32_16x16x32_bf16 v[92:95], v[164:167], v[196:199], v[92:95]\n\tds_read_b64_tr_b16 v[180:181], v13 offset:1024\n\t"
            "ds_read_b64_tr_b16 v[182:183], v13 offset:1536\n\tv_mfma_f32_16x16x32_bf16 v[104:107], v[168:171], v[196:199], v[104:107]\n\tds_read_b64_tr_b16 v[184:185], v13 offset:2048\n\tds_read_b64_tr_b16 v[186:187], v13 offset:2560\n\tv_mfma_f32_16x16x32_bf16 v[116:119], v[172:175], v[196:199], v[116:119]\n\ts_waitcnt lgkmcnt(10)\n\t"
            "v_mfma_f32_16x16x32_bf16 v[36:39], v[176:179], v[120:123], v[36:39]\n\ts_waitcnt lgkmcnt(9)\n\tv_mfma_f32_16x16x32_bf16 v[48:51], v[176:179], v[124:127], v[48:51]\n\ts_waitcnt lgkmcnt(8)\n\tv_mfma_f32_16x16x32_bf16 v[60:63], v[176:179], v[128:131], v[60:63]\n\tds_read_b64_tr_b16 v[188:189], v13 offset:12288\n\t"
            "ds_read_b64_tr_b16 v[190:191], v13 offset:12800\n\ts_waitcnt lgkmcnt(9)\n\tv_mfma_f32_16x16x32_bf16 v[72:75], v[176:179], v[132:135], v[72:75]\n\tds_read_b128 v[148:151], v9\n\ts_waitcnt lgkmcnt(9)\n\tv_mfma_f32_16x16x32_bf16 v[84:87], v[176:179], v[136:139], v[84:87]\n\t"
            "ds_read_b128 v[152:155], v5 offset:2048\n\ts_waitcnt lgkmcnt(9)\n\tv_mfma_f32_16x16x32_bf16 v[96:99], v[176:179], v[140:143], v[96:99]\n\tds_read_b128 v[156:159], v5 offset:4096\n\ts_waitcnt lgkmcnt(9)\n\tv_mfma_f32_16x16x32_bf16 v[108:111], v[176:179], v[144:147], v[108:111]\n\t"
            "ds_read_b128 v[160:163], v5 offset:6144\n\ts_waitcnt lgkmcnt(8)\n\tv_mfma_f32_16x16x32_bf16 v[40:43], v[180:183], v[120:123], v[40:43]\n\tds_read_b128 v[164:167], v5 offset:8192\n\tv_mfma_f32_16x16x32_bf16 v[52:55], v[180:183], v[124:127], v[52:55]\n\tds_read_b128 v[168:171], v5 offset:10240\n\t"
            "v_mfma_f32_16x16x32_bf16 v[64:67], v[180:183], v[128:131], v[64:67]\n\tds_read_b128 v[172:175], v5 offset:12288\n\tv_mfma_f32_16x16x32_bf16 v[76:79], v[180:183], v[132:135], v[76:79]\n\tds_read_b64_tr_b16 v[192:193], v13 offset:13312\n\tds_read_b64_tr_b16 v[194:195], v13 offset:13824\n\tv_mfma_f32_16x16x32_bf16 v[88:91], v[180:183], v[136:139], v[88:91]\n\t"
            "ds_read_b64_tr_b16 v[196:197], v13 offset:14336\n\tds_read_b64_tr_b16 v[198:199], v13 offset:14848\n\tv_mfma_f32_16x16x32_bf16 v[100:103], v[180:183], v[140:143], v[100:103]\n\tv_mfma_f32_16x16x32_bf16 v[112:115], v[180:183], v[144:147], v[112:115]\n\ts_waitcnt lgkmcnt(13)\n\tv_mfma_f32_16x16x32_bf16 v[44:47], v[120:123], v[184:187], v[44:47]\n\t"
            "v_mfma_f32_16x16x32_bf16 v[56:59], v[124:127], v[184:187], v[56:59]\n\tv_mfma_f32_16x16x32_bf16 v[68:71], v[128:131], v[184:187], v[68:71]\n\tv_mfma_f32_16x16x32_bf16 v[80:83], v[132:135], v[184:187], v[80:83]\n\tv_mfma_f32_16x16x32_bf16 v[92:95], v[136:139], v[184:187], v[92:95]\n\tv_mfma_f32_16x16x32_bf16 v[104:107], v[140:143], v[184:187], v[104:107]\n\tv_mfma_f32_16x16x32_bf16 v[116:119], v[144:147], v[184:187], v[116:119]\n\t"
            "s_waitcnt lgkmcnt(10)\n\tv_mfma_f32_16x16x32_bf16 v[36:39], v[188:191], v[148:151], v[36:39]\n\ts_waitcnt lgkmcnt(9)\n\tv_mfma_f32_16x16x32_bf16 v[48:51], v[188:191], v[152:155], v[48:51]\n\ts_waitcnt lgkmcnt(8)\n\tv_mfma_f32_16x16x32_bf16 v[60:63], v[188:191], v[156:159], v[60:63]\n\t"
            "s_waitcnt lgkmcnt(7)\n\tv_mfma_f32_16x16x32_bf16 v[72:75], v[188:191], v[160:163], v[72:75]\n\ts_waitcnt lgkmcnt(6)\n\tv_mfma_f32_16x16x32_bf16 v[84:87], v[188:191], v[164:167], v[84:87]\n\ts_waitcnt lgkmcnt(5)\n\tv_mfma_f32_16x16x32_bf16 v[96:99], v[188:191], v[168:171], v[96:99]\n\t"
            "s_waitcnt lgkmcnt(4)\n\tv_mfma_f32_16x16x32_bf16 v[108:111], v[188:191], v[172:175], v[108:111]\n\ts_waitcnt lgkmcnt(2)\n\tv_mfma_f32_16x16x32_bf16 v[40:43], v[192:195], v[148:151], v[40:43]\n\tv_mfma_f32_16x16x32_bf16 v[52:55], v[192:195], v[152:155], v[52:55]\n\tv_mfma_f32_16x16x32_bf16 v[64:67], v[192:195], v[156:159], v[64:67]\n\t"
            "v_mfma_f32_16x16x32_bf16 v[76:79], v[192:195], v[160:163], v[76:79]\n\ts_waitcnt lgkmcnt(0)\n\ts_barrier\n\t"
            ST(7)
            "ds_read_b64_tr_b16 v[176:177], v13 offset:38912\n\tds_read_b64_tr_b16 v[178:179], v13 offset:39424\n\tv_mfma_f32_16x16x32_bf16 v[88:91], v[192:195], v[164:167], v[88:91]\n\tds_read_b128 v[120:123], v10\n\tv_mfma_f32_16x16x32_bf16 v[100:103], v[192:195], v[168:171], v[100:103]\n\tds_read_b128 v[124:127], v4 offset:40960\n\t"
            "v_mfma_f32_16x16x32_bf16 v[112:115], v[192:195], v[172:175], v[112:115]\n\tds_read_b128 v[128:131], v4 offset:43008\n\tv_mfma_f32_16x16x32_bf16 v[44:47], v[148:151], v[196:199], v[44:47]\n\tds_read_b128 v[132:135], v4 offset:45056\n\tv_mfma_f32_16x16x32_bf16 v[56:59], v[152:155], v[196:199], v[56:59]\n\tds_read_b128 v[136:139], v4 offset:47104\n\t"
            "v_mfma_f32_16x16x32_bf16 v[68:71], v[156:159], v[196:199], v[68:71]\n\tds_read_b128 v[140:143], v4 offset:49152\n\tv_mfma_f32_16x16x32_bf16 v[80:83], v[160:163], v[196:199], v[80:83]\n\tds_read_b128 v[144:147], v4 offset:51200\n\tv_mfma_f32_16x16x32_bf16 v[92:95], v[164:167], v[196:199], v[92:95]\n\tds_read_b64_tr_b16 v[180:181], v13 offset:39936\n\t"
            "ds_read_b64_tr_b16 v[182:183], v13 offset:40448\n\tv_mfma_f32_16x16x32_bf16 v[104:107], v[168:171], v[196:199], v[104:107]\n\tds_read_b64_tr_b16 v[184:185], v13 offset:40960\n\tds_read_b64_tr_b16 v[186:187], v13 offset:41472\n\tv_mfma_f32_16x16x32_bf16 v[116:119], v[172:175], v[196:199], v[116:119]\n\ts_waitcnt lgkmcnt(10)\n\t"
            "v_mfma_f32_16x16x32_bf16 v[36:39], v[176:179], v[120:123], v[36:39]\n\ts_waitcnt lgkmcnt(9)\n\tv_mfma_f32_16x16x32_bf16 v[48:51], v[176:179], v[124:127], v[48:51]\n\ts_waitcnt lgkmcnt(8)\n\tv_mfma_f32_16x16x32_bf16 v[60:63], v[176:179], v[128:131], v[60:63]\n\tds_read_b64_tr_b16 v[188:189], v13 offset:51200\n\t"
            "ds_read_b64_tr_b16 v[190:191], v13 offset:51712\n\ts_waitcnt lgkmcnt(9)\n\tv_mfma_f32_16x16x32_bf16 v[72:75], v[176:179], v[132:135], v[72:75]\n\tds_read_b128 v[148:151], v11\n\ts_waitcnt lgkmcnt(9)\n\tv_mfma_f32_16x16x32_bf16 v[84:87], v[176:179], v[136:139], v[84:87]\n\t"
            "ds_read_b128 v[152:155], v5 offset:40960\n\ts_waitcnt lgkmcnt(9)\n\tv_mfma_f32_16x16x32_bf16 v[96:99], v[176:179], v[140:143], v[96:99]\n\tds_read_b128 v[156:159], v5 offset:43008\n\ts_waitcnt lgkmcnt(9)\n\tv_mfma_f32_16x16x32_bf16 v[108:111], v[176:179], v[144:147], v[108:111]\n\t"
            "ds_read_b128 v[160:163], v5 offset:45056\n\ts_waitcnt lgkmcnt(8)\n\tv_mfma_f32_16x16x32_bf16 v[40:43], v[180:183], v[120:123], v[40:43]\n\tds_read_b128 v[164:167], v5 offset:47104\n\tv_mfma_f32_16x16x32_bf16 v[52:55], v[180:183], v[124:127], v[52:55]\n\tds_read_b128 v[168:171], v5 offset:49152\n\t"
            "v_mfma_f32_16x16x32_bf16 v[64:67], v[180:183], v[128:131], v[64:67]\n\tds_read_b128 v[172:175], v5 offset:51200\n\tv_mfma_f32_16x16x32_bf16 v[76:79], v[180:183], v[132:135], v[76:79]\n\tds_read_b64_tr_b16 v[192:193], v13 offset:52224\n\tds_read_b64_tr_b16 v[194:195], v13 offset:52736\n\tv_mfma_f32_16x16x32_bf16 v[88:91], v[180:183], v[136:139], v[88:91]\n\t"
            "ds_read_b64_tr_b16 v[196:197], v13 offset:53248\n\tds_read_b64_tr_b16 v[198:199], v13 offset:53760\n\tv_mfma_f32_16x16x32_bf16 v[100:103], v[180:183], v[140:143], v[100:103]\n\tv_mfma_f32_16x16x32_bf16 v[112:115], v[180:183], v[144:147], v[112:115]\n\ts_waitcnt lgkmcnt(13)\n\tv_mfma_f32_16x16x32_bf16 v[44:47], v[120:123], v[184:187], v[44:47]\n\t"
            "v_mfma_f32_16x16x32_bf16 v[56:59], v[124:127], v[184:187], v[56:59]\n\tv_mfma_f32_16x16x32_bf16 v[68:71], v[128:131], v[184:187], v[68:71]\n\tv_mfma_f32_16x16x32_bf16 v[80:83], v[132:135], v[184:187], v[80:83]\n\tv_mfma_f32_16x16x32_bf16 v[92:95], v[136:139], v[184:187], v[92:95]\n\tv_mfma_f32_16x16x32_bf16 v[104:107], v[140:143], v[184:187], v[104:107]\n\tv_mfma_f32_16x16x32_bf16 v[116:119], v[144:147], v[184:187], v[116:119]\n\t"
            "s_waitcnt lgkmcnt(10)\n\tv_mfma_f32_16x16x32_bf16 v[36:39], v[188:191], v[148:151], v[36:39]\n\ts_waitcnt lgkmcnt(9)\n\tv_mfma_f32_16x16x32_bf16 v[48:51], v[188:191], v[152:155], v[48:51]\n\ts_waitcnt lgkmcnt(8)\n\tv_mfma_f32_16x16x32_bf16 v[60:63], v[188:191], v[156:159], v[60:63]\n\t"
            "s_waitcnt lgkmcnt(7)\n\tv_mfma_f32_16x16x32_bf16 v[72:75], v[188:191], v[160:163], v[72:75]\n\ts_waitcnt lgkmcnt(6)\n\tv_mfma_f32_16x16x32_bf16 v[84:87], v[188:191], v[164:167], v[84:87]\n\ts_waitcnt lgkmcnt(5)\n\tv_mfma_f32_16x16x32_bf16 v[96:99], v[188:191], v[168:171], v[96:99]\n\t"
            "s_waitcnt lgkmcnt(4)\n\tv_mfma_f32_16x16x32_bf16 v[108:111], v[188:191], v[172:175], v[108:111]\n\ts_waitcnt lgkmcnt(2)\n\tv_mfma_f32_16x16x32_bf16 v[40:43], v[192:195], v[148:151], v[40:43]\n\tv_mfma_f32_16x16x32_bf16 v[52:55], v[192:195], v[152:155], v[52:55]\n\tv_mfma_f32_16x16x32_bf16 v[64:67], v[192:195], v[156:159], v[64:67]\n\t"
            "v_mfma_f32_16x16x32_bf16 v[76:79], v[192:195], v[160:163], v[76:79]\n\ts_waitcnt lgkmcnt(0)\n\ts_barrier\n\t"
            ST(9)
            "ds_read_b64_tr_b16 v[176:177], v12 offset:0\n\tds_read_b64_tr_b16 v[178:179], v12 offset:512\n\tv_mfma_f32_16x16x32_bf16 v[88:91], v[192:195], v[164:167], v[88:91]\n\tds_read_b128 v[120:123], v6\n\tv_mfma_f32_16x16x32_bf16 v[100:103], v[192:195], v[168:171], v[100:103]\n\tds_read_b128 v[124:127], v2 offset:2048\n\t"
            "v_mfma_f32_16x16x32_bf16 v[112:115], v[192:195], v[172:175], v[112:115]\n\tds_read_b128 v[128:131], v2 offset:4096\n\tv_mfma_f32_16x16x32_bf16 v[44:47], v[148:151], v[196:199], v[44:47]\n\tds_read_b128 v[132:135], v2 offset:6144\n\tv_mfma_f32_16x16x32_bf16 v[56:59], v[152:155], v[196:199], v[56:59]\n\tds_read_b128 v[136:139], v2 offset:8192\n\t"
            "v_mfma_f32_16x16x32_bf16 v[68:71], v[156:159], v[196:199], v[68:71]\n\tds_read_b128 v[140:143], v2 offset:10240\n\tv_mfma_f32_16x16x32_bf16 v[80:83], v[160:163], v[196:199], v[80:83]\n\tds_read_b128 v[144:147], v2 offset:12288\n\tv_mfma_f32_16x16x32_bf16 v[92:95], v[164:167], v[196:199], v[92:95]\n\tds_read_b64_tr_b16 v[180:181], v12 offset:1024\n\t"
            "ds_read_b64_tr_b16 v[182:183], v12 offset:1536\n\tv_mfma_f32_16x16x32_bf16 v[104:107], v[168:171], v[196:199], v[104:107]\n\tds_read_b64_tr_b16 v[184:185], v12 offset:2048\n\tds_read_b64_tr_b16 v[186:187], v12 offset:2560\n\tv_mfma_f32_16x16x32_bf16 v[116:119], v[172:175], v[196:199], v[116:119]\n\ts_waitcnt lgkmcnt(10)\n\t"
            "v_mfma_f32_16x16x32_bf16 v[36:39], v[176:179], v[120:123], v[36:39]\n\ts_waitcnt lgkmcnt(9)\n\tv_mfma_f32_16x16x32_bf16 v[48:51], v[176:179], v[124:127], v[48:51]\n\ts_waitcnt lgkmcnt(8)\n\tv_mfma_f32_16x16x32_bf16 v[60:63], v[176:179], v[128:131], v[60:63]\n\tds_read_b64_tr_b16 v[188:189], v12 offset:12288\n\t"
            "ds_read_b64_tr_b16 v[190:191], v12 offset:12800\n\ts_waitcnt lgkmcnt(9)\n\tv_mfma_f32_16x16x32_bf16 v[72:75], v[176:179], v[132:135], v[72:75]\n\tds_read_b128 v[148:151], v7\n\ts_waitcnt lgkmcnt(9)\n\tv_mfma_f32_16x16x32_bf16 v[84:87], v[176:179], v[136:139], v[84:87]\n\t"
            "ds_read_b128 v[152:155], v3 offset:2048\n\ts_waitcnt lgkmcnt(9)\n\tv_mfma_f32_16x16x32_bf16 v[96:99], v[176:179], v[140:143], v[96:99]\n\tds_read_b128 v[156:159], v3 offset:4096\n\ts_waitcnt lgkmcnt(9)\n\tv_mfma_f32_16x16x32_bf16 v[108:111], v[176:179], v[144:147], v[108:111]\n\t"
            "ds_read_b128 v[160:163], v3 offset:6144\n\ts_waitcnt lgkmcnt(8)\n\tv_mfma_f32_16x16x32_bf16 v[40:43], v[180:183], v[120:123], v[40:43]\n\tds_read_b128 v[164:167], v3 offset:8192\n\tv_mfma_f32_16x16x32_bf16 v[52:55], v[180:183], v[124:127], v[52:55]\n\tds_read_b128 v[168:171], v3 offset:10240\n\t"
            "v_mfma_f32_16x16x32_bf16 v[64:67], v[180:183], v[128:131], v[64:67]\n\tds_read_b128 v[172:175], v3 offset:12288\n\tv_mfma_f32_16x16x32_bf16 v[76:79], v[180:183], v[132:135], v[76:79]\n\tds_read_b64_tr_b16 v[192:193], v12 offset:13312\n\tds_read_b64_tr_b16 v[194:195], v12 offset:13824\n\tv_mfma_f32_16x16x32_bf16 v[88:91], v[180:183], v[136:139], v[88:91]\n\t"
            "ds_read_b64_tr_b16 v[196:197], v12 offset:14336\n\tds_read_b64_tr_b16 v[198:199], v12 offset:14848\n\tv_mfma_f32_16x16x32_bf16 v[100:103], v[180:183], v[140:143], v[100:103]\n\tv_mfma_f32_16x16x32_bf16 v[112:115], v[180:183], v[144:147], v[112:115]\n\ts_waitcnt lgkmcnt(13)\n\tv_mfma_f32_16x16x32_bf16 v[44:47], v[120:123], v[184:187], v[44:47]\n\t"
            "v_mfma_f32_16x16x32_bf16 v[56:59], v[124:127], v[184:187], v[56:59]\n\tv_mfma_f32_16x16x32_bf16 v[68:71], v[128:131], v[184:187], v[68:71]\n\tv_mfma_f32_16x16x32_bf16 v[80:83], v[132:135], v[184:187], v[80:83]\n\tv_mfma_f32_16x16x32_bf16 v[92:95], v[136:139], v[184:187], v[92:95]\n\tv_mfma_f32_16x16x32_bf16 v[104:107], v[140:143], v[184:187], v[104:107]\n\tv_mfma_f32_16x16x32_bf16 v[116:119], v[144:147], v[184:187], v[116:119]\n\t"
            "s_waitcnt lgkmcnt(10)\n\tv_mfma_f32_16x16x32_bf16 v[36:39], v[188:191], v[148:151], v[36:39]\n\ts_waitcnt lgkmcnt(9)\n\tv_mfma_f32_16x16x32_bf16 v[48:51], v[188:191], v[152:155], v[48:51]\n\ts_waitcnt lgkmcnt(8)\n\tv_mfma_f32_16x16x32_bf16 v[60:63], v[188:191], v[156:159], v[60:63]\n\t"
            "s_waitcnt lgkmcnt(7)\n\tv_mfma_f32_16x16x32_bf16 v[72:75], v[188:191], v[160:163], v[72:75]\n\ts_waitcnt lgkmcnt(6)\n\tv_mfma_f32_16x16x32_bf16 v[84:87], v[188:191], v[164:167], v[84:87]\n\ts_waitcnt lgkmcnt(5)\n\tv_mfma_f32_16x16x32_bf16 v[96:99], v[188:191], v[168:171], v[96:99]\n\t"
            "s_waitcnt lgkmcnt(4)\n\tv_mfma_f32_16x16x32_bf16 v[108:111], v[188:191], v[172:175], v[108:111]\n\ts_waitcnt lgkmcnt(2)\n\tv_mfma_f32_16x16x32_bf16 v[40:43], v[192:195], v[148:151], v[40:43]\n\tv_mfma_f32_16x16x32_bf16 v[52:55], v[192:195], v[152:155], v[52:55]\n\tv_mfma_f32_16x16x32_bf16 v[64:67], v[192:195], v[156:159], v[64:67]\n\t"
            "v_mfma_f32_16x16x32_bf16 v[76:79], v[192:195], v[160:163], v[76:79]\n\ts_waitcnt lgkmcnt(0)\n\ts_barrier\n\t"
            ST(11)
            "ds_read_b64_tr_b16 v[176:177], v13 offset:0\n\tds_read_b64_tr_b16 v[178:179], v13 offset:512\n\tv_mfma_f32_16x16x32_bf16 v[88:91], v[192:195], v[164:167], v[88:91]\n\tds_read_b128 v[120:123], v8\n\tv_mfma_f32_16x16x32_bf16 v[100:103], v[192:195], v[168:171], v[100:103]\n\tds_read_b128 v[124:127], v4 offset:2048\n\t"
            "v_mfma_f32_16x16x32_bf16 v[112:115], v[192:195], v[172:175], v[112:115]\n\tds_read_b128 v[128:131], v4 offset:4096\n\tv_mfma_f32_16x16x32_bf16 v[44:47], v[148:151], v[196:199], v[44:47]\n\tds_read_b128 v[132:135], v4 offset:6144\n\tv_mfma_f32_16x16x32_bf16 v[56:59], v[152:155], v[196:199], v[56:59]\n\tds_read_b128 v[136:139], v4 offset:8192\n\t"
            "v_mfma_f32_16x16x32_bf16 v[68:71], v[156:159], v[196:199], v[68:71]\n\tds_read_b128 v[140:143], v4 offset:10240\n\tv_mfma_f32_16x16x32_bf16 v[80:83], v[160:163], v[196:199], v[80:83]\n\tds_read_b128 v[144:147], v4 offset:12288\n\tv_mfma_f32_16x16x32_bf16 v[92:95], v[164:167], v[196:199], v[92:95]\n\tds_read_b64_tr_b16 v[180:181], v13 offset:1024\n\t"
            "ds_read_b64_tr_b16 v[182:183], v13 offset:1536\n\tv_mfma_f32_16x16x32_bf16 v[104:107], v[168:171], v[196:199], v[104:107]\n\tds_read_b64_tr_b16 v[184:185], v13 offset:2048\n\tds_read_b64_tr_b16 v[186:187], v13 offset:2560\n\tv_mfma_f32_16x16x32_bf16 v[116:119], v[172:175], v[196:199], v[116:119]\n\ts_waitcnt lgkmcnt(10)\n\t"
            "v_mfma_f32_16x16x32_bf16 v[36:39], v[176:179], v[120:123], v[36:39]\n\ts_waitcnt lgkmcnt(9)\n\tv_mfma_f32_16x16x32_bf16 v[48:51], v[176:179], v[124:127], v[48:51]\n\ts_waitcnt lgkmcnt(8)\n\tv_mfma_f32_16x16x32_bf16 v[60:63], v[176:179], v[128:131], v[60:63]\n\tds_read_b64_tr_b16 v[188:189], v13 offset:12288\n\t"
            "ds_read_b64_tr_b16 v[190:191], v13 offset:12800\n\ts_waitcnt lgkmcnt(9)\n\tv_mfma_f32_16x16x32_bf16 v[72:75], v[176:179], v[132:135], v[72:75]\n\tds_read_b128 v[148:151], v9\n\ts_waitcnt lgkmcnt(9)\n\tv_mfma_f32_16x16x32_bf16 v[84:87], v[176:179], v[136:139], v[84:87]\n\t"
            "ds_read_b128 v[152:155], v5 offset:2048\n\ts_waitcnt lgkmcnt(9)\n\tv_mfma_f32_16x16x32_bf16 v[96:99], v[176:179], v[140:143], v[96:99]\n\tds_read_b128 v[156:159], v5 offset:4096\n\ts_waitcnt lgkmcnt(9)\n\tv_mfma_f32_16x16x32_bf16 v[108:111], v[176:179], v[144:147], v[108:111]\n\t"
            "ds_read_b128 v[160:163], v5 offset:6144\n\ts_waitcnt lgkmcnt(8)\n\tv_mfma_f32_16x16x32_bf16 v[40:43], v[180:183], v[120:123], v[40:43]\n\tds_read_b128 v[164:167], v5 offset:8192\n\tv_mfma_f32_16x16x32_bf16 v[52:55], v[180:183], v[124:127], v[52:55]\n\tds_read_b128 v[168:171], v5 offset:10240\n\t"
            "v_mfma_f32_16x16x32_bf16 v[64:67], v[180:183], v[128:131], v[64:67]\n\tds_read_b128 v[172:175], v5 offset:12288\n\tv_mfma_f32_16x16x32_bf16 v[76:79], v[180:183], v[132:135], v[76:79]\n\tds_read_b64_tr_b16 v[192:193], v13 offset:13312\n\tds_read_b64_tr_b16 v[194:195], v13 offset:13824\n\tv_mfma_f32_16x16x32_bf16 v[88:91], v[180:183], v[136:139], v[88:91]\n\t"
            "ds_read_b64_tr_b16 v[196:197], v13 offset:14336\n\tds_read_b64_tr_b16 v[198:199], v13 offset:14848\n\tv_mfma_f32_16x16x32_bf16 v[100:103], v[180:183], v[140:143], v[100:103]\n\tv_mfma_f32_16x16x32_bf16 v[112:115], v[180:183], v[144:147], v[112:115]\n\ts_waitcnt lgkmcnt(13)\n\tv_mfma_f32_16x16x32_bf16 v[44:47], v[120:123], v[184:187], v[44:47]\n\t"
            "v_mfma_f32_16x16x32_bf16 v[56:59], v[124:127], v[184:187], v[56:59]\n\tv_mfma_f32_16x16x32_bf16 v[68:71], v[128:131], v[184:187], v[68:71]\n\tv_mfma_f32_16x16x32_bf16 v[80:83], v[132:135], v[184:187], v[80:83]\n\tv_mfma_f32_16x16x32_bf16 v[92:95], v[136:139], v[184:187], v[92:95]\n\tv_mfma_f32_16x16x32_bf16 v[104:107], v[140:143], v[184:187], v[104:107]\n\tv_mfma_f32_16x16x32_bf16 v[116:119], v[144:147], v[184:187], v[116:119]\n\t"
            "s_waitcnt lgkmcnt(10)\n\tv_mfma_f32_16x16x32_bf16 v[36:39], v[188:191], v[148:151], v[36:39]\n\ts_waitcnt lgkmcnt(9)\n\tv_mfma_f32_16x16x32_bf16 v[48:51], v[188:191], v[152:155], v[48:51]\n\ts_waitcnt lgkmcnt(8)\n\tv_mfma_f32_16x16x32_bf16 v[60:63], v[188:191], v[156:159], v[60:63]\n\t"
            "s_waitcnt lgkmcnt(7)\n\tv_mfma_f32_16x16x32_bf16 v[72:75], v[188:191], v[160:163], v[72:75]\n\ts_waitcnt lgkmcnt(6)\n\tv_mfma_f32_16x16x32_bf16 v[84:87], v[188:191], v[164:167], v[84:87]\n\ts_waitcnt lgkmcnt(5)\n\tv_mfma_f32_16x16x32_bf16 v[96:99], v[188:191], v[168:171], v[96:99]\n\t"
            "s_waitcnt lgkmcnt(4)\n\tv_mfma_f32_16x16x32_bf16 v[108:111], v[188:191], v[172:175], v[108:111]\n\ts_waitcnt lgkmcnt(2)\n\tv_mfma_f32_16x16x32_bf16 v[40:43], v[192:195], v[148:151], v[40:43]\n\tv_mfma_f32_16x16x32_bf16 v[52:55], v[192:195], v[152:155], v[52:55]\n\tv_mfma_f32_16x16x32_bf16 v[64:67], v[192:195], v[156:159], v[64:67]\n\t"
            "v_mfma_f32_16x16x32_bf16 v[76:79], v[192:195], v[160:163], v[76:79]\n\ts_waitcnt lgkmcnt(0)\n\ts_barrier\n\t"
            ST(13)
            "ds_read_b64_tr_b16 v[176:177], v13 offset:38912\n\tds_read_b64_tr_b16 v[178:179], v13 offset:39424\n\tv_mfma_f32_16x16x32_bf16 v[88:91], v[192:195], v[164:167], v[88:91]\n\tds_read_b128 v[120:123], v10\n\tv_mfma_f32_16x16x32_bf16 v[100:103], v[192:195], v[168:171], v[100:103]\n\tds_read_b128 v[124:127], v4 offset:40960\n\t"
            "v_mfma_f32_16x16x32_bf16 v[112:115], v[192:195], v[172:175], v[112:115]\n\tds_read_b128 v[128:131], v4 offset:43008\n\tv_mfma_f32_16x16x32_bf16 v[44:47], v[148:151], v[196:199], v[44:47]\n\tds_read_b128 v[132:135], v4 offset:45056\n\tv_mfma_f32_16x16x32_bf16 v[56:59], v[152:155], v[196:199], v[56:59]\n\tds_read_b128 v[136:139], v4 offset:47104\n\t"
            "v_mfma_f32_16x16x32_bf16 v[68:71], v[156:159], v[196:199], v[68:71]\n\tds_read_b128 v[140:143], v4 offset:49152\n\tv_mfma_f32_16x16x32_bf16 v[80:83], v[160:163], v[196:199], v[80:83]\n\tds_read_b128 v[144:147], v4 offset:51200\n\tv_mfma_f32_16x16x32_bf16 v[92:95], v[164:167], v[196:199], v[92:95]\n\tds_read_b64_tr_b16 v[180:181], v13 offset:39936\n\t"
            "ds_read_b64_tr_b16 v[182:183], v13 offset:40448\n\tv_mfma_f32_16x16x32_bf16 v[104:107], v[168:171], v[196:199], v[104:107]\n\tds_read_b64_tr_b16 v[184:185], v13 offset:40960\n\tds_read_b64_tr_b16 v[186:187], v13 offset:41472\n\tv_mfma_f32_16x16x32_bf16 v[116:119], v[172:175], v[196:199], v[116:119]\n\ts_waitcnt lgkmcnt(10)\n\t"
            "v_mfma_f32_16x16x32_bf16 v[36:39], v[176:179], v[120:123], v[36:39]\n\ts_waitcnt lgkmcnt(9)\n\tv_mfma_f32_16x16x32_bf16 v[48:51], v[176:179], v[124:127], v[48:51]\n\ts_waitcnt lgkmcnt(8)\n\tv_mfma_f32_16x16x32_bf16 v[60:63], v[176:179], v[128:131], v[60:63]\n\tds_read_b64_tr_b16 v[188:189], v13 offset:51200\n\t"
            "ds_read_b64_tr_b16 v[190:191], v13 offset:51712\n\ts_waitcnt lgkmcnt(9)\n\tv_mfma_f32_16x16x32_bf16 v[72:75], v[176:179], v[132:135], v[72:75]\n\tds_read_b128 v[148:151], v11\n\ts_waitcnt lgkmcnt(9)\n\tv_mfma_f32_16x16x32_bf16 v[84:87], v[176:179], v[136:139], v[84:87]\n\t"
            "ds_read_b128 v[152:155], v5 offset:40960\n\ts_waitcnt lgkmcnt(9)\n\tv_mfma_f32_16x16x32_bf16 v[96:99], v[176:179], v[140:143], v[96:99]\n\tds_read_b128 v[156:159], v5 offset:43008\n\ts_waitcnt lgkmcnt(9)\n\tv_mfma_f32_16x16x32_bf16 v[108:111], v[176:179], v[144:147], v[108:111]\n\t"
            "ds_read_b128 v[160:163], v5 offset:45056\n\ts_waitcnt lgkmcnt(8)\n\tv_mfma_f32_16x16x32_bf16 v[40:43], v[180:183], v[120:123], v[40:43]\n\tds_read_b128 v[164:167], v5 offset:47104\n\tv_mfma_f32_16x16x32_bf16 v[52:55], v[180:183], v[124:127], v[52:55]\n\tds_read_b128 v[168:171], v5 offset:49152\n\t"
            "v_mfma_f32_16x16x32_bf16 v[64:67], v[180:183], v[128:131], v[64:67]\n\tds_read_b128 v[172:175], v5 offset:51200\n\tv_mfma_f32_16x16x32_bf16 v[76:79], v[180:183], v[132:135], v[76:79]\n\tds_read_b64_tr_b16 v[192:193], v13 offset:52224\n\tds_read_b64_tr_b16 v[194:195], v13 offset:52736\n\tv_mfma_f32_16x16x32_bf16 v[88:91], v[180:183], v[136:139], v[88:91]\n\t"
            "ds_read_b64_tr_b16 v[196:197], v13 offset:53248\n\tds_read_b64_tr_b16 v[198:199], v13 offset:53760\n\tv_mfma_f32_16x16x32_bf16 v[100:103], v[180:183], v[140:143], v[100:103]\n\tv_mfma_f32_16x16x32_bf16 v[112:115], v[180:183], v[144:147], v[112:115]\n\ts_waitcnt lgkmcnt(13)\n\tv_mfma_f32_16x16x32_bf16 v[44:47], v[120:123], v[184:187], v[44:47]\n\t"
            "v_mfma_f32_16x16x32_bf16 v[56:59], v[124:127], v[184:187], v[56:59]\n\tv_mfma_f32_16x16x32_bf16 v[68:71], v[128:131], v[184:187], v[68:71]\n\tv_mfma_f32_16x16x32_bf16 v[80:83], v[132:135], v[184:187], v[80:83]\n\tv_mfma_f32_16x16x32_bf16 v[92:95], v[136:139], v[184:187], v[92:95]\n\tv_mfma_f32_16x16x32_bf16 v[104:107], v[140:143], v[184:187], v[104:107]\n\tv_mfma_f32_16x16x32_bf16 v[116:119], v[144:147], v[184:187], v[116:119]\n\t"
            "s_waitcnt lgkmcnt(10)\n\tv_mfma_f32_16x16x32_bf16 v[36:39], v[188:191], v[148:151], v[36:39]\n\ts_waitcnt lgkmcnt(9)\n\tv_mfma_f32_16x16x32_bf16 v[48:51], v[188:191], v[152:155], v[48:51]\n\ts_waitcnt lgkmcnt(8)\n\tv_mfma_f32_16x16x32_bf16 v[60:63], v[188:191], v[156:159], v[60:63]\n\t"
            "s_waitcnt lgkmcnt(7)\n\tv_mfma_f32_16x16x32_bf16 v[72:75], v[188:191], v[160:163], v[72:75]\n\ts_waitcnt lgkmcnt(6)\n\tv_mfma_f32_16x16x32_bf16 v[84:87], v[188:191], v[164:167], v[84:87]\n\ts_waitcnt lgkmcnt(5)\n\tv_mfma_f32_16x16x32_bf16 v[96:99], v[188:191], v[168:171], v[96:99]\n\t"
            "s_waitcnt lgkmcnt(4)\n\tv_mfma_f32_16x16x32_bf16 v[108:111], v[188:191], v[172:175], v[108:111]\n\ts_waitcnt lgkmcnt(2)\n\tv_mfma_f32_16x16x32_bf16 v[40:43], v[192:195], v[148:151], v[40:43]\n\tv_mfma_f32_16x16x32_bf16 v[52:55], v[192:195], v[152:155], v[52:55]\n\tv_mfma_f32_16x16x32_bf16 v[64:67], v[192:195], v[156:159], v[64:67]\n\t"
            "v_mfma_f32_16x16x32_bf16 v[76:79], v[192:195], v[160:163], v[76:79]\n\ts_waitcnt lgkmcnt(0)\n\ts_barrier\n\t"
            ST(15)
            "ds_read_b64_tr_b16 v[176:177], v12 offset:0\n\tds_read_b64_tr_b16 v[178:179], v12 offset:512\n\tv_mfma_f32_16x16x32_bf16 v[88:91], v[192:195], v[164:167], v[88:91]\n\tds_read_b128 v[120:123], v6\n\tv_mfma_f32_16x16x32_bf16 v[100:103], v[192:195], v[168:171], v[100:103]\n\tds_read_b128 v[124:127], v2 offset:2048\n\t"
            "v_mfma_f32_16x16x32_bf16 v[112:115], v[192:195], v[172:175], v[112:115]\n\tds_read_b128 v[128:131], v2 offset:4096\n\tv_mfma_f32_16x16x32_bf16 v[44:47], v[148:151], v[196:199], v[44:47]\n\tds_read_b128 v[132:135], v2 offset:6144\n\tv_mfma_f32_16x16x32_bf16 v[56:59], v[152:155], v[196:199], v[56:59]\n\tds_read_b128 v[136:139], v2 offset:8192\n\t"
            "v_mfma_f32_16x16x32_bf16 v[68:71], v[156:159], v[196:199], v[68:71]\n\tds_read_b128 v[140:143], v2 offset:10240\n\tv_mfma_f32_16x16x32_bf16 v[80:83], v[160:163], v[196:199], v[80:83]\n\tds_read_b128 v[144:147], v2 offset:12288\n\tv_mfma_f32_16x16x32_bf16 v[92:95], v[164:167], v[196:199], v[92:95]\n\tds_read_b64_tr_b16 v[180:181], v12 offset:1024\n\t"
            "ds_read_b64_tr_b16 v[182:183], v12 offset:1536\n\tv_mfma_f32_16x16x32_bf16 v[104:107], v[168:171], v[196:199], v[104:107]\n\tv_mfma_f32_16x16x32_bf16 v[116:119], v[172:175], v[196:199], v[116:119]\n\ts_waitcnt lgkmcnt(8)\n\tv_mfma_f32_16x16x32_bf16 v[36:39], v[176:179], v[120:123], v[36:39]\n\ts_waitcnt lgkmcnt(7)\n\t"
            "v_mfma_f32_16x16x32_bf16 v[48:51], v[176:179], v[124:127], v[48:51]\n\ts_waitcnt lgkmcnt(6)\n\tv_mfma_f32_16x16x32_bf16 v[60:63], v[176:179], v[128:131], v[60:63]\n\tds_read_b64_tr_b16 v[188:189], v12 offset:12288\n\tds_read_b64_tr_b16 v[190:191], v12 offset:12800\n\ts_waitcnt lgkmcnt(7)\n\t"
            "v_mfma_f32_16x16x32_bf16 v[72:75], v[176:179], v[132:135], v[72:75]\n\tds_read_b128 v[148:151], v7\n\ts_waitcnt lgkmcnt(7)\n\tv_mfma_f32_16x16x32_bf16 v[84:87], v[176:179], v[136:139], v[84:87]\n\tds_read_b128 v[152:155], v3 offset:2048\n\ts_waitcnt lgkmcnt(7)\n\t"
            "v_mfma_f32_16x16x32_bf16 v[96:99], v[176:179], v[140:143], v[96:99]\n\tds_read_b128 v[156:159], v3 offset:4096\n\ts_waitcnt lgkmcnt(7)\n\tv_mfma_f32_16x16x32_bf16 v[108:111], v[176:179], v[144:147], v[108:111]\n\tds_read_b128 v[160:163], v3 offset:6144\n\ts_waitcnt lgkmcnt(6)\n\t"
            "v_mfma_f32_16x16x32_bf16 v[40:43], v[180:183], v[120:123], v[40:43]\n\tds_read_b128 v[164:167], v3 offset:8192\n\tv_mfma_f32_16x16x32_bf16 v[52:55], v[180:183], v[124:127], v[52:55]\n\tds_read_b128 v[168:171], v3 offset:10240\n\tv_mfma_f32_16x16x32_bf16 v[64:67], v[180:183], v[128:131], v[64:67]\n\tds_read_b128 v[172:175], v3 offset:12288\n\t"
            "v_mfma_f32_16x16x32_bf16 v[76:79], v[180:183], v[132:135], v[76:79]\n\tds_read_b64_tr_b16 v[192:193], v12 offset:13312\n\tds_read_b64_tr_b16 v[194:195], v12 offset:13824\n\tv_mfma_f32_16x16x32_bf16 v[88:91], v[180:183], v[136:139], v[88:91]\n\tv_mfma_f32_16x16x32_bf16 v[100:103], v[180:183], v[140:143], v[100:103]\n\tv_mfma_f32_16x16x32_bf16 v[112:115], v[180:183], v[144:147], v[112:115]\n\t"
            "s_waitcnt lgkmcnt(8)\n\tv_mfma_f32_16x16x32_bf16 v[36:39], v[188:191], v[148:151], v[36:39]\n\ts_waitcnt lgkmcnt(7)\n\tv_mfma_f32_16x16x32_bf16 v[48:51], v[188:191], v[152:155], v[48:51]\n\ts_waitcnt lgkmcnt(6)\n\tv_mfma_f32_16x16x32_bf16 v[60:63], v[188:191], v[156:159], v[60:63]\n\t"
            "s_waitcnt lgkmcnt(5)\n\tv_mfma_f32_16x16x32_bf16 v[72:75], v[188:191], v[160:163], v[72:75]\n\ts_waitcnt lgkmcnt(0)\n\ts_barrier\n\t"
            ST(17)
            "ds_read_b64_tr_b16 v[176:177], v13 offset:0\n\tds_read_b64_tr_b16 v[178:179], v13 offset:512\n\tv_mfma_f32_16x16x32_bf16 v[84:87], v[188:191], v[164:167], v[84:87]\n\tds_read_b128 v[120:123], v8\n\tv_mfma_f32_16x16x32_bf16 v[96:99], v[188:191], v[168:171], v[96:99]\n\tds_read_b128 v[124:127], v4 offset:2048\n\t"
            "v_mfma_f32_16x16x32_bf16 v[108:111], v[188:191], v[172:175], v[108:111]\n\tds_read_b128 v[128:131], v4 offset:4096\n\tv_mfma_f32_16x16x32_bf16 v[40:43], v[192:195], v[148:151], v[40:43]\n\tds_read_b128 v[132:135], v4 offset:6144\n\tv_mfma_f32_16x16x32_bf16 v[52:55], v[192:195], v[152:155], v[52:55]\n\tds_read_b128 v[136:139], v4 offset:8192\n\t"
            "v_mfma_f32_16x16x32_bf16 v[64:67], v[192:195], v[156:159], v[64:67]\n\tds_read_b128 v[140:143], v4 offset:10240\n\tv_mfma_f32_16x16x32_bf16 v[76:79], v[192:195], v[160:163], v[76:79]\n\tds_read_b128 v[144:147], v4 offset:12288\n\tv_mfma_f32_16x16x32_bf16 v[88:91], v[192:195], v[164:167], v[88:91]\n\tds_read_b64_tr_b16 v[180:181], v13 offset:1024\n\t"
            "ds_read_b64_tr_b16 v[182:183], v13 offset:1536\n\tv_mfma_f32_16x16x32_bf16 v[100:103], v[192:195], v[168:171], v[100:103]\n\tds_read_b64_tr_b16 v[184:185], v13 offset:2048\n\tds_read_b64_tr_b16 v[186:187], v13 offset:2560\n\tv_mfma_f32_16x16x32_bf16 v[112:115], v[192:195], v[172:175], v[112:115]\n\tds_read2_b32 v[208:209], v14 offset1:16\n\t"
            SA(2) PRIA
            "s_mov_b64 vcc, s[18:19]\n\ts_bitcmp1_b32 9, s69\n\tds_read_b64_tr_b16 v[188:189], v13 offset:12288\n\tv_mov_b32 v252, -1\n\tds_read_b64_tr_b16 v[190:191], v13 offset:12800\n\tv_mov_b32 v254, -1\n\t"
            "ds_read_b128 v[148:151], v9\n\ts_waitcnt lgkmcnt(6)\n\tv_mfma_f32_16x16x32_bf16 v[40:43], v[180:183], v[120:123], v[40:43]\n\tds_read_b128 v[152:155], v5 offset:2048\n\tv_mfma_f32_16x16x32_bf16 v[36:39], v[176:179], v[120:123], v[36:39]\n\tds_read_b128 v[156:159], v5 offset:4096\n\t"
            "v_mfma_f32_16x16x32_bf16 v[52:55], v[180:183], v[124:127], v[52:55]\n\tds_read_b128 v[160:163], v5 offset:6144\n\tv_mfma_f32_16x16x32_bf16 v[48:51], v[176:179], v[124:127], v[48:51]\n\tds_read_b128 v[164:167], v5 offset:8192\n\tv_mfma_f32_16x16x32_bf16 v[64:67], v[180:183], v[128:131], v[64:67]\n\tds_read_b128 v[168:171], v5 offset:10240\n\t"
            "v_mfma_f32_16x16x32_bf16 v[60:63], v[176:179], v[128:131], v[60:63]\n\tds_read_b128 v[172:175], v5 offset:12288\n\tv_mfma_f32_16x16x32_bf16 v[76:79], v[180:183], v[132:135], v[76:79]\n\tds_read_b64_tr_b16 v[192:193], v13 offset:13312\n\ts_waitcnt lgkmcnt(7)\n\tv_mfma_f32_16x16x32_bf16 v[36:39], v[188:191], v[148:151], v[36:39]\n\t"
            "ds_read_b64_tr_b16 v[194:195], v13 offset:13824\n\tds_read_b64_tr_b16 v[196:197], v13 offset:14336\n\ts_waitcnt lgkmcnt(8)\n\tv_mfma_f32_16x16x32_bf16 v[48:51], v[188:191], v[152:155], v[48:51]\n\tds_read_b64_tr_b16 v[198:199], v13 offset:14848\n\tds_read2_b32 v[210:211], v14 offset0:32 offset1:48\n\t"
            "s_waitcnt lgkmcnt(9)\n\tv_mfma_f32_16x16x32_bf16 v[60:63], v[188:191], v[156:159], v[60:63]\n\tv_mov_b32_dpp v200, v36 row_newbcast:0 row_mask:0xf bank_mask:0xf bound_ctrl:1\n\tds_read2_b32 v[212:213], v14 offset0:64 offset1:80\n\tv_mfma_f32_16x16x32_bf16 v[72:75], v[176:179], v[132:135], v[72:75]\n\tv_mov_b32_dpp v201, v37 row_newbcast:0 row_mask:0xf bank_mask:0xf bound_ctrl:1\n\t"
            "ds_read_b32 v214, v14 offset:384\n\ts_waitcnt lgkmcnt(8)\n\tv_mfma_f32_16x16x32_bf16 v[72:75], v[188:191], v[160:163], v[72:75]\n\tv_mov_b32_dpp v202, v38 row_newbcast:0 row_mask:0xf bank_mask:0xf bound_ctrl:1\n\tv_mov_b32_dpp v203, v39 row_newbcast:0 row_mask:0xf bank_mask:0xf bound_ctrl:1\n\tv_mfma_f32_16x16x32_bf16 v[88:91], v[180:183], v[136:139], v[88:91]\n\t"
            "v_fmac_f32 v36, v208, v200\n\tv_fmac_f32 v37, v208, v201\n\tv_mfma_f32_16x16x32_bf16 v[84:87], v[176:179], v[136:139], v[84:87]\n\tv_fmac_f32 v38, v208, v202\n\tv_fmac_f32 v39, v208, v203\n\ts_waitcnt lgkmcnt(4)\n\t"
            "v_mfma_f32_16x16x32_bf16 v[40:43], v[192:195], v[148:151], v[40:43]\n\tv_cvt_pk_bf16_f32 v250, v36, v37\n\tv_cvt_pk_bf16_f32 v251, v38, v39\n\tv_mfma_f32_16x16x32_bf16 v[52:55], v[192:195], v[152:155], v[52:55]\n\tv_and_b32 v36, v250, v18\n\tv_and_b32 v37, v251, v18\n\t"
            "v_mfma_f32_16x16x32_bf16 v[64:67], v[192:195], v[156:159], v[64:67]\n\ts_nop 1\n\tv_mov_b32_dpp v204, v40 row_newbcast:0 row_mask:0xf bank_mask:0xf bound_ctrl:1\n\tv_mov_b32_dpp v205, v41 row_newbcast:0 row_mask:0xf bank_mask:0xf bound_ctrl:1\n\tv_mfma_f32_16x16x32_bf16 v[76:79], v[192:195], v[160:163], v[76:79]\n\tv_mov_b32_dpp v206, v42 row_newbcast:0 row_mask:0xf bank_mask:0xf bound_ctrl:1\n\t"
            "v_mov_b32_dpp v207, v43 row_newbcast:0 row_mask:0xf bank_mask:0xf bound_ctrl:1\n\tv_mfma_f32_16x16x32_bf16 v[88:91], v[192:195], v[164:167], v[88:91]\n\tv_fmac_f32 v40, v208, v204\n\tv_fmac_f32 v41, v208, v205\n\tv_mfma_f32_16x16x32_bf16 v[84:87], v[188:191], v[164:167], v[84:87]\n\tv_fmac_f32 v42, v208, v206\n\t"
            "v_fmac_f32 v43, v208, v207\n\tv_mfma_f32_16x16x32_bf16 v[100:103], v[180:183], v[140:143], v[100:103]\n\tv_cvt_pk_bf16_f32 v40, v40, v41\n\tv_cvt_pk_bf16_f32 v41, v42, v43\n\tv_mfma_f32_16x16x32_bf16 v[96:99], v[176:179], v[140:143], v[96:99]\n\tv_cndmask_b32 v42, v40, v40, vcc\n\t"
            "v_cndmask_b32 v43, v41, v41, vcc\n\tv_mfma_f32_16x16x32_bf16 v[100:103], v[192:195], v[168:171], v[100:103]\n\tv_and_b32 v38, v250, v19\n\tv_and_b32 v39, v251, v19\n\tv_mfma_f32_16x16x32_bf16 v[96:99], v[188:191], v[168:171], v[96:99]\n\tv_and_b32 v222, v250, v20\n\t"
            "v_and_b32 v223, v251, v20\n\tv_mfma_f32_16x16x32_bf16 v[218:221], v[40:43], v[36:39], 0\n\tv_and_b32 v224, v250, v21\n\tv_and_b32 v225, v251, v21\n\tv_mfma_f32_16x16x32_bf16 v[112:115], v[180:183], v[144:147], v[112:115]\n\tv_fmac_f32 v52, v209, v204\n\t"
            "v_fmac_f32 v53, v209, v205\n\tv_mfma_f32_16x16x32_bf16 v[226:229], v[40:43], v[222:225], 0\n\ts_nop 1\n\tv_cndmask_b32 v36, v220, v218, s[14:15]\n\tv_cndmask_b32 v37, v221, v219, s[16:17]\n\tv_mfma_f32_16x16x32_bf16 v[108:111], v[176:179], v[144:147], v[108:111]\n\t"
            "v_fmac_f32 v54, v209, v206\n\tv_fmac_f32 v55, v209, v207\n\tv_mfma_f32_16x16x32_bf16 v[112:115], v[192:195], v[172:175], v[112:115]\n\tv_cndmask_b32 v38, v228, v226, s[14:15]\n\tv_cndmask_b32 v39, v229, v227, s[16:17]\n\tv_mfma_f32_16x16x32_bf16 v[108:111], v[188:191], v[172:175], v[108:111]\n\t"
            "v_fmac_f32 v48, v209, v200\n\tv_fmac_f32 v49, v209, v201\n\tv_fmac_f32 v50, v209, v202\n\tv_fmac_f32 v51, v209, v203\n\tv_cvt_pk_bf16_f32 v52, v52, v53\n\tv_cvt_pk_bf16_f32 v53, v54, v55\n\t"
            "v_cvt_pk_bf16_f32 v250, v48, v49\n\tv_cvt_pk_bf16_f32 v251, v50, v51\n\tv_cndmask_b32 v54, v40, v52, vcc\n\tv_cndmask_b32 v55, v41, v53, vcc\n\tv_and_b32 v48, v250, v18\n\tv_and_b32 v49, v251, v18\n\t"
            "v_and_b32 v50, v250, v19\n\tv_and_b32 v51, v251, v19\n\tv_and_b32 v234, v250, v20\n\tv_and_b32 v235, v251, v20\n\tv_mfma_f32_16x16x32_bf16 v[230:233], v[52:55], v[48:51], 0\n\tv_and_b32 v236, v250, v21\n\t"
            "v_and_b32 v237, v251, v21\n\ts_waitcnt lgkmcnt(0)\n\tv_fmac_f32 v64, v210, v204\n\tv_fmac_f32 v65, v210, v205\n\tv_mfma_f32_16x16x32_bf16 v[238:241], v[52:55], v[234:237], 0\n\tv_fmac_f32 v66, v210, v206\n\t"
            "s_nop 0\n\tv_cndmask_b32 v48, v232, v230, s[14:15]\n\tv_cndmask_b32 v49, v233, v231, s[16:17]\n\tv_fmac_f32 v67, v210, v207\n\tv_fmac_f32 v60, v210, v200\n\tv_fmac_f32 v61, v210, v201\n\t"
            "v_fmac_f32 v62, v210, v202\n\tv_cndmask_b32 v50, v240, v238, s[14:15]\n\tv_cndmask_b32 v51, v241, v239, s[16:17]\n\tv_fmac_f32 v63, v210, v203\n\tv_cvt_pk_bf16_f32 v64, v64, v65\n\tv_cvt_pk_bf16_f32 v65, v66, v67\n\t"
            "v_cvt_pk_bf16_f32 v250, v60, v61\n\tv_cvt_pk_bf16_f32 v251, v62, v63\n\tv_cndmask_b32 v66, v52, v64, vcc\n\tv_cndmask_b32 v67, v53, v65, vcc\n\tv_and_b32 v60, v250, v18\n\tv_and_b32 v61, v251, v18\n\t"
            "v_and_b32 v62, v250, v19\n\tv_and_b32 v63, v251, v19\n\tv_and_b32 v246, v250, v20\n\tv_and_b32 v247, v251, v20\n\tv_mfma_f32_16x16x32_bf16 v[242:245], v[64:67], v[60:63], 0\n\tv_and_b32 v248, v250, v21\n\t"
            "v_and_b32 v249, v251, v21\n\tv_fmac_f32 v76, v211, v204\n\tv_fmac_f32 v77, v211, v205\n\tv_mfma_f32_16x16x32_bf16 v[218:221], v[64:67], v[246:249], 0\n\tv_fmac_f32 v78, v211, v206\n\ts_nop 1\n\t"
            "v_cndmask_b32 v60, v244, v242, s[14:15]\n\tv_cndmask_b32 v61, v245, v243, s[16:17]\n\tv_fmac_f32 v79, v211, v207\n\tv_fmac_f32 v72, v211, v200\n\tv_fmac_f32 v73, v211, v201\n\tv_fmac_f32 v74, v211, v202\n\t"
            "v_cndmask_b32 v62, v220, v218, s[14:15]\n\tv_cndmask_b32 v63, v221, v219, s[16:17]\n\tv_fmac_f32 v75, v211, v203\n\tv_cvt_pk_bf16_f32 v76, v76, v77\n\tv_cvt_pk_bf16_f32 v77, v78, v79\n\tv_cvt_pk_bf16_f32 v250, v72, v73\n\t"
            "v_cvt_pk_bf16_f32 v251, v74, v75\n\tv_cndmask_b32 v78, v64, v76, vcc\n\tv_cndmask_b32 v79, v65, v77, vcc\n\tv_and_b32 v72, v250, v18\n\tv_and_b32 v73, v251, v18\n\tv_and_b32 v74, v250, v19\n\t"
            "v_and_b32 v75, v251, v19\n\tv_mfma_f32_16x16x32_bf16 v[44:47], v[120:123], v[184:187], v[44:47]\n\tds_read_b128 v[120:123], v6\n\tv_and_b32 v226, v250, v20\n\tv_mfma_f32_16x16x32_bf16 v[222:225], v[76:79], v[72:75], 0\n\tv_and_b32 v227, v251, v20\n\t"
            "v_and_b32 v228, v250, v21\n\tv_mfma_f32_16x16x32_bf16 v[56:59], v[124:127], v[184:187], v[56:59]\n\tds_read_b128 v[124:127], v2 offset:2048\n\tv_and_b32 v229, v251, v21\n\tv_mfma_f32_16x16x32_bf16 v[68:71], v[128:131], v[184:187], v[68:71]\n\tds_read_b128 v[128:131], v2 offset:4096\n\t"
            "s_nop 0\n\tv_cndmask_b32 v72, v224, v222, s[14:15]\n\tv_mfma_f32_16x16x32_bf16 v[230:233], v[76:79], v[226:229], 0\n\tv_cndmask_b32 v73, v225, v223, s[16:17]\n\ts_cbranch_scc0 1f\n\tv_cvt_pk_bf16_f32 v253, v36, v37\n\t"
            "v_min_u32 v253, -2, v253\n\tv_cvt_pk_bf16_f32 v255, v48, v49\n\tv_min_u32 v255, -2, v255\n\tbuffer_store_dwordx4 v[252:255], v17, s[44:47], s20 offen sc1\n\ts_nop 1\n\tv_cvt_pk_bf16_f32 v253, v60, v61\n\t"
            "v_min_u32 v253, -2, v253\n\tv_cvt_pk_bf16_f32 v255, v72, v73\n\tv_min_u32 v255, -2, v255\n\tbuffer_store_dwordx4 v[252:255], v17, s[44:47], s21 offen sc1\n\t1:\nv_mfma_f32_16x16x32_bf16 v[80:83], v[132:135], v[184:187], v[80:83]\n\t"
            "ds_read_b128 v[132:135], v2 offset:6144\n\tv_fmac_f32 v88, v212, v204\n\tv_mfma_f32_16x16x32_bf16 v[92:95], v[136:139], v[184:187], v[92:95]\n\tds_read_b128 v[136:139], v2 offset:8192\n\ts_nop 0\n\tv_cndmask_b32 v74, v232, v230, s[14:15]\n\t"
            "v_mfma_f32_16x16x32_bf16 v[104:107], v[140:143], v[184:187], v[104:107]\n\tds_read_b128 v[140:143], v2 offset:10240\n\tv_cndmask_b32 v75, v233, v231, s[16:17]\n\tv_mfma_f32_16x16x32_bf16 v[116:119], v[144:147], v[184:187], v[116:119]\n\tds_read_b64_tr_b16 v[184:185], v12 offset:2048\n\tv_fmac_f32 v89, v212, v205\n\t"
            "ds_read_b64_tr_b16 v[186:187], v12 offset:2560\n\tv_mfma_f32_16x16x32_bf16 v[44:47], v[148:151], v[196:199], v[44:47]\n\tds_read_b128 v[148:151], v7\n\tv_fmac_f32 v90, v212, v206\n\tv_mfma_f32_16x16x32_bf16 v[56:59], v[152:155], v[196:199], v[56:59]\n\tds_write_b64 v28, v[36:37] offset:0\n\t"
            "v_fmac_f32 v91, v212, v207\n\tv_mfma_f32_16x16x32_bf16 v[68:71], v[156:159], v[196:199], v[68:71]\n\tv_fmac_f32 v84, v212, v200\n\tv_fmac_f32 v85, v212, v201\n\tds_write_b64 v29, v[38:39] offset:0\n\tv_mfma_f32_16x16x32_bf16 v[80:83], v[160:163], v[196:199], v[80:83]\n\t"
            "v_fmac_f32 v86, v212, v202\n\tv_fmac_f32 v87, v212, v203\n\tv_mfma_f32_16x16x32_bf16 v[92:95], v[164:167], v[196:199], v[92:95]\n\tds_write_b64 v28, v[48:49] offset:512\n\tv_cvt_pk_bf16_f32 v88, v88, v89\n\tv_mfma_f32_16x16x32_bf16 v[104:107], v[168:171], v[196:199], v[104:107]\n\t"
            "v_cvt_pk_bf16_f32 v89, v90, v91\n\tv_cvt_pk_bf16_f32 v250, v84, v85\n\tds_write_b64 v29, v[50:51] offset:512\n\tv_mfma_f32_16x16x32_bf16 v[116:119], v[172:175], v[196:199], v[116:119]\n\tv_cvt_pk_bf16_f32 v251, v86, v87\n\tv_cndmask_b32 v90, v76, v88, vcc\n\t"
            "s_waitcnt lgkmcnt(4)\n\tv_mfma_f32_16x16x32_bf16 v[44:47], v[120:123], v[184:187], v[44:47]\n\tds_write_b64 v28, v[60:61] offset:1024\n\tv_cndmask_b32 v91, v77, v89, vcc\n\tv_mfma_f32_16x16x32_bf16 v[56:59], v[124:127], v[184:187], v[56:59]\n\tv_and_b32 v84, v250, v18\n\t"
            "v_and_b32 v85, v251, v18\n\tds_write_b64 v29, v[62:63] offset:1024\n\tv_mfma_f32_16x16x32_bf16 v[68:71], v[128:131], v[184:187], v[68:71]\n\tv_and_b32 v86, v250, v19\n\tv_and_b32 v87, v251, v19\n\tv_mfma_f32_16x16x32_bf16 v[80:83], v[132:135], v[184:187], v[80:83]\n\t"
            "ds_write_b64 v28, v[72:73] offset:1536\n\tv_and_b32 v238, v250, v20\n\tv_mfma_f32_16x16x32_bf16 v[234:237], v[88:91], v[84:87], 0\n\tv_and_b32 v239, v251, v20\n\tv_and_b32 v240, v250, v21\n\tds_write_b64 v29, v[74:75] offset:1536\n\t"
            "v_and_b32 v241, v251, v21\n\tv_fmac_f32 v100, v213, v204\n\tv_fmac_f32 v101, v213, v205\n\tv_mfma_f32_16x16x32_bf16 v[242:245], v[88:91], v[238:241], 0\n\tds_read_b64_tr_b16 v[196:197], v12 offset:14336\n\tv_cndmask_b32 v84, v236, v234, s[14:15]\n\t"
            "ds_read_b64_tr_b16 v[198:199], v12 offset:14848\n\tv_cndmask_b32 v85, v237, v235, s[16:17]\n\tds_read_b128 v[152:155], v3 offset:2048\n\tv_fmac_f32 v102, v213, v206\n\ts_waitcnt lgkmcnt(0)\n\ts_barrier\n\t"
            "v_cndmask_b32 v86, v244, v242, s[14:15]\n\tds_write_b64 v28, v[84:85] offset:38912\n\tv_cndmask_b32 v87, v245, v243, s[16:17]\n\tv_fmac_f32 v103, v213, v207\n\tv_fmac_f32 v96, v213, v200\n\tv_fmac_f32 v97, v213, v201\n\t"
            "v_fmac_f32 v98, v213, v202\n\tds_write_b64 v29, v[86:87] offset:38912\n\tv_fmac_f32 v99, v213, v203\n\tv_cvt_pk_bf16_f32 v100, v100, v101\n\tv_cvt_pk_bf16_f32 v101, v102, v103\n\tv_cvt_pk_bf16_f32 v250, v96, v97\n\t"
            "v_cvt_pk_bf16_f32 v251, v98, v99\n\tds_read_b128 v[156:159], v3 offset:4096\n\tv_cndmask_b32 v102, v88, v100, vcc\n\tv_cndmask_b32 v103, v89, v101, vcc\n\tv_and_b32 v96, v250, v18\n\tds_read_b128 v[36:39], v15 offset:0\n\t"
            "v_and_b32 v97, v251, v18\n\tv_and_b32 v98, v250, v19\n\tv_and_b32 v99, v251, v19\n\tds_read_b128 v[160:163], v3 offset:6144\n\tv_and_b32 v218, v250, v20\n\tv_mfma_f32_16x16x32_bf16 v[246:249], v[100:103], v[96:99], 0\n\t"
            "ds_read_b128 v[48:51], v15 offset:64\n\tv_and_b32 v219, v251, v20\n\tv_mfma_f32_16x16x32_bf16 v[44:47], v[148:151], v[196:199], v[44:47]\n\tds_read_b128 v[60:63], v15 offset:128\n\tv_and_b32 v220, v250, v21\n\tv_mfma_f32_16x16x32_bf16 v[56:59], v[152:155], v[196:199], v[56:59]\n\t"
            "ds_read_b128 v[72:75], v15 offset:192\n\tv_and_b32 v221, v251, v21\n\tv_cndmask_b32 v96, v248, v246, s[14:15]\n\tv_cndmask_b32 v97, v249, v247, s[16:17]\n\tds_write_b64 v28, v[96:97] offset:39424\n\tv_mfma_f32_16x16x32_bf16 v[222:225], v[100:103], v[218:221], 0\n\t"
            "v_fmac_f32 v112, v214, v204\n\tv_fmac_f32 v113, v214, v205\n\ts_waitcnt lgkmcnt(5)\n\tv_mfma_f32_16x16x32_bf16 v[68:71], v[156:159], v[196:199], v[68:71]\n\tds_read_b128 v[144:147], v2 offset:12288\n\tv_fmac_f32 v114, v214, v206\n\t"
            "v_fmac_f32 v115, v214, v207\n\ts_nop 0\n\tv_cndmask_b32 v98, v224, v222, s[14:15]\n\tds_read_b128 v[164:167], v3 offset:8192\n\ts_waitcnt lgkmcnt(5)\n\tv_mfma_f32_16x16x32_bf16 v[80:83], v[160:163], v[196:199], v[80:83]\n\t"
            "v_cndmask_b32 v99, v225, v223, s[16:17]\n\tds_write_b64 v29, v[98:99] offset:39424\n\tv_fmac_f32 v108, v214, v200\n\tv_fmac_f32 v109, v214, v201\n\tv_fmac_f32 v110, v214, v202\n\tv_fmac_f32 v111, v214, v203\n\t"
            "v_cvt_pk_bf16_f32 v112, v112, v113\n\tds_read_b128 v[168:171], v3 offset:10240\n\tv_cvt_pk_bf16_f32 v113, v114, v115\n\tv_cvt_pk_bf16_f32 v250, v108, v109\n\tv_cvt_pk_bf16_f32 v251, v110, v111\n\tds_read_b128 v[172:175], v3 offset:12288\n\t"
            "v_cndmask_b32 v114, v100, v112, vcc\n\tv_cndmask_b32 v115, v101, v113, vcc\n\tv_and_b32 v108, v250, v18\n\tv_and_b32 v109, v251, v18\n\tv_and_b32 v110, v250, v19\n\tv_and_b32 v111, v251, v19\n\t"
            "v_and_b32 v230, v250, v20\n\tv_and_b32 v231, v251, v20\n\tv_mfma_f32_16x16x32_bf16 v[226:229], v[112:115], v[108:111], 0\n\tv_and_b32 v232, v250, v21\n\tv_and_b32 v233, v251, v21\n\tv_mfma_f32_16x16x32_bf16 v[92:95], v[136:139], v[184:187], v[92:95]\n\t"
            "v_mov_b32 v216, v44\n\tv_mov_b32 v217, v44\n\tv_mfma_f32_16x16x32_bf16 v[234:237], v[112:115], v[230:233], 0\n\ts_nop 1\n\tv_cndmask_b32 v108, v228, v226, s[14:15]\n\tv_cndmask_b32 v109, v229, v227, s[16:17]\n\t"
            "ds_write_b64 v28, v[108:109] offset:39936\n\ts_cbranch_scc0 1f\n\tv_cvt_pk_bf16_f32 v253, v84, v85\n\tv_min_u32 v253, -2, v253\n\tv_cvt_pk_bf16_f32 v255, v96, v97\n\tv_min_u32 v255, -2, v255\n\t"
            "buffer_store_dwordx4 v[252:255], v17, s[44:47], s22 offen sc1\n\ts_nop 1\n\tv_cvt_pk_bf16_f32 v253, v108, v109\n\tv_min_u32 v253, -2, v253\n\tbuffer_store_dwordx4 v[252:255], v17, s[44:47], s23 offen sc1\n\t1:\n"
            "v_mfma_f32_16x16x32_bf16 v[104:107], v[140:143], v[184:187], v[104:107]\n\tv_permlane32_swap_b32 v216, v217\n\tv_cndmask_b32 v110, v236, v234, s[14:15]\n\ts_waitcnt lgkmcnt(1)\n\tv_mfma_f32_16x16x32_bf16 v[116:119], v[144:147], v[184:187], v[116:119]\n\tv_cndmask_b32 v111, v237, v235, s[16:17]\n\t"
            "ds_write_b64 v29, v[110:111] offset:39936\n\tv_mfma_f32_16x16x32_bf16 v[92:95], v[164:167], v[196:199], v[92:95]\n\tv_mov_b32 v217, v216\n\tv_mov_b32 v218, 0\n\tv_mfma_f32_16x16x32_bf16 v[104:107], v[168:171], v[196:199], v[104:107]\n\tds_write_b32 v33, v33\n\t"
            SA(3)
            "v_permlane16_swap_b32 v216, v217\n\tv_mfma_f32_16x16x32_bf16 v[116:119], v[172:175], v[196:199], v[116:119]\n\tv_fmac_f32 v44, v36, v216\n\tv_fmac_f32 v45, v37, v216\n\tv_fmac_f32 v46, v38, v216\n\tv_fmac_f32 v47, v39, v216\n\t"
            "v_cvt_pk_bf16_f32 v44, v44, v45\n\tv_cvt_pk_bf16_f32 v45, v46, v47\n\tv_and_b32 v220, v44, v22\n\tv_and_b32 v221, v45, v22\n\tv_and_b32 v236, v44, v23\n\tv_and_b32 v237, v45, v23\n\t"
            "v_fmac_f32 v56, v48, v216\n\tv_fmac_f32 v57, v49, v216\n\tv_fmac_f32 v58, v50, v216\n\tv_fmac_f32 v59, v51, v216\n\tv_cvt_pk_bf16_f32 v56, v56, v57\n\tv_cvt_pk_bf16_f32 v57, v58, v59\n\t"
            "v_and_b32 v222, v56, v22\n\tv_and_b32 v223, v57, v22\n\tv_and_b32 v238, v56, v23\n\tv_and_b32 v239, v57, v23\n\tv_fmac_f32 v68, v60, v216\n\tv_fmac_f32 v69, v61, v216\n\t"
            "v_fmac_f32 v70, v62, v216\n\tv_fmac_f32 v71, v63, v216\n\tv_cvt_pk_bf16_f32 v68, v68, v69\n\tv_cvt_pk_bf16_f32 v69, v70, v71\n\tv_and_b32 v224, v68, v22\n\tv_and_b32 v225, v69, v22\n\t"
            "v_and_b32 v240, v68, v23\n\tv_and_b32 v241, v69, v23\n\tv_fmac_f32 v80, v72, v216\n\tv_fmac_f32 v81, v73, v216\n\tv_fmac_f32 v82, v74, v216\n\tv_fmac_f32 v83, v75, v216\n\t"
            "ds_read_b32 v27, v32\n\tds_read2st64_b32 v[42:43], v31 offset0:0 offset1:1\n\tds_read2st64_b32 v[40:41], v30 offset0:0 offset1:1\n\tds_read2st64_b32 v[66:67], v31 offset0:2 offset1:3\n\tds_read2st64_b32 v[64:65], v30 offset0:2 offset1:3\n\tv_cvt_pk_bf16_f32 v80, v80, v81\n\t"
            "v_cvt_pk_bf16_f32 v81, v82, v83\n\tv_and_b32 v226, v80, v22\n\tv_and_b32 v227, v81, v22\n\tv_and_b32 v242, v80, v23\n\tv_and_b32 v243, v81, v23\n\tds_read_b128 v[84:87], v15 offset:256\n\t"
            "v_mov_b32 v219, 0\n\tv_mov_b32 v234, 0\n\tv_mov_b32 v235, 0\n\tds_read_b128 v[96:99], v15 offset:320\n\tv_mov_b32 v176, 0\n\tv_mov_b32 v180, 0\n\t"
            "v_mov_b32 v188, 0\n\tds_read_b128 v[108:111], v15 offset:384\n\tv_mov_b32 v192, 0\n\t"
            SA(6)
            "s_waitcnt lgkmcnt(5)\n\tv_cmp_ge_u32 vcc, 0, v27\n\ts_cbranch_vccnz 10f\n\t20:\nv_and_b32 v177, v42, v24\n\tv_and_b32 v178, v42, v25\n\t"
            "v_and_b32 v179, v42, v26\n\tv_and_b32 v189, v43, v24\n\tv_and_b32 v190, v43, v25\n\tv_and_b32 v191, v43, v26\n\tv_mfma_f32_16x16x32_bf16 v[44:47], v[234:237], v[176:179], 0\n\tv_and_b32 v181, v40, v24\n\t"
            "v_and_b32 v182, v40, v25\n\tv_mfma_f32_16x16x32_bf16 v[56:59], v[236:239], v[188:191], 0\n\tv_and_b32 v183, v40, v26\n\tv_and_b32 v193, v41, v24\n\tv_and_b32 v194, v41, v25\n\tv_and_b32 v195, v41, v26\n\t"
            "v_mfma_f32_16x16x32_bf16 v[44:47], v[218:221], v[180:183], v[44:47]\n\ts_waitcnt lgkmcnt(0)\n\tv_and_b32 v177, v66, v24\n\tv_and_b32 v178, v66, v25\n\tv_mfma_f32_16x16x32_bf16 v[56:59], v[220:223], v[192:195], v[56:59]\n\tv_and_b32 v179, v66, v26\n\t"
            "v_and_b32 v189, v67, v24\n\tv_and_b32 v190, v67, v25\n\ts_nop 0\n\tv_cvt_pk_bf16_f32 v36, v44, v45\n\tv_cvt_pk_bf16_f32 v37, v46, v47\n\tv_min_u32 v36, -2, v36\n\t"
            "v_and_b32 v191, v67, v26\n\tv_cvt_pk_bf16_f32 v38, v56, v57\n\tv_cvt_pk_bf16_f32 v39, v58, v59\n\tv_min_u32 v38, -2, v38\n\tv_mfma_f32_16x16x32_bf16 v[68:71], v[238:241], v[176:179], 0\n\ts_nop 0\n\t"
            "v_permlane16_swap_b32 v36, v38\n\tv_permlane16_swap_b32 v37, v39\n\tbuffer_store_dwordx4 v[36:39], v16, s[44:47], s20 offen sc1\n\tv_mfma_f32_16x16x32_bf16 v[80:83], v[240:243], v[188:191], 0\n\tv_and_b32 v181, v64, v24\n\tv_and_b32 v182, v64, v25\n\t"
            "v_and_b32 v183, v64, v26\n\tv_and_b32 v193, v65, v24\n\tv_and_b32 v194, v65, v25\n\tv_and_b32 v195, v65, v26\n\tv_mfma_f32_16x16x32_bf16 v[68:71], v[222:225], v[180:183], v[68:71]\n\tv_fmac_f32 v92, v84, v216\n\t"
            "v_fmac_f32 v93, v85, v216\n\tv_mfma_f32_16x16x32_bf16 v[80:83], v[224:227], v[192:195], v[80:83]\n\tv_fmac_f32 v94, v86, v216\n\tv_fmac_f32 v95, v87, v216\n\tv_cvt_pk_bf16_f32 v92, v92, v93\n\ts_nop 1\n\t"
            "v_cvt_pk_bf16_f32 v60, v68, v69\n\tv_cvt_pk_bf16_f32 v61, v70, v71\n\tv_min_u32 v60, -2, v60\n\tv_fmac_f32 v104, v96, v216\n\tv_cvt_pk_bf16_f32 v62, v80, v81\n\tv_cvt_pk_bf16_f32 v63, v82, v83\n\t"
            "v_min_u32 v62, -2, v62\n\tv_fmac_f32 v105, v97, v216\n\tv_permlane16_swap_b32 v61, v63\n\tv_permlane16_swap_b32 v60, v62\n\tbuffer_store_dwordx4 v[60:63], v16, s[44:47], s21 offen sc1\n\tds_write_b32 v34, v35\n\t"
            SA(4)
            "v_cvt_pk_bf16_f32 v93, v94, v95\n\tv_fmac_f32 v106, v98, v216\n\tv_fmac_f32 v107, v99, v216\n\tv_cvt_pk_bf16_f32 v104, v104, v105\n\tv_fmac_f32 v116, v108, v216\n\tv_fmac_f32 v117, v109, v216\n\t"
            "v_and_b32 v228, v92, v22\n\tds_read_b32 v27, v32\n\tds_read2st64_b32 v[102:103], v31 offset0:152 offset1:153\n\tds_read2st64_b32 v[100:101], v30 offset0:152 offset1:153\n\tds_read2st64_b32 v[78:79], v31 offset0:154 offset1:155\n\tds_read2st64_b32 v[76:77], v30 offset0:154 offset1:155\n\t"
            "v_and_b32 v229, v93, v22\n\tv_and_b32 v244, v92, v23\n\tv_and_b32 v245, v93, v23\n\tv_cvt_pk_bf16_f32 v105, v106, v107\n\tv_fmac_f32 v118, v110, v216\n\tv_fmac_f32 v119, v111, v216\n\t"
            "v_cvt_pk_bf16_f32 v116, v116, v117\n\tv_and_b32 v230, v104, v22\n\tv_and_b32 v231, v105, v22\n\tv_and_b32 v246, v104, v23\n\tv_and_b32 v247, v105, v23\n\tv_cvt_pk_bf16_f32 v117, v118, v119\n\t"
            "v_and_b32 v232, v116, v22\n\tv_and_b32 v233, v117, v22\n\tv_and_b32 v248, v116, v23\n\tv_and_b32 v249, v117, v23\n\ts_waitcnt lgkmcnt(3)\n\tv_cmp_ge_u32 vcc, 1, v27\n\t"
            "s_cbranch_vccnz 11f\n\t21:\nv_and_b32 v177, v102, v24\n\tv_and_b32 v178, v102, v25\n\tv_and_b32 v179, v102, v26\n\tv_and_b32 v189, v103, v24\n\t"
            "v_and_b32 v190, v103, v25\n\tv_and_b32 v191, v103, v26\n\tv_mfma_f32_16x16x32_bf16 v[92:95], v[242:245], v[176:179], 0\n\ts_waitcnt lgkmcnt(0)\n\tv_and_b32 v181, v100, v24\n\tv_and_b32 v182, v100, v25\n\t"
            "v_mfma_f32_16x16x32_bf16 v[104:107], v[244:247], v[188:191], 0\n\tv_and_b32 v183, v100, v26\n\tv_and_b32 v193, v101, v24\n\tv_and_b32 v194, v101, v25\n\tv_and_b32 v195, v101, v26\n\tv_mfma_f32_16x16x32_bf16 v[92:95], v[226:229], v[180:183], v[92:95]\n\t"
            "v_and_b32 v177, v78, v24\n\tv_and_b32 v178, v78, v25\n\tv_mfma_f32_16x16x32_bf16 v[104:107], v[228:231], v[192:195], v[104:107]\n\tv_and_b32 v179, v78, v26\n\tv_and_b32 v181, v76, v24\n\tv_and_b32 v182, v76, v25\n\t"
            "v_mfma_f32_16x16x32_bf16 v[116:119], v[246:249], v[176:179], 0\n\tv_and_b32 v183, v76, v26\n\tv_cvt_pk_bf16_f32 v84, v92, v93\n\ts_nop 1\n\tv_cvt_pk_bf16_f32 v86, v104, v105\n\tv_mfma_f32_16x16x32_bf16 v[116:119], v[230:233], v[180:183], v[116:119]\n\t"
            "v_cvt_pk_bf16_f32 v85, v94, v95\n\tv_min_u32 v84, -2, v84\n\tv_cvt_pk_bf16_f32 v87, v106, v107\n\tv_min_u32 v86, -2, v86\n\ts_nop 0\n\tv_permlane16_swap_b32 v85, v87\n\t"
            "v_permlane16_swap_b32 v84, v86\n\tbuffer_store_dwordx4 v[84:87], v16, s[44:47], s22 offen sc1\n\tv_cvt_pk_bf16_f32 v108, v116, v117\n\tv_cvt_pk_bf16_f32 v109, v118, v119\n\tv_min_u32 v108, -2, v108\n\ts_nop 0\n\t"
            "v_permlane16_swap_b32 v109, v111\n\tv_permlane16_swap_b32 v108, v110\n\tbuffer_store_dwordx4 v[108:111], v215, s[44:47], s23 offen sc1\n\tds_write_b32 v34, v34\n\t"
            SA(5)
            "s_setprio 0\n\t"
            DUMP
            "s_cmp_eq_u32 s69, 1\n\ts_cbranch_scc1 .Lout\n\ts_endpgm\n\t10:\nds_read_b32 v27, v32\n\tds_read2st64_b32 v[42:43], v31 offset0:0 offset1:1\n\t"
            "ds_read2st64_b32 v[40:41], v30 offset0:0 offset1:1\n\tds_read2st64_b32 v[66:67], v31 offset0:2 offset1:3\n\tds_read2st64_b32 v[64:65], v30 offset0:2 offset1:3\n\ts_waitcnt lgkmcnt(0)\n\tv_cmp_ge_u32 vcc, 0, v27\n\ts_cbranch_vccz 20b\n\t"
            ASLP
            "s_sub_u32 s68, s68, 1\n\ts_cbranch_scc0 10b\n\ts_branch 20b\n\t11:\nds_read_b32 v27, v32\n\tds_read2st64_b32 v[102:103], v31 offset0:152 offset1:153\n\t"
            "ds_read2st64_b32 v[100:101], v30 offset0:152 offset1:153\n\tds_read2st64_b32 v[78:79], v31 offset0:154 offset1:155\n\tds_read2st64_b32 v[76:77], v30 offset0:154 offset1:155\n\ts_waitcnt lgkmcnt(0)\n\tv_cmp_ge_u32 vcc, 1, v27\n\ts_cbranch_vccz 21b\n\t"
            ASLP
            "s_sub_u32 s68, s68, 1\n\ts_cbranch_scc0 11b\n\ts_branch 21b\n\t.LB:\ns_and_b32 s72, s56, 1\n\ts_lshr_b32 s73, s56, 1\n\t"
            "v_and_b32 v156, 2, v51\n\tv_lshlrev_b32 v156, 1, v156\n\tv_and_b32 v157, 7, v52\n\tv_xor_b32 v157, v156, v157\n\tv_mul_u32_u24 v156, 0x500, v51\n\tv_lshl_add_u32 v156, v157, 4, v156\n\t"
            "s_mul_i32 s74, s72, 0x2800\n\ts_mul_i32 s75, s73, 0xf000\n\ts_add_i32 s75, s75, s74\n\tv_add_u32 v154, s75, v156\n\ts_mul_i32 s75, s73, 0x5000\n\ts_add_i32 s75, s75, s74\n\t"
            "s_addk_i32 s75, 0x5000\n\tv_add_u32 v156, s75, v156\n\ts_cmp_lg_u32 s63, 0\n\ts_cselect_b32 s74, 81920, 0\n\ts_max_u32 s74, s74, 65536\n\tv_add_u32 v158, s62, v52\n\t"
            "v_min_u32 v159, 47, v52\n\ts_add_i32 s75, s62, 64\n\tv_add_u32 v159, s75, v159\n\tv_max_i32 v158, 0, v158\n\tv_min_i32 v159, 0x3ff, v159\n\tv_lshl_add_u32 v158, v158, 4, s74\n\t"
            "v_lshl_add_u32 v159, v159, 4, s74\n\tv_lshl_add_u32 v59, v53, 4, v57\n\ts_cmp_lg_u32 s73, 0\n\ts_cselect_b64 vcc, -1, 0\n\tv_cndmask_b32 v97, v59, v52, vcc\n\ts_lshl_b32 s75, s72, 6\n\t"
            "s_add_i32 s75, s75, s62\n\tv_add_u32 v97, s75, v97\n\tv_mov_b32 v60, s62\n\tv_add_u32 v60, 0x6f, v60\n\tv_min_i32 v60, 0x3ff, v60\n\tv_med3_i32 v97, v97, 0, v60\n\t"
            "s_lshl3_add_u32 s74, s73, s74\n\tv_lshl_add_u32 v97, v97, 4, s74\n\ts_mul_i32 s30, s73, 576\n\ts_lshl_b32 s75, s72, 8\n\ts_add_i32 s30, s30, s75\n\ts_add_i32 s30, s30, 160000\n\t"
            "s_lshl_b32 s74, s59, 7\n\ts_add_i32 s74, s74, 8192\n\tv_min_u32 v96, 9, v52\n\tv_lshl_add_u32 v96, v96, 2, s74\n\ts_lshr_b32 s75, s58, 5\n\ts_add_i32 s74, s74, s75\n\t"
            "s_cmp_eq_u32 s56, 3\n\ts_cselect_b32 s74, s74, 0x40000000\n\tv_lshl_add_u32 v98, v52, 24, s74\n\ts_cselect_b32 s75, 1, 0\n\ts_lshl2_add_u32 s74, s59, s56\n\ts_mul_i32 s74, s74, 0xa000\n\t"
            "s_add_i32 s74, s74, 155648\n\tv_lshl_add_u32 v101, v52, 4, s74\n\tv_and_b32 v60, s75, v53\n\tv_lshl_add_u32 v2, v60, 30, v101\n\ts_cmp_ge_u32 s56, s57\n\ts_addc_u32 s76, s56, 0\n\t"
            "s_and_b32 s16, s76, 3\n\ts_lshl_b32 s77, s76, 1\n\ts_max_u32 s78, s77, 1\n\ts_sub_u32 s78, s78, 1\n\ts_min_u32 s79, s77, 7\n\tv_lshlrev_b32 v60, 9, v53\n\t"
            "v_lshl_add_u32 v60, v54, 3, v60\n\ts_mul_i32 s80, s78, 0xc00\n\ts_mul_i32 s81, s79, 0xc00\n\ts_addk_i32 s80, 14336\n\ts_addk_i32 s81, 14336\n\tv_add_u32 v24, s80, v60\n\t"
            "v_add_u32 v26, s81, v60\n\tv_add_u32 v25, 38912, v24\n\tv_add_u32 v27, 38912, v26\n\tv_lshl_add_u32 v29, v52, 4, s80\n\ts_lshr_b32 s78, s78, 1\n\ts_lshr_b32 s79, s79, 1\n\t"
            "s_sub_u32 s79, s79, s78\n\ts_lshl2_add_u32 s78, s78, 159744\n\tv_and_b32 v28, s79, v52\n\tv_lshl_add_u32 v28, v28, 2, s78\n\ts_lshl2_add_u32 s78, s56, 159744\n\tv_mov_b32 v30, s78\n\t"
            "s_mov_b32 s78, 160576\n\tv_lshl_add_u32 v153, v59, 2, s78\n\ts_mul_i32 s79, s59, 163840\n\ts_lshl_b32 s80, s58, 5\n\ts_add_i32 s79, s79, s80\n\ts_cmp_eq_u32 s76, 0\n\t"
            "s_cselect_b32 s80, 0, 0xc00\n\ts_add_i32 s79, s79, s80\n\ts_add_i32 s79, s79, 155652\n\tv_lshl_add_u32 v31, v54, 4, s79\n\tv_lshrrev_b32 v60, 1, v53\n\tv_mul_u32_u24 v60, 0xa000, v60\n\t"
            "v_add_u32 v31, v60, v31\n\tv_and_b32 v60, 1, v53\n\tv_lshl_add_u32 v31, v60, 3, v31\n\ts_mov_b32 s31, 0x14000\n\ts_cmp_lt_i32 s62, 0\n\ts_cselect_b32 s79, 8, -16\n\t"
            "v_mov_b32 v61, 0xff800000\n\tv_mov_b32 v142, 1\n\tv_add_u32 v60, 0, v59\n\tv_and_b32 v60, -8, v60\n\tv_add_u32 v60, 0, v60\n\tv_cmp_gt_i32 vcc, s79, v60\n\t"
            "v_lshl_add_u32 v160, v60, 2, s78\n\ts_nop 0\n\tv_cndmask_b32 v128, 0, v61, vcc\n\tv_add_u32 v60, 4, v59\n\tv_and_b32 v60, -8, v60\n\tv_add_u32 v60, -4, v60\n\t"
            "v_cmp_gt_i32 vcc, s79, v60\n\tv_lshl_add_u32 v161, v60, 2, s78\n\ts_nop 0\n\tv_cndmask_b32 v129, 0, v61, vcc\n\tv_add_u32 v60, -1, v59\n\tv_and_b32 v60, -8, v60\n\t"
            "v_add_u32 v60, 1, v60\n\tv_cmp_gt_i32 vcc, s79, v60\n\tv_lshl_add_u32 v162, v60, 2, s78\n\ts_nop 0\n\tv_cndmask_b32 v130, 0, v61, vcc\n\tv_add_u32 v60, 3, v59\n\t"
            "v_and_b32 v60, -8, v60\n\tv_add_u32 v60, -3, v60\n\tv_cmp_gt_i32 vcc, s79, v60\n\tv_lshl_add_u32 v163, v60, 2, s78\n\ts_nop 0\n\tv_cndmask_b32 v131, 0, v61, vcc\n\t"
            "v_add_u32 v60, -2, v59\n\tv_and_b32 v60, -8, v60\n\tv_add_u32 v60, 2, v60\n\tv_cmp_gt_i32 vcc, s79, v60\n\tv_lshl_add_u32 v164, v60, 2, s78\n\ts_nop 0\n\t"
            "v_cndmask_b32 v132, 0, v61, vcc\n\tv_add_u32 v60, 2, v59\n\tv_and_b32 v60, -8, v60\n\tv_add_u32 v60, -2, v60\n\tv_cmp_gt_i32 vcc, s79, v60\n\tv_lshl_add_u32 v165, v60, 2, s78\n\t"
            "s_nop 0\n\tv_cndmask_b32 v133, 0, v61, vcc\n\tv_add_u32 v60, -3, v59\n\tv_and_b32 v60, -8, v60\n\tv_add_u32 v60, 3, v60\n\tv_cmp_gt_i32 vcc, s79, v60\n\t"
            "v_lshl_add_u32 v166, v60, 2, s78\n\ts_nop 0\n\tv_cndmask_b32 v134, 0, v61, vcc\n\tv_add_u32 v60, 1, v59\n\tv_and_b32 v60, -8, v60\n\tv_add_u32 v60, -1, v60\n\t"
            "v_cmp_gt_i32 vcc, s79, v60\n\tv_lshl_add_u32 v167, v60, 2, s78\n\ts_nop 0\n\tv_cndmask_b32 v135, 0, v61, vcc\n\ts_lshl2_add_u32 s80, s71, s56\n\ts_lshl_b32 s80, s80, 5\n\t"
            "v_add_u32 v100, s80, v52\n\tv_mul_u32_u24 v100, 0x500, v100\n\ts_add_i32 s80, s58, 3072\n\tv_add_u32 v100, s80, v100\n\tv_min_u32 v104, 51, v52\n\tv_lshlrev_b32 v104, 4, v104\n\t"
            "v_add_u32 v104, 0x1000, v104\n\ts_lshl_b32 s4, s64, 13\n\ts_add_u32 s5, s4, 0x1000\n\ts_add_u32 s6, s5, 0x1000\n\ts_cmpk_eq_u32 s6, 0xa000\n\ts_cselect_b32 s6, 0, s6\n\t"
            "s_add_u32 s7, s6, 0x1000\n\ts_add_u32 s8, s7, 0x1000\n\ts_cmpk_eq_u32 s8, 0xa000\n\ts_cselect_b32 s8, 0, s8\n\ts_add_u32 s9, s8, 0x1000\n\ts_add_u32 s10, s9, 0x1000\n\t"
            "s_cmpk_eq_u32 s10, 0xa000\n\ts_cselect_b32 s10, 0, s10\n\ts_add_u32 s11, s10, 0x1000\n\ts_add_u32 s12, s11, 0x1000\n\ts_cmpk_eq_u32 s12, 0xa000\n\ts_cselect_b32 s12, 0, s12\n\t"
            "s_add_u32 s13, s12, 0x1000\n\ts_mul_i32 s20, s4, 20\n\ts_add_i32 s20, s20, s58\n\ts_addk_i32 s20, 3072\n\ts_mul_i32 s21, s5, 20\n\ts_add_i32 s21, s21, s58\n\t"
            "s_addk_i32 s21, 2048\n\ts_mul_i32 s22, s6, 20\n\ts_add_i32 s22, s22, s58\n\ts_addk_i32 s22, 1024\n\ts_mul_i32 s23, s7, 20\n\ts_add_i32 s23, s23, s58\n\t"
            "s_mul_i32 s24, s8, 20\n\ts_add_i32 s24, s24, s58\n\ts_addk_i32 s24, 3072\n\ts_mul_i32 s25, s9, 20\n\ts_add_i32 s25, s25, s58\n\ts_addk_i32 s25, 2048\n\t"
            "s_mul_i32 s26, s10, 20\n\ts_add_i32 s26, s26, s58\n\ts_addk_i32 s26, 1024\n\ts_mul_i32 s27, s11, 20\n\ts_add_i32 s27, s27, s58\n\ts_mul_i32 s28, s12, 20\n\t"
            "s_add_i32 s28, s28, s58\n\ts_addk_i32 s28, 3072\n\ts_mul_i32 s29, s13, 20\n\ts_add_i32 s29, s29, s58\n\ts_addk_i32 s29, 2048\n\ts_mul_i32 s72, s60, 1920\n\t"
            "s_sub_u32 s14, s61, s72\n\ts_add_u32 s17, s63, 0x500\n\ts_add_u32 s60, s60, 256\n\ts_cmp_eq_u32 s60, s17\n\ts_cselect_b32 s60, s63, s60\n\ts_mul_i32 s15, s56, 0x2800\n\t"
            "s_add_u32 s15, s15, 77824\n\tv_lshl_add_u32 v152, v52, 4, s15\n\ts_lshl_b32 s18, s58, 5\n\ts_mov_b32 s50, 822272\n\ts_mov_b32 s51, 0x20000\n\ts_waitcnt lgkmcnt(0)\n\t"
            "s_load_dword s67, s[44:45], 0x0\n\ts_sub_u32 s48, s48, 3072\n\ts_subb_u32 s49, s49, 0\n\ts_lshl_b32 s78, s56, 5\n\tv_add_u32 v12, s78, v57\n\tv_add_u32 v3, s62, v12\n\t"
            "v_and_b32 v13, 1, v53\n\tv_lshrrev_b32 v14, 1, v53\n\tv_lshl_add_u32 v15, v13, 1, v14\n\ts_add_u32 s79, s63, s58\n\tv_lshl_add_u32 v15, v15, 4, s79\n\ts_movk_i32 s79, 0xa00\n\t"
            "v_mad_i32_i24 v3, v3, s79, v15\n\tv_cmp_gt_u32 vcc, 8, v12\n\ts_cmp_eq_u32 s56, 3\n\ts_cselect_b32 s79, 2.0, 0xa000\n\tv_add_u32 v11, s79, v3\n\tv_cndmask_b32 v3, v3, 2.0, vcc\n\t"
            "v_mov_b32 v8, 159744\n\tv_and_b32 v9, 31, v52\n\tv_add_u32 v9, s78, v9\n\tv_lshl_add_u32 v9, v9, 2, v8\n\tv_lshl_add_u32 v10, v13, 4, v12\n\tv_lshl_add_u32 v10, v10, 2, v8\n\t"
            "v_mul_u32_u24 v4, 0x2800, v14\n\tv_lshl_add_u32 v4, v56, 7, v4\n\tv_lshl_add_u32 v4, v55, 3, v4\n\tv_lshl_add_u32 v4, v13, 5, v4\n\tv_bfe_u32 v5, v52, 3, 1\n\tv_lshl_add_u32 v4, v5, 6, v4\n\t"
            "v_add_u32 v4, 77824, v4\n\tv_xor_b32 v5, 64, v4\n\tv_add_u32 v6, 0x5000, v4\n\tv_add_u32 v7, 0x5000, v5\n\tv_lshlrev_b32 v102, 16, v52\n\tv_add_u32 v102, 0xc00, v102\n\t"
            "s_cmp_eq_u32 s56, 0\n\ts_cbranch_scc0 1f\n\tbuffer_load_dword v103, v102, s[48:51], s58 offen\n\t1:\ns_cmp_eq_u32 s56, 1\n\ts_cbranch_scc0 1f\n\t"
            "s_mul_i32 s80, s62, 0xa00\n\ts_max_i32 s80, s80, 0\n\ts_mov_b64 exec, 0x1f\n\tv_add_u32 v102, s80, v102\n\ts_waitcnt vmcnt(0)\n\tbuffer_load_dword v103, v102, s[52:55], 0 offen\n\t"
            "s_mov_b64 exec, -1\n\t1:\ns_cmp_lt_u32 s56, 2\n\ts_cbranch_scc1 1f\n\ts_waitcnt vmcnt(0)\n\t1:\n"
            "s_waitcnt vmcnt(1) lgkmcnt(0)\n\ts_barrier\n\tv_mov_b32 v136, -1\n\tv_mov_b32 v137, -1\n\tv_mov_b32 v138, -1\n\tv_mov_b32 v139, -1\n\t"
            "s_add_u32 m0, s66, 0\n\ts_mul_i32 s61, s60, 1920\n\ts_add_u32 s61, s61, s14\n\tbuffer_load_dwordx4 v40, s[40:43], s61 offen lds\n\tbuffer_load_dwordx4 v41, s[40:43], s61 offen offset:1024 lds\n\tbuffer_load_dwordx4 v42, s[40:43], s61 offen offset:2048 lds\n\t"
            "buffer_load_dwordx4 v43, s[40:43], s61 offen offset:3072 lds\n\ts_addk_i32 m0, 0x1000\n\ts_add_u32 s68, s60, 128\n\tbuffer_load_dwordx4 v44, s[40:43], s61 offen lds\n\tbuffer_load_dwordx4 v45, s[40:43], s61 offen offset:1024 lds\n\ts_add_u32 m0, s65, 0\n\t"
            "s_cmp_eq_u32 s68, s17\n\tbuffer_load_dwordx4 v36, s[36:39], s60 offen lds\n\tbuffer_load_dwordx4 v37, s[36:39], s60 offen offset:1024 lds\n\tbuffer_load_dwordx4 v38, s[36:39], s60 offen offset:2048 lds\n\ts_mov_b32 exec_hi, 0\n\tbuffer_load_dwordx4 v39, s[36:39], s60 offen offset:3072 lds\n\t"
            "s_mov_b32 exec_hi, -1\n\ts_cselect_b32 s60, s63, s68\n\ts_mul_i32 s67, s67, 0x19660d\n\ts_add_u32 s67, s67, 0x3c6ef35f\n\tbuffer_store_dwordx4 v[136:139], v101, s[44:47], s18 offen offset:0 sc1\n\tbuffer_store_dwordx4 v[136:139], v2, s[44:47], s18 offen offset:1024 sc1\n\t"
            "buffer_store_dwordx4 v[136:139], v2, s[44:47], s18 offen offset:2048 sc1\n\tbuffer_store_dwordx4 v[136:139], v101, s[44:47], s18 offen offset:3072 sc1\n\ts_barrier\n\ts_add_u32 m0, s66, 38912\n\ts_mul_i32 s61, s60, 1920\n\ts_add_u32 s61, s61, s14\n\t"
            "buffer_load_dwordx4 v40, s[40:43], s61 offen lds\n\tbuffer_load_dwordx4 v41, s[40:43], s61 offen offset:1024 lds\n\tbuffer_load_dwordx4 v42, s[40:43], s61 offen offset:2048 lds\n\tbuffer_load_dwordx4 v43, s[40:43], s61 offen offset:3072 lds\n\ts_addk_i32 m0, 0x1000\n\ts_add_u32 s68, s60, 128\n\t"
            "buffer_load_dwordx4 v44, s[40:43], s61 offen lds\n\tbuffer_load_dwordx4 v45, s[40:43], s61 offen offset:1024 lds\n\ts_add_u32 m0, s65, 38912\n\ts_cmp_eq_u32 s68, s17\n\tbuffer_load_dwordx4 v36, s[36:39], s60 offen lds\n\tbuffer_load_dwordx4 v37, s[36:39], s60 offen offset:1024 lds\n\t"
            "buffer_load_dwordx4 v38, s[36:39], s60 offen offset:2048 lds\n\ts_mov_b32 exec_hi, 0\n\tbuffer_load_dwordx4 v39, s[36:39], s60 offen offset:3072 lds\n\ts_mov_b32 exec_hi, -1\n\ts_cselect_b32 s60, s63, s68\n\ts_waitcnt vmcnt(14)\n\t"
            "s_barrier\n\ts_add_u32 m0, s66, 77824\n\ts_mul_i32 s61, s60, 1920\n\ts_add_u32 s61, s61, s14\n\tbuffer_load_dwordx4 v40, s[40:43], s61 offen lds\n\tbuffer_load_dwordx4 v41, s[40:43], s61 offen offset:1024 lds\n\t"
            "buffer_load_dwordx4 v42, s[40:43], s61 offen offset:2048 lds\n\tbuffer_load_dwordx4 v43, s[40:43], s61 offen offset:3072 lds\n\ts_addk_i32 m0, 0x1000\n\ts_add_u32 s68, s60, 128\n\tbuffer_load_dwordx4 v44, s[40:43], s61 offen lds\n\tbuffer_load_dwordx4 v45, s[40:43], s61 offen offset:1024 lds\n\t"
            "s_add_u32 m0, s65, 77824\n\ts_cmp_eq_u32 s68, s17\n\tbuffer_load_dwordx4 v36, s[36:39], s60 offen lds\n\tbuffer_load_dwordx4 v37, s[36:39], s60 offen offset:1024 lds\n\tbuffer_load_dwordx4 v38, s[36:39], s60 offen offset:2048 lds\n\ts_mov_b32 exec_hi, 0\n\t"
            "buffer_load_dwordx4 v39, s[36:39], s60 offen offset:3072 lds\n\ts_mov_b32 exec_hi, -1\n\ts_cselect_b32 s60, s63, s68\n\ts_waitcnt vmcnt(10)\n\ts_barrier\n\ts_add_u32 m0, s66, 0\n\t"
            "s_mul_i32 s61, s60, 1920\n\ts_add_u32 s61, s61, s14\n\tbuffer_load_dwordx4 v40, s[40:43], s61 offen lds\n\tbuffer_load_dwordx4 v41, s[40:43], s61 offen offset:1024 lds\n\tbuffer_load_dwordx4 v42, s[40:43], s61 offen offset:2048 lds\n\tbuffer_load_dwordx4 v43, s[40:43], s61 offen offset:3072 lds\n\t"
            "s_addk_i32 m0, 0x1000\n\ts_add_u32 s68, s60, 128\n\tbuffer_load_dwordx4 v44, s[40:43], s61 offen lds\n\tbuffer_load_dwordx4 v45, s[40:43], s61 offen offset:1024 lds\n\ts_add_u32 m0, s65, 0\n\ts_cmp_eq_u32 s68, s17\n\t"
            "buffer_load_dwordx4 v36, s[36:39], s60 offen lds\n\tbuffer_load_dwordx4 v37, s[36:39], s60 offen offset:1024 lds\n\tbuffer_load_dwordx4 v38, s[36:39], s60 offen offset:2048 lds\n\ts_mov_b32 exec_hi, 0\n\tbuffer_load_dwordx4 v39, s[36:39], s60 offen offset:3072 lds\n\ts_mov_b32 exec_hi, -1\n\t"
            "s_cselect_b32 s60, s63, s68\n\tv_mov_b32 v140, s67\n\tbuffer_store_dword v140, v98, s[44:47], 0 offen sc1\n\ts_waitcnt vmcnt(11)\n\ts_barrier\n\ts_add_u32 m0, s66, 38912\n\t"
            "s_mul_i32 s61, s60, 1920\n\ts_add_u32 s61, s61, s14\n\tbuffer_load_dwordx4 v40, s[40:43], s61 offen lds\n\tbuffer_load_dwordx4 v41, s[40:43], s61 offen offset:1024 lds\n\tbuffer_load_dwordx4 v42, s[40:43], s61 offen offset:2048 lds\n\tbuffer_load_dwordx4 v43, s[40:43], s61 offen offset:3072 lds\n\t"
            "s_addk_i32 m0, 0x1000\n\ts_add_u32 s68, s60, 128\n\tbuffer_load_dwordx4 v44, s[40:43], s61 offen lds\n\tbuffer_load_dwordx4 v45, s[40:43], s61 offen offset:1024 lds\n\ts_add_u32 m0, s65, 38912\n\ts_cmp_eq_u32 s68, s17\n\t"
            "buffer_load_dwordx4 v36, s[36:39], s60 offen lds\n\tbuffer_load_dwordx4 v37, s[36:39], s60 offen offset:1024 lds\n\tbuffer_load_dwordx4 v38, s[36:39], s60 offen offset:2048 lds\n\ts_mov_b32 exec_hi, 0\n\tbuffer_load_dwordx4 v39, s[36:39], s60 offen offset:3072 lds\n\ts_mov_b32 exec_hi, -1\n\t"
            "s_cselect_b32 s60, s63, s68\n\ts_waitcnt vmcnt(11)\n\ts_barrier\n\ts_add_u32 m0, s66, 77824\n\ts_mul_i32 s61, s60, 1920\n\ts_add_u32 s61, s61, s14\n\t"
            "buffer_load_dwordx4 v40, s[40:43], s61 offen lds\n\tbuffer_load_dwordx4 v41, s[40:43], s61 offen offset:1024 lds\n\tbuffer_load_dwordx4 v42, s[40:43], s61 offen offset:2048 lds\n\tbuffer_load_dwordx4 v43, s[40:43], s61 offen offset:3072 lds\n\ts_addk_i32 m0, 0x1000\n\ts_add_u32 s68, s60, 128\n\t"
            "buffer_load_dwordx4 v44, s[40:43], s61 offen lds\n\tbuffer_load_dwordx4 v45, s[40:43], s61 offen offset:1024 lds\n\ts_add_u32 m0, s65, 77824\n\ts_cmp_eq_u32 s68, s17\n\tbuffer_load_dwordx4 v36, s[36:39], s60 offen lds\n\tbuffer_load_dwordx4 v37, s[36:39], s60 offen offset:1024 lds\n\t"
            "buffer_load_dwordx4 v38, s[36:39], s60 offen offset:2048 lds\n\ts_mov_b32 exec_hi, 0\n\tbuffer_load_dwordx4 v39, s[36:39], s60 offen offset:3072 lds\n\ts_mov_b32 exec_hi, -1\n\ts_cselect_b32 s60, s63, s68\n\ts_waitcnt vmcnt(10)\n\t"
            "s_barrier\n\ts_cmp_lg_u32 s56, 3\n\ts_cbranch_scc1 1f\n\tbuffer_load_dwordx4 v[106:109], v158, s[44:47], 0 offen sc1\n\tbuffer_load_dwordx4 v[110:113], v159, s[44:47], 0 offen sc1\n\tbuffer_load_dwordx4 v[114:117], v104, s[44:47], 0 offen sc1\n\t"
            "buffer_load_dword v118, v96, s[44:47], 0 offen sc1\n\t1:\ns_add_u32 m0, s66, 0\n\ts_mul_i32 s61, s60, 1920\n\ts_add_u32 s61, s61, s14\n\tbuffer_load_dwordx4 v40, s[40:43], s61 offen lds\n\t"
            "buffer_load_dwordx4 v41, s[40:43], s61 offen offset:1024 lds\n\tbuffer_load_dwordx4 v42, s[40:43], s61 offen offset:2048 lds\n\tbuffer_load_dwordx4 v43, s[40:43], s61 offen offset:3072 lds\n\ts_addk_i32 m0, 0x1000\n\ts_add_u32 s68, s60, 128\n\tbuffer_load_dwordx4 v44, s[40:43], s61 offen lds\n\t"
            "buffer_load_dwordx4 v45, s[40:43], s61 offen offset:1024 lds\n\ts_add_u32 m0, s65, 0\n\ts_cmp_eq_u32 s68, s17\n\tbuffer_load_dwordx4 v36, s[36:39], s60 offen lds\n\tbuffer_load_dwordx4 v37, s[36:39], s60 offen offset:1024 lds\n\tbuffer_load_dwordx4 v38, s[36:39], s60 offen offset:2048 lds\n\t"
            "s_mov_b32 exec_hi, 0\n\tbuffer_load_dwordx4 v39, s[36:39], s60 offen offset:3072 lds\n\ts_mov_b32 exec_hi, -1\n\ts_cselect_b32 s60, s63, s68\n\ts_waitcnt vmcnt(10)\n\ts_cmp_lg_u32 s56, 3\n\t"
            "s_cbranch_scc1 2f\n\ts_mov_b32 s68, 0x40000\n\tv_xor_b32 v106, s67, v107\n\tv_xor_b32 v108, s67, v109\n\tv_xor_b32 v110, s67, v111\n\tv_xor_b32 v112, s67, v113\n\t"
            "v_xor_b32 v114, s67, v115\n\tv_xor_b32 v116, s67, v117\n\tv_xor_b32 v118, s67, v118\n\tv_or3_b32 v106, v106, v108, v110\n\tv_or3_b32 v118, v118, v114, v116\n\tv_or3_b32 v106, v106, v112, v118\n\t"
            "v_cmp_ne_u32 vcc, 0, v106\n\ts_cbranch_vccnz 30f\n\t31:\n2:\ns_waitcnt vmcnt(10)\n\ts_barrier\n\t"
            "s_add_u32 m0, s66, 38912\n\ts_mul_i32 s61, s60, 1920\n\ts_add_u32 s61, s61, s14\n\tbuffer_load_dwordx4 v40, s[40:43], s61 offen lds\n\tbuffer_load_dwordx4 v41, s[40:43], s61 offen offset:1024 lds\n\tbuffer_load_dwordx4 v42, s[40:43], s61 offen offset:2048 lds\n\t"
            "buffer_load_dwordx4 v43, s[40:43], s61 offen offset:3072 lds\n\ts_addk_i32 m0, 0x1000\n\ts_add_u32 s68, s60, 128\n\tbuffer_load_dwordx4 v44, s[40:43], s61 offen lds\n\tbuffer_load_dwordx4 v45, s[40:43], s61 offen offset:1024 lds\n\ts_add_u32 m0, s65, 38912\n\t"
            "s_cmp_eq_u32 s68, s17\n\tbuffer_load_dwordx4 v36, s[36:39], s60 offen lds\n\tbuffer_load_dwordx4 v37, s[36:39], s60 offen offset:1024 lds\n\tbuffer_load_dwordx4 v38, s[36:39], s60 offen offset:2048 lds\n\ts_mov_b32 exec_hi, 0\n\tbuffer_load_dwordx4 v39, s[36:39], s60 offen offset:3072 lds\n\t"
            "s_mov_b32 exec_hi, -1\n\ts_cselect_b32 s60, s63, s68\n\ts_mov_b32 m0, s30\n\ts_nop 0\n\tbuffer_load_dword v97, s[44:47], 0 offen sc1 lds\n\ts_waitcnt vmcnt(11)\n\t"
            "s_barrier\n\ts_add_u32 m0, s15, 40960\n\ts_nop 0\n\tbuffer_load_dwordx4 v156, s[48:51], s20 offen lds\n\tbuffer_load_dwordx4 v156, s[48:51], s21 offen offset:1024 lds\n\tbuffer_load_dwordx4 v156, s[48:51], s22 offen offset:2048 lds\n\t"
            "buffer_load_dwordx4 v156, s[48:51], s23 offen offset:3072 lds\n\ts_add_u32 m0, s15, 45056\n\ts_nop 0\n\tbuffer_load_dwordx4 v156, s[48:51], s24 offen lds\n\tbuffer_load_dwordx4 v156, s[48:51], s25 offen offset:1024 lds\n\tbuffer_load_dwordx4 v156, s[48:51], s26 offen offset:2048 lds\n\t"
            "s_waitcnt vmcnt(7)\n\ts_barrier\n\ts_add_u32 m0, s15, 45056\n\ts_nop 0\n\tbuffer_load_dwordx4 v156, s[48:51], s27 offen offset:3072 lds\n\ts_add_u32 m0, s15, 49152\n\t"
            "s_nop 0\n\tbuffer_load_dwordx4 v156, s[48:51], s28 offen lds\n\tbuffer_load_dwordx4 v156, s[48:51], s29 offen offset:1024 lds\n\ts_add_u32 m0, s15, 0\n\ts_nop 0\n\tbuffer_load_dwordx4 v154, s[48:51], s20 offen lds\n\t"
            "buffer_load_dwordx4 v154, s[48:51], s21 offen offset:1024 lds\n\tbuffer_load_dwordx4 v154, s[48:51], s22 offen offset:2048 lds\n\tbuffer_load_dwordx4 v154, s[48:51], s23 offen offset:3072 lds\n\ts_add_u32 m0, s15, 4096\n\ts_nop 0\n\tbuffer_load_dwordx4 v154, s[48:51], s24 offen lds\n\t"
            "buffer_load_dwordx4 v154, s[48:51], s25 offen offset:1024 lds\n\tbuffer_load_dwordx4 v154, s[48:51], s26 offen offset:2048 lds\n\tbuffer_load_dwordx4 v154, s[48:51], s27 offen offset:3072 lds\n\ts_add_u32 m0, s15, 8192\n\ts_nop 0\n\tbuffer_load_dwordx4 v154, s[48:51], s28 offen lds\n\t"
            "buffer_load_dwordx4 v154, s[48:51], s29 offen offset:1024 lds\n\ts_cmp_lg_u32 s56, 0\n\ts_cbranch_scc1 1f\n\ts_mov_b32 m0, 162624\n\ts_nop 0\n\tbuffer_load_dwordx4 v104, s[44:47], 0 offen sc1 lds\n\t"
            "1:\nbuffer_load_dwordx4 v[240:243], v3, s[36:39], 0 offen offset:0\n\tbuffer_load_dwordx4 v[244:247], v11, s[36:39], 0 offen offset:0\n\tbuffer_load_dwordx4 v[248:251], v3, s[36:39], 0 offen offset:64\n\tbuffer_load_dwordx4 v[252:255], v11, s[36:39], 0 offen offset:64\n\tds_read_b32 v20, v9 offset:256\n\t"
            "ds_read_b32 v21, v10 offset:832\n\tds_read2st64_b32 v[138:139], v153 offset1:1\n\tds_read2st64_b32 v[176:177], v160 offset1:1\n\tds_read2st64_b32 v[178:179], v161 offset1:1\n\tds_read2st64_b32 v[180:181], v162 offset1:1\n\tds_read2st64_b32 v[182:183], v163 offset1:1\n\t"
            "ds_read2st64_b32 v[184:185], v164 offset1:1\n\tds_read2st64_b32 v[186:187], v165 offset1:1\n\tds_read2st64_b32 v[188:189], v166 offset1:1\n\tds_read2st64_b32 v[190:191], v167 offset1:1\n\ts_mov_b32 s68, 0x40000\n\ts_waitcnt lgkmcnt(8)\n\t"
            "v_mul_f32 v138, 0x3ea52b5c, v138\n\tv_mul_f32 v139, 0x3ea52b5c, v139\n\ts_waitcnt lgkmcnt(7)\n\tv_mul_f32 v192, v176, v138\n\tv_mul_f32 v200, v177, v139\n\ts_waitcnt lgkmcnt(6)\n\t"
            "v_mul_f32 v193, v178, v138\n\tv_mul_f32 v201, v179, v139\n\ts_waitcnt lgkmcnt(5)\n\tv_mul_f32 v194, v180, v138\n\tv_mul_f32 v202, v181, v139\n\ts_waitcnt lgkmcnt(4)\n\t"
            "v_mul_f32 v195, v182, v138\n\tv_mul_f32 v203, v183, v139\n\ts_waitcnt lgkmcnt(3)\n\tv_mul_f32 v196, v184, v138\n\tv_mul_f32 v204, v185, v139\n\ts_waitcnt lgkmcnt(2)\n\t"
            "v_mul_f32 v197, v186, v138\n\tv_mul_f32 v205, v187, v139\n\ts_waitcnt lgkmcnt(1)\n\tv_mul_f32 v198, v188, v138\n\tv_mul_f32 v206, v189, v139\n\ts_waitcnt lgkmcnt(0)\n\t"
            "v_mul_f32 v199, v190, v138\n\tv_mul_f32 v207, v191, v139\n\ts_mov_b32 s19, 0\n\ts_barrier\n\t"
            SB_(1) PRIS
            "50:\nds_read2_b64 v[208:211], v24 offset0:0 offset1:16\n\tds_read2_b64 v[212:215], v24 offset0:32 offset1:48\n\tds_read2_b64 v[216:219], v26 offset0:0 offset1:16\n\tds_read2_b64 v[220:223], v26 offset0:32 offset1:48\n\ts_cmp_eq_u32 s16, 0\n\t"
            "s_cbranch_scc1 56f\n\t58:\ns_cbranch_scc0 1f\n\tbuffer_load_dword v232, v31, s[44:47], s31 offen offset:0 sc1\n\tbuffer_load_dword v233, v31, s[44:47], s31 offen offset:256 sc1\n\tbuffer_load_dword v234, v31, s[44:47], s31 offen offset:512 sc1\n\t"
            "buffer_load_dword v235, v31, s[44:47], s31 offen offset:768 sc1\n\t1:\ns_waitcnt lgkmcnt(1)\n\tv_add_f32 v208, v208, v216\n\tv_add_f32 v209, v209, v217\n\tv_add_f32 v210, v210, v218\n\t"
            "v_add_f32 v211, v211, v219\n\ts_waitcnt lgkmcnt(0)\n\tv_add_f32 v212, v212, v220\n\tv_add_f32 v213, v213, v221\n\tv_add_f32 v214, v214, v222\n\tv_add_f32 v215, v215, v223\n\t"
            "v_fma_f32 v208, v208, v192, v128\n\tv_fma_f32 v209, v209, v193, v129\n\tv_fma_f32 v210, v210, v194, v130\n\tv_fma_f32 v211, v211, v195, v131\n\tv_fma_f32 v212, v212, v196, v132\n\tv_fma_f32 v213, v213, v197, v133\n\t"
            "v_fma_f32 v214, v214, v198, v134\n\tv_fma_f32 v215, v215, v199, v135\n\tv_max3_f32 v136, v208, v209, v210\n\tv_max3_f32 v137, v211, v212, v213\n\tv_max3_f32 v136, v136, v214, v215\n\tv_max_f32 v136, v136, v137\n\t"
            "v_sub_f32 v208, v208, v136\n\tv_sub_f32 v209, v209, v136\n\tv_sub_f32 v210, v210, v136\n\tv_sub_f32 v211, v211, v136\n\tv_sub_f32 v212, v212, v136\n\tv_sub_f32 v213, v213, v136\n\t"
            "v_sub_f32 v214, v214, v136\n\tv_sub_f32 v215, v215, v136\n\tv_exp_f32 v208, v208\n\tv_exp_f32 v209, v209\n\tv_exp_f32 v210, v210\n\tv_exp_f32 v211, v211\n\t"
            "v_exp_f32 v212, v212\n\tv_exp_f32 v213, v213\n\tv_exp_f32 v214, v214\n\tv_exp_f32 v215, v215\n\tv_add_f32 v216, v208, v209\n\tv_add_f32 v217, v210, v211\n\t"
            "v_add_f32 v218, v212, v213\n\tv_add_f32 v219, v214, v215\n\tv_add_f32 v216, v216, v217\n\tv_add_f32 v218, v218, v219\n\tv_mul_f32 v208, v208, v176\n\tv_add_f32 v216, v216, v218\n\t"
            "v_mul_f32 v216, 0xbfb8aa3b, v216\n\tv_rcp_f32 v216, v216\n\tv_mul_f32 v209, v209, v178\n\tv_mul_f32 v210, v210, v180\n\tv_mul_f32 v211, v211, v182\n\tv_mul_f32 v212, v212, v184\n\t"
            "v_mul_f32 v213, v213, v186\n\tv_mul_f32 v214, v214, v188\n\tv_mul_f32 v215, v215, v190\n\tv_mul_f32 v208, v208, v216\n\tv_mul_f32 v209, v209, v216\n\tv_mul_f32 v210, v210, v216\n\t"
            "v_mul_f32 v211, v211, v216\n\tv_mul_f32 v212, v212, v216\n\tv_mul_f32 v213, v213, v216\n\tv_mul_f32 v214, v214, v216\n\tv_mul_f32 v215, v215, v216\n\tv_cvt_pk_bf16_f32 v220, v208, v209\n\t"
            "v_cvt_pk_bf16_f32 v221, v210, v211\n\tv_cvt_pk_bf16_f32 v222, v212, v213\n\tv_cvt_pk_bf16_f32 v223, v214, v215\n\tds_write_b128 v29, v[220:223] offset:0\n\tds_write_b32 v30, v142 offset:16\n\t"
            SP0 PRIW
            "51:\nds_read_b32 v136, v28\n\tds_read2_b64 v[208:211], v25 offset0:0 offset1:16\n\tds_read2_b64 v[212:215], v25 offset0:32 offset1:48\n\tds_read2_b64 v[216:219], v27 offset0:0 offset1:16\n\tds_read2_b64 v[220:223], v27 offset0:32 offset1:48\n\t"
            "s_waitcnt lgkmcnt(4)\n\tv_cmp_eq_u32 vcc, 0, v136\n\ts_cbranch_vccnz 53f\n\t55:\n"
            PRI1
            "s_cmp_eq_u32 s16, 0\n\ts_cbranch_scc1 57f\n\t59:\ns_waitcnt lgkmcnt(1)\n\tv_add_f32 v208, v208, v216\n\tv_add_f32 v209, v209, v217\n\t"
            "v_add_f32 v210, v210, v218\n\tv_add_f32 v211, v211, v219\n\ts_waitcnt lgkmcnt(0)\n\tv_add_f32 v212, v212, v220\n\tv_add_f32 v213, v213, v221\n\tv_add_f32 v214, v214, v222\n\t"
            "v_add_f32 v215, v215, v223\n\tv_mul_f32 v208, v208, v200\n\tv_mul_f32 v209, v209, v201\n\tv_mul_f32 v210, v210, v202\n\tv_mul_f32 v211, v211, v203\n\tv_mul_f32 v212, v212, v204\n\t"
            "v_mul_f32 v213, v213, v205\n\tv_mul_f32 v214, v214, v206\n\tv_mul_f32 v215, v215, v207\n\tv_max3_f32 v136, v208, v209, v210\n\tv_max3_f32 v137, v211, v212, v213\n\tv_max3_f32 v136, v136, v214, v215\n\t"
            "v_max_f32 v136, v136, v137\n\tv_sub_f32 v208, v208, v136\n\tv_sub_f32 v209, v209, v136\n\tv_sub_f32 v210, v210, v136\n\tv_sub_f32 v211, v211, v136\n\tv_sub_f32 v212, v212, v136\n\t"
            "v_sub_f32 v213, v213, v136\n\tv_sub_f32 v214, v214, v136\n\tv_sub_f32 v215, v215, v136\n\tv_exp_f32 v208, v208\n\tv_exp_f32 v209, v209\n\tv_exp_f32 v210, v210\n\t"
            "v_exp_f32 v211, v211\n\tv_exp_f32 v212, v212\n\tv_exp_f32 v213, v213\n\tv_exp_f32 v214, v214\n\tv_exp_f32 v215, v215\n\tv_add_f32 v216, v208, v209\n\t"
            "v_add_f32 v217, v210, v211\n\tv_add_f32 v218, v212, v213\n\tv_add_f32 v219, v214, v215\n\tv_add_f32 v216, v216, v217\n\tv_add_f32 v218, v218, v219\n\tv_mul_f32 v208, v208, v177\n\t"
            "v_add_f32 v216, v216, v218\n\tv_mul_f32 v216, 0xbfb8aa3b, v216\n\tv_rcp_f32 v216, v216\n\tv_mul_f32 v209, v209, v179\n\tv_mul_f32 v210, v210, v181\n\tv_mul_f32 v211, v211, v183\n\t"
            "v_mul_f32 v212, v212, v185\n\tv_mul_f32 v213, v213, v187\n\tv_mul_f32 v214, v214, v189\n\tv_mul_f32 v215, v215, v191\n\tv_mul_f32 v208, v208, v216\n\tv_mul_f32 v209, v209, v216\n\t"
            "v_mul_f32 v210, v210, v216\n\tv_mul_f32 v211, v211, v216\n\tv_mul_f32 v212, v212, v216\n\tv_mul_f32 v213, v213, v216\n\tv_mul_f32 v214, v214, v216\n\tv_mul_f32 v215, v215, v216\n\t"
            "v_cvt_pk_bf16_f32 v220, v208, v209\n\tv_cvt_pk_bf16_f32 v221, v210, v211\n\tv_cvt_pk_bf16_f32 v222, v212, v213\n\tv_cvt_pk_bf16_f32 v223, v214, v215\n\tds_write_b128 v29, v[220:223] offset:38912\n\tds_write_b32 v30, v30 offset:16\n\t"
            SB_(2) PRIG
            "s_waitcnt vmcnt(0)\n\tds_write_b32 v30, v30 offset:48\n\ts_branch .LOL\n\t30:\nbuffer_load_dwordx4 v[106:109], v158, s[44:47], 0 offen sc1\n\tbuffer_load_dwordx4 v[110:113], v159, s[44:47], 0 offen sc1\n\t"
            "buffer_load_dwordx4 v[114:117], v104, s[44:47], 0 offen sc1\n\tbuffer_load_dword v118, v96, s[44:47], 0 offen sc1\n\t"
            GINC
            "s_waitcnt vmcnt(3)\n\tv_xor_b32 v106, s67, v107\n\tv_xor_b32 v108, s67, v109\n\ts_waitcnt vmcnt(2)\n\tv_xor_b32 v110, s67, v111\n\tv_xor_b32 v112, s67, v113\n\t"
            "s_waitcnt vmcnt(1)\n\tv_xor_b32 v114, s67, v115\n\tv_xor_b32 v116, s67, v117\n\ts_waitcnt vmcnt(0)\n\tv_xor_b32 v118, s67, v118\n\tv_or3_b32 v106, v106, v108, v110\n\t"
            "v_or3_b32 v118, v118, v114, v116\n\tv_or3_b32 v106, v106, v112, v118\n\tv_cmp_ne_u32 vcc, 0, v106\n\ts_cbranch_vccz 31b\n\ts_sub_u32 s68, s68, 1\n\ts_cbranch_scc0 30b\n\t"
            "s_branch 31b\n\t53:\n"
            PSLP
            "s_sub_u32 s68, s68, 1\n\ts_cbranch_scc0 51b\n\ts_branch 55b\n\t56:\n"
            RSL0
            "buffer_load_dword v224, v31, s[44:47], 0 offen offset:0 sc1\n\tbuffer_load_dword v225, v31, s[44:47], 0 offen offset:256 sc1\n\tbuffer_load_dword v226, v31, s[44:47], 0 offen offset:512 sc1\n\tbuffer_load_dword v227, v31, s[44:47], 0 offen offset:768 sc1\n\ts_waitcnt vmcnt(4)\n\t"
            PRIR
            "s_call_b64 s[76:77], .LG0\n\t"
            PRIX
            "s_branch 2f\n\t1:\ns_waitcnt vmcnt(3)\n\tbuffer_load_dword v224, v31, s[44:47], 0 offen offset:0 sc1\n\ts_waitcnt vmcnt(3)\n\tbuffer_load_dword v225, v31, s[44:47], 0 offen offset:256 sc1\n\t"
            "s_waitcnt vmcnt(3)\n\tbuffer_load_dword v226, v31, s[44:47], 0 offen offset:512 sc1\n\ts_waitcnt vmcnt(3)\n\tbuffer_load_dword v227, v31, s[44:47], 0 offen offset:768 sc1\n\ts_cmp_lg_u32 s19, 0\n\ts_cbranch_scc1 3f\n\t"
            "s_call_b64 s[76:77], .LG1\n\t3:\ns_cmp_eq_u32 s19, 2\n\ts_cbranch_scc1 2f\n\ts_call_b64 s[76:77], .LG2\n\t2:\n"
            "s_waitcnt vmcnt(0) lgkmcnt(0)\n\tds_write_b32 v30, v30 offset:48\n\tv_max3_u32 v136, v224, v225, v226\n\tv_max_u32 v136, v136, v227\n\tv_cmp_eq_u32 vcc, -1, v136\n\tv_lshlrev_b32 v216, 16, v224\n\t"
            "v_and_b32 v217, 0xffff0000, v224\n\tv_lshlrev_b32 v218, 16, v225\n\tv_and_b32 v219, 0xffff0000, v225\n\tv_lshlrev_b32 v220, 16, v226\n\tv_and_b32 v221, 0xffff0000, v226\n\tv_lshlrev_b32 v222, 16, v227\n\t"
            "v_and_b32 v223, 0xffff0000, v227\n\t"
            LINC
            "s_cmp_eq_u32 0, 0\n\ts_cbranch_vccz 58b\n\ts_sub_u32 s68, s68, 1\n\ts_cbranch_scc0 1b\n\ts_branch 58b\n\t57:\n"
            "s_branch 2f\n\t1:\nbuffer_load_dword v232, v31, s[44:47], s31 offen offset:0 sc1\n\tbuffer_load_dword v233, v31, s[44:47], s31 offen offset:256 sc1\n\tbuffer_load_dword v234, v31, s[44:47], s31 offen offset:512 sc1\n\tbuffer_load_dword v235, v31, s[44:47], s31 offen offset:768 sc1\n\t"
            "s_cmp_lg_u32 s19, 0\n\ts_cbranch_scc1 3f\n\ts_call_b64 s[76:77], .LG1\n\t3:\ns_cmp_eq_u32 s19, 2\n\ts_cbranch_scc1 2f\n\t"
            "s_call_b64 s[76:77], .LG2\n\t2:\ns_waitcnt vmcnt(0) lgkmcnt(0)\n\tv_max3_u32 v136, v232, v233, v234\n\tv_max_u32 v136, v136, v235\n\tv_cmp_eq_u32 vcc, -1, v136\n\t"
            "v_lshlrev_b32 v216, 16, v232\n\tv_and_b32 v217, 0xffff0000, v232\n\tv_lshlrev_b32 v218, 16, v233\n\tv_and_b32 v219, 0xffff0000, v233\n\tv_lshlrev_b32 v220, 16, v234\n\tv_and_b32 v221, 0xffff0000, v234\n\t"
            "v_lshlrev_b32 v222, 16, v235\n\tv_and_b32 v223, 0xffff0000, v235\n\t"
            LINC
            "s_cmp_eq_u32 0, 0\n\ts_cbranch_vccz 59b\n\ts_sub_u32 s68, s68, 1\n\ts_cbranch_scc0 1b\n\ts_branch 59b\n\t.LOL:\n"
            "s_mov_b32 s71, 1\n\ts_mov_b32 s68, 0x40000\n\ts_cmp_eq_u32 s16, 0\n\ts_cbranch_scc1 3f\n\ts_call_b64 s[76:77], .LG0\n\t3:\n"
            "ds_read_b64 v[14:15], v8 offset:32\n\ts_waitcnt lgkmcnt(0)\n\tv_min_u32 v12, v14, v15\n\tv_cmp_lt_u32 vcc, s71, v12\n\ts_cbranch_vccnz 4f\n\ts_sleep 1\n\t"
            "s_sub_u32 s68, s68, 1\n\ts_cbranch_scc0 3b\n\t4:\ns_cmp_eq_u32 s16, 0\n\ts_cbranch_scc1 1f\n\ts_call_b64 s[76:77], .LG1\n\t"
            "1:\ns_cmp_lt_u32 s56, 2\n\ts_cbranch_scc1 1f\n\ts_sleep 3\n\t1:\n"
            SO(4) RZ
            "buffer_load_dwordx4 v[176:179], v2, s[44:47], s4 offen offset:1024 sc1\n\tbuffer_load_dwordx4 v[180:183], v2, s[44:47], s4 offen offset:2048 sc1\n\tbuffer_load_dwordx4 v[184:187], v2, s[44:47], s5 offen offset:1024 sc1\n\tbuffer_load_dwordx4 v[188:191], v2, s[44:47], s5 offen offset:2048 sc1\n\tbuffer_load_dwordx4 v[192:195], v2, s[44:47], s6 offen offset:1024 sc1\n\tbuffer_load_dwordx4 v[196:199], v2, s[44:47], s6 offen offset:2048 sc1\n\t"
            "s_cmp_lg_u32 s19, 0\n\ts_cbranch_scc1 1f\n\ts_call_b64 s[76:77], .LG1\n\t1:\ns_cmp_eq_u32 s19, 2\n\ts_cbranch_scc1 1f\n\t"
            "s_call_b64 s[76:77], .LG2\n\t1:\nbuffer_load_dwordx4 v[200:203], v2, s[44:47], s7 offen offset:1024 sc1\n\tbuffer_load_dwordx4 v[204:207], v2, s[44:47], s7 offen offset:2048 sc1\n\tbuffer_load_dwordx4 v[208:211], v2, s[44:47], s8 offen offset:1024 sc1\n\tbuffer_load_dwordx4 v[212:215], v2, s[44:47], s8 offen offset:2048 sc1\n\t"
            "ds_read_b128 v[16:19], v8 offset:48\n\tds_read_b64_tr_b16 v[128:129], v4 offset:40960\n\tds_read_b64_tr_b16 v[130:131], v4 offset:41472\n\tds_read_b64_tr_b16 v[132:133], v5 offset:40960\n\tds_read_b64_tr_b16 v[134:135], v5 offset:41472\n\tds_read_b64_tr_b16 v[136:137], v6 offset:40960\n\t"
            "ds_read_b64_tr_b16 v[138:139], v6 offset:41472\n\tds_read_b64_tr_b16 v[140:141], v7 offset:40960\n\tds_read_b64_tr_b16 v[142:143], v7 offset:41472\n\tds_read_b64_tr_b16 v[144:145], v4 offset:41984\n\tds_read_b64_tr_b16 v[146:147], v4 offset:42496\n\tds_read_b64_tr_b16 v[148:149], v5 offset:41984\n\t"
            "ds_read_b64_tr_b16 v[150:151], v5 offset:42496\n\tds_read_b64_tr_b16 v[152:153], v6 offset:41984\n\tds_read_b64_tr_b16 v[154:155], v6 offset:42496\n\tds_read_b64_tr_b16 v[156:157], v7 offset:41984\n\tds_read_b64_tr_b16 v[158:159], v7 offset:42496\n\tv_mul_f32 v48, v48, v112\n\t"
            "v_mul_f32 v49, v49, v113\n\tv_mul_f32 v50, v50, v114\n\tv_mul_f32 v51, v51, v115\n\tv_mul_f32 v52, v52, v116\n\tv_mul_f32 v53, v53, v117\n\tv_mul_f32 v54, v54, v118\n\t"
            "v_mul_f32 v55, v55, v119\n\tv_mul_f32 v56, v56, v120\n\tv_mul_f32 v57, v57, v121\n\tv_mul_f32 v58, v58, v122\n\tv_mul_f32 v59, v59, v123\n\tv_mul_f32 v60, v60, v124\n\t"
            "v_mul_f32 v61, v61, v125\n\tv_mul_f32 v62, v62, v126\n\tv_mul_f32 v63, v63, v127\n\t"
            SO(3)
            "s_waitcnt lgkmcnt(15)\n\tv_min3_u32 v16, v16, v17, v18\n\tv_min_u32 v16, v16, v19\n\tv_cmp_eq_u32 vcc, 0, v16\n\ts_cbranch_vccnz 62f\n\t63:\n"
            PRIO
            "s_waitcnt vmcnt(8)\n\tv_max3_u32 v12, v176, v178, v180\n\tv_max_u32 v12, v12, v182\n\tv_cmp_eq_u32 vcc, -1, v12\n\ts_cbranch_vccnz 100f\n\t200:\n"
            SO(5)
            "s_waitcnt lgkmcnt(8)\n\tv_mfma_f32_32x32x16_bf16 v[96:111], v[128:131], v[176:179], 0\n\tds_read_b64_tr_b16 v[160:161], v4 offset:43008\n\tds_read_b64_tr_b16 v[162:163], v4 offset:43520\n\tv_mfma_f32_32x32x16_bf16 v[112:127], v[132:135], v[176:179], 0\n\tds_read_b64_tr_b16 v[164:165], v5 offset:43008\n\t"
            "ds_read_b64_tr_b16 v[166:167], v5 offset:43520\n\tbuffer_load_dwordx4 v[216:219], v2, s[44:47], s9 offen offset:1024 sc1\n\tv_mfma_f32_32x32x16_bf16 v[96:111], v[136:139], v[180:183], v[96:111]\n\tds_read_b64_tr_b16 v[168:169], v6 offset:43008\n\tds_read_b64_tr_b16 v[170:171], v6 offset:43520\n\tbuffer_load_dwordx4 v[220:223], v2, s[44:47], s9 offen offset:2048 sc1\n\t"
            "s_waitcnt vmcnt(8)\n\tv_max3_u32 v12, v184, v186, v188\n\tv_max_u32 v12, v12, v190\n\tv_cmp_eq_u32 vcc, -1, v12\n\tv_mfma_f32_32x32x16_bf16 v[112:127], v[140:143], v[180:183], v[112:127]\n\tds_read_b64_tr_b16 v[172:173], v7 offset:43008\n\t"
            "ds_read_b64_tr_b16 v[174:175], v7 offset:43520\n\ts_cbranch_vccnz 101f\n\t201:\ns_waitcnt lgkmcnt(8)\n\tv_mfma_f32_32x32x16_bf16 v[96:111], v[144:147], v[184:187], v[96:111]\n\tds_read_b64_tr_b16 v[128:129], v4 offset:44032\n\t"
            "ds_read_b64_tr_b16 v[130:131], v4 offset:44544\n\tv_mfma_f32_32x32x16_bf16 v[112:127], v[148:151], v[184:187], v[112:127]\n\tds_read_b64_tr_b16 v[132:133], v5 offset:44032\n\tds_read_b64_tr_b16 v[134:135], v5 offset:44544\n\tbuffer_load_dwordx4 v[224:227], v2, s[44:47], s10 offen offset:1024 sc1\n\tv_mfma_f32_32x32x16_bf16 v[96:111], v[152:155], v[188:191], v[96:111]\n\t"
            "ds_read_b64_tr_b16 v[136:137], v6 offset:44032\n\tds_read_b64_tr_b16 v[138:139], v6 offset:44544\n\tbuffer_load_dwordx4 v[228:231], v2, s[44:47], s10 offen offset:2048 sc1\n\ts_waitcnt vmcnt(8)\n\tv_max3_u32 v12, v192, v194, v196\n\tv_max_u32 v12, v12, v198\n\t"
            "v_cmp_eq_u32 vcc, -1, v12\n\tv_mfma_f32_32x32x16_bf16 v[112:127], v[156:159], v[188:191], v[112:127]\n\tds_read_b64_tr_b16 v[140:141], v7 offset:44032\n\tds_read_b64_tr_b16 v[142:143], v7 offset:44544\n\ts_cbranch_vccnz 102f\n\t202:\n"
            "s_waitcnt lgkmcnt(8)\n\tv_mfma_f32_32x32x16_bf16 v[96:111], v[160:163], v[192:195], v[96:111]\n\tds_read_b64_tr_b16 v[144:145], v4 offset:45056\n\tds_read_b64_tr_b16 v[146:147], v4 offset:45568\n\tv_mfma_f32_32x32x16_bf16 v[112:127], v[164:167], v[192:195], v[112:127]\n\tds_read_b64_tr_b16 v[148:149], v5 offset:45056\n\t"
            "ds_read_b64_tr_b16 v[150:151], v5 offset:45568\n\tbuffer_load_dwordx4 v[232:235], v2, s[44:47], s11 offen offset:1024 sc1\n\tv_mfma_f32_32x32x16_bf16 v[96:111], v[168:171], v[196:199], v[96:111]\n\tds_read_b64_tr_b16 v[152:153], v6 offset:45056\n\tds_read_b64_tr_b16 v[154:155], v6 offset:45568\n\tbuffer_load_dwordx4 v[236:239], v2, s[44:47], s11 offen offset:2048 sc1\n\t"
            "s_waitcnt vmcnt(8)\n\tv_max3_u32 v12, v200, v202, v204\n\tv_max_u32 v12, v12, v206\n\tv_cmp_eq_u32 vcc, -1, v12\n\tv_mfma_f32_32x32x16_bf16 v[112:127], v[172:175], v[196:199], v[112:127]\n\tds_read_b64_tr_b16 v[156:157], v7 offset:45056\n\t"
            "ds_read_b64_tr_b16 v[158:159], v7 offset:45568\n\ts_cbranch_vccnz 103f\n\t203:\ns_waitcnt lgkmcnt(8)\n\tv_mfma_f32_32x32x16_bf16 v[96:111], v[128:131], v[200:203], v[96:111]\n\tds_read_b64_tr_b16 v[160:161], v4 offset:46080\n\t"
            "ds_read_b64_tr_b16 v[162:163], v4 offset:46592\n\tv_mfma_f32_32x32x16_bf16 v[112:127], v[132:135], v[200:203], v[112:127]\n\tds_read_b64_tr_b16 v[164:165], v5 offset:46080\n\tds_read_b64_tr_b16 v[166:167], v5 offset:46592\n\tds_read_b64 v[14:15], v8 offset:40\n\tbuffer_load_dwordx4 v[176:179], v2, s[44:47], s12 offen offset:1024 sc1\n\t"
            "v_mfma_f32_32x32x16_bf16 v[96:111], v[136:139], v[204:207], v[96:111]\n\tds_read_b64_tr_b16 v[168:169], v6 offset:46080\n\tds_read_b64_tr_b16 v[170:171], v6 offset:46592\n\tbuffer_load_dwordx4 v[180:183], v2, s[44:47], s12 offen offset:2048 sc1\n\ts_waitcnt vmcnt(8)\n\tv_max3_u32 v12, v208, v210, v212\n\t"
            "v_max_u32 v12, v12, v214\n\tv_cmp_eq_u32 vcc, -1, v12\n\tv_mfma_f32_32x32x16_bf16 v[112:127], v[140:143], v[204:207], v[112:127]\n\tds_read_b64_tr_b16 v[172:173], v7 offset:46080\n\tds_read_b64_tr_b16 v[174:175], v7 offset:46592\n\ts_cbranch_vccnz 104f\n\t"
            "204:\ns_waitcnt lgkmcnt(9)\n\tv_mfma_f32_32x32x16_bf16 v[96:111], v[144:147], v[208:211], v[96:111]\n\tds_read_b64_tr_b16 v[128:129], v4 offset:47104\n\tds_read_b64_tr_b16 v[130:131], v4 offset:47616\n\tv_mfma_f32_32x32x16_bf16 v[112:127], v[148:151], v[208:211], v[112:127]\n\t"
            "ds_read_b64_tr_b16 v[132:133], v5 offset:47104\n\tds_read_b64_tr_b16 v[134:135], v5 offset:47616\n\tbuffer_load_dwordx4 v[184:187], v2, s[44:47], s13 offen offset:1024 sc1\n\tv_mfma_f32_32x32x16_bf16 v[96:111], v[152:155], v[212:215], v[96:111]\n\tds_read_b64_tr_b16 v[136:137], v6 offset:47104\n\tds_read_b64_tr_b16 v[138:139], v6 offset:47616\n\t"
            "buffer_load_dwordx4 v[188:191], v2, s[44:47], s13 offen offset:2048 sc1\n\ts_waitcnt vmcnt(8)\n\tv_max3_u32 v12, v216, v218, v220\n\tv_max_u32 v12, v12, v222\n\tv_cmp_eq_u32 vcc, -1, v12\n\tv_mfma_f32_32x32x16_bf16 v[112:127], v[156:159], v[212:215], v[112:127]\n\t"
            "ds_read_b64_tr_b16 v[140:141], v7 offset:47104\n\tds_read_b64_tr_b16 v[142:143], v7 offset:47616\n\ts_cbranch_vccnz 105f\n\t205:\ns_waitcnt lgkmcnt(8)\n\tv_mfma_f32_32x32x16_bf16 v[96:111], v[160:163], v[216:219], v[96:111]\n\t"
            "v_min_u32 v14, v14, v15\n\tv_cmp_ge_u32 vcc, s71, v14\n\tds_read_b64_tr_b16 v[144:145], v4 offset:48128\n\tds_read_b64_tr_b16 v[146:147], v4 offset:48640\n\tv_mfma_f32_32x32x16_bf16 v[112:127], v[164:167], v[216:219], v[112:127]\n\ts_cbranch_vccnz 5f\n\t"
            "6:\nds_read_b64_tr_b16 v[148:149], v5 offset:48128\n\tds_read_b64_tr_b16 v[150:151], v5 offset:48640\n\tbuffer_load_dwordx4 v[192:195], v2, s[44:47], s4 offen offset:0 sc1\n\tv_mfma_f32_32x32x16_bf16 v[96:111], v[168:171], v[220:223], v[96:111]\n\tds_read_b64_tr_b16 v[152:153], v6 offset:48128\n\t"
            "ds_read_b64_tr_b16 v[154:155], v6 offset:48640\n\tbuffer_load_dwordx4 v[196:199], v2, s[44:47], s4 offen offset:3072 sc1\n\ts_waitcnt vmcnt(8)\n\tv_max3_u32 v12, v224, v226, v228\n\tv_max_u32 v12, v12, v230\n\tv_cmp_eq_u32 vcc, -1, v12\n\t"
            "v_mfma_f32_32x32x16_bf16 v[112:127], v[172:175], v[220:223], v[112:127]\n\tds_read_b64_tr_b16 v[156:157], v7 offset:48128\n\tds_read_b64_tr_b16 v[158:159], v7 offset:48640\n\ts_cbranch_vccnz 106f\n\t206:\ns_waitcnt lgkmcnt(8)\n\t"
            "v_mfma_f32_32x32x16_bf16 v[96:111], v[128:131], v[224:227], v[96:111]\n\tds_read_b64_tr_b16 v[160:161], v4 offset:49152\n\tds_read_b64_tr_b16 v[162:163], v4 offset:49664\n\tv_mfma_f32_32x32x16_bf16 v[112:127], v[132:135], v[224:227], v[112:127]\n\tds_read_b64_tr_b16 v[164:165], v5 offset:49152\n\tds_read_b64_tr_b16 v[166:167], v5 offset:49664\n\t"
            "buffer_load_dwordx4 v[200:203], v2, s[44:47], s5 offen offset:0 sc1\n\tv_mfma_f32_32x32x16_bf16 v[96:111], v[136:139], v[228:231], v[96:111]\n\tds_read_b64_tr_b16 v[168:169], v6 offset:49152\n\tds_read_b64_tr_b16 v[170:171], v6 offset:49664\n\tbuffer_load_dwordx4 v[204:207], v2, s[44:47], s5 offen offset:3072 sc1\n\ts_waitcnt vmcnt(8)\n\t"
            "v_max3_u32 v12, v232, v234, v236\n\tv_max_u32 v12, v12, v238\n\tv_cmp_eq_u32 vcc, -1, v12\n\tv_mfma_f32_32x32x16_bf16 v[112:127], v[140:143], v[228:231], v[112:127]\n\tds_read_b64_tr_b16 v[172:173], v7 offset:49152\n\tds_read_b64_tr_b16 v[174:175], v7 offset:49664\n\t"
            "s_cbranch_vccnz 107f\n\t207:\ns_waitcnt lgkmcnt(8)\n\tv_mfma_f32_32x32x16_bf16 v[96:111], v[144:147], v[232:235], v[96:111]\n\tds_read_b64_tr_b16 v[128:129], v4 offset:50176\n\tds_read_b64_tr_b16 v[130:131], v4 offset:50688\n\t"
            "v_mfma_f32_32x32x16_bf16 v[112:127], v[148:151], v[232:235], v[112:127]\n\tds_read_b64_tr_b16 v[132:133], v5 offset:50176\n\tds_read_b64_tr_b16 v[134:135], v5 offset:50688\n\tbuffer_load_dwordx4 v[208:211], v2, s[44:47], s6 offen offset:0 sc1\n\tv_mfma_f32_32x32x16_bf16 v[96:111], v[152:155], v[236:239], v[96:111]\n\tds_read_b64_tr_b16 v[136:137], v6 offset:50176\n\t"
            "ds_read_b64_tr_b16 v[138:139], v6 offset:50688\n\tbuffer_load_dwordx4 v[212:215], v2, s[44:47], s6 offen offset:3072 sc1\n\ts_waitcnt vmcnt(8)\n\tv_max3_u32 v12, v176, v178, v180\n\tv_max_u32 v12, v12, v182\n\tv_cmp_eq_u32 vcc, -1, v12\n\t"
            "v_mfma_f32_32x32x16_bf16 v[112:127], v[156:159], v[236:239], v[112:127]\n\tds_read_b64_tr_b16 v[140:141], v7 offset:50176\n\tds_read_b64_tr_b16 v[142:143], v7 offset:50688\n\ts_cbranch_vccnz 108f\n\t208:\ns_waitcnt lgkmcnt(8)\n\t"
            "v_mfma_f32_32x32x16_bf16 v[96:111], v[160:163], v[176:179], v[96:111]\n\tds_read_b64_tr_b16 v[144:145], v4 offset:0\n\tds_read_b64_tr_b16 v[146:147], v4 offset:512\n\tv_mfma_f32_32x32x16_bf16 v[112:127], v[164:167], v[176:179], v[112:127]\n\tds_read_b64_tr_b16 v[148:149], v5 offset:0\n\tds_read_b64_tr_b16 v[150:151], v5 offset:512\n\t"
            "buffer_load_dwordx4 v[216:219], v2, s[44:47], s7 offen offset:0 sc1\n\tv_mfma_f32_32x32x16_bf16 v[96:111], v[168:171], v[180:183], v[96:111]\n\tds_read_b64_tr_b16 v[152:153], v6 offset:0\n\tds_read_b64_tr_b16 v[154:155], v6 offset:512\n\tbuffer_load_dwordx4 v[220:223], v2, s[44:47], s7 offen offset:3072 sc1\n\ts_waitcnt vmcnt(8)\n\t"
            "v_max3_u32 v12, v184, v186, v188\n\tv_max_u32 v12, v12, v190\n\tv_cmp_eq_u32 vcc, -1, v12\n\tv_mfma_f32_32x32x16_bf16 v[112:127], v[172:175], v[180:183], v[112:127]\n\tds_read_b64_tr_b16 v[156:157], v7 offset:0\n\tds_read_b64_tr_b16 v[158:159], v7 offset:512\n\t"
            "s_cbranch_vccnz 109f\n\t209:\ns_waitcnt lgkmcnt(8)\n\tv_mfma_f32_32x32x16_bf16 v[96:111], v[128:131], v[184:187], v[96:111]\n\tds_read_b64_tr_b16 v[160:161], v4 offset:1024\n\tds_read_b64_tr_b16 v[162:163], v4 offset:1536\n\t"
            "v_mfma_f32_32x32x16_bf16 v[112:127], v[132:135], v[184:187], v[112:127]\n\tds_read_b64_tr_b16 v[164:165], v5 offset:1024\n\tds_read_b64_tr_b16 v[166:167], v5 offset:1536\n\tbuffer_load_dwordx4 v[224:227], v2, s[44:47], s8 offen offset:0 sc1\n\tv_mfma_f32_32x32x16_bf16 v[96:111], v[136:139], v[188:191], v[96:111]\n\tds_read_b64_tr_b16 v[168:169], v6 offset:1024\n\t"
            "ds_read_b64_tr_b16 v[170:171], v6 offset:1536\n\tbuffer_load_dwordx4 v[228:231], v2, s[44:47], s8 offen offset:3072 sc1\n\ts_waitcnt vmcnt(8)\n\tv_max3_u32 v12, v192, v194, v196\n\tv_max_u32 v12, v12, v198\n\tv_cmp_eq_u32 vcc, -1, v12\n\t"
            "v_mfma_f32_32x32x16_bf16 v[112:127], v[140:143], v[188:191], v[112:127]\n\tds_read_b64_tr_b16 v[172:173], v7 offset:1024\n\tds_read_b64_tr_b16 v[174:175], v7 offset:1536\n\ts_cbranch_vccnz 110f\n\t210:\ns_waitcnt lgkmcnt(8)\n\t"
            "v_mfma_f32_32x32x16_bf16 v[96:111], v[144:147], v[192:195], v[96:111]\n\tds_read_b64_tr_b16 v[128:129], v4 offset:2048\n\tds_read_b64_tr_b16 v[130:131], v4 offset:2560\n\tv_mfma_f32_32x32x16_bf16 v[112:127], v[148:151], v[192:195], v[112:127]\n\tds_read_b64_tr_b16 v[132:133], v5 offset:2048\n\tds_read_b64_tr_b16 v[134:135], v5 offset:2560\n\t"
            "buffer_load_dwordx4 v[232:235], v2, s[44:47], s9 offen offset:0 sc1\n\tv_mfma_f32_32x32x16_bf16 v[96:111], v[152:155], v[196:199], v[96:111]\n\tds_read_b64_tr_b16 v[136:137], v6 offset:2048\n\tds_read_b64_tr_b16 v[138:139], v6 offset:2560\n\tbuffer_load_dwordx4 v[236:239], v2, s[44:47], s9 offen offset:3072 sc1\n\ts_waitcnt vmcnt(8)\n\t"
            "v_max3_u32 v12, v200, v202, v204\n\tv_max_u32 v12, v12, v206\n\tv_cmp_eq_u32 vcc, -1, v12\n\tv_mfma_f32_32x32x16_bf16 v[112:127], v[156:159], v[196:199], v[112:127]\n\tds_read_b64_tr_b16 v[140:141], v7 offset:2048\n\tds_read_b64_tr_b16 v[142:143], v7 offset:2560\n\t"
            "s_cbranch_vccnz 111f\n\t211:\ns_waitcnt lgkmcnt(8)\n\tv_mfma_f32_32x32x16_bf16 v[96:111], v[160:163], v[200:203], v[96:111]\n\tds_read_b64_tr_b16 v[144:145], v4 offset:3072\n\tds_read_b64_tr_b16 v[146:147], v4 offset:3584\n\t"
            "v_mfma_f32_32x32x16_bf16 v[112:127], v[164:167], v[200:203], v[112:127]\n\tds_read_b64_tr_b16 v[148:149], v5 offset:3072\n\tds_read_b64_tr_b16 v[150:151], v5 offset:3584\n\tbuffer_load_dwordx4 v[176:179], v2, s[44:47], s10 offen offset:0 sc1\n\tv_mfma_f32_32x32x16_bf16 v[96:111], v[168:171], v[204:207], v[96:111]\n\tds_read_b64_tr_b16 v[152:153], v6 offset:3072\n\t"
            "ds_read_b64_tr_b16 v[154:155], v6 offset:3584\n\tbuffer_load_dwordx4 v[180:183], v2, s[44:47], s10 offen offset:3072 sc1\n\ts_waitcnt vmcnt(8)\n\tv_max3_u32 v12, v208, v210, v212\n\tv_max_u32 v12, v12, v214\n\tv_cmp_eq_u32 vcc, -1, v12\n\t"
            "v_mfma_f32_32x32x16_bf16 v[112:127], v[172:175], v[204:207], v[112:127]\n\tds_read_b64_tr_b16 v[156:157], v7 offset:3072\n\tds_read_b64_tr_b16 v[158:159], v7 offset:3584\n\ts_cbranch_vccnz 112f\n\t212:\ns_waitcnt lgkmcnt(8)\n\t"
            "v_mfma_f32_32x32x16_bf16 v[96:111], v[128:131], v[208:211], v[96:111]\n\tds_read_b64_tr_b16 v[160:161], v4 offset:4096\n\tds_read_b64_tr_b16 v[162:163], v4 offset:4608\n\tv_mfma_f32_32x32x16_bf16 v[112:127], v[132:135], v[208:211], v[112:127]\n\tds_read_b64_tr_b16 v[164:165], v5 offset:4096\n\tds_read_b64_tr_b16 v[166:167], v5 offset:4608\n\t"
            "buffer_load_dwordx4 v[184:187], v2, s[44:47], s11 offen offset:0 sc1\n\tv_mfma_f32_32x32x16_bf16 v[96:111], v[136:139], v[212:215], v[96:111]\n\tds_read_b64_tr_b16 v[168:169], v6 offset:4096\n\tds_read_b64_tr_b16 v[170:171], v6 offset:4608\n\tbuffer_load_dwordx4 v[188:191], v2, s[44:47], s11 offen offset:3072 sc1\n\ts_waitcnt vmcnt(8)\n\t"
            "v_max3_u32 v12, v216, v218, v220\n\tv_max_u32 v12, v12, v222\n\tv_cmp_eq_u32 vcc, -1, v12\n\tv_mfma_f32_32x32x16_bf16 v[112:127], v[140:143], v[212:215], v[112:127]\n\tds_read_b64_tr_b16 v[172:173], v7 offset:4096\n\tds_read_b64_tr_b16 v[174:175], v7 offset:4608\n\t"
            "s_cbranch_vccnz 113f\n\t213:\ns_waitcnt lgkmcnt(8)\n\tv_mfma_f32_32x32x16_bf16 v[96:111], v[144:147], v[216:219], v[96:111]\n\tds_read_b64_tr_b16 v[128:129], v4 offset:5120\n\tds_read_b64_tr_b16 v[130:131], v4 offset:5632\n\t"
            "v_mfma_f32_32x32x16_bf16 v[112:127], v[148:151], v[216:219], v[112:127]\n\tds_read_b64_tr_b16 v[132:133], v5 offset:5120\n\tds_read_b64_tr_b16 v[134:135], v5 offset:5632\n\tbuffer_load_dwordx4 v[192:195], v2, s[44:47], s12 offen offset:0 sc1\n\tv_mfma_f32_32x32x16_bf16 v[96:111], v[152:155], v[220:223], v[96:111]\n\tds_read_b64_tr_b16 v[136:137], v6 offset:5120\n\t"
            "ds_read_b64_tr_b16 v[138:139], v6 offset:5632\n\tbuffer_load_dwordx4 v[196:199], v2, s[44:47], s12 offen offset:3072 sc1\n\ts_waitcnt vmcnt(8)\n\tv_max3_u32 v12, v224, v226, v228\n\tv_max_u32 v12, v12, v230\n\tv_cmp_eq_u32 vcc, -1, v12\n\t"
            "v_mfma_f32_32x32x16_bf16 v[112:127], v[156:159], v[220:223], v[112:127]\n\tds_read_b64_tr_b16 v[140:141], v7 offset:5120\n\tds_read_b64_tr_b16 v[142:143], v7 offset:5632\n\ts_cbranch_vccnz 114f\n\t214:\ns_waitcnt lgkmcnt(8)\n\t"
            "v_mfma_f32_32x32x16_bf16 v[96:111], v[160:163], v[224:227], v[96:111]\n\tds_read_b64_tr_b16 v[144:145], v4 offset:6144\n\tds_read_b64_tr_b16 v[146:147], v4 offset:6656\n\tv_mfma_f32_32x32x16_bf16 v[112:127], v[164:167], v[224:227], v[112:127]\n\tds_read_b64_tr_b16 v[148:149], v5 offset:6144\n\tds_read_b64_tr_b16 v[150:151], v5 offset:6656\n\t"
            "buffer_load_dwordx4 v[200:203], v2, s[44:47], s13 offen offset:0 sc1\n\tv_mfma_f32_32x32x16_bf16 v[96:111], v[168:171], v[228:231], v[96:111]\n\tds_read_b64_tr_b16 v[152:153], v6 offset:6144\n\tds_read_b64_tr_b16 v[154:155], v6 offset:6656\n\tbuffer_load_dwordx4 v[204:207], v2, s[44:47], s13 offen offset:3072 sc1\n\ts_waitcnt vmcnt(8)\n\t"
            "v_max3_u32 v12, v232, v234, v236\n\tv_max_u32 v12, v12, v238\n\tv_cmp_eq_u32 vcc, -1, v12\n\tv_mfma_f32_32x32x16_bf16 v[112:127], v[172:175], v[228:231], v[112:127]\n\tds_read_b64_tr_b16 v[156:157], v7 offset:6144\n\tds_read_b64_tr_b16 v[158:159], v7 offset:6656\n\t"
            "s_cbranch_vccnz 115f\n\t215:\ns_waitcnt lgkmcnt(8)\n\tv_mfma_f32_32x32x16_bf16 v[96:111], v[128:131], v[232:235], v[96:111]\n\tds_read_b64_tr_b16 v[160:161], v4 offset:7168\n\tds_read_b64_tr_b16 v[162:163], v4 offset:7680\n\t"
            "v_mfma_f32_32x32x16_bf16 v[112:127], v[132:135], v[232:235], v[112:127]\n\tds_read_b64_tr_b16 v[164:165], v5 offset:7168\n\tds_read_b64_tr_b16 v[166:167], v5 offset:7680\n\tv_mfma_f32_32x32x16_bf16 v[96:111], v[136:139], v[236:239], v[96:111]\n\tds_read_b64_tr_b16 v[168:169], v6 offset:7168\n\tds_read_b64_tr_b16 v[170:171], v6 offset:7680\n\t"
            "s_waitcnt vmcnt(6)\n\tv_max3_u32 v12, v176, v178, v180\n\tv_max_u32 v12, v12, v182\n\tv_cmp_eq_u32 vcc, -1, v12\n\tv_mfma_f32_32x32x16_bf16 v[112:127], v[140:143], v[236:239], v[112:127]\n\tds_read_b64_tr_b16 v[172:173], v7 offset:7168\n\t"
            "ds_read_b64_tr_b16 v[174:175], v7 offset:7680\n\ts_cbranch_vccnz 116f\n\t216:\ns_waitcnt lgkmcnt(8)\n\tv_mfma_f32_32x32x16_bf16 v[96:111], v[144:147], v[176:179], v[96:111]\n\tds_read_b64_tr_b16 v[128:129], v4 offset:8192\n\t"
            "ds_read_b64_tr_b16 v[130:131], v4 offset:8704\n\tv_mfma_f32_32x32x16_bf16 v[112:127], v[148:151], v[176:179], v[112:127]\n\tds_read_b64_tr_b16 v[132:133], v5 offset:8192\n\tds_read_b64_tr_b16 v[134:135], v5 offset:8704\n\tv_mfma_f32_32x32x16_bf16 v[96:111], v[152:155], v[180:183], v[96:111]\n\tds_read_b64_tr_b16 v[136:137], v6 offset:8192\n\t"
            "ds_read_b64_tr_b16 v[138:139], v6 offset:8704\n\ts_waitcnt vmcnt(4)\n\tv_max3_u32 v12, v184, v186, v188\n\tv_max_u32 v12, v12, v190\n\tv_cmp_eq_u32 vcc, -1, v12\n\tv_mfma_f32_32x32x16_bf16 v[112:127], v[156:159], v[180:183], v[112:127]\n\t"
            "ds_read_b64_tr_b16 v[140:141], v7 offset:8192\n\tds_read_b64_tr_b16 v[142:143], v7 offset:8704\n\ts_cbranch_vccnz 117f\n\t217:\nds_read_b32 v16, v8 offset:64\n\tds_read_b32 v17, v9 offset:1344\n\t"
            "s_waitcnt lgkmcnt(10)\n\tv_mfma_f32_32x32x16_bf16 v[96:111], v[160:163], v[184:187], v[96:111]\n\tds_read_b64_tr_b16 v[144:145], v4 offset:9216\n\tds_read_b64_tr_b16 v[146:147], v4 offset:9728\n\tds_read_b64_tr_b16 v[148:149], v5 offset:9216\n\tds_read_b64_tr_b16 v[150:151], v5 offset:9728\n\t"
            "ds_read_b64_tr_b16 v[152:153], v6 offset:9216\n\tds_read_b64_tr_b16 v[154:155], v6 offset:9728\n\ts_waitcnt vmcnt(2)\n\tv_max3_u32 v12, v192, v194, v196\n\tv_max_u32 v12, v12, v198\n\tv_cmp_eq_u32 vcc, -1, v12\n\t"
            "v_mfma_f32_32x32x16_bf16 v[96:111], v[168:171], v[188:191], v[96:111]\n\tds_read_b64_tr_b16 v[156:157], v7 offset:9216\n\tds_read_b64_tr_b16 v[158:159], v7 offset:9728\n\ts_cbranch_vccnz 118f\n\t218:\ns_waitcnt lgkmcnt(10)\n\t"
            "v_mfma_f32_32x32x16_bf16 v[96:111], v[128:131], v[192:195], v[96:111]\n\ts_waitcnt vmcnt(0)\n\tv_max3_u32 v12, v200, v202, v204\n\tv_max_u32 v12, v12, v206\n\tv_cmp_eq_u32 vcc, -1, v12\n\tv_mfma_f32_32x32x16_bf16 v[96:111], v[136:139], v[196:199], v[96:111]\n\t"
            "s_cbranch_vccnz 119f\n\t219:\ns_waitcnt lgkmcnt(0)\n\tv_mfma_f32_32x32x16_bf16 v[96:111], v[144:147], v[200:203], v[96:111]\n\tv_cmp_ne_u32 s[74:75], 0, v17\n\tv_cmp_eq_u32 vcc, 0, v16\n\t"
            "v_mfma_f32_32x32x16_bf16 v[96:111], v[152:155], v[204:207], v[96:111]\n\ts_or_b64 vcc, vcc, s[74:75]\n\tv_mfma_f32_32x32x16_bf16 v[112:127], v[164:167], v[184:187], v[112:127]\n\ts_cbranch_scc1 7f\n\t8:\ns_nop 5\n\t"
            "v_mfma_f32_32x32x16_bf16 v[112:127], v[172:175], v[188:191], v[112:127]\n\ts_nop 1\n\tv_fmac_f32 v64, v96, v32\n\tv_fmac_f32 v65, v97, v33\n\tv_fmac_f32 v66, v98, v34\n\tv_fmac_f32 v67, v99, v35\n\t"
            "v_fmac_f32 v68, v100, v36\n\tv_fmac_f32 v69, v101, v37\n\tv_fmac_f32 v70, v102, v38\n\tv_mfma_f32_32x32x16_bf16 v[112:127], v[132:135], v[192:195], v[112:127]\n\tv_fmac_f32 v71, v103, v39\n\tv_fmac_f32 v72, v104, v40\n\t"
            "v_fmac_f32 v73, v105, v41\n\tv_fmac_f32 v74, v106, v42\n\tv_fmac_f32 v75, v107, v43\n\tv_fmac_f32 v76, v108, v44\n\tv_fmac_f32 v77, v109, v45\n\tv_mfma_f32_32x32x16_bf16 v[112:127], v[140:143], v[196:199], v[112:127]\n\t"
            "v_fmac_f32 v78, v110, v46\n\tv_fmac_f32 v79, v111, v47\n\tv_cvt_pk_bf16_f32 v240, v64, v65\n\tv_cvt_pk_bf16_f32 v242, v68, v69\n\tv_cvt_pk_bf16_f32 v241, v66, v67\n\tv_cvt_pk_bf16_f32 v243, v70, v71\n\t"
            "v_cvt_pk_bf16_f32 v244, v72, v73\n\tv_mfma_f32_32x32x16_bf16 v[112:127], v[148:151], v[200:203], v[112:127]\n\tv_cvt_pk_bf16_f32 v246, v76, v77\n\tv_cvt_pk_bf16_f32 v245, v74, v75\n\tv_cvt_pk_bf16_f32 v247, v78, v79\n\tv_permlane32_swap_b32 v240, v242\n\t"
            "v_permlane32_swap_b32 v241, v243\n\tv_permlane32_swap_b32 v244, v246\n\tv_permlane32_swap_b32 v245, v247\n\tv_mfma_f32_32x32x16_bf16 v[112:127], v[156:159], v[204:207], v[112:127]\n\tv_permlane16_swap_b32 v240, v244\n\tv_permlane16_swap_b32 v242, v246\n\t"
            "v_permlane16_swap_b32 v241, v245\n\tv_permlane16_swap_b32 v243, v247\n\tbuffer_store_dwordx4 v[240:243], v3, s[52:55], 0 offen offset:0 sc1\n\tbuffer_store_dwordx4 v[244:247], v11, s[52:55], 0 offen offset:0 sc1\n\t"
            SO(6)
            "s_nop 5\n\tv_fmac_f32 v80, v112, v48\n\tv_fmac_f32 v81, v113, v49\n\tv_fmac_f32 v82, v114, v50\n\tv_fmac_f32 v83, v115, v51\n\tv_fmac_f32 v84, v116, v52\n\t"
            "v_fmac_f32 v85, v117, v53\n\tv_fmac_f32 v86, v118, v54\n\tv_fmac_f32 v87, v119, v55\n\tv_fmac_f32 v88, v120, v56\n\tv_fmac_f32 v89, v121, v57\n\tv_fmac_f32 v90, v122, v58\n\t"
            "v_fmac_f32 v91, v123, v59\n\tv_fmac_f32 v92, v124, v60\n\tv_fmac_f32 v93, v125, v61\n\tv_fmac_f32 v94, v126, v62\n\tv_fmac_f32 v95, v127, v63\n\tv_cvt_pk_bf16_f32 v248, v80, v81\n\t"
            "v_cvt_pk_bf16_f32 v250, v84, v85\n\tv_cvt_pk_bf16_f32 v249, v82, v83\n\tv_cvt_pk_bf16_f32 v251, v86, v87\n\tv_cvt_pk_bf16_f32 v252, v88, v89\n\tv_cvt_pk_bf16_f32 v254, v92, v93\n\tv_cvt_pk_bf16_f32 v253, v90, v91\n\t"
            "v_cvt_pk_bf16_f32 v255, v94, v95\n\tv_permlane32_swap_b32 v248, v250\n\tv_permlane32_swap_b32 v249, v251\n\tv_permlane32_swap_b32 v252, v254\n\tv_permlane32_swap_b32 v253, v255\n\ts_nop 0\n\t"
            "v_permlane16_swap_b32 v248, v252\n\tv_permlane16_swap_b32 v250, v254\n\tv_permlane16_swap_b32 v249, v253\n\tv_permlane16_swap_b32 v251, v255\n\tbuffer_store_dwordx4 v[248:251], v3, s[52:55], 0 offen offset:64 sc1\n\tbuffer_store_dwordx4 v[252:255], v11, s[52:55], 0 offen offset:64 sc1\n\t"
            "9:\n"
            RCNT DUMP
            "s_endpgm\n\t.LG0:\nv_mul_f32 v21, 0xbfb8aa3b, v21\n\tv_mul_f32 v20, v20, v21\n\tv_permlane16_swap_b32 v240, v244\n\tv_permlane16_swap_b32 v241, v245\n\t"
            "v_permlane16_swap_b32 v242, v246\n\tv_permlane16_swap_b32 v243, v247\n\tv_permlane16_swap_b32 v248, v252\n\tv_permlane16_swap_b32 v249, v253\n\tv_permlane16_swap_b32 v250, v254\n\tv_permlane16_swap_b32 v251, v255\n\t"
            "v_permlane32_swap_b32 v240, v242\n\tv_permlane32_swap_b32 v241, v243\n\tv_permlane32_swap_b32 v244, v246\n\tv_permlane32_swap_b32 v245, v247\n\tv_permlane32_swap_b32 v248, v250\n\tv_permlane32_swap_b32 v249, v251\n\t"
            "v_permlane32_swap_b32 v252, v254\n\tv_permlane32_swap_b32 v253, v255\n\tv_lshlrev_b32 v64, 16, v240\n\tv_and_b32 v65, 0xffff0000, v240\n\tv_lshlrev_b32 v66, 16, v241\n\tv_and_b32 v67, 0xffff0000, v241\n\t"
            "v_lshlrev_b32 v68, 16, v242\n\tv_and_b32 v69, 0xffff0000, v242\n\tv_lshlrev_b32 v70, 16, v243\n\tv_and_b32 v71, 0xffff0000, v243\n\tv_lshlrev_b32 v72, 16, v244\n\tv_and_b32 v73, 0xffff0000, v244\n\t"
            "v_lshlrev_b32 v74, 16, v245\n\tv_and_b32 v75, 0xffff0000, v245\n\tv_lshlrev_b32 v76, 16, v246\n\tv_and_b32 v77, 0xffff0000, v246\n\tv_lshlrev_b32 v78, 16, v247\n\tv_and_b32 v79, 0xffff0000, v247\n\t"
            "v_lshlrev_b32 v80, 16, v248\n\tv_and_b32 v81, 0xffff0000, v248\n\tv_lshlrev_b32 v82, 16, v249\n\tv_and_b32 v83, 0xffff0000, v249\n\tv_lshlrev_b32 v84, 16, v250\n\tv_and_b32 v85, 0xffff0000, v250\n\t"
            "v_lshlrev_b32 v86, 16, v251\n\tv_and_b32 v87, 0xffff0000, v251\n\tv_lshlrev_b32 v88, 16, v252\n\tv_and_b32 v89, 0xffff0000, v252\n\tv_lshlrev_b32 v90, 16, v253\n\tv_and_b32 v91, 0xffff0000, v253\n\t"
            "v_lshlrev_b32 v92, 16, v254\n\tv_and_b32 v93, 0xffff0000, v254\n\tv_lshlrev_b32 v94, 16, v255\n\tv_and_b32 v95, 0xffff0000, v255\n\tv_fma_f32 v96, v64, v21, v20\n\tv_fma_f32 v97, v65, v21, v20\n\t"
            "v_fma_f32 v98, v66, v21, v20\n\tv_fma_f32 v99, v67, v21, v20\n\tv_fma_f32 v100, v68, v21, v20\n\tv_fma_f32 v101, v69, v21, v20\n\tv_fma_f32 v102, v70, v21, v20\n\tv_fma_f32 v103, v71, v21, v20\n\t"
            "v_fma_f32 v104, v72, v21, v20\n\tv_fma_f32 v105, v73, v21, v20\n\tv_fma_f32 v106, v74, v21, v20\n\tv_fma_f32 v107, v75, v21, v20\n\tv_fma_f32 v108, v76, v21, v20\n\tv_fma_f32 v109, v77, v21, v20\n\t"
            "v_fma_f32 v110, v78, v21, v20\n\tv_fma_f32 v111, v79, v21, v20\n\tv_fma_f32 v112, v80, v21, v20\n\tv_fma_f32 v113, v81, v21, v20\n\tv_fma_f32 v114, v82, v21, v20\n\tv_fma_f32 v115, v83, v21, v20\n\t"
            "v_fma_f32 v116, v84, v21, v20\n\tv_fma_f32 v117, v85, v21, v20\n\tv_fma_f32 v118, v86, v21, v20\n\tv_fma_f32 v119, v87, v21, v20\n\tv_fma_f32 v120, v88, v21, v20\n\tv_fma_f32 v121, v89, v21, v20\n\t"
            "v_fma_f32 v122, v90, v21, v20\n\tv_fma_f32 v123, v91, v21, v20\n\tv_fma_f32 v124, v92, v21, v20\n\tv_fma_f32 v125, v93, v21, v20\n\tv_fma_f32 v126, v94, v21, v20\n\tv_fma_f32 v127, v95, v21, v20\n\t"
            "s_setpc_b64 s[76:77]\n\t.LG1:\nv_exp_f32 v32, v96\n\tv_exp_f32 v33, v97\n\tv_exp_f32 v34, v98\n\tv_exp_f32 v35, v99\n\t"
            "v_exp_f32 v36, v100\n\tv_exp_f32 v37, v101\n\tv_exp_f32 v38, v102\n\tv_exp_f32 v39, v103\n\tv_exp_f32 v40, v104\n\tv_exp_f32 v41, v105\n\t"
            "v_exp_f32 v42, v106\n\tv_exp_f32 v43, v107\n\tv_exp_f32 v44, v108\n\tv_exp_f32 v45, v109\n\tv_exp_f32 v46, v110\n\tv_exp_f32 v47, v111\n\t"
            "v_exp_f32 v48, v112\n\tv_exp_f32 v49, v113\n\tv_exp_f32 v50, v114\n\tv_exp_f32 v51, v115\n\tv_exp_f32 v52, v116\n\tv_exp_f32 v53, v117\n\t"
            "v_exp_f32 v54, v118\n\tv_exp_f32 v55, v119\n\tv_exp_f32 v56, v120\n\tv_exp_f32 v57, v121\n\tv_exp_f32 v58, v122\n\tv_exp_f32 v59, v123\n\t"
            "v_exp_f32 v60, v124\n\tv_exp_f32 v61, v125\n\tv_exp_f32 v62, v126\n\tv_exp_f32 v63, v127\n\tv_add_f32 v32, 1.0, v32\n\tv_add_f32 v33, 1.0, v33\n\t"
            "v_add_f32 v34, 1.0, v34\n\tv_add_f32 v35, 1.0, v35\n\tv_add_f32 v36, 1.0, v36\n\tv_add_f32 v37, 1.0, v37\n\tv_add_f32 v38, 1.0, v38\n\tv_add_f32 v39, 1.0, v39\n\t"
            "v_add_f32 v40, 1.0, v40\n\tv_add_f32 v41, 1.0, v41\n\tv_add_f32 v42, 1.0, v42\n\tv_add_f32 v43, 1.0, v43\n\tv_add_f32 v44, 1.0, v44\n\tv_add_f32 v45, 1.0, v45\n\t"
            "v_add_f32 v46, 1.0, v46\n\tv_add_f32 v47, 1.0, v47\n\tv_add_f32 v48, 1.0, v48\n\tv_add_f32 v49, 1.0, v49\n\tv_add_f32 v50, 1.0, v50\n\tv_add_f32 v51, 1.0, v51\n\t"
            "v_add_f32 v52, 1.0, v52\n\tv_add_f32 v53, 1.0, v53\n\tv_add_f32 v54, 1.0, v54\n\tv_add_f32 v55, 1.0, v55\n\tv_add_f32 v56, 1.0, v56\n\tv_add_f32 v57, 1.0, v57\n\t"
            "v_add_f32 v58, 1.0, v58\n\tv_add_f32 v59, 1.0, v59\n\tv_add_f32 v60, 1.0, v60\n\tv_add_f32 v61, 1.0, v61\n\tv_add_f32 v62, 1.0, v62\n\tv_add_f32 v63, 1.0, v63\n\t"
            "v_rcp_f32 v32, v32\n\tv_rcp_f32 v33, v33\n\tv_rcp_f32 v34, v34\n\tv_rcp_f32 v35, v35\n\ts_mov_b32 s19, 1\n\ts_setpc_b64 s[76:77]\n\t"
            ".LG2:\nv_rcp_f32 v36, v36\n\tv_rcp_f32 v37, v37\n\tv_rcp_f32 v38, v38\n\tv_rcp_f32 v39, v39\n\tv_rcp_f32 v40, v40\n\t"
            "v_rcp_f32 v41, v41\n\tv_rcp_f32 v42, v42\n\tv_rcp_f32 v43, v43\n\tv_rcp_f32 v44, v44\n\tv_rcp_f32 v45, v45\n\tv_rcp_f32 v46, v46\n\t"
            "v_rcp_f32 v47, v47\n\tv_rcp_f32 v48, v48\n\tv_rcp_f32 v49, v49\n\tv_rcp_f32 v50, v50\n\tv_rcp_f32 v51, v51\n\tv_rcp_f32 v52, v52\n\t"
            "v_rcp_f32 v53, v53\n\tv_rcp_f32 v54, v54\n\tv_rcp_f32 v55, v55\n\tv_rcp_f32 v56, v56\n\tv_rcp_f32 v57, v57\n\tv_rcp_f32 v58, v58\n\t"
            "v_rcp_f32 v59, v59\n\tv_rcp_f32 v60, v60\n\tv_rcp_f32 v61, v61\n\tv_rcp_f32 v62, v62\n\tv_rcp_f32 v63, v63\n\tv_mul_f32 v32, v32, v96\n\t"
            "v_mul_f32 v33, v33, v97\n\tv_mul_f32 v34, v34, v98\n\tv_mul_f32 v35, v35, v99\n\tv_mul_f32 v36, v36, v100\n\tv_mul_f32 v37, v37, v101\n\tv_mul_f32 v38, v38, v102\n\t"
            "v_mul_f32 v39, v39, v103\n\tv_mul_f32 v40, v40, v104\n\tv_mul_f32 v41, v41, v105\n\tv_mul_f32 v42, v42, v106\n\tv_mul_f32 v43, v43, v107\n\tv_mul_f32 v44, v44, v108\n\t"
            "v_mul_f32 v45, v45, v109\n\tv_mul_f32 v46, v46, v110\n\tv_mul_f32 v47, v47, v111\n\ts_mov_b32 s19, 2\n\ts_setpc_b64 s[76:77]\n\t7:\n"
            "ds_read_b32 v16, v8 offset:64\n\tds_read_b32 v17, v9 offset:1344\n\ts_waitcnt lgkmcnt(0)\n\tv_cmp_ne_u32 s[74:75], 0, v17\n\tv_cmp_eq_u32 vcc, 0, v16\n\ts_cbranch_vccz 71f\n\t"
            "s_sleep 1\n\ts_sub_u32 s68, s68, 1\n\ts_cbranch_scc0 7b\n\ts_branch 9b\n\t71:\nv_lshrrev_b32 v12, 5, v2\n\t"
            "v_and_b32 v12, 16, v12\n\tv_add_u32 v12, v12, v17\n\ts_mov_b64 exec, s[74:75]\n\ts_cbranch_execz 72f\n\tds_read_b128 v[32:35], v12 offset:0\n\tds_read_b128 v[36:39], v12 offset:32\n\t"
            "ds_read_b128 v[40:43], v12 offset:64\n\tds_read_b128 v[44:47], v12 offset:96\n\tds_read_b128 v[48:51], v12 offset:128\n\tds_read_b128 v[52:55], v12 offset:160\n\tds_read_b128 v[56:59], v12 offset:192\n\tds_read_b128 v[60:63], v12 offset:224\n\t"
            "72:\ns_mov_b64 exec, -1\n\ts_waitcnt lgkmcnt(0)\n\ts_branch 8b\n\t5:\ns_sleep 1\n\t"
            "ds_read_b64 v[14:15], v8 offset:40\n\ts_waitcnt lgkmcnt(0)\n\tv_min_u32 v14, v14, v15\n\tv_cmp_ge_u32 vcc, s71, v14\n\ts_cbranch_vccz 61f\n\ts_sub_u32 s68, s68, 1\n\t"
            "s_cbranch_scc0 5b\n\ts_branch 9b\n\t61:\n"
            TRG2D
            "s_branch 6b\n\t62:\nds_read_b128 v[16:19], v8 offset:48\n\tds_read_b64_tr_b16 v[128:129], v4 offset:40960\n\tds_read_b64_tr_b16 v[130:131], v4 offset:41472\n\tds_read_b64_tr_b16 v[132:133], v5 offset:40960\n\t"
            "ds_read_b64_tr_b16 v[134:135], v5 offset:41472\n\tds_read_b64_tr_b16 v[136:137], v6 offset:40960\n\tds_read_b64_tr_b16 v[138:139], v6 offset:41472\n\tds_read_b64_tr_b16 v[140:141], v7 offset:40960\n\tds_read_b64_tr_b16 v[142:143], v7 offset:41472\n\tds_read_b64_tr_b16 v[144:145], v4 offset:41984\n\t"
            "ds_read_b64_tr_b16 v[146:147], v4 offset:42496\n\tds_read_b64_tr_b16 v[148:149], v5 offset:41984\n\tds_read_b64_tr_b16 v[150:151], v5 offset:42496\n\tds_read_b64_tr_b16 v[152:153], v6 offset:41984\n\tds_read_b64_tr_b16 v[154:155], v6 offset:42496\n\tds_read_b64_tr_b16 v[156:157], v7 offset:41984\n\t"
            "ds_read_b64_tr_b16 v[158:159], v7 offset:42496\n\ts_waitcnt lgkmcnt(15)\n\tv_min3_u32 v16, v16, v17, v18\n\tv_min_u32 v16, v16, v19\n\tv_cmp_eq_u32 vcc, 0, v16\n\ts_cbranch_vccz 63b\n\t"
            "s_sub_u32 s68, s68, 1\n\ts_cbranch_scc0 62b\n\ts_branch 9b\n\t100:\ns_mov_b32 s70, 4\n\ts_call_b64 s[72:73], 300f\n\t"
            "v_max3_u32 v12, v176, v178, v180\n\tv_max_u32 v12, v12, v182\n\tv_cmp_eq_u32 vcc, -1, v12\n\ts_cbranch_vccnz 100b\n\ts_branch 200b\n\t101:\n"
            "s_mov_b32 s70, 5\n\ts_call_b64 s[72:73], 301f\n\tv_max3_u32 v12, v184, v186, v188\n\tv_max_u32 v12, v12, v190\n\tv_cmp_eq_u32 vcc, -1, v12\n\ts_cbranch_vccnz 101b\n\t"
            "s_branch 201b\n\t102:\ns_mov_b32 s70, 6\n\ts_call_b64 s[72:73], 302f\n\tv_max3_u32 v12, v192, v194, v196\n\tv_max_u32 v12, v12, v198\n\t"
            "v_cmp_eq_u32 vcc, -1, v12\n\ts_cbranch_vccnz 102b\n\ts_branch 202b\n\t103:\ns_mov_b32 s70, 7\n\ts_call_b64 s[72:73], 303f\n\t"
            "v_max3_u32 v12, v200, v202, v204\n\tv_max_u32 v12, v12, v206\n\tv_cmp_eq_u32 vcc, -1, v12\n\ts_cbranch_vccnz 103b\n\ts_branch 203b\n\t104:\n"
            "s_mov_b32 s70, 8\n\ts_call_b64 s[72:73], 304f\n\tv_max3_u32 v12, v208, v210, v212\n\tv_max_u32 v12, v12, v214\n\tv_cmp_eq_u32 vcc, -1, v12\n\ts_cbranch_vccnz 104b\n\t"
            "s_branch 204b\n\t105:\ns_mov_b32 s70, 9\n\ts_call_b64 s[72:73], 305f\n\tv_max3_u32 v12, v216, v218, v220\n\tv_max_u32 v12, v12, v222\n\t"
            "v_cmp_eq_u32 vcc, -1, v12\n\ts_cbranch_vccnz 105b\n\ts_branch 205b\n\t106:\ns_mov_b32 s70, 10\n\ts_call_b64 s[72:73], 306f\n\t"
            "v_max3_u32 v12, v224, v226, v228\n\tv_max_u32 v12, v12, v230\n\tv_cmp_eq_u32 vcc, -1, v12\n\ts_cbranch_vccnz 106b\n\ts_branch 206b\n\t107:\n"
            "s_mov_b32 s70, 11\n\ts_call_b64 s[72:73], 307f\n\tv_max3_u32 v12, v232, v234, v236\n\tv_max_u32 v12, v12, v238\n\tv_cmp_eq_u32 vcc, -1, v12\n\ts_cbranch_vccnz 107b\n\t"
            "s_branch 207b\n\t108:\ns_mov_b32 s70, 12\n\ts_call_b64 s[72:73], 308f\n\tv_max3_u32 v12, v176, v178, v180\n\tv_max_u32 v12, v12, v182\n\t"
            "v_cmp_eq_u32 vcc, -1, v12\n\ts_cbranch_vccnz 108b\n\ts_branch 208b\n\t109:\ns_mov_b32 s70, 13\n\ts_call_b64 s[72:73], 309f\n\t"
            "v_max3_u32 v12, v184, v186, v188\n\tv_max_u32 v12, v12, v190\n\tv_cmp_eq_u32 vcc, -1, v12\n\ts_cbranch_vccnz 109b\n\ts_branch 209b\n\t110:\n"
            "s_mov_b32 s70, 14\n\ts_call_b64 s[72:73], 310f\n\tv_max3_u32 v12, v192, v194, v196\n\tv_max_u32 v12, v12, v198\n\tv_cmp_eq_u32 vcc, -1, v12\n\ts_cbranch_vccnz 110b\n\t"
            "s_branch 210b\n\t111:\ns_mov_b32 s70, 15\n\ts_call_b64 s[72:73], 311f\n\tv_max3_u32 v12, v200, v202, v204\n\tv_max_u32 v12, v12, v206\n\t"
            "v_cmp_eq_u32 vcc, -1, v12\n\ts_cbranch_vccnz 111b\n\ts_branch 211b\n\t112:\ns_mov_b32 s70, 16\n\ts_call_b64 s[72:73], 312f\n\t"
            "v_max3_u32 v12, v208, v210, v212\n\tv_max_u32 v12, v12, v214\n\tv_cmp_eq_u32 vcc, -1, v12\n\ts_cbranch_vccnz 112b\n\ts_branch 212b\n\t113:\n"
            "s_mov_b32 s70, 17\n\ts_call_b64 s[72:73], 313f\n\tv_max3_u32 v12, v216, v218, v220\n\tv_max_u32 v12, v12, v222\n\tv_cmp_eq_u32 vcc, -1, v12\n\ts_cbranch_vccnz 113b\n\t"
            "s_branch 213b\n\t114:\ns_mov_b32 s70, 18\n\ts_call_b64 s[72:73], 314f\n\tv_max3_u32 v12, v224, v226, v228\n\tv_max_u32 v12, v12, v230\n\t"
            "v_cmp_eq_u32 vcc, -1, v12\n\ts_cbranch_vccnz 114b\n\ts_branch 214b\n\t115:\ns_mov_b32 s70, 19\n\ts_call_b64 s[72:73], 315f\n\t"
            "v_max3_u32 v12, v232, v234, v236\n\tv_max_u32 v12, v12, v238\n\tv_cmp_eq_u32 vcc, -1, v12\n\ts_cbranch_vccnz 115b\n\ts_branch 215b\n\t116:\n"
            "s_mov_b32 s70, 19\n\ts_call_b64 s[72:73], 316f\n\tv_max3_u32 v12, v176, v178, v180\n\tv_max_u32 v12, v12, v182\n\tv_cmp_eq_u32 vcc, -1, v12\n\ts_cbranch_vccnz 116b\n\t"
            "s_branch 216b\n\t117:\ns_mov_b32 s70, 19\n\ts_call_b64 s[72:73], 317f\n\tv_max3_u32 v12, v184, v186, v188\n\tv_max_u32 v12, v12, v190\n\t"
            "v_cmp_eq_u32 vcc, -1, v12\n\ts_cbranch_vccnz 117b\n\ts_branch 217b\n\t118:\ns_mov_b32 s70, 19\n\ts_call_b64 s[72:73], 318f\n\t"
            "v_max3_u32 v12, v192, v194, v196\n\tv_max_u32 v12, v12, v198\n\tv_cmp_eq_u32 vcc, -1, v12\n\ts_cbranch_vccnz 118b\n\ts_branch 218b\n\t119:\n"
            "s_mov_b32 s70, 19\n\ts_call_b64 s[72:73], 319f\n\tv_max3_u32 v12, v200, v202, v204\n\tv_max_u32 v12, v12, v206\n\tv_cmp_eq_u32 vcc, -1, v12\n\ts_cbranch_vccnz 119b\n\t"
            "s_branch 219b\n\t300:\ns_waitcnt vmcnt(0)\n\t400:\nv_max3_u32 v12, v176, v178, v180\n\tv_max_u32 v12, v12, v182\n\t"
            "v_cmp_eq_u32 vcc, -1, v12\n\ts_cbranch_vccz 11f\n\tbuffer_load_dwordx4 v[176:179], v2, s[44:47], s4 offen offset:1024 sc1\n\tbuffer_load_dwordx4 v[180:183], v2, s[44:47], s4 offen offset:2048 sc1\n\t11:\ns_cmp_eq_u32 s70, 0\n\t"
            "s_cbranch_scc0 401f\n\ts_branch 12f\n\t301:\ns_waitcnt vmcnt(0)\n\t401:\nv_max3_u32 v12, v184, v186, v188\n\t"
            "v_max_u32 v12, v12, v190\n\tv_cmp_eq_u32 vcc, -1, v12\n\ts_cbranch_vccz 11f\n\tbuffer_load_dwordx4 v[184:187], v2, s[44:47], s5 offen offset:1024 sc1\n\tbuffer_load_dwordx4 v[188:191], v2, s[44:47], s5 offen offset:2048 sc1\n\t11:\n"
            "s_cmp_eq_u32 s70, 1\n\ts_cbranch_scc0 402f\n\ts_branch 12f\n\t302:\ns_waitcnt vmcnt(0)\n\t402:\n"
            "v_max3_u32 v12, v192, v194, v196\n\tv_max_u32 v12, v12, v198\n\tv_cmp_eq_u32 vcc, -1, v12\n\ts_cbranch_vccz 11f\n\tbuffer_load_dwordx4 v[192:195], v2, s[44:47], s6 offen offset:1024 sc1\n\tbuffer_load_dwordx4 v[196:199], v2, s[44:47], s6 offen offset:2048 sc1\n\t"
            "11:\ns_cmp_eq_u32 s70, 2\n\ts_cbranch_scc0 403f\n\ts_branch 12f\n\t303:\ns_waitcnt vmcnt(0)\n\t"
            "403:\nv_max3_u32 v12, v200, v202, v204\n\tv_max_u32 v12, v12, v206\n\tv_cmp_eq_u32 vcc, -1, v12\n\ts_cbranch_vccz 11f\n\tbuffer_load_dwordx4 v[200:203], v2, s[44:47], s7 offen offset:1024 sc1\n\t"
            "buffer_load_dwordx4 v[204:207], v2, s[44:47], s7 offen offset:2048 sc1\n\t11:\ns_cmp_eq_u32 s70, 3\n\ts_cbranch_scc0 404f\n\ts_branch 12f\n\t304:\n"
            "s_waitcnt vmcnt(0)\n\t404:\nv_max3_u32 v12, v208, v210, v212\n\tv_max_u32 v12, v12, v214\n\tv_cmp_eq_u32 vcc, -1, v12\n\ts_cbranch_vccz 11f\n\t"
            "buffer_load_dwordx4 v[208:211], v2, s[44:47], s8 offen offset:1024 sc1\n\tbuffer_load_dwordx4 v[212:215], v2, s[44:47], s8 offen offset:2048 sc1\n\t11:\ns_cmp_eq_u32 s70, 4\n\ts_cbranch_scc0 405f\n\ts_branch 12f\n\t"
            "305:\ns_waitcnt vmcnt(0)\n\t405:\nv_max3_u32 v12, v216, v218, v220\n\tv_max_u32 v12, v12, v222\n\tv_cmp_eq_u32 vcc, -1, v12\n\t"
            "s_cbranch_vccz 11f\n\tbuffer_load_dwordx4 v[216:219], v2, s[44:47], s9 offen offset:1024 sc1\n\tbuffer_load_dwordx4 v[220:223], v2, s[44:47], s9 offen offset:2048 sc1\n\t11:\ns_cmp_eq_u32 s70, 5\n\ts_cbranch_scc0 406f\n\t"
            "s_branch 12f\n\t306:\ns_waitcnt vmcnt(0)\n\t406:\nv_max3_u32 v12, v224, v226, v228\n\tv_max_u32 v12, v12, v230\n\t"
            "v_cmp_eq_u32 vcc, -1, v12\n\ts_cbranch_vccz 11f\n\tbuffer_load_dwordx4 v[224:227], v2, s[44:47], s10 offen offset:1024 sc1\n\tbuffer_load_dwordx4 v[228:231], v2, s[44:47], s10 offen offset:2048 sc1\n\t11:\ns_cmp_eq_u32 s70, 6\n\t"
            "s_cbranch_scc0 407f\n\ts_branch 12f\n\t307:\ns_waitcnt vmcnt(0)\n\t407:\nv_max3_u32 v12, v232, v234, v236\n\t"
            "v_max_u32 v12, v12, v238\n\tv_cmp_eq_u32 vcc, -1, v12\n\ts_cbranch_vccz 11f\n\tbuffer_load_dwordx4 v[232:235], v2, s[44:47], s11 offen offset:1024 sc1\n\tbuffer_load_dwordx4 v[236:239], v2, s[44:47], s11 offen offset:2048 sc1\n\t11:\n"
            "s_cmp_eq_u32 s70, 7\n\ts_cbranch_scc0 408f\n\ts_branch 12f\n\t308:\ns_waitcnt vmcnt(0)\n\t408:\n"
            "v_max3_u32 v12, v176, v178, v180\n\tv_max_u32 v12, v12, v182\n\tv_cmp_eq_u32 vcc, -1, v12\n\ts_cbranch_vccz 11f\n\tbuffer_load_dwordx4 v[176:179], v2, s[44:47], s12 offen offset:1024 sc1\n\tbuffer_load_dwordx4 v[180:183], v2, s[44:47], s12 offen offset:2048 sc1\n\t"
            "11:\ns_cmp_eq_u32 s70, 8\n\ts_cbranch_scc0 409f\n\ts_branch 12f\n\t309:\ns_waitcnt vmcnt(0)\n\t"
            "409:\nv_max3_u32 v12, v184, v186, v188\n\tv_max_u32 v12, v12, v190\n\tv_cmp_eq_u32 vcc, -1, v12\n\ts_cbranch_vccz 11f\n\tbuffer_load_dwordx4 v[184:187], v2, s[44:47], s13 offen offset:1024 sc1\n\t"
            "buffer_load_dwordx4 v[188:191], v2, s[44:47], s13 offen offset:2048 sc1\n\t11:\ns_cmp_eq_u32 s70, 9\n\ts_cbranch_scc0 410f\n\ts_branch 12f\n\t310:\n"
            "s_waitcnt vmcnt(0)\n\t410:\nv_max3_u32 v12, v192, v194, v196\n\tv_max_u32 v12, v12, v198\n\tv_cmp_eq_u32 vcc, -1, v12\n\ts_cbranch_vccz 11f\n\t"
            "buffer_load_dwordx4 v[192:195], v2, s[44:47], s4 offen offset:0 sc1\n\tbuffer_load_dwordx4 v[196:199], v2, s[44:47], s4 offen offset:3072 sc1\n\t11:\ns_cmp_eq_u32 s70, 10\n\ts_cbranch_scc0 411f\n\ts_branch 12f\n\t"
            "311:\ns_waitcnt vmcnt(0)\n\t411:\nv_max3_u32 v12, v200, v202, v204\n\tv_max_u32 v12, v12, v206\n\tv_cmp_eq_u32 vcc, -1, v12\n\t"
            "s_cbranch_vccz 11f\n\tbuffer_load_dwordx4 v[200:203], v2, s[44:47], s5 offen offset:0 sc1\n\tbuffer_load_dwordx4 v[204:207], v2, s[44:47], s5 offen offset:3072 sc1\n\t11:\ns_cmp_eq_u32 s70, 11\n\ts_cbranch_scc0 412f\n\t"
            "s_branch 12f\n\t312:\ns_waitcnt vmcnt(0)\n\t412:\nv_max3_u32 v12, v208, v210, v212\n\tv_max_u32 v12, v12, v214\n\t"
            "v_cmp_eq_u32 vcc, -1, v12\n\ts_cbranch_vccz 11f\n\tbuffer_load_dwordx4 v[208:211], v2, s[44:47], s6 offen offset:0 sc1\n\tbuffer_load_dwordx4 v[212:215], v2, s[44:47], s6 offen offset:3072 sc1\n\t11:\ns_cmp_eq_u32 s70, 12\n\t"
            "s_cbranch_scc0 413f\n\ts_branch 12f\n\t313:\ns_waitcnt vmcnt(0)\n\t413:\nv_max3_u32 v12, v216, v218, v220\n\t"
            "v_max_u32 v12, v12, v222\n\tv_cmp_eq_u32 vcc, -1, v12\n\ts_cbranch_vccz 11f\n\tbuffer_load_dwordx4 v[216:219], v2, s[44:47], s7 offen offset:0 sc1\n\tbuffer_load_dwordx4 v[220:223], v2, s[44:47], s7 offen offset:3072 sc1\n\t11:\n"
            "s_cmp_eq_u32 s70, 13\n\ts_cbranch_scc0 414f\n\ts_branch 12f\n\t314:\ns_waitcnt vmcnt(0)\n\t414:\n"
            "v_max3_u32 v12, v224, v226, v228\n\tv_max_u32 v12, v12, v230\n\tv_cmp_eq_u32 vcc, -1, v12\n\ts_cbranch_vccz 11f\n\tbuffer_load_dwordx4 v[224:227], v2, s[44:47], s8 offen offset:0 sc1\n\tbuffer_load_dwordx4 v[228:231], v2, s[44:47], s8 offen offset:3072 sc1\n\t"
            "11:\ns_cmp_eq_u32 s70, 14\n\ts_cbranch_scc0 415f\n\ts_branch 12f\n\t315:\ns_waitcnt vmcnt(0)\n\t"
            "415:\nv_max3_u32 v12, v232, v234, v236\n\tv_max_u32 v12, v12, v238\n\tv_cmp_eq_u32 vcc, -1, v12\n\ts_cbranch_vccz 11f\n\tbuffer_load_dwordx4 v[232:235], v2, s[44:47], s9 offen offset:0 sc1\n\t"
            "buffer_load_dwordx4 v[236:239], v2, s[44:47], s9 offen offset:3072 sc1\n\t11:\ns_cmp_eq_u32 s70, 15\n\ts_cbranch_scc0 416f\n\ts_branch 12f\n\t316:\n"
            "s_waitcnt vmcnt(0)\n\t416:\nv_max3_u32 v12, v176, v178, v180\n\tv_max_u32 v12, v12, v182\n\tv_cmp_eq_u32 vcc, -1, v12\n\ts_cbranch_vccz 11f\n\t"
            "buffer_load_dwordx4 v[176:179], v2, s[44:47], s10 offen offset:0 sc1\n\tbuffer_load_dwordx4 v[180:183], v2, s[44:47], s10 offen offset:3072 sc1\n\t11:\ns_cmp_eq_u32 s70, 16\n\ts_cbranch_scc0 417f\n\ts_branch 12f\n\t"
            "317:\ns_waitcnt vmcnt(0)\n\t417:\nv_max3_u32 v12, v184, v186, v188\n\tv_max_u32 v12, v12, v190\n\tv_cmp_eq_u32 vcc, -1, v12\n\t"
            "s_cbranch_vccz 11f\n\tbuffer_load_dwordx4 v[184:187], v2, s[44:47], s11 offen offset:0 sc1\n\tbuffer_load_dwordx4 v[188:191], v2, s[44:47], s11 offen offset:3072 sc1\n\t11:\ns_cmp_eq_u32 s70, 17\n\ts_cbranch_scc0 418f\n\t"
            "s_branch 12f\n\t318:\ns_waitcnt vmcnt(0)\n\t418:\nv_max3_u32 v12, v192, v194, v196\n\tv_max_u32 v12, v12, v198\n\t"
            "v_cmp_eq_u32 vcc, -1, v12\n\ts_cbranch_vccz 11f\n\tbuffer_load_dwordx4 v[192:195], v2, s[44:47], s12 offen offset:0 sc1\n\tbuffer_load_dwordx4 v[196:199], v2, s[44:47], s12 offen offset:3072 sc1\n\t11:\ns_cmp_eq_u32 s70, 18\n\t"
            "s_cbranch_scc0 419f\n\ts_branch 12f\n\t319:\ns_waitcnt vmcnt(0)\n\t419:\nv_max3_u32 v12, v200, v202, v204\n\t"
            "v_max_u32 v12, v12, v206\n\tv_cmp_eq_u32 vcc, -1, v12\n\ts_cbranch_vccz 11f\n\tbuffer_load_dwordx4 v[200:203], v2, s[44:47], s13 offen offset:0 sc1\n\tbuffer_load_dwordx4 v[204:207], v2, s[44:47], s13 offen offset:3072 sc1\n\t11:\n"
            "12:\n"
            RINC
            "s_sub_u32 s68, s68, 1\n\ts_cbranch_scc1 9b\n\ts_waitcnt vmcnt(0)\n\ts_setpc_b64 s[72:73]\n\t.subsection 1\n\t.Lce:\n"
            ".subsection 0\n\t.Lout:\ns_waitcnt lgkmcnt(0)\n\t"
            : "=&{s67}"(tag), "=&{v[52:83]}"(sa), "=&{v[84:115]}"(sb), "=&{v[116:123]}"(sc) : [t] "v"(tid), [b] "s"(bid), [k] "s"(ka)
            : "memory", "scc", "vcc", "s4", "s5", "s6", "s7", "s8", "s9", "s10", "s11", "s12", "s13", "s14", "s15", "s16", "s17", "s18", "s19", "s20", "s21", "s22", "s23", "s24", "s25", "s26", "s27", "s28", "s29", "s30", "s31", "s32", "s33", "s34", "s35", "s36", "s37", "s38", "s39", "s40", "s41", "s42", "s43", "s44", "s45", "s46", "s47", "s48", "s49", "s50", "s51", "s52", "s53", "s54", "s55", "s56", "s57", "s58", "s59", "s60", "s61", "s62", "s63", "s64", "s65", "s66", "s68", "s69", "s70", "s71", "s72", "s73", "s74", "s75", "s76", "s77", "s78", "s79", "s80", "s81", "s82", "s83", "s84", "s85", "s86", "s87", "s88", "s89", "s90", "s91", "s92", "s93", "s94", "s95", "s96", "s97", "s98", "s99", "s100", "s101", "v2", "v3", "v4", "v5", "v6", "v7", "v8", "v9", "v10", "v11", "v12", "v13", "v14", "v15", "v16", "v17", "v18", "v19", "v20", "v21", "v22", "v23", "v24", "v25", "v26", "v27", "v28", "v29", "v30", "v31", "v32", "v33", "v34", "v35", "v36", "v37", "v38", "v39", "v40", "v41", "v42", "v43", "v44", "v45", "v46", "v47", "v48", "v49", "v50", "v51", "v124", "v125", "v126", "v127", "v128", "v129", "v130", "v131", "v132", "v133", "v134", "v135", "v136", "v137", "v138", "v139", "v140", "v141", "v142", "v143", "v144", "v145", "v146", "v147", "v148", "v149", "v150", "v151", "v152", "v153", "v154", "v155", "v156", "v157", "v158", "v159", "v160", "v161", "v162", "v163", "v164", "v165", "v166", "v167", "v168", "v169", "v170", "v171", "v172", "v173", "v174", "v175", "v176", "v177", "v178", "v179", "v180", "v181", "v182", "v183", "v184", "v185", "v186", "v187", "v188", "v189", "v190", "v191", "v192", "v193", "v194", "v195", "v196", "v197", "v198", "v199", "v200", "v201", "v202", "v203", "v204", "v205", "v206", "v207", "v208", "v209", "v210", "v211", "v212", "v213", "v214", "v215", "v216", "v217", "v218", "v219", "v220", "v221", "v222", "v223", "v224", "v225", "v226", "v227", "v228", "v229", "v230", "v231", "v232", "v233", "v234", "v235", "v236", "v237", "v238", "v239", "v240", "v241", "v242", "v243", "v244", "v245", "v246", "v247", "v248", "v249", "v250", "v251", "v252", "v253", "v254", "v255");
    tag = RFL(tag);
    asm volatile("" : "+s"(bid), "+s"(ka)); asm volatile("" : "+v"(tid));
    const u16 *x = (const u16*)ka[0], *wo = (const u16*)ka[2], *w_r = (const u16*)ka[3], *wg = (const u16*)ka[4], *wu = (const u16*)ka[5], *wd = (const u16*)ka[6]; char* ws = (char*)ka[7];
    const int wv = __builtin_amdgcn_readfirstlane(tid >> 6);
    const unsigned lds0 = (unsigned)(size_t)(lchar*)smem;
#define LANE() const int lane = lane_id(); const int g = lane >> 4, li = lane & 15, jq = 4 * (li & 3) + (li >> 2); (void)jq
    const int xcd = bid & 7, j = ((bid >> 3) + 25) & 31;
    const bool gemm = j < 25;
    const int fp = xcd >> 1, bl = (j * 13) >> 6, cl = j - bl * 5;
    const int rb = fp * 5 + bl, cg = (xcd & 1) * 5 + cl;
    const int par = fp >> 1, I0 = ((fp & 1) * 5 + bl) * 104 - 8;
    const __amdgpu_buffer_rsrc_t rs = __builtin_amdgcn_make_buffer_rsrc((void*)ws, 0, W_END, 0x00020000);
    auto wait_flag = [&](unsigned a) { SPIN { if (RFL(ldsrv<unsigned>(a))) break; __builtin_amdgcn_s_sleep(1); } MEMB(); };
    auto all4 = [&](u32x4 c) { return RFL(min(min(c.x, c.y), min(c.z, c.w))); };
    unsigned ep = 0;
    auto epld = [&]() { asm volatile("s_load_dword %0, %1, 0x0" : "=s"(ep) : "s"(ws) : "memory"); };
    auto mktag = [&]() { asm volatile("s_waitcnt lgkmcnt(0)" : "+s"(ep) :: "memory"); tag = ep * 1664525u + 1013904223u; };
        if (gemm) {
            LANE();
            wait_flag(lds0 + FL_W);
            const u32x4 kk = ldsrv<u32x4>(lds0 + L_KEY + lane * 16);
            const uint2 t2 = top2wave(lane < 52 ? kk.x : 0u, lane < 52 ? kk.z : 0u);
            _Pragma("nounroll") for (int k = 0; k < 2; ++k) {
                const unsigned key = k ? t2.y : t2.x, rr = (0xffffu - (key & 0xffffu)) & 2047u; const int pos = (int)(rr >> 1) - I0;
                if (__builtin_expect((int)(rr & 1) == par && pos >= 8 && pos < 112, 0)) {
                    const float sg = rbf(__builtin_amdgcn_rcpf(1.f + __builtin_amdgcn_exp2f(keyscore(key) * -1.4426950408889634f)));
                    const int m = (pos & ~15) + 4 * (pos & 3) + ((pos >> 2) & 3);
                    const float xr_ = lo2f(x[rr * 640 + cg * 64 + lane]), nm = ldsrv<float>(lds0 + L_NM + m * 4), rsj = ldsrv<float>(lds0 + L_RS + pos * 4);
                    u32x2 y[5];
                    SPIN {
                        MEMB();
                        bool old = false;
#pragma unroll
                        for (int i = 0; i < 5; ++i) y[i] = __builtin_amdgcn_raw_buffer_load_b64(rs, W_Y + (cg * 64 + lane) * 16 + k * 8, i * 10240, SC1);
#pragma unroll
                        for (int i = 0; i < 5; ++i) old = old || y[i].y != tag;
                        if (__builtin_amdgcn_ballot_w64(old) == 0) break;
                        __builtin_amdgcn_s_sleep(YS2);
                    }
                    const float ys = ((__uint_as_float(y[0].x) + __uint_as_float(y[1].x)) + (__uint_as_float(y[2].x) + __uint_as_float(y[3].x))) + __uint_as_float(y[4].x);
                    const float xn = rbf(rbf(xr_ + nm) * rsj), u = rbf(rbf(ys) * sg);
                    ldsw<float>(lds0 + L_RG + k * 256 + lane * 4, silu_b(rbf(xn + u)) * -1.4426950408889634f);
                    ldsw<unsigned>(lds0 + L_RM + m * 4, L_RG + k * 256);
                }
            }
            ldsw<unsigned>(lds0 + FL_R, 1u);
            END();
        }
    {
        TL(0);
        LANE();
        const int fq = (j - 25) * 2 + (xcd & 1), f = fp * 14 + fq;
        const int n0 = fq * 40 + wv * 4 + g;
        epld();
        if (wv == 7 && lane < 16) ldsw<u32x4>(lds0 + MISC + lane * 16, (u32x4){0u, 0u, 0u, 0u});
        const int u2 = max(f - 6, 0), kr = (u2 * 26) >> 8, fc = u2 - kr * 10, n1 = wv < 4 ? 3 : 2;
        const int d0 = kr * 11 + wv, d1 = kr * 11 + 4 + wv; const bool two = wv >= 4 && wv < 7 && d1 < 54;
        const char* w1 = (const char*)(f & 1 ? wu : wg) + min(f >> 1, 26) * 128; const char* w2 = (const char*)wd + fc * 128;
        unsigned fv[2][4];
#pragma unroll
        for (int i = 0; i < 4; ++i) { const unsigned c = (((lane & 7) ^ (2 * ((lane >> 4) & 1) + 4 * (i & 1))) * 16) - i * 1024; fv[0][i] = ((lane >> 3) + i * 8) * (F_ * 2) + c; fv[1][i] = ((lane >> 3) + i * 8) * 1280 + c; }
        auto issue = [&](int m, int ks, int slot) {
            asm volatile("s_mov_b32 m0, %[l]\n\ts_nop 0\n\tglobal_load_lds_dwordx4 %0, %[b]" FPOL "\n\tglobal_load_lds_dwordx4 %1, %[b] offset:1024" FPOL "\n\tglobal_load_lds_dwordx4 %2, %[b] offset:2048" FPOL "\n\tglobal_load_lds_dwordx4 %3, %[b] offset:3072" FPOL
                         :: "v"(fv[m][0]), "v"(fv[m][1]), "v"(fv[m][2]), "v"(fv[m][3]), [b] "s"((m ? w2 : w1) + (unsigned)(ks * 32 * (m ? 1280 : F_ * 2))), [l] "s"(lds0 + wv * 16384 + slot * 4096) : "memory");
        };
        if (TCH && wv == 5) {
            const int l = min(f * 14 + lane, 770), o = l < 280 ? W_KEY + l * 128 : l < 536 ? W_ST + (l - 280) * 128 : W_Y + (l - 536) * 256;
            if (lane < 14) asm volatile("s_mov_b32 m0, %[l]\n\ts_nop 0\n\tbuffer_load_dword %0, %1, 0 offen lds" :: "v"(o), "s"(rs), [l] "s"(lds0 + L_DUM) : "memory");
        }
        BARN();
        auto wopf = [&]() {
            const int i = min(((j - 25) * 8 + wv) * 58 + lane, 3199), rw = (i * 13108) >> 16;
            asm volatile("s_mov_b32 m0, %[l]\n\ts_nop 0\n\tglobal_load_lds_dword %0, %[b]" :: "v"(rw * 640 + i * 128 + (xcd & 1) * 640), [b] "s"(wo), [l] "s"(lds0 + L_DUM) : "memory"); };
        if (WPF == 1) wopf();
        if (EARLYK) issue(0, wv, 0);
        asm volatile("s_waitcnt vmcnt(%2)" : "+{v[84:115]}"(sb), "+{v[116:123]}"(sc) : "n"(5 + 4 * EARLYK + (WPF == 1)) : "memory");
#define SXR(i) ((i) < 20 ? sa[(12 + (i)) & 31] : (i) < 52 ? sb[((i) - 20) & 31] : sc[((i) - 52) & 7])
        mktag();
        {
            const unsigned one2 = 0x3F803F80u;
            float wsum = 0.f;
#pragma unroll
            for (int e = 0; e < 20; ++e) wsum = dot2(SXR(40 + e), one2, wsum);
            wsum = sum16(wsum);
            auto stat = [&](int p) {
                float s1 = 0.f, s2 = 0.f, sr = 0.f;
#pragma unroll
                for (int e = 0; e < 20; ++e) { const unsigned u = SXR(20 * p + e); s1 = dot2(u, one2, s1); s2 = dot2(u, u, s2); sr = dot2(u, SXR(40 + e), sr); }
                const float mean = sum16(s1) * (1.f / 640.f);
                const unsigned mb2 = cvtpk(mean, mean); const float mb = lo2f(mb2);
                const float q = fmaf(40.f * mb, mb, fmaf(-2.f * mb, s1, s2));
                const float dm = mean - mb;
                const float var = fmaxf(sum16(q) * (1.f / 640.f) - dm * dm, 0.f);
                const float sd = rbf(__builtin_amdgcn_sqrtf(rbf(rbf(var) + 1e-5f)));
                const float rsc = rbf(fmaf(-mb, wsum, sum16(sr)) / sd);
                const int n = n0 + 32 * p, i = (fp & 1) * 512 + n;
                if (li == 0) {
                    asm volatile("buffer_store_dwordx4 %0, %1, %2, 0 offen sc1\n\ts_nop 1" :: "v"((u32x4){(mb2 << 16) ^ 0x80000000u, tag, __float_as_uint(__builtin_amdgcn_rcpf(sd)), tag}), "v"(n < 512 ? W_ST + (par * 1024 + i) * 16 : 0x40000000), "s"(rs) : "memory");
                    ldsw<unsigned>(lds0 + LS_ROW + (p * 32 + wv * 4 + g) * 4, n < 512 ? rkey(rsc, 2 * i + par) : 0u);
                }
            };
            asm volatile("" : "+v"(wsum));
            if (wv < 2) stat(1);
            asm volatile("s_cmp_lt_u32 %2, 2\n\ts_cbranch_scc1 1f\n\ts_waitcnt vmcnt(%1)\n1:\n\ts_waitcnt vmcnt(%1 + 1)" : "+{v[52:83]}"(sa) : "n"(4 * EARLYK + (WPF == 1)), "s"(wv) : "memory", "scc");
            TL(1);
            if (EK2 && wv < EK2) issue(0, wv, 0);
            stat(0);
            if (E2 && wv < 7) issue(0, wv + 8, 1);
        }
        BAR();
        if (wv == 6) asm volatile("buffer_store_dword %0, %1, %2, 0 offen sc1" :: "v"(tag), "v"(lane ? 0x40000000 : W_ACK + (f < 40 ? (f >> 1) * 128 + 40 + (f & 1) * 4 : 2560 + (f - 40) * 4)), "s"(rs) : "memory");
        if (wv == 7) {
            const unsigned k = ldsrv<unsigned>(lds0 + LS_ROW + min(lane, 39) * 4);
            const uint2 t2 = top2wave(lane < 40 ? k : 0u, 0u);
            if (lane == 0 && fq < 13) asm volatile("buffer_store_dwordx4 %0, %1, %2, 0 offen sc1\n\ts_nop 1" :: "v"((u32x4){t2.x, tag, t2.y, tag}), "v"(W_KEY + (fp * 13 + fq) * 16), "s"(rs) : "memory");
        }
        BARN();
        TL(2);
        if (WPF == 2) wopf();
        if (!EARLYK && wv >= EK2) issue(0, wv, 0);
        if (!E2 || wv == 7) issue(0, wv + 8, 1);
        if (wv < 4) issue(0, wv + 16, 2);
        else if (!WPF) asm volatile("s_mov_b32 m0, %[l]\n\ts_mov_b32 exec_hi, 0\n\tglobal_load_lds_dword %0, %[b]\n\ts_mov_b32 exec_hi, -1" :: "v"(min(f * 128 + (wv - 4) * 32 + lane, 6399) * 128), [b] "s"(wo), [l] "s"(lds0 + L_DUM) : "memory");
        if (wv >= 4 && wv < 4 + P2A) issue(1, d0, 3);
        u32x4 kk;
        BARN();
        asm volatile("s_cmp_lg_u32 %3, 7\n\ts_cbranch_scc1 1f\n\ts_sleep " KSL "\n\tbuffer_load_dwordx4 %0, %1, %2, 0 offen sc1\n1:\n\ts_barrier" : "=&v"(kk) : "v"(W_KEY + min(lane, 51) * 16), "s"(rs), "s"(wv) : "memory", "scc");
        if (wv != 7 && !(wv >= 4 && wv < 4 + P2A)) issue(1, d0, 3);
        if (P2A && two) issue(1, d1, 2);
        asm volatile("s_cmp_lg_u32 %1, 7\n\ts_cbranch_scc1 1f\n\ts_waitcnt vmcnt(0)\n1:" : "+v"(kk) : "s"(wv) : "memory", "scc");
        if (wv == 7) {
            SPIN {
                if (__builtin_amdgcn_ballot_w64(kk.y != tag || kk.w != tag) == 0) break;
                asm volatile("buffer_load_dwordx4 %0, %1, %2, 0 offen sc1\n\ts_waitcnt vmcnt(0)" : "=&v"(kk) : "v"(W_KEY + min(lane, 51) * 16), "s"(rs) : "memory");
            }
            const uint2 t2 = top2wave(lane < 52 ? kk.x : 0u, lane < 52 ? kk.z : 0u);
            if (lane < 2) ldsw<unsigned>(lds0 + ROUT + lane * 8, (0xffffu - ((lane ? t2.y : t2.x) & 0xffffu)) & 2047u);
        }
        BAR();
        TL(3);
        const unsigned row = ldsrv<unsigned>(lds0 + ROUT + (li & 1) * 8);
        u32x4 af[3], st;
        asm volatile("global_load_dwordx4 %0, %4, %5\n\tglobal_load_dwordx4 %1, %4, %5 offset:512\n\tglobal_load_dwordx4 %2, %6, %5 offset:1024\n\tbuffer_load_dwordx4 %3, %7, %8, 0 offen sc1"
                     : "=&v"(af[0]), "=&v"(af[1]), "=&v"(af[2]), "=&v"(st) : "v"(row * 1280u + wv * 64 + g * 16), "s"(x), "v"(row * 1280u + (wv & 3) * 64 + g * 16), "v"(wv ? 0x40000000 : W_ST + ((row & 1) * 1024 + (row >> 1)) * 16), "s"(rs) : "memory");
        bf16x8 wf[3][4];
        unsigned ra[4];
#pragma unroll
        for (int q = 0; q < 4; ++q) ra[q] = lds0 + wv * 16384 + (unsigned)((8 * g + (li >> 2)) * 128 + (((q ^ (((li >> 3) & 1) + 2 * (g & 1))) * 32) + (li & 3) * 8));
        auto rdf = [&](int k, int slot) {
#pragma unroll
            for (int q = 0; q < 4; ++q) wf[k][q] = cat8(trread(ra[q] + slot * 4096), trread(ra[q] + slot * 4096 + 512));
        };
        if (wv == 7) issue(1, d0, 3);
        if (!P2A && two) issue(1, d1, 2);
        if (two) wvm<12>(); else wvm<8>();
        rdf(0, 0); rdf(1, 1); if (wv < 4) rdf(2, 2);
        asm volatile("s_cmp_eq_u32 %4, 0\n\ts_cbranch_scc0 1f\n\ts_waitcnt vmcnt(0)\n1:\n\ts_waitcnt vmcnt(4)" : "+v"(af[0]), "+v"(af[1]), "+v"(af[2]), "+v"(st) : "s"(RFL((!P2A && two) || wv == 7)) : "memory", "scc");
        f32x4 c[4];
#pragma unroll
        for (int q = 0; q < 4; ++q) c[q] = (f32x4){0.f, 0.f, 0.f, 0.f};
#pragma unroll
        for (int k = 0; k < 3; ++k) if (k < n1) {
#pragma unroll
            for (int e = 0; e < 4; ++e) af[k][e] = li & 2 ? 0x3F803F80u : af[k][e];
#pragma unroll
            for (int q = 0; q < 4; ++q) c[q] = mfma32(__builtin_bit_cast(bf16x8, af[k]), wf[k][q], c[q]);
        }
        auto total = [&](int off) {
            ldsw<f32x4>(lds0 + wv * 16384 + off + lane * 16, g == 0 ? c[0] : g == 1 ? c[1] : g == 2 ? c[2] : c[3]);
            BAR();
            if (wv) return c[0];
            f32x4 pr[8];
#pragma unroll
            for (int w = 0; w < 8; ++w) pr[w] = ldsrv<f32x4>(lds0 + w * 16384 + off + lane * 16);
            return ((pr[0] + pr[1]) + (pr[2] + pr[3])) + ((pr[4] + pr[5]) + (pr[6] + pr[7]));
        };
        TL(4);
        {
            const f32x4 s = total(0);
            if (wv == 0 && f < 54) {
                SPIN {
                    if (__builtin_amdgcn_ballot_w64(st.y != tag || st.w != tag) == 0) break;
                    asm volatile("buffer_load_dwordx4 %0, %1, %2, 0 offen sc1\n\ts_waitcnt vmcnt(0)" : "=&v"(st) : "v"(W_ST + ((row & 1) * 1024 + (row >> 1)) * 16), "s"(rs) : "memory");
                }
                const float n0_ = __uint_as_float(__builtin_amdgcn_readlane(st.x, 0)), n1_ = __uint_as_float(__builtin_amdgcn_readlane(st.x, 1)), i0 = __uint_as_float(__builtin_amdgcn_readlane(st.z, 0)), i1 = __uint_as_float(__builtin_amdgcn_readlane(st.z, 1));
                float v0 = rbf(fmaf(n0_, s.z, s.x) * i0), v1 = rbf(fmaf(n1_, s.z, s.y) * i1);
                if (!(f & 1)) { v0 = silu_b(v0); v1 = silu_b(v1); }
                const unsigned hb = cvtpk(v0, v1), nb = dppu<0xB1>(hb);
                const unsigned pay = (lane & 1) ? __builtin_amdgcn_perm(hb, nb, 0x07060302u) : __builtin_amdgcn_perm(nb, hb, 0x05040100u);
                asm volatile("buffer_store_dwordx2 %0, %1, %2, 0 offen sc1" :: "v"((u32x2){pay, tag}), "v"(W_H + ((f & 1) * 2 + (lane & 1)) * 6912 + ((f >> 1) * 32 + (lane >> 1)) * 8), "s"(rs) : "memory");
            }
        }
        TL(5);
        if (f == 0 && wv == 1) {
            SPIN {
                u32x4 a_;
                asm volatile("buffer_load_dwordx4 %0, %1, %2, 0 offen sc1\n\ts_waitcnt vmcnt(0)" : "=&v"(a_) : "v"(W_ACK + (lane < 60 ? ((lane * 43) >> 7) * 80 + lane * 16 : 1600 + lane * 16)), "s"(rs) : "memory");
                if (__builtin_amdgcn_ballot_w64(a_.x != tag || a_.y != tag || a_.z != tag || a_.w != tag) == 0) break;
                __builtin_amdgcn_s_sleep(8);
            }
            if (lane == 0) __builtin_amdgcn_raw_buffer_store_b32(tag, rs, W_EP, 0, SC1);
        }
        if (f >= 6) {
            wvm<0>();
            rdf(0, 3); rdf(1, 2);
            const int ho = W_H + ((lane >> 4) & 1) * 6912 + ((lane >> 5 && two ? d1 : d0) * 16 + li) * 8;
            u32x2 hg, hu;
            __builtin_amdgcn_s_sleep(HSL);
            SPIN {
                asm volatile("buffer_load_dwordx2 %0, %2, %3, 0 offen sc1\n\tbuffer_load_dwordx2 %1, %2, %3, %4 offen sc1\n\ts_waitcnt vmcnt(0)" : "=&v"(hg), "=&v"(hu) : "v"(ho), "s"(rs), "s"(13824) : "memory");
                if (__builtin_amdgcn_ballot_w64(hg.y != tag || hu.y != tag) == 0) break;
                __builtin_amdgcn_s_sleep(HS2);
            }
            ldsw<unsigned>(lds0 + wv * 16384 + 4096 + lane * 4, cvtpk(lo2f(hg.x) * lo2f(hu.x), hi2f(hg.x) * hi2f(hu.x)));
#pragma unroll
            for (int q = 0; q < 4; ++q) c[q] = (f32x4){0.f, 0.f, 0.f, 0.f};
#pragma unroll
            for (int k = 0; k < 2; ++k) if (k == 0 || two) {
                const u32x4 h = ldsrv<u32x4>(lds0 + wv * 16384 + 4096 + k * 128 + (li & 1) * 64 + g * 16);
#pragma unroll
                for (int q = 0; q < 4; ++q) c[q] = mfma32(__builtin_bit_cast(bf16x8, h), wf[k][q], c[q]);
            }
            const f32x4 ys = total(1024);
            if (wv == 0) __builtin_amdgcn_raw_buffer_store_b128((u32x4){__float_as_uint(ys.x), tag, __float_as_uint(ys.y), tag}, rs, W_Y + kr * 10240 + (fc * 64 + lane) * 16, 0, SC1);
        }
        TL(6);
        TLEND();
        wvm<0>();
    }
}

extern "C" void kernel_launch(void* const* d_in, const int* in_sizes, int n_in, void* d_out, int out_size, void* d_ws,
                              size_t ws_size, hipStream_t stream) {
    (void)hipFuncSetAttribute((const void*)fused, hipFuncAttributeMaxDynamicSharedMemorySize, LDSZ);
    fused<<<256, 512, LDSZ, stream>>>((const u16*)d_in[0], (const u16*)d_in[1], (const u16*)d_in[2], (const u16*)d_in[4],
                                      (const u16*)d_in[5], (const u16*)d_in[6], (const u16*)d_in[7], (char*)d_ws, (u16*)d_out);
}
```

```cpp
#include <hip/hip_runtime.h>
#include <type_traits>

typedef __attribute__((ext_vector_type(8))) short bf16x8;
typedef __attribute__((ext_vector_type(4))) short bf16x4;
typedef __attribute__((ext_vector_type(4))) float f32x4;
typedef __attribute__((ext_vector_type(2))) float f32x2;
typedef __attribute__((ext_vector_type(4))) unsigned u32x4;
typedef __attribute__((ext_vector_type(2))) unsigned u32x2;
typedef __attribute__((ext_vector_type(2))) __bf16 bf16x2v;
typedef __attribute__((ext_vector_type(8))) unsigned u32x8;
typedef __attribute__((ext_vector_type(16))) unsigned u32x16;
typedef unsigned short u16;
typedef __attribute__((address_space(3))) char lchar;

#define F_ 1728
constexpr int W_EP = 0;
constexpr int W_KEY = 4096;
constexpr int W_ACK = 8192;
constexpr int W_H = 12288;
constexpr int W_ST = 65536;
constexpr int W_Y = 98304;
constexpr int W_O = 155648;
constexpr int W_END = W_O + 80 * 40960;
#define SENT 0xffffffffu
constexpr int XS = 14336, SLOT = XS + 24576;
constexpr int WO0 = 2 * SLOT;
constexpr int MISC = WO0 + 81920;
constexpr int FL_S = MISC, FL_P = MISC + 16, FL_E = MISC + 32, FL_W = MISC + 48, FL_R = MISC + 64, FL_AUX = MISC + 80, ONES = MISC + 128, ROUT = MISC + 144, LTAG = MISC + 160;
constexpr int L_NM = MISC + 256, L_RS = L_NM + 512 + 64, L_RM = L_RS + 512, L_RG = L_RM + 512, L_DUM = L_RG + 512, L_KEY = L_DUM + 512;
constexpr int LS_ROW = L_NM;
constexpr int LDSZ = 163840;
static_assert(L_KEY + 1024 <= LDSZ, "lds");
#define SPIN_MAX (1 << 21)
#define SPIN _Pragma("nounroll") for (int it = 0; it < SPIN_MAX; ++it)

__device__ __forceinline__ unsigned cvtpk(float lo, float hi) { return __builtin_bit_cast(unsigned, __builtin_convertvector((f32x2){lo, hi}, bf16x2v)); }
__device__ __forceinline__ unsigned nosent(unsigned u) { return u < 0xfffffffeu ? u : 0xfffffffeu; }
__device__ __forceinline__ float lo2f(unsigned u) { return __uint_as_float(u << 16); }
__device__ __forceinline__ float hi2f(unsigned u) { return __uint_as_float(u & 0xffff0000u); }
__device__ __forceinline__ float rbf(float f) { return lo2f(cvtpk(f, f)); }
template <int CTRL> __device__ __forceinline__ float dppf(float x) { return __builtin_bit_cast(float, __builtin_amdgcn_mov_dpp(__builtin_bit_cast(int, x), CTRL, 0xf, 0xf, true)); }
template <int CTRL> __device__ __forceinline__ unsigned dppu(unsigned x) { return (unsigned)__builtin_amdgcn_mov_dpp((int)x, CTRL, 0xf, 0xf, true); }
__device__ __forceinline__ void sw16(unsigned& a, unsigned& b) { const auto r = __builtin_amdgcn_permlane16_swap(a, b, false, false); a = r[0]; b = r[1]; }
__device__ __forceinline__ void sw32(unsigned& a, unsigned& b) { const auto r = __builtin_amdgcn_permlane32_swap(a, b, false, false); a = r[0]; b = r[1]; }
__device__ __forceinline__ void sw16f(float& a, float& b) { unsigned x = __float_as_uint(a), y = __float_as_uint(b); sw16(x, y); a = __uint_as_float(x); b = __uint_as_float(y); }
__device__ __forceinline__ void sw32f(float& a, float& b) { unsigned x = __float_as_uint(a), y = __float_as_uint(b); sw32(x, y); a = __uint_as_float(x); b = __uint_as_float(y); }
__device__ __forceinline__ float vmax(float a, float b) { return __builtin_elementwise_maximum(a, b); }
__device__ __forceinline__ float sum16(float x) { x += dppf<0xB1>(x); x += dppf<0x4E>(x); x += dppf<0x141>(x); x += dppf<0x140>(x); return x; }
__device__ __forceinline__ float dot2(unsigned a, unsigned b, float c) { return __builtin_amdgcn_fdot2_f32_bf16(__builtin_bit_cast(bf16x2v, a), __builtin_bit_cast(bf16x2v, b), c, false); }
__device__ __forceinline__ float silu_b(float v) { return v * __builtin_amdgcn_rcpf(__builtin_amdgcn_exp2f(v * -1.4426950408889634f) + 1.f); }
__device__ __forceinline__ unsigned rkey(float rb, int row) {
    unsigned b = __float_as_uint(rb) >> 16; b = (b & 0x8000u) ? (~b & 0xffffu) : (b | 0x8000u);
    return (b << 16) | (unsigned)(0xffff - row);
}
__device__ __forceinline__ float keyscore(unsigned k) { const unsigned b = k >> 16; return __uint_as_float(((b & 0x8000u) ? (b & 0x7fffu) : (~b & 0xffffu)) << 16); }
#define T2M(b0, b1) { const unsigned _b0 = (b0), _b1 = (b1); const unsigned _m = min(k0, _b0); k0 = max(k0, _b0); k1 = max(_m, max(k1, _b1)); }
__device__ __forceinline__ uint2 top2wave(unsigned k0, unsigned k1) {
    T2M(dppu<0xB1>(k0), dppu<0xB1>(k1)); T2M(dppu<0x4E>(k0), dppu<0x4E>(k1));
    T2M(dppu<0x141>(k0), dppu<0x141>(k1)); T2M(dppu<0x140>(k0), dppu<0x140>(k1));
    const unsigned a0 = __builtin_amdgcn_readlane(k0, 0), a1 = __builtin_amdgcn_readlane(k1, 0);
    const unsigned c0 = __builtin_amdgcn_readlane(k0, 16), c1 = __builtin_amdgcn_readlane(k1, 16);
    const unsigned d0 = __builtin_amdgcn_readlane(k0, 32), d1 = __builtin_amdgcn_readlane(k1, 32);
    const unsigned e0 = __builtin_amdgcn_readlane(k0, 48), e1 = __builtin_amdgcn_readlane(k1, 48);
    k0 = a0; k1 = a1; T2M(c0, c1); T2M(d0, d1); T2M(e0, e1);
    return make_uint2(k0, k1);
}
template <int N> __device__ __forceinline__ void wvm() { asm volatile("" ::: "memory"); __builtin_amdgcn_s_waitcnt((N & 15) | ((N >> 4) << 14) | 0x0F70); asm volatile("" ::: "memory"); }
__device__ __forceinline__ bf16x4 trread(unsigned a) { return __builtin_amdgcn_ds_read_tr16_b64_v4i16((__attribute__((address_space(3))) bf16x4*)(size_t)a); }
template <class T> __device__ __forceinline__ T ldsr(unsigned a) { return *(const __attribute__((address_space(3))) T*)(size_t)a; }
template <class T> __device__ __forceinline__ T ldsrv(unsigned a) { return *(volatile __attribute__((address_space(3))) T*)(size_t)a; }
template <class T> __device__ __forceinline__ void ldsw(unsigned a, T v) { *(volatile __attribute__((address_space(3))) T*)(size_t)a = v; }
__device__ __forceinline__ bf16x8 cat8(bf16x4 lo, bf16x4 hi) { return (bf16x8){lo[0], lo[1], lo[2], lo[3], hi[0], hi[1], hi[2], hi[3]}; }
__device__ __forceinline__ bf16x8 frag4(unsigned a, unsigned b, unsigned c, unsigned d) { return __builtin_bit_cast(bf16x8, (u32x4){a, b, c, d}); }
__device__ __forceinline__ f32x4 mfma32(bf16x8 a, bf16x8 b, f32x4 c) { return __builtin_amdgcn_mfma_f32_16x16x32_bf16(a, b, c, 0, 0, 0); }
__device__ __forceinline__ int lane_id() { int l; asm volatile("v_mbcnt_lo_u32_b32 %0, -1, 0\n\tv_mbcnt_hi_u32_b32 %0, -1, %0" : "=v"(l)); __builtin_assume(l >= 0 && l < 64); return l; }
#define SB() __builtin_amdgcn_sched_barrier(0)
#define BAR() asm volatile("s_waitcnt lgkmcnt(0)\n\ts_barrier" ::: "memory")
#define MEMB() asm volatile("" ::: "memory")
#define RFL(x) ((unsigned)__builtin_amdgcn_readfirstlane((int)(x)))
#define SC1 16

#define WOPF 1
#define DSEL "0"
#define TRG1D
#define TRG2D
#define PRIA "s_setprio 2\n\t"
#define TCHD
#define RSLP "s_sleep 3\n\t"
#define PRIR "s_setprio 0\n\t"
#define PRI1 "s_setprio 3\n\t"
#define PRIW
#define PRIX "s_setprio 3\n\t"
#define PRIO
#define RSL0 "s_sleep 6\n\t"
#define RSL1 "s_sleep 3\n\t"
#define ASLP "s_sleep 1\n\t"
#define PRIS
#define PRIG "s_setprio 0\n\t"
#define PSLP "s_sleep 1\n\t"
#define HSL 26
#define HS2 10
#define TCH 1
#define KSL "0"
#define E2 0
#define P2A 0
#define FPOL " nt"
#define EK2 7
#define YS2 2
#define QPF 0
#define WPF 0
#define EARLYK 0
#define NOPK(v) asm("" : "+v"(v))
#define OPQ(v) asm volatile("" : "+v"(v))
#define IB(m) __builtin_amdgcn_inverse_ballot_w64(m)
#define END() do { asm volatile("s_endpgm"); __builtin_amdgcn_s_waitcnt(0); return; } while (0)
#define BARN() asm volatile("s_barrier" ::: "memory")
#define TL(i)
#define TA(i)
#define TLEND()
__device__ __forceinline__ constexpr int phys(int S) { return (S + 1) % 3; }

__global__ __launch_bounds__(512, 2) void fused(const u16* x_, const u16* wqkv_, const u16* wo_, const u16* w_r_, const u16* wg_, const u16* wu_, const u16* wd_, char* ws_, u16* out_) {
    extern __shared__ __attribute__((aligned(16))) char smem[];
#define SL(k)
#define DUMP
#define SZ
#define SB_(k)
#define SO(k)
#define RINC
#define RZ
#define SA(k)
#define ST(h)
#define SP0
#define LINC
#define GINC
#define RCNT
    typedef __attribute__((ext_vector_type(32))) unsigned u32x32;
    u32x32 sa, sb; u32x8 sc;
    unsigned tag, bid = blockIdx.x; int tid = threadIdx.x;
    typedef __attribute__((address_space(1))) char gchar;
    typedef gchar* const __attribute__((address_space(4)))* karg_t;
    karg_t ka = (karg_t)__builtin_amdgcn_kernarg_segment_ptr();
    asm volatile(
            SL(0) SZ
            "s_load_dwordx2 s[40:41], %[k], 0x8\n\ts_load_dwordx2 s[36:37], %[k], 0x0\n\tv_readfirstlane_b32 s69, %[t]\n\ts_lshr_b32 s70, %[b], 3\n\ts_cmpk_lg_u32 s69, 0xc0\n\ts_cbranch_scc1 1f\n\t"
            "s_getpc_b64 s[72:73]\n\t.Lch:\ns_lshl3_add_u32 s71, s70, 0xffffff40\n\tv_add_lshl_u32 v36, s71, %[t], 7\n\tv_min_u32 v36, .Lce-.Lch-4, v36\n\ts_mov_b32 m0, 162368\n\t"
            "s_mov_b64 exec, 0xff\n\tglobal_load_lds_dword v36, s[72:73]\n\ts_mov_b64 exec, -1\n\t1:\ns_add_i32 s70, s70, 25\n\ts_and_b32 s70, s70, 31\n\t"
            "s_cmp_lt_u32 s70, 25\n\ts_cbranch_scc0 .LF\n\tv_bfe_u32 v36, %[t], 3, 3\n\tv_and_b32 v37, 7, %[t]\n\ts_mul_i32 s71, s70, 13\n\ts_lshr_b32 s71, s71, 6\n\t"
            "v_and_b32 v38, 2, v36\n\tv_xor_b32 v46, v38, v37\n\ts_lshl_b32 s64, s71, 2\n\ts_sub_i32 s64, s70, s64\n\ts_sub_i32 s57, s64, s71\n\tv_mul_u32_u24 v40, 0xf00, v36\n\t"
            "s_sub_i32 s72, s64, 5\n\ts_min_u32 s64, s64, s72\n\tv_lshl_add_u32 v40, v46, 4, v40\n\ts_bfe_u32 s56, s69, 0x20006\n\ts_and_b32 s72, %[b], 1\n\ts_mul_i32 s72, s72, 5\n\t"
            "s_add_i32 s58, s57, s72\n\ts_lshl_b32 s58, s58, 7\n\ts_mul_i32 s61, s64, 0x78000\n\ts_mul_i32 s73, s56, 0xf000\n\ts_add_i32 s73, s73, s58\n\ts_add_i32 s61, s61, s73\n\t"
            "s_mul_i32 s66, s56, 0x1800\n\ts_add_i32 m0, s66, 53248\n\ts_mov_b32 s42, 0x258000\n\ts_mov_b32 s43, 0x20000\n\tv_add_u32 v41, 0x100, v40\n\tv_add_u32 v42, 0x200, v40\n\t"
            "v_xor_b32 v43, 64, v40\n\tv_add_u32 v43, 0x6c00, v43\n\tv_add_u32 v44, 0x1100, v43\n\tv_add_u32 v45, 0x1200, v43\n\ts_cmpk_lt_u32 s69, 0x100\n\ts_cbranch_scc1 1f\n\t"
            "s_waitcnt lgkmcnt(0)\n\tbuffer_load_dwordx4 v40, s[40:43], s61 offen lds\n\tbuffer_load_dwordx4 v41, s[40:43], s61 offen offset:1024 lds\n\tbuffer_load_dwordx4 v42, s[40:43], s61 offen offset:2048 lds\n\tbuffer_load_dwordx4 v43, s[40:43], s61 offen offset:3072 lds\n\ts_addk_i32 m0, 0x1000\n\t"
            "s_nop 0\n\tbuffer_load_dwordx4 v44, s[40:43], s61 offen lds\n\tbuffer_load_dwordx4 v45, s[40:43], s61 offen offset:1024 lds\n\t1:\ns_mul_i32 s72, s56, 28\n\tv_add_u32 v39, s72, v36\n\t"
            "s_bfe_u32 s62, %[b], 0x10001\n\ts_mul_i32 s62, s62, 5\n\ts_add_i32 s62, s62, s71\n\ts_mul_i32 s62, s62, 104\n\ts_sub_i32 s62, s62, 8\n\tv_and_b32 v47, 3, v39\n\t"
            "v_lshl_add_u32 v47, v47, 2, s62\n\tv_and_b32 v48, 0x70, v39\n\tv_bfe_u32 v49, v39, 2, 2\n\tv_add3_u32 v48, v48, v49, v47\n\tv_bfe_u32 v49, v39, 1, 3\n\tv_xor_b32 v50, v49, v37\n\t"
            "v_mul_i32_i24 v48, 0xa00, v48\n\ts_bfe_u32 s63, %[b], 0x10002\n\ts_mul_i32 s63, s63, 0x500\n\ts_lshl_b32 s60, s64, 8\n\tv_mov_b32 v51, v36\n\tv_lshl_add_u32 v36, v50, 4, v48\n\t"
            "s_add_i32 s60, s60, s63\n\ts_mul_i32 s65, s56, 0xe00\n\ts_add_i32 m0, s65, 38912\n\ts_mov_b32 s38, 0x280000\n\ts_mov_b32 s39, 0x20000\n\tv_add_u32 v38, 0x9800, v36\n\t"
            "s_cmpk_lt_u32 s69, 0x100\n\ts_cbranch_scc1 1f\n\tbuffer_load_dwordx4 v36, s[36:39], s60 offen lds\n\tbuffer_load_dwordx4 v38, s[36:39], s60 offen offset:2048 lds\n\ts_barrier\n\t1:\n"
            "s_load_dwordx2 s[44:45], %[k], 0x38\n\ts_load_dwordx2 s[48:49], %[k], 0x10\n\ts_load_dwordx2 s[52:53], %[k], 0x40\n\tv_add_u32 v39, 8, v39\n\tv_and_b32 v48, 0x70, v39\n\tv_bfe_u32 v49, v39, 2, 2\n\t"
            "v_add3_u32 v48, v48, v49, v47\n\tv_xor_b32 v50, 4, v50\n\tv_mul_i32_i24 v48, 0xa00, v48\n\tv_lshl_add_u32 v37, v50, 4, v48\n\tv_subrev_u32 v37, 0x400, v37\n\tv_add_u32 v39, 0x9800, v37\n\t"
            "s_addk_i32 s66, 14336\n\ts_bfe_u32 s59, %[b], 0x20001\n\ts_mul_i32 s59, s59, 5\n\ts_add_i32 s59, s59, s71\n\ts_mov_b32 s46, 3432448\n\ts_mov_b32 s47, 0x20000\n\t"
            "s_mov_b32 s54, 0x280000\n\ts_mov_b32 s55, 0x20000\n\tv_and_b32 v52, 63, %[t]\n\tv_bfe_u32 v53, %[t], 4, 2\n\tv_and_b32 v54, 15, %[t]\n\tv_and_b32 v55, 3, %[t]\n\t"
            "v_bfe_u32 v56, %[t], 2, 2\n\tv_lshl_add_u32 v57, v55, 2, v56\n\ts_lshr_b32 s69, s69, 6\n\ts_cmp_lt_u32 s69, 4\n\ts_cbranch_scc0 .LB\n\ts_branch .LA\n\t"
            ".LF:\ns_load_dwordx2 s[72:73], %[k], 0x18\n\ts_and_b32 s71, %[b], 1\n\ts_lshl1_add_u32 s71, s70, s71\n\ts_mul_i32 s71, s71, 40\n\ts_sub_u32 s71, s71, 2000\n\t"
            "v_lshrrev_b32 v2, 4, %[t]\n\tv_add_u32 v2, s71, v2\n\tv_add_u32 v3, 32, v2\n\tv_min_u32 v2, 0x1ff, v2\n\tv_min_u32 v3, 0x1ff, v3\n\tv_and_b32 v4, 15, %[t]\n\t"
            "v_lshlrev_b32 v4, 4, v4\n\ts_bfe_u32 s71, %[b], 0x10001\n\ts_mul_i32 s71, s71, 0x140000\n\ts_bfe_u32 s74, %[b], 0x10002\n\ts_mul_i32 s74, s74, 0x500\n\ts_add_u32 s71, s71, s74\n\t"
            "v_add_u32 v5, s71, v4\n\ts_movk_i32 s71, 0xa00\n\tv_mad_u32_u24 v2, v2, s71, v5\n\tv_mad_u32_u24 v3, v3, s71, v5\n\ts_waitcnt lgkmcnt(0)\n\tglobal_load_dwordx4 v[104:107], v4, s[72:73] offset:0\n\t"
            "global_load_dwordx4 v[108:111], v4, s[72:73] offset:256\n\tglobal_load_dwordx4 v[112:115], v4, s[72:73] offset:512\n\tglobal_load_dwordx4 v[116:119], v4, s[72:73] offset:768\n\tglobal_load_dwordx4 v[120:123], v4, s[72:73] offset:1024\n\ts_cmpk_ge_u32 s69, 0x80\n\ts_cbranch_scc1 1f\n\t"
            "global_load_dwordx4 v[84:87], v3, s[36:37] offset:0\n\tglobal_load_dwordx4 v[88:91], v3, s[36:37] offset:256\n\tglobal_load_dwordx4 v[92:95], v3, s[36:37] offset:512\n\tglobal_load_dwordx4 v[96:99], v3, s[36:37] offset:768\n\tglobal_load_dwordx4 v[100:103], v3, s[36:37] offset:1024\n\t1:\n"
            "global_load_dwordx4 v[64:67], v2, s[36:37] offset:0\n\tglobal_load_dwordx4 v[68:71], v2, s[36:37] offset:256\n\tglobal_load_dwordx4 v[72:75], v2, s[36:37] offset:512\n\tglobal_load_dwordx4 v[76:79], v2, s[36:37] offset:768\n\tglobal_load_dwordx4 v[80:83], v2, s[36:37] offset:1024\n\ts_branch .Lout\n\t"
            ".LA:\nv_lshrrev_b32 v2, 1, v54\n\tv_xor_b32 v2, v2, v53\n\tv_lshlrev_b32 v3, 7, v54\n\tv_lshl_add_u32 v2, v2, 4, v3\n\tv_xor_b32 v3, 64, v2\n\t"
            "v_add_u32 v4, 38912, v2\n\tv_add_u32 v5, 38912, v3\n\tv_add_u32 v10, 77824, v2\n\tv_add_u32 v11, 77824, v3\n\tv_mov_b32 v12, 159872\n\tv_cmp_eq_u32 vcc, 0, v54\n\t"
            "v_mov_b32 v60, 0\n\tv_mov_b32 v61, 0\n\tv_mov_b32 v62, 0x3f803f80\n\tv_lshlrev_b32 v63, 3, v52\n\tv_cndmask_b32 v6, v2, v12, vcc\n\tv_cndmask_b32 v7, v3, v12, vcc\n\t"
            "v_cndmask_b32 v8, v4, v12, vcc\n\tv_cndmask_b32 v9, v5, v12, vcc\n\tv_cndmask_b32 v10, v10, v12, vcc\n\tv_cndmask_b32 v11, v11, v12, vcc\n\tv_lshlrev_b32 v64, 2, v52\n\tv_lshrrev_b32 v65, 2, v52\n\t"
            "v_cmp_eq_u32 vcc, 8, v65\n\tv_add_u32 v66, 161088, v63\n\tv_add_u32 v64, 159744, v64\n\tv_cndmask_b32 v62, 0, v62, vcc\n\ts_cmp_lg_u32 s69, 1\n\ts_cbranch_scc1 1f\n\t"
            "ds_write_b32 v64, v62\n\tds_write_b64 v66, v[60:61]\n\t1:\nv_bfe_u32 v12, v52, 3, 2\n\tv_xor_b32 v12, s69, v12\n\tv_lshlrev_b32 v13, 7, v56\n\t"
            "v_lshl_add_u32 v13, v55, 3, v13\n\tv_lshl_add_u32 v12, v12, 5, v13\n\tv_mul_u32_u24 v13, 0xc00, v53\n\ts_movk_i32 s72, 14336\n\tv_add3_u32 v12, v12, v13, s72\n\tv_add_u32 v13, 38912, v12\n\t"
            "s_mov_b32 s72, 160000\n\tv_lshl_add_u32 v14, v54, 2, s72\n\tv_lshl_add_u32 v15, v53, 4, s72\n\ts_cmp_eq_u32 s69, 3\n\ts_cselect_b32 s74, 1, 0\n\ts_cselect_b64 s[76:77], -1, 0\n\t"
            "s_cselect_b32 s75, 4, 0\n\ts_add_i32 s72, s69, 1\n\ts_bfe_u32 s72, s72, 0x10001\n\ts_xor_b32 s72, s72, 1\n\ts_lshr_b32 s73, s69, 1\n\ts_lshl1_add_u32 s78, s72, s73\n\t"
            "s_lshl2_add_u32 s78, s78, 159776\n\tv_mov_b32 v34, s78\n\ts_lshl2_add_u32 s78, s69, 159744\n\tv_mov_b32 v33, s78\n\ts_mul_i32 s78, s59, 163840\n\ts_lshl_b32 s79, s58, 5\n\t"
            "s_add_i32 s78, s78, s79\n\ts_lshl_b32 s79, s69, 10\n\ts_add_i32 s78, s78, s79\n\ts_add_i32 s78, s78, 155648\n\tv_lshl_add_u32 v16, v52, 4, s78\n\tv_and_b32 v215, 1, v53\n\t"
            "v_lshl_add_u32 v215, v215, 30, v16\n\ts_lshl_b32 s79, s74, 11\n\ts_add_i32 s78, s78, s79\n\ts_sub_i32 s78, s78, 0x400\n\ts_cmp_lg_u32 s57, s75\n\ts_cselect_b32 s79, s72, 0\n\t"
            "s_cmp_lg_u32 s79, 0\n\ts_cselect_b32 s78, s78, 0x40000000\n\tv_lshl_add_u32 v17, v52, 4, s78\n\ts_sub_i32 s78, s69, s57\n\ts_add_i32 s79, s78, 5\n\ts_min_u32 s78, s78, s79\n\t"
            "v_cmp_gt_u32 s[80:81], s78, v53\n\tv_cmp_gt_u32 s[82:83], s78, v56\n\ts_mov_b32 s14, 0x33363333\n\ts_mov_b32 s15, 0x36663366\n\ts_mov_b32 s16, 0x666c6666\n\ts_mov_b32 s17, 0x6ccc66cc\n\t"
            "s_mov_b32 s18, 0x33333333\n\ts_mov_b32 s19, 0x33333333\n\ts_xor_b64 s[80:81], s[80:81], s[76:77]\n\ts_xor_b64 s[82:83], s[82:83], s[76:77]\n\ts_andn2_b64 vcc, s[80:81], s[18:19]\n\tv_cndmask_b32 v18, 0, -1, vcc\n\t"
            "s_and_b64 vcc, s[80:81], s[18:19]\n\tv_cndmask_b32 v19, 0, -1, vcc\n\ts_nor_b64 vcc, s[80:81], s[18:19]\n\tv_cndmask_b32 v20, 0, -1, vcc\n\ts_andn2_b64 vcc, s[18:19], s[80:81]\n\tv_cndmask_b32 v21, 0, -1, vcc\n\t"
            "v_cndmask_b32 v22, 0, -1, s[82:83]\n\tv_cndmask_b32 v23, -1, 0, s[82:83]\n\tv_sub_u32 v58, v57, v53\n\tv_add_u32 v58, 4, v58\n\tv_and_b32 v58, 28, v58\n\tv_lshlrev_b32 v58, 1, v58\n\t"
            "v_mov_b32 v24, -1\n\tv_mov_b32 v25, 0\n\tv_lshlrev_b64 v[24:25], v58, v[24:25]\n\tv_lshlrev_b64 v[24:25], v58, v[24:25]\n\tv_sub_u32 v58, 32, v58\n\tv_min_u32 v58, 16, v58\n\t"
            "v_lshrrev_b32 v26, v58, -1\n\tv_lshrrev_b32 v26, v58, v26\n\ts_lshl1_add_u32 s78, s69, s74\n\ts_xor_b32 s79, s78, 1\n\ts_mul_i32 s78, s78, 0xc00\n\ts_mul_i32 s79, s79, 0xc00\n\t"
            "s_addk_i32 s78, 14336\n\ts_addk_i32 s79, 14336\n\tv_add_u32 v28, s78, v63\n\tv_add_u32 v29, s79, v63\n\ts_cmp_eq_u32 s69, s57\n\ts_addc_u32 s78, s69, 0\n\t"
            "s_add_i32 s79, s69, 1\n\ts_cmp_eq_u32 s79, s57\n\ts_subb_u32 s79, s79, 0\n\ts_cmp_eq_u32 s74, 1\n\ts_cselect_b32 s80, s79, s78\n\ts_cselect_b32 s81, s78, s79\n\t"
            "v_lshlrev_b32 v58, 2, v53\n\tv_lshl_add_u32 v58, v54, 4, v58\n\ts_cmp_gt_u32 s80, s57\n\ts_subb_u32 s82, s80, 0\n\ts_lshl2_add_u32 s82, s82, 159760\n\tv_mov_b32 v59, s82\n\t"
            "s_lshl_b32 s82, s80, 1\n\ts_max_u32 s82, s82, 1\n\ts_mul_i32 s82, s82, 0xc00\n\ts_addk_i32 s82, 11264\n\tv_add_u32 v30, s82, v58\n\ts_cmp_gt_u32 s81, s57\n\t"
            "s_subb_u32 s82, s81, 0\n\ts_lshl2_add_u32 s82, s82, 159760\n\tv_mov_b32 v60, s82\n\ts_lshl_b32 s82, s81, 1\n\ts_max_u32 s82, s82, 1\n\ts_mul_i32 s82, s82, 0xc00\n\t"
            "s_addk_i32 s82, 11264\n\tv_add_u32 v31, s82, v58\n\tv_cndmask_b32 v32, v59, v60, s[18:19]\n\tv_mov_b32 v35, 1\n\ts_waitcnt lgkmcnt(0)\n\ts_barrier\n\t"
            "s_load_dword s67, s[44:45], 0x0\n\ts_mov_b32 s20, 0\n\ts_mov_b32 s21, 40960\n\ts_mov_b32 s22, 81920\n\ts_mov_b32 s23, 122880\n\ts_add_u32 m0, s65, 38912\n\t"
            "s_nop 0\n\tbuffer_load_dwordx4 v37, s[36:39], s60 offen offset:1024 lds\n\ts_mov_b32 exec_hi, 0\n\tbuffer_load_dwordx4 v39, s[36:39], s60 offen offset:3072 lds\n\ts_mov_b32 exec_hi, -1\n\ts_cmp_eq_u32 s64, 4\n\t"
            "s_cselect_b32 s72, 0, 0\n\ts_add_i32 s72, s72, 128\n\ts_mul_i32 s73, s72, 1920\n\ts_add_u32 s60, s60, s72\n\ts_add_u32 m0, s66, 77824\n\ts_add_u32 s61, s61, s73\n\t"
            "buffer_load_dwordx4 v40, s[40:43], s61 offen lds\n\tbuffer_load_dwordx4 v41, s[40:43], s61 offen offset:1024 lds\n\tbuffer_load_dwordx4 v42, s[40:43], s61 offen offset:2048 lds\n\tbuffer_load_dwordx4 v43, s[40:43], s61 offen offset:3072 lds\n\ts_addk_i32 m0, 0x1000\n\ts_nop 0\n\t"
            "buffer_load_dwordx4 v44, s[40:43], s61 offen lds\n\tbuffer_load_dwordx4 v45, s[40:43], s61 offen offset:1024 lds\n\ts_add_u32 m0, s65, 77824\n\ts_nop 0\n\tbuffer_load_dwordx4 v36, s[36:39], s60 offen lds\n\tbuffer_load_dwordx4 v37, s[36:39], s60 offen offset:1024 lds\n\t"
            "buffer_load_dwordx4 v38, s[36:39], s60 offen offset:2048 lds\n\ts_mov_b32 exec_hi, 0\n\tbuffer_load_dwordx4 v39, s[36:39], s60 offen offset:3072 lds\n\ts_mov_b32 exec_hi, -1\n\ts_mov_b32 s68, 0x200000\n\ts_waitcnt lgkmcnt(0)\n\t"
            "s_mul_i32 s67, s67, 0x19660d\n\ts_add_u32 s67, s67, 0x3c6ef35f\n\ts_waitcnt vmcnt(10)\n\ts_barrier\n\t"
            SA(1) ST(-1)
            "ds_read_b64_tr_b16 v[176:177], v13 offset:0\n\tds_read_b64_tr_b16 v[178:179], v13 offset:512\n\tds_read_b128 v[120:123], v8\n\tds_read_b128 v[124:127], v4 offset:2048\n\tds_read_b128 v[128:131], v4 offset:4096\n\tds_read_b128 v[132:135], v4 offset:6144\n\t"
            "ds_read_b128 v[136:139], v4 offset:8192\n\tds_read_b128 v[140:143], v4 offset:10240\n\tds_read_b128 v[144:147], v4 offset:12288\n\tds_read_b64_tr_b16 v[180:181], v13 offset:1024\n\tds_read_b64_tr_b16 v[182:183], v13 offset:1536\n\tds_read_b64_tr_b16 v[184:185], v13 offset:2048\n\t"
            "ds_read_b64_tr_b16 v[186:187], v13 offset:2560\n\ts_waitcnt lgkmcnt(10)\n\tv_mfma_f32_16x16x32_bf16 v[36:39], v[176:179], v[120:123], 0\n\ts_waitcnt lgkmcnt(9)\n\tv_mfma_f32_16x16x32_bf16 v[48:51], v[176:179], v[124:127], 0\n\ts_waitcnt lgkmcnt(8)\n\t"
            "v_mfma_f32_16x16x32_bf16 v[60:63], v[176:179], v[128:131], 0\n\tds_read_b64_tr_b16 v[188:189], v13 offset:12288\n\tds_read_b64_tr_b16 v[190:191], v13 offset:12800\n\ts_waitcnt lgkmcnt(9)\n\tv_mfma_f32_16x16x32_bf16 v[72:75], v[176:179], v[132:135], 0\n\tds_read_b128 v[148:151], v9\n\t"
            "s_waitcnt lgkmcnt(9)\n\tv_mfma_f32_16x16x32_bf16 v[84:87], v[176:179], v[136:139], 0\n\tds_read_b128 v[152:155], v5 offset:2048\n\ts_waitcnt lgkmcnt(9)\n\tv_mfma_f32_16x16x32_bf16 v[96:99], v[176:179], v[140:143], 0\n\tds_read_b128 v[156:159], v5 offset:4096\n\t"
            "s_waitcnt lgkmcnt(9)\n\tv_mfma_f32_16x16x32_bf16 v[108:111], v[176:179], v[144:147], 0\n\tds_read_b128 v[160:163], v5 offset:6144\n\ts_waitcnt lgkmcnt(8)\n\tv_mfma_f32_16x16x32_bf16 v[40:43], v[180:183], v[120:123], 0\n\tds_read_b128 v[164:167], v5 offset:8192\n\t"
            "v_mfma_f32_16x16x32_bf16 v[52:55], v[180:183], v[124:127], 0\n\tds_read_b128 v[168:171], v5 offset:10240\n\tv_mfma_f32_16x16x32_bf16 v[64:67], v[180:183], v[128:131], 0\n\tds_read_b128 v[172:175], v5 offset:12288\n\tv_mfma_f32_16x16x32_bf16 v[76:79], v[180:183], v[132:135], 0\n\tds_read_b64_tr_b16 v[192:193], v13 offset:13312\n\t"
            "ds_read_b64_tr_b16 v[194:195], v13 offset:13824\n\tv_mfma_f32_16x16x32_bf16 v[88:91], v[180:183], v[136:139], 0\n\tds_read_b64_tr_b16 v[196:197], v13 offset:14336\n\tds_read_b64_tr_b16 v[198:199], v13 offset:14848\n\tv_mfma_f32_16x16x32_bf16 v[100:103], v[180:183], v[140:143], 0\n\tv_mfma_f32_16x16x32_bf16 v[112:115], v[180:183], v[144:147], 0\n\t"
            "s_waitcnt lgkmcnt(13)\n\tv_mfma_f32_16x16x32_bf16 v[44:47], v[120:123], v[184:187], 0\n\tv_mfma_f32_16x16x32_bf16 v[56:59], v[124:127], v[184:187], 0\n\tv_mfma_f32_16x16x32_bf16 v[68:71], v[128:131], v[184:187], 0\n\tv_mfma_f32_16x16x32_bf16 v[80:83], v[132:135], v[184:187], 0\n\tv_mfma_f32_16x16x32_bf16 v[92:95], v[136:139], v[184:187], 0\n\t"
            "v_mfma_f32_16x16x32_bf16 v[104:107], v[140:143], v[184:187], 0\n\tv_mfma_f32_16x16x32_bf16 v[116:119], v[144:147], v[184:187], 0\n\ts_waitcnt lgkmcnt(10)\n\tv_mfma_f32_16x16x32_bf16 v[36:39], v[188:191], v[148:151], v[36:39]\n\ts_waitcnt lgkmcnt(9)\n\tv_mfma_f32_16x16x32_bf16 v[48:51], v[188:191], v[152:155], v[48:51]\n\t"
            "s_waitcnt lgkmcnt(8)\n\tv_mfma_f32_16x16x32_bf16 v[60:63], v[188:191], v[156:159], v[60:63]\n\ts_waitcnt lgkmcnt(7)\n\tv_mfma_f32_16x16x32_bf16 v[72:75], v[188:191], v[160:163], v[72:75]\n\ts_waitcnt lgkmcnt(6)\n\tv_mfma_f32_16x16x32_bf16 v[84:87], v[188:191], v[164:167], v[84:87]\n\t"
            "s_waitcnt lgkmcnt(5)\n\tv_mfma_f32_16x16x32_bf16 v[96:99], v[188:191], v[168:171], v[96:99]\n\ts_waitcnt lgkmcnt(4)\n\tv_mfma_f32_16x16x32_bf16 v[108:111], v[188:191], v[172:175], v[108:111]\n\ts_waitcnt lgkmcnt(2)\n\tv_mfma_f32_16x16x32_bf16 v[40:43], v[192:195], v[148:151], v[40:43]\n\t"
            "v_mfma_f32_16x16x32_bf16 v[52:55], v[192:195], v[152:155], v[52:55]\n\tv_mfma_f32_16x16x32_bf16 v[64:67], v[192:195], v[156:159], v[64:67]\n\tv_mfma_f32_16x16x32_bf16 v[76:79], v[192:195], v[160:163], v[76:79]\n\ts_waitcnt vmcnt(0) lgkmcnt(0)\n\ts_barrier\n\t"
            ST(1)
            "ds_read_b64_tr_b16 v[176:177], v13 offset:38912\n\tds_read_b64_tr_b16 v[178:179], v13 offset:39424\n\tv_mfma_f32_16x16x32_bf16 v[88:91], v[192:195], v[164:167], v[88:91]\n\tds_read_b128 v[120:123], v10\n\tv_mfma_f32_16x16x32_bf16 v[100:103], v[192:195], v[168:171], v[100:103]\n\tds_read_b128 v[124:127], v4 offset:40960\n\t"
            "v_mfma_f32_16x16x32_bf16 v[112:115], v[192:195], v[172:175], v[112:115]\n\tds_read_b128 v[128:131], v4 offset:43008\n\tv_mfma_f32_16x16x32_bf16 v[44:47], v[148:151], v[196:199], v[44:47]\n\tds_read_b128 v[132:135], v4 offset:45056\n\tv_mfma_f32_16x16x32_bf16 v[56:59], v[152:155], v[196:199], v[56:59]\n\tds_read_b128 v[136:139], v4 offset:47104\n\t"
            "v_mfma_f32_16x16x32_bf16 v[68:71], v[156:159], v[196:199], v[68:71]\n\tds_read_b128 v[140:143], v4 offset:49152\n\tv_mfma_f32_16x16x32_bf16 v[80:83], v[160:163], v[196:199], v[80:83]\n\tds_read_b128 v[144:147], v4 offset:51200\n\tv_mfma_f32_16x16x32_bf16 v[92:95], v[164:167], v[196:199], v[92:95]\n\tds_read_b64_tr_b16 v[180:181], v13 offset:39936\n\t"
            "ds_read_b64_tr_b16 v[182:183], v13 offset:40448\n\tv_mfma_f32_16x16x32_bf16 v[104:107], v[168:171], v[196:199], v[104:107]\n\tds_read_b64_tr_b16 v[184:185], v13 offset:40960\n\tds_read_b64_tr_b16 v[186:187], v13 offset:41472\n\tv_mfma_f32_16x16x32_bf16 v[116:119], v[172:175], v[196:199], v[116:119]\n\ts_waitcnt lgkmcnt(10)\n\t"
            "v_mfma_f32_16x16x32_bf16 v[36:39], v[176:179], v[120:123], v[36:39]\n\ts_waitcnt lgkmcnt(9)\n\tv_mfma_f32_16x16x32_bf16 v[48:51], v[176:179], v[124:127], v[48:51]\n\ts_waitcnt lgkmcnt(8)\n\tv_mfma_f32_16x16x32_bf16 v[60:63], v[176:179], v[128:131], v[60:63]\n\tds_read_b64_tr_b16 v[188:189], v13 offset:51200\n\t"
            "ds_read_b64_tr_b16 v[190:191], v13 offset:51712\n\ts_waitcnt lgkmcnt(9)\n\tv_mfma_f32_16x16x32_bf16 v[72:75], v[176:179], v[132:135], v[72:75]\n\tds_read_b128 v[148:151], v11\n\ts_waitcnt lgkmcnt(9)\n\tv_mfma_f32_16x16x32_bf16 v[84:87], v[176:179], v[136:139], v[84:87]\n\t"
            "ds_read_b128 v[152:155], v5 offset:40960\n\ts_waitcnt lgkmcnt(9)\n\tv_mfma_f32_16x16x32_bf16 v[96:99], v[176:179], v[140:143], v[96:99]\n\tds_read_b128 v[156:159], v5 offset:43008\n\ts_waitcnt lgkmcnt(9)\n\tv_mfma_f32_16x16x32_bf16 v[108:111], v[176:179], v[144:147], v[108:111]\n\t"
            "ds_read_b128 v[160:163], v5 offset:45056\n\ts_waitcnt lgkmcnt(8)\n\tv_mfma_f32_16x16x32_bf16 v[40:43], v[180:183], v[120:123], v[40:43]\n\tds_read_b128 v[164:167], v5 offset:47104\n\tv_mfma_f32_16x16x32_bf16 v[52:55], v[180:183], v[124:127], v[52:55]\n\tds_read_b128 v[168:171], v5 offset:49152\n\t"
            "v_mfma_f32_16x16x32_bf16 v[64:67], v[180:183], v[128:131], v[64:67]\n\tds_read_b128 v[172:175], v5 offset:51200\n\tv_mfma_f32_16x16x32_bf16 v[76:79], v[180:183], v[132:135], v[76:79]\n\tds_read_b64_tr_b16 v[192:193], v13 offset:52224\n\tds_read_b64_tr_b16 v[194:195], v13 offset:52736\n\tv_mfma_f32_16x16x32_bf16 v[88:91], v[180:183], v[136:139], v[88:91]\n\t"
            "ds_read_b64_tr_b16 v[196:197], v13 offset:53248\n\tds_read_b64_tr_b16 v[198:199], v13 offset:53760\n\tv_mfma_f32_16x16x32_bf16 v[100:103], v[180:183], v[140:143], v[100:103]\n\tv_mfma_f32_16x16x32_bf16 v[112:115], v[180:183], v[144:147], v[112:115]\n\ts_waitcnt lgkmcnt(13)\n\tv_mfma_f32_16x16x32_bf16 v[44:47], v[120:123], v[184:187], v[44:47]\n\t"
            "v_mfma_f32_16x16x32_bf16 v[56:59], v[124:127], v[184:187], v[56:59]\n\tv_mfma_f32_16x16x32_bf16 v[68:71], v[128:131], v[184:187], v[68:71]\n\tv_mfma_f32_16x16x32_bf16 v[80:83], v[132:135], v[184:187], v[80:83]\n\tv_mfma_f32_16x16x32_bf16 v[92:95], v[136:139], v[184:187], v[92:95]\n\tv_mfma_f32_16x16x32_bf16 v[104:107], v[140:143], v[184:187], v[104:107]\n\tv_mfma_f32_16x16x32_bf16 v[116:119], v[144:147], v[184:187], v[116:119]\n\t"
            "s_waitcnt lgkmcnt(10)\n\tv_mfma_f32_16x16x32_bf16 v[36:39], v[188:191], v[148:151], v[36:39]\n\ts_waitcnt lgkmcnt(9)\n\tv_mfma_f32_16x16x32_bf16 v[48:51], v[188:191], v[152:155], v[48:51]\n\ts_waitcnt lgkmcnt(8)\n\tv_mfma_f32_16x16x32_bf16 v[60:63], v[188:191], v[156:159], v[60:63]\n\t"
            "s_waitcnt lgkmcnt(7)\n\tv_mfma_f32_16x16x32_bf16 v[72:75], v[188:191], v[160:163], v[72:75]\n\ts_waitcnt lgkmcnt(6)\n\tv_mfma_f32_16x16x32_bf16 v[84:87], v[188:191], v[164:167], v[84:87]\n\ts_waitcnt lgkmcnt(5)\n\tv_mfma_f32_16x16x32_bf16 v[96:99], v[188:191], v[168:171], v[96:99]\n\t"
            "s_waitcnt lgkmcnt(4)\n\tv_mfma_f32_16x16x32_bf16 v[108:111], v[188:191], v[172:175], v[108:111]\n\ts_waitcnt lgkmcnt(2)\n\tv_mfma_f32_16x16x32_bf16 v[40:43], v[192:195], v[148:151], v[40:43]\n\tv_mfma_f32_16x16x32_bf16 v[52:55], v[192:195], v[152:155], v[52:55]\n\tv_mfma_f32_16x16x32_bf16 v[64:67], v[192:195], v[156:159], v[64:67]\n\t"
            "v_mfma_f32_16x16x32_bf16 v[76:79], v[192:195], v[160:163], v[76:79]\n\ts_waitcnt lgkmcnt(0)\n\ts_barrier\n\t"
            ST(3)
            "ds_read_b64_tr_b16 v[176:177], v12 offset:0\n\tds_read_b64_tr_b16 v[178:179], v12 offset:512\n\tv_mfma_f32_16x16x32_bf16 v[88:91], v[192:195], v[164:167], v[88:91]\n\tds_read_b128 v[120:123], v6\n\tv_mfma_f32_16x16x32_bf16 v[100:103], v[192:195], v[168:171], v[100:103]\n\tds_read_b128 v[124:127], v2 offset:2048\n\t"
            "v_mfma_f32_16x16x32_bf16 v[112:115], v[192:195], v[172:175], v[112:115]\n\tds_read_b128 v[128:131], v2 offset:4096\n\tv_mfma_f32_16x16x32_bf16 v[44:47], v[148:151], v[196:199], v[44:47]\n\tds_read_b128 v[132:135], v2 offset:6144\n\tv_mfma_f32_16x16x32_bf16 v[56:59], v[152:155], v[196:199], v[56:59]\n\tds_read_b128 v[136:139], v2 offset:8192\n\t"
            "v_mfma_f32_16x16x32_bf16 v[68:71], v[156:159], v[196:199], v[68:71]\n\tds_read_b128 v[140:143], v2 offset:10240\n\tv_mfma_f32_16x16x32_bf16 v[80:83], v[160:163], v[196:199], v[80:83]\n\tds_read_b128 v[144:147], v2 offset:12288\n\tv_mfma_f32_16x16x32_bf16 v[92:95], v[164:167], v[196:199], v[92:95]\n\tds_read_b64_tr_b16 v[180:181], v12 offset:1024\n\t"
            "ds_read_b64_tr_b16 v[182:183], v12 offset:1536\n\tv_mfma_f32_16x16x32_bf16 v[104:107], v[168:171], v[196:199], v[104:107]\n\tds_read_b64_tr_b16 v[184:185], v12 offset:2048\n\tds_read_b64_tr_b16 v[186:187], v12 offset:2560\n\tv_mfma_f32_16x16x32_bf16 v[116:119], v[172:175], v[196:199], v[116:119]\n\ts_waitcnt lgkmcnt(10)\n\t"
            "v_mfma_f32_16x16x32_bf16 v[36:39], v[176:179], v[120:123], v[36:39]\n\ts_waitcnt lgkmcnt(9)\n\tv_mfma_f32_16x16x32_bf16 v[48:51], v[176:179], v[124:127], v[48:51]\n\ts_waitcnt lgkmcnt(8)\n\tv_mfma_f32_16x16x32_bf16 v[60:63], v[176:179], v[128:131], v[60:63]\n\tds_read_b64_tr_b16 v[188:189], v12 offset:12288\n\t"
            "ds_read_b64_tr_b16 v[190:191], v12 offset:12800\n\ts_waitcnt lgkmcnt(9)\n\tv_mfma_f32_16x16x32_bf16 v[72:75], v[176:179], v[132:135], v[72:75]\n\tds_read_b128 v[148:151], v7\n\ts_waitcnt lgkmcnt(9)\n\tv_mfma_f32_16x16x32_bf16 v[84:87], v[176:179], v[136:139], v[84:87]\n\t"
            "ds_read_b128 v[152:155], v3 offset:2048\n\ts_waitcnt lgkmcnt(9)\n\tv_mfma_f32_16x16x32_bf16 v[96:99], v[176:179], v[140:143], v[96:99]\n\tds_read_b128 v[156:159], v3 offset:4096\n\ts_waitcnt lgkmcnt(9)\n\tv_mfma_f32_16x16x32_bf16 v[108:111], v[176:179], v[144:147], v[108:111]\n\t"
            "ds_read_b128 v[160:163], v3 offset:6144\n\ts_waitcnt lgkmcnt(8)\n\tv_mfma_f32_16x16x32_bf16 v[40:43], v[180:183], v[120:123], v[40:43]\n\tds_read_b128 v[164:167], v3 offset:8192\n\tv_mfma_f32_16x16x32_bf16 v[52:55], v[180:183], v[124:127], v[52:55]\n\tds_read_b128 v[168:171], v3 offset:10240\n\t"
            "v_mfma_f32_16x16x32_bf16 v[64:67], v[180:183], v[128:131], v[64:67]\n\tds_read_b128 v[172:175], v3 offset:12288\n\tv_mfma_f32_16x16x32_bf16 v[76:79], v[180:183], v[132:135], v[76:79]\n\tds_read_b64_tr_b16 v[192:193], v12 offset:13312\n\tds_read_b64_tr_b16 v[194:195], v12 offset:13824\n\tv_mfma_f32_16x16x32_bf16 v[88:91], v[180:183], v[136:139], v[88:91]\n\t"
            "ds_read_b64_tr_b16 v[196:197], v12 offset:14336\n\tds_read_b64_tr_b16 v[198:199], v12 offset:14848\n\tv_mfma_f32_16x16x32_bf16 v[100:103], v[180:183], v[140:143], v[100:103]\n\tv_mfma_f32_16x16x32_bf16 v[112:115], v[180:183], v[144:147], v[112:115]\n\ts_waitcnt lgkmcnt(13)\n\tv_mfma_f32_16x16x32_bf16 v[44:47], v[120:123], v[184:187], v[44:47]\n\t"
            "v_mfma_f32_16x16x32_bf16 v[56:59], v[124:127], v[184:187], v[56:59]\n\tv_mfma_f32_16x16x32_bf16 v[68:71], v[128:131], v[184:187], v[68:71]\n\tv_mfma_f32_16x16x32_bf16 v[80:83], v[132:135], v[184:187], v[80:83]\n\tv_mfma_f32_16x16x32_bf16 v[92:95], v[136:139], v[184:187], v[92:95]\n\tv_mfma_f32_16x16x32_bf16 v[104:107], v[140:143], v[184:187], v[104:107]\n\tv_mfma_f32_16x16x32_bf16 v[116:119], v[144:147], v[184:187], v[116:119]\n\t"
            "s_waitcnt lgkmcnt(10)\n\tv_mfma_f32_16x16x32_bf16 v[36:39], v[188:191], v[148:151], v[36:39]\n\ts_waitcnt lgkmcnt(9)\n\tv_mfma_f32_16x16x32_bf16 v[48:51], v[188:191], v[152:155], v[48:51]\n\ts_waitcnt lgkmcnt(8)\n\tv_mfma_f32_16x16x32_bf16 v[60:63], v[188:191], v[156:159], v[60:63]\n\t"
            "s_waitcnt lgkmcnt(7)\n\tv_mfma_f32_16x16x32_bf16 v[72:75], v[188:191], v[160:163], v[72:75]\n\ts_waitcnt lgkmcnt(6)\n\tv_mfma_f32_16x16x32_bf16 v[84:87], v[188:191], v[164:167], v[84:87]\n\ts_waitcnt lgkmcnt(5)\n\tv_mfma_f32_16x16x32_bf16 v[96:99], v[188:191], v[168:171], v[96:99]\n\t"
            "s_waitcnt lgkmcnt(4)\n\tv_mfma_f32_16x16x32_bf16 v[108:111], v[188:191], v[172:175], v[108:111]\n\ts_waitcnt lgkmcnt(2)\n\tv_mfma_f32_16x16x32_bf16 v[40:43], v[192:195], v[148:151], v[40:43]\n\tv_mfma_f32_16x16x32_bf16 v[52:55], v[192:195], v[152:155], v[52:55]\n\tv_mfma_f32_16x16x32_bf16 v[64:67], v[192:195], v[156:159], v[64:67]\n\t"
            "v_mfma_f32_16x16x32_bf16 v[76:79], v[192:195], v[160:163], v[76:79]\n\ts_waitcnt lgkmcnt(0)\n\ts_barrier\n\t"
            ST(5)
            "ds_read_b64_tr_b16 v[176:177], v13 offset:0\n\tds_read_b64_tr_b16 v[178:179], v13 offset:512\n\tv_mfma_f32_16x16x32_bf16 v[88:91], v[192:195], v[164:167], v[88:91]\n\tds_read_b128 v[120:123], v8\n\tv_mfma_f32_16x16x32_bf16 v[100:103], v[192:195], v[168:171], v[100:103]\n\tds_read_b128 v[124:127], v4 offset:2048\n\t"
            "v_mfma_f32_16x16x32_bf16 v[112:115], v[192:195], v[172:175], v[112:115]\n\tds_read_b128 v[128:131], v4 offset:4096\n\tv_mfma_f32_16x16x32_bf16 v[44:47], v[148:151], v[196:199], v[44:47]\n\tds_read_b128 v[132:135], v4 offset:6144\n\tv_mfma_f32_16x16x32_bf16 v[56:59], v[152:155], v[196:199], v[56:59]\n\tds_read_b128 v[136:139], v4 offset:8192\n\t"
            "v_mfma_f32_16x16x32_bf16 v[68:71], v[156:159], v[196:199], v[68:71]\n\tds_read_b128 v[140:143], v4 offset:10240\n\tv_mfma_f32_16x16x32_bf16 v[80:83], v[160:163], v[196:199], v[80:83]\n\tds_read_b128 v[144:147], v4 offset:12288\n\tv_mfma_f32_16x16x32_bf16 v[92:95], v[164:167], v[196:199], v[92:95]\n\tds_read_b64_tr_b16 v[180:181], v13 offset:1024\n\t"
            "ds_read_b64_tr_b16 v[182:183], v13 offset:1536\n\tv_mfma_f32_16x16x32_bf16 v[104:107], v[168:171], v[196:199], v[104:107]\n\tds_read_b64_tr_b16 v[184:185], v13 offset:2048\n\tds_read_b64_tr_b16 v[186:187], v13 offset:2560\n\tv_mfma_f32_16x16x32_bf16 v[116:119], v[172:175], v[196:199], v[116:119]\n\ts_waitcnt lgkmcnt(10)\n\t"
            "v_mfma_f32_16x16x32_bf16 v[36:39], v[176:179], v[120:123], v[36:39]\n\ts_waitcnt lgkmcnt(9)\n\tv_mfma_f32_16x16x32_bf16 v[48:51], v[176:179], v[124:127], v[48:51]\n\ts_waitcnt lgkmcnt(8)\n\tv_mfma_f32_16x16x32_bf16 v[60:63], v[176:179], v[128:131], v[60:63]\n\tds_read_b64_tr_b16 v[188:189], v13 offset:12288\n\t"
            "ds_read_b64_tr_b16 v[190:191], v13 offset:12800\n\ts_waitcnt lgkmcnt(9)\n\tv_mfma_f32_16x16x32_bf16 v[72:75], v[176:179], v[132:135], v[72:75]\n\tds_read_b128 v[148:151], v9\n\ts_waitcnt lgkmcnt(9)\n\tv_mfma_f32_16x16x32_bf16 v[84:87], v[176:179], v[136:139], v[84:87]\n\t"
            "ds_read_b128 v[152:155], v5 offset:2048\n\ts_waitcnt lgkmcnt(9)\n\tv_mfma_f32_16x16x32_bf16 v[96:99], v[176:179], v[140:143], v[96:99]\n\tds_read_b128 v[156:159], v5 offset:4096\n\ts_waitcnt lgkmcnt(9)\n\tv_mfma_f32_16x16x32_bf16 v[108:111], v[176:179], v[144:147], v[108:111]\n\t"
            "ds_read_b128 v[160:163], v5 offset:6144\n\ts_waitcnt lgkmcnt(8)\n\tv_mfma_f32_16x16x32_bf16 v[40:43], v[180:183], v[120:123], v[40:43]\n\tds_read_b128 v[164:167], v5 offset:8192\n\tv_mfma_f32_16x16x32_bf16 v[52:55], v[180:183], v[124:127], v[52:55]\n\tds_read_b128 v[168:171], v5 offset:10240\n\t"
            "v_mfma_f32_16x16x32_bf16 v[64:67], v[180:183], v[128:131], v[64:67]\n\tds_read_b128 v[172:175], v5 offset:12288\n\tv_mfma_f32_16x16x32_bf16 v[76:79], v[180:183], v[132:135], v[76:79]\n\tds_read_b64_tr_b16 v[192:193], v13 offset:13312\n\tds_read_b64_tr_b16 v[194:195], v13 offset:13824\n\tv_mfma_f32_16x16x32_bf16 v[88:91], v[180:183], v[136:139], v[88:91]\n\t"
            "ds_read_b64_tr_b16 v[196:197], v13 offset:14336\n\tds_read_b64_tr_b16 v[198:199], v13 offset:14848\n\tv_mfma_f32_16x16x32_bf16 v[100:103], v[180:183], v[140:143], v[100:103]\n\tv_mfma_f32_16x16x32_bf16 v[112:115], v[180:183], v[144:147], v[112:115]\n\ts_waitcnt lgkmcnt(13)\n\tv_mfma_f32_16x16x32_bf16 v[44:47], v[120:123], v[184:187], v[44:47]\n\t"
            "v_mfma_f32_16x16x32_bf16 v[56:59], v[124:127], v[184:187], v[56:59]\n\tv_mfma_f32_16x16x32_bf16 v[68:71], v[128:131], v[184:187], v[68:71]\n\tv_mfma_f32_16x16x32_bf16 v[80:83], v[132:135], v[184:187], v[80:83]\n\tv_mfma_f32_16x16x32_bf16 v[92:95], v[136:139], v[184:187], v[92:95]\n\tv_mfma_f32_16x16x32_bf16 v[104:107], v[140:143], v[184:187], v[104:107]\n\tv_mfma_f32_16x16x32_bf16 v[116:119], v[144:147], v[184:187], v[116:119]\n\t"
            "s_waitcnt lgkmcnt(10)\n\tv_mfma_f32_16x16x32_bf16 v[36:39], v[188:191], v[148:151], v[36:39]\n\ts_waitcnt lgkmcnt(9)\n\tv_mfma_f32_16x16x32_bf16 v[48:51], v[188:191], v[152:155], v[48:51]\n\ts_waitcnt lgkmcnt(8)\n\tv_mfma_f32_16x16x32_bf16 v[60:63], v[188:191], v[156:159], v[60:63]\n\t"
            "s_waitcnt lgkmcnt(7)\n\tv_mfma_f32_16x16x32_bf16 v[72:75], v[188:191], v[160:163], v[72:75]\n\ts_waitcnt lgkmcnt(6)\n\tv_mfma_f32_16x16x32_bf16 v[84:87], v[188:191], v[164:167], v[84:87]\n\ts_waitcnt lgkmcnt(5)\n\tv_mfma_f32_16x16x32_bf16 v[96:99], v[188:191], v[168:171], v[96:99]\n\t"
            "s_waitcnt lgkmcnt(4)\n\tv_mfma_f32_16x16x32_bf16 v[108:111], v[188:191], v[172:175], v[108:111]\n\ts_waitcnt lgkmcnt(2)\n\tv_mfma_f32_16x16x32_bf16 v[40:43], v[192:195], v[148:151], v[40:43]\n\tv_mfma_f32_16x16x32_bf16 v[52:55], v[192:195], v[152:155], v[52:55]\n\tv_mfma_f32_16x16x32_bf16 v[64:67], v[192:195], v[156:159], v[64:67]\n\t"
            "v_mfma_f32_16x16x32_bf16 v[76:79], v[192:195], v[160:163], v[76:79]\n\ts_waitcnt lgkmcnt(0)\n\ts_barrier\n\t"
            ST(7)
            "ds_read_b64_tr_b16 v[176:177], v13 offset:38912\n\tds_read_b64_tr_b16 v[178:179], v13 offset:39424\n\tv_mfma_f32_16x16x32_bf16 v[88:91], v[192:195], v[164:167], v[88:91]\n\tds_read_b128 v[120:123], v10\n\tv_mfma_f32_16x16x32_bf16 v[100:103], v[192:195], v[168:171], v[100:103]\n\tds_read_b128 v[124:127], v4 offset:40960\n\t"
            "v_mfma_f32_16x16x32_bf16 v[112:115], v[192:195], v[172:175], v[112:115]\n\tds_read_b128 v[128:131], v4 offset:43008\n\tv_mfma_f32_16x16x32_bf16 v[44:47], v[148:151], v[196:199], v[44:47]\n\tds_read_b128 v[132:135], v4 offset:45056\n\tv_mfma_f32_16x16x32_bf16 v[56:59], v[152:155], v[196:199], v[56:59]\n\tds_read_b128 v[136:139], v4 offset:47104\n\t"
            "v_mfma_f32_16x16x32_bf16 v[68:71], v[156:159], v[196:199], v[68:71]\n\tds_read_b128 v[140:143], v4 offset:49152\n\tv_mfma_f32_16x16x32_bf16 v[80:83], v[160:163], v[196:199], v[80:83]\n\tds_read_b128 v[144:147], v4 offset:51200\n\tv_mfma_f32_16x16x32_bf16 v[92:95], v[164:167], v[196:199], v[92:95]\n\tds_read_b64_tr_b16 v[180:181], v13 offset:39936\n\t"
            "ds_read_b64_tr_b16 v[182:183], v13 offset:40448\n\tv_mfma_f32_16x16x32_bf16 v[104:107], v[168:171], v[196:199], v[104:107]\n\tds_read_b64_tr_b16 v[184:185], v13 offset:40960\n\tds_read_b64_tr_b16 v[186:187], v13 offset:41472\n\tv_mfma_f32_16x16x32_bf16 v[116:119], v[172:175], v[196:199], v[116:119]\n\ts_waitcnt lgkmcnt(10)\n\t"
            "v_mfma_f32_16x16x32_bf16 v[36:39], v[176:179], v[120:123], v[36:39]\n\ts_waitcnt lgkmcnt(9)\n\tv_mfma_f32_16x16x32_bf16 v[48:51], v[176:179], v[124:127], v[48:51]\n\ts_waitcnt lgkmcnt(8)\n\tv_mfma_f32_16x16x32_bf16 v[60:63], v[176:179], v[128:131], v[60:63]\n\tds_read_b64_tr_b16 v[188:189], v13 offset:51200\n\t"
            "ds_read_b64_tr_b16 v[190:191], v13 offset:51712\n\ts_waitcnt lgkmcnt(9)\n\tv_mfma_f32_16x16x32_bf16 v[72:75], v[176:179], v[132:135], v[72:75]\n\tds_read_b128 v[148:151], v11\n\ts_waitcnt lgkmcnt(9)\n\tv_mfma_f32_16x16x32_bf16 v[84:87], v[176:179], v[136:139], v[84:87]\n\t"
            "ds_read_b128 v[152:155], v5 offset:40960\n\ts_waitcnt lgkmcnt(9)\n\tv_mfma_f32_16x16x32_bf16 v[96:99], v[176:179], v[140:143], v[96:99]\n\tds_read_b128 v[156:159], v5 offset:43008\n\ts_waitcnt lgkmcnt(9)\n\tv_mfma_f32_16x16x32_bf16 v[108:111], v[176:179], v[144:147], v[108:111]\n\t"
            "ds_read_b128 v[160:163], v5 offset:45056\n\ts_waitcnt lgkmcnt(8)\n\tv_mfma_f32_16x16x32_bf16 v[40:43], v[180:183], v[120:123], v[40:43]\n\tds_read_b128 v[164:167], v5 offset:47104\n\tv_mfma_f32_16x16x32_bf16 v[52:55], v[180:183], v[124:127], v[52:55]\n\tds_read_b128 v[168:171], v5 offset:49152\n\t"
            "v_mfma_f32_16x16x32_bf16 v[64:67], v[180:183], v[128:131], v[64:67]\n\tds_read_b128 v[172:175], v5 offset:51200\n\tv_mfma_f32_16x16x32_bf16 v[76:79], v[180:183], v[132:135], v[76:79]\n\tds_read_b64_tr_b16 v[192:193], v13 offset:52224\n\tds_read_b64_tr_b16 v[194:195], v13 offset:52736\n\tv_mfma_f32_16x16x32_bf16 v[88:91], v[180:183], v[136:139], v[88:91]\n\t"
            "ds_read_b64_tr_b16 v[196:197], v13 offset:53248\n\tds_read_b64_tr_b16 v[198:199], v13 offset:53760\n\tv_mfma_f32_16x16x32_bf16 v[100:103], v[180:183], v[140:143], v[100:103]\n\tv_mfma_f32_16x16x32_bf16 v[112:115], v[180:183], v[144:147], v[112:115]\n\ts_waitcnt lgkmcnt(13)\n\tv_mfma_f32_16x16x32_bf16 v[44:47], v[120:123], v[184:187], v[44:47]\n\t"
            "v_mfma_f32_16x16x32_bf16 v[56:59], v[124:127], v[184:187], v[56:59]\n\tv_mfma_f32_16x16x32_bf16 v[68:71], v[128:131], v[184:187], v[68:71]\n\tv_mfma_f32_16x16x32_bf16 v[80:83], v[132:135], v[184:187], v[80:83]\n\tv_mfma_f32_16x16x32_bf16 v[92:95], v[136:139], v[184:187], v[92:95]\n\tv_mfma_f32_16x16x32_bf16 v[104:107], v[140:143], v[184:187], v[104:107]\n\tv_mfma_f32_16x16x32_bf16 v[116:119], v[144:147], v[184:187], v[116:119]\n\t"
            "s_waitcnt lgkmcnt(10)\n\tv_mfma_f32_16x16x32_bf16 v[36:39], v[188:191], v[148:151], v[36:39]\n\ts_waitcnt lgkmcnt(9)\n\tv_mfma_f32_16x16x32_bf16 v[48:51], v[188:191], v[152:155], v[48:51]\n\ts_waitcnt lgkmcnt(8)\n\tv_mfma_f32_16x16x32_bf16 v[60:63], v[188:191], v[156:159], v[60:63]\n\t"
            "s_waitcnt lgkmcnt(7)\n\tv_mfma_f32_16x16x32_bf16 v[72:75], v[188:191], v[160:163], v[72:75]\n\ts_waitcnt lgkmcnt(6)\n\tv_mfma_f32_16x16x32_bf16 v[84:87], v[188:191], v[164:167], v[84:87]\n\ts_waitcnt lgkmcnt(5)\n\tv_mfma_f32_16x16x32_bf16 v[96:99], v[188:191], v[168:171], v[96:99]\n\t"
            "s_waitcnt lgkmcnt(4)\n\tv_mfma_f32_16x16x32_bf16 v[108:111], v[188:191], v[172:175], v[108:111]\n\ts_waitcnt lgkmcnt(2)\n\tv_mfma_f32_16x16x32_bf16 v[40:43], v[192:195], v[148:151], v[40:43]\n\tv_mfma_f32_16x16x32_bf16 v[52:55], v[192:195], v[152:155], v[52:55]\n\tv_mfma_f32_16x16x32_bf16 v[64:67], v[192:195], v[156:159], v[64:67]\n\t"
            "v_mfma_f32_16x16x32_bf16 v[76:79], v[192:195], v[160:163], v[76:79]\n\ts_waitcnt lgkmcnt(0)\n\ts_barrier\n\t"
            ST(9)
            "ds_read_b64_tr_b16 v[176:177], v12 offset:0\n\tds_read_b64_tr_b16 v[178:179], v12 offset:512\n\tv_mfma_f32_16x16x32_bf16 v[88:91], v[192:195], v[164:167], v[88:91]\n\tds_read_b128 v[120:123], v6\n\tv_mfma_f32_16x16x32_bf16 v[100:103], v[192:195], v[168:171], v[100:103]\n\tds_read_b128 v[124:127], v2 offset:2048\n\t"
            "v_mfma_f32_16x16x32_bf16 v[112:115], v[192:195], v[172:175], v[112:115]\n\tds_read_b128 v[128:131], v2 offset:4096\n\tv_mfma_f32_16x16x32_bf16 v[44:47], v[148:151], v[196:199], v[44:47]\n\tds_read_b128 v[132:135], v2 offset:6144\n\tv_mfma_f32_16x16x32_bf16 v[56:59], v[152:155], v[196:199], v[56:59]\n\tds_read_b128 v[136:139], v2 offset:8192\n\t"
            "v_mfma_f32_16x16x32_bf16 v[68:71], v[156:159], v[196:199], v[68:71]\n\tds_read_b128 v[140:143], v2 offset:10240\n\tv_mfma_f32_16x16x32_bf16 v[80:83], v[160:163], v[196:199], v[80:83]\n\tds_read_b128 v[144:147], v2 offset:12288\n\tv_mfma_f32_16x16x32_bf16 v[92:95], v[164:167], v[196:199], v[92:95]\n\tds_read_b64_tr_b16 v[180:181], v12 offset:1024\n\t"
            "ds_read_b64_tr_b16 v[182:183], v12 offset:1536\n\tv_mfma_f32_16x16x32_bf16 v[104:107], v[168:171], v[196:199], v[104:107]\n\tds_read_b64_tr_b16 v[184:185], v12 offset:2048\n\tds_read_b64_tr_b16 v[186:187], v12 offset:2560\n\tv_mfma_f32_16x16x32_bf16 v[116:119], v[172:175], v[196:199], v[116:119]\n\ts_waitcnt lgkmcnt(10)\n\t"
            "v_mfma_f32_16x16x32_bf16 v[36:39], v[176:179], v[120:123], v[36:39]\n\ts_waitcnt lgkmcnt(9)\n\tv_mfma_f32_16x16x32_bf16 v[48:51], v[176:179], v[124:127], v[48:51]\n\ts_waitcnt lgkmcnt(8)\n\tv_mfma_f32_16x16x32_bf16 v[60:63], v[176:179], v[128:131], v[60:63]\n\tds_read_b64_tr_b16 v[188:189], v12 offset:12288\n\t"
            "ds_read_b64_tr_b16 v[190:191], v12 offset:12800\n\ts_waitcnt lgkmcnt(9)\n\tv_mfma_f32_16x16x32_bf16 v[72:75], v[176:179], v[132:135], v[72:75]\n\tds_read_b128 v[148:151], v7\n\ts_waitcnt lgkmcnt(9)\n\tv_mfma_f32_16x16x32_bf16 v[84:87], v[176:179], v[136:139], v[84:87]\n\t"
            "ds_read_b128 v[152:155], v3 offset:2048\n\ts_waitcnt lgkmcnt(9)\n\tv_mfma_f32_16x16x32_bf16 v[96:99], v[176:179], v[140:143], v[96:99]\n\tds_read_b128 v[156:159], v3 offset:4096\n\ts_waitcnt lgkmcnt(9)\n\tv_mfma_f32_16x16x32_bf16 v[108:111], v[176:179], v[144:147], v[108:111]\n\t"
            "ds_read_b128 v[160:163], v3 offset:6144\n\ts_waitcnt lgkmcnt(8)\n\tv_mfma_f32_16x16x32_bf16 v[40:43], v[180:183], v[120:123], v[40:43]\n\tds_read_b128 v[164:167], v3 offset:8192\n\tv_mfma_f32_16x16x32_bf16 v[52:55], v[180:183], v[124:127], v[52:55]\n\tds_read_b128 v[168:171], v3 offset:10240\n\t"
            "v_mfma_f32_16x16x32_bf16 v[64:67], v[180:183], v[128:131], v[64:67]\n\tds_read_b128 v[172:175], v3 offset:12288\n\tv_mfma_f32_16x16x32_bf16 v[76:79], v[180:183], v[132:135], v[76:79]\n\tds_read_b64_tr_b16 v[192:193], v12 offset:13312\n\tds_read_b64_tr_b16 v[194:195], v12 offset:13824\n\tv_mfma_f32_16x16x32_bf16 v[88:91], v[180:183], v[136:139], v[88:91]\n\t"
            "ds_read_b64_tr_b16 v[196:197], v12 offset:14336\n\tds_read_b64_tr_b16 v[198:199], v12 offset:14848\n\tv_mfma_f32_16x16x32_bf16 v[100:103], v[180:183], v[140:143], v[100:103]\n\tv_mfma_f32_16x16x32_bf16 v[112:115], v[180:183], v[144:147], v[112:115]\n\ts_waitcnt lgkmcnt(13)\n\tv_mfma_f32_16x16x32_bf16 v[44:47], v[120:123], v[184:187], v[44:47]\n\t"
            "v_mfma_f32_16x16x32_bf16 v[56:59], v[124:127], v[184:187], v[56:59]\n\tv_mfma_f32_16x16x32_bf16 v[68:71], v[128:131], v[184:187], v[68:71]\n\tv_mfma_f32_16x16x32_bf16 v[80:83], v[132:135], v[184:187], v[80:83]\n\tv_mfma_f32_16x16x32_bf16 v[92:95], v[136:139], v[184:187], v[92:95]\n\tv_mfma_f32_16x16x32_bf16 v[104:107], v[140:143], v[184:187], v[104:107]\n\tv_mfma_f32_16x16x32_bf16 v[116:119], v[144:147], v[184:187], v[116:119]\n\t"
            "s_waitcnt lgkmcnt(10)\n\tv_mfma_f32_16x16x32_bf16 v[36:39], v[188:191], v[148:151], v[36:39]\n\ts_waitcnt lgkmcnt(9)\n\tv_mfma_f32_16x16x32_bf16 v[48:51], v[188:191], v[152:155], v[48:51]\n\ts_waitcnt lgkmcnt(8)\n\tv_mfma_f32_16x16x32_bf16 v[60:63], v[188:191], v[156:159], v[60:63]\n\t"
            "s_waitcnt lgkmcnt(7)\n\tv_mfma_f32_16x16x32_bf16 v[72:75], v[188:191], v[160:163], v[72:75]\n\ts_waitcnt lgkmcnt(6)\n\tv_mfma_f32_16x16x32_bf16 v[84:87], v[188:191], v[164:167], v[84:87]\n\ts_waitcnt lgkmcnt(5)\n\tv_mfma_f32_16x16x32_bf16 v[96:99], v[188:191], v[168:171], v[96:99]\n\t"
            "s_waitcnt lgkmcnt(4)\n\tv_mfma_f32_16x16x32_bf16 v[108:111], v[188:191], v[172:175], v[108:111]\n\ts_waitcnt lgkmcnt(2)\n\tv_mfma_f32_16x16x32_bf16 v[40:43], v[192:195], v[148:151], v[40:43]\n\tv_mfma_f32_16x16x32_bf16 v[52:55], v[192:195], v[152:155], v[52:55]\n\tv_mfma_f32_16x16x32_bf16 v[64:67], v[192:195], v[156:159], v[64:67]\n\t"
            "v_mfma_f32_16x16x32_bf16 v[76:79], v[192:195], v[160:163], v[76:79]\n\ts_waitcnt lgkmcnt(0)\n\ts_barrier\n\t"
            ST(11)
            "ds_read_b64_tr_b16 v[176:177], v13 offset:0\n\tds_read_b64_tr_b16 v[178:179], v13 offset:512\n\tv_mfma_f32_16x16x32_bf16 v[88:91], v[192:195], v[164:167], v[88:91]\n\tds_read_b128 v[120:123], v8\n\tv_mfma_f32_16x16x32_bf16 v[100:103], v[192:195], v[168:171], v[100:103]\n\tds_read_b128 v[124:127], v4 offset:2048\n\t"
            "v_mfma_f32_16x16x32_bf16 v[112:115], v[192:195], v[172:175], v[112:115]\n\tds_read_b128 v[128:131], v4 offset:4096\n\tv_mfma_f32_16x16x32_bf16 v[44:47], v[148:151], v[196:199], v[44:47]\n\tds_read_b128 v[132:135], v4 offset:6144\n\tv_mfma_f32_16x16x32_bf16 v[56:59], v[152:155], v[196:199], v[56:59]\n\tds_read_b128 v[136:139], v4 offset:8192\n\t"
            "v_mfma_f32_16x16x32_bf16 v[68:71], v[156:159], v[196:199], v[68:71]\n\tds_read_b128 v[140:143], v4 offset:10240\n\tv_mfma_f32_16x16x32_bf16 v[80:83], v[160:163], v[196:199], v[80:83]\n\tds_read_b128 v[144:147], v4 offset:12288\n\tv_mfma_f32_16x16x32_bf16 v[92:95], v[164:167], v[196:199], v[92:95]\n\tds_read_b64_tr_b16 v[180:181], v13 offset:1024\n\t"
            "ds_read_b64_tr_b16 v[182:183], v13 offset:1536\n\tv_mfma_f32_16x16x32_bf16 v[104:107], v[168:171], v[196:199], v[104:107]\n\tds_read_b64_tr_b16 v[184:185], v13 offset:2048\n\tds_read_b64_tr_b16 v[186:187], v13 offset:2560\n\tv_mfma_f32_16x16x32_bf16 v[116:119], v[172:175], v[196:199], v[116:119]\n\ts_waitcnt lgkmcnt(10)\n\t"
            "v_mfma_f32_16x16x32_bf16 v[36:39], v[176:179], v[120:123], v[36:39]\n\ts_waitcnt lgkmcnt(9)\n\tv_mfma_f32_16x16x32_bf16 v[48:51], v[176:179], v[124:127], v[48:51]\n\ts_waitcnt lgkmcnt(8)\n\tv_mfma_f32_16x16x32_bf16 v[60:63], v[176:179], v[128:131], v[60:63]\n\tds_read_b64_tr_b16 v[188:189], v13 offset:12288\n\t"
            "ds_read_b64_tr_b16 v[190:191], v13 offset:12800\n\ts_waitcnt lgkmcnt(9)\n\tv_mfma_f32_16x16x32_bf16 v[72:75], v[176:179], v[132:135], v[72:75]\n\tds_read_b128 v[148:151], v9\n\ts_waitcnt lgkmcnt(9)\n\tv_mfma_f32_16x16x32_bf16 v[84:87], v[176:179], v[136:139], v[84:87]\n\t"
            "ds_read_b128 v[152:155], v5 offset:2048\n\ts_waitcnt lgkmcnt(9)\n\tv_mfma_f32_16x16x32_bf16 v[96:99], v[176:179], v[140:143], v[96:99]\n\tds_read_b128 v[156:159], v5 offset:4096\n\ts_waitcnt lgkmcnt(9)\n\tv_mfma_f32_16x16x32_bf16 v[108:111], v[176:179], v[144:147], v[108:111]\n\t"
            "ds_read_b128 v[160:163], v5 offset:6144\n\ts_waitcnt lgkmcnt(8)\n\tv_mfma_f32_16x16x32_bf16 v[40:43], v[180:183], v[120:123], v[40:43]\n\tds_read_b128 v[164:167], v5 offset:8192\n\tv_mfma_f32_16x16x32_bf16 v[52:55], v[180:183], v[124:127], v[52:55]\n\tds_read_b128 v[168:171], v5 offset:10240\n\t"
            "v_mfma_f32_16x16x32_bf16 v[64:67], v[180:183], v[128:131], v[64:67]\n\tds_read_b128 v[172:175], v5 offset:12288\n\tv_mfma_f32_16x16x32_bf16 v[76:79], v[180:183], v[132:135], v[76:79]\n\tds_read_b64_tr_b16 v[192:193], v13 offset:13312\n\tds_read_b64_tr_b16 v[194:195], v13 offset:13824\n\tv_mfma_f32_16x16x32_bf16 v[88:91], v[180:183], v[136:139], v[88:91]\n\t"
            "ds_read_b64_tr_b16 v[196:197], v13 offset:14336\n\tds_read_b64_tr_b16 v[198:199], v13 offset:14848\n\tv_mfma_f32_16x16x32_bf16 v[100:103], v[180:183], v[140:143], v[100:103]\n\tv_mfma_f32_16x16x32_bf16 v[112:115], v[180:183], v[144:147], v[112:115]\n\ts_waitcnt lgkmcnt(13)\n\tv_mfma_f32_16x16x32_bf16 v[44:47], v[120:123], v[184:187], v[44:47]\n\t"
            "v_mfma_f32_16x16x32_bf16 v[56:59], v[124:127], v[184:187], v[56:59]\n\tv_mfma_f32_16x16x32_bf16 v[68:71], v[128:131], v[184:187], v[68:71]\n\tv_mfma_f32_16x16x32_bf16 v[80:83], v[132:135], v[184:187], v[80:83]\n\tv_mfma_f32_16x16x32_bf16 v[92:95], v[136:139], v[184:187], v[92:95]\n\tv_mfma_f32_16x16x32_bf16 v[104:107], v[140:143], v[184:187], v[104:107]\n\tv_mfma_f32_16x16x32_bf16 v[116:119], v[144:147], v[184:187], v[116:119]\n\t"
            "s_waitcnt lgkmcnt(10)\n\tv_mfma_f32_16x16x32_bf16 v[36:39], v[188:191], v[148:151], v[36:39]\n\ts_waitcnt lgkmcnt(9)\n\tv_mfma_f32_16x16x32_bf16 v[48:51], v[188:191], v[152:155], v[48:51]\n\ts_waitcnt lgkmcnt(8)\n\tv_mfma_f32_16x16x32_bf16 v[60:63], v[188:191], v[156:159], v[60:63]\n\t"
            "s_waitcnt lgkmcnt(7)\n\tv_mfma_f32_16x16x32_bf16 v[72:75], v[188:191], v[160:163], v[72:75]\n\ts_waitcnt lgkmcnt(6)\n\tv_mfma_f32_16x16x32_bf16 v[84:87], v[188:191], v[164:167], v[84:87]\n\ts_waitcnt lgkmcnt(5)\n\tv_mfma_f32_16x16x32_bf16 v[96:99], v[188:191], v[168:171], v[96:99]\n\t"
            "s_waitcnt lgkmcnt(4)\n\tv_mfma_f32_16x16x32_bf16 v[108:111], v[188:191], v[172:175], v[108:111]\n\ts_waitcnt lgkmcnt(2)\n\tv_mfma_f32_16x16x32_bf16 v[40:43], v[192:195], v[148:151], v[40:43]\n\tv_mfma_f32_16x16x32_bf16 v[52:55], v[192:195], v[152:155], v[52:55]\n\tv_mfma_f32_16x16x32_bf16 v[64:67], v[192:195], v[156:159], v[64:67]\n\t"
            "v_mfma_f32_16x16x32_bf16 v[76:79], v[192:195], v[160:163], v[76:79]\n\ts_waitcnt lgkmcnt(0)\n\ts_barrier\n\t"
            ST(13)
            "ds_read_b64_tr_b16 v[176:177], v13 offset:38912\n\tds_read_b64_tr_b16 v[178:179], v13 offset:39424\n\tv_mfma_f32_16x16x32_bf16 v[88:91], v[192:195], v[164:167], v[88:91]\n\tds_read_b128 v[120:123], v10\n\tv_mfma_f32_16x16x32_bf16 v[100:103], v[192:195], v[168:171], v[100:103]\n\tds_read_b128 v[124:127], v4 offset:40960\n\t"
            "v_mfma_f32_16x16x32_bf16 v[112:115], v[192:195], v[172:175], v[112:115]\n\tds_read_b128 v[128:131], v4 offset:43008\n\tv_mfma_f32_16x16x32_bf16 v[44:47], v[148:151], v[196:199], v[44:47]\n\tds_read_b128 v[132:135], v4 offset:45056\n\tv_mfma_f32_16x16x32_bf16 v[56:59], v[152:155], v[196:199], v[56:59]\n\tds_read_b128 v[136:139], v4 offset:47104\n\t"
            "v_mfma_f32_16x16x32_bf16 v[68:71], v[156:159], v[196:199], v[68:71]\n\tds_read_b128 v[140:143], v4 offset:49152\n\tv_mfma_f32_16x16x32_bf16 v[80:83], v[160:163], v[196:199], v[80:83]\n\tds_read_b128 v[144:147], v4 offset:51200\n\tv_mfma_f32_16x16x32_bf16 v[92:95], v[164:167], v[196:199], v[92:95]\n\tds_read_b64_tr_b16 v[180:181], v13 offset:39936\n\t"
            "ds_read_b64_tr_b16 v[182:183], v13 offset:40448\n\tv_mfma_f32_16x16x32_bf16 v[104:107], v[168:171], v[196:199], v[104:107]\n\tds_read_b64_tr_b16 v[184:185], v13 offset:40960\n\tds_read_b64_tr_b16 v[186:187], v13 offset:41472\n\tv_mfma_f32_16x16x32_bf16 v[116:119], v[172:175], v[196:199], v[116:119]\n\ts_waitcnt lgkmcnt(10)\n\t"
            "v_mfma_f32_16x16x32_bf16 v[36:39], v[176:179], v[120:123], v[36:39]\n\ts_waitcnt lgkmcnt(9)\n\tv_mfma_f32_16x16x32_bf16 v[48:51], v[176:179], v[124:127], v[48:51]\n\ts_waitcnt lgkmcnt(8)\n\tv_mfma_f32_16x16x32_bf16 v[60:63], v[176:179], v[128:131], v[60:63]\n\tds_read_b64_tr_b16 v[188:189], v13 offset:51200\n\t"
            "ds_read_b64_tr_b16 v[190:191], v13 offset:51712\n\ts_waitcnt lgkmcnt(9)\n\tv_mfma_f32_16x16x32_bf16 v[72:75], v[176:179], v[132:135], v[72:75]\n\tds_read_b128 v[148:151], v11\n\ts_waitcnt lgkmcnt(9)\n\tv_mfma_f32_16x16x32_bf16 v[84:87], v[176:179], v[136:139], v[84:87]\n\t"
            "ds_read_b128 v[152:155], v5 offset:40960\n\ts_waitcnt lgkmcnt(9)\n\tv_mfma_f32_16x16x32_bf16 v[96:99], v[176:179], v[140:143], v[96:99]\n\tds_read_b128 v[156:159], v5 offset:43008\n\ts_waitcnt lgkmcnt(9)\n\tv_mfma_f32_16x16x32_bf16 v[108:111], v[176:179], v[144:147], v[108:111]\n\t"
            "ds_read_b128 v[160:163], v5 offset:45056\n\ts_waitcnt lgkmcnt(8)\n\tv_mfma_f32_16x16x32_bf16 v[40:43], v[180:183], v[120:123], v[40:43]\n\tds_read_b128 v[164:167], v5 offset:47104\n\tv_mfma_f32_16x16x32_bf16 v[52:55], v[180:183], v[124:127], v[52:55]\n\tds_read_b128 v[168:171], v5 offset:49152\n\t"
            "v_mfma_f32_16x16x32_bf16 v[64:67], v[180:183], v[128:131], v[64:67]\n\tds_read_b128 v[172:175], v5 offset:51200\n\tv_mfma_f32_16x16x32_bf16 v[76:79], v[180:183], v[132:135], v[76:79]\n\tds_read_b64_tr_b16 v[192:193], v13 offset:52224\n\tds_read_b64_tr_b16 v[194:195], v13 offset:52736\n\tv_mfma_f32_16x16x32_bf16 v[88:91], v[180:183], v[136:139], v[88:91]\n\t"
            "ds_read_b64_tr_b16 v[196:197], v13 offset:53248\n\tds_read_b64_tr_b16 v[198:199], v13 offset:53760\n\tv_mfma_f32_16x16x32_bf16 v[100:103], v[180:183], v[140:143], v[100:103]\n\tv_mfma_f32_16x16x32_bf16 v[112:115], v[180:183], v[144:147], v[112:115]\n\ts_waitcnt lgkmcnt(13)\n\tv_mfma_f32_16x16x32_bf16 v[44:47], v[120:123], v[184:187], v[44:47]\n\t"
            "v_mfma_f32_16x16x32_bf16 v[56:59], v[124:127], v[184:187], v[56:59]\n\tv_mfma_f32_16x16x32_bf16 v[68:71], v[128:131], v[184:187], v[68:71]\n\tv_mfma_f32_16x16x32_bf16 v[80:83], v[132:135], v[184:187], v[80:83]\n\tv_mfma_f32_16x16x32_bf16 v[92:95], v[136:139], v[184:187], v[92:95]\n\tv_mfma_f32_16x16x32_bf16 v[104:107], v[140:143], v[184:187], v[104:107]\n\tv_mfma_f32_16x16x32_bf16 v[116:119], v[144:147], v[184:187], v[116:119]\n\t"
            "s_waitcnt lgkmcnt(10)\n\tv_mfma_f32_16x16x32_bf16 v[36:39], v[188:191], v[148:151], v[36:39]\n\ts_waitcnt lgkmcnt(9)\n\tv_mfma_f32_16x16x32_bf16 v[48:51], v[188:191], v[152:155], v[48:51]\n\ts_waitcnt lgkmcnt(8)\n\tv_mfma_f32_16x16x32_bf16 v[60:63], v[188:191], v[156:159], v[60:63]\n\t"
            "s_waitcnt lgkmcnt(7)\n\tv_mfma_f32_16x16x32_bf16 v[72:75], v[188:191], v[160:163], v[72:75]\n\ts_waitcnt lgkmcnt(6)\n\tv_mfma_f32_16x16x32_bf16 v[84:87], v[188:191], v[164:167], v[84:87]\n\ts_waitcnt lgkmcnt(5)\n\tv_mfma_f32_16x16x32_bf16 v[96:99], v[188:191], v[168:171], v[96:99]\n\t"
            "s_waitcnt lgkmcnt(4)\n\tv_mfma_f32_16x16x32_bf16 v[108:111], v[188:191], v[172:175], v[108:111]\n\ts_waitcnt lgkmcnt(2)\n\tv_mfma_f32_16x16x32_bf16 v[40:43], v[192:195], v[148:151], v[40:43]\n\tv_mfma_f32_16x16x32_bf16 v[52:55], v[192:195], v[152:155], v[52:55]\n\tv_mfma_f32_16x16x32_bf16 v[64:67], v[192:195], v[156:159], v[64:67]\n\t"
            "v_mfma_f32_16x16x32_bf16 v[76:79], v[192:195], v[160:163], v[76:79]\n\ts_waitcnt lgkmcnt(0)\n\ts_barrier\n\t"
            ST(15)
            "ds_read_b64_tr_b16 v[176:177], v12 offset:0\n\tds_read_b64_tr_b16 v[178:179], v12 offset:512\n\tv_mfma_f32_16x16x32_bf16 v[88:91], v[192:195], v[164:167], v[88:91]\n\tds_read_b128 v[120:123], v6\n\tv_mfma_f32_16x16x32_bf16 v[100:103], v[192:195], v[168:171], v[100:103]\n\tds_read_b128 v[124:127], v2 offset:2048\n\t"
            "v_mfma_f32_16x16x32_bf16 v[112:115], v[192:195], v[172:175], v[112:115]\n\tds_read_b128 v[128:131], v2 offset:4096\n\tv_mfma_f32_16x16x32_bf16 v[44:47], v[148:151], v[196:199], v[44:47]\n\tds_read_b128 v[132:135], v2 offset:6144\n\tv_mfma_f32_16x16x32_bf16 v[56:59], v[152:155], v[196:199], v[56:59]\n\tds_read_b128 v[136:139], v2 offset:8192\n\t"
            "v_mfma_f32_16x16x32_bf16 v[68:71], v[156:159], v[196:199], v[68:71]\n\tds_read_b128 v[140:143], v2 offset:10240\n\tv_mfma_f32_16x16x32_bf16 v[80:83], v[160:163], v[196:199], v[80:83]\n\tds_read_b128 v[144:147], v2 offset:12288\n\tv_mfma_f32_16x16x32_bf16 v[92:95], v[164:167], v[196:199], v[92:95]\n\tds_read_b64_tr_b16 v[180:181], v12 offset:1024\n\t"
            "ds_read_b64_tr_b16 v[182:183], v12 offset:1536\n\tv_mfma_f32_16x16x32_bf16 v[104:107], v[168:171], v[196:199], v[104:107]\n\tv_mfma_f32_16x16x32_bf16 v[116:119], v[172:175], v[196:199], v[116:119]\n\ts_waitcnt lgkmcnt(8)\n\tv_mfma_f32_16x16x32_bf16 v[36:39], v[176:179], v[120:123], v[36:39]\n\ts_waitcnt lgkmcnt(7)\n\t"
            "v_mfma_f32_16x16x32_bf16 v[48:51], v[176:179], v[124:127], v[48:51]\n\ts_waitcnt lgkmcnt(6)\n\tv_mfma_f32_16x16x32_bf16 v[60:63], v[176:179], v[128:131], v[60:63]\n\tds_read_b64_tr_b16 v[188:189], v12 offset:12288\n\tds_read_b64_tr_b16 v[190:191], v12 offset:12800\n\ts_waitcnt lgkmcnt(7)\n\t"
            "v_mfma_f32_16x16x32_bf16 v[72:75], v[176:179], v[132:135], v[72:75]\n\tds_read_b128 v[148:151], v7\n\ts_waitcnt lgkmcnt(7)\n\tv_mfma_f32_16x16x32_bf16 v[84:87], v[176:179], v[136:139], v[84:87]\n\tds_read_b128 v[152:155], v3 offset:2048\n\ts_waitcnt lgkmcnt(7)\n\t"
            "v_mfma_f32_16x16x32_bf16 v[96:99], v[176:179], v[140:143], v[96:99]\n\tds_read_b128 v[156:159], v3 offset:4096\n\ts_waitcnt lgkmcnt(7)\n\tv_mfma_f32_16x16x32_bf16 v[108:111], v[176:179], v[144:147], v[108:111]\n\tds_read_b128 v[160:163], v3 offset:6144\n\ts_waitcnt lgkmcnt(6)\n\t"
            "v_mfma_f32_16x16x32_bf16 v[40:43], v[180:183], v[120:123], v[40:43]\n\tds_read_b128 v[164:167], v3 offset:8192\n\tv_mfma_f32_16x16x32_bf16 v[52:55], v[180:183], v[124:127], v[52:55]\n\tds_read_b128 v[168:171], v3 offset:10240\n\tv_mfma_f32_16x16x32_bf16 v[64:67], v[180:183], v[128:131], v[64:67]\n\tds_read_b128 v[172:175], v3 offset:12288\n\t"
            "v_mfma_f32_16x16x32_bf16 v[76:79], v[180:183], v[132:135], v[76:79]\n\tds_read_b64_tr_b16 v[192:193], v12 offset:13312\n\tds_read_b64_tr_b16 v[194:195], v12 offset:13824\n\tv_mfma_f32_16x16x32_bf16 v[88:91], v[180:183], v[136:139], v[88:91]\n\tv_mfma_f32_16x16x32_bf16 v[100:103], v[180:183], v[140:143], v[100:103]\n\tv_mfma_f32_16x16x32_bf16 v[112:115], v[180:183], v[144:147], v[112:115]\n\t"
            "s_waitcnt lgkmcnt(8)\n\tv_mfma_f32_16x16x32_bf16 v[36:39], v[188:191], v[148:151], v[36:39]\n\ts_waitcnt lgkmcnt(7)\n\tv_mfma_f32_16x16x32_bf16 v[48:51], v[188:191], v[152:155], v[48:51]\n\ts_waitcnt lgkmcnt(6)\n\tv_mfma_f32_16x16x32_bf16 v[60:63], v[188:191], v[156:159], v[60:63]\n\t"
            "s_waitcnt lgkmcnt(5)\n\tv_mfma_f32_16x16x32_bf16 v[72:75], v[188:191], v[160:163], v[72:75]\n\ts_waitcnt lgkmcnt(0)\n\ts_barrier\n\t"
            ST(17)
            "ds_read_b64_tr_b16 v[176:177], v13 offset:0\n\tds_read_b64_tr_b16 v[178:179], v13 offset:512\n\tv_mfma_f32_16x16x32_bf16 v[84:87], v[188:191], v[164:167], v[84:87]\n\tds_read_b128 v[120:123], v8\n\tv_mfma_f32_16x16x32_bf16 v[96:99], v[188:191], v[168:171], v[96:99]\n\tds_read_b128 v[124:127], v4 offset:2048\n\t"
            "v_mfma_f32_16x16x32_bf16 v[108:111], v[188:191], v[172:175], v[108:111]\n\tds_read_b128 v[128:131], v4 offset:4096\n\tv_mfma_f32_16x16x32_bf16 v[40:43], v[192:195], v[148:151], v[40:43]\n\tds_read_b128 v[132:135], v4 offset:6144\n\tv_mfma_f32_16x16x32_bf16 v[52:55], v[192:195], v[152:155], v[52:55]\n\tds_read_b128 v[136:139], v4 offset:8192\n\t"
            "v_mfma_f32_16x16x32_bf16 v[64:67], v[192:195], v[156:159], v[64:67]\n\tds_read_b128 v[140:143], v4 offset:10240\n\tv_mfma_f32_16x16x32_bf16 v[76:79], v[192:195], v[160:163], v[76:79]\n\tds_read_b128 v[144:147], v4 offset:12288\n\tv_mfma_f32_16x16x32_bf16 v[88:91], v[192:195], v[164:167], v[88:91]\n\tds_read_b64_tr_b16 v[180:181], v13 offset:1024\n\t"
            "ds_read_b64_tr_b16 v[182:183], v13 offset:1536\n\tv_mfma_f32_16x16x32_bf16 v[100:103], v[192:195], v[168:171], v[100:103]\n\tds_read_b64_tr_b16 v[184:185], v13 offset:2048\n\tds_read_b64_tr_b16 v[186:187], v13 offset:2560\n\tv_mfma_f32_16x16x32_bf16 v[112:115], v[192:195], v[172:175], v[112:115]\n\tds_read2_b32 v[208:209], v14 offset1:16\n\t"
            SA(2) PRIA
            "s_mov_b64 vcc, s[18:19]\n\ts_bitcmp1_b32 9, s69\n\tds_read_b64_tr_b16 v[188:189], v13 offset:12288\n\tv_mov_b32 v252, -1\n\tds_read_b64_tr_b16 v[190:191], v13 offset:12800\n\tv_mov_b32 v254, -1\n\t"
            "ds_read_b128 v[148:151], v9\n\ts_waitcnt lgkmcnt(6)\n\tv_mfma_f32_16x16x32_bf16 v[40:43], v[180:183], v[120:123], v[40:43]\n\tds_read_b128 v[152:155], v5 offset:2048\n\tv_mfma_f32_16x16x32_bf16 v[36:39], v[176:179], v[120:123], v[36:39]\n\tds_read_b128 v[156:159], v5 offset:4096\n\t"
            "v_mfma_f32_16x16x32_bf16 v[48:51], v[176:179], v[124:127], v[48:51]\n\tds_read_b128 v[160:163], v5 offset:6144\n\tv_mfma_f32_16x16x32_bf16 v[60:63], v[176:179], v[128:131], v[60:63]\n\tds_read_b128 v[164:167], v5 offset:8192\n\tv_mfma_f32_16x16x32_bf16 v[52:55], v[180:183], v[124:127], v[52:55]\n\tds_read_b128 v[168:171], v5 offset:10240\n\t"
            "v_mfma_f32_16x16x32_bf16 v[64:67], v[180:183], v[128:131], v[64:67]\n\tds_read_b128 v[172:175], v5 offset:12288\n\tv_mfma_f32_16x16x32_bf16 v[76:79], v[180:183], v[132:135], v[76:79]\n\tds_read_b64_tr_b16 v[192:193], v13 offset:13312\n\ts_waitcnt lgkmcnt(7)\n\tv_mfma_f32_16x16x32_bf16 v[36:39], v[188:191], v[148:151], v[36:39]\n\t"
            "ds_read_b64_tr_b16 v[194:195], v13 offset:13824\n\tds_read_b64_tr_b16 v[196:197], v13 offset:14336\n\ts_waitcnt lgkmcnt(8)\n\tv_mfma_f32_16x16x32_bf16 v[48:51], v[188:191], v[152:155], v[48:51]\n\tds_read_b64_tr_b16 v[198:199], v13 offset:14848\n\tds_read2_b32 v[210:211], v14 offset0:32 offset1:48\n\t"
            "v_mfma_f32_16x16x32_bf16 v[72:75], v[176:179], v[132:135], v[72:75]\n\ts_nop 0\n\tv_mov_b32_dpp v200, v36 row_newbcast:0 row_mask:0xf bank_mask:0xf bound_ctrl:1\n\tds_read2_b32 v[212:213], v14 offset0:64 offset1:80\n\ts_waitcnt lgkmcnt(9)\n\tv_mfma_f32_16x16x32_bf16 v[60:63], v[188:191], v[156:159], v[60:63]\n\t"
            "v_mov_b32_dpp v201, v37 row_newbcast:0 row_mask:0xf bank_mask:0xf bound_ctrl:1\n\tds_read_b32 v214, v14 offset:384\n\tv_mfma_f32_16x16x32_bf16 v[72:75], v[188:191], v[160:163], v[72:75]\n\tv_mov_b32_dpp v202, v38 row_newbcast:0 row_mask:0xf bank_mask:0xf bound_ctrl:1\n\tv_mov_b32_dpp v203, v39 row_newbcast:0 row_mask:0xf bank_mask:0xf bound_ctrl:1\n\tv_fmac_f32 v36, v208, v200\n\t"
            "v_fmac_f32 v37, v208, v201\n\tv_fmac_f32 v38, v208, v202\n\tv_fmac_f32 v39, v208, v203\n\tv_cvt_pk_bf16_f32 v250, v36, v37\n\tv_cvt_pk_bf16_f32 v251, v38, v39\n\tv_and_b32 v36, v250, v18\n\t"
            "v_and_b32 v37, v251, v18\n\ts_waitcnt lgkmcnt(2)\n\tv_mfma_f32_16x16x32_bf16 v[40:43], v[192:195], v[148:151], v[40:43]\n\tv_and_b32 v38, v250, v19\n\tv_and_b32 v39, v251, v19\n\tv_mfma_f32_16x16x32_bf16 v[52:55], v[192:195], v[152:155], v[52:55]\n\t"
            "v_and_b32 v222, v250, v20\n\tv_and_b32 v223, v251, v20\n\tv_mfma_f32_16x16x32_bf16 v[64:67], v[192:195], v[156:159], v[64:67]\n\ts_nop 1\n\tv_mov_b32_dpp v204, v40 row_newbcast:0 row_mask:0xf bank_mask:0xf bound_ctrl:1\n\tv_mov_b32_dpp v205, v41 row_newbcast:0 row_mask:0xf bank_mask:0xf bound_ctrl:1\n\t"
            "v_mfma_f32_16x16x32_bf16 v[76:79], v[192:195], v[160:163], v[76:79]\n\tv_mov_b32_dpp v206, v42 row_newbcast:0 row_mask:0xf bank_mask:0xf bound_ctrl:1\n\tv_mov_b32_dpp v207, v43 row_newbcast:0 row_mask:0xf bank_mask:0xf bound_ctrl:1\n\tv_fmac_f32 v40, v208, v204\n\tv_fmac_f32 v41, v208, v205\n\tv_fmac_f32 v42, v208, v206\n\t"
            "v_fmac_f32 v43, v208, v207\n\tv_cvt_pk_bf16_f32 v40, v40, v41\n\tv_cvt_pk_bf16_f32 v41, v42, v43\n\tv_cndmask_b32 v42, v40, v40, vcc\n\tv_cndmask_b32 v43, v41, v41, vcc\n\tv_and_b32 v224, v250, v21\n\t"
            "v_and_b32 v225, v251, v21\n\tv_mfma_f32_16x16x32_bf16 v[218:221], v[40:43], v[36:39], 0\n\tv_fmac_f32 v48, v209, v200\n\tv_fmac_f32 v49, v209, v201\n\tv_mfma_f32_16x16x32_bf16 v[226:229], v[40:43], v[222:225], 0\n\tv_fmac_f32 v50, v209, v202\n\t"
            "v_fmac_f32 v51, v209, v203\n\tv_cvt_pk_bf16_f32 v250, v48, v49\n\ts_nop 1\n\tv_cndmask_b32 v36, v220, v218, s[14:15]\n\tv_cndmask_b32 v37, v221, v219, s[16:17]\n\tv_cvt_pk_bf16_f32 v251, v50, v51\n\t"
            "v_and_b32 v48, v250, v18\n\tv_cndmask_b32 v38, v228, v226, s[14:15]\n\tv_cndmask_b32 v39, v229, v227, s[16:17]\n\tv_and_b32 v49, v251, v18\n\tv_and_b32 v50, v250, v19\n\tv_and_b32 v51, v251, v19\n\t"
            "v_and_b32 v234, v250, v20\n\tv_and_b32 v235, v251, v20\n\tv_and_b32 v236, v250, v21\n\tv_and_b32 v237, v251, v21\n\tv_fmac_f32 v60, v210, v200\n\tv_fmac_f32 v61, v210, v201\n\t"
            "v_fmac_f32 v62, v210, v202\n\tv_fmac_f32 v63, v210, v203\n\tv_fmac_f32 v52, v209, v204\n\tv_fmac_f32 v53, v209, v205\n\tv_fmac_f32 v64, v210, v204\n\tv_fmac_f32 v65, v210, v205\n\t"
            "v_cvt_pk_bf16_f32 v250, v60, v61\n\tv_cvt_pk_bf16_f32 v251, v62, v63\n\tv_fmac_f32 v76, v211, v204\n\tv_fmac_f32 v77, v211, v205\n\tv_fmac_f32 v54, v209, v206\n\tv_fmac_f32 v55, v209, v207\n\t"
            "v_cvt_pk_bf16_f32 v52, v52, v53\n\tv_fmac_f32 v66, v210, v206\n\tv_fmac_f32 v67, v210, v207\n\tv_cvt_pk_bf16_f32 v64, v64, v65\n\tv_and_b32 v60, v250, v18\n\tv_and_b32 v61, v251, v18\n\t"
            "v_and_b32 v62, v250, v19\n\tv_and_b32 v63, v251, v19\n\tv_and_b32 v246, v250, v20\n\tv_and_b32 v247, v251, v20\n\tv_and_b32 v248, v250, v21\n\tv_and_b32 v249, v251, v21\n\t"
            "v_fmac_f32 v78, v211, v206\n\tv_fmac_f32 v79, v211, v207\n\tv_fmac_f32 v72, v211, v200\n\tv_fmac_f32 v73, v211, v201\n\tv_fmac_f32 v74, v211, v202\n\tv_fmac_f32 v75, v211, v203\n\t"
            "v_cvt_pk_bf16_f32 v76, v76, v77\n\tv_cvt_pk_bf16_f32 v53, v54, v55\n\tv_cvt_pk_bf16_f32 v65, v66, v67\n\tv_cvt_pk_bf16_f32 v77, v78, v79\n\tv_cvt_pk_bf16_f32 v250, v72, v73\n\tv_cvt_pk_bf16_f32 v251, v74, v75\n\t"
            "v_cndmask_b32 v54, v40, v52, vcc\n\tv_cndmask_b32 v55, v41, v53, vcc\n\tv_cndmask_b32 v66, v52, v64, vcc\n\tv_cndmask_b32 v67, v53, v65, vcc\n\tv_mfma_f32_16x16x32_bf16 v[230:233], v[52:55], v[48:51], 0\n\tv_cndmask_b32 v78, v64, v76, vcc\n\t"
            "v_cndmask_b32 v79, v65, v77, vcc\n\tv_mfma_f32_16x16x32_bf16 v[242:245], v[64:67], v[60:63], 0\n\tv_and_b32 v72, v250, v18\n\tv_and_b32 v73, v251, v18\n\tv_mfma_f32_16x16x32_bf16 v[238:241], v[52:55], v[234:237], 0\n\tv_and_b32 v74, v250, v19\n\t"
            "v_and_b32 v75, v251, v19\n\tv_mfma_f32_16x16x32_bf16 v[218:221], v[64:67], v[246:249], 0\n\tv_cndmask_b32 v48, v232, v230, s[14:15]\n\tv_cndmask_b32 v49, v233, v231, s[16:17]\n\tv_mfma_f32_16x16x32_bf16 v[222:225], v[76:79], v[72:75], 0\n\tv_cndmask_b32 v60, v244, v242, s[14:15]\n\t"
            "v_cndmask_b32 v61, v245, v243, s[16:17]\n\tv_mfma_f32_16x16x32_bf16 v[84:87], v[176:179], v[136:139], v[84:87]\n\tv_cndmask_b32 v50, v240, v238, s[14:15]\n\tv_cndmask_b32 v51, v241, v239, s[16:17]\n\tv_mfma_f32_16x16x32_bf16 v[96:99], v[176:179], v[140:143], v[96:99]\n\ts_nop 1\n\t"
            "v_cndmask_b32 v72, v224, v222, s[14:15]\n\tv_cndmask_b32 v73, v225, v223, s[16:17]\n\tv_mfma_f32_16x16x32_bf16 v[88:91], v[180:183], v[136:139], v[88:91]\n\ts_cbranch_scc0 1f\n\tv_cvt_pk_bf16_f32 v253, v36, v37\n\tv_min_u32 v253, -2, v253\n\t"
            "v_cvt_pk_bf16_f32 v255, v48, v49\n\tv_min_u32 v255, -2, v255\n\tbuffer_store_dwordx4 v[252:255], v17, s[44:47], s20 offen sc1\n\ts_nop 1\n\tv_cvt_pk_bf16_f32 v253, v60, v61\n\tv_min_u32 v253, -2, v253\n\t"
            "v_cvt_pk_bf16_f32 v255, v72, v73\n\tv_min_u32 v255, -2, v255\n\tbuffer_store_dwordx4 v[252:255], v17, s[44:47], s21 offen sc1\n\t1:\nv_cndmask_b32 v62, v220, v218, s[14:15]\n\tv_mfma_f32_16x16x32_bf16 v[100:103], v[180:183], v[140:143], v[100:103]\n\t"
            "v_cndmask_b32 v63, v221, v219, s[16:17]\n\tv_and_b32 v226, v250, v20\n\tv_mfma_f32_16x16x32_bf16 v[112:115], v[180:183], v[144:147], v[112:115]\n\tv_and_b32 v227, v251, v20\n\tv_and_b32 v228, v250, v21\n\tv_mfma_f32_16x16x32_bf16 v[84:87], v[188:191], v[164:167], v[84:87]\n\t"
            "v_and_b32 v229, v251, v21\n\tv_mov_b32 v180, 0\n\tv_mfma_f32_16x16x32_bf16 v[108:111], v[176:179], v[144:147], v[108:111]\n\tv_mov_b32 v176, 0\n\tds_write_b64 v28, v[36:37] offset:0\n\tv_mfma_f32_16x16x32_bf16 v[230:233], v[76:79], v[226:229], 0\n\t"
            "s_waitcnt lgkmcnt(1)\n\ts_nop 0\n\tv_fmac_f32 v84, v212, v200\n\tv_fmac_f32 v85, v212, v201\n\tv_mfma_f32_16x16x32_bf16 v[96:99], v[188:191], v[168:171], v[96:99]\n\tds_write_b64 v29, v[38:39] offset:0\n\t"
            "v_fmac_f32 v86, v212, v202\n\tv_mfma_f32_16x16x32_bf16 v[88:91], v[192:195], v[164:167], v[88:91]\n\tv_cndmask_b32 v74, v232, v230, s[14:15]\n\tv_cndmask_b32 v75, v233, v231, s[16:17]\n\tds_write_b64 v28, v[48:49] offset:512\n\tv_mfma_f32_16x16x32_bf16 v[100:103], v[192:195], v[168:171], v[100:103]\n\t"
            "v_fmac_f32 v87, v212, v203\n\tv_cvt_pk_bf16_f32 v250, v84, v85\n\tv_mfma_f32_16x16x32_bf16 v[112:115], v[192:195], v[172:175], v[112:115]\n\tds_write_b64 v29, v[50:51] offset:512\n\tv_cvt_pk_bf16_f32 v251, v86, v87\n\tv_mfma_f32_16x16x32_bf16 v[108:111], v[188:191], v[172:175], v[108:111]\n\t"
            "v_and_b32 v84, v250, v18\n\tv_and_b32 v85, v251, v18\n\tds_write_b64 v28, v[60:61] offset:1024\n\tv_and_b32 v86, v250, v19\n\tv_and_b32 v87, v251, v19\n\tv_and_b32 v238, v250, v20\n\t"
            "v_and_b32 v239, v251, v20\n\tv_and_b32 v240, v250, v21\n\tds_write_b64 v29, v[62:63] offset:1024\n\tv_and_b32 v241, v251, v21\n\tv_fmac_f32 v96, v213, v200\n\tv_fmac_f32 v97, v213, v201\n\t"
            "v_fmac_f32 v98, v213, v202\n\tv_fmac_f32 v99, v213, v203\n\tds_write_b64 v28, v[72:73] offset:1536\n\tv_fmac_f32 v88, v212, v204\n\tv_fmac_f32 v89, v212, v205\n\tv_fmac_f32 v100, v213, v204\n\t"
            "v_fmac_f32 v101, v213, v205\n\tv_cvt_pk_bf16_f32 v250, v96, v97\n\tds_write_b64 v29, v[74:75] offset:1536\n\tv_cvt_pk_bf16_f32 v251, v98, v99\n\tv_fmac_f32 v112, v214, v204\n\tv_fmac_f32 v113, v214, v205\n\t"
            "v_fmac_f32 v90, v212, v206\n\tv_fmac_f32 v91, v212, v207\n\tds_read_b128 v[36:39], v15 offset:0\n\tv_cvt_pk_bf16_f32 v88, v88, v89\n\tv_fmac_f32 v102, v213, v206\n\tv_fmac_f32 v103, v213, v207\n\t"
            "ds_read_b128 v[48:51], v15 offset:64\n\tv_cvt_pk_bf16_f32 v100, v100, v101\n\ts_waitcnt lgkmcnt(0)\n\ts_barrier\n\tv_and_b32 v96, v250, v18\n\tds_read_b128 v[60:63], v15 offset:128\n\t"
            "v_and_b32 v97, v251, v18\n\tv_and_b32 v98, v250, v19\n\tv_and_b32 v99, v251, v19\n\tds_read_b128 v[72:75], v15 offset:192\n\tv_and_b32 v218, v250, v20\n\tv_and_b32 v219, v251, v20\n\t"
            "v_and_b32 v220, v250, v21\n\tv_and_b32 v221, v251, v21\n\tv_fmac_f32 v114, v214, v206\n\tv_fmac_f32 v115, v214, v207\n\tv_fmac_f32 v108, v214, v200\n\tv_fmac_f32 v109, v214, v201\n\t"
            "v_fmac_f32 v110, v214, v202\n\tv_fmac_f32 v111, v214, v203\n\tv_cvt_pk_bf16_f32 v112, v112, v113\n\tv_cvt_pk_bf16_f32 v89, v90, v91\n\tv_cvt_pk_bf16_f32 v101, v102, v103\n\tv_cvt_pk_bf16_f32 v113, v114, v115\n\t"
            "v_cvt_pk_bf16_f32 v250, v108, v109\n\tv_cvt_pk_bf16_f32 v251, v110, v111\n\tv_cndmask_b32 v90, v76, v88, vcc\n\tv_cndmask_b32 v91, v77, v89, vcc\n\tv_cndmask_b32 v102, v88, v100, vcc\n\tv_cndmask_b32 v103, v89, v101, vcc\n\t"
            "v_mfma_f32_16x16x32_bf16 v[234:237], v[88:91], v[84:87], 0\n\tv_cndmask_b32 v114, v100, v112, vcc\n\tv_cndmask_b32 v115, v101, v113, vcc\n\tv_mfma_f32_16x16x32_bf16 v[246:249], v[100:103], v[96:99], 0\n\tv_and_b32 v108, v250, v18\n\tv_and_b32 v109, v251, v18\n\t"
            "v_mfma_f32_16x16x32_bf16 v[242:245], v[88:91], v[238:241], 0\n\tv_and_b32 v110, v250, v19\n\tv_and_b32 v111, v251, v19\n\tv_mfma_f32_16x16x32_bf16 v[222:225], v[100:103], v[218:221], 0\n\tv_cndmask_b32 v84, v236, v234, s[14:15]\n\tv_cndmask_b32 v85, v237, v235, s[16:17]\n\t"
            "ds_write_b64 v28, v[84:85] offset:38912\n\tv_mfma_f32_16x16x32_bf16 v[226:229], v[112:115], v[108:111], 0\n\tv_cndmask_b32 v96, v248, v246, s[14:15]\n\tv_cndmask_b32 v97, v249, v247, s[16:17]\n\tv_cndmask_b32 v86, v244, v242, s[14:15]\n\tds_write_b64 v28, v[96:97] offset:39424\n\t"
            "v_cndmask_b32 v87, v245, v243, s[16:17]\n\tv_cndmask_b32 v98, v224, v222, s[14:15]\n\tv_cndmask_b32 v99, v225, v223, s[16:17]\n\ts_nop 0\n\tv_cndmask_b32 v108, v228, v226, s[14:15]\n\tv_cndmask_b32 v109, v229, v227, s[16:17]\n\t"
            "ds_write_b64 v29, v[86:87] offset:38912\n\ts_cbranch_scc0 1f\n\tv_cvt_pk_bf16_f32 v253, v84, v85\n\tv_min_u32 v253, -2, v253\n\tv_cvt_pk_bf16_f32 v255, v96, v97\n\tv_min_u32 v255, -2, v255\n\t"
            "buffer_store_dwordx4 v[252:255], v17, s[44:47], s22 offen sc1\n\ts_nop 1\n\tv_cvt_pk_bf16_f32 v253, v108, v109\n\tv_min_u32 v253, -2, v253\n\tbuffer_store_dwordx4 v[252:255], v17, s[44:47], s23 offen sc1\n\t1:\n"
            "v_and_b32 v230, v250, v20\n\tv_and_b32 v231, v251, v20\n\tv_and_b32 v232, v250, v21\n\tv_and_b32 v233, v251, v21\n\tds_write_b64 v29, v[98:99] offset:39424\n\tv_mfma_f32_16x16x32_bf16 v[44:47], v[120:123], v[184:187], v[44:47]\n\t"
            "v_mov_b32 v218, 0\n\tv_mov_b32 v219, 0\n\tv_mfma_f32_16x16x32_bf16 v[234:237], v[112:115], v[230:233], 0\n\tds_write_b64 v28, v[108:109] offset:39936\n\tv_mov_b32 v188, 0\n\tv_mfma_f32_16x16x32_bf16 v[56:59], v[124:127], v[184:187], v[56:59]\n\t"
            "v_mov_b32 v192, 0\n\tds_read_b128 v[120:123], v6\n\ts_nop 2\n\tv_cndmask_b32 v110, v236, v234, s[14:15]\n\tv_cndmask_b32 v111, v237, v235, s[16:17]\n\tv_mfma_f32_16x16x32_bf16 v[68:71], v[128:131], v[184:187], v[68:71]\n\t"
            "ds_write_b64 v29, v[110:111] offset:39936\n\tv_mov_b32 v234, 0\n\tv_mfma_f32_16x16x32_bf16 v[80:83], v[132:135], v[184:187], v[80:83]\n\tv_mov_b32 v235, 0\n\tds_write_b32 v33, v33\n\t"
            SA(3)
            "v_mfma_f32_16x16x32_bf16 v[92:95], v[136:139], v[184:187], v[92:95]\n\tds_read_b128 v[124:127], v2 offset:2048\n\tv_mfma_f32_16x16x32_bf16 v[104:107], v[140:143], v[184:187], v[104:107]\n\tds_read_b128 v[128:131], v2 offset:4096\n\tv_mfma_f32_16x16x32_bf16 v[116:119], v[144:147], v[184:187], v[116:119]\n\tds_read_b64_tr_b16 v[184:185], v12 offset:2048\n\t"
            "v_mfma_f32_16x16x32_bf16 v[44:47], v[148:151], v[196:199], v[44:47]\n\tds_read_b64_tr_b16 v[186:187], v12 offset:2560\n\tds_read_b128 v[148:151], v7\n\tv_mfma_f32_16x16x32_bf16 v[56:59], v[152:155], v[196:199], v[56:59]\n\tds_read_b128 v[152:155], v3 offset:2048\n\tv_mfma_f32_16x16x32_bf16 v[68:71], v[156:159], v[196:199], v[68:71]\n\t"
            "ds_read_b128 v[132:135], v2 offset:6144\n\tv_mfma_f32_16x16x32_bf16 v[80:83], v[160:163], v[196:199], v[80:83]\n\tds_read_b128 v[156:159], v3 offset:4096\n\tv_mfma_f32_16x16x32_bf16 v[92:95], v[164:167], v[196:199], v[92:95]\n\tds_read_b128 v[160:163], v3 offset:6144\n\tv_mfma_f32_16x16x32_bf16 v[104:107], v[168:171], v[196:199], v[104:107]\n\t"
            "ds_read_b128 v[136:139], v2 offset:8192\n\tv_mfma_f32_16x16x32_bf16 v[116:119], v[172:175], v[196:199], v[116:119]\n\tds_read_b64_tr_b16 v[196:197], v12 offset:14336\n\tds_read_b64_tr_b16 v[198:199], v12 offset:14848\n\ts_waitcnt lgkmcnt(8)\n\tv_mfma_f32_16x16x32_bf16 v[44:47], v[120:123], v[184:187], v[44:47]\n\t"
            "ds_read_b128 v[140:143], v2 offset:10240\n\tv_mfma_f32_16x16x32_bf16 v[56:59], v[124:127], v[184:187], v[56:59]\n\tds_read_b128 v[144:147], v2 offset:12288\n\tv_mfma_f32_16x16x32_bf16 v[68:71], v[128:131], v[184:187], v[68:71]\n\tds_read_b128 v[164:167], v3 offset:8192\n\ts_waitcnt lgkmcnt(8)\n\t"
            "v_mfma_f32_16x16x32_bf16 v[80:83], v[132:135], v[184:187], v[80:83]\n\tds_read_b128 v[168:171], v3 offset:10240\n\tds_read_b128 v[172:175], v3 offset:12288\n\ts_waitcnt lgkmcnt(7)\n\tv_mfma_f32_16x16x32_bf16 v[92:95], v[136:139], v[184:187], v[92:95]\n\tds_read_b128 v[84:87], v15 offset:256\n\t"
            "s_waitcnt lgkmcnt(6)\n\tv_mfma_f32_16x16x32_bf16 v[44:47], v[148:151], v[196:199], v[44:47]\n\tds_read_b128 v[96:99], v15 offset:320\n\tv_mfma_f32_16x16x32_bf16 v[56:59], v[152:155], v[196:199], v[56:59]\n\tds_read_b128 v[108:111], v15 offset:384\n\tv_mfma_f32_16x16x32_bf16 v[68:71], v[156:159], v[196:199], v[68:71]\n\t"
            "s_nop 3\n\tv_mov_b32 v216, v44\n\tv_mov_b32 v217, v44\n\tv_mfma_f32_16x16x32_bf16 v[80:83], v[160:163], v[196:199], v[80:83]\n\ts_nop 0\n\tv_permlane32_swap_b32 v216, v217\n\t"
            "v_mov_b32 v217, v216\n\ts_waitcnt lgkmcnt(5)\n\tv_mfma_f32_16x16x32_bf16 v[104:107], v[140:143], v[184:187], v[104:107]\n\tv_permlane16_swap_b32 v216, v217\n\tv_fmac_f32 v44, v36, v216\n\tv_mfma_f32_16x16x32_bf16 v[116:119], v[144:147], v[184:187], v[116:119]\n\t"
            "v_fmac_f32 v45, v37, v216\n\tv_fmac_f32 v46, v38, v216\n\tv_fmac_f32 v47, v39, v216\n\tv_cvt_pk_bf16_f32 v44, v44, v45\n\tv_cvt_pk_bf16_f32 v45, v46, v47\n\tv_mfma_f32_16x16x32_bf16 v[92:95], v[164:167], v[196:199], v[92:95]\n\t"
            "v_and_b32 v220, v44, v22\n\tv_and_b32 v221, v45, v22\n\ts_waitcnt lgkmcnt(0)\n\tv_mfma_f32_16x16x32_bf16 v[104:107], v[168:171], v[196:199], v[104:107]\n\tv_and_b32 v236, v44, v23\n\tv_and_b32 v237, v45, v23\n\t"
            "v_mfma_f32_16x16x32_bf16 v[116:119], v[172:175], v[196:199], v[116:119]\n\tv_fmac_f32 v56, v48, v216\n\tv_fmac_f32 v57, v49, v216\n\tv_fmac_f32 v58, v50, v216\n\tv_fmac_f32 v59, v51, v216\n\tv_cvt_pk_bf16_f32 v56, v56, v57\n\t"
            "v_cvt_pk_bf16_f32 v57, v58, v59\n\tv_and_b32 v222, v56, v22\n\tv_and_b32 v223, v57, v22\n\tv_and_b32 v238, v56, v23\n\tv_and_b32 v239, v57, v23\n\tv_fmac_f32 v68, v60, v216\n\t"
            "v_fmac_f32 v69, v61, v216\n\tv_fmac_f32 v70, v62, v216\n\tv_fmac_f32 v71, v63, v216\n\tds_read_b32 v27, v32\n\tds_read2st64_b32 v[42:43], v31 offset0:0 offset1:1\n\tds_read2st64_b32 v[40:41], v30 offset0:0 offset1:1\n\t"
            "ds_read2st64_b32 v[66:67], v31 offset0:2 offset1:3\n\tds_read2st64_b32 v[64:65], v30 offset0:2 offset1:3\n\tv_cvt_pk_bf16_f32 v68, v68, v69\n\tv_cvt_pk_bf16_f32 v69, v70, v71\n\tv_and_b32 v224, v68, v22\n\tv_and_b32 v225, v69, v22\n\t"
            "v_and_b32 v240, v68, v23\n\tv_and_b32 v241, v69, v23\n\tv_fmac_f32 v80, v72, v216\n\tv_fmac_f32 v81, v73, v216\n\tv_fmac_f32 v82, v74, v216\n\tv_fmac_f32 v83, v75, v216\n\t"
            "v_cvt_pk_bf16_f32 v80, v80, v81\n\tv_cvt_pk_bf16_f32 v81, v82, v83\n\tv_and_b32 v226, v80, v22\n\tv_and_b32 v227, v81, v22\n\tv_and_b32 v242, v80, v23\n\tv_and_b32 v243, v81, v23\n\t"
            SA(6)
            "s_waitcnt lgkmcnt(2)\n\tv_cmp_ge_u32 vcc, 0, v27\n\ts_cbranch_vccnz 10f\n\t20:\nv_and_b32 v177, v42, v24\n\tv_and_b32 v178, v42, v25\n\t"
            "v_and_b32 v179, v42, v26\n\tv_and_b32 v189, v43, v24\n\tv_and_b32 v190, v43, v25\n\tv_and_b32 v191, v43, v26\n\tv_mfma_f32_16x16x32_bf16 v[44:47], v[234:237], v[176:179], 0\n\tv_and_b32 v181, v40, v24\n\t"
            "v_and_b32 v182, v40, v25\n\tv_mfma_f32_16x16x32_bf16 v[56:59], v[236:239], v[188:191], 0\n\tv_and_b32 v183, v40, v26\n\tv_and_b32 v193, v41, v24\n\tv_and_b32 v194, v41, v25\n\tv_and_b32 v195, v41, v26\n\t"
            "v_mfma_f32_16x16x32_bf16 v[44:47], v[218:221], v[180:183], v[44:47]\n\ts_waitcnt lgkmcnt(0)\n\tv_and_b32 v177, v66, v24\n\tv_and_b32 v178, v66, v25\n\tv_mfma_f32_16x16x32_bf16 v[56:59], v[220:223], v[192:195], v[56:59]\n\tv_and_b32 v179, v66, v26\n\t"
            "v_and_b32 v189, v67, v24\n\tv_and_b32 v190, v67, v25\n\ts_nop 0\n\tv_cvt_pk_bf16_f32 v36, v44, v45\n\tv_cvt_pk_bf16_f32 v37, v46, v47\n\tv_min_u32 v36, -2, v36\n\t"
            "v_and_b32 v191, v67, v26\n\tv_cvt_pk_bf16_f32 v38, v56, v57\n\tv_cvt_pk_bf16_f32 v39, v58, v59\n\tv_min_u32 v38, -2, v38\n\tv_mfma_f32_16x16x32_bf16 v[68:71], v[238:241], v[176:179], 0\n\ts_nop 0\n\t"
            "v_permlane16_swap_b32 v36, v38\n\tv_permlane16_swap_b32 v37, v39\n\tbuffer_store_dwordx4 v[36:39], v16, s[44:47], s20 offen sc1\n\tv_mfma_f32_16x16x32_bf16 v[80:83], v[240:243], v[188:191], 0\n\tv_and_b32 v181, v64, v24\n\tv_and_b32 v182, v64, v25\n\t"
            "v_and_b32 v183, v64, v26\n\tv_and_b32 v193, v65, v24\n\tv_and_b32 v194, v65, v25\n\tv_and_b32 v195, v65, v26\n\tv_mfma_f32_16x16x32_bf16 v[68:71], v[222:225], v[180:183], v[68:71]\n\tv_fmac_f32 v92, v84, v216\n\t"
            "v_fmac_f32 v93, v85, v216\n\tv_mfma_f32_16x16x32_bf16 v[80:83], v[224:227], v[192:195], v[80:83]\n\tv_fmac_f32 v94, v86, v216\n\tv_fmac_f32 v95, v87, v216\n\tv_cvt_pk_bf16_f32 v92, v92, v93\n\ts_nop 1\n\t"
            "v_cvt_pk_bf16_f32 v60, v68, v69\n\tv_cvt_pk_bf16_f32 v61, v70, v71\n\tv_min_u32 v60, -2, v60\n\tv_fmac_f32 v104, v96, v216\n\tv_cvt_pk_bf16_f32 v62, v80, v81\n\tv_cvt_pk_bf16_f32 v63, v82, v83\n\t"
            "v_min_u32 v62, -2, v62\n\tv_fmac_f32 v105, v97, v216\n\tv_permlane16_swap_b32 v61, v63\n\tv_permlane16_swap_b32 v60, v62\n\tbuffer_store_dwordx4 v[60:63], v16, s[44:47], s21 offen sc1\n\tds_write_b32 v34, v35\n\t"
            SA(4)
            "v_cvt_pk_bf16_f32 v93, v94, v95\n\tv_fmac_f32 v106, v98, v216\n\tv_fmac_f32 v107, v99, v216\n\tv_cvt_pk_bf16_f32 v104, v104, v105\n\tv_fmac_f32 v116, v108, v216\n\tv_fmac_f32 v117, v109, v216\n\t"
            "v_and_b32 v228, v92, v22\n\tds_read_b32 v27, v32\n\tds_read2st64_b32 v[102:103], v31 offset0:152 offset1:153\n\tds_read2st64_b32 v[100:101], v30 offset0:152 offset1:153\n\tds_read2st64_b32 v[78:79], v31 offset0:154 offset1:155\n\tds_read2st64_b32 v[76:77], v30 offset0:154 offset1:155\n\t"
            "v_and_b32 v229, v93, v22\n\tv_and_b32 v244, v92, v23\n\tv_and_b32 v245, v93, v23\n\tv_cvt_pk_bf16_f32 v105, v106, v107\n\tv_fmac_f32 v118, v110, v216\n\tv_fmac_f32 v119, v111, v216\n\t"
            "v_cvt_pk_bf16_f32 v116, v116, v117\n\tv_and_b32 v230, v104, v22\n\tv_and_b32 v231, v105, v22\n\tv_and_b32 v246, v104, v23\n\tv_and_b32 v247, v105, v23\n\tv_cvt_pk_bf16_f32 v117, v118, v119\n\t"
            "v_and_b32 v232, v116, v22\n\tv_and_b32 v233, v117, v22\n\tv_and_b32 v248, v116, v23\n\tv_and_b32 v249, v117, v23\n\ts_waitcnt lgkmcnt(3)\n\tv_cmp_ge_u32 vcc, 1, v27\n\t"
            "s_cbranch_vccnz 11f\n\t21:\nv_and_b32 v177, v102, v24\n\tv_and_b32 v178, v102, v25\n\tv_and_b32 v179, v102, v26\n\tv_and_b32 v189, v103, v24\n\t"
            "v_and_b32 v190, v103, v25\n\tv_and_b32 v191, v103, v26\n\tv_mfma_f32_16x16x32_bf16 v[92:95], v[242:245], v[176:179], 0\n\ts_waitcnt lgkmcnt(0)\n\tv_and_b32 v181, v100, v24\n\tv_and_b32 v182, v100, v25\n\t"
            "v_mfma_f32_16x16x32_bf16 v[104:107], v[244:247], v[188:191], 0\n\tv_and_b32 v183, v100, v26\n\tv_and_b32 v193, v101, v24\n\tv_and_b32 v194, v101, v25\n\tv_and_b32 v195, v101, v26\n\tv_mfma_f32_16x16x32_bf16 v[92:95], v[226:229], v[180:183], v[92:95]\n\t"
            "v_and_b32 v177, v78, v24\n\tv_and_b32 v178, v78, v25\n\tv_mfma_f32_16x16x32_bf16 v[104:107], v[228:231], v[192:195], v[104:107]\n\tv_and_b32 v179, v78, v26\n\tv_and_b32 v181, v76, v24\n\tv_and_b32 v182, v76, v25\n\t"
            "v_mfma_f32_16x16x32_bf16 v[116:119], v[246:249], v[176:179], 0\n\tv_and_b32 v183, v76, v26\n\tv_cvt_pk_bf16_f32 v84, v92, v93\n\ts_nop 1\n\tv_cvt_pk_bf16_f32 v86, v104, v105\n\tv_mfma_f32_16x16x32_bf16 v[116:119], v[230:233], v[180:183], v[116:119]\n\t"
            "v_cvt_pk_bf16_f32 v85, v94, v95\n\tv_min_u32 v84, -2, v84\n\tv_cvt_pk_bf16_f32 v87, v106, v107\n\tv_min_u32 v86, -2, v86\n\ts_nop 0\n\tv_permlane16_swap_b32 v85, v87\n\t"
            "v_permlane16_swap_b32 v84, v86\n\tbuffer_store_dwordx4 v[84:87], v16, s[44:47], s22 offen sc1\n\tv_cvt_pk_bf16_f32 v108, v116, v117\n\tv_cvt_pk_bf16_f32 v109, v118, v119\n\tv_min_u32 v108, -2, v108\n\ts_nop 0\n\t"
            "v_permlane16_swap_b32 v109, v111\n\tv_permlane16_swap_b32 v108, v110\n\tbuffer_store_dwordx4 v[108:111], v215, s[44:47], s23 offen sc1\n\tds_write_b32 v34, v34\n\t"
            SA(5)
            "s_setprio 0\n\t"
            DUMP
            "s_cmp_eq_u32 s69, 1\n\ts_cbranch_scc1 .Lout\n\ts_endpgm\n\t10:\nds_read_b32 v27, v32\n\tds_read2st64_b32 v[42:43], v31 offset0:0 offset1:1\n\t"
            "ds_read2st64_b32 v[40:41], v30 offset0:0 offset1:1\n\tds_read2st64_b32 v[66:67], v31 offset0:2 offset1:3\n\tds_read2st64_b32 v[64:65], v30 offset0:2 offset1:3\n\ts_waitcnt lgkmcnt(0)\n\tv_cmp_ge_u32 vcc, 0, v27\n\ts_cbranch_vccz 20b\n\t"
            ASLP
            "s_sub_u32 s68, s68, 1\n\ts_cbranch_scc0 10b\n\ts_branch 20b\n\t11:\nds_read_b32 v27, v32\n\tds_read2st64_b32 v[102:103], v31 offset0:152 offset1:153\n\t"
            "ds_read2st64_b32 v[100:101], v30 offset0:152 offset1:153\n\tds_read2st64_b32 v[78:79], v31 offset0:154 offset1:155\n\tds_read2st64_b32 v[76:77], v30 offset0:154 offset1:155\n\ts_waitcnt lgkmcnt(0)\n\tv_cmp_ge_u32 vcc, 1, v27\n\ts_cbranch_vccz 21b\n\t"
            ASLP
            "s_sub_u32 s68, s68, 1\n\ts_cbranch_scc0 11b\n\ts_branch 21b\n\t.LB:\ns_and_b32 s72, s56, 1\n\ts_lshr_b32 s73, s56, 1\n\t"
            "v_and_b32 v156, 2, v51\n\tv_lshlrev_b32 v156, 1, v156\n\tv_and_b32 v157, 7, v52\n\tv_xor_b32 v157, v156, v157\n\tv_mul_u32_u24 v156, 0x500, v51\n\tv_lshl_add_u32 v156, v157, 4, v156\n\t"
            "s_mul_i32 s74, s72, 0x2800\n\ts_mul_i32 s75, s73, 0xf000\n\ts_add_i32 s75, s75, s74\n\tv_add_u32 v154, s75, v156\n\ts_mul_i32 s75, s73, 0x5000\n\ts_add_i32 s75, s75, s74\n\t"
            "s_addk_i32 s75, 0x5000\n\tv_add_u32 v156, s75, v156\n\ts_cmp_lg_u32 s63, 0\n\ts_cselect_b32 s74, 81920, 0\n\ts_max_u32 s74, s74, 65536\n\tv_add_u32 v158, s62, v52\n\t"
            "v_min_u32 v159, 47, v52\n\ts_add_i32 s75, s62, 64\n\tv_add_u32 v159, s75, v159\n\tv_max_i32 v158, 0, v158\n\tv_min_i32 v159, 0x3ff, v159\n\tv_lshl_add_u32 v158, v158, 4, s74\n\t"
            "v_lshl_add_u32 v159, v159, 4, s74\n\tv_lshl_add_u32 v59, v53, 4, v57\n\ts_cmp_lg_u32 s73, 0\n\ts_cselect_b64 vcc, -1, 0\n\tv_cndmask_b32 v97, v59, v52, vcc\n\ts_lshl_b32 s75, s72, 6\n\t"
            "s_add_i32 s75, s75, s62\n\tv_add_u32 v97, s75, v97\n\tv_mov_b32 v60, s62\n\tv_add_u32 v60, 0x6f, v60\n\tv_min_i32 v60, 0x3ff, v60\n\tv_med3_i32 v97, v97, 0, v60\n\t"
            "s_lshl3_add_u32 s74, s73, s74\n\tv_lshl_add_u32 v97, v97, 4, s74\n\ts_mul_i32 s30, s73, 576\n\ts_lshl_b32 s75, s72, 8\n\ts_add_i32 s30, s30, s75\n\ts_add_i32 s30, s30, 160000\n\t"
            "s_lshl_b32 s74, s59, 7\n\ts_add_i32 s74, s74, 8192\n\tv_min_u32 v96, 9, v52\n\tv_lshl_add_u32 v96, v96, 2, s74\n\ts_lshr_b32 s75, s58, 5\n\ts_add_i32 s74, s74, s75\n\t"
            "s_cmp_eq_u32 s56, 3\n\ts_cselect_b32 s74, s74, 0x40000000\n\tv_lshl_add_u32 v98, v52, 24, s74\n\ts_cselect_b32 s75, 1, 0\n\ts_lshl2_add_u32 s74, s59, s56\n\ts_mul_i32 s74, s74, 0xa000\n\t"
            "s_add_i32 s74, s74, 155648\n\tv_lshl_add_u32 v101, v52, 4, s74\n\tv_and_b32 v60, s75, v53\n\tv_lshl_add_u32 v2, v60, 30, v101\n\ts_cmp_ge_u32 s56, s57\n\ts_addc_u32 s76, s56, 0\n\t"
            "s_and_b32 s16, s76, 3\n\ts_lshl_b32 s77, s76, 1\n\ts_max_u32 s78, s77, 1\n\ts_sub_u32 s78, s78, 1\n\ts_min_u32 s79, s77, 7\n\tv_lshlrev_b32 v60, 9, v53\n\t"
            "v_lshl_add_u32 v60, v54, 3, v60\n\ts_mul_i32 s80, s78, 0xc00\n\ts_mul_i32 s81, s79, 0xc00\n\ts_addk_i32 s80, 14336\n\ts_addk_i32 s81, 14336\n\tv_add_u32 v24, s80, v60\n\t"
            "v_add_u32 v26, s81, v60\n\tv_add_u32 v25, 38912, v24\n\tv_add_u32 v27, 38912, v26\n\tv_lshl_add_u32 v29, v52, 4, s80\n\ts_lshr_b32 s78, s78, 1\n\ts_lshr_b32 s79, s79, 1\n\t"
            "s_sub_u32 s79, s79, s78\n\ts_lshl2_add_u32 s78, s78, 159744\n\tv_and_b32 v28, s79, v52\n\tv_lshl_add_u32 v28, v28, 2, s78\n\ts_lshl2_add_u32 s78, s56, 159744\n\tv_mov_b32 v30, s78\n\t"
            "s_mov_b32 s78, 160576\n\tv_lshl_add_u32 v153, v59, 2, s78\n\ts_mul_i32 s79, s59, 163840\n\ts_lshl_b32 s80, s58, 5\n\ts_add_i32 s79, s79, s80\n\ts_cmp_eq_u32 s76, 0\n\t"
            "s_cselect_b32 s80, 0, 0xc00\n\ts_add_i32 s79, s79, s80\n\ts_add_i32 s79, s79, 155652\n\tv_lshl_add_u32 v31, v54, 4, s79\n\tv_lshrrev_b32 v60, 1, v53\n\tv_mul_u32_u24 v60, 0xa000, v60\n\t"
            "v_add_u32 v31, v60, v31\n\tv_and_b32 v60, 1, v53\n\tv_lshl_add_u32 v31, v60, 3, v31\n\ts_mov_b32 s31, 0x14000\n\ts_cmp_lt_i32 s62, 0\n\ts_cselect_b32 s79, 8, -16\n\t"
            "v_mov_b32 v61, 0xff800000\n\tv_mov_b32 v142, 1\n\tv_add_u32 v60, 0, v59\n\tv_and_b32 v60, -8, v60\n\tv_add_u32 v60, 0, v60\n\tv_cmp_gt_i32 vcc, s79, v60\n\t"
            "v_lshl_add_u32 v160, v60, 2, s78\n\ts_nop 0\n\tv_cndmask_b32 v128, 0, v61, vcc\n\tv_add_u32 v60, 4, v59\n\tv_and_b32 v60, -8, v60\n\tv_add_u32 v60, -4, v60\n\t"
            "v_cmp_gt_i32 vcc, s79, v60\n\tv_lshl_add_u32 v161, v60, 2, s78\n\ts_nop 0\n\tv_cndmask_b32 v129, 0, v61, vcc\n\tv_add_u32 v60, -1, v59\n\tv_and_b32 v60, -8, v60\n\t"
            "v_add_u32 v60, 1, v60\n\tv_cmp_gt_i32 vcc, s79, v60\n\tv_lshl_add_u32 v162, v60, 2, s78\n\ts_nop 0\n\tv_cndmask_b32 v130, 0, v61, vcc\n\tv_add_u32 v60, 3, v59\n\t"
            "v_and_b32 v60, -8, v60\n\tv_add_u32 v60, -3, v60\n\tv_cmp_gt_i32 vcc, s79, v60\n\tv_lshl_add_u32 v163, v60, 2, s78\n\ts_nop 0\n\tv_cndmask_b32 v131, 0, v61, vcc\n\t"
            "v_add_u32 v60, -2, v59\n\tv_and_b32 v60, -8, v60\n\tv_add_u32 v60, 2, v60\n\tv_cmp_gt_i32 vcc, s79, v60\n\tv_lshl_add_u32 v164, v60, 2, s78\n\ts_nop 0\n\t"
            "v_cndmask_b32 v132, 0, v61, vcc\n\tv_add_u32 v60, 2, v59\n\tv_and_b32 v60, -8, v60\n\tv_add_u32 v60, -2, v60\n\tv_cmp_gt_i32 vcc, s79, v60\n\tv_lshl_add_u32 v165, v60, 2, s78\n\t"
            "s_nop 0\n\tv_cndmask_b32 v133, 0, v61, vcc\n\tv_add_u32 v60, -3, v59\n\tv_and_b32 v60, -8, v60\n\tv_add_u32 v60, 3, v60\n\tv_cmp_gt_i32 vcc, s79, v60\n\t"
            "v_lshl_add_u32 v166, v60, 2, s78\n\ts_nop 0\n\tv_cndmask_b32 v134, 0, v61, vcc\n\tv_add_u32 v60, 1, v59\n\tv_and_b32 v60, -8, v60\n\tv_add_u32 v60, -1, v60\n\t"
            "v_cmp_gt_i32 vcc, s79, v60\n\tv_lshl_add_u32 v167, v60, 2, s78\n\ts_nop 0\n\tv_cndmask_b32 v135, 0, v61, vcc\n\ts_lshl2_add_u32 s80, s71, s56\n\ts_lshl_b32 s80, s80, 5\n\t"
            "v_add_u32 v100, s80, v52\n\tv_mul_u32_u24 v100, 0x500, v100\n\ts_add_i32 s80, s58, 3072\n\tv_add_u32 v100, s80, v100\n\tv_min_u32 v104, 51, v52\n\tv_lshlrev_b32 v104, 4, v104\n\t"
            "v_add_u32 v104, 0x1000, v104\n\ts_lshl_b32 s4, s64, 12\n\ts_add_u32 s5, s4, 0x1000\n\ts_cmpk_eq_u32 s5, 0x5000\n\ts_cselect_b32 s5, 0, s5\n\ts_add_u32 s6, s5, 0x1000\n\t"
            "s_cmpk_eq_u32 s6, 0x5000\n\ts_cselect_b32 s6, 0, s6\n\ts_add_u32 s7, s6, 0x1000\n\ts_cmpk_eq_u32 s7, 0x5000\n\ts_cselect_b32 s7, 0, s7\n\ts_add_u32 s8, s7, 0x1000\n\t"
            "s_cmpk_eq_u32 s8, 0x5000\n\ts_cselect_b32 s8, 0, s8\n\ts_cmpk_lt_u32 s58, 0x280\n\ts_cselect_b32 s80, 0, 0x5000\n\ts_xor_b32 s81, s80, 0x5000\n\ts_add_u32 s9, s4, s81\n\t"
            "s_add_u32 s4, s4, s80\n\ts_add_u32 s10, s5, s81\n\ts_add_u32 s5, s5, s80\n\ts_add_u32 s11, s6, s81\n\ts_add_u32 s6, s6, s80\n\ts_add_u32 s12, s7, s81\n\t"
            "s_add_u32 s7, s7, s80\n\ts_add_u32 s13, s8, s81\n\ts_add_u32 s8, s8, s80\n\ts_mul_i32 s20, s4, 20\n\ts_add_i32 s20, s20, s58\n\ts_addk_i32 s20, 3072\n\t"
            "s_mul_i32 s21, s5, 20\n\ts_add_i32 s21, s21, s58\n\ts_addk_i32 s21, 2048\n\ts_mul_i32 s22, s6, 20\n\ts_add_i32 s22, s22, s58\n\ts_addk_i32 s22, 1024\n\t"
            "s_mul_i32 s23, s7, 20\n\ts_add_i32 s23, s23, s58\n\ts_mul_i32 s24, s8, 20\n\ts_add_i32 s24, s24, s58\n\ts_addk_i32 s24, 3072\n\ts_mul_i32 s25, s9, 20\n\t"
            "s_add_i32 s25, s25, s58\n\ts_addk_i32 s25, 2048\n\ts_mul_i32 s26, s10, 20\n\ts_add_i32 s26, s26, s58\n\ts_addk_i32 s26, 1024\n\ts_mul_i32 s27, s11, 20\n\t"
            "s_add_i32 s27, s27, s58\n\ts_mul_i32 s28, s12, 20\n\ts_add_i32 s28, s28, s58\n\ts_addk_i32 s28, 3072\n\ts_mul_i32 s29, s13, 20\n\ts_add_i32 s29, s29, s58\n\t"
            "s_addk_i32 s29, 2048\n\ts_mul_i32 s72, s60, 1920\n\ts_sub_u32 s14, s61, s72\n\ts_mul_i32 s15, s56, 0x2800\n\ts_add_u32 s15, s15, 77824\n\tv_lshl_add_u32 v152, v52, 4, s15\n\t"
            "s_lshl_b32 s18, s58, 5\n\ts_mov_b32 s50, 822272\n\ts_mov_b32 s51, 0x20000\n\ts_waitcnt lgkmcnt(0)\n\ts_load_dword s67, s[44:45], 0x0\n\ts_sub_u32 s48, s48, 3072\n\t"
            "s_subb_u32 s49, s49, 0\n\ts_lshl_b32 s78, s56, 5\n\tv_add_u32 v12, s78, v57\n\tv_add_u32 v3, s62, v12\n\tv_and_b32 v13, 1, v53\n\tv_lshrrev_b32 v14, 1, v53\n\t"
            "v_lshl_add_u32 v15, v13, 1, v14\n\ts_add_u32 s79, s63, s58\n\tv_lshl_add_u32 v15, v15, 4, s79\n\ts_movk_i32 s79, 0xa00\n\tv_mad_i32_i24 v3, v3, s79, v15\n\tv_cmp_gt_u32 vcc, 8, v12\n\t"
            "s_cmp_eq_u32 s56, 3\n\ts_cselect_b32 s79, 2.0, 0xa000\n\tv_add_u32 v11, s79, v3\n\tv_cndmask_b32 v3, v3, 2.0, vcc\n\tv_mov_b32 v8, 159744\n\tv_and_b32 v9, 31, v52\n\t"
            "v_add_u32 v9, s78, v9\n\tv_lshl_add_u32 v9, v9, 2, v8\n\tv_lshl_add_u32 v10, v13, 4, v12\n\tv_lshl_add_u32 v10, v10, 2, v8\n\tv_mul_u32_u24 v4, 0x2800, v14\n\tv_lshl_add_u32 v4, v56, 7, v4\n\t"
            "v_lshl_add_u32 v4, v55, 3, v4\n\tv_lshl_add_u32 v4, v13, 5, v4\n\tv_bfe_u32 v5, v52, 3, 1\n\tv_lshl_add_u32 v4, v5, 6, v4\n\tv_add_u32 v4, 77824, v4\n\tv_xor_b32 v5, 64, v4\n\t"
            "v_add_u32 v6, 0x5000, v4\n\tv_add_u32 v7, 0x5000, v5\n\ts_add_u32 s78, s63, 0x500\n\ts_mov_b32 s32, s60\n\ts_mov_b32 s83, 0\n\ts_add_u32 s33, s32, 256\n\t"
            "s_cmp_eq_u32 s33, s78\n\ts_cselect_b32 s33, s63, s33\n\ts_add_u32 s34, s33, 256\n\ts_cmp_eq_u32 s34, s78\n\ts_cselect_b32 s34, s63, s34\n\ts_add_u32 s35, s34, 256\n\t"
            "s_cmp_eq_u32 s35, s78\n\ts_cselect_b32 s35, s63, s35\n\ts_add_u32 s17, s35, 256\n\ts_cmp_eq_u32 s17, s78\n\ts_cselect_b32 s17, s63, s17\n\ts_waitcnt vmcnt(0) lgkmcnt(0)\n\t"
            "s_barrier\n\tv_mov_b32 v136, -1\n\tv_mov_b32 v137, -1\n\tv_mov_b32 v138, -1\n\tv_mov_b32 v139, -1\n\ts_add_u32 s60, s33, 0\n\t"
            "s_mul_i32 s61, s60, 1920\n\ts_add_u32 m0, s66, 0\n\ts_add_u32 s61, s61, s14\n\tbuffer_load_dwordx4 v40, s[40:43], s61 offen lds\n\tbuffer_load_dwordx4 v41, s[40:43], s61 offen offset:1024 lds\n\tbuffer_load_dwordx4 v42, s[40:43], s61 offen offset:2048 lds\n\t"
            "buffer_load_dwordx4 v43, s[40:43], s61 offen offset:3072 lds\n\ts_addk_i32 m0, 0x1000\n\ts_nop 0\n\tbuffer_load_dwordx4 v44, s[40:43], s61 offen lds\n\tbuffer_load_dwordx4 v45, s[40:43], s61 offen offset:1024 lds\n\ts_add_u32 m0, s65, 0\n\t"
            "s_mov_b32 exec_hi, 0\n\tbuffer_load_dwordx4 v39, s[36:39], s60 offen offset:3072 lds\n\ts_mov_b32 exec_hi, -1\n\tbuffer_load_dwordx4 v36, s[36:39], s60 offen lds\n\tbuffer_load_dwordx4 v37, s[36:39], s60 offen offset:1024 lds\n\tbuffer_load_dwordx4 v38, s[36:39], s60 offen offset:2048 lds\n\t"
            "s_mul_i32 s67, s67, 0x19660d\n\ts_add_u32 s67, s67, 0x3c6ef35f\n\tbuffer_store_dwordx4 v[136:139], v101, s[44:47], s18 offen offset:0 sc1\n\tbuffer_store_dwordx4 v[136:139], v2, s[44:47], s18 offen offset:1024 sc1\n\tbuffer_store_dwordx4 v[136:139], v2, s[44:47], s18 offen offset:2048 sc1\n\tbuffer_store_dwordx4 v[136:139], v101, s[44:47], s18 offen offset:3072 sc1\n\t"
            "s_barrier\n\ts_add_u32 s60, s33, 128\n\ts_mul_i32 s61, s60, 1920\n\ts_add_u32 m0, s66, 38912\n\ts_add_u32 s61, s61, s14\n\tbuffer_load_dwordx4 v40, s[40:43], s61 offen lds\n\t"
            "buffer_load_dwordx4 v41, s[40:43], s61 offen offset:1024 lds\n\tbuffer_load_dwordx4 v42, s[40:43], s61 offen offset:2048 lds\n\tbuffer_load_dwordx4 v43, s[40:43], s61 offen offset:3072 lds\n\ts_addk_i32 m0, 0x1000\n\ts_nop 0\n\tbuffer_load_dwordx4 v44, s[40:43], s61 offen lds\n\t"
            "buffer_load_dwordx4 v45, s[40:43], s61 offen offset:1024 lds\n\ts_add_u32 m0, s65, 38912\n\ts_mov_b32 exec_hi, 0\n\tbuffer_load_dwordx4 v39, s[36:39], s60 offen offset:3072 lds\n\ts_mov_b32 exec_hi, -1\n\tbuffer_load_dwordx4 v36, s[36:39], s60 offen lds\n\t"
            "buffer_load_dwordx4 v37, s[36:39], s60 offen offset:1024 lds\n\tbuffer_load_dwordx4 v38, s[36:39], s60 offen offset:2048 lds\n\ts_waitcnt vmcnt(14)\n\ts_barrier\n\ts_add_u32 s60, s34, 0\n\ts_mul_i32 s61, s60, 1920\n\t"
            "s_add_u32 m0, s66, 77824\n\ts_add_u32 s61, s61, s14\n\tbuffer_load_dwordx4 v40, s[40:43], s61 offen lds\n\tbuffer_load_dwordx4 v41, s[40:43], s61 offen offset:1024 lds\n\tbuffer_load_dwordx4 v42, s[40:43], s61 offen offset:2048 lds\n\tbuffer_load_dwordx4 v43, s[40:43], s61 offen offset:3072 lds\n\t"
            "s_addk_i32 m0, 0x1000\n\ts_nop 0\n\tbuffer_load_dwordx4 v44, s[40:43], s61 offen lds\n\tbuffer_load_dwordx4 v45, s[40:43], s61 offen offset:1024 lds\n\ts_add_u32 m0, s65, 77824\n\ts_mov_b32 exec_hi, 0\n\t"
            "buffer_load_dwordx4 v39, s[36:39], s60 offen offset:3072 lds\n\ts_mov_b32 exec_hi, -1\n\tbuffer_load_dwordx4 v36, s[36:39], s60 offen lds\n\tbuffer_load_dwordx4 v37, s[36:39], s60 offen offset:1024 lds\n\tbuffer_load_dwordx4 v38, s[36:39], s60 offen offset:2048 lds\n\ts_waitcnt vmcnt(10)\n\t"
            "s_barrier\n\ts_add_u32 s60, s34, 128\n\ts_mul_i32 s61, s60, 1920\n\ts_add_u32 m0, s66, 0\n\ts_add_u32 s61, s61, s14\n\tbuffer_load_dwordx4 v40, s[40:43], s61 offen lds\n\t"
            "buffer_load_dwordx4 v41, s[40:43], s61 offen offset:1024 lds\n\tbuffer_load_dwordx4 v42, s[40:43], s61 offen offset:2048 lds\n\tbuffer_load_dwordx4 v43, s[40:43], s61 offen offset:3072 lds\n\ts_addk_i32 m0, 0x1000\n\ts_nop 0\n\tbuffer_load_dwordx4 v44, s[40:43], s61 offen lds\n\t"
            "buffer_load_dwordx4 v45, s[40:43], s61 offen offset:1024 lds\n\ts_add_u32 m0, s65, 0\n\ts_mov_b32 exec_hi, 0\n\tbuffer_load_dwordx4 v39, s[36:39], s60 offen offset:3072 lds\n\ts_mov_b32 exec_hi, -1\n\tbuffer_load_dwordx4 v36, s[36:39], s60 offen lds\n\t"
            "buffer_load_dwordx4 v37, s[36:39], s60 offen offset:1024 lds\n\tbuffer_load_dwordx4 v38, s[36:39], s60 offen offset:2048 lds\n\tv_mov_b32 v140, s67\n\tbuffer_store_dword v140, v98, s[44:47], 0 offen sc1\n\ts_waitcnt vmcnt(11)\n\ts_barrier\n\t"
            "s_add_u32 s60, s35, 0\n\ts_mul_i32 s61, s60, 1920\n\ts_add_u32 m0, s66, 38912\n\ts_add_u32 s61, s61, s14\n\tbuffer_load_dwordx4 v40, s[40:43], s61 offen lds\n\tbuffer_load_dwordx4 v41, s[40:43], s61 offen offset:1024 lds\n\t"
            "buffer_load_dwordx4 v42, s[40:43], s61 offen offset:2048 lds\n\tbuffer_load_dwordx4 v43, s[40:43], s61 offen offset:3072 lds\n\ts_addk_i32 m0, 0x1000\n\ts_nop 0\n\tbuffer_load_dwordx4 v44, s[40:43], s61 offen lds\n\tbuffer_load_dwordx4 v45, s[40:43], s61 offen offset:1024 lds\n\t"
            "s_add_u32 m0, s65, 38912\n\ts_mov_b32 exec_hi, 0\n\tbuffer_load_dwordx4 v39, s[36:39], s60 offen offset:3072 lds\n\ts_mov_b32 exec_hi, -1\n\tbuffer_load_dwordx4 v36, s[36:39], s60 offen lds\n\tbuffer_load_dwordx4 v37, s[36:39], s60 offen offset:1024 lds\n\t"
            "buffer_load_dwordx4 v38, s[36:39], s60 offen offset:2048 lds\n\ts_waitcnt vmcnt(11)\n\ts_barrier\n\ts_add_u32 s60, s35, 128\n\ts_mul_i32 s61, s60, 1920\n\ts_add_u32 m0, s66, 77824\n\t"
            "s_add_u32 s61, s61, s14\n\tbuffer_load_dwordx4 v40, s[40:43], s61 offen lds\n\tbuffer_load_dwordx4 v41, s[40:43], s61 offen offset:1024 lds\n\tbuffer_load_dwordx4 v42, s[40:43], s61 offen offset:2048 lds\n\tbuffer_load_dwordx4 v43, s[40:43], s61 offen offset:3072 lds\n\ts_addk_i32 m0, 0x1000\n\t"
            "s_nop 0\n\tbuffer_load_dwordx4 v44, s[40:43], s61 offen lds\n\tbuffer_load_dwordx4 v45, s[40:43], s61 offen offset:1024 lds\n\ts_add_u32 m0, s65, 77824\n\ts_mov_b32 exec_hi, 0\n\tbuffer_load_dwordx4 v39, s[36:39], s60 offen offset:3072 lds\n\t"
            "s_mov_b32 exec_hi, -1\n\tbuffer_load_dwordx4 v36, s[36:39], s60 offen lds\n\tbuffer_load_dwordx4 v37, s[36:39], s60 offen offset:1024 lds\n\tbuffer_load_dwordx4 v38, s[36:39], s60 offen offset:2048 lds\n\ts_waitcnt vmcnt(10)\n\ts_barrier\n\t"
            "s_cmp_lg_u32 s56, 3\n\ts_cbranch_scc1 1f\n\tbuffer_load_dwordx4 v[106:109], v158, s[44:47], 0 offen sc1\n\tbuffer_load_dwordx4 v[110:113], v159, s[44:47], 0 offen sc1\n\tbuffer_load_dwordx4 v[114:117], v104, s[44:47], 0 offen sc1\n\tbuffer_load_dword v118, v96, s[44:47], 0 offen sc1\n\t"
            "1:\ns_add_u32 s60, s17, 0\n\ts_mul_i32 s61, s60, 1920\n\ts_add_u32 m0, s66, 0\n\ts_add_u32 s61, s61, s14\n\tbuffer_load_dwordx4 v40, s[40:43], s61 offen lds\n\t"
            "buffer_load_dwordx4 v41, s[40:43], s61 offen offset:1024 lds\n\tbuffer_load_dwordx4 v42, s[40:43], s61 offen offset:2048 lds\n\tbuffer_load_dwordx4 v43, s[40:43], s61 offen offset:3072 lds\n\ts_addk_i32 m0, 0x1000\n\ts_nop 0\n\tbuffer_load_dwordx4 v44, s[40:43], s61 offen lds\n\t"
            "buffer_load_dwordx4 v45, s[40:43], s61 offen offset:1024 lds\n\ts_add_u32 m0, s65, 0\n\ts_mov_b32 exec_hi, 0\n\tbuffer_load_dwordx4 v39, s[36:39], s60 offen offset:3072 lds\n\ts_mov_b32 exec_hi, -1\n\tbuffer_load_dwordx4 v36, s[36:39], s60 offen lds\n\t"
            "buffer_load_dwordx4 v37, s[36:39], s60 offen offset:1024 lds\n\tbuffer_load_dwordx4 v38, s[36:39], s60 offen offset:2048 lds\n\ts_waitcnt vmcnt(10)\n\ts_cmp_lg_u32 s56, 3\n\ts_cbranch_scc1 2f\n\ts_mov_b32 s68, 0x200000\n\t"
            "v_xor_b32 v106, s67, v107\n\tv_xor_b32 v108, s67, v109\n\tv_xor_b32 v110, s67, v111\n\tv_xor_b32 v112, s67, v113\n\tv_xor_b32 v114, s67, v115\n\tv_xor_b32 v116, s67, v117\n\t"
            "v_xor_b32 v118, s67, v118\n\tv_or3_b32 v106, v106, v108, v110\n\tv_or3_b32 v118, v118, v114, v116\n\tv_or3_b32 v106, v106, v112, v118\n\tv_cmp_ne_u32 vcc, 0, v106\n\ts_cbranch_vccnz 30f\n\t"
            "31:\n2:\ns_waitcnt vmcnt(10)\n\ts_barrier\n\ts_add_u32 s60, s17, 128\n\ts_mul_i32 s61, s60, 1920\n\t"
            "s_add_u32 m0, s66, 38912\n\ts_add_u32 s61, s61, s14\n\tbuffer_load_dwordx4 v40, s[40:43], s61 offen lds\n\tbuffer_load_dwordx4 v41, s[40:43], s61 offen offset:1024 lds\n\tbuffer_load_dwordx4 v42, s[40:43], s61 offen offset:2048 lds\n\tbuffer_load_dwordx4 v43, s[40:43], s61 offen offset:3072 lds\n\t"
            "s_addk_i32 m0, 0x1000\n\ts_nop 0\n\tbuffer_load_dwordx4 v44, s[40:43], s61 offen lds\n\tbuffer_load_dwordx4 v45, s[40:43], s61 offen offset:1024 lds\n\ts_add_u32 m0, s65, 38912\n\ts_mov_b32 exec_hi, 0\n\t"
            "buffer_load_dwordx4 v39, s[36:39], s60 offen offset:3072 lds\n\ts_mov_b32 exec_hi, -1\n\tbuffer_load_dwordx4 v36, s[36:39], s60 offen lds\n\tbuffer_load_dwordx4 v37, s[36:39], s60 offen offset:1024 lds\n\tbuffer_load_dwordx4 v38, s[36:39], s60 offen offset:2048 lds\n\ts_mov_b32 m0, s30\n\t"
            "s_nop 0\n\tbuffer_load_dword v97, s[44:47], 0 offen sc1 lds\n\ts_waitcnt vmcnt(11)\n\ts_barrier\n\ts_add_u32 m0, s15, 40960\n\ts_nop 0\n\t"
            "buffer_load_dwordx4 v156, s[48:51], s20 offen lds\n\tbuffer_load_dwordx4 v156, s[48:51], s21 offen offset:1024 lds\n\tbuffer_load_dwordx4 v156, s[48:51], s22 offen offset:2048 lds\n\tbuffer_load_dwordx4 v156, s[48:51], s23 offen offset:3072 lds\n\ts_add_u32 m0, s15, 45056\n\ts_nop 0\n\t"
            "buffer_load_dwordx4 v156, s[48:51], s24 offen lds\n\tbuffer_load_dwordx4 v156, s[48:51], s25 offen offset:1024 lds\n\tbuffer_load_dwordx4 v156, s[48:51], s26 offen offset:2048 lds\n\ts_waitcnt vmcnt(7)\n\ts_barrier\n\ts_add_u32 m0, s15, 45056\n\t"
            "s_nop 0\n\tbuffer_load_dwordx4 v156, s[48:51], s27 offen offset:3072 lds\n\ts_add_u32 m0, s15, 49152\n\ts_nop 0\n\tbuffer_load_dwordx4 v156, s[48:51], s28 offen lds\n\tbuffer_load_dwordx4 v156, s[48:51], s29 offen offset:1024 lds\n\t"
            "s_add_u32 m0, s15, 0\n\ts_nop 0\n\tbuffer_load_dwordx4 v154, s[48:51], s20 offen lds\n\tbuffer_load_dwordx4 v154, s[48:51], s21 offen offset:1024 lds\n\tbuffer_load_dwordx4 v154, s[48:51], s22 offen offset:2048 lds\n\tbuffer_load_dwordx4 v154, s[48:51], s23 offen offset:3072 lds\n\t"
            "s_add_u32 m0, s15, 4096\n\ts_nop 0\n\tbuffer_load_dwordx4 v154, s[48:51], s24 offen lds\n\tbuffer_load_dwordx4 v154, s[48:51], s25 offen offset:1024 lds\n\tbuffer_load_dwordx4 v154, s[48:51], s26 offen offset:2048 lds\n\tbuffer_load_dwordx4 v154, s[48:51], s27 offen offset:3072 lds\n\t"
            "s_add_u32 m0, s15, 8192\n\ts_nop 0\n\tbuffer_load_dwordx4 v154, s[48:51], s28 offen lds\n\tbuffer_load_dwordx4 v154, s[48:51], s29 offen offset:1024 lds\n\ts_cmp_lg_u32 s56, 0\n\ts_cbranch_scc1 1f\n\t"
            "s_mov_b32 m0, 162624\n\ts_nop 0\n\tbuffer_load_dwordx4 v104, s[44:47], 0 offen sc1 lds\n\t1:\nbuffer_load_dwordx4 v[240:243], v3, s[36:39], 0 offen offset:0\n\tbuffer_load_dwordx4 v[244:247], v11, s[36:39], 0 offen offset:0\n\t"
            "buffer_load_dwordx4 v[248:251], v3, s[36:39], 0 offen offset:64\n\tbuffer_load_dwordx4 v[252:255], v11, s[36:39], 0 offen offset:64\n\tds_read_b32 v20, v9 offset:256\n\tds_read_b32 v21, v10 offset:832\n\tds_read2st64_b32 v[138:139], v153 offset1:1\n\tds_read2st64_b32 v[176:177], v160 offset1:1\n\t"
            "ds_read2st64_b32 v[178:179], v161 offset1:1\n\tds_read2st64_b32 v[180:181], v162 offset1:1\n\tds_read2st64_b32 v[182:183], v163 offset1:1\n\tds_read2st64_b32 v[184:185], v164 offset1:1\n\tds_read2st64_b32 v[186:187], v165 offset1:1\n\tds_read2st64_b32 v[188:189], v166 offset1:1\n\t"
            "ds_read2st64_b32 v[190:191], v167 offset1:1\n\ts_mov_b32 s68, 0x200000\n\ts_waitcnt lgkmcnt(8)\n\tv_mul_f32 v138, 0x3ea52b5c, v138\n\tv_mul_f32 v139, 0x3ea52b5c, v139\n\ts_waitcnt lgkmcnt(7)\n\t"
            "v_mul_f32 v192, v176, v138\n\tv_mul_f32 v200, v177, v139\n\ts_waitcnt lgkmcnt(6)\n\tv_mul_f32 v193, v178, v138\n\tv_mul_f32 v201, v179, v139\n\ts_waitcnt lgkmcnt(5)\n\t"
            "v_mul_f32 v194, v180, v138\n\tv_mul_f32 v202, v181, v139\n\ts_waitcnt lgkmcnt(4)\n\tv_mul_f32 v195, v182, v138\n\tv_mul_f32 v203, v183, v139\n\ts_waitcnt lgkmcnt(3)\n\t"
            "v_mul_f32 v196, v184, v138\n\tv_mul_f32 v204, v185, v139\n\ts_waitcnt lgkmcnt(2)\n\tv_mul_f32 v197, v186, v138\n\tv_mul_f32 v205, v187, v139\n\ts_waitcnt lgkmcnt(1)\n\t"
            "v_mul_f32 v198, v188, v138\n\tv_mul_f32 v206, v189, v139\n\ts_waitcnt lgkmcnt(0)\n\tv_mul_f32 v199, v190, v138\n\tv_mul_f32 v207, v191, v139\n\ts_barrier\n\t"
            SB_(1) PRIS
            "50:\nds_read2_b64 v[208:211], v24 offset0:0 offset1:16\n\tds_read2_b64 v[212:215], v24 offset0:32 offset1:48\n\tds_read2_b64 v[216:219], v26 offset0:0 offset1:16\n\tds_read2_b64 v[220:223], v26 offset0:32 offset1:48\n\ts_cmp_eq_u32 s16, 0\n\t"
            "s_cbranch_scc1 56f\n\t58:\ns_cbranch_scc0 1f\n\tbuffer_load_dword v232, v31, s[44:47], s31 offen offset:0 sc1\n\tbuffer_load_dword v233, v31, s[44:47], s31 offen offset:256 sc1\n\tbuffer_load_dword v234, v31, s[44:47], s31 offen offset:512 sc1\n\t"
            "buffer_load_dword v235, v31, s[44:47], s31 offen offset:768 sc1\n\t1:\ns_waitcnt lgkmcnt(1)\n\tv_add_f32 v208, v208, v216\n\tv_add_f32 v209, v209, v217\n\tv_add_f32 v210, v210, v218\n\t"
            "v_add_f32 v211, v211, v219\n\ts_waitcnt lgkmcnt(0)\n\tv_add_f32 v212, v212, v220\n\tv_add_f32 v213, v213, v221\n\tv_add_f32 v214, v214, v222\n\tv_add_f32 v215, v215, v223\n\t"
            "v_fma_f32 v208, v208, v192, v128\n\tv_fma_f32 v209, v209, v193, v129\n\tv_fma_f32 v210, v210, v194, v130\n\tv_fma_f32 v211, v211, v195, v131\n\tv_fma_f32 v212, v212, v196, v132\n\tv_fma_f32 v213, v213, v197, v133\n\t"
            "v_fma_f32 v214, v214, v198, v134\n\tv_fma_f32 v215, v215, v199, v135\n\tv_max3_f32 v136, v208, v209, v210\n\tv_max3_f32 v137, v211, v212, v213\n\tv_max3_f32 v136, v136, v214, v215\n\tv_max_f32 v136, v136, v137\n\t"
            "v_sub_f32 v208, v208, v136\n\tv_sub_f32 v209, v209, v136\n\tv_sub_f32 v210, v210, v136\n\tv_sub_f32 v211, v211, v136\n\tv_sub_f32 v212, v212, v136\n\tv_sub_f32 v213, v213, v136\n\t"
            "v_sub_f32 v214, v214, v136\n\tv_sub_f32 v215, v215, v136\n\tv_exp_f32 v208, v208\n\tv_exp_f32 v209, v209\n\tv_exp_f32 v210, v210\n\tv_exp_f32 v211, v211\n\t"
            "v_exp_f32 v212, v212\n\tv_exp_f32 v213, v213\n\tv_exp_f32 v214, v214\n\tv_exp_f32 v215, v215\n\tv_add_f32 v216, v208, v209\n\tv_add_f32 v217, v210, v211\n\t"
            "v_add_f32 v218, v212, v213\n\tv_add_f32 v219, v214, v215\n\tv_add_f32 v216, v216, v217\n\tv_add_f32 v218, v218, v219\n\tv_mul_f32 v208, v208, v176\n\tv_add_f32 v216, v216, v218\n\t"
            "v_mul_f32 v216, 0xbfb8aa3b, v216\n\tv_rcp_f32 v216, v216\n\tv_mul_f32 v209, v209, v178\n\tv_mul_f32 v210, v210, v180\n\tv_mul_f32 v211, v211, v182\n\tv_mul_f32 v212, v212, v184\n\t"
            "v_mul_f32 v213, v213, v186\n\tv_mul_f32 v214, v214, v188\n\tv_mul_f32 v215, v215, v190\n\tv_mul_f32 v208, v208, v216\n\tv_mul_f32 v209, v209, v216\n\tv_mul_f32 v210, v210, v216\n\t"
            "v_mul_f32 v211, v211, v216\n\tv_mul_f32 v212, v212, v216\n\tv_mul_f32 v213, v213, v216\n\tv_mul_f32 v214, v214, v216\n\tv_mul_f32 v215, v215, v216\n\tv_cvt_pk_bf16_f32 v220, v208, v209\n\t"
            "v_cvt_pk_bf16_f32 v221, v210, v211\n\tv_cvt_pk_bf16_f32 v222, v212, v213\n\tv_cvt_pk_bf16_f32 v223, v214, v215\n\tds_write_b128 v29, v[220:223] offset:0\n\tds_write_b32 v30, v142 offset:16\n\t"
            SP0 PRIW
            "51:\nds_read_b32 v136, v28\n\tds_read2_b64 v[208:211], v25 offset0:0 offset1:16\n\tds_read2_b64 v[212:215], v25 offset0:32 offset1:48\n\tds_read2_b64 v[216:219], v27 offset0:0 offset1:16\n\tds_read2_b64 v[220:223], v27 offset0:32 offset1:48\n\t"
            "s_waitcnt lgkmcnt(4)\n\tv_cmp_eq_u32 vcc, 0, v136\n\ts_cbranch_vccnz 53f\n\t55:\n"
            PRI1
            "s_cmp_eq_u32 s16, 0\n\ts_cbranch_scc1 57f\n\t59:\ns_waitcnt lgkmcnt(1)\n\tv_add_f32 v208, v208, v216\n\tv_add_f32 v209, v209, v217\n\t"
            "v_add_f32 v210, v210, v218\n\tv_add_f32 v211, v211, v219\n\ts_waitcnt lgkmcnt(0)\n\tv_add_f32 v212, v212, v220\n\tv_add_f32 v213, v213, v221\n\tv_add_f32 v214, v214, v222\n\t"
            "v_add_f32 v215, v215, v223\n\tv_mul_f32 v208, v208, v200\n\tv_mul_f32 v209, v209, v201\n\tv_mul_f32 v210, v210, v202\n\tv_mul_f32 v211, v211, v203\n\tv_mul_f32 v212, v212, v204\n\t"
            "v_mul_f32 v213, v213, v205\n\tv_mul_f32 v214, v214, v206\n\tv_mul_f32 v215, v215, v207\n\tv_max3_f32 v136, v208, v209, v210\n\tv_max3_f32 v137, v211, v212, v213\n\tv_max3_f32 v136, v136, v214, v215\n\t"
            "v_max_f32 v136, v136, v137\n\tv_sub_f32 v208, v208, v136\n\tv_sub_f32 v209, v209, v136\n\tv_sub_f32 v210, v210, v136\n\tv_sub_f32 v211, v211, v136\n\tv_sub_f32 v212, v212, v136\n\t"
            "v_sub_f32 v213, v213, v136\n\tv_sub_f32 v214, v214, v136\n\tv_sub_f32 v215, v215, v136\n\tv_exp_f32 v208, v208\n\tv_exp_f32 v209, v209\n\tv_exp_f32 v210, v210\n\t"
            "v_exp_f32 v211, v211\n\tv_exp_f32 v212, v212\n\tv_exp_f32 v213, v213\n\tv_exp_f32 v214, v214\n\tv_exp_f32 v215, v215\n\tv_add_f32 v216, v208, v209\n\t"
            "v_add_f32 v217, v210, v211\n\tv_add_f32 v218, v212, v213\n\tv_add_f32 v219, v214, v215\n\tv_add_f32 v216, v216, v217\n\tv_add_f32 v218, v218, v219\n\tv_mul_f32 v208, v208, v177\n\t"
            "v_add_f32 v216, v216, v218\n\tv_mul_f32 v216, 0xbfb8aa3b, v216\n\tv_rcp_f32 v216, v216\n\tv_mul_f32 v209, v209, v179\n\tv_mul_f32 v210, v210, v181\n\tv_mul_f32 v211, v211, v183\n\t"
            "v_mul_f32 v212, v212, v185\n\tv_mul_f32 v213, v213, v187\n\tv_mul_f32 v214, v214, v189\n\tv_mul_f32 v215, v215, v191\n\tv_mul_f32 v208, v208, v216\n\tv_mul_f32 v209, v209, v216\n\t"
            "v_mul_f32 v210, v210, v216\n\tv_mul_f32 v211, v211, v216\n\tv_mul_f32 v212, v212, v216\n\tv_mul_f32 v213, v213, v216\n\tv_mul_f32 v214, v214, v216\n\tv_mul_f32 v215, v215, v216\n\t"
            "v_cvt_pk_bf16_f32 v220, v208, v209\n\tv_cvt_pk_bf16_f32 v221, v210, v211\n\tv_cvt_pk_bf16_f32 v222, v212, v213\n\tv_cvt_pk_bf16_f32 v223, v214, v215\n\tds_write_b128 v29, v[220:223] offset:38912\n\tds_write_b32 v30, v30 offset:16\n\t"
            SB_(2) PRIG
            "s_waitcnt vmcnt(0)\n\tds_write_b32 v30, v30 offset:48\n\ts_branch .LOL\n\t30:\nbuffer_load_dwordx4 v[106:109], v158, s[44:47], 0 offen sc1\n\tbuffer_load_dwordx4 v[110:113], v159, s[44:47], 0 offen sc1\n\t"
            "buffer_load_dwordx4 v[114:117], v104, s[44:47], 0 offen sc1\n\tbuffer_load_dword v118, v96, s[44:47], 0 offen sc1\n\t"
            GINC
            "s_waitcnt vmcnt(3)\n\tv_xor_b32 v106, s67, v107\n\tv_xor_b32 v108, s67, v109\n\ts_waitcnt vmcnt(2)\n\tv_xor_b32 v110, s67, v111\n\tv_xor_b32 v112, s67, v113\n\t"
            "s_waitcnt vmcnt(1)\n\tv_xor_b32 v114, s67, v115\n\tv_xor_b32 v116, s67, v117\n\ts_waitcnt vmcnt(0)\n\tv_xor_b32 v118, s67, v118\n\tv_or3_b32 v106, v106, v108, v110\n\t"
            "v_or3_b32 v118, v118, v114, v116\n\tv_or3_b32 v106, v106, v112, v118\n\tv_cmp_ne_u32 vcc, 0, v106\n\ts_cbranch_vccz 31b\n\ts_sub_u32 s68, s68, 1\n\ts_cbranch_scc0 30b\n\t"
            "s_branch 31b\n\t53:\n"
            PSLP
            "s_sub_u32 s68, s68, 1\n\ts_cbranch_scc0 51b\n\ts_branch 55b\n\t56:\n"
            RSL0
            "buffer_load_dword v224, v31, s[44:47], 0 offen offset:0 sc1\n\tbuffer_load_dword v225, v31, s[44:47], 0 offen offset:256 sc1\n\tbuffer_load_dword v226, v31, s[44:47], 0 offen offset:512 sc1\n\tbuffer_load_dword v227, v31, s[44:47], 0 offen offset:768 sc1\n\ts_waitcnt vmcnt(4)\n\t"
            PRIR
            "s_call_b64 s[76:77], .LG0\n\t"
            PRIX
            "s_branch 2f\n\t1:\ns_waitcnt vmcnt(3)\n\tbuffer_load_dword v224, v31, s[44:47], 0 offen offset:0 sc1\n\ts_waitcnt vmcnt(3)\n\tbuffer_load_dword v225, v31, s[44:47], 0 offen offset:256 sc1\n\t"
            "s_waitcnt vmcnt(3)\n\tbuffer_load_dword v226, v31, s[44:47], 0 offen offset:512 sc1\n\ts_waitcnt vmcnt(3)\n\tbuffer_load_dword v227, v31, s[44:47], 0 offen offset:768 sc1\n\ts_cmp_lg_u32 s83, 0\n\ts_cbranch_scc1 3f\n\t"
            "s_call_b64 s[76:77], .LG1\n\t3:\ns_cmp_eq_u32 s83, 2\n\ts_cbranch_scc1 2f\n\ts_call_b64 s[76:77], .LG2\n\t2:\n"
            "s_waitcnt vmcnt(0) lgkmcnt(0)\n\t4:\nds_write_b32 v30, v30 offset:48\n\tv_max3_u32 v136, v224, v225, v226\n\tv_max_u32 v136, v136, v227\n\tv_cmp_eq_u32 vcc, -1, v136\n\t"
            "v_lshlrev_b32 v216, 16, v224\n\tv_and_b32 v217, 0xffff0000, v224\n\tv_lshlrev_b32 v218, 16, v225\n\tv_and_b32 v219, 0xffff0000, v225\n\tv_lshlrev_b32 v220, 16, v226\n\tv_and_b32 v221, 0xffff0000, v226\n\t"
            "v_lshlrev_b32 v222, 16, v227\n\tv_and_b32 v223, 0xffff0000, v227\n\t"
            LINC
            "s_cmp_eq_u32 0, 0\n\ts_cbranch_vccz 58b\n\ts_sub_u32 s68, s68, 1\n\ts_cbranch_scc0 1b\n\ts_branch 58b\n\t57:\n"
            "s_branch 2f\n\t1:\nbuffer_load_dword v232, v31, s[44:47], s31 offen offset:0 sc1\n\tbuffer_load_dword v233, v31, s[44:47], s31 offen offset:256 sc1\n\tbuffer_load_dword v234, v31, s[44:47], s31 offen offset:512 sc1\n\tbuffer_load_dword v235, v31, s[44:47], s31 offen offset:768 sc1\n\t"
            "s_cmp_lg_u32 s83, 0\n\ts_cbranch_scc1 3f\n\ts_call_b64 s[76:77], .LG1\n\t3:\ns_cmp_eq_u32 s83, 2\n\ts_cbranch_scc1 2f\n\t"
            "s_call_b64 s[76:77], .LG2\n\t2:\ns_waitcnt vmcnt(0) lgkmcnt(0)\n\t4:\nv_max3_u32 v136, v232, v233, v234\n\tv_max_u32 v136, v136, v235\n\t"
            "v_cmp_eq_u32 vcc, -1, v136\n\tv_lshlrev_b32 v216, 16, v232\n\tv_and_b32 v217, 0xffff0000, v232\n\tv_lshlrev_b32 v218, 16, v233\n\tv_and_b32 v219, 0xffff0000, v233\n\tv_lshlrev_b32 v220, 16, v234\n\t"
            "v_and_b32 v221, 0xffff0000, v234\n\tv_lshlrev_b32 v222, 16, v235\n\tv_and_b32 v223, 0xffff0000, v235\n\t"
            LINC
            "s_cmp_eq_u32 0, 0\n\ts_cbranch_vccz 59b\n\ts_sub_u32 s68, s68, 1\n\ts_cbranch_scc0 1b\n\ts_branch 59b\n\t.LOL:\n"
            "s_lshr_b32 s71, s56, 1\n\ts_mov_b32 s68, 0x200000\n\ts_cmp_eq_u32 s16, 0\n\ts_cbranch_scc1 3f\n\ts_call_b64 s[76:77], .LG0\n\t3:\n"
            "ds_read_b64 v[14:15], v8 offset:32\n\ts_waitcnt lgkmcnt(0)\n\tv_min_u32 v12, v14, v15\n\tv_cmp_lt_u32 vcc, s71, v12\n\ts_cbranch_vccnz 4f\n\ts_sleep 1\n\t"
            "s_sub_u32 s68, s68, 1\n\ts_cbranch_scc0 3b\n\t4:\ns_cmp_eq_u32 s16, 0\n\ts_cbranch_scc1 1f\n\ts_call_b64 s[76:77], .LG1\n\t"
            "1:\ns_cmp_lt_u32 s56, 2\n\ts_cbranch_scc1 1f\n\ts_sleep 3\n\t1:\n"
            SO(4) RZ
            "buffer_load_dwordx4 v[176:179], v2, s[44:47], s4 offen offset:1024 sc1\n\tbuffer_load_dwordx4 v[180:183], v2, s[44:47], s4 offen offset:2048 sc1\n\tbuffer_load_dwordx4 v[184:187], v2, s[44:47], s5 offen offset:1024 sc1\n\tbuffer_load_dwordx4 v[188:191], v2, s[44:47], s5 offen offset:2048 sc1\n\tbuffer_load_dwordx4 v[192:195], v2, s[44:47], s6 offen offset:1024 sc1\n\tbuffer_load_dwordx4 v[196:199], v2, s[44:47], s6 offen offset:2048 sc1\n\t"
            "buffer_load_dwordx4 v[200:203], v2, s[44:47], s7 offen offset:1024 sc1\n\tbuffer_load_dwordx4 v[204:207], v2, s[44:47], s7 offen offset:2048 sc1\n\tbuffer_load_dwordx4 v[208:211], v2, s[44:47], s8 offen offset:1024 sc1\n\tbuffer_load_dwordx4 v[212:215], v2, s[44:47], s8 offen offset:2048 sc1\n\ts_cmp_lg_u32 s83, 0\n\ts_cbranch_scc1 1f\n\t"
            "s_call_b64 s[76:77], .LG1\n\t1:\ns_cmp_eq_u32 s83, 2\n\ts_cbranch_scc1 1f\n\ts_call_b64 s[76:77], .LG2\n\t1:\n"
            "ds_read_b128 v[16:19], v8 offset:48\n\tds_read_b64_tr_b16 v[128:129], v4 offset:40960\n\tds_read_b64_tr_b16 v[130:131], v4 offset:41472\n\tds_read_b64_tr_b16 v[132:133], v5 offset:40960\n\tds_read_b64_tr_b16 v[134:135], v5 offset:41472\n\tds_read_b64_tr_b16 v[136:137], v6 offset:40960\n\t"
            "ds_read_b64_tr_b16 v[138:139], v6 offset:41472\n\tds_read_b64_tr_b16 v[140:141], v7 offset:40960\n\tds_read_b64_tr_b16 v[142:143], v7 offset:41472\n\tds_read_b64_tr_b16 v[144:145], v4 offset:41984\n\tds_read_b64_tr_b16 v[146:147], v4 offset:42496\n\tds_read_b64_tr_b16 v[148:149], v5 offset:41984\n\t"
            "ds_read_b64_tr_b16 v[150:151], v5 offset:42496\n\tds_read_b64_tr_b16 v[152:153], v6 offset:41984\n\tds_read_b64_tr_b16 v[154:155], v6 offset:42496\n\tds_read_b64_tr_b16 v[156:157], v7 offset:41984\n\tds_read_b64_tr_b16 v[158:159], v7 offset:42496\n\tv_mul_f32 v48, v48, v112\n\t"
            "v_mul_f32 v49, v49, v113\n\tv_mul_f32 v50, v50, v114\n\tv_mul_f32 v51, v51, v115\n\tv_mul_f32 v52, v52, v116\n\tv_mul_f32 v53, v53, v117\n\tv_mul_f32 v54, v54, v118\n\t"
            "v_mul_f32 v55, v55, v119\n\tv_mul_f32 v56, v56, v120\n\tv_mul_f32 v57, v57, v121\n\tv_mul_f32 v58, v58, v122\n\tv_mul_f32 v59, v59, v123\n\tv_mul_f32 v60, v60, v124\n\t"
            "v_mul_f32 v61, v61, v125\n\tv_mul_f32 v62, v62, v126\n\tv_mul_f32 v63, v63, v127\n\t"
            SO(3)
            "s_waitcnt lgkmcnt(15)\n\tv_min3_u32 v16, v16, v17, v18\n\tv_min_u32 v16, v16, v19\n\tv_cmp_eq_u32 vcc, 0, v16\n\ts_cbranch_vccnz 62f\n\t63:\n"
            PRIO
            "s_waitcnt vmcnt(8)\n\tv_max3_u32 v12, v176, v178, v180\n\tv_max_u32 v12, v12, v182\n\tv_cmp_eq_u32 vcc, -1, v12\n\ts_cbranch_vccnz 100f\n\t200:\n"
            SO(5)
            "s_waitcnt lgkmcnt(8)\n\tv_mfma_f32_32x32x16_bf16 v[96:111], v[128:131], v[176:179], 0\n\tds_read_b64_tr_b16 v[160:161], v4 offset:43008\n\tds_read_b64_tr_b16 v[162:163], v4 offset:43520\n\tbuffer_load_dwordx4 v[216:219], v2, s[44:47], s9 offen offset:1024 sc1\n\tv_mfma_f32_32x32x16_bf16 v[112:127], v[132:135], v[176:179], 0\n\t"
            "ds_read_b64_tr_b16 v[164:165], v5 offset:43008\n\tds_read_b64_tr_b16 v[166:167], v5 offset:43520\n\tbuffer_load_dwordx4 v[220:223], v2, s[44:47], s9 offen offset:2048 sc1\n\tv_mfma_f32_32x32x16_bf16 v[96:111], v[136:139], v[180:183], v[96:111]\n\tds_read_b64_tr_b16 v[168:169], v6 offset:43008\n\tds_read_b64_tr_b16 v[170:171], v6 offset:43520\n\t"
            "s_waitcnt vmcnt(8)\n\tv_max3_u32 v12, v184, v186, v188\n\tv_max_u32 v12, v12, v190\n\tv_cmp_eq_u32 vcc, -1, v12\n\tv_mfma_f32_32x32x16_bf16 v[112:127], v[140:143], v[180:183], v[112:127]\n\tds_read_b64_tr_b16 v[172:173], v7 offset:43008\n\t"
            "ds_read_b64_tr_b16 v[174:175], v7 offset:43520\n\ts_cbranch_vccnz 101f\n\t201:\ns_waitcnt lgkmcnt(8)\n\tv_mfma_f32_32x32x16_bf16 v[96:111], v[144:147], v[184:187], v[96:111]\n\tds_read_b64_tr_b16 v[128:129], v4 offset:44032\n\t"
            "ds_read_b64_tr_b16 v[130:131], v4 offset:44544\n\tbuffer_load_dwordx4 v[224:227], v2, s[44:47], s10 offen offset:1024 sc1\n\tv_mfma_f32_32x32x16_bf16 v[112:127], v[148:151], v[184:187], v[112:127]\n\tds_read_b64_tr_b16 v[132:133], v5 offset:44032\n\tds_read_b64_tr_b16 v[134:135], v5 offset:44544\n\tbuffer_load_dwordx4 v[228:231], v2, s[44:47], s10 offen offset:2048 sc1\n\t"
            "v_mfma_f32_32x32x16_bf16 v[96:111], v[152:155], v[188:191], v[96:111]\n\tds_read_b64_tr_b16 v[136:137], v6 offset:44032\n\tds_read_b64_tr_b16 v[138:139], v6 offset:44544\n\ts_waitcnt vmcnt(8)\n\tv_max3_u32 v12, v192, v194, v196\n\tv_max_u32 v12, v12, v198\n\t"
            "v_cmp_eq_u32 vcc, -1, v12\n\tv_mfma_f32_32x32x16_bf16 v[112:127], v[156:159], v[188:191], v[112:127]\n\tds_read_b64_tr_b16 v[140:141], v7 offset:44032\n\tds_read_b64_tr_b16 v[142:143], v7 offset:44544\n\ts_cbranch_vccnz 102f\n\t202:\n"
            "s_waitcnt lgkmcnt(8)\n\tv_mfma_f32_32x32x16_bf16 v[96:111], v[160:163], v[192:195], v[96:111]\n\tds_read_b64_tr_b16 v[144:145], v4 offset:45056\n\tds_read_b64_tr_b16 v[146:147], v4 offset:45568\n\tbuffer_load_dwordx4 v[232:235], v2, s[44:47], s11 offen offset:1024 sc1\n\tv_mfma_f32_32x32x16_bf16 v[112:127], v[164:167], v[192:195], v[112:127]\n\t"
            "ds_read_b64_tr_b16 v[148:149], v5 offset:45056\n\tds_read_b64_tr_b16 v[150:151], v5 offset:45568\n\tbuffer_load_dwordx4 v[236:239], v2, s[44:47], s11 offen offset:2048 sc1\n\tv_mfma_f32_32x32x16_bf16 v[96:111], v[168:171], v[196:199], v[96:111]\n\tds_read_b64_tr_b16 v[152:153], v6 offset:45056\n\tds_read_b64_tr_b16 v[154:155], v6 offset:45568\n\t"
            "s_waitcnt vmcnt(8)\n\tv_max3_u32 v12, v200, v202, v204\n\tv_max_u32 v12, v12, v206\n\tv_cmp_eq_u32 vcc, -1, v12\n\tv_mfma_f32_32x32x16_bf16 v[112:127], v[172:175], v[196:199], v[112:127]\n\tds_read_b64_tr_b16 v[156:157], v7 offset:45056\n\t"
            "ds_read_b64_tr_b16 v[158:159], v7 offset:45568\n\ts_cbranch_vccnz 103f\n\t203:\ns_waitcnt lgkmcnt(8)\n\tv_mfma_f32_32x32x16_bf16 v[96:111], v[128:131], v[200:203], v[96:111]\n\tds_read_b64_tr_b16 v[160:161], v4 offset:46080\n\t"
            "ds_read_b64_tr_b16 v[162:163], v4 offset:46592\n\tbuffer_load_dwordx4 v[176:179], v2, s[44:47], s12 offen offset:1024 sc1\n\tv_mfma_f32_32x32x16_bf16 v[112:127], v[132:135], v[200:203], v[112:127]\n\tds_read_b64_tr_b16 v[164:165], v5 offset:46080\n\tds_read_b64_tr_b16 v[166:167], v5 offset:46592\n\tds_read_b64 v[14:15], v8 offset:40\n\t"
            "buffer_load_dwordx4 v[180:183], v2, s[44:47], s12 offen offset:2048 sc1\n\tv_mfma_f32_32x32x16_bf16 v[96:111], v[136:139], v[204:207], v[96:111]\n\tds_read_b64_tr_b16 v[168:169], v6 offset:46080\n\tds_read_b64_tr_b16 v[170:171], v6 offset:46592\n\ts_waitcnt vmcnt(8)\n\tv_max3_u32 v12, v208, v210, v212\n\t"
            "v_max_u32 v12, v12, v214\n\tv_cmp_eq_u32 vcc, -1, v12\n\tv_mfma_f32_32x32x16_bf16 v[112:127], v[140:143], v[204:207], v[112:127]\n\tds_read_b64_tr_b16 v[172:173], v7 offset:46080\n\tds_read_b64_tr_b16 v[174:175], v7 offset:46592\n\ts_cbranch_vccnz 104f\n\t"
            "204:\ns_waitcnt lgkmcnt(9)\n\tv_mfma_f32_32x32x16_bf16 v[96:111], v[144:147], v[208:211], v[96:111]\n\tds_read_b64_tr_b16 v[128:129], v4 offset:47104\n\tds_read_b64_tr_b16 v[130:131], v4 offset:47616\n\tbuffer_load_dwordx4 v[184:187], v2, s[44:47], s13 offen offset:1024 sc1\n\t"
            "v_mfma_f32_32x32x16_bf16 v[112:127], v[148:151], v[208:211], v[112:127]\n\tds_read_b64_tr_b16 v[132:133], v5 offset:47104\n\tds_read_b64_tr_b16 v[134:135], v5 offset:47616\n\tbuffer_load_dwordx4 v[188:191], v2, s[44:47], s13 offen offset:2048 sc1\n\tv_mfma_f32_32x32x16_bf16 v[96:111], v[152:155], v[212:215], v[96:111]\n\tds_read_b64_tr_b16 v[136:137], v6 offset:47104\n\t"
            "ds_read_b64_tr_b16 v[138:139], v6 offset:47616\n\ts_waitcnt vmcnt(8)\n\tv_max3_u32 v12, v216, v218, v220\n\tv_max_u32 v12, v12, v222\n\tv_cmp_eq_u32 vcc, -1, v12\n\tv_mfma_f32_32x32x16_bf16 v[112:127], v[156:159], v[212:215], v[112:127]\n\t"
            "ds_read_b64_tr_b16 v[140:141], v7 offset:47104\n\tds_read_b64_tr_b16 v[142:143], v7 offset:47616\n\ts_cbranch_vccnz 105f\n\t205:\ns_waitcnt lgkmcnt(8)\n\tv_mfma_f32_32x32x16_bf16 v[96:111], v[160:163], v[216:219], v[96:111]\n\t"
            "v_min_u32 v14, v14, v15\n\tv_cmp_ge_u32 vcc, s71, v14\n\tds_read_b64_tr_b16 v[144:145], v4 offset:48128\n\tds_read_b64_tr_b16 v[146:147], v4 offset:48640\n\tv_mfma_f32_32x32x16_bf16 v[112:127], v[164:167], v[216:219], v[112:127]\n\ts_cbranch_vccnz 5f\n\t"
            "6:\nds_read_b64_tr_b16 v[148:149], v5 offset:48128\n\tds_read_b64_tr_b16 v[150:151], v5 offset:48640\n\tbuffer_load_dwordx4 v[192:195], v2, s[44:47], s4 offen offset:0 sc1\n\tbuffer_load_dwordx4 v[196:199], v2, s[44:47], s4 offen offset:3072 sc1\n\tv_mfma_f32_32x32x16_bf16 v[96:111], v[168:171], v[220:223], v[96:111]\n\t"
            "ds_read_b64_tr_b16 v[152:153], v6 offset:48128\n\tds_read_b64_tr_b16 v[154:155], v6 offset:48640\n\ts_waitcnt vmcnt(8)\n\tv_max3_u32 v12, v224, v226, v228\n\tv_max_u32 v12, v12, v230\n\tv_cmp_eq_u32 vcc, -1, v12\n\t"
            "v_mfma_f32_32x32x16_bf16 v[112:127], v[172:175], v[220:223], v[112:127]\n\tds_read_b64_tr_b16 v[156:157], v7 offset:48128\n\tds_read_b64_tr_b16 v[158:159], v7 offset:48640\n\ts_cbranch_vccnz 106f\n\t206:\ns_waitcnt lgkmcnt(8)\n\t"
            "v_mfma_f32_32x32x16_bf16 v[96:111], v[128:131], v[224:227], v[96:111]\n\tds_read_b64_tr_b16 v[160:161], v4 offset:49152\n\tds_read_b64_tr_b16 v[162:163], v4 offset:49664\n\tbuffer_load_dwordx4 v[200:203], v2, s[44:47], s5 offen offset:0 sc1\n\tv_mfma_f32_32x32x16_bf16 v[112:127], v[132:135], v[224:227], v[112:127]\n\tds_read_b64_tr_b16 v[164:165], v5 offset:49152\n\t"
            "ds_read_b64_tr_b16 v[166:167], v5 offset:49664\n\tbuffer_load_dwordx4 v[204:207], v2, s[44:47], s5 offen offset:3072 sc1\n\tv_mfma_f32_32x32x16_bf16 v[96:111], v[136:139], v[228:231], v[96:111]\n\tds_read_b64_tr_b16 v[168:169], v6 offset:49152\n\tds_read_b64_tr_b16 v[170:171], v6 offset:49664\n\ts_waitcnt vmcnt(8)\n\t"
            "v_max3_u32 v12, v232, v234, v236\n\tv_max_u32 v12, v12, v238\n\tv_cmp_eq_u32 vcc, -1, v12\n\tv_mfma_f32_32x32x16_bf16 v[112:127], v[140:143], v[228:231], v[112:127]\n\tds_read_b64_tr_b16 v[172:173], v7 offset:49152\n\tds_read_b64_tr_b16 v[174:175], v7 offset:49664\n\t"
            "s_cbranch_vccnz 107f\n\t207:\ns_waitcnt lgkmcnt(8)\n\tv_mfma_f32_32x32x16_bf16 v[96:111], v[144:147], v[232:235], v[96:111]\n\tds_read_b64_tr_b16 v[128:129], v4 offset:50176\n\tds_read_b64_tr_b16 v[130:131], v4 offset:50688\n\t"
            "buffer_load_dwordx4 v[208:211], v2, s[44:47], s6 offen offset:0 sc1\n\tv_mfma_f32_32x32x16_bf16 v[112:127], v[148:151], v[232:235], v[112:127]\n\tds_read_b64_tr_b16 v[132:133], v5 offset:50176\n\tds_read_b64_tr_b16 v[134:135], v5 offset:50688\n\tbuffer_load_dwordx4 v[212:215], v2, s[44:47], s6 offen offset:3072 sc1\n\tv_mfma_f32_32x32x16_bf16 v[96:111], v[152:155], v[236:239], v[96:111]\n\t"
            "ds_read_b64_tr_b16 v[136:137], v6 offset:50176\n\tds_read_b64_tr_b16 v[138:139], v6 offset:50688\n\ts_waitcnt vmcnt(8)\n\tv_max3_u32 v12, v176, v178, v180\n\tv_max_u32 v12, v12, v182\n\tv_cmp_eq_u32 vcc, -1, v12\n\t"
            "v_mfma_f32_32x32x16_bf16 v[112:127], v[156:159], v[236:239], v[112:127]\n\tds_read_b64_tr_b16 v[140:141], v7 offset:50176\n\tds_read_b64_tr_b16 v[142:143], v7 offset:50688\n\ts_cbranch_vccnz 108f\n\t208:\ns_waitcnt lgkmcnt(8)\n\t"
            "v_mfma_f32_32x32x16_bf16 v[96:111], v[160:163], v[176:179], v[96:111]\n\tds_read_b64_tr_b16 v[144:145], v4 offset:0\n\tds_read_b64_tr_b16 v[146:147], v4 offset:512\n\tbuffer_load_dwordx4 v[216:219], v2, s[44:47], s7 offen offset:0 sc1\n\tv_mfma_f32_32x32x16_bf16 v[112:127], v[164:167], v[176:179], v[112:127]\n\tds_read_b64_tr_b16 v[148:149], v5 offset:0\n\t"
            "ds_read_b64_tr_b16 v[150:151], v5 offset:512\n\tbuffer_load_dwordx4 v[220:223], v2, s[44:47], s7 offen offset:3072 sc1\n\tv_mfma_f32_32x32x16_bf16 v[96:111], v[168:171], v[180:183], v[96:111]\n\tds_read_b64_tr_b16 v[152:153], v6 offset:0\n\tds_read_b64_tr_b16 v[154:155], v6 offset:512\n\ts_waitcnt vmcnt(8)\n\t"
            "v_max3_u32 v12, v184, v186, v188\n\tv_max_u32 v12, v12, v190\n\tv_cmp_eq_u32 vcc, -1, v12\n\tv_mfma_f32_32x32x16_bf16 v[112:127], v[172:175], v[180:183], v[112:127]\n\tds_read_b64_tr_b16 v[156:157], v7 offset:0\n\tds_read_b64_tr_b16 v[158:159], v7 offset:512\n\t"
            "s_cbranch_vccnz 109f\n\t209:\ns_waitcnt lgkmcnt(8)\n\tv_mfma_f32_32x32x16_bf16 v[96:111], v[128:131], v[184:187], v[96:111]\n\tds_read_b64_tr_b16 v[160:161], v4 offset:1024\n\tds_read_b64_tr_b16 v[162:163], v4 offset:1536\n\t"
            "buffer_load_dwordx4 v[224:227], v2, s[44:47], s8 offen offset:0 sc1\n\tv_mfma_f32_32x32x16_bf16 v[112:127], v[132:135], v[184:187], v[112:127]\n\tds_read_b64_tr_b16 v[164:165], v5 offset:1024\n\tds_read_b64_tr_b16 v[166:167], v5 offset:1536\n\tbuffer_load_dwordx4 v[228:231], v2, s[44:47], s8 offen offset:3072 sc1\n\tv_mfma_f32_32x32x16_bf16 v[96:111], v[136:139], v[188:191], v[96:111]\n\t"
            "ds_read_b64_tr_b16 v[168:169], v6 offset:1024\n\tds_read_b64_tr_b16 v[170:171], v6 offset:1536\n\ts_waitcnt vmcnt(8)\n\tv_max3_u32 v12, v192, v194, v196\n\tv_max_u32 v12, v12, v198\n\tv_cmp_eq_u32 vcc, -1, v12\n\t"
            "v_mfma_f32_32x32x16_bf16 v[112:127], v[140:143], v[188:191], v[112:127]\n\tds_read_b64_tr_b16 v[172:173], v7 offset:1024\n\tds_read_b64_tr_b16 v[174:175], v7 offset:1536\n\ts_cbranch_vccnz 110f\n\t210:\ns_waitcnt lgkmcnt(8)\n\t"
            "v_mfma_f32_32x32x16_bf16 v[96:111], v[144:147], v[192:195], v[96:111]\n\tds_read_b64_tr_b16 v[128:129], v4 offset:2048\n\tds_read_b64_tr_b16 v[130:131], v4 offset:2560\n\tbuffer_load_dwordx4 v[232:235], v2, s[44:47], s9 offen offset:0 sc1\n\tv_mfma_f32_32x32x16_bf16 v[112:127], v[148:151], v[192:195], v[112:127]\n\tds_read_b64_tr_b16 v[132:133], v5 offset:2048\n\t"
            "ds_read_b64_tr_b16 v[134:135], v5 offset:2560\n\tbuffer_load_dwordx4 v[236:239], v2, s[44:47], s9 offen offset:3072 sc1\n\tv_mfma_f32_32x32x16_bf16 v[96:111], v[152:155], v[196:199], v[96:111]\n\tds_read_b64_tr_b16 v[136:137], v6 offset:2048\n\tds_read_b64_tr_b16 v[138:139], v6 offset:2560\n\ts_waitcnt vmcnt(8)\n\t"
            "v_max3_u32 v12, v200, v202, v204\n\tv_max_u32 v12, v12, v206\n\tv_cmp_eq_u32 vcc, -1, v12\n\tv_mfma_f32_32x32x16_bf16 v[112:127], v[156:159], v[196:199], v[112:127]\n\tds_read_b64_tr_b16 v[140:141], v7 offset:2048\n\tds_read_b64_tr_b16 v[142:143], v7 offset:2560\n\t"
            "s_cbranch_vccnz 111f\n\t211:\ns_waitcnt lgkmcnt(8)\n\tv_mfma_f32_32x32x16_bf16 v[96:111], v[160:163], v[200:203], v[96:111]\n\tds_read_b64_tr_b16 v[144:145], v4 offset:3072\n\tds_read_b64_tr_b16 v[146:147], v4 offset:3584\n\t"
            "buffer_load_dwordx4 v[176:179], v2, s[44:47], s10 offen offset:0 sc1\n\tv_mfma_f32_32x32x16_bf16 v[112:127], v[164:167], v[200:203], v[112:127]\n\tds_read_b64_tr_b16 v[148:149], v5 offset:3072\n\tds_read_b64_tr_b16 v[150:151], v5 offset:3584\n\tbuffer_load_dwordx4 v[180:183], v2, s[44:47], s10 offen offset:3072 sc1\n\tv_mfma_f32_32x32x16_bf16 v[96:111], v[168:171], v[204:207], v[96:111]\n\t"
            "ds_read_b64_tr_b16 v[152:153], v6 offset:3072\n\tds_read_b64_tr_b16 v[154:155], v6 offset:3584\n\ts_waitcnt vmcnt(8)\n\tv_max3_u32 v12, v208, v210, v212\n\tv_max_u32 v12, v12, v214\n\tv_cmp_eq_u32 vcc, -1, v12\n\t"
            "v_mfma_f32_32x32x16_bf16 v[112:127], v[172:175], v[204:207], v[112:127]\n\tds_read_b64_tr_b16 v[156:157], v7 offset:3072\n\tds_read_b64_tr_b16 v[158:159], v7 offset:3584\n\ts_cbranch_vccnz 112f\n\t212:\ns_waitcnt lgkmcnt(8)\n\t"
            "v_mfma_f32_32x32x16_bf16 v[96:111], v[128:131], v[208:211], v[96:111]\n\tds_read_b64_tr_b16 v[160:161], v4 offset:4096\n\tds_read_b64_tr_b16 v[162:163], v4 offset:4608\n\tbuffer_load_dwordx4 v[184:187], v2, s[44:47], s11 offen offset:0 sc1\n\tv_mfma_f32_32x32x16_bf16 v[112:127], v[132:135], v[208:211], v[112:127]\n\tds_read_b64_tr_b16 v[164:165], v5 offset:4096\n\t"
            "ds_read_b64_tr_b16 v[166:167], v5 offset:4608\n\tbuffer_load_dwordx4 v[188:191], v2, s[44:47], s11 offen offset:3072 sc1\n\tv_mfma_f32_32x32x16_bf16 v[96:111], v[136:139], v[212:215], v[96:111]\n\tds_read_b64_tr_b16 v[168:169], v6 offset:4096\n\tds_read_b64_tr_b16 v[170:171], v6 offset:4608\n\ts_waitcnt vmcnt(8)\n\t"
            "v_max3_u32 v12, v216, v218, v220\n\tv_max_u32 v12, v12, v222\n\tv_cmp_eq_u32 vcc, -1, v12\n\tv_mfma_f32_32x32x16_bf16 v[112:127], v[140:143], v[212:215], v[112:127]\n\tds_read_b64_tr_b16 v[172:173], v7 offset:4096\n\tds_read_b64_tr_b16 v[174:175], v7 offset:4608\n\t"
            "s_cbranch_vccnz 113f\n\t213:\ns_waitcnt lgkmcnt(8)\n\tv_mfma_f32_32x32x16_bf16 v[96:111], v[144:147], v[216:219], v[96:111]\n\tds_read_b64_tr_b16 v[128:129], v4 offset:5120\n\tds_read_b64_tr_b16 v[130:131], v4 offset:5632\n\t"
            "buffer_load_dwordx4 v[192:195], v2, s[44:47], s12 offen offset:0 sc1\n\tv_mfma_f32_32x32x16_bf16 v[112:127], v[148:151], v[216:219], v[112:127]\n\tds_read_b64_tr_b16 v[132:133], v5 offset:5120\n\tds_read_b64_tr_b16 v[134:135], v5 offset:5632\n\tbuffer_load_dwordx4 v[196:199], v2, s[44:47], s12 offen offset:3072 sc1\n\tv_mfma_f32_32x32x16_bf16 v[96:111], v[152:155], v[220:223], v[96:111]\n\t"
            "ds_read_b64_tr_b16 v[136:137], v6 offset:5120\n\tds_read_b64_tr_b16 v[138:139], v6 offset:5632\n\ts_waitcnt vmcnt(8)\n\tv_max3_u32 v12, v224, v226, v228\n\tv_max_u32 v12, v12, v230\n\tv_cmp_eq_u32 vcc, -1, v12\n\t"
            "v_mfma_f32_32x32x16_bf16 v[112:127], v[156:159], v[220:223], v[112:127]\n\tds_read_b64_tr_b16 v[140:141], v7 offset:5120\n\tds_read_b64_tr_b16 v[142:143], v7 offset:5632\n\ts_cbranch_vccnz 114f\n\t214:\ns_waitcnt lgkmcnt(8)\n\t"
            "v_mfma_f32_32x32x16_bf16 v[96:111], v[160:163], v[224:227], v[96:111]\n\tds_read_b64_tr_b16 v[144:145], v4 offset:6144\n\tds_read_b64_tr_b16 v[146:147], v4 offset:6656\n\tbuffer_load_dwordx4 v[200:203], v2, s[44:47], s13 offen offset:0 sc1\n\tv_mfma_f32_32x32x16_bf16 v[112:127], v[164:167], v[224:227], v[112:127]\n\tds_read_b64_tr_b16 v[148:149], v5 offset:6144\n\t"
            "ds_read_b64_tr_b16 v[150:151], v5 offset:6656\n\tbuffer_load_dwordx4 v[204:207], v2, s[44:47], s13 offen offset:3072 sc1\n\tv_mfma_f32_32x32x16_bf16 v[96:111], v[168:171], v[228:231], v[96:111]\n\tds_read_b64_tr_b16 v[152:153], v6 offset:6144\n\tds_read_b64_tr_b16 v[154:155], v6 offset:6656\n\ts_waitcnt vmcnt(8)\n\t"
            "v_max3_u32 v12, v232, v234, v236\n\tv_max_u32 v12, v12, v238\n\tv_cmp_eq_u32 vcc, -1, v12\n\tv_mfma_f32_32x32x16_bf16 v[112:127], v[172:175], v[228:231], v[112:127]\n\tds_read_b64_tr_b16 v[156:157], v7 offset:6144\n\tds_read_b64_tr_b16 v[158:159], v7 offset:6656\n\t"
            "s_cbranch_vccnz 115f\n\t215:\ns_waitcnt lgkmcnt(8)\n\tv_mfma_f32_32x32x16_bf16 v[96:111], v[128:131], v[232:235], v[96:111]\n\tds_read_b64_tr_b16 v[160:161], v4 offset:7168\n\tds_read_b64_tr_b16 v[162:163], v4 offset:7680\n\t"
            "v_mfma_f32_32x32x16_bf16 v[112:127], v[132:135], v[232:235], v[112:127]\n\tds_read_b64_tr_b16 v[164:165], v5 offset:7168\n\tds_read_b64_tr_b16 v[166:167], v5 offset:7680\n\tv_mfma_f32_32x32x16_bf16 v[96:111], v[136:139], v[236:239], v[96:111]\n\tds_read_b64_tr_b16 v[168:169], v6 offset:7168\n\tds_read_b64_tr_b16 v[170:171], v6 offset:7680\n\t"
            "s_waitcnt vmcnt(6)\n\tv_max3_u32 v12, v176, v178, v180\n\tv_max_u32 v12, v12, v182\n\tv_cmp_eq_u32 vcc, -1, v12\n\tv_mfma_f32_32x32x16_bf16 v[112:127], v[140:143], v[236:239], v[112:127]\n\tds_read_b64_tr_b16 v[172:173], v7 offset:7168\n\t"
            "ds_read_b64_tr_b16 v[174:175], v7 offset:7680\n\ts_cbranch_vccnz 116f\n\t216:\ns_waitcnt lgkmcnt(8)\n\tv_mfma_f32_32x32x16_bf16 v[96:111], v[144:147], v[176:179], v[96:111]\n\tds_read_b64_tr_b16 v[128:129], v4 offset:8192\n\t"
            "ds_read_b64_tr_b16 v[130:131], v4 offset:8704\n\tv_mfma_f32_32x32x16_bf16 v[112:127], v[148:151], v[176:179], v[112:127]\n\tds_read_b64_tr_b16 v[132:133], v5 offset:8192\n\tds_read_b64_tr_b16 v[134:135], v5 offset:8704\n\tv_mfma_f32_32x32x16_bf16 v[96:111], v[152:155], v[180:183], v[96:111]\n\tds_read_b64_tr_b16 v[136:137], v6 offset:8192\n\t"
            "ds_read_b64_tr_b16 v[138:139], v6 offset:8704\n\ts_waitcnt vmcnt(4)\n\tv_max3_u32 v12, v184, v186, v188\n\tv_max_u32 v12, v12, v190\n\tv_cmp_eq_u32 vcc, -1, v12\n\tv_mfma_f32_32x32x16_bf16 v[112:127], v[156:159], v[180:183], v[112:127]\n\t"
            "ds_read_b64_tr_b16 v[140:141], v7 offset:8192\n\tds_read_b64_tr_b16 v[142:143], v7 offset:8704\n\ts_cbranch_vccnz 117f\n\t217:\nds_read_b32 v16, v8 offset:64\n\tds_read_b32 v17, v9 offset:1344\n\t"
            "s_waitcnt lgkmcnt(10)\n\tv_mfma_f32_32x32x16_bf16 v[96:111], v[160:163], v[184:187], v[96:111]\n\tds_read_b64_tr_b16 v[144:145], v4 offset:9216\n\tds_read_b64_tr_b16 v[146:147], v4 offset:9728\n\tds_read_b64_tr_b16 v[148:149], v5 offset:9216\n\tds_read_b64_tr_b16 v[150:151], v5 offset:9728\n\t"
            "ds_read_b64_tr_b16 v[152:153], v6 offset:9216\n\tds_read_b64_tr_b16 v[154:155], v6 offset:9728\n\ts_waitcnt vmcnt(2)\n\tv_max3_u32 v12, v192, v194, v196\n\tv_max_u32 v12, v12, v198\n\tv_cmp_eq_u32 vcc, -1, v12\n\t"
            "v_mfma_f32_32x32x16_bf16 v[96:111], v[168:171], v[188:191], v[96:111]\n\tds_read_b64_tr_b16 v[156:157], v7 offset:9216\n\tds_read_b64_tr_b16 v[158:159], v7 offset:9728\n\ts_cbranch_vccnz 118f\n\t218:\ns_waitcnt lgkmcnt(10)\n\t"
            "v_mfma_f32_32x32x16_bf16 v[96:111], v[128:131], v[192:195], v[96:111]\n\ts_waitcnt vmcnt(0)\n\tv_max3_u32 v12, v200, v202, v204\n\tv_max_u32 v12, v12, v206\n\tv_cmp_eq_u32 vcc, -1, v12\n\tv_mfma_f32_32x32x16_bf16 v[96:111], v[136:139], v[196:199], v[96:111]\n\t"
            "s_cbranch_vccnz 119f\n\t219:\ns_waitcnt lgkmcnt(0)\n\tv_mfma_f32_32x32x16_bf16 v[96:111], v[144:147], v[200:203], v[96:111]\n\tv_cmp_ne_u32 s[74:75], 0, v17\n\tv_cmp_eq_u32 vcc, 0, v16\n\t"
            "v_mfma_f32_32x32x16_bf16 v[96:111], v[152:155], v[204:207], v[96:111]\n\ts_or_b64 vcc, vcc, s[74:75]\n\tv_mfma_f32_32x32x16_bf16 v[112:127], v[164:167], v[184:187], v[112:127]\n\ts_cbranch_scc1 7f\n\t8:\ns_nop 5\n\t"
            "v_mfma_f32_32x32x16_bf16 v[112:127], v[172:175], v[188:191], v[112:127]\n\ts_nop 1\n\tv_fmac_f32 v64, v96, v32\n\tv_fmac_f32 v65, v97, v33\n\tv_fmac_f32 v66, v98, v34\n\tv_fmac_f32 v67, v99, v35\n\t"
            "v_fmac_f32 v68, v100, v36\n\tv_fmac_f32 v69, v101, v37\n\tv_fmac_f32 v70, v102, v38\n\tv_mfma_f32_32x32x16_bf16 v[112:127], v[132:135], v[192:195], v[112:127]\n\tv_fmac_f32 v71, v103, v39\n\tv_fmac_f32 v72, v104, v40\n\t"
            "v_fmac_f32 v73, v105, v41\n\tv_fmac_f32 v74, v106, v42\n\tv_fmac_f32 v75, v107, v43\n\tv_fmac_f32 v76, v108, v44\n\tv_fmac_f32 v77, v109, v45\n\tv_mfma_f32_32x32x16_bf16 v[112:127], v[140:143], v[196:199], v[112:127]\n\t"
            "v_fmac_f32 v78, v110, v46\n\tv_fmac_f32 v79, v111, v47\n\tv_cvt_pk_bf16_f32 v240, v64, v65\n\tv_cvt_pk_bf16_f32 v242, v68, v69\n\tv_cvt_pk_bf16_f32 v241, v66, v67\n\tv_cvt_pk_bf16_f32 v243, v70, v71\n\t"
            "v_cvt_pk_bf16_f32 v244, v72, v73\n\tv_mfma_f32_32x32x16_bf16 v[112:127], v[148:151], v[200:203], v[112:127]\n\tv_cvt_pk_bf16_f32 v246, v76, v77\n\tv_cvt_pk_bf16_f32 v245, v74, v75\n\tv_cvt_pk_bf16_f32 v247, v78, v79\n\tv_permlane32_swap_b32 v240, v242\n\t"
            "v_permlane32_swap_b32 v241, v243\n\tv_permlane32_swap_b32 v244, v246\n\tv_permlane32_swap_b32 v245, v247\n\tv_mfma_f32_32x32x16_bf16 v[112:127], v[156:159], v[204:207], v[112:127]\n\tv_permlane16_swap_b32 v240, v244\n\tv_permlane16_swap_b32 v242, v246\n\t"
            "v_permlane16_swap_b32 v241, v245\n\tv_permlane16_swap_b32 v243, v247\n\tbuffer_store_dwordx4 v[240:243], v3, s[52:55], 0 offen offset:0 sc1\n\tbuffer_store_dwordx4 v[244:247], v11, s[52:55], 0 offen offset:0 sc1\n\t"
            SO(6)
            "s_nop 5\n\tv_fmac_f32 v80, v112, v48\n\tv_fmac_f32 v81, v113, v49\n\tv_fmac_f32 v82, v114, v50\n\tv_fmac_f32 v83, v115, v51\n\tv_fmac_f32 v84, v116, v52\n\t"
            "v_fmac_f32 v85, v117, v53\n\tv_fmac_f32 v86, v118, v54\n\tv_fmac_f32 v87, v119, v55\n\tv_fmac_f32 v88, v120, v56\n\tv_fmac_f32 v89, v121, v57\n\tv_fmac_f32 v90, v122, v58\n\t"
            "v_fmac_f32 v91, v123, v59\n\tv_fmac_f32 v92, v124, v60\n\tv_fmac_f32 v93, v125, v61\n\tv_fmac_f32 v94, v126, v62\n\tv_fmac_f32 v95, v127, v63\n\tv_cvt_pk_bf16_f32 v248, v80, v81\n\t"
            "v_cvt_pk_bf16_f32 v250, v84, v85\n\tv_cvt_pk_bf16_f32 v249, v82, v83\n\tv_cvt_pk_bf16_f32 v251, v86, v87\n\tv_cvt_pk_bf16_f32 v252, v88, v89\n\tv_cvt_pk_bf16_f32 v254, v92, v93\n\tv_cvt_pk_bf16_f32 v253, v90, v91\n\t"
            "v_cvt_pk_bf16_f32 v255, v94, v95\n\tv_permlane32_swap_b32 v248, v250\n\tv_permlane32_swap_b32 v249, v251\n\tv_permlane32_swap_b32 v252, v254\n\tv_permlane32_swap_b32 v253, v255\n\ts_nop 0\n\t"
            "v_permlane16_swap_b32 v248, v252\n\tv_permlane16_swap_b32 v250, v254\n\tv_permlane16_swap_b32 v249, v253\n\tv_permlane16_swap_b32 v251, v255\n\tbuffer_store_dwordx4 v[248:251], v3, s[52:55], 0 offen offset:64 sc1\n\tbuffer_store_dwordx4 v[252:255], v11, s[52:55], 0 offen offset:64 sc1\n\t"
            "9:\n"
            RCNT DUMP
            "s_endpgm\n\t.LG0:\nv_mul_f32 v21, 0xbfb8aa3b, v21\n\tv_mul_f32 v20, v20, v21\n\tv_permlane16_swap_b32 v240, v244\n\tv_permlane16_swap_b32 v241, v245\n\t"
            "v_permlane16_swap_b32 v242, v246\n\tv_permlane16_swap_b32 v243, v247\n\tv_permlane16_swap_b32 v248, v252\n\tv_permlane16_swap_b32 v249, v253\n\tv_permlane16_swap_b32 v250, v254\n\tv_permlane16_swap_b32 v251, v255\n\t"
            "v_permlane32_swap_b32 v240, v242\n\tv_permlane32_swap_b32 v241, v243\n\tv_permlane32_swap_b32 v244, v246\n\tv_permlane32_swap_b32 v245, v247\n\tv_permlane32_swap_b32 v248, v250\n\tv_permlane32_swap_b32 v249, v251\n\t"
            "v_permlane32_swap_b32 v252, v254\n\tv_permlane32_swap_b32 v253, v255\n\tv_lshlrev_b32 v64, 16, v240\n\tv_and_b32 v65, 0xffff0000, v240\n\tv_lshlrev_b32 v66, 16, v241\n\tv_and_b32 v67, 0xffff0000, v241\n\t"
            "v_lshlrev_b32 v68, 16, v242\n\tv_and_b32 v69, 0xffff0000, v242\n\tv_lshlrev_b32 v70, 16, v243\n\tv_and_b32 v71, 0xffff0000, v243\n\tv_lshlrev_b32 v72, 16, v244\n\tv_and_b32 v73, 0xffff0000, v244\n\t"
            "v_lshlrev_b32 v74, 16, v245\n\tv_and_b32 v75, 0xffff0000, v245\n\tv_lshlrev_b32 v76, 16, v246\n\tv_and_b32 v77, 0xffff0000, v246\n\tv_lshlrev_b32 v78, 16, v247\n\tv_and_b32 v79, 0xffff0000, v247\n\t"
            "v_lshlrev_b32 v80, 16, v248\n\tv_and_b32 v81, 0xffff0000, v248\n\tv_lshlrev_b32 v82, 16, v249\n\tv_and_b32 v83, 0xffff0000, v249\n\tv_lshlrev_b32 v84, 16, v250\n\tv_and_b32 v85, 0xffff0000, v250\n\t"
            "v_lshlrev_b32 v86, 16, v251\n\tv_and_b32 v87, 0xffff0000, v251\n\tv_lshlrev_b32 v88, 16, v252\n\tv_and_b32 v89, 0xffff0000, v252\n\tv_lshlrev_b32 v90, 16, v253\n\tv_and_b32 v91, 0xffff0000, v253\n\t"
            "v_lshlrev_b32 v92, 16, v254\n\tv_and_b32 v93, 0xffff0000, v254\n\tv_lshlrev_b32 v94, 16, v255\n\tv_and_b32 v95, 0xffff0000, v255\n\tv_fma_f32 v96, v64, v21, v20\n\tv_fma_f32 v97, v65, v21, v20\n\t"
            "v_fma_f32 v98, v66, v21, v20\n\tv_fma_f32 v99, v67, v21, v20\n\tv_fma_f32 v100, v68, v21, v20\n\tv_fma_f32 v101, v69, v21, v20\n\tv_fma_f32 v102, v70, v21, v20\n\tv_fma_f32 v103, v71, v21, v20\n\t"
            "v_fma_f32 v104, v72, v21, v20\n\tv_fma_f32 v105, v73, v21, v20\n\tv_fma_f32 v106, v74, v21, v20\n\tv_fma_f32 v107, v75, v21, v20\n\tv_fma_f32 v108, v76, v21, v20\n\tv_fma_f32 v109, v77, v21, v20\n\t"
            "v_fma_f32 v110, v78, v21, v20\n\tv_fma_f32 v111, v79, v21, v20\n\tv_fma_f32 v112, v80, v21, v20\n\tv_fma_f32 v113, v81, v21, v20\n\tv_fma_f32 v114, v82, v21, v20\n\tv_fma_f32 v115, v83, v21, v20\n\t"
            "v_fma_f32 v116, v84, v21, v20\n\tv_fma_f32 v117, v85, v21, v20\n\tv_fma_f32 v118, v86, v21, v20\n\tv_fma_f32 v119, v87, v21, v20\n\tv_fma_f32 v120, v88, v21, v20\n\tv_fma_f32 v121, v89, v21, v20\n\t"
            "v_fma_f32 v122, v90, v21, v20\n\tv_fma_f32 v123, v91, v21, v20\n\tv_fma_f32 v124, v92, v21, v20\n\tv_fma_f32 v125, v93, v21, v20\n\tv_fma_f32 v126, v94, v21, v20\n\tv_fma_f32 v127, v95, v21, v20\n\t"
            "s_setpc_b64 s[76:77]\n\t.LG1:\nv_exp_f32 v32, v96\n\tv_exp_f32 v33, v97\n\tv_exp_f32 v34, v98\n\tv_exp_f32 v35, v99\n\t"
            "v_exp_f32 v36, v100\n\tv_exp_f32 v37, v101\n\tv_exp_f32 v38, v102\n\tv_exp_f32 v39, v103\n\tv_exp_f32 v40, v104\n\tv_exp_f32 v41, v105\n\t"
            "v_exp_f32 v42, v106\n\tv_exp_f32 v43, v107\n\tv_exp_f32 v44, v108\n\tv_exp_f32 v45, v109\n\tv_exp_f32 v46, v110\n\tv_exp_f32 v47, v111\n\t"
            "v_exp_f32 v48, v112\n\tv_exp_f32 v49, v113\n\tv_exp_f32 v50, v114\n\tv_exp_f32 v51, v115\n\tv_exp_f32 v52, v116\n\tv_exp_f32 v53, v117\n\t"
            "v_exp_f32 v54, v118\n\tv_exp_f32 v55, v119\n\tv_exp_f32 v56, v120\n\tv_exp_f32 v57, v121\n\tv_exp_f32 v58, v122\n\tv_exp_f32 v59, v123\n\t"
            "v_exp_f32 v60, v124\n\tv_exp_f32 v61, v125\n\tv_exp_f32 v62, v126\n\tv_exp_f32 v63, v127\n\tv_add_f32 v32, 1.0, v32\n\tv_add_f32 v33, 1.0, v33\n\t"
            "v_add_f32 v34, 1.0, v34\n\tv_add_f32 v35, 1.0, v35\n\tv_add_f32 v36, 1.0, v36\n\tv_add_f32 v37, 1.0, v37\n\tv_add_f32 v38, 1.0, v38\n\tv_add_f32 v39, 1.0, v39\n\t"
            "v_add_f32 v40, 1.0, v40\n\tv_add_f32 v41, 1.0, v41\n\tv_add_f32 v42, 1.0, v42\n\tv_add_f32 v43, 1.0, v43\n\tv_add_f32 v44, 1.0, v44\n\tv_add_f32 v45, 1.0, v45\n\t"
            "v_add_f32 v46, 1.0, v46\n\tv_add_f32 v47, 1.0, v47\n\tv_add_f32 v48, 1.0, v48\n\tv_add_f32 v49, 1.0, v49\n\tv_add_f32 v50, 1.0, v50\n\tv_add_f32 v51, 1.0, v51\n\t"
            "v_add_f32 v52, 1.0, v52\n\tv_add_f32 v53, 1.0, v53\n\tv_add_f32 v54, 1.0, v54\n\tv_add_f32 v55, 1.0, v55\n\tv_add_f32 v56, 1.0, v56\n\tv_add_f32 v57, 1.0, v57\n\t"
            "v_add_f32 v58, 1.0, v58\n\tv_add_f32 v59, 1.0, v59\n\tv_add_f32 v60, 1.0, v60\n\tv_add_f32 v61, 1.0, v61\n\tv_add_f32 v62, 1.0, v62\n\tv_add_f32 v63, 1.0, v63\n\t"
            "v_rcp_f32 v32, v32\n\tv_rcp_f32 v33, v33\n\tv_rcp_f32 v34, v34\n\tv_rcp_f32 v35, v35\n\ts_mov_b32 s83, 1\n\ts_setpc_b64 s[76:77]\n\t"
            ".LG2:\nv_rcp_f32 v36, v36\n\tv_rcp_f32 v37, v37\n\tv_rcp_f32 v38, v38\n\tv_rcp_f32 v39, v39\n\tv_rcp_f32 v40, v40\n\t"
            "v_rcp_f32 v41, v41\n\tv_rcp_f32 v42, v42\n\tv_rcp_f32 v43, v43\n\tv_rcp_f32 v44, v44\n\tv_rcp_f32 v45, v45\n\tv_rcp_f32 v46, v46\n\t"
            "v_rcp_f32 v47, v47\n\tv_rcp_f32 v48, v48\n\tv_rcp_f32 v49, v49\n\tv_rcp_f32 v50, v50\n\tv_rcp_f32 v51, v51\n\tv_rcp_f32 v52, v52\n\t"
            "v_rcp_f32 v53, v53\n\tv_rcp_f32 v54, v54\n\tv_rcp_f32 v55, v55\n\tv_rcp_f32 v56, v56\n\tv_rcp_f32 v57, v57\n\tv_rcp_f32 v58, v58\n\t"
            "v_rcp_f32 v59, v59\n\tv_rcp_f32 v60, v60\n\tv_rcp_f32 v61, v61\n\tv_rcp_f32 v62, v62\n\tv_rcp_f32 v63, v63\n\tv_mul_f32 v32, v32, v96\n\t"
            "v_mul_f32 v33, v33, v97\n\tv_mul_f32 v34, v34, v98\n\tv_mul_f32 v35, v35, v99\n\tv_mul_f32 v36, v36, v100\n\tv_mul_f32 v37, v37, v101\n\tv_mul_f32 v38, v38, v102\n\t"
            "v_mul_f32 v39, v39, v103\n\tv_mul_f32 v40, v40, v104\n\tv_mul_f32 v41, v41, v105\n\tv_mul_f32 v42, v42, v106\n\tv_mul_f32 v43, v43, v107\n\tv_mul_f32 v44, v44, v108\n\t"
            "v_mul_f32 v45, v45, v109\n\tv_mul_f32 v46, v46, v110\n\tv_mul_f32 v47, v47, v111\n\ts_mov_b32 s83, 2\n\ts_setpc_b64 s[76:77]\n\t7:\n"
            "ds_read_b32 v16, v8 offset:64\n\tds_read_b32 v17, v9 offset:1344\n\ts_waitcnt lgkmcnt(0)\n\tv_cmp_ne_u32 s[74:75], 0, v17\n\tv_cmp_eq_u32 vcc, 0, v16\n\ts_cbranch_vccz 71f\n\t"
            "s_sleep 1\n\ts_sub_u32 s68, s68, 1\n\ts_cbranch_scc0 7b\n\ts_branch 9b\n\t71:\nv_lshrrev_b32 v12, 5, v2\n\t"
            "v_and_b32 v12, 16, v12\n\tv_add_u32 v12, v12, v17\n\ts_mov_b64 exec, s[74:75]\n\ts_cbranch_execz 72f\n\tds_read_b128 v[32:35], v12 offset:0\n\tds_read_b128 v[36:39], v12 offset:32\n\t"
            "ds_read_b128 v[40:43], v12 offset:64\n\tds_read_b128 v[44:47], v12 offset:96\n\tds_read_b128 v[48:51], v12 offset:128\n\tds_read_b128 v[52:55], v12 offset:160\n\tds_read_b128 v[56:59], v12 offset:192\n\tds_read_b128 v[60:63], v12 offset:224\n\t"
            "72:\ns_mov_b64 exec, -1\n\ts_waitcnt lgkmcnt(0)\n\ts_branch 8b\n\t5:\ns_sleep 1\n\t"
            "ds_read_b64 v[14:15], v8 offset:40\n\ts_waitcnt lgkmcnt(0)\n\tv_min_u32 v14, v14, v15\n\tv_cmp_ge_u32 vcc, s71, v14\n\ts_cbranch_vccz 61f\n\ts_sub_u32 s68, s68, 1\n\t"
            "s_cbranch_scc0 5b\n\ts_branch 9b\n\t61:\n"
            TRG2D
            "s_branch 6b\n\t62:\nds_read_b128 v[16:19], v8 offset:48\n\tds_read_b64_tr_b16 v[128:129], v4 offset:40960\n\tds_read_b64_tr_b16 v[130:131], v4 offset:41472\n\tds_read_b64_tr_b16 v[132:133], v5 offset:40960\n\t"
            "ds_read_b64_tr_b16 v[134:135], v5 offset:41472\n\tds_read_b64_tr_b16 v[136:137], v6 offset:40960\n\tds_read_b64_tr_b16 v[138:139], v6 offset:41472\n\tds_read_b64_tr_b16 v[140:141], v7 offset:40960\n\tds_read_b64_tr_b16 v[142:143], v7 offset:41472\n\tds_read_b64_tr_b16 v[144:145], v4 offset:41984\n\t"
            "ds_read_b64_tr_b16 v[146:147], v4 offset:42496\n\tds_read_b64_tr_b16 v[148:149], v5 offset:41984\n\tds_read_b64_tr_b16 v[150:151], v5 offset:42496\n\tds_read_b64_tr_b16 v[152:153], v6 offset:41984\n\tds_read_b64_tr_b16 v[154:155], v6 offset:42496\n\tds_read_b64_tr_b16 v[156:157], v7 offset:41984\n\t"
            "ds_read_b64_tr_b16 v[158:159], v7 offset:42496\n\ts_waitcnt lgkmcnt(15)\n\tv_min3_u32 v16, v16, v17, v18\n\tv_min_u32 v16, v16, v19\n\tv_cmp_eq_u32 vcc, 0, v16\n\ts_cbranch_vccz 63b\n\t"
            "s_sub_u32 s68, s68, 1\n\ts_cbranch_scc0 62b\n\ts_branch 9b\n\t100:\ns_mov_b32 s70, 4\n\ts_call_b64 s[72:73], 300f\n\t"
            "v_max3_u32 v12, v176, v178, v180\n\tv_max_u32 v12, v12, v182\n\tv_cmp_eq_u32 vcc, -1, v12\n\ts_cbranch_vccnz 100b\n\ts_branch 200b\n\t101:\n"
            "s_mov_b32 s70, 5\n\ts_call_b64 s[72:73], 301f\n\tv_max3_u32 v12, v184, v186, v188\n\tv_max_u32 v12, v12, v190\n\tv_cmp_eq_u32 vcc, -1, v12\n\ts_cbranch_vccnz 101b\n\t"
            "s_branch 201b\n\t102:\ns_mov_b32 s70, 6\n\ts_call_b64 s[72:73], 302f\n\tv_max3_u32 v12, v192, v194, v196\n\tv_max_u32 v12, v12, v198\n\t"
            "v_cmp_eq_u32 vcc, -1, v12\n\ts_cbranch_vccnz 102b\n\ts_branch 202b\n\t103:\ns_mov_b32 s70, 7\n\ts_call_b64 s[72:73], 303f\n\t"
            "v_max3_u32 v12, v200, v202, v204\n\tv_max_u32 v12, v12, v206\n\tv_cmp_eq_u32 vcc, -1, v12\n\ts_cbranch_vccnz 103b\n\ts_branch 203b\n\t104:\n"
            "s_mov_b32 s70, 8\n\ts_call_b64 s[72:73], 304f\n\tv_max3_u32 v12, v208, v210, v212\n\tv_max_u32 v12, v12, v214\n\tv_cmp_eq_u32 vcc, -1, v12\n\ts_cbranch_vccnz 104b\n\t"
            "s_branch 204b\n\t105:\ns_mov_b32 s70, 9\n\ts_call_b64 s[72:73], 305f\n\tv_max3_u32 v12, v216, v218, v220\n\tv_max_u32 v12, v12, v222\n\t"
            "v_cmp_eq_u32 vcc, -1, v12\n\ts_cbranch_vccnz 105b\n\ts_branch 205b\n\t106:\ns_mov_b32 s70, 10\n\ts_call_b64 s[72:73], 306f\n\t"
            "v_max3_u32 v12, v224, v226, v228\n\tv_max_u32 v12, v12, v230\n\tv_cmp_eq_u32 vcc, -1, v12\n\ts_cbranch_vccnz 106b\n\ts_branch 206b\n\t107:\n"
            "s_mov_b32 s70, 11\n\ts_call_b64 s[72:73], 307f\n\tv_max3_u32 v12, v232, v234, v236\n\tv_max_u32 v12, v12, v238\n\tv_cmp_eq_u32 vcc, -1, v12\n\ts_cbranch_vccnz 107b\n\t"
            "s_branch 207b\n\t108:\ns_mov_b32 s70, 12\n\ts_call_b64 s[72:73], 308f\n\tv_max3_u32 v12, v176, v178, v180\n\tv_max_u32 v12, v12, v182\n\t"
            "v_cmp_eq_u32 vcc, -1, v12\n\ts_cbranch_vccnz 108b\n\ts_branch 208b\n\t109:\ns_mov_b32 s70, 13\n\ts_call_b64 s[72:73], 309f\n\t"
            "v_max3_u32 v12, v184, v186, v188\n\tv_max_u32 v12, v12, v190\n\tv_cmp_eq_u32 vcc, -1, v12\n\ts_cbranch_vccnz 109b\n\ts_branch 209b\n\t110:\n"
            "s_mov_b32 s70, 14\n\ts_call_b64 s[72:73], 310f\n\tv_max3_u32 v12, v192, v194, v196\n\tv_max_u32 v12, v12, v198\n\tv_cmp_eq_u32 vcc, -1, v12\n\ts_cbranch_vccnz 110b\n\t"
            "s_branch 210b\n\t111:\ns_mov_b32 s70, 15\n\ts_call_b64 s[72:73], 311f\n\tv_max3_u32 v12, v200, v202, v204\n\tv_max_u32 v12, v12, v206\n\t"
            "v_cmp_eq_u32 vcc, -1, v12\n\ts_cbranch_vccnz 111b\n\ts_branch 211b\n\t112:\ns_mov_b32 s70, 16\n\ts_call_b64 s[72:73], 312f\n\t"
            "v_max3_u32 v12, v208, v210, v212\n\tv_max_u32 v12, v12, v214\n\tv_cmp_eq_u32 vcc, -1, v12\n\ts_cbranch_vccnz 112b\n\ts_branch 212b\n\t113:\n"
            "s_mov_b32 s70, 17\n\ts_call_b64 s[72:73], 313f\n\tv_max3_u32 v12, v216, v218, v220\n\tv_max_u32 v12, v12, v222\n\tv_cmp_eq_u32 vcc, -1, v12\n\ts_cbranch_vccnz 113b\n\t"
            "s_branch 213b\n\t114:\ns_mov_b32 s70, 18\n\ts_call_b64 s[72:73], 314f\n\tv_max3_u32 v12, v224, v226, v228\n\tv_max_u32 v12, v12, v230\n\t"
            "v_cmp_eq_u32 vcc, -1, v12\n\ts_cbranch_vccnz 114b\n\ts_branch 214b\n\t115:\ns_mov_b32 s70, 19\n\ts_call_b64 s[72:73], 315f\n\t"
            "v_max3_u32 v12, v232, v234, v236\n\tv_max_u32 v12, v12, v238\n\tv_cmp_eq_u32 vcc, -1, v12\n\ts_cbranch_vccnz 115b\n\ts_branch 215b\n\t116:\n"
            "s_mov_b32 s70, 19\n\ts_call_b64 s[72:73], 316f\n\tv_max3_u32 v12, v176, v178, v180\n\tv_max_u32 v12, v12, v182\n\tv_cmp_eq_u32 vcc, -1, v12\n\ts_cbranch_vccnz 116b\n\t"
            "s_branch 216b\n\t117:\ns_mov_b32 s70, 19\n\ts_call_b64 s[72:73], 317f\n\tv_max3_u32 v12, v184, v186, v188\n\tv_max_u32 v12, v12, v190\n\t"
            "v_cmp_eq_u32 vcc, -1, v12\n\ts_cbranch_vccnz 117b\n\ts_branch 217b\n\t118:\ns_mov_b32 s70, 19\n\ts_call_b64 s[72:73], 318f\n\t"
            "v_max3_u32 v12, v192, v194, v196\n\tv_max_u32 v12, v12, v198\n\tv_cmp_eq_u32 vcc, -1, v12\n\ts_cbranch_vccnz 118b\n\ts_branch 218b\n\t119:\n"
            "s_mov_b32 s70, 19\n\ts_call_b64 s[72:73], 319f\n\tv_max3_u32 v12, v200, v202, v204\n\tv_max_u32 v12, v12, v206\n\tv_cmp_eq_u32 vcc, -1, v12\n\ts_cbranch_vccnz 119b\n\t"
            "s_branch 219b\n\t400:\nv_max3_u32 v12, v176, v178, v180\n\tv_max_u32 v12, v12, v182\n\tv_cmp_eq_u32 vcc, -1, v12\n\ts_cbranch_vccz 11f\n\t"
            "buffer_load_dwordx4 v[176:179], v2, s[44:47], s4 offen offset:1024 sc1\n\tbuffer_load_dwordx4 v[180:183], v2, s[44:47], s4 offen offset:2048 sc1\n\ts_branch 11f\n\t300:\nbuffer_load_dwordx4 v[176:179], v2, s[44:47], s4 offen offset:1024 sc1\n\tbuffer_load_dwordx4 v[180:183], v2, s[44:47], s4 offen offset:2048 sc1\n\t"
            "s_waitcnt vmcnt(2)\n\t11:\ns_cmp_eq_u32 s70, 0\n\ts_cbranch_scc0 401f\n\ts_branch 12f\n\t401:\n"
            "v_max3_u32 v12, v184, v186, v188\n\tv_max_u32 v12, v12, v190\n\tv_cmp_eq_u32 vcc, -1, v12\n\ts_cbranch_vccz 11f\n\tbuffer_load_dwordx4 v[184:187], v2, s[44:47], s5 offen offset:1024 sc1\n\tbuffer_load_dwordx4 v[188:191], v2, s[44:47], s5 offen offset:2048 sc1\n\t"
            "s_branch 11f\n\t301:\nbuffer_load_dwordx4 v[184:187], v2, s[44:47], s5 offen offset:1024 sc1\n\tbuffer_load_dwordx4 v[188:191], v2, s[44:47], s5 offen offset:2048 sc1\n\ts_waitcnt vmcnt(2)\n\t11:\n"
            "s_cmp_eq_u32 s70, 1\n\ts_cbranch_scc0 402f\n\ts_branch 12f\n\t402:\nv_max3_u32 v12, v192, v194, v196\n\tv_max_u32 v12, v12, v198\n\t"
            "v_cmp_eq_u32 vcc, -1, v12\n\ts_cbranch_vccz 11f\n\tbuffer_load_dwordx4 v[192:195], v2, s[44:47], s6 offen offset:1024 sc1\n\tbuffer_load_dwordx4 v[196:199], v2, s[44:47], s6 offen offset:2048 sc1\n\ts_branch 11f\n\t302:\n"
            "buffer_load_dwordx4 v[192:195], v2, s[44:47], s6 offen offset:1024 sc1\n\tbuffer_load_dwordx4 v[196:199], v2, s[44:47], s6 offen offset:2048 sc1\n\ts_waitcnt vmcnt(2)\n\t11:\ns_cmp_eq_u32 s70, 2\n\ts_cbranch_scc0 403f\n\t"
            "s_branch 12f\n\t403:\nv_max3_u32 v12, v200, v202, v204\n\tv_max_u32 v12, v12, v206\n\tv_cmp_eq_u32 vcc, -1, v12\n\ts_cbranch_vccz 11f\n\t"
            "buffer_load_dwordx4 v[200:203], v2, s[44:47], s7 offen offset:1024 sc1\n\tbuffer_load_dwordx4 v[204:207], v2, s[44:47], s7 offen offset:2048 sc1\n\ts_branch 11f\n\t303:\nbuffer_load_dwordx4 v[200:203], v2, s[44:47], s7 offen offset:1024 sc1\n\tbuffer_load_dwordx4 v[204:207], v2, s[44:47], s7 offen offset:2048 sc1\n\t"
            "s_waitcnt vmcnt(2)\n\t11:\ns_cmp_eq_u32 s70, 3\n\ts_cbranch_scc0 404f\n\ts_branch 12f\n\t404:\n"
            "v_max3_u32 v12, v208, v210, v212\n\tv_max_u32 v12, v12, v214\n\tv_cmp_eq_u32 vcc, -1, v12\n\ts_cbranch_vccz 11f\n\tbuffer_load_dwordx4 v[208:211], v2, s[44:47], s8 offen offset:1024 sc1\n\tbuffer_load_dwordx4 v[212:215], v2, s[44:47], s8 offen offset:2048 sc1\n\t"
            "s_branch 11f\n\t304:\nbuffer_load_dwordx4 v[208:211], v2, s[44:47], s8 offen offset:1024 sc1\n\tbuffer_load_dwordx4 v[212:215], v2, s[44:47], s8 offen offset:2048 sc1\n\ts_waitcnt vmcnt(2)\n\t11:\n"
            "s_cmp_eq_u32 s70, 4\n\ts_cbranch_scc0 405f\n\ts_branch 12f\n\t405:\nv_max3_u32 v12, v216, v218, v220\n\tv_max_u32 v12, v12, v222\n\t"
            "v_cmp_eq_u32 vcc, -1, v12\n\ts_cbranch_vccz 11f\n\tbuffer_load_dwordx4 v[216:219], v2, s[44:47], s9 offen offset:1024 sc1\n\tbuffer_load_dwordx4 v[220:223], v2, s[44:47], s9 offen offset:2048 sc1\n\ts_branch 11f\n\t305:\n"
            "buffer_load_dwordx4 v[216:219], v2, s[44:47], s9 offen offset:1024 sc1\n\tbuffer_load_dwordx4 v[220:223], v2, s[44:47], s9 offen offset:2048 sc1\n\ts_waitcnt vmcnt(2)\n\t11:\ns_cmp_eq_u32 s70, 5\n\ts_cbranch_scc0 406f\n\t"
            "s_branch 12f\n\t406:\nv_max3_u32 v12, v224, v226, v228\n\tv_max_u32 v12, v12, v230\n\tv_cmp_eq_u32 vcc, -1, v12\n\ts_cbranch_vccz 11f\n\t"
            "buffer_load_dwordx4 v[224:227], v2, s[44:47], s10 offen offset:1024 sc1\n\tbuffer_load_dwordx4 v[228:231], v2, s[44:47], s10 offen offset:2048 sc1\n\ts_branch 11f\n\t306:\nbuffer_load_dwordx4 v[224:227], v2, s[44:47], s10 offen offset:1024 sc1\n\tbuffer_load_dwordx4 v[228:231], v2, s[44:47], s10 offen offset:2048 sc1\n\t"
            "s_waitcnt vmcnt(2)\n\t11:\ns_cmp_eq_u32 s70, 6\n\ts_cbranch_scc0 407f\n\ts_branch 12f\n\t407:\n"
            "v_max3_u32 v12, v232, v234, v236\n\tv_max_u32 v12, v12, v238\n\tv_cmp_eq_u32 vcc, -1, v12\n\ts_cbranch_vccz 11f\n\tbuffer_load_dwordx4 v[232:235], v2, s[44:47], s11 offen offset:1024 sc1\n\tbuffer_load_dwordx4 v[236:239], v2, s[44:47], s11 offen offset:2048 sc1\n\t"
            "s_branch 11f\n\t307:\nbuffer_load_dwordx4 v[232:235], v2, s[44:47], s11 offen offset:1024 sc1\n\tbuffer_load_dwordx4 v[236:239], v2, s[44:47], s11 offen offset:2048 sc1\n\ts_waitcnt vmcnt(2)\n\t11:\n"
            "s_cmp_eq_u32 s70, 7\n\ts_cbranch_scc0 408f\n\ts_branch 12f\n\t408:\nv_max3_u32 v12, v176, v178, v180\n\tv_max_u32 v12, v12, v182\n\t"
            "v_cmp_eq_u32 vcc, -1, v12\n\ts_cbranch_vccz 11f\n\tbuffer_load_dwordx4 v[176:179], v2, s[44:47], s12 offen offset:1024 sc1\n\tbuffer_load_dwordx4 v[180:183], v2, s[44:47], s12 offen offset:2048 sc1\n\ts_branch 11f\n\t308:\n"
            "buffer_load_dwordx4 v[176:179], v2, s[44:47], s12 offen offset:1024 sc1\n\tbuffer_load_dwordx4 v[180:183], v2, s[44:47], s12 offen offset:2048 sc1\n\ts_waitcnt vmcnt(2)\n\t11:\ns_cmp_eq_u32 s70, 8\n\ts_cbranch_scc0 409f\n\t"
            "s_branch 12f\n\t409:\nv_max3_u32 v12, v184, v186, v188\n\tv_max_u32 v12, v12, v190\n\tv_cmp_eq_u32 vcc, -1, v12\n\ts_cbranch_vccz 11f\n\t"
            "buffer_load_dwordx4 v[184:187], v2, s[44:47], s13 offen offset:1024 sc1\n\tbuffer_load_dwordx4 v[188:191], v2, s[44:47], s13 offen offset:2048 sc1\n\ts_branch 11f\n\t309:\nbuffer_load_dwordx4 v[184:187], v2, s[44:47], s13 offen offset:1024 sc1\n\tbuffer_load_dwordx4 v[188:191], v2, s[44:47], s13 offen offset:2048 sc1\n\t"
            "s_waitcnt vmcnt(2)\n\t11:\ns_cmp_eq_u32 s70, 9\n\ts_cbranch_scc0 410f\n\ts_branch 12f\n\t410:\n"
            "v_max3_u32 v12, v192, v194, v196\n\tv_max_u32 v12, v12, v198\n\tv_cmp_eq_u32 vcc, -1, v12\n\ts_cbranch_vccz 11f\n\tbuffer_load_dwordx4 v[192:195], v2, s[44:47], s4 offen offset:0 sc1\n\tbuffer_load_dwordx4 v[196:199], v2, s[44:47], s4 offen offset:3072 sc1\n\t"
            "s_branch 11f\n\t310:\nbuffer_load_dwordx4 v[192:195], v2, s[44:47], s4 offen offset:0 sc1\n\tbuffer_load_dwordx4 v[196:199], v2, s[44:47], s4 offen offset:3072 sc1\n\ts_waitcnt vmcnt(2)\n\t11:\n"
            "s_cmp_eq_u32 s70, 10\n\ts_cbranch_scc0 411f\n\ts_branch 12f\n\t411:\nv_max3_u32 v12, v200, v202, v204\n\tv_max_u32 v12, v12, v206\n\t"
            "v_cmp_eq_u32 vcc, -1, v12\n\ts_cbranch_vccz 11f\n\tbuffer_load_dwordx4 v[200:203], v2, s[44:47], s5 offen offset:0 sc1\n\tbuffer_load_dwordx4 v[204:207], v2, s[44:47], s5 offen offset:3072 sc1\n\ts_branch 11f\n\t311:\n"
            "buffer_load_dwordx4 v[200:203], v2, s[44:47], s5 offen offset:0 sc1\n\tbuffer_load_dwordx4 v[204:207], v2, s[44:47], s5 offen offset:3072 sc1\n\ts_waitcnt vmcnt(2)\n\t11:\ns_cmp_eq_u32 s70, 11\n\ts_cbranch_scc0 412f\n\t"
            "s_branch 12f\n\t412:\nv_max3_u32 v12, v208, v210, v212\n\tv_max_u32 v12, v12, v214\n\tv_cmp_eq_u32 vcc, -1, v12\n\ts_cbranch_vccz 11f\n\t"
            "buffer_load_dwordx4 v[208:211], v2, s[44:47], s6 offen offset:0 sc1\n\tbuffer_load_dwordx4 v[212:215], v2, s[44:47], s6 offen offset:3072 sc1\n\ts_branch 11f\n\t312:\nbuffer_load_dwordx4 v[208:211], v2, s[44:47], s6 offen offset:0 sc1\n\tbuffer_load_dwordx4 v[212:215], v2, s[44:47], s6 offen offset:3072 sc1\n\t"
            "s_waitcnt vmcnt(2)\n\t11:\ns_cmp_eq_u32 s70, 12\n\ts_cbranch_scc0 413f\n\ts_branch 12f\n\t413:\n"
            "v_max3_u32 v12, v216, v218, v220\n\tv_max_u32 v12, v12, v222\n\tv_cmp_eq_u32 vcc, -1, v12\n\ts_cbranch_vccz 11f\n\tbuffer_load_dwordx4 v[216:219], v2, s[44:47], s7 offen offset:0 sc1\n\tbuffer_load_dwordx4 v[220:223], v2, s[44:47], s7 offen offset:3072 sc1\n\t"
            "s_branch 11f\n\t313:\nbuffer_load_dwordx4 v[216:219], v2, s[44:47], s7 offen offset:0 sc1\n\tbuffer_load_dwordx4 v[220:223], v2, s[44:47], s7 offen offset:3072 sc1\n\ts_waitcnt vmcnt(2)\n\t11:\n"
            "s_cmp_eq_u32 s70, 13\n\ts_cbranch_scc0 414f\n\ts_branch 12f\n\t414:\nv_max3_u32 v12, v224, v226, v228\n\tv_max_u32 v12, v12, v230\n\t"
            "v_cmp_eq_u32 vcc, -1, v12\n\ts_cbranch_vccz 11f\n\tbuffer_load_dwordx4 v[224:227], v2, s[44:47], s8 offen offset:0 sc1\n\tbuffer_load_dwordx4 v[228:231], v2, s[44:47], s8 offen offset:3072 sc1\n\ts_branch 11f\n\t314:\n"
            "buffer_load_dwordx4 v[224:227], v2, s[44:47], s8 offen offset:0 sc1\n\tbuffer_load_dwordx4 v[228:231], v2, s[44:47], s8 offen offset:3072 sc1\n\ts_waitcnt vmcnt(2)\n\t11:\ns_cmp_eq_u32 s70, 14\n\ts_cbranch_scc0 415f\n\t"
            "s_branch 12f\n\t415:\nv_max3_u32 v12, v232, v234, v236\n\tv_max_u32 v12, v12, v238\n\tv_cmp_eq_u32 vcc, -1, v12\n\ts_cbranch_vccz 11f\n\t"
            "buffer_load_dwordx4 v[232:235], v2, s[44:47], s9 offen offset:0 sc1\n\tbuffer_load_dwordx4 v[236:239], v2, s[44:47], s9 offen offset:3072 sc1\n\ts_branch 11f\n\t315:\nbuffer_load_dwordx4 v[232:235], v2, s[44:47], s9 offen offset:0 sc1\n\tbuffer_load_dwordx4 v[236:239], v2, s[44:47], s9 offen offset:3072 sc1\n\t"
            "s_waitcnt vmcnt(2)\n\t11:\ns_cmp_eq_u32 s70, 15\n\ts_cbranch_scc0 416f\n\ts_branch 12f\n\t416:\n"
            "v_max3_u32 v12, v176, v178, v180\n\tv_max_u32 v12, v12, v182\n\tv_cmp_eq_u32 vcc, -1, v12\n\ts_cbranch_vccz 11f\n\tbuffer_load_dwordx4 v[176:179], v2, s[44:47], s10 offen offset:0 sc1\n\tbuffer_load_dwordx4 v[180:183], v2, s[44:47], s10 offen offset:3072 sc1\n\t"
            "s_branch 11f\n\t316:\nbuffer_load_dwordx4 v[176:179], v2, s[44:47], s10 offen offset:0 sc1\n\tbuffer_load_dwordx4 v[180:183], v2, s[44:47], s10 offen offset:3072 sc1\n\ts_waitcnt vmcnt(2)\n\t11:\n"
            "s_cmp_eq_u32 s70, 16\n\ts_cbranch_scc0 417f\n\ts_branch 12f\n\t417:\nv_max3_u32 v12, v184, v186, v188\n\tv_max_u32 v12, v12, v190\n\t"
            "v_cmp_eq_u32 vcc, -1, v12\n\ts_cbranch_vccz 11f\n\tbuffer_load_dwordx4 v[184:187], v2, s[44:47], s11 offen offset:0 sc1\n\tbuffer_load_dwordx4 v[188:191], v2, s[44:47], s11 offen offset:3072 sc1\n\ts_branch 11f\n\t317:\n"
            "buffer_load_dwordx4 v[184:187], v2, s[44:47], s11 offen offset:0 sc1\n\tbuffer_load_dwordx4 v[188:191], v2, s[44:47], s11 offen offset:3072 sc1\n\ts_waitcnt vmcnt(2)\n\t11:\ns_cmp_eq_u32 s70, 17\n\ts_cbranch_scc0 418f\n\t"
            "s_branch 12f\n\t418:\nv_max3_u32 v12, v192, v194, v196\n\tv_max_u32 v12, v12, v198\n\tv_cmp_eq_u32 vcc, -1, v12\n\ts_cbranch_vccz 11f\n\t"
            "buffer_load_dwordx4 v[192:195], v2, s[44:47], s12 offen offset:0 sc1\n\tbuffer_load_dwordx4 v[196:199], v2, s[44:47], s12 offen offset:3072 sc1\n\ts_branch 11f\n\t318:\nbuffer_load_dwordx4 v[192:195], v2, s[44:47], s12 offen offset:0 sc1\n\tbuffer_load_dwordx4 v[196:199], v2, s[44:47], s12 offen offset:3072 sc1\n\t"
            "s_waitcnt vmcnt(2)\n\t11:\ns_cmp_eq_u32 s70, 18\n\ts_cbranch_scc0 419f\n\ts_branch 12f\n\t419:\n"
            "v_max3_u32 v12, v200, v202, v204\n\tv_max_u32 v12, v12, v206\n\tv_cmp_eq_u32 vcc, -1, v12\n\ts_cbranch_vccz 11f\n\tbuffer_load_dwordx4 v[200:203], v2, s[44:47], s13 offen offset:0 sc1\n\tbuffer_load_dwordx4 v[204:207], v2, s[44:47], s13 offen offset:3072 sc1\n\t"
            "s_branch 11f\n\t319:\nbuffer_load_dwordx4 v[200:203], v2, s[44:47], s13 offen offset:0 sc1\n\tbuffer_load_dwordx4 v[204:207], v2, s[44:47], s13 offen offset:3072 sc1\n\ts_waitcnt vmcnt(2)\n\t11:\n"
            "12:\n"
            RINC
            "s_sub_u32 s68, s68, 1\n\ts_cbranch_scc1 9b\n\ts_waitcnt vmcnt(0)\n\ts_setpc_b64 s[72:73]\n\t.subsection 1\n\t.Lce:\n"
            ".subsection 0\n\t.Lout:\ns_waitcnt lgkmcnt(0)\n\t"
            : "=&{s67}"(tag), "=&{v[52:83]}"(sa), "=&{v[84:115]}"(sb), "=&{v[116:123]}"(sc) : [t] "v"(tid), [b] "s"(bid), [k] "s"(ka)
            : "memory", "scc", "vcc", "s4", "s5", "s6", "s7", "s8", "s9", "s10", "s11", "s12", "s13", "s14", "s15", "s16", "s17", "s18", "s19", "s20", "s21", "s22", "s23", "s24", "s25", "s26", "s27", "s28", "s29", "s30", "s31", "s32", "s33", "s34", "s35", "s36", "s37", "s38", "s39", "s40", "s41", "s42", "s43", "s44", "s45", "s46", "s47", "s48", "s49", "s50", "s51", "s52", "s53", "s54", "s55", "s56", "s57", "s58", "s59", "s60", "s61", "s62", "s63", "s64", "s65", "s66", "s68", "s69", "s70", "s71", "s72", "s73", "s74", "s75", "s76", "s77", "s78", "s79", "s80", "s81", "s82", "s83", "s84", "s85", "s86", "s87", "s88", "s89", "s90", "s91", "s92", "s93", "s94", "s95", "s96", "s97", "s98", "s99", "s100", "s101", "v2", "v3", "v4", "v5", "v6", "v7", "v8", "v9", "v10", "v11", "v12", "v13", "v14", "v15", "v16", "v17", "v18", "v19", "v20", "v21", "v22", "v23", "v24", "v25", "v26", "v27", "v28", "v29", "v30", "v31", "v32", "v33", "v34", "v35", "v36", "v37", "v38", "v39", "v40", "v41", "v42", "v43", "v44", "v45", "v46", "v47", "v48", "v49", "v50", "v51", "v124", "v125", "v126", "v127", "v128", "v129", "v130", "v131", "v132", "v133", "v134", "v135", "v136", "v137", "v138", "v139", "v140", "v141", "v142", "v143", "v144", "v145", "v146", "v147", "v148", "v149", "v150", "v151", "v152", "v153", "v154", "v155", "v156", "v157", "v158", "v159", "v160", "v161", "v162", "v163", "v164", "v165", "v166", "v167", "v168", "v169", "v170", "v171", "v172", "v173", "v174", "v175", "v176", "v177", "v178", "v179", "v180", "v181", "v182", "v183", "v184", "v185", "v186", "v187", "v188", "v189", "v190", "v191", "v192", "v193", "v194", "v195", "v196", "v197", "v198", "v199", "v200", "v201", "v202", "v203", "v204", "v205", "v206", "v207", "v208", "v209", "v210", "v211", "v212", "v213", "v214", "v215", "v216", "v217", "v218", "v219", "v220", "v221", "v222", "v223", "v224", "v225", "v226", "v227", "v228", "v229", "v230", "v231", "v232", "v233", "v234", "v235", "v236", "v237", "v238", "v239", "v240", "v241", "v242", "v243", "v244", "v245", "v246", "v247", "v248", "v249", "v250", "v251", "v252", "v253", "v254", "v255");
    tag = RFL(tag);
    asm volatile("" : "+s"(bid), "+s"(ka)); asm volatile("" : "+v"(tid));
    const u16 *x = (const u16*)ka[0], *wo = (const u16*)ka[2], *w_r = (const u16*)ka[3], *wg = (const u16*)ka[4], *wu = (const u16*)ka[5], *wd = (const u16*)ka[6]; char* ws = (char*)ka[7];
    const int wv = __builtin_amdgcn_readfirstlane(tid >> 6);
    const unsigned lds0 = (unsigned)(size_t)(lchar*)smem;
#define LANE() const int lane = lane_id(); const int g = lane >> 4, li = lane & 15, jq = 4 * (li & 3) + (li >> 2); (void)jq
    const int xcd = bid & 7, j = ((bid >> 3) + 25) & 31;
    const bool gemm = j < 25;
    const int fp = xcd >> 1, bl = (j * 13) >> 6, cl = j - bl * 5;
    const int rb = fp * 5 + bl, cg = (xcd & 1) * 5 + cl;
    const int par = fp >> 1, I0 = ((fp & 1) * 5 + bl) * 104 - 8;
    const __amdgpu_buffer_rsrc_t rs = __builtin_amdgcn_make_buffer_rsrc((void*)ws, 0, W_END, 0x00020000);
    auto wait_flag = [&](unsigned a) { SPIN { if (RFL(ldsrv<unsigned>(a))) break; __builtin_amdgcn_s_sleep(1); } MEMB(); };
    auto all4 = [&](u32x4 c) { return RFL(min(min(c.x, c.y), min(c.z, c.w))); };
    unsigned ep = 0;
    auto epld = [&]() { asm volatile("s_load_dword %0, %1, 0x0" : "=s"(ep) : "s"(ws) : "memory"); };
    auto mktag = [&]() { asm volatile("s_waitcnt lgkmcnt(0)" : "+s"(ep) :: "memory"); tag = ep * 1664525u + 1013904223u; };
        if (gemm) {
            LANE();
            wait_flag(lds0 + FL_W);
            const u32x4 kk = ldsrv<u32x4>(lds0 + L_KEY + lane * 16);
            const uint2 t2 = top2wave(lane < 52 ? kk.x : 0u, lane < 52 ? kk.z : 0u);
            _Pragma("nounroll") for (int k = 0; k < 2; ++k) {
                const unsigned key = k ? t2.y : t2.x, rr = (0xffffu - (key & 0xffffu)) & 2047u; const int pos = (int)(rr >> 1) - I0;
                if (__builtin_expect((int)(rr & 1) == par && pos >= 8 && pos < 112, 0)) {
                    const float sg = rbf(__builtin_amdgcn_rcpf(1.f + __builtin_amdgcn_exp2f(keyscore(key) * -1.4426950408889634f)));
                    const int m = (pos & ~15) + 4 * (pos & 3) + ((pos >> 2) & 3);
                    const float xr_ = lo2f(x[rr * 640 + cg * 64 + lane]), nm = ldsrv<float>(lds0 + L_NM + m * 4), rsj = ldsrv<float>(lds0 + L_RS + pos * 4);
                    u32x2 y[5];
                    SPIN {
                        MEMB();
                        bool old = false;
#pragma unroll
                        for (int i = 0; i < 5; ++i) y[i] = __builtin_amdgcn_raw_buffer_load_b64(rs, W_Y + (cg * 64 + lane) * 16 + k * 8, i * 10240, SC1);
#pragma unroll
                        for (int i = 0; i < 5; ++i) old = old || y[i].y != tag;
                        if (__builtin_amdgcn_ballot_w64(old) == 0) break;
                        __builtin_amdgcn_s_sleep(YS2);
                    }
                    const float ys = ((__uint_as_float(y[0].x) + __uint_as_float(y[1].x)) + (__uint_as_float(y[2].x) + __uint_as_float(y[3].x))) + __uint_as_float(y[4].x);
                    const float xn = rbf(rbf(xr_ + nm) * rsj), u = rbf(rbf(ys) * sg);
                    ldsw<float>(lds0 + L_RG + k * 256 + lane * 4, silu_b(rbf(xn + u)) * -1.4426950408889634f);
                    ldsw<unsigned>(lds0 + L_RM + m * 4, L_RG + k * 256);
                }
            }
            ldsw<unsigned>(lds0 + FL_R, 1u);
            END();
        }
    {
        TL(0);
        LANE();
        const int fq = (j - 25) * 2 + (xcd & 1), f = fp * 14 + fq;
        const int n0 = fq * 40 + wv * 4 + g;
        epld();
        if (wv == 7 && lane < 16) ldsw<u32x4>(lds0 + MISC + lane * 16, (u32x4){0u, 0u, 0u, 0u});
        const int u2 = max(f - 6, 0), kr = (u2 * 26) >> 8, fc = u2 - kr * 10, n1 = wv < 4 ? 3 : 2;
        const int d0 = kr * 11 + wv, d1 = kr * 11 + 4 + wv; const bool two = wv >= 4 && wv < 7 && d1 < 54;
        const char* w1 = (const char*)(f & 1 ? wu : wg) + min(f >> 1, 26) * 128; const char* w2 = (const char*)wd + fc * 128;
        unsigned fv[2][4];
#pragma unroll
        for (int i = 0; i < 4; ++i) { const unsigned c = (((lane & 7) ^ (2 * ((lane >> 4) & 1) + 4 * (i & 1))) * 16) - i * 1024; fv[0][i] = ((lane >> 3) + i * 8) * (F_ * 2) + c; fv[1][i] = ((lane >> 3) + i * 8) * 1280 + c; }
        auto issue = [&](int m, int ks, int slot) {
            asm volatile("s_mov_b32 m0, %[l]\n\ts_nop 0\n\tglobal_load_lds_dwordx4 %0, %[b]" FPOL "\n\tglobal_load_lds_dwordx4 %1, %[b] offset:1024" FPOL "\n\tglobal_load_lds_dwordx4 %2, %[b] offset:2048" FPOL "\n\tglobal_load_lds_dwordx4 %3, %[b] offset:3072" FPOL
                         :: "v"(fv[m][0]), "v"(fv[m][1]), "v"(fv[m][2]), "v"(fv[m][3]), [b] "s"((m ? w2 : w1) + (unsigned)(ks * 32 * (m ? 1280 : F_ * 2))), [l] "s"(lds0 + wv * 16384 + slot * 4096) : "memory");
        };
        if (TCH && wv == 5) {
            const int l = min(f * 14 + lane, 770), o = l < 280 ? W_KEY + l * 128 : l < 536 ? W_ST + (l - 280) * 128 : W_Y + (l - 536) * 256;
            if (lane < 14) asm volatile("s_mov_b32 m0, %[l]\n\ts_nop 0\n\tbuffer_load_dword %0, %1, 0 offen lds" :: "v"(o), "s"(rs), [l] "s"(lds0 + L_DUM) : "memory");
        }
        if (QPF && wv >= 8 - QPF) {
            const int i = min(f * 172 + (7 - wv) * 64 + lane, 9599), rw = (i * 34953) >> 20;
            asm volatile("s_mov_b32 m0, %[l]\n\ts_nop 0\n\tglobal_load_lds_dword %0, %[b]" :: "v"(i * 128 + ((rw >> 6) + 1) * 245760), [b] "s"(ka[1]), [l] "s"(lds0 + L_DUM) : "memory");
        }
        BARN();
        auto wopf = [&]() {
            const int i = min(((j - 25) * 8 + wv) * 58 + lane, 3199), rw = (i * 13108) >> 16;
            asm volatile("s_mov_b32 m0, %[l]\n\ts_nop 0\n\tglobal_load_lds_dword %0, %[b]" :: "v"(rw * 640 + i * 128 + (xcd & 1) * 640), [b] "s"(wo), [l] "s"(lds0 + L_DUM) : "memory"); };
        if (WPF == 1) wopf();
        if (EARLYK) issue(0, wv, 0);
        asm volatile("s_waitcnt vmcnt(%2)" : "+{v[84:115]}"(sb), "+{v[116:123]}"(sc) : "n"(5 + 4 * EARLYK + (WPF == 1)) : "memory");
#define SXR(i) ((i) < 20 ? sa[(12 + (i)) & 31] : (i) < 52 ? sb[((i) - 20) & 31] : sc[((i) - 52) & 7])
        mktag();
        {
            const unsigned one2 = 0x3F803F80u;
            float wsum = 0.f;
#pragma unroll
            for (int e = 0; e < 20; ++e) wsum = dot2(SXR(40 + e), one2, wsum);
            wsum = sum16(wsum);
            auto stat = [&](int p) {
                float s1 = 0.f, s2 = 0.f, sr = 0.f;
#pragma unroll
                for (int e = 0; e < 20; ++e) { const unsigned u = SXR(20 * p + e); s1 = dot2(u, one2, s1); s2 = dot2(u, u, s2); sr = dot2(u, SXR(40 + e), sr); }
                const float mean = sum16(s1) * (1.f / 640.f);
                const unsigned mb2 = cvtpk(mean, mean); const float mb = lo2f(mb2);
                const float q = fmaf(40.f * mb, mb, fmaf(-2.f * mb, s1, s2));
                const float dm = mean - mb;
                const float var = fmaxf(sum16(q) * (1.f / 640.f) - dm * dm, 0.f);
                const float sd = rbf(__builtin_amdgcn_sqrtf(rbf(rbf(var) + 1e-5f)));
                const float rsc = rbf(fmaf(-mb, wsum, sum16(sr)) / sd);
                const int n = n0 + 32 * p, i = (fp & 1) * 512 + n;
                if (li == 0) {
                    asm volatile("buffer_store_dwordx4 %0, %1, %2, 0 offen sc1\n\ts_nop 1" :: "v"((u32x4){(mb2 << 16) ^ 0x80000000u, tag, __float_as_uint(__builtin_amdgcn_rcpf(sd)), tag}), "v"(n < 512 ? W_ST + (par * 1024 + i) * 16 : 0x40000000), "s"(rs) : "memory");
                    ldsw<unsigned>(lds0 + LS_ROW + (p * 32 + wv * 4 + g) * 4, n < 512 ? rkey(rsc, 2 * i + par) : 0u);
                }
            };
            asm volatile("" : "+v"(wsum));
            if (wv < 2) stat(1);
            asm volatile("s_cmp_lt_u32 %2, 2\n\ts_cbranch_scc1 1f\n\ts_waitcnt vmcnt(%1)\n1:\n\ts_waitcnt vmcnt(%1 + 1)" : "+{v[52:83]}"(sa) : "n"(4 * EARLYK + (WPF == 1)), "s"(wv) : "memory", "scc");
            TL(1);
            if (EK2 && wv < EK2) issue(0, wv, 0);
            stat(0);
            if (E2 && wv < 7) issue(0, wv + 8, 1);
        }
        BAR();
        if (wv == 6) asm volatile("buffer_store_dword %0, %1, %2, 0 offen sc1" :: "v"(tag), "v"(lane ? 0x40000000 : W_ACK + (f < 40 ? (f >> 1) * 128 + 40 + (f & 1) * 4 : 2560 + (f - 40) * 4)), "s"(rs) : "memory");
        if (wv == 7) {
            const unsigned k = ldsrv<unsigned>(lds0 + LS_ROW + min(lane, 39) * 4);
            const uint2 t2 = top2wave(lane < 40 ? k : 0u, 0u);
            if (lane == 0 && fq < 13) asm volatile("buffer_store_dwordx4 %0, %1, %2, 0 offen sc1\n\ts_nop 1" :: "v"((u32x4){t2.x, tag, t2.y, tag}), "v"(W_KEY + (fp * 13 + fq) * 16), "s"(rs) : "memory");
        }
        BARN();
        TL(2);
        if (WPF == 2) wopf();
        if (!EARLYK && wv >= EK2) issue(0, wv, 0);
        if (!E2 || wv == 7) issue(0, wv + 8, 1);
        if (wv < 4) issue(0, wv + 16, 2);
        else if (!WPF) asm volatile("s_mov_b32 m0, %[l]\n\ts_mov_b32 exec_hi, 0\n\tglobal_load_lds_dword %0, %[b]\n\ts_mov_b32 exec_hi, -1" :: "v"(min(f * 128 + (wv - 4) * 32 + lane, 6399) * 128), [b] "s"(wo), [l] "s"(lds0 + L_DUM) : "memory");
        if (wv >= 4 && wv < 4 + P2A) issue(1, d0, 3);
        u32x4 kk;
        BARN();
        asm volatile("s_cmp_lg_u32 %3, 7\n\ts_cbranch_scc1 1f\n\ts_sleep " KSL "\n\tbuffer_load_dwordx4 %0, %1, %2, 0 offen sc1\n1:\n\ts_barrier" : "=&v"(kk) : "v"(W_KEY + min(lane, 51) * 16), "s"(rs), "s"(wv) : "memory", "scc");
        if (wv != 7 && !(wv >= 4 && wv < 4 + P2A)) issue(1, d0, 3);
        if (P2A && two) issue(1, d1, 2);
        asm volatile("s_cmp_lg_u32 %1, 7\n\ts_cbranch_scc1 1f\n\ts_waitcnt vmcnt(0)\n1:" : "+v"(kk) : "s"(wv) : "memory", "scc");
        if (wv == 7) {
            SPIN {
                if (__builtin_amdgcn_ballot_w64(kk.y != tag || kk.w != tag) == 0) break;
                asm volatile("buffer_load_dwordx4 %0, %1, %2, 0 offen sc1\n\ts_waitcnt vmcnt(0)" : "=&v"(kk) : "v"(W_KEY + min(lane, 51) * 16), "s"(rs) : "memory");
            }
            const uint2 t2 = top2wave(lane < 52 ? kk.x : 0u, lane < 52 ? kk.z : 0u);
            if (lane < 2) ldsw<unsigned>(lds0 + ROUT + lane * 8, (0xffffu - ((lane ? t2.y : t2.x) & 0xffffu)) & 2047u);
        }
        BAR();
        TL(3);
        const unsigned row = ldsrv<unsigned>(lds0 + ROUT + (li & 1) * 8);
        u32x4 af[3], st;
        asm volatile("global_load_dwordx4 %0, %4, %5\n\tglobal_load_dwordx4 %1, %4, %5 offset:512\n\tglobal_load_dwordx4 %2, %6, %5 offset:1024\n\tbuffer_load_dwordx4 %3, %7, %8, 0 offen sc1"
                     : "=&v"(af[0]), "=&v"(af[1]), "=&v"(af[2]), "=&v"(st) : "v"(row * 1280u + wv * 64 + g * 16), "s"(x), "v"(row * 1280u + (wv & 3) * 64 + g * 16), "v"(wv ? 0x40000000 : W_ST + ((row & 1) * 1024 + (row >> 1)) * 16), "s"(rs) : "memory");
        bf16x8 wf[3][4];
        unsigned ra[4];
#pragma unroll
        for (int q = 0; q < 4; ++q) ra[q] = lds0 + wv * 16384 + (unsigned)((8 * g + (li >> 2)) * 128 + (((q ^ (((li >> 3) & 1) + 2 * (g & 1))) * 32) + (li & 3) * 8));
        auto rdf = [&](int k, int slot) {
#pragma unroll
            for (int q = 0; q < 4; ++q) wf[k][q] = cat8(trread(ra[q] + slot * 4096), trread(ra[q] + slot * 4096 + 512));
        };
        if (wv == 7) issue(1, d0, 3);
        if (!P2A && two) issue(1, d1, 2);
        if (two) wvm<12>(); else wvm<8>();
        rdf(0, 0); rdf(1, 1); if (wv < 4) rdf(2, 2);
        asm volatile("s_cmp_eq_u32 %4, 0\n\ts_cbranch_scc0 1f\n\ts_waitcnt vmcnt(0)\n1:\n\ts_waitcnt vmcnt(4)" : "+v"(af[0]), "+v"(af[1]), "+v"(af[2]), "+v"(st) : "s"(RFL((!P2A && two) || wv == 7)) : "memory", "scc");
        f32x4 c[4];
#pragma unroll
        for (int q = 0; q < 4; ++q) c[q] = (f32x4){0.f, 0.f, 0.f, 0.f};
#pragma unroll
        for (int k = 0; k < 3; ++k) if (k < n1) {
#pragma unroll
            for (int e = 0; e < 4; ++e) af[k][e] = li & 2 ? 0x3F803F80u : af[k][e];
#pragma unroll
            for (int q = 0; q < 4; ++q) c[q] = mfma32(__builtin_bit_cast(bf16x8, af[k]), wf[k][q], c[q]);
        }
        auto total = [&](int off) {
            ldsw<f32x4>(lds0 + wv * 16384 + off + lane * 16, g == 0 ? c[0] : g == 1 ? c[1] : g == 2 ? c[2] : c[3]);
            BAR();
            if (wv) return c[0];
            f32x4 pr[8];
#pragma unroll
            for (int w = 0; w < 8; ++w) pr[w] = ldsrv<f32x4>(lds0 + w * 16384 + off + lane * 16);
            return ((pr[0] + pr[1]) + (pr[2] + pr[3])) + ((pr[4] + pr[5]) + (pr[6] + pr[7]));
        };
        TL(4);
        {
            const f32x4 s = total(0);
            if (wv == 0 && f < 54) {
                SPIN {
                    if (__builtin_amdgcn_ballot_w64(st.y != tag || st.w != tag) == 0) break;
                    asm volatile("buffer_load_dwordx4 %0, %1, %2, 0 offen sc1\n\ts_waitcnt vmcnt(0)" : "=&v"(st) : "v"(W_ST + ((row & 1) * 1024 + (row >> 1)) * 16), "s"(rs) : "memory");
                }
                const float n0_ = __uint_as_float(__builtin_amdgcn_readlane(st.x, 0)), n1_ = __uint_as_float(__builtin_amdgcn_readlane(st.x, 1)), i0 = __uint_as_float(__builtin_amdgcn_readlane(st.z, 0)), i1 = __uint_as_float(__builtin_amdgcn_readlane(st.z, 1));
                float v0 = rbf(fmaf(n0_, s.z, s.x) * i0), v1 = rbf(fmaf(n1_, s.z, s.y) * i1);
                if (!(f & 1)) { v0 = silu_b(v0); v1 = silu_b(v1); }
                const unsigned hb = cvtpk(v0, v1), nb = dppu<0xB1>(hb);
                const unsigned pay = (lane & 1) ? __builtin_amdgcn_perm(hb, nb, 0x07060302u) : __builtin_amdgcn_perm(nb, hb, 0x05040100u);
                asm volatile("buffer_store_dwordx2 %0, %1, %2, 0 offen sc1" :: "v"((u32x2){pay, tag}), "v"(W_H + ((f & 1) * 2 + (lane & 1)) * 6912 + ((f >> 1) * 32 + (lane >> 1)) * 8), "s"(rs) : "memory");
            }
        }
        TL(5);
        if (f == 0 && wv == 1) {
            SPIN {
                u32x4 a_;
                asm volatile("buffer_load_dwordx4 %0, %1, %2, 0 offen sc1\n\ts_waitcnt vmcnt(0)" : "=&v"(a_) : "v"(W_ACK + (lane < 60 ? ((lane * 43) >> 7) * 80 + lane * 16 : 1600 + lane * 16)), "s"(rs) : "memory");
                if (__builtin_amdgcn_ballot_w64(a_.x != tag || a_.y != tag || a_.z != tag || a_.w != tag) == 0) break;
                __builtin_amdgcn_s_sleep(8);
            }
            if (lane == 0) __builtin_amdgcn_raw_buffer_store_b32(tag, rs, W_EP, 0, SC1);
        }
        if (f >= 6) {
            wvm<0>();
            rdf(0, 3); rdf(1, 2);
            const int ho = W_H + ((lane >> 4) & 1) * 6912 + ((lane >> 5 && two ? d1 : d0) * 16 + li) * 8;
            u32x2 hg, hu;
            __builtin_amdgcn_s_sleep(HSL);
            SPIN {
                asm volatile("buffer_load_dwordx2 %0, %2, %3, 0 offen sc1\n\tbuffer_load_dwordx2 %1, %2, %3, %4 offen sc1\n\ts_waitcnt vmcnt(0)" : "=&v"(hg), "=&v"(hu) : "v"(ho), "s"(rs), "s"(13824) : "memory");
                if (__builtin_amdgcn_ballot_w64(hg.y != tag || hu.y != tag) == 0) break;
                __builtin_amdgcn_s_sleep(HS2);
            }
            ldsw<unsigned>(lds0 + wv * 16384 + 4096 + lane * 4, cvtpk(lo2f(hg.x) * lo2f(hu.x), hi2f(hg.x) * hi2f(hu.x)));
#pragma unroll
            for (int q = 0; q < 4; ++q) c[q] = (f32x4){0.f, 0.f, 0.f, 0.f};
#pragma unroll
            for (int k = 0; k < 2; ++k) if (k == 0 || two) {
                const u32x4 h = ldsrv<u32x4>(lds0 + wv * 16384 + 4096 + k * 128 + (li & 1) * 64 + g * 16);
#pragma unroll
                for (int q = 0; q < 4; ++q) c[q] = mfma32(__builtin_bit_cast(bf16x8, h), wf[k][q], c[q]);
            }
            const f32x4 ys = total(1024);
            if (wv == 0) __builtin_amdgcn_raw_buffer_store_b128((u32x4){__float_as_uint(ys.x), tag, __float_as_uint(ys.y), tag}, rs, W_Y + kr * 10240 + (fc * 64 + lane) * 16, 0, SC1);
        }
        TL(6);
        TLEND();
        wvm<0>();
    }
}

extern "C" void kernel_launch(void* const* d_in, const int* in_sizes, int n_in, void* d_out, int out_size, void* d_ws,
                              size_t ws_size, hipStream_t stream) {
    (void)hipFuncSetAttribute((const void*)fused, hipFuncAttributeMaxDynamicSharedMemorySize, LDSZ);
    fused<<<256, 512, LDSZ, stream>>>((const u16*)d_in[0], (const u16*)d_in[1], (const u16*)d_in[2], (const u16*)d_in[4],
                                      (const u16*)d_in[5], (const u16*)d_in[6], (const u16*)d_in[7], (char*)d_ws, (u16*)d_out);
}
```

```cpp
#include <hip/hip_runtime.h>
#include <type_traits>

typedef __attribute__((ext_vector_type(8))) short bf16x8;
typedef __attribute__((ext_vector_type(4))) short bf16x4;
typedef __attribute__((ext_vector_type(4))) float f32x4;
typedef __attribute__((ext_vector_type(2))) float f32x2;
typedef __attribute__((ext_vector_type(4))) unsigned u32x4;
typedef __attribute__((ext_vector_type(2))) unsigned u32x2;
typedef __attribute__((ext_vector_type(2))) __bf16 bf16x2v;
typedef __attribute__((ext_vector_type(8))) unsigned u32x8;
typedef __attribute__((ext_vector_type(16))) unsigned u32x16;
typedef unsigned short u16;
typedef __attribute__((address_space(3))) char lchar;

#define F_ 1728
constexpr int W_EP = 0;
constexpr int W_KEY = 4096;
constexpr int W_ACK = 8192;
constexpr int W_H = 12288;
constexpr int W_ST = 65536;
constexpr int W_Y = 98304;
constexpr int W_O = 155648;
constexpr int W_END = W_O + 80 * 40960;
#define SENT 0xffffffffu
constexpr int XS = 14336, SLOT = XS + 24576;
constexpr int WO0 = 2 * SLOT;
constexpr int MISC = WO0 + 81920;
constexpr int FL_S = MISC, FL_P = MISC + 16, FL_E = MISC + 32, FL_W = MISC + 48, FL_R = MISC + 64, FL_AUX = MISC + 80, ONES = MISC + 128, ROUT = MISC + 144, LTAG = MISC + 160;
constexpr int L_NM = MISC + 256, L_RS = L_NM + 512 + 64, L_RM = L_RS + 512, L_RG = L_RM + 512, L_DUM = L_RG + 512, L_KEY = L_DUM + 512;
constexpr int LS_ROW = L_NM;
constexpr int LDSZ = 163840;
static_assert(L_KEY + 1024 <= LDSZ, "lds");
#define SPIN_MAX (1 << 21)
#define SPIN _Pragma("nounroll") for (int it = 0; it < SPIN_MAX; ++it)

__device__ __forceinline__ unsigned cvtpk(float lo, float hi) { return __builtin_bit_cast(unsigned, __builtin_convertvector((f32x2){lo, hi}, bf16x2v)); }
__device__ __forceinline__ unsigned nosent(unsigned u) { return u < 0xfffffffeu ? u : 0xfffffffeu; }
__device__ __forceinline__ float lo2f(unsigned u) { return __uint_as_float(u << 16); }
__device__ __forceinline__ float hi2f(unsigned u) { return __uint_as_float(u & 0xffff0000u); }
__device__ __forceinline__ float rbf(float f) { return lo2f(cvtpk(f, f)); }
template <int CTRL> __device__ __forceinline__ float dppf(float x) { return __builtin_bit_cast(float, __builtin_amdgcn_mov_dpp(__builtin_bit_cast(int, x), CTRL, 0xf, 0xf, true)); }
template <int CTRL> __device__ __forceinline__ unsigned dppu(unsigned x) { return (unsigned)__builtin_amdgcn_mov_dpp((int)x, CTRL, 0xf, 0xf, true); }
__device__ __forceinline__ void sw16(unsigned& a, unsigned& b) { const auto r = __builtin_amdgcn_permlane16_swap(a, b, false, false); a = r[0]; b = r[1]; }
__device__ __forceinline__ void sw32(unsigned& a, unsigned& b) { const auto r = __builtin_amdgcn_permlane32_swap(a, b, false, false); a = r[0]; b = r[1]; }
__device__ __forceinline__ void sw16f(float& a, float& b) { unsigned x = __float_as_uint(a), y = __float_as_uint(b); sw16(x, y); a = __uint_as_float(x); b = __uint_as_float(y); }
__device__ __forceinline__ void sw32f(float& a, float& b) { unsigned x = __float_as_uint(a), y = __float_as_uint(b); sw32(x, y); a = __uint_as_float(x); b = __uint_as_float(y); }
__device__ __forceinline__ float vmax(float a, float b) { return __builtin_elementwise_maximum(a, b); }
__device__ __forceinline__ float sum16(float x) { x += dppf<0xB1>(x); x += dppf<0x4E>(x); x += dppf<0x141>(x); x += dppf<0x140>(x); return x; }
__device__ __forceinline__ float dot2(unsigned a, unsigned b, float c) { return __builtin_amdgcn_fdot2_f32_bf16(__builtin_bit_cast(bf16x2v, a), __builtin_bit_cast(bf16x2v, b), c, false); }
__device__ __forceinline__ float silu_b(float v) { return v * __builtin_amdgcn_rcpf(__builtin_amdgcn_exp2f(v * -1.4426950408889634f) + 1.f); }
__device__ __forceinline__ unsigned rkey(float rb, int row) {
    unsigned b = __float_as_uint(rb) >> 16; b = (b & 0x8000u) ? (~b & 0xffffu) : (b | 0x8000u);
    return (b << 16) | (unsigned)(0xffff - row);
}
__device__ __forceinline__ float keyscore(unsigned k) { const unsigned b = k >> 16; return __uint_as_float(((b & 0x8000u) ? (b & 0x7fffu) : (~b & 0xffffu)) << 16); }
#define T2M(b0, b1) { const unsigned _b0 = (b0), _b1 = (b1); const unsigned _m = min(k0, _b0); k0 = max(k0, _b0); k1 = max(_m, max(k1, _b1)); }
__device__ __forceinline__ uint2 top2wave(unsigned k0, unsigned k1) {
    T2M(dppu<0xB1>(k0), dppu<0xB1>(k1)); T2M(dppu<0x4E>(k0), dppu<0x4E>(k1));
    T2M(dppu<0x141>(k0), dppu<0x141>(k1)); T2M(dppu<0x140>(k0), dppu<0x140>(k1));
    const unsigned a0 = __builtin_amdgcn_readlane(k0, 0), a1 = __builtin_amdgcn_readlane(k1, 0);
    const unsigned c0 = __builtin_amdgcn_readlane(k0, 16), c1 = __builtin_amdgcn_readlane(k1, 16);
    const unsigned d0 = __builtin_amdgcn_readlane(k0, 32), d1 = __builtin_amdgcn_readlane(k1, 32);
    const unsigned e0 = __builtin_amdgcn_readlane(k0, 48), e1 = __builtin_amdgcn_readlane(k1, 48);
    k0 = a0; k1 = a1; T2M(c0, c1); T2M(d0, d1); T2M(e0, e1);
    return make_uint2(k0, k1);
}
template <int N> __device__ __forceinline__ void wvm() { asm volatile("" ::: "memory"); __builtin_amdgcn_s_waitcnt((N & 15) | ((N >> 4) << 14) | 0x0F70); asm volatile("" ::: "memory"); }
__device__ __forceinline__ bf16x4 trread(unsigned a) { return __builtin_amdgcn_ds_read_tr16_b64_v4i16((__attribute__((address_space(3))) bf16x4*)(size_t)a); }
template <class T> __device__ __forceinline__ T ldsr(unsigned a) { return *(const __attribute__((address_space(3))) T*)(size_t)a; }
template <class T> __device__ __forceinline__ T ldsrv(unsigned a) { return *(volatile __attribute__((address_space(3))) T*)(size_t)a; }
template <class T> __device__ __forceinline__ void ldsw(unsigned a, T v) { *(volatile __attribute__((address_space(3))) T*)(size_t)a = v; }
__device__ __forceinline__ bf16x8 cat8(bf16x4 lo, bf16x4 hi) { return (bf16x8){lo[0], lo[1], lo[2], lo[3], hi[0], hi[1], hi[2], hi[3]}; }
__device__ __forceinline__ bf16x8 frag4(unsigned a, unsigned b, unsigned c, unsigned d) { return __builtin_bit_cast(bf16x8, (u32x4){a, b, c, d}); }
__device__ __forceinline__ f32x4 mfma32(bf16x8 a, bf16x8 b, f32x4 c) { return __builtin_amdgcn_mfma_f32_16x16x32_bf16(a, b, c, 0, 0, 0); }
__device__ __forceinline__ int lane_id() { int l; asm volatile("v_mbcnt_lo_u32_b32 %0, -1, 0\n\tv_mbcnt_hi_u32_b32 %0, -1, %0" : "=v"(l)); __builtin_assume(l >= 0 && l < 64); return l; }
#define SB() __builtin_amdgcn_sched_barrier(0)
#define BAR() asm volatile("s_waitcnt lgkmcnt(0)\n\ts_barrier" ::: "memory")
#define MEMB() asm volatile("" ::: "memory")
#define RFL(x) ((unsigned)__builtin_amdgcn_readfirstlane((int)(x)))
#define SC1 16

#define WOPF 1
#define DSEL "0"
#define TRG1D
#define TRG2D
#define PRIA "s_setprio 2\n\t"
#define TCHD
#define RSLP "s_sleep 3\n\t"
#define PRIR "s_setprio 0\n\t"
#define PRI1 "s_setprio 3\n\t"
#define PRIW
#define PRIX "s_setprio 3\n\t"
#define PRIO
#define RSL0 "s_sleep 6\n\t"
#define RSL1 "s_sleep 3\n\t"
#define ASLP "s_sleep 1\n\t"
#define PRIS
#define PRIG "s_setprio 0\n\t"
#define PSLP "s_sleep 1\n\t"
#define HSL 26
#define HS2 10
#define TCH 1
#define KSL "0"
#define E2 0
#define P2A 0
#define FPOL " nt"
#define EK2 7
#define YS2 0
#define QPF 3
#define QW "0xf3"
#define YSL 8
#define KE 0
#define PFH "0"
#define RP0 0
#define RP1 3
#define WPF 0
#define EARLYK 0
#define NOPK(v) asm("" : "+v"(v))
#define OPQ(v) asm volatile("" : "+v"(v))
#define IB(m) __builtin_amdgcn_inverse_ballot_w64(m)
#define END() do { asm volatile("s_endpgm"); __builtin_amdgcn_s_waitcnt(0); return; } while (0)
#define BARN() asm volatile("s_barrier" ::: "memory")
#define TL(i)
#define TA(i)
#define TLEND()
__device__ __forceinline__ constexpr int phys(int S) { return (S + 1) % 3; }

__global__ __launch_bounds__(512, 2) void fused(const u16* x_, const u16* wqkv_, const u16* wo_, const u16* w_r_, const u16* wg_, const u16* wu_, const u16* wd_, char* ws_, u16* out_) {
    extern __shared__ __attribute__((aligned(16))) char smem[];
#define SL(k)
#define DUMP
#define SZ
#define SB_(k)
#define SO(k)
#define RINC
#define RZ
#define SA(k)
#define ST(h)
#define SP0
#define LINC
#define GINC
#define RCNT
    typedef __attribute__((ext_vector_type(32))) unsigned u32x32;
    u32x32 sa, sb; u32x8 sc;
    unsigned tag, bid = blockIdx.x; int tid = threadIdx.x;
    typedef __attribute__((address_space(1))) char gchar;
    typedef gchar* const __attribute__((address_space(4)))* karg_t;
    karg_t ka = (karg_t)__builtin_amdgcn_kernarg_segment_ptr();
    asm volatile(
            SL(0) SZ
            "s_load_dwordx2 s[40:41], %[k], 0x8\n\ts_load_dwordx2 s[36:37], %[k], 0x0\n\tv_readfirstlane_b32 s69, %[t]\n\ts_lshr_b32 s70, %[b], 3\n\ts_cmpk_lg_u32 s69, 0xc0\n\ts_cbranch_scc1 1f\n\t"
            "s_getpc_b64 s[72:73]\n\t.Lch:\ns_lshl3_add_u32 s71, s70, 0xffffff40\n\tv_add_lshl_u32 v36, s71, %[t], 7\n\tv_min_u32 v36, .Lce-.Lch-4, v36\n\ts_mov_b32 m0, 162368\n\t"
            "s_mov_b64 exec, 0xff\n\tglobal_load_lds_dword v36, s[72:73]\n\ts_mov_b64 exec, -1\n\t1:\ns_add_i32 s70, s70, 25\n\ts_and_b32 s70, s70, 31\n\t"
            "s_cmp_lt_u32 s70, 25\n\ts_cbranch_scc0 .LF\n\tv_bfe_u32 v36, %[t], 3, 3\n\tv_and_b32 v37, 7, %[t]\n\ts_mul_i32 s71, s70, 13\n\ts_lshr_b32 s71, s71, 6\n\t"
            "v_and_b32 v38, 2, v36\n\tv_xor_b32 v46, v38, v37\n\ts_lshl_b32 s64, s71, 2\n\ts_sub_i32 s64, s70, s64\n\ts_sub_i32 s57, s64, s71\n\tv_mul_u32_u24 v40, 0xf00, v36\n\t"
            "s_sub_i32 s72, s64, 5\n\ts_min_u32 s64, s64, s72\n\tv_lshl_add_u32 v40, v46, 4, v40\n\ts_bfe_u32 s56, s69, 0x20006\n\ts_and_b32 s72, %[b], 1\n\ts_mul_i32 s72, s72, 5\n\t"
            "s_add_i32 s58, s57, s72\n\ts_lshl_b32 s58, s58, 7\n\ts_mul_i32 s61, s64, 0x78000\n\ts_mul_i32 s73, s56, 0xf000\n\ts_add_i32 s73, s73, s58\n\ts_add_i32 s61, s61, s73\n\t"
            "s_mul_i32 s66, s56, 0x1800\n\ts_add_i32 m0, s66, 53248\n\ts_mov_b32 s42, 0x258000\n\ts_mov_b32 s43, 0x20000\n\tv_add_u32 v41, 0x100, v40\n\tv_add_u32 v42, 0x200, v40\n\t"
            "v_xor_b32 v43, 64, v40\n\tv_add_u32 v43, 0x6c00, v43\n\tv_add_u32 v44, 0x1100, v43\n\tv_add_u32 v45, 0x1200, v43\n\ts_cmpk_lt_u32 s69, 0x100\n\ts_cbranch_scc1 1f\n\t"
            "s_waitcnt lgkmcnt(0)\n\tbuffer_load_dwordx4 v40, s[40:43], s61 offen lds\n\tbuffer_load_dwordx4 v41, s[40:43], s61 offen offset:1024 lds\n\tbuffer_load_dwordx4 v42, s[40:43], s61 offen offset:2048 lds\n\tbuffer_load_dwordx4 v43, s[40:43], s61 offen offset:3072 lds\n\ts_addk_i32 m0, 0x1000\n\t"
            "s_nop 0\n\tbuffer_load_dwordx4 v44, s[40:43], s61 offen lds\n\tbuffer_load_dwordx4 v45, s[40:43], s61 offen offset:1024 lds\n\t1:\ns_mul_i32 s72, s56, 28\n\tv_add_u32 v39, s72, v36\n\t"
            "s_bfe_u32 s62, %[b], 0x10001\n\ts_mul_i32 s62, s62, 5\n\ts_add_i32 s62, s62, s71\n\ts_mul_i32 s62, s62, 104\n\ts_sub_i32 s62, s62, 8\n\tv_and_b32 v47, 3, v39\n\t"
            "v_lshl_add_u32 v47, v47, 2, s62\n\tv_and_b32 v48, 0x70, v39\n\tv_bfe_u32 v49, v39, 2, 2\n\tv_add3_u32 v48, v48, v49, v47\n\tv_bfe_u32 v49, v39, 1, 3\n\tv_xor_b32 v50, v49, v37\n\t"
            "v_mul_i32_i24 v48, 0xa00, v48\n\ts_bfe_u32 s63, %[b], 0x10002\n\ts_mul_i32 s63, s63, 0x500\n\ts_lshl_b32 s60, s64, 8\n\tv_mov_b32 v51, v36\n\tv_lshl_add_u32 v36, v50, 4, v48\n\t"
            "s_add_i32 s60, s60, s63\n\ts_mul_i32 s65, s56, 0xe00\n\ts_add_i32 m0, s65, 38912\n\ts_mov_b32 s38, 0x280000\n\ts_mov_b32 s39, 0x20000\n\tv_add_u32 v38, 0x9800, v36\n\t"
            "s_cmpk_lt_u32 s69, 0x100\n\ts_cbranch_scc1 1f\n\tbuffer_load_dwordx4 v36, s[36:39], s60 offen lds\n\tbuffer_load_dwordx4 v38, s[36:39], s60 offen offset:2048 lds\n\ts_barrier\n\t1:\n"
            "s_load_dwordx2 s[44:45], %[k], 0x38\n\ts_load_dwordx2 s[48:49], %[k], 0x10\n\ts_load_dwordx2 s[52:53], %[k], 0x40\n\tv_add_u32 v39, 8, v39\n\tv_and_b32 v48, 0x70, v39\n\tv_bfe_u32 v49, v39, 2, 2\n\t"
            "v_add3_u32 v48, v48, v49, v47\n\tv_xor_b32 v50, 4, v50\n\tv_mul_i32_i24 v48, 0xa00, v48\n\tv_lshl_add_u32 v37, v50, 4, v48\n\tv_subrev_u32 v37, 0x400, v37\n\tv_add_u32 v39, 0x9800, v37\n\t"
            "s_addk_i32 s66, 14336\n\ts_bfe_u32 s59, %[b], 0x20001\n\ts_mul_i32 s59, s59, 5\n\ts_add_i32 s59, s59, s71\n\ts_mov_b32 s46, 3432448\n\ts_mov_b32 s47, 0x20000\n\t"
            "s_mov_b32 s54, 0x280000\n\ts_mov_b32 s55, 0x20000\n\tv_and_b32 v52, 63, %[t]\n\tv_bfe_u32 v53, %[t], 4, 2\n\tv_and_b32 v54, 15, %[t]\n\tv_and_b32 v55, 3, %[t]\n\t"
            "v_bfe_u32 v56, %[t], 2, 2\n\tv_lshl_add_u32 v57, v55, 2, v56\n\ts_lshr_b32 s69, s69, 6\n\ts_cmp_lt_u32 s69, 4\n\ts_cbranch_scc0 .LB\n\ts_branch .LA\n\t"
            ".LF:\ns_load_dwordx2 s[72:73], %[k], 0x18\n\ts_and_b32 s71, %[b], 1\n\ts_lshl1_add_u32 s71, s70, s71\n\ts_mul_i32 s71, s71, 40\n\ts_sub_u32 s71, s71, 2000\n\t"
            "v_lshrrev_b32 v2, 4, %[t]\n\tv_add_u32 v2, s71, v2\n\tv_add_u32 v3, 32, v2\n\tv_min_u32 v2, 0x1ff, v2\n\tv_min_u32 v3, 0x1ff, v3\n\tv_and_b32 v4, 15, %[t]\n\t"
            "v_lshlrev_b32 v4, 4, v4\n\ts_bfe_u32 s71, %[b], 0x10001\n\ts_mul_i32 s71, s71, 0x140000\n\ts_bfe_u32 s74, %[b], 0x10002\n\ts_mul_i32 s74, s74, 0x500\n\ts_add_u32 s71, s71, s74\n\t"
            "v_add_u32 v5, s71, v4\n\ts_movk_i32 s71, 0xa00\n\tv_mad_u32_u24 v2, v2, s71, v5\n\tv_mad_u32_u24 v3, v3, s71, v5\n\ts_waitcnt lgkmcnt(0)\n\tglobal_load_dwordx4 v[104:107], v4, s[72:73] offset:0\n\t"
            "global_load_dwordx4 v[108:111], v4, s[72:73] offset:256\n\tglobal_load_dwordx4 v[112:115], v4, s[72:73] offset:512\n\tglobal_load_dwordx4 v[116:119], v4, s[72:73] offset:768\n\tglobal_load_dwordx4 v[120:123], v4, s[72:73] offset:1024\n\ts_cmpk_ge_u32 s69, 0x80\n\ts_cbranch_scc1 1f\n\t"
            "global_load_dwordx4 v[84:87], v3, s[36:37] offset:0\n\tglobal_load_dwordx4 v[88:91], v3, s[36:37] offset:256\n\tglobal_load_dwordx4 v[92:95], v3, s[36:37] offset:512\n\tglobal_load_dwordx4 v[96:99], v3, s[36:37] offset:768\n\tglobal_load_dwordx4 v[100:103], v3, s[36:37] offset:1024\n\t1:\n"
            "global_load_dwordx4 v[64:67], v2, s[36:37] offset:0\n\tglobal_load_dwordx4 v[68:71], v2, s[36:37] offset:256\n\tglobal_load_dwordx4 v[72:75], v2, s[36:37] offset:512\n\tglobal_load_dwordx4 v[76:79], v2, s[36:37] offset:768\n\tglobal_load_dwordx4 v[80:83], v2, s[36:37] offset:1024\n\ts_branch .Lout\n\t"
            ".LA:\nv_lshrrev_b32 v2, 1, v54\n\tv_xor_b32 v2, v2, v53\n\tv_lshlrev_b32 v3, 7, v54\n\tv_lshl_add_u32 v2, v2, 4, v3\n\tv_xor_b32 v3, 64, v2\n\t"
            "v_add_u32 v4, 38912, v2\n\tv_add_u32 v5, 38912, v3\n\tv_add_u32 v10, 77824, v2\n\tv_add_u32 v11, 77824, v3\n\tv_mov_b32 v12, 159872\n\tv_cmp_eq_u32 vcc, 0, v54\n\t"
            "v_mov_b32 v60, 0\n\tv_mov_b32 v61, 0\n\tv_mov_b32 v62, 0x3f803f80\n\tv_lshlrev_b32 v63, 3, v52\n\tv_cndmask_b32 v6, v2, v12, vcc\n\tv_cndmask_b32 v7, v3, v12, vcc\n\t"
            "v_cndmask_b32 v8, v4, v12, vcc\n\tv_cndmask_b32 v9, v5, v12, vcc\n\tv_cndmask_b32 v10, v10, v12, vcc\n\tv_cndmask_b32 v11, v11, v12, vcc\n\tv_lshlrev_b32 v64, 2, v52\n\tv_lshrrev_b32 v65, 2, v52\n\t"
            "v_cmp_eq_u32 vcc, 8, v65\n\tv_add_u32 v66, 161088, v63\n\tv_add_u32 v64, 159744, v64\n\tv_cndmask_b32 v62, 0, v62, vcc\n\ts_cmp_lg_u32 s69, 1\n\ts_cbranch_scc1 1f\n\t"
            "ds_write_b32 v64, v62\n\tds_write_b64 v66, v[60:61]\n\t1:\nv_bfe_u32 v12, v52, 3, 2\n\tv_xor_b32 v12, s69, v12\n\tv_lshlrev_b32 v13, 7, v56\n\t"
            "v_lshl_add_u32 v13, v55, 3, v13\n\tv_lshl_add_u32 v12, v12, 5, v13\n\tv_mul_u32_u24 v13, 0xc00, v53\n\ts_movk_i32 s72, 14336\n\tv_add3_u32 v12, v12, v13, s72\n\tv_add_u32 v13, 38912, v12\n\t"
            "s_mov_b32 s72, 160000\n\tv_lshl_add_u32 v14, v54, 2, s72\n\tv_lshl_add_u32 v15, v53, 4, s72\n\ts_cmp_eq_u32 s69, 3\n\ts_cselect_b32 s74, 1, 0\n\ts_cselect_b64 s[76:77], -1, 0\n\t"
            "s_cselect_b32 s75, 4, 0\n\ts_add_i32 s72, s69, 1\n\ts_bfe_u32 s72, s72, 0x10001\n\ts_xor_b32 s72, s72, 1\n\ts_lshr_b32 s73, s69, 1\n\ts_lshl1_add_u32 s78, s72, s73\n\t"
            "s_lshl2_add_u32 s78, s78, 159776\n\tv_mov_b32 v34, s78\n\ts_lshl2_add_u32 s78, s69, 159744\n\tv_mov_b32 v33, s78\n\ts_mul_i32 s78, s59, 163840\n\ts_lshl_b32 s79, s58, 5\n\t"
            "s_add_i32 s78, s78, s79\n\ts_lshl_b32 s79, s69, 10\n\ts_add_i32 s78, s78, s79\n\ts_add_i32 s78, s78, 155648\n\tv_lshl_add_u32 v16, v52, 4, s78\n\tv_and_b32 v215, 1, v53\n\t"
            "v_lshl_add_u32 v215, v215, 30, v16\n\ts_lshl_b32 s79, s74, 11\n\ts_add_i32 s78, s78, s79\n\ts_sub_i32 s78, s78, 0x400\n\ts_cmp_lg_u32 s57, s75\n\ts_cselect_b32 s79, s72, 0\n\t"
            "s_cmp_lg_u32 s79, 0\n\ts_cselect_b32 s78, s78, 0x40000000\n\tv_lshl_add_u32 v17, v52, 4, s78\n\ts_sub_i32 s78, s69, s57\n\ts_add_i32 s79, s78, 5\n\ts_min_u32 s78, s78, s79\n\t"
            "v_cmp_gt_u32 s[80:81], s78, v53\n\tv_cmp_gt_u32 s[82:83], s78, v56\n\ts_mov_b32 s14, 0x33363333\n\ts_mov_b32 s15, 0x36663366\n\ts_mov_b32 s16, 0x666c6666\n\ts_mov_b32 s17, 0x6ccc66cc\n\t"
            "s_mov_b32 s18, 0x33333333\n\ts_mov_b32 s19, 0x33333333\n\ts_xor_b64 s[80:81], s[80:81], s[76:77]\n\ts_xor_b64 s[82:83], s[82:83], s[76:77]\n\ts_andn2_b64 vcc, s[80:81], s[18:19]\n\tv_cndmask_b32 v18, 0, -1, vcc\n\t"
            "s_and_b64 vcc, s[80:81], s[18:19]\n\tv_cndmask_b32 v19, 0, -1, vcc\n\ts_nor_b64 vcc, s[80:81], s[18:19]\n\tv_cndmask_b32 v20, 0, -1, vcc\n\ts_andn2_b64 vcc, s[18:19], s[80:81]\n\tv_cndmask_b32 v21, 0, -1, vcc\n\t"
            "v_cndmask_b32 v22, 0, -1, s[82:83]\n\tv_cndmask_b32 v23, -1, 0, s[82:83]\n\tv_sub_u32 v58, v57, v53\n\tv_add_u32 v58, 4, v58\n\tv_and_b32 v58, 28, v58\n\tv_lshlrev_b32 v58, 1, v58\n\t"
            "v_mov_b32 v24, -1\n\tv_mov_b32 v25, 0\n\tv_lshlrev_b64 v[24:25], v58, v[24:25]\n\tv_lshlrev_b64 v[24:25], v58, v[24:25]\n\tv_sub_u32 v58, 32, v58\n\tv_min_u32 v58, 16, v58\n\t"
            "v_lshrrev_b32 v26, v58, -1\n\tv_lshrrev_b32 v26, v58, v26\n\ts_lshl1_add_u32 s78, s69, s74\n\ts_xor_b32 s79, s78, 1\n\ts_mul_i32 s78, s78, 0xc00\n\ts_mul_i32 s79, s79, 0xc00\n\t"
            "s_addk_i32 s78, 14336\n\ts_addk_i32 s79, 14336\n\tv_add_u32 v28, s78, v63\n\tv_add_u32 v29, s79, v63\n\ts_cmp_eq_u32 s69, s57\n\ts_addc_u32 s78, s69, 0\n\t"
            "s_add_i32 s79, s69, 1\n\ts_cmp_eq_u32 s79, s57\n\ts_subb_u32 s79, s79, 0\n\ts_cmp_eq_u32 s74, 1\n\ts_cselect_b32 s80, s79, s78\n\ts_cselect_b32 s81, s78, s79\n\t"
            "v_lshlrev_b32 v58, 2, v53\n\tv_lshl_add_u32 v58, v54, 4, v58\n\ts_cmp_gt_u32 s80, s57\n\ts_subb_u32 s82, s80, 0\n\ts_lshl2_add_u32 s82, s82, 159760\n\tv_mov_b32 v59, s82\n\t"
            "s_lshl_b32 s82, s80, 1\n\ts_max_u32 s82, s82, 1\n\ts_mul_i32 s82, s82, 0xc00\n\ts_addk_i32 s82, 11264\n\tv_add_u32 v30, s82, v58\n\ts_cmp_gt_u32 s81, s57\n\t"
            "s_subb_u32 s82, s81, 0\n\ts_lshl2_add_u32 s82, s82, 159760\n\tv_mov_b32 v60, s82\n\ts_lshl_b32 s82, s81, 1\n\ts_max_u32 s82, s82, 1\n\ts_mul_i32 s82, s82, 0xc00\n\t"
            "s_addk_i32 s82, 11264\n\tv_add_u32 v31, s82, v58\n\tv_cndmask_b32 v32, v59, v60, s[18:19]\n\tv_mov_b32 v35, 1\n\ts_waitcnt lgkmcnt(0)\n\ts_barrier\n\t"
            "s_load_dword s67, s[44:45], 0x0\n\ts_mov_b32 s20, 0\n\ts_mov_b32 s21, 40960\n\ts_mov_b32 s22, 81920\n\ts_mov_b32 s23, 122880\n\ts_add_u32 m0, s65, 38912\n\t"
            "s_nop 0\n\tbuffer_load_dwordx4 v37, s[36:39], s60 offen offset:1024 lds\n\ts_mov_b32 exec_hi, 0\n\tbuffer_load_dwordx4 v39, s[36:39], s60 offen offset:3072 lds\n\ts_mov_b32 exec_hi, -1\n\ts_cmp_eq_u32 s64, 4\n\t"
            "s_cselect_b32 s72, -1280, 0\n\ts_add_i32 s72, s72, 256\n\ts_bitcmp1_b32 31, s64\n\ts_cselect_b32 s72, s72, 128\n\ts_mul_i32 s73, s72, 1920\n\ts_add_u32 s60, s60, s72\n\t"
            "s_add_u32 m0, s66, 77824\n\ts_add_u32 s61, s61, s73\n\tbuffer_load_dwordx4 v40, s[40:43], s61 offen lds\n\tbuffer_load_dwordx4 v41, s[40:43], s61 offen offset:1024 lds\n\tbuffer_load_dwordx4 v42, s[40:43], s61 offen offset:2048 lds\n\tbuffer_load_dwordx4 v43, s[40:43], s61 offen offset:3072 lds\n\t"
            "s_addk_i32 m0, 0x1000\n\ts_nop 0\n\tbuffer_load_dwordx4 v44, s[40:43], s61 offen lds\n\tbuffer_load_dwordx4 v45, s[40:43], s61 offen offset:1024 lds\n\ts_add_u32 m0, s65, 77824\n\ts_nop 0\n\t"
            "buffer_load_dwordx4 v36, s[36:39], s60 offen lds\n\tbuffer_load_dwordx4 v37, s[36:39], s60 offen offset:1024 lds\n\tbuffer_load_dwordx4 v38, s[36:39], s60 offen offset:2048 lds\n\ts_mov_b32 exec_hi, 0\n\tbuffer_load_dwordx4 v39, s[36:39], s60 offen offset:3072 lds\n\ts_mov_b32 exec_hi, -1\n\t"
            "s_mov_b32 s68, 0x200000\n\ts_waitcnt lgkmcnt(0)\n\ts_mul_i32 s67, s67, 0x19660d\n\ts_add_u32 s67, s67, 0x3c6ef35f\n\ts_waitcnt vmcnt(10)\n\ts_barrier\n\t"
            SA(1) ST(-1)
            "ds_read_b64_tr_b16 v[176:177], v13 offset:0\n\tds_read_b64_tr_b16 v[178:179], v13 offset:512\n\tds_read_b128 v[120:123], v8\n\tds_read_b128 v[124:127], v4 offset:2048\n\tds_read_b128 v[128:131], v4 offset:4096\n\tds_read_b128 v[132:135], v4 offset:6144\n\t"
            "ds_read_b128 v[136:139], v4 offset:8192\n\tds_read_b128 v[140:143], v4 offset:10240\n\tds_read_b128 v[144:147], v4 offset:12288\n\tds_read_b64_tr_b16 v[180:181], v13 offset:1024\n\tds_read_b64_tr_b16 v[182:183], v13 offset:1536\n\tds_read_b64_tr_b16 v[184:185], v13 offset:2048\n\t"
            "ds_read_b64_tr_b16 v[186:187], v13 offset:2560\n\ts_waitcnt lgkmcnt(10)\n\tv_mfma_f32_16x16x32_bf16 v[36:39], v[176:179], v[120:123], 0\n\ts_waitcnt lgkmcnt(9)\n\tv_mfma_f32_16x16x32_bf16 v[48:51], v[176:179], v[124:127], 0\n\ts_waitcnt lgkmcnt(8)\n\t"
            "v_mfma_f32_16x16x32_bf16 v[60:63], v[176:179], v[128:131], 0\n\tds_read_b64_tr_b16 v[188:189], v13 offset:12288\n\tds_read_b64_tr_b16 v[190:191], v13 offset:12800\n\ts_waitcnt lgkmcnt(9)\n\tv_mfma_f32_16x16x32_bf16 v[72:75], v[176:179], v[132:135], 0\n\tds_read_b128 v[148:151], v9\n\t"
            "s_waitcnt lgkmcnt(9)\n\tv_mfma_f32_16x16x32_bf16 v[84:87], v[176:179], v[136:139], 0\n\tds_read_b128 v[152:155], v5 offset:2048\n\ts_waitcnt lgkmcnt(9)\n\tv_mfma_f32_16x16x32_bf16 v[96:99], v[176:179], v[140:143], 0\n\tds_read_b128 v[156:159], v5 offset:4096\n\t"
            "s_waitcnt lgkmcnt(9)\n\tv_mfma_f32_16x16x32_bf16 v[108:111], v[176:179], v[144:147], 0\n\tds_read_b128 v[160:163], v5 offset:6144\n\ts_waitcnt lgkmcnt(8)\n\tv_mfma_f32_16x16x32_bf16 v[40:43], v[180:183], v[120:123], 0\n\tds_read_b128 v[164:167], v5 offset:8192\n\t"
            "v_mfma_f32_16x16x32_bf16 v[52:55], v[180:183], v[124:127], 0\n\tds_read_b128 v[168:171], v5 offset:10240\n\tv_mfma_f32_16x16x32_bf16 v[64:67], v[180:183], v[128:131], 0\n\tds_read_b128 v[172:175], v5 offset:12288\n\tv_mfma_f32_16x16x32_bf16 v[76:79], v[180:183], v[132:135], 0\n\tds_read_b64_tr_b16 v[192:193], v13 offset:13312\n\t"
            "ds_read_b64_tr_b16 v[194:195], v13 offset:13824\n\tv_mfma_f32_16x16x32_bf16 v[88:91], v[180:183], v[136:139], 0\n\tds_read_b64_tr_b16 v[196:197], v13 offset:14336\n\tds_read_b64_tr_b16 v[198:199], v13 offset:14848\n\tv_mfma_f32_16x16x32_bf16 v[100:103], v[180:183], v[140:143], 0\n\tv_mfma_f32_16x16x32_bf16 v[112:115], v[180:183], v[144:147], 0\n\t"
            "s_waitcnt lgkmcnt(13)\n\tv_mfma_f32_16x16x32_bf16 v[44:47], v[120:123], v[184:187], 0\n\tv_mfma_f32_16x16x32_bf16 v[56:59], v[124:127], v[184:187], 0\n\tv_mfma_f32_16x16x32_bf16 v[68:71], v[128:131], v[184:187], 0\n\tv_mfma_f32_16x16x32_bf16 v[80:83], v[132:135], v[184:187], 0\n\tv_mfma_f32_16x16x32_bf16 v[92:95], v[136:139], v[184:187], 0\n\t"
            "v_mfma_f32_16x16x32_bf16 v[104:107], v[140:143], v[184:187], 0\n\tv_mfma_f32_16x16x32_bf16 v[116:119], v[144:147], v[184:187], 0\n\ts_waitcnt lgkmcnt(10)\n\tv_mfma_f32_16x16x32_bf16 v[36:39], v[188:191], v[148:151], v[36:39]\n\ts_waitcnt lgkmcnt(9)\n\tv_mfma_f32_16x16x32_bf16 v[48:51], v[188:191], v[152:155], v[48:51]\n\t"
            "s_waitcnt lgkmcnt(8)\n\tv_mfma_f32_16x16x32_bf16 v[60:63], v[188:191], v[156:159], v[60:63]\n\ts_waitcnt lgkmcnt(7)\n\tv_mfma_f32_16x16x32_bf16 v[72:75], v[188:191], v[160:163], v[72:75]\n\ts_waitcnt lgkmcnt(6)\n\tv_mfma_f32_16x16x32_bf16 v[84:87], v[188:191], v[164:167], v[84:87]\n\t"
            "s_waitcnt lgkmcnt(5)\n\tv_mfma_f32_16x16x32_bf16 v[96:99], v[188:191], v[168:171], v[96:99]\n\ts_waitcnt lgkmcnt(4)\n\tv_mfma_f32_16x16x32_bf16 v[108:111], v[188:191], v[172:175], v[108:111]\n\ts_waitcnt lgkmcnt(2)\n\tv_mfma_f32_16x16x32_bf16 v[40:43], v[192:195], v[148:151], v[40:43]\n\t"
            "v_mfma_f32_16x16x32_bf16 v[52:55], v[192:195], v[152:155], v[52:55]\n\tv_mfma_f32_16x16x32_bf16 v[64:67], v[192:195], v[156:159], v[64:67]\n\tv_mfma_f32_16x16x32_bf16 v[76:79], v[192:195], v[160:163], v[76:79]\n\ts_waitcnt vmcnt(0) lgkmcnt(0)\n\ts_barrier\n\t"
            ST(1)
            "ds_read_b64_tr_b16 v[176:177], v13 offset:38912\n\tds_read_b64_tr_b16 v[178:179], v13 offset:39424\n\tv_mfma_f32_16x16x32_bf16 v[88:91], v[192:195], v[164:167], v[88:91]\n\tds_read_b128 v[120:123], v10\n\tv_mfma_f32_16x16x32_bf16 v[100:103], v[192:195], v[168:171], v[100:103]\n\tds_read_b128 v[124:127], v4 offset:40960\n\t"
            "v_mfma_f32_16x16x32_bf16 v[112:115], v[192:195], v[172:175], v[112:115]\n\tds_read_b128 v[128:131], v4 offset:43008\n\tv_mfma_f32_16x16x32_bf16 v[44:47], v[148:151], v[196:199], v[44:47]\n\tds_read_b128 v[132:135], v4 offset:45056\n\tv_mfma_f32_16x16x32_bf16 v[56:59], v[152:155], v[196:199], v[56:59]\n\tds_read_b128 v[136:139], v4 offset:47104\n\t"
            "v_mfma_f32_16x16x32_bf16 v[68:71], v[156:159], v[196:199], v[68:71]\n\tds_read_b128 v[140:143], v4 offset:49152\n\tv_mfma_f32_16x16x32_bf16 v[80:83], v[160:163], v[196:199], v[80:83]\n\tds_read_b128 v[144:147], v4 offset:51200\n\tv_mfma_f32_16x16x32_bf16 v[92:95], v[164:167], v[196:199], v[92:95]\n\tds_read_b64_tr_b16 v[180:181], v13 offset:39936\n\t"
            "ds_read_b64_tr_b16 v[182:183], v13 offset:40448\n\tv_mfma_f32_16x16x32_bf16 v[104:107], v[168:171], v[196:199], v[104:107]\n\tds_read_b64_tr_b16 v[184:185], v13 offset:40960\n\tds_read_b64_tr_b16 v[186:187], v13 offset:41472\n\tv_mfma_f32_16x16x32_bf16 v[116:119], v[172:175], v[196:199], v[116:119]\n\ts_waitcnt lgkmcnt(10)\n\t"
            "v_mfma_f32_16x16x32_bf16 v[36:39], v[176:179], v[120:123], v[36:39]\n\ts_waitcnt lgkmcnt(9)\n\tv_mfma_f32_16x16x32_bf16 v[48:51], v[176:179], v[124:127], v[48:51]\n\ts_waitcnt lgkmcnt(8)\n\tv_mfma_f32_16x16x32_bf16 v[60:63], v[176:179], v[128:131], v[60:63]\n\tds_read_b64_tr_b16 v[188:189], v13 offset:51200\n\t"
            "ds_read_b64_tr_b16 v[190:191], v13 offset:51712\n\ts_waitcnt lgkmcnt(9)\n\tv_mfma_f32_16x16x32_bf16 v[72:75], v[176:179], v[132:135], v[72:75]\n\tds_read_b128 v[148:151], v11\n\ts_waitcnt lgkmcnt(9)\n\tv_mfma_f32_16x16x32_bf16 v[84:87], v[176:179], v[136:139], v[84:87]\n\t"
            "ds_read_b128 v[152:155], v5 offset:40960\n\ts_waitcnt lgkmcnt(9)\n\tv_mfma_f32_16x16x32_bf16 v[96:99], v[176:179], v[140:143], v[96:99]\n\tds_read_b128 v[156:159], v5 offset:43008\n\ts_waitcnt lgkmcnt(9)\n\tv_mfma_f32_16x16x32_bf16 v[108:111], v[176:179], v[144:147], v[108:111]\n\t"
            "ds_read_b128 v[160:163], v5 offset:45056\n\ts_waitcnt lgkmcnt(8)\n\tv_mfma_f32_16x16x32_bf16 v[40:43], v[180:183], v[120:123], v[40:43]\n\tds_read_b128 v[164:167], v5 offset:47104\n\tv_mfma_f32_16x16x32_bf16 v[52:55], v[180:183], v[124:127], v[52:55]\n\tds_read_b128 v[168:171], v5 offset:49152\n\t"
            "v_mfma_f32_16x16x32_bf16 v[64:67], v[180:183], v[128:131], v[64:67]\n\tds_read_b128 v[172:175], v5 offset:51200\n\tv_mfma_f32_16x16x32_bf16 v[76:79], v[180:183], v[132:135], v[76:79]\n\tds_read_b64_tr_b16 v[192:193], v13 offset:52224\n\tds_read_b64_tr_b16 v[194:195], v13 offset:52736\n\tv_mfma_f32_16x16x32_bf16 v[88:91], v[180:183], v[136:139], v[88:91]\n\t"
            "ds_read_b64_tr_b16 v[196:197], v13 offset:53248\n\tds_read_b64_tr_b16 v[198:199], v13 offset:53760\n\tv_mfma_f32_16x16x32_bf16 v[100:103], v[180:183], v[140:143], v[100:103]\n\tv_mfma_f32_16x16x32_bf16 v[112:115], v[180:183], v[144:147], v[112:115]\n\ts_waitcnt lgkmcnt(13)\n\tv_mfma_f32_16x16x32_bf16 v[44:47], v[120:123], v[184:187], v[44:47]\n\t"
            "v_mfma_f32_16x16x32_bf16 v[56:59], v[124:127], v[184:187], v[56:59]\n\tv_mfma_f32_16x16x32_bf16 v[68:71], v[128:131], v[184:187], v[68:71]\n\tv_mfma_f32_16x16x32_bf16 v[80:83], v[132:135], v[184:187], v[80:83]\n\tv_mfma_f32_16x16x32_bf16 v[92:95], v[136:139], v[184:187], v[92:95]\n\tv_mfma_f32_16x16x32_bf16 v[104:107], v[140:143], v[184:187], v[104:107]\n\tv_mfma_f32_16x16x32_bf16 v[116:119], v[144:147], v[184:187], v[116:119]\n\t"
            "s_waitcnt lgkmcnt(10)\n\tv_mfma_f32_16x16x32_bf16 v[36:39], v[188:191], v[148:151], v[36:39]\n\ts_waitcnt lgkmcnt(9)\n\tv_mfma_f32_16x16x32_bf16 v[48:51], v[188:191], v[152:155], v[48:51]\n\ts_waitcnt lgkmcnt(8)\n\tv_mfma_f32_16x16x32_bf16 v[60:63], v[188:191], v[156:159], v[60:63]\n\t"
            "s_waitcnt lgkmcnt(7)\n\tv_mfma_f32_16x16x32_bf16 v[72:75], v[188:191], v[160:163], v[72:75]\n\ts_waitcnt lgkmcnt(6)\n\tv_mfma_f32_16x16x32_bf16 v[84:87], v[188:191], v[164:167], v[84:87]\n\ts_waitcnt lgkmcnt(5)\n\tv_mfma_f32_16x16x32_bf16 v[96:99], v[188:191], v[168:171], v[96:99]\n\t"
            "s_waitcnt lgkmcnt(4)\n\tv_mfma_f32_16x16x32_bf16 v[108:111], v[188:191], v[172:175], v[108:111]\n\ts_waitcnt lgkmcnt(2)\n\tv_mfma_f32_16x16x32_bf16 v[40:43], v[192:195], v[148:151], v[40:43]\n\tv_mfma_f32_16x16x32_bf16 v[52:55], v[192:195], v[152:155], v[52:55]\n\tv_mfma_f32_16x16x32_bf16 v[64:67], v[192:195], v[156:159], v[64:67]\n\t"
            "v_mfma_f32_16x16x32_bf16 v[76:79], v[192:195], v[160:163], v[76:79]\n\ts_waitcnt lgkmcnt(0)\n\ts_barrier\n\t"
            ST(3)
            "ds_read_b64_tr_b16 v[176:177], v12 offset:0\n\tds_read_b64_tr_b16 v[178:179], v12 offset:512\n\tv_mfma_f32_16x16x32_bf16 v[88:91], v[192:195], v[164:167], v[88:91]\n\tds_read_b128 v[120:123], v6\n\tv_mfma_f32_16x16x32_bf16 v[100:103], v[192:195], v[168:171], v[100:103]\n\tds_read_b128 v[124:127], v2 offset:2048\n\t"
            "v_mfma_f32_16x16x32_bf16 v[112:115], v[192:195], v[172:175], v[112:115]\n\tds_read_b128 v[128:131], v2 offset:4096\n\tv_mfma_f32_16x16x32_bf16 v[44:47], v[148:151], v[196:199], v[44:47]\n\tds_read_b128 v[132:135], v2 offset:6144\n\tv_mfma_f32_16x16x32_bf16 v[56:59], v[152:155], v[196:199], v[56:59]\n\tds_read_b128 v[136:139], v2 offset:8192\n\t"
            "v_mfma_f32_16x16x32_bf16 v[68:71], v[156:159], v[196:199], v[68:71]\n\tds_read_b128 v[140:143], v2 offset:10240\n\tv_mfma_f32_16x16x32_bf16 v[80:83], v[160:163], v[196:199], v[80:83]\n\tds_read_b128 v[144:147], v2 offset:12288\n\tv_mfma_f32_16x16x32_bf16 v[92:95], v[164:167], v[196:199], v[92:95]\n\tds_read_b64_tr_b16 v[180:181], v12 offset:1024\n\t"
            "ds_read_b64_tr_b16 v[182:183], v12 offset:1536\n\tv_mfma_f32_16x16x32_bf16 v[104:107], v[168:171], v[196:199], v[104:107]\n\tds_read_b64_tr_b16 v[184:185], v12 offset:2048\n\tds_read_b64_tr_b16 v[186:187], v12 offset:2560\n\tv_mfma_f32_16x16x32_bf16 v[116:119], v[172:175], v[196:199], v[116:119]\n\ts_waitcnt lgkmcnt(10)\n\t"
            "v_mfma_f32_16x16x32_bf16 v[36:39], v[176:179], v[120:123], v[36:39]\n\ts_waitcnt lgkmcnt(9)\n\tv_mfma_f32_16x16x32_bf16 v[48:51], v[176:179], v[124:127], v[48:51]\n\ts_waitcnt lgkmcnt(8)\n\tv_mfma_f32_16x16x32_bf16 v[60:63], v[176:179], v[128:131], v[60:63]\n\tds_read_b64_tr_b16 v[188:189], v12 offset:12288\n\t"
            "ds_read_b64_tr_b16 v[190:191], v12 offset:12800\n\ts_waitcnt lgkmcnt(9)\n\tv_mfma_f32_16x16x32_bf16 v[72:75], v[176:179], v[132:135], v[72:75]\n\tds_read_b128 v[148:151], v7\n\ts_waitcnt lgkmcnt(9)\n\tv_mfma_f32_16x16x32_bf16 v[84:87], v[176:179], v[136:139], v[84:87]\n\t"
            "ds_read_b128 v[152:155], v3 offset:2048\n\ts_waitcnt lgkmcnt(9)\n\tv_mfma_f32_16x16x32_bf16 v[96:99], v[176:179], v[140:143], v[96:99]\n\tds_read_b128 v[156:159], v3 offset:4096\n\ts_waitcnt lgkmcnt(9)\n\tv_mfma_f32_16x16x32_bf16 v[108:111], v[176:179], v[144:147], v[108:111]\n\t"
            "ds_read_b128 v[160:163], v3 offset:6144\n\ts_waitcnt lgkmcnt(8)\n\tv_mfma_f32_16x16x32_bf16 v[40:43], v[180:183], v[120:123], v[40:43]\n\tds_read_b128 v[164:167], v3 offset:8192\n\tv_mfma_f32_16x16x32_bf16 v[52:55], v[180:183], v[124:127], v[52:55]\n\tds_read_b128 v[168:171], v3 offset:10240\n\t"
            "v_mfma_f32_16x16x32_bf16 v[64:67], v[180:183], v[128:131], v[64:67]\n\tds_read_b128 v[172:175], v3 offset:12288\n\tv_mfma_f32_16x16x32_bf16 v[76:79], v[180:183], v[132:135], v[76:79]\n\tds_read_b64_tr_b16 v[192:193], v12 offset:13312\n\tds_read_b64_tr_b16 v[194:195], v12 offset:13824\n\tv_mfma_f32_16x16x32_bf16 v[88:91], v[180:183], v[136:139], v[88:91]\n\t"
            "ds_read_b64_tr_b16 v[196:197], v12 offset:14336\n\tds_read_b64_tr_b16 v[198:199], v12 offset:14848\n\tv_mfma_f32_16x16x32_bf16 v[100:103], v[180:183], v[140:143], v[100:103]\n\tv_mfma_f32_16x16x32_bf16 v[112:115], v[180:183], v[144:147], v[112:115]\n\ts_waitcnt lgkmcnt(13)\n\tv_mfma_f32_16x16x32_bf16 v[44:47], v[120:123], v[184:187], v[44:47]\n\t"
            "v_mfma_f32_16x16x32_bf16 v[56:59], v[124:127], v[184:187], v[56:59]\n\tv_mfma_f32_16x16x32_bf16 v[68:71], v[128:131], v[184:187], v[68:71]\n\tv_mfma_f32_16x16x32_bf16 v[80:83], v[132:135], v[184:187], v[80:83]\n\tv_mfma_f32_16x16x32_bf16 v[92:95], v[136:139], v[184:187], v[92:95]\n\tv_mfma_f32_16x16x32_bf16 v[104:107], v[140:143], v[184:187], v[104:107]\n\tv_mfma_f32_16x16x32_bf16 v[116:119], v[144:147], v[184:187], v[116:119]\n\t"
            "s_waitcnt lgkmcnt(10)\n\tv_mfma_f32_16x16x32_bf16 v[36:39], v[188:191], v[148:151], v[36:39]\n\ts_waitcnt lgkmcnt(9)\n\tv_mfma_f32_16x16x32_bf16 v[48:51], v[188:191], v[152:155], v[48:51]\n\ts_waitcnt lgkmcnt(8)\n\tv_mfma_f32_16x16x32_bf16 v[60:63], v[188:191], v[156:159], v[60:63]\n\t"
            "s_waitcnt lgkmcnt(7)\n\tv_mfma_f32_16x16x32_bf16 v[72:75], v[188:191], v[160:163], v[72:75]\n\ts_waitcnt lgkmcnt(6)\n\tv_mfma_f32_16x16x32_bf16 v[84:87], v[188:191], v[164:167], v[84:87]\n\ts_waitcnt lgkmcnt(5)\n\tv_mfma_f32_16x16x32_bf16 v[96:99], v[188:191], v[168:171], v[96:99]\n\t"
            "s_waitcnt lgkmcnt(4)\n\tv_mfma_f32_16x16x32_bf16 v[108:111], v[188:191], v[172:175], v[108:111]\n\ts_waitcnt lgkmcnt(2)\n\tv_mfma_f32_16x16x32_bf16 v[40:43], v[192:195], v[148:151], v[40:43]\n\tv_mfma_f32_16x16x32_bf16 v[52:55], v[192:195], v[152:155], v[52:55]\n\tv_mfma_f32_16x16x32_bf16 v[64:67], v[192:195], v[156:159], v[64:67]\n\t"
            "v_mfma_f32_16x16x32_bf16 v[76:79], v[192:195], v[160:163], v[76:79]\n\ts_waitcnt lgkmcnt(0)\n\ts_barrier\n\t"
            ST(5)
            "ds_read_b64_tr_b16 v[176:177], v13 offset:0\n\tds_read_b64_tr_b16 v[178:179], v13 offset:512\n\tv_mfma_f32_16x16x32_bf16 v[88:91], v[192:195], v[164:167], v[88:91]\n\tds_read_b128 v[120:123], v8\n\tv_mfma_f32_16x16x32_bf16 v[100:103], v[192:195], v[168:171], v[100:103]\n\tds_read_b128 v[124:127], v4 offset:2048\n\t"
            "v_mfma_f32_16x16x32_bf16 v[112:115], v[192:195], v[172:175], v[112:115]\n\tds_read_b128 v[128:131], v4 offset:4096\n\tv_mfma_f32_16x16x32_bf16 v[44:47], v[148:151], v[196:199], v[44:47]\n\tds_read_b128 v[132:135], v4 offset:6144\n\tv_mfma_f32_16x16x32_bf16 v[56:59], v[152:155], v[196:199], v[56:59]\n\tds_read_b128 v[136:139], v4 offset:8192\n\t"
            "v_mfma_f32_16x16x32_bf16 v[68:71], v[156:159], v[196:199], v[68:71]\n\tds_read_b128 v[140:143], v4 offset:10240\n\tv_mfma_f32_16x16x32_bf16 v[80:83], v[160:163], v[196:199], v[80:83]\n\tds_read_b128 v[144:147], v4 offset:12288\n\tv_mfma_f32_16x16x32_bf16 v[92:95], v[164:167], v[196:199], v[92:95]\n\tds_read_b64_tr_b16 v[180:181], v13 offset:1024\n\t"
            "ds_read_b64_tr_b16 v[182:183], v13 offset:1536\n\tv_mfma_f32_16x16x32_bf16 v[104:107], v[168:171], v[196:199], v[104:107]\n\tds_read_b64_tr_b16 v[184:185], v13 offset:2048\n\tds_read_b64_tr_b16 v[186:187], v13 offset:2560\n\tv_mfma_f32_16x16x32_bf16 v[116:119], v[172:175], v[196:199], v[116:119]\n\ts_waitcnt lgkmcnt(10)\n\t"
            "v_mfma_f32_16x16x32_bf16 v[36:39], v[176:179], v[120:123], v[36:39]\n\ts_waitcnt lgkmcnt(9)\n\tv_mfma_f32_16x16x32_bf16 v[48:51], v[176:179], v[124:127], v[48:51]\n\ts_waitcnt lgkmcnt(8)\n\tv_mfma_f32_16x16x32_bf16 v[60:63], v[176:179], v[128:131], v[60:63]\n\tds_read_b64_tr_b16 v[188:189], v13 offset:12288\n\t"
            "ds_read_b64_tr_b16 v[190:191], v13 offset:12800\n\ts_waitcnt lgkmcnt(9)\n\tv_mfma_f32_16x16x32_bf16 v[72:75], v[176:179], v[132:135], v[72:75]\n\tds_read_b128 v[148:151], v9\n\ts_waitcnt lgkmcnt(9)\n\tv_mfma_f32_16x16x32_bf16 v[84:87], v[176:179], v[136:139], v[84:87]\n\t"
            "ds_read_b128 v[152:155], v5 offset:2048\n\ts_waitcnt lgkmcnt(9)\n\tv_mfma_f32_16x16x32_bf16 v[96:99], v[176:179], v[140:143], v[96:99]\n\tds_read_b128 v[156:159], v5 offset:4096\n\ts_waitcnt lgkmcnt(9)\n\tv_mfma_f32_16x16x32_bf16 v[108:111], v[176:179], v[144:147], v[108:111]\n\t"
            "ds_read_b128 v[160:163], v5 offset:6144\n\ts_waitcnt lgkmcnt(8)\n\tv_mfma_f32_16x16x32_bf16 v[40:43], v[180:183], v[120:123], v[40:43]\n\tds_read_b128 v[164:167], v5 offset:8192\n\tv_mfma_f32_16x16x32_bf16 v[52:55], v[180:183], v[124:127], v[52:55]\n\tds_read_b128 v[168:171], v5 offset:10240\n\t"
            "v_mfma_f32_16x16x32_bf16 v[64:67], v[180:183], v[128:131], v[64:67]\n\tds_read_b128 v[172:175], v5 offset:12288\n\tv_mfma_f32_16x16x32_bf16 v[76:79], v[180:183], v[132:135], v[76:79]\n\tds_read_b64_tr_b16 v[192:193], v13 offset:13312\n\tds_read_b64_tr_b16 v[194:195], v13 offset:13824\n\tv_mfma_f32_16x16x32_bf16 v[88:91], v[180:183], v[136:139], v[88:91]\n\t"
            "ds_read_b64_tr_b16 v[196:197], v13 offset:14336\n\tds_read_b64_tr_b16 v[198:199], v13 offset:14848\n\tv_mfma_f32_16x16x32_bf16 v[100:103], v[180:183], v[140:143], v[100:103]\n\tv_mfma_f32_16x16x32_bf16 v[112:115], v[180:183], v[144:147], v[112:115]\n\ts_waitcnt lgkmcnt(13)\n\tv_mfma_f32_16x16x32_bf16 v[44:47], v[120:123], v[184:187], v[44:47]\n\t"
            "v_mfma_f32_16x16x32_bf16 v[56:59], v[124:127], v[184:187], v[56:59]\n\tv_mfma_f32_16x16x32_bf16 v[68:71], v[128:131], v[184:187], v[68:71]\n\tv_mfma_f32_16x16x32_bf16 v[80:83], v[132:135], v[184:187], v[80:83]\n\tv_mfma_f32_16x16x32_bf16 v[92:95], v[136:139], v[184:187], v[92:95]\n\tv_mfma_f32_16x16x32_bf16 v[104:107], v[140:143], v[184:187], v[104:107]\n\tv_mfma_f32_16x16x32_bf16 v[116:119], v[144:147], v[184:187], v[116:119]\n\t"
            "s_waitcnt lgkmcnt(10)\n\tv_mfma_f32_16x16x32_bf16 v[36:39], v[188:191], v[148:151], v[36:39]\n\ts_waitcnt lgkmcnt(9)\n\tv_mfma_f32_16x16x32_bf16 v[48:51], v[188:191], v[152:155], v[48:51]\n\ts_waitcnt lgkmcnt(8)\n\tv_mfma_f32_16x16x32_bf16 v[60:63], v[188:191], v[156:159], v[60:63]\n\t"
            "s_waitcnt lgkmcnt(7)\n\tv_mfma_f32_16x16x32_bf16 v[72:75], v[188:191], v[160:163], v[72:75]\n\ts_waitcnt lgkmcnt(6)\n\tv_mfma_f32_16x16x32_bf16 v[84:87], v[188:191], v[164:167], v[84:87]\n\ts_waitcnt lgkmcnt(5)\n\tv_mfma_f32_16x16x32_bf16 v[96:99], v[188:191], v[168:171], v[96:99]\n\t"
            "s_waitcnt lgkmcnt(4)\n\tv_mfma_f32_16x16x32_bf16 v[108:111], v[188:191], v[172:175], v[108:111]\n\ts_waitcnt lgkmcnt(2)\n\tv_mfma_f32_16x16x32_bf16 v[40:43], v[192:195], v[148:151], v[40:43]\n\tv_mfma_f32_16x16x32_bf16 v[52:55], v[192:195], v[152:155], v[52:55]\n\tv_mfma_f32_16x16x32_bf16 v[64:67], v[192:195], v[156:159], v[64:67]\n\t"
            "v_mfma_f32_16x16x32_bf16 v[76:79], v[192:195], v[160:163], v[76:79]\n\ts_waitcnt lgkmcnt(0)\n\ts_barrier\n\t"
            ST(7)
            "ds_read_b64_tr_b16 v[176:177], v13 offset:38912\n\tds_read_b64_tr_b16 v[178:179], v13 offset:39424\n\tv_mfma_f32_16x16x32_bf16 v[88:91], v[192:195], v[164:167], v[88:91]\n\tds_read_b128 v[120:123], v10\n\tv_mfma_f32_16x16x32_bf16 v[100:103], v[192:195], v[168:171], v[100:103]\n\tds_read_b128 v[124:127], v4 offset:40960\n\t"
            "v_mfma_f32_16x16x32_bf16 v[112:115], v[192:195], v[172:175], v[112:115]\n\tds_read_b128 v[128:131], v4 offset:43008\n\tv_mfma_f32_16x16x32_bf16 v[44:47], v[148:151], v[196:199], v[44:47]\n\tds_read_b128 v[132:135], v4 offset:45056\n\tv_mfma_f32_16x16x32_bf16 v[56:59], v[152:155], v[196:199], v[56:59]\n\tds_read_b128 v[136:139], v4 offset:47104\n\t"
            "v_mfma_f32_16x16x32_bf16 v[68:71], v[156:159], v[196:199], v[68:71]\n\tds_read_b128 v[140:143], v4 offset:49152\n\tv_mfma_f32_16x16x32_bf16 v[80:83], v[160:163], v[196:199], v[80:83]\n\tds_read_b128 v[144:147], v4 offset:51200\n\tv_mfma_f32_16x16x32_bf16 v[92:95], v[164:167], v[196:199], v[92:95]\n\tds_read_b64_tr_b16 v[180:181], v13 offset:39936\n\t"
            "ds_read_b64_tr_b16 v[182:183], v13 offset:40448\n\tv_mfma_f32_16x16x32_bf16 v[104:107], v[168:171], v[196:199], v[104:107]\n\tds_read_b64_tr_b16 v[184:185], v13 offset:40960\n\tds_read_b64_tr_b16 v[186:187], v13 offset:41472\n\tv_mfma_f32_16x16x32_bf16 v[116:119], v[172:175], v[196:199], v[116:119]\n\ts_waitcnt lgkmcnt(10)\n\t"
            "v_mfma_f32_16x16x32_bf16 v[36:39], v[176:179], v[120:123], v[36:39]\n\ts_waitcnt lgkmcnt(9)\n\tv_mfma_f32_16x16x32_bf16 v[48:51], v[176:179], v[124:127], v[48:51]\n\ts_waitcnt lgkmcnt(8)\n\tv_mfma_f32_16x16x32_bf16 v[60:63], v[176:179], v[128:131], v[60:63]\n\tds_read_b64_tr_b16 v[188:189], v13 offset:51200\n\t"
            "ds_read_b64_tr_b16 v[190:191], v13 offset:51712\n\ts_waitcnt lgkmcnt(9)\n\tv_mfma_f32_16x16x32_bf16 v[72:75], v[176:179], v[132:135], v[72:75]\n\tds_read_b128 v[148:151], v11\n\ts_waitcnt lgkmcnt(9)\n\tv_mfma_f32_16x16x32_bf16 v[84:87], v[176:179], v[136:139], v[84:87]\n\t"
            "ds_read_b128 v[152:155], v5 offset:40960\n\ts_waitcnt lgkmcnt(9)\n\tv_mfma_f32_16x16x32_bf16 v[96:99], v[176:179], v[140:143], v[96:99]\n\tds_read_b128 v[156:159], v5 offset:43008\n\ts_waitcnt lgkmcnt(9)\n\tv_mfma_f32_16x16x32_bf16 v[108:111], v[176:179], v[144:147], v[108:111]\n\t"
            "ds_read_b128 v[160:163], v5 offset:45056\n\ts_waitcnt lgkmcnt(8)\n\tv_mfma_f32_16x16x32_bf16 v[40:43], v[180:183], v[120:123], v[40:43]\n\tds_read_b128 v[164:167], v5 offset:47104\n\tv_mfma_f32_16x16x32_bf16 v[52:55], v[180:183], v[124:127], v[52:55]\n\tds_read_b128 v[168:171], v5 offset:49152\n\t"
            "v_mfma_f32_16x16x32_bf16 v[64:67], v[180:183], v[128:131], v[64:67]\n\tds_read_b128 v[172:175], v5 offset:51200\n\tv_mfma_f32_16x16x32_bf16 v[76:79], v[180:183], v[132:135], v[76:79]\n\tds_read_b64_tr_b16 v[192:193], v13 offset:52224\n\tds_read_b64_tr_b16 v[194:195], v13 offset:52736\n\tv_mfma_f32_16x16x32_bf16 v[88:91], v[180:183], v[136:139], v[88:91]\n\t"
            "ds_read_b64_tr_b16 v[196:197], v13 offset:53248\n\tds_read_b64_tr_b16 v[198:199], v13 offset:53760\n\tv_mfma_f32_16x16x32_bf16 v[100:103], v[180:183], v[140:143], v[100:103]\n\tv_mfma_f32_16x16x32_bf16 v[112:115], v[180:183], v[144:147], v[112:115]\n\ts_waitcnt lgkmcnt(13)\n\tv_mfma_f32_16x16x32_bf16 v[44:47], v[120:123], v[184:187], v[44:47]\n\t"
            "v_mfma_f32_16x16x32_bf16 v[56:59], v[124:127], v[184:187], v[56:59]\n\tv_mfma_f32_16x16x32_bf16 v[68:71], v[128:131], v[184:187], v[68:71]\n\tv_mfma_f32_16x16x32_bf16 v[80:83], v[132:135], v[184:187], v[80:83]\n\tv_mfma_f32_16x16x32_bf16 v[92:95], v[136:139], v[184:187], v[92:95]\n\tv_mfma_f32_16x16x32_bf16 v[104:107], v[140:143], v[184:187], v[104:107]\n\tv_mfma_f32_16x16x32_bf16 v[116:119], v[144:147], v[184:187], v[116:119]\n\t"
            "s_waitcnt lgkmcnt(10)\n\tv_mfma_f32_16x16x32_bf16 v[36:39], v[188:191], v[148:151], v[36:39]\n\ts_waitcnt lgkmcnt(9)\n\tv_mfma_f32_16x16x32_bf16 v[48:51], v[188:191], v[152:155], v[48:51]\n\ts_waitcnt lgkmcnt(8)\n\tv_mfma_f32_16x16x32_bf16 v[60:63], v[188:191], v[156:159], v[60:63]\n\t"
            "s_waitcnt lgkmcnt(7)\n\tv_mfma_f32_16x16x32_bf16 v[72:75], v[188:191], v[160:163], v[72:75]\n\ts_waitcnt lgkmcnt(6)\n\tv_mfma_f32_16x16x32_bf16 v[84:87], v[188:191], v[164:167], v[84:87]\n\ts_waitcnt lgkmcnt(5)\n\tv_mfma_f32_16x16x32_bf16 v[96:99], v[188:191], v[168:171], v[96:99]\n\t"
            "s_waitcnt lgkmcnt(4)\n\tv_mfma_f32_16x16x32_bf16 v[108:111], v[188:191], v[172:175], v[108:111]\n\ts_waitcnt lgkmcnt(2)\n\tv_mfma_f32_16x16x32_bf16 v[40:43], v[192:195], v[148:151], v[40:43]\n\tv_mfma_f32_16x16x32_bf16 v[52:55], v[192:195], v[152:155], v[52:55]\n\tv_mfma_f32_16x16x32_bf16 v[64:67], v[192:195], v[156:159], v[64:67]\n\t"
            "v_mfma_f32_16x16x32_bf16 v[76:79], v[192:195], v[160:163], v[76:79]\n\ts_waitcnt lgkmcnt(0)\n\ts_barrier\n\t"
            ST(9)
            "ds_read_b64_tr_b16 v[176:177], v12 offset:0\n\tds_read_b64_tr_b16 v[178:179], v12 offset:512\n\tv_mfma_f32_16x16x32_bf16 v[88:91], v[192:195], v[164:167], v[88:91]\n\tds_read_b128 v[120:123], v6\n\tv_mfma_f32_16x16x32_bf16 v[100:103], v[192:195], v[168:171], v[100:103]\n\tds_read_b128 v[124:127], v2 offset:2048\n\t"
            "v_mfma_f32_16x16x32_bf16 v[112:115], v[192:195], v[172:175], v[112:115]\n\tds_read_b128 v[128:131], v2 offset:4096\n\tv_mfma_f32_16x16x32_bf16 v[44:47], v[148:151], v[196:199], v[44:47]\n\tds_read_b128 v[132:135], v2 offset:6144\n\tv_mfma_f32_16x16x32_bf16 v[56:59], v[152:155], v[196:199], v[56:59]\n\tds_read_b128 v[136:139], v2 offset:8192\n\t"
            "v_mfma_f32_16x16x32_bf16 v[68:71], v[156:159], v[196:199], v[68:71]\n\tds_read_b128 v[140:143], v2 offset:10240\n\tv_mfma_f32_16x16x32_bf16 v[80:83], v[160:163], v[196:199], v[80:83]\n\tds_read_b128 v[144:147], v2 offset:12288\n\tv_mfma_f32_16x16x32_bf16 v[92:95], v[164:167], v[196:199], v[92:95]\n\tds_read_b64_tr_b16 v[180:181], v12 offset:1024\n\t"
            "ds_read_b64_tr_b16 v[182:183], v12 offset:1536\n\tv_mfma_f32_16x16x32_bf16 v[104:107], v[168:171], v[196:199], v[104:107]\n\tds_read_b64_tr_b16 v[184:185], v12 offset:2048\n\tds_read_b64_tr_b16 v[186:187], v12 offset:2560\n\tv_mfma_f32_16x16x32_bf16 v[116:119], v[172:175], v[196:199], v[116:119]\n\ts_waitcnt lgkmcnt(10)\n\t"
            "v_mfma_f32_16x16x32_bf16 v[36:39], v[176:179], v[120:123], v[36:39]\n\ts_waitcnt lgkmcnt(9)\n\tv_mfma_f32_16x16x32_bf16 v[48:51], v[176:179], v[124:127], v[48:51]\n\ts_waitcnt lgkmcnt(8)\n\tv_mfma_f32_16x16x32_bf16 v[60:63], v[176:179], v[128:131], v[60:63]\n\tds_read_b64_tr_b16 v[188:189], v12 offset:12288\n\t"
            "ds_read_b64_tr_b16 v[190:191], v12 offset:12800\n\ts_waitcnt lgkmcnt(9)\n\tv_mfma_f32_16x16x32_bf16 v[72:75], v[176:179], v[132:135], v[72:75]\n\tds_read_b128 v[148:151], v7\n\ts_waitcnt lgkmcnt(9)\n\tv_mfma_f32_16x16x32_bf16 v[84:87], v[176:179], v[136:139], v[84:87]\n\t"
            "ds_read_b128 v[152:155], v3 offset:2048\n\ts_waitcnt lgkmcnt(9)\n\tv_mfma_f32_16x16x32_bf16 v[96:99], v[176:179], v[140:143], v[96:99]\n\tds_read_b128 v[156:159], v3 offset:4096\n\ts_waitcnt lgkmcnt(9)\n\tv_mfma_f32_16x16x32_bf16 v[108:111], v[176:179], v[144:147], v[108:111]\n\t"
            "ds_read_b128 v[160:163], v3 offset:6144\n\ts_waitcnt lgkmcnt(8)\n\tv_mfma_f32_16x16x32_bf16 v[40:43], v[180:183], v[120:123], v[40:43]\n\tds_read_b128 v[164:167], v3 offset:8192\n\tv_mfma_f32_16x16x32_bf16 v[52:55], v[180:183], v[124:127], v[52:55]\n\tds_read_b128 v[168:171], v3 offset:10240\n\t"
            "v_mfma_f32_16x16x32_bf16 v[64:67], v[180:183], v[128:131], v[64:67]\n\tds_read_b128 v[172:175], v3 offset:12288\n\tv_mfma_f32_16x16x32_bf16 v[76:79], v[180:183], v[132:135], v[76:79]\n\tds_read_b64_tr_b16 v[192:193], v12 offset:13312\n\tds_read_b64_tr_b16 v[194:195], v12 offset:13824\n\tv_mfma_f32_16x16x32_bf16 v[88:91], v[180:183], v[136:139], v[88:91]\n\t"
            "ds_read_b64_tr_b16 v[196:197], v12 offset:14336\n\tds_read_b64_tr_b16 v[198:199], v12 offset:14848\n\tv_mfma_f32_16x16x32_bf16 v[100:103], v[180:183], v[140:143], v[100:103]\n\tv_mfma_f32_16x16x32_bf16 v[112:115], v[180:183], v[144:147], v[112:115]\n\ts_waitcnt lgkmcnt(13)\n\tv_mfma_f32_16x16x32_bf16 v[44:47], v[120:123], v[184:187], v[44:47]\n\t"
            "v_mfma_f32_16x16x32_bf16 v[56:59], v[124:127], v[184:187], v[56:59]\n\tv_mfma_f32_16x16x32_bf16 v[68:71], v[128:131], v[184:187], v[68:71]\n\tv_mfma_f32_16x16x32_bf16 v[80:83], v[132:135], v[184:187], v[80:83]\n\tv_mfma_f32_16x16x32_bf16 v[92:95], v[136:139], v[184:187], v[92:95]\n\tv_mfma_f32_16x16x32_bf16 v[104:107], v[140:143], v[184:187], v[104:107]\n\tv_mfma_f32_16x16x32_bf16 v[116:119], v[144:147], v[184:187], v[116:119]\n\t"
            "s_waitcnt lgkmcnt(10)\n\tv_mfma_f32_16x16x32_bf16 v[36:39], v[188:191], v[148:151], v[36:39]\n\ts_waitcnt lgkmcnt(9)\n\tv_mfma_f32_16x16x32_bf16 v[48:51], v[188:191], v[152:155], v[48:51]\n\ts_waitcnt lgkmcnt(8)\n\tv_mfma_f32_16x16x32_bf16 v[60:63], v[188:191], v[156:159], v[60:63]\n\t"
            "s_waitcnt lgkmcnt(7)\n\tv_mfma_f32_16x16x32_bf16 v[72:75], v[188:191], v[160:163], v[72:75]\n\ts_waitcnt lgkmcnt(6)\n\tv_mfma_f32_16x16x32_bf16 v[84:87], v[188:191], v[164:167], v[84:87]\n\ts_waitcnt lgkmcnt(5)\n\tv_mfma_f32_16x16x32_bf16 v[96:99], v[188:191], v[168:171], v[96:99]\n\t"
            "s_waitcnt lgkmcnt(4)\n\tv_mfma_f32_16x16x32_bf16 v[108:111], v[188:191], v[172:175], v[108:111]\n\ts_waitcnt lgkmcnt(2)\n\tv_mfma_f32_16x16x32_bf16 v[40:43], v[192:195], v[148:151], v[40:43]\n\tv_mfma_f32_16x16x32_bf16 v[52:55], v[192:195], v[152:155], v[52:55]\n\tv_mfma_f32_16x16x32_bf16 v[64:67], v[192:195], v[156:159], v[64:67]\n\t"
            "v_mfma_f32_16x16x32_bf16 v[76:79], v[192:195], v[160:163], v[76:79]\n\ts_waitcnt lgkmcnt(0)\n\ts_barrier\n\t"
            ST(11)
            "ds_read_b64_tr_b16 v[176:177], v13 offset:0\n\tds_read_b64_tr_b16 v[178:179], v13 offset:512\n\tv_mfma_f32_16x16x32_bf16 v[88:91], v[192:195], v[164:167], v[88:91]\n\tds_read_b128 v[120:123], v8\n\tv_mfma_f32_16x16x32_bf16 v[100:103], v[192:195], v[168:171], v[100:103]\n\tds_read_b128 v[124:127], v4 offset:2048\n\t"
            "v_mfma_f32_16x16x32_bf16 v[112:115], v[192:195], v[172:175], v[112:115]\n\tds_read_b128 v[128:131], v4 offset:4096\n\tv_mfma_f32_16x16x32_bf16 v[44:47], v[148:151], v[196:199], v[44:47]\n\tds_read_b128 v[132:135], v4 offset:6144\n\tv_mfma_f32_16x16x32_bf16 v[56:59], v[152:155], v[196:199], v[56:59]\n\tds_read_b128 v[136:139], v4 offset:8192\n\t"
            "v_mfma_f32_16x16x32_bf16 v[68:71], v[156:159], v[196:199], v[68:71]\n\tds_read_b128 v[140:143], v4 offset:10240\n\tv_mfma_f32_16x16x32_bf16 v[80:83], v[160:163], v[196:199], v[80:83]\n\tds_read_b128 v[144:147], v4 offset:12288\n\tv_mfma_f32_16x16x32_bf16 v[92:95], v[164:167], v[196:199], v[92:95]\n\tds_read_b64_tr_b16 v[180:181], v13 offset:1024\n\t"
            "ds_read_b64_tr_b16 v[182:183], v13 offset:1536\n\tv_mfma_f32_16x16x32_bf16 v[104:107], v[168:171], v[196:199], v[104:107]\n\tds_read_b64_tr_b16 v[184:185], v13 offset:2048\n\tds_read_b64_tr_b16 v[186:187], v13 offset:2560\n\tv_mfma_f32_16x16x32_bf16 v[116:119], v[172:175], v[196:199], v[116:119]\n\ts_waitcnt lgkmcnt(10)\n\t"
            "v_mfma_f32_16x16x32_bf16 v[36:39], v[176:179], v[120:123], v[36:39]\n\ts_waitcnt lgkmcnt(9)\n\tv_mfma_f32_16x16x32_bf16 v[48:51], v[176:179], v[124:127], v[48:51]\n\ts_waitcnt lgkmcnt(8)\n\tv_mfma_f32_16x16x32_bf16 v[60:63], v[176:179], v[128:131], v[60:63]\n\tds_read_b64_tr_b16 v[188:189], v13 offset:12288\n\t"
            "ds_read_b64_tr_b16 v[190:191], v13 offset:12800\n\ts_waitcnt lgkmcnt(9)\n\tv_mfma_f32_16x16x32_bf16 v[72:75], v[176:179], v[132:135], v[72:75]\n\tds_read_b128 v[148:151], v9\n\ts_waitcnt lgkmcnt(9)\n\tv_mfma_f32_16x16x32_bf16 v[84:87], v[176:179], v[136:139], v[84:87]\n\t"
            "ds_read_b128 v[152:155], v5 offset:2048\n\ts_waitcnt lgkmcnt(9)\n\tv_mfma_f32_16x16x32_bf16 v[96:99], v[176:179], v[140:143], v[96:99]\n\tds_read_b128 v[156:159], v5 offset:4096\n\ts_waitcnt lgkmcnt(9)\n\tv_mfma_f32_16x16x32_bf16 v[108:111], v[176:179], v[144:147], v[108:111]\n\t"
            "ds_read_b128 v[160:163], v5 offset:6144\n\ts_waitcnt lgkmcnt(8)\n\tv_mfma_f32_16x16x32_bf16 v[40:43], v[180:183], v[120:123], v[40:43]\n\tds_read_b128 v[164:167], v5 offset:8192\n\tv_mfma_f32_16x16x32_bf16 v[52:55], v[180:183], v[124:127], v[52:55]\n\tds_read_b128 v[168:171], v5 offset:10240\n\t"
            "v_mfma_f32_16x16x32_bf16 v[64:67], v[180:183], v[128:131], v[64:67]\n\tds_read_b128 v[172:175], v5 offset:12288\n\tv_mfma_f32_16x16x32_bf16 v[76:79], v[180:183], v[132:135], v[76:79]\n\tds_read_b64_tr_b16 v[192:193], v13 offset:13312\n\tds_read_b64_tr_b16 v[194:195], v13 offset:13824\n\tv_mfma_f32_16x16x32_bf16 v[88:91], v[180:183], v[136:139], v[88:91]\n\t"
            "ds_read_b64_tr_b16 v[196:197], v13 offset:14336\n\tds_read_b64_tr_b16 v[198:199], v13 offset:14848\n\tv_mfma_f32_16x16x32_bf16 v[100:103], v[180:183], v[140:143], v[100:103]\n\tv_mfma_f32_16x16x32_bf16 v[112:115], v[180:183], v[144:147], v[112:115]\n\ts_waitcnt lgkmcnt(13)\n\tv_mfma_f32_16x16x32_bf16 v[44:47], v[120:123], v[184:187], v[44:47]\n\t"
            "v_mfma_f32_16x16x32_bf16 v[56:59], v[124:127], v[184:187], v[56:59]\n\tv_mfma_f32_16x16x32_bf16 v[68:71], v[128:131], v[184:187], v[68:71]\n\tv_mfma_f32_16x16x32_bf16 v[80:83], v[132:135], v[184:187], v[80:83]\n\tv_mfma_f32_16x16x32_bf16 v[92:95], v[136:139], v[184:187], v[92:95]\n\tv_mfma_f32_16x16x32_bf16 v[104:107], v[140:143], v[184:187], v[104:107]\n\tv_mfma_f32_16x16x32_bf16 v[116:119], v[144:147], v[184:187], v[116:119]\n\t"
            "s_waitcnt lgkmcnt(10)\n\tv_mfma_f32_16x16x32_bf16 v[36:39], v[188:191], v[148:151], v[36:39]\n\ts_waitcnt lgkmcnt(9)\n\tv_mfma_f32_16x16x32_bf16 v[48:51], v[188:191], v[152:155], v[48:51]\n\ts_waitcnt lgkmcnt(8)\n\tv_mfma_f32_16x16x32_bf16 v[60:63], v[188:191], v[156:159], v[60:63]\n\t"
            "s_waitcnt lgkmcnt(7)\n\tv_mfma_f32_16x16x32_bf16 v[72:75], v[188:191], v[160:163], v[72:75]\n\ts_waitcnt lgkmcnt(6)\n\tv_mfma_f32_16x16x32_bf16 v[84:87], v[188:191], v[164:167], v[84:87]\n\ts_waitcnt lgkmcnt(5)\n\tv_mfma_f32_16x16x32_bf16 v[96:99], v[188:191], v[168:171], v[96:99]\n\t"
            "s_waitcnt lgkmcnt(4)\n\tv_mfma_f32_16x16x32_bf16 v[108:111], v[188:191], v[172:175], v[108:111]\n\ts_waitcnt lgkmcnt(2)\n\tv_mfma_f32_16x16x32_bf16 v[40:43], v[192:195], v[148:151], v[40:43]\n\tv_mfma_f32_16x16x32_bf16 v[52:55], v[192:195], v[152:155], v[52:55]\n\tv_mfma_f32_16x16x32_bf16 v[64:67], v[192:195], v[156:159], v[64:67]\n\t"
            "v_mfma_f32_16x16x32_bf16 v[76:79], v[192:195], v[160:163], v[76:79]\n\ts_waitcnt lgkmcnt(0)\n\ts_barrier\n\t"
            ST(13)
            "ds_read_b64_tr_b16 v[176:177], v13 offset:38912\n\tds_read_b64_tr_b16 v[178:179], v13 offset:39424\n\tv_mfma_f32_16x16x32_bf16 v[88:91], v[192:195], v[164:167], v[88:91]\n\tds_read_b128 v[120:123], v10\n\tv_mfma_f32_16x16x32_bf16 v[100:103], v[192:195], v[168:171], v[100:103]\n\tds_read_b128 v[124:127], v4 offset:40960\n\t"
            "v_mfma_f32_16x16x32_bf16 v[112:115], v[192:195], v[172:175], v[112:115]\n\tds_read_b128 v[128:131], v4 offset:43008\n\tv_mfma_f32_16x16x32_bf16 v[44:47], v[148:151], v[196:199], v[44:47]\n\tds_read_b128 v[132:135], v4 offset:45056\n\tv_mfma_f32_16x16x32_bf16 v[56:59], v[152:155], v[196:199], v[56:59]\n\tds_read_b128 v[136:139], v4 offset:47104\n\t"
            "v_mfma_f32_16x16x32_bf16 v[68:71], v[156:159], v[196:199], v[68:71]\n\tds_read_b128 v[140:143], v4 offset:49152\n\tv_mfma_f32_16x16x32_bf16 v[80:83], v[160:163], v[196:199], v[80:83]\n\tds_read_b128 v[144:147], v4 offset:51200\n\tv_mfma_f32_16x16x32_bf16 v[92:95], v[164:167], v[196:199], v[92:95]\n\tds_read_b64_tr_b16 v[180:181], v13 offset:39936\n\t"
            "ds_read_b64_tr_b16 v[182:183], v13 offset:40448\n\tv_mfma_f32_16x16x32_bf16 v[104:107], v[168:171], v[196:199], v[104:107]\n\tds_read_b64_tr_b16 v[184:185], v13 offset:40960\n\tds_read_b64_tr_b16 v[186:187], v13 offset:41472\n\tv_mfma_f32_16x16x32_bf16 v[116:119], v[172:175], v[196:199], v[116:119]\n\ts_waitcnt lgkmcnt(10)\n\t"
            "v_mfma_f32_16x16x32_bf16 v[36:39], v[176:179], v[120:123], v[36:39]\n\ts_waitcnt lgkmcnt(9)\n\tv_mfma_f32_16x16x32_bf16 v[48:51], v[176:179], v[124:127], v[48:51]\n\ts_waitcnt lgkmcnt(8)\n\tv_mfma_f32_16x16x32_bf16 v[60:63], v[176:179], v[128:131], v[60:63]\n\tds_read_b64_tr_b16 v[188:189], v13 offset:51200\n\t"
            "ds_read_b64_tr_b16 v[190:191], v13 offset:51712\n\ts_waitcnt lgkmcnt(9)\n\tv_mfma_f32_16x16x32_bf16 v[72:75], v[176:179], v[132:135], v[72:75]\n\tds_read_b128 v[148:151], v11\n\ts_waitcnt lgkmcnt(9)\n\tv_mfma_f32_16x16x32_bf16 v[84:87], v[176:179], v[136:139], v[84:87]\n\t"
            "ds_read_b128 v[152:155], v5 offset:40960\n\ts_waitcnt lgkmcnt(9)\n\tv_mfma_f32_16x16x32_bf16 v[96:99], v[176:179], v[140:143], v[96:99]\n\tds_read_b128 v[156:159], v5 offset:43008\n\ts_waitcnt lgkmcnt(9)\n\tv_mfma_f32_16x16x32_bf16 v[108:111], v[176:179], v[144:147], v[108:111]\n\t"
            "ds_read_b128 v[160:163], v5 offset:45056\n\ts_waitcnt lgkmcnt(8)\n\tv_mfma_f32_16x16x32_bf16 v[40:43], v[180:183], v[120:123], v[40:43]\n\tds_read_b128 v[164:167], v5 offset:47104\n\tv_mfma_f32_16x16x32_bf16 v[52:55], v[180:183], v[124:127], v[52:55]\n\tds_read_b128 v[168:171], v5 offset:49152\n\t"
            "v_mfma_f32_16x16x32_bf16 v[64:67], v[180:183], v[128:131], v[64:67]\n\tds_read_b128 v[172:175], v5 offset:51200\n\tv_mfma_f32_16x16x32_bf16 v[76:79], v[180:183], v[132:135], v[76:79]\n\tds_read_b64_tr_b16 v[192:193], v13 offset:52224\n\tds_read_b64_tr_b16 v[194:195], v13 offset:52736\n\tv_mfma_f32_16x16x32_bf16 v[88:91], v[180:183], v[136:139], v[88:91]\n\t"
            "ds_read_b64_tr_b16 v[196:197], v13 offset:53248\n\tds_read_b64_tr_b16 v[198:199], v13 offset:53760\n\tv_mfma_f32_16x16x32_bf16 v[100:103], v[180:183], v[140:143], v[100:103]\n\tv_mfma_f32_16x16x32_bf16 v[112:115], v[180:183], v[144:147], v[112:115]\n\ts_waitcnt lgkmcnt(13)\n\tv_mfma_f32_16x16x32_bf16 v[44:47], v[120:123], v[184:187], v[44:47]\n\t"
            "v_mfma_f32_16x16x32_bf16 v[56:59], v[124:127], v[184:187], v[56:59]\n\tv_mfma_f32_16x16x32_bf16 v[68:71], v[128:131], v[184:187], v[68:71]\n\tv_mfma_f32_16x16x32_bf16 v[80:83], v[132:135], v[184:187], v[80:83]\n\tv_mfma_f32_16x16x32_bf16 v[92:95], v[136:139], v[184:187], v[92:95]\n\tv_mfma_f32_16x16x32_bf16 v[104:107], v[140:143], v[184:187], v[104:107]\n\tv_mfma_f32_16x16x32_bf16 v[116:119], v[144:147], v[184:187], v[116:119]\n\t"
            "s_waitcnt lgkmcnt(10)\n\tv_mfma_f32_16x16x32_bf16 v[36:39], v[188:191], v[148:151], v[36:39]\n\ts_waitcnt lgkmcnt(9)\n\tv_mfma_f32_16x16x32_bf16 v[48:51], v[188:191], v[152:155], v[48:51]\n\ts_waitcnt lgkmcnt(8)\n\tv_mfma_f32_16x16x32_bf16 v[60:63], v[188:191], v[156:159], v[60:63]\n\t"
            "s_waitcnt lgkmcnt(7)\n\tv_mfma_f32_16x16x32_bf16 v[72:75], v[188:191], v[160:163], v[72:75]\n\ts_waitcnt lgkmcnt(6)\n\tv_mfma_f32_16x16x32_bf16 v[84:87], v[188:191], v[164:167], v[84:87]\n\ts_waitcnt lgkmcnt(5)\n\tv_mfma_f32_16x16x32_bf16 v[96:99], v[188:191], v[168:171], v[96:99]\n\t"
            "s_waitcnt lgkmcnt(4)\n\tv_mfma_f32_16x16x32_bf16 v[108:111], v[188:191], v[172:175], v[108:111]\n\ts_waitcnt lgkmcnt(2)\n\tv_mfma_f32_16x16x32_bf16 v[40:43], v[192:195], v[148:151], v[40:43]\n\tv_mfma_f32_16x16x32_bf16 v[52:55], v[192:195], v[152:155], v[52:55]\n\tv_mfma_f32_16x16x32_bf16 v[64:67], v[192:195], v[156:159], v[64:67]\n\t"
            "v_mfma_f32_16x16x32_bf16 v[76:79], v[192:195], v[160:163], v[76:79]\n\ts_waitcnt lgkmcnt(0)\n\ts_barrier\n\t"
            ST(15)
            "ds_read_b64_tr_b16 v[176:177], v12 offset:0\n\tds_read_b64_tr_b16 v[178:179], v12 offset:512\n\tv_mfma_f32_16x16x32_bf16 v[88:91], v[192:195], v[164:167], v[88:91]\n\tds_read_b128 v[120:123], v6\n\tv_mfma_f32_16x16x32_bf16 v[100:103], v[192:195], v[168:171], v[100:103]\n\tds_read_b128 v[124:127], v2 offset:2048\n\t"
            "v_mfma_f32_16x16x32_bf16 v[112:115], v[192:195], v[172:175], v[112:115]\n\tds_read_b128 v[128:131], v2 offset:4096\n\tv_mfma_f32_16x16x32_bf16 v[44:47], v[148:151], v[196:199], v[44:47]\n\tds_read_b128 v[132:135], v2 offset:6144\n\tv_mfma_f32_16x16x32_bf16 v[56:59], v[152:155], v[196:199], v[56:59]\n\tds_read_b128 v[136:139], v2 offset:8192\n\t"
            "v_mfma_f32_16x16x32_bf16 v[68:71], v[156:159], v[196:199], v[68:71]\n\tds_read_b128 v[140:143], v2 offset:10240\n\tv_mfma_f32_16x16x32_bf16 v[80:83], v[160:163], v[196:199], v[80:83]\n\tds_read_b128 v[144:147], v2 offset:12288\n\tv_mfma_f32_16x16x32_bf16 v[92:95], v[164:167], v[196:199], v[92:95]\n\tds_read_b64_tr_b16 v[180:181], v12 offset:1024\n\t"
            "ds_read_b64_tr_b16 v[182:183], v12 offset:1536\n\tv_mfma_f32_16x16x32_bf16 v[104:107], v[168:171], v[196:199], v[104:107]\n\tv_mfma_f32_16x16x32_bf16 v[116:119], v[172:175], v[196:199], v[116:119]\n\ts_waitcnt lgkmcnt(8)\n\tv_mfma_f32_16x16x32_bf16 v[36:39], v[176:179], v[120:123], v[36:39]\n\ts_waitcnt lgkmcnt(7)\n\t"
            "v_mfma_f32_16x16x32_bf16 v[48:51], v[176:179], v[124:127], v[48:51]\n\ts_waitcnt lgkmcnt(6)\n\tv_mfma_f32_16x16x32_bf16 v[60:63], v[176:179], v[128:131], v[60:63]\n\tds_read_b64_tr_b16 v[188:189], v12 offset:12288\n\tds_read_b64_tr_b16 v[190:191], v12 offset:12800\n\ts_waitcnt lgkmcnt(7)\n\t"
            "v_mfma_f32_16x16x32_bf16 v[72:75], v[176:179], v[132:135], v[72:75]\n\tds_read_b128 v[148:151], v7\n\ts_waitcnt lgkmcnt(7)\n\tv_mfma_f32_16x16x32_bf16 v[84:87], v[176:179], v[136:139], v[84:87]\n\tds_read_b128 v[152:155], v3 offset:2048\n\ts_waitcnt lgkmcnt(7)\n\t"
            "v_mfma_f32_16x16x32_bf16 v[96:99], v[176:179], v[140:143], v[96:99]\n\tds_read_b128 v[156:159], v3 offset:4096\n\ts_waitcnt lgkmcnt(7)\n\tv_mfma_f32_16x16x32_bf16 v[108:111], v[176:179], v[144:147], v[108:111]\n\tds_read_b128 v[160:163], v3 offset:6144\n\ts_waitcnt lgkmcnt(6)\n\t"
            "v_mfma_f32_16x16x32_bf16 v[40:43], v[180:183], v[120:123], v[40:43]\n\tds_read_b128 v[164:167], v3 offset:8192\n\tv_mfma_f32_16x16x32_bf16 v[52:55], v[180:183], v[124:127], v[52:55]\n\tds_read_b128 v[168:171], v3 offset:10240\n\tv_mfma_f32_16x16x32_bf16 v[64:67], v[180:183], v[128:131], v[64:67]\n\tds_read_b128 v[172:175], v3 offset:12288\n\t"
            "v_mfma_f32_16x16x32_bf16 v[76:79], v[180:183], v[132:135], v[76:79]\n\tds_read_b64_tr_b16 v[192:193], v12 offset:13312\n\tds_read_b64_tr_b16 v[194:195], v12 offset:13824\n\tv_mfma_f32_16x16x32_bf16 v[88:91], v[180:183], v[136:139], v[88:91]\n\tv_mfma_f32_16x16x32_bf16 v[100:103], v[180:183], v[140:143], v[100:103]\n\tv_mfma_f32_16x16x32_bf16 v[112:115], v[180:183], v[144:147], v[112:115]\n\t"
            "s_waitcnt lgkmcnt(8)\n\tv_mfma_f32_16x16x32_bf16 v[36:39], v[188:191], v[148:151], v[36:39]\n\ts_waitcnt lgkmcnt(7)\n\tv_mfma_f32_16x16x32_bf16 v[48:51], v[188:191], v[152:155], v[48:51]\n\ts_waitcnt lgkmcnt(6)\n\tv_mfma_f32_16x16x32_bf16 v[60:63], v[188:191], v[156:159], v[60:63]\n\t"
            "s_waitcnt lgkmcnt(5)\n\tv_mfma_f32_16x16x32_bf16 v[72:75], v[188:191], v[160:163], v[72:75]\n\ts_waitcnt lgkmcnt(0)\n\ts_barrier\n\t"
            ST(17)
            "ds_read_b64_tr_b16 v[176:177], v13 offset:0\n\tds_read_b64_tr_b16 v[178:179], v13 offset:512\n\tv_mfma_f32_16x16x32_bf16 v[84:87], v[188:191], v[164:167], v[84:87]\n\tds_read_b128 v[120:123], v8\n\tv_mfma_f32_16x16x32_bf16 v[96:99], v[188:191], v[168:171], v[96:99]\n\tds_read_b128 v[124:127], v4 offset:2048\n\t"
            "v_mfma_f32_16x16x32_bf16 v[108:111], v[188:191], v[172:175], v[108:111]\n\tds_read_b128 v[128:131], v4 offset:4096\n\tv_mfma_f32_16x16x32_bf16 v[40:43], v[192:195], v[148:151], v[40:43]\n\tds_read_b128 v[132:135], v4 offset:6144\n\tv_mfma_f32_16x16x32_bf16 v[52:55], v[192:195], v[152:155], v[52:55]\n\tds_read_b128 v[136:139], v4 offset:8192\n\t"
            "v_mfma_f32_16x16x32_bf16 v[64:67], v[192:195], v[156:159], v[64:67]\n\tds_read_b128 v[140:143], v4 offset:10240\n\tv_mfma_f32_16x16x32_bf16 v[76:79], v[192:195], v[160:163], v[76:79]\n\tds_read_b128 v[144:147], v4 offset:12288\n\tv_mfma_f32_16x16x32_bf16 v[88:91], v[192:195], v[164:167], v[88:91]\n\tds_read_b64_tr_b16 v[180:181], v13 offset:1024\n\t"
            "ds_read_b64_tr_b16 v[182:183], v13 offset:1536\n\tv_mfma_f32_16x16x32_bf16 v[100:103], v[192:195], v[168:171], v[100:103]\n\tds_read_b64_tr_b16 v[184:185], v13 offset:2048\n\tds_read_b64_tr_b16 v[186:187], v13 offset:2560\n\tv_mfma_f32_16x16x32_bf16 v[112:115], v[192:195], v[172:175], v[112:115]\n\tds_read2_b32 v[208:209], v14 offset1:16\n\t"
            SA(2) PRIA
            "s_mov_b64 vcc, s[18:19]\n\ts_bitcmp1_b32 9, s69\n\tds_read_b64_tr_b16 v[188:189], v13 offset:12288\n\tv_mov_b32 v252, -1\n\tds_read_b64_tr_b16 v[190:191], v13 offset:12800\n\tv_mov_b32 v254, -1\n\t"
            "ds_read_b128 v[148:151], v9\n\ts_waitcnt lgkmcnt(6)\n\tv_mfma_f32_16x16x32_bf16 v[40:43], v[180:183], v[120:123], v[40:43]\n\tds_read_b128 v[152:155], v5 offset:2048\n\tv_mfma_f32_16x16x32_bf16 v[36:39], v[176:179], v[120:123], v[36:39]\n\tds_read_b128 v[156:159], v5 offset:4096\n\t"
            "v_mfma_f32_16x16x32_bf16 v[48:51], v[176:179], v[124:127], v[48:51]\n\tds_read_b128 v[160:163], v5 offset:6144\n\tv_mfma_f32_16x16x32_bf16 v[60:63], v[176:179], v[128:131], v[60:63]\n\tds_read_b128 v[164:167], v5 offset:8192\n\tv_mfma_f32_16x16x32_bf16 v[52:55], v[180:183], v[124:127], v[52:55]\n\tds_read_b128 v[168:171], v5 offset:10240\n\t"
            "v_mfma_f32_16x16x32_bf16 v[64:67], v[180:183], v[128:131], v[64:67]\n\tds_read_b128 v[172:175], v5 offset:12288\n\tv_mfma_f32_16x16x32_bf16 v[76:79], v[180:183], v[132:135], v[76:79]\n\tds_read_b64_tr_b16 v[192:193], v13 offset:13312\n\ts_waitcnt lgkmcnt(7)\n\tv_mfma_f32_16x16x32_bf16 v[36:39], v[188:191], v[148:151], v[36:39]\n\t"
            "ds_read_b64_tr_b16 v[194:195], v13 offset:13824\n\tds_read_b64_tr_b16 v[196:197], v13 offset:14336\n\ts_waitcnt lgkmcnt(8)\n\tv_mfma_f32_16x16x32_bf16 v[48:51], v[188:191], v[152:155], v[48:51]\n\tds_read_b64_tr_b16 v[198:199], v13 offset:14848\n\tds_read2_b32 v[210:211], v14 offset0:32 offset1:48\n\t"
            "v_mfma_f32_16x16x32_bf16 v[72:75], v[176:179], v[132:135], v[72:75]\n\ts_nop 0\n\tv_mov_b32_dpp v200, v36 row_newbcast:0 row_mask:0xf bank_mask:0xf bound_ctrl:1\n\tds_read2_b32 v[212:213], v14 offset0:64 offset1:80\n\ts_waitcnt lgkmcnt(9)\n\tv_mfma_f32_16x16x32_bf16 v[60:63], v[188:191], v[156:159], v[60:63]\n\t"
            "v_mov_b32_dpp v201, v37 row_newbcast:0 row_mask:0xf bank_mask:0xf bound_ctrl:1\n\tds_read_b32 v214, v14 offset:384\n\tv_mfma_f32_16x16x32_bf16 v[72:75], v[188:191], v[160:163], v[72:75]\n\tv_mov_b32_dpp v202, v38 row_newbcast:0 row_mask:0xf bank_mask:0xf bound_ctrl:1\n\tv_mov_b32_dpp v203, v39 row_newbcast:0 row_mask:0xf bank_mask:0xf bound_ctrl:1\n\tv_fmac_f32 v36, v208, v200\n\t"
            "v_fmac_f32 v37, v208, v201\n\tv_fmac_f32 v38, v208, v202\n\tv_fmac_f32 v39, v208, v203\n\tv_cvt_pk_bf16_f32 v250, v36, v37\n\tv_cvt_pk_bf16_f32 v251, v38, v39\n\tv_and_b32 v36, v250, v18\n\t"
            "v_and_b32 v37, v251, v18\n\ts_waitcnt lgkmcnt(2)\n\tv_mfma_f32_16x16x32_bf16 v[40:43], v[192:195], v[148:151], v[40:43]\n\tv_and_b32 v38, v250, v19\n\tv_and_b32 v39, v251, v19\n\tv_mfma_f32_16x16x32_bf16 v[52:55], v[192:195], v[152:155], v[52:55]\n\t"
            "v_and_b32 v222, v250, v20\n\tv_and_b32 v223, v251, v20\n\tv_mfma_f32_16x16x32_bf16 v[64:67], v[192:195], v[156:159], v[64:67]\n\ts_nop 1\n\tv_mov_b32_dpp v204, v40 row_newbcast:0 row_mask:0xf bank_mask:0xf bound_ctrl:1\n\tv_mov_b32_dpp v205, v41 row_newbcast:0 row_mask:0xf bank_mask:0xf bound_ctrl:1\n\t"
            "v_mfma_f32_16x16x32_bf16 v[76:79], v[192:195], v[160:163], v[76:79]\n\tv_mov_b32_dpp v206, v42 row_newbcast:0 row_mask:0xf bank_mask:0xf bound_ctrl:1\n\tv_mov_b32_dpp v207, v43 row_newbcast:0 row_mask:0xf bank_mask:0xf bound_ctrl:1\n\tv_fmac_f32 v40, v208, v204\n\tv_fmac_f32 v41, v208, v205\n\tv_fmac_f32 v42, v208, v206\n\t"
            "v_fmac_f32 v43, v208, v207\n\tv_cvt_pk_bf16_f32 v40, v40, v41\n\tv_cvt_pk_bf16_f32 v41, v42, v43\n\tv_cndmask_b32 v42, v40, v40, vcc\n\tv_cndmask_b32 v43, v41, v41, vcc\n\tv_and_b32 v224, v250, v21\n\t"
            "v_and_b32 v225, v251, v21\n\tv_mfma_f32_16x16x32_bf16 v[218:221], v[40:43], v[36:39], 0\n\tv_fmac_f32 v48, v209, v200\n\tv_fmac_f32 v49, v209, v201\n\tv_mfma_f32_16x16x32_bf16 v[226:229], v[40:43], v[222:225], 0\n\tv_fmac_f32 v50, v209, v202\n\t"
            "v_fmac_f32 v51, v209, v203\n\tv_cvt_pk_bf16_f32 v250, v48, v49\n\ts_nop 1\n\tv_cndmask_b32 v36, v220, v218, s[14:15]\n\tv_cndmask_b32 v37, v221, v219, s[16:17]\n\tv_cvt_pk_bf16_f32 v251, v50, v51\n\t"
            "v_and_b32 v48, v250, v18\n\tv_cndmask_b32 v38, v228, v226, s[14:15]\n\tv_cndmask_b32 v39, v229, v227, s[16:17]\n\tv_and_b32 v49, v251, v18\n\tv_and_b32 v50, v250, v19\n\tv_and_b32 v51, v251, v19\n\t"
            "v_and_b32 v234, v250, v20\n\tv_and_b32 v235, v251, v20\n\tv_and_b32 v236, v250, v21\n\tv_and_b32 v237, v251, v21\n\tv_fmac_f32 v60, v210, v200\n\tv_fmac_f32 v61, v210, v201\n\t"
            "v_fmac_f32 v62, v210, v202\n\tv_fmac_f32 v63, v210, v203\n\tv_fmac_f32 v52, v209, v204\n\tv_fmac_f32 v53, v209, v205\n\tv_fmac_f32 v64, v210, v204\n\tv_fmac_f32 v65, v210, v205\n\t"
            "v_cvt_pk_bf16_f32 v250, v60, v61\n\tv_cvt_pk_bf16_f32 v251, v62, v63\n\tv_fmac_f32 v76, v211, v204\n\tv_fmac_f32 v77, v211, v205\n\tv_fmac_f32 v54, v209, v206\n\tv_fmac_f32 v55, v209, v207\n\t"
            "v_cvt_pk_bf16_f32 v52, v52, v53\n\tv_fmac_f32 v66, v210, v206\n\tv_fmac_f32 v67, v210, v207\n\tv_cvt_pk_bf16_f32 v64, v64, v65\n\tv_and_b32 v60, v250, v18\n\tv_and_b32 v61, v251, v18\n\t"
            "v_and_b32 v62, v250, v19\n\tv_and_b32 v63, v251, v19\n\tv_and_b32 v246, v250, v20\n\tv_and_b32 v247, v251, v20\n\tv_and_b32 v248, v250, v21\n\tv_and_b32 v249, v251, v21\n\t"
            "v_fmac_f32 v78, v211, v206\n\tv_fmac_f32 v79, v211, v207\n\tv_fmac_f32 v72, v211, v200\n\tv_fmac_f32 v73, v211, v201\n\tv_fmac_f32 v74, v211, v202\n\tv_fmac_f32 v75, v211, v203\n\t"
            "v_cvt_pk_bf16_f32 v76, v76, v77\n\tv_cvt_pk_bf16_f32 v53, v54, v55\n\tv_cvt_pk_bf16_f32 v65, v66, v67\n\tv_cvt_pk_bf16_f32 v77, v78, v79\n\tv_cvt_pk_bf16_f32 v250, v72, v73\n\tv_cvt_pk_bf16_f32 v251, v74, v75\n\t"
            "v_cndmask_b32 v54, v40, v52, vcc\n\tv_cndmask_b32 v55, v41, v53, vcc\n\tv_cndmask_b32 v66, v52, v64, vcc\n\tv_cndmask_b32 v67, v53, v65, vcc\n\tv_mfma_f32_16x16x32_bf16 v[230:233], v[52:55], v[48:51], 0\n\tv_cndmask_b32 v78, v64, v76, vcc\n\t"
            "v_cndmask_b32 v79, v65, v77, vcc\n\tv_mfma_f32_16x16x32_bf16 v[242:245], v[64:67], v[60:63], 0\n\tv_and_b32 v72, v250, v18\n\tv_and_b32 v73, v251, v18\n\tv_mfma_f32_16x16x32_bf16 v[238:241], v[52:55], v[234:237], 0\n\tv_and_b32 v74, v250, v19\n\t"
            "v_and_b32 v75, v251, v19\n\tv_mfma_f32_16x16x32_bf16 v[218:221], v[64:67], v[246:249], 0\n\tv_cndmask_b32 v48, v232, v230, s[14:15]\n\tv_cndmask_b32 v49, v233, v231, s[16:17]\n\tv_mfma_f32_16x16x32_bf16 v[222:225], v[76:79], v[72:75], 0\n\tv_cndmask_b32 v60, v244, v242, s[14:15]\n\t"
            "v_cndmask_b32 v61, v245, v243, s[16:17]\n\tv_mfma_f32_16x16x32_bf16 v[84:87], v[176:179], v[136:139], v[84:87]\n\tv_cndmask_b32 v50, v240, v238, s[14:15]\n\tv_cndmask_b32 v51, v241, v239, s[16:17]\n\tv_mfma_f32_16x16x32_bf16 v[96:99], v[176:179], v[140:143], v[96:99]\n\ts_nop 1\n\t"
            "v_cndmask_b32 v72, v224, v222, s[14:15]\n\tv_cndmask_b32 v73, v225, v223, s[16:17]\n\tv_mfma_f32_16x16x32_bf16 v[88:91], v[180:183], v[136:139], v[88:91]\n\ts_cbranch_scc0 1f\n\tv_cvt_pk_bf16_f32 v253, v36, v37\n\tv_min_u32 v253, -2, v253\n\t"
            "v_cvt_pk_bf16_f32 v255, v48, v49\n\tv_min_u32 v255, -2, v255\n\tbuffer_store_dwordx4 v[252:255], v17, s[44:47], s20 offen sc1\n\ts_nop 1\n\tv_cvt_pk_bf16_f32 v253, v60, v61\n\tv_min_u32 v253, -2, v253\n\t"
            "v_cvt_pk_bf16_f32 v255, v72, v73\n\tv_min_u32 v255, -2, v255\n\tbuffer_store_dwordx4 v[252:255], v17, s[44:47], s21 offen sc1\n\t1:\nv_cndmask_b32 v62, v220, v218, s[14:15]\n\tv_mfma_f32_16x16x32_bf16 v[100:103], v[180:183], v[140:143], v[100:103]\n\t"
            "v_cndmask_b32 v63, v221, v219, s[16:17]\n\tv_and_b32 v226, v250, v20\n\tv_mfma_f32_16x16x32_bf16 v[112:115], v[180:183], v[144:147], v[112:115]\n\tv_and_b32 v227, v251, v20\n\tv_and_b32 v228, v250, v21\n\tv_mfma_f32_16x16x32_bf16 v[84:87], v[188:191], v[164:167], v[84:87]\n\t"
            "v_and_b32 v229, v251, v21\n\tv_mov_b32 v180, 0\n\tv_mfma_f32_16x16x32_bf16 v[108:111], v[176:179], v[144:147], v[108:111]\n\tv_mov_b32 v176, 0\n\tds_write_b64 v28, v[36:37] offset:0\n\tv_mfma_f32_16x16x32_bf16 v[230:233], v[76:79], v[226:229], 0\n\t"
            "s_waitcnt lgkmcnt(1)\n\ts_nop 0\n\tv_fmac_f32 v84, v212, v200\n\tv_fmac_f32 v85, v212, v201\n\tv_mfma_f32_16x16x32_bf16 v[96:99], v[188:191], v[168:171], v[96:99]\n\tds_write_b64 v29, v[38:39] offset:0\n\t"
            "v_fmac_f32 v86, v212, v202\n\tv_mfma_f32_16x16x32_bf16 v[88:91], v[192:195], v[164:167], v[88:91]\n\tv_cndmask_b32 v74, v232, v230, s[14:15]\n\tv_cndmask_b32 v75, v233, v231, s[16:17]\n\tds_write_b64 v28, v[48:49] offset:512\n\tv_mfma_f32_16x16x32_bf16 v[100:103], v[192:195], v[168:171], v[100:103]\n\t"
            "v_fmac_f32 v87, v212, v203\n\tv_cvt_pk_bf16_f32 v250, v84, v85\n\tv_mfma_f32_16x16x32_bf16 v[112:115], v[192:195], v[172:175], v[112:115]\n\tds_write_b64 v29, v[50:51] offset:512\n\tv_cvt_pk_bf16_f32 v251, v86, v87\n\tv_mfma_f32_16x16x32_bf16 v[108:111], v[188:191], v[172:175], v[108:111]\n\t"
            "v_and_b32 v84, v250, v18\n\tv_and_b32 v85, v251, v18\n\tds_write_b64 v28, v[60:61] offset:1024\n\tv_and_b32 v86, v250, v19\n\tv_and_b32 v87, v251, v19\n\tv_and_b32 v238, v250, v20\n\t"
            "v_and_b32 v239, v251, v20\n\tv_and_b32 v240, v250, v21\n\tds_write_b64 v29, v[62:63] offset:1024\n\tv_and_b32 v241, v251, v21\n\tv_fmac_f32 v96, v213, v200\n\tv_fmac_f32 v97, v213, v201\n\t"
            "v_fmac_f32 v98, v213, v202\n\tv_fmac_f32 v99, v213, v203\n\tds_write_b64 v28, v[72:73] offset:1536\n\tv_fmac_f32 v88, v212, v204\n\tv_fmac_f32 v89, v212, v205\n\tv_fmac_f32 v100, v213, v204\n\t"
            "v_fmac_f32 v101, v213, v205\n\tv_cvt_pk_bf16_f32 v250, v96, v97\n\tds_write_b64 v29, v[74:75] offset:1536\n\tv_cvt_pk_bf16_f32 v251, v98, v99\n\tv_fmac_f32 v112, v214, v204\n\tv_fmac_f32 v113, v214, v205\n\t"
            "v_fmac_f32 v90, v212, v206\n\tv_fmac_f32 v91, v212, v207\n\tds_read_b128 v[36:39], v15 offset:0\n\tv_cvt_pk_bf16_f32 v88, v88, v89\n\tv_fmac_f32 v102, v213, v206\n\tv_fmac_f32 v103, v213, v207\n\t"
            "ds_read_b128 v[48:51], v15 offset:64\n\tv_cvt_pk_bf16_f32 v100, v100, v101\n\ts_waitcnt lgkmcnt(0)\n\ts_barrier\n\tv_and_b32 v96, v250, v18\n\tds_read_b128 v[60:63], v15 offset:128\n\t"
            "v_and_b32 v97, v251, v18\n\tv_and_b32 v98, v250, v19\n\tv_and_b32 v99, v251, v19\n\tds_read_b128 v[72:75], v15 offset:192\n\tv_and_b32 v218, v250, v20\n\tv_and_b32 v219, v251, v20\n\t"
            "v_and_b32 v220, v250, v21\n\tv_and_b32 v221, v251, v21\n\tv_fmac_f32 v114, v214, v206\n\tv_fmac_f32 v115, v214, v207\n\tv_fmac_f32 v108, v214, v200\n\tv_fmac_f32 v109, v214, v201\n\t"
            "v_fmac_f32 v110, v214, v202\n\tv_fmac_f32 v111, v214, v203\n\tv_cvt_pk_bf16_f32 v112, v112, v113\n\tv_cvt_pk_bf16_f32 v89, v90, v91\n\tv_cvt_pk_bf16_f32 v101, v102, v103\n\tv_cvt_pk_bf16_f32 v113, v114, v115\n\t"
            "v_cvt_pk_bf16_f32 v250, v108, v109\n\tv_cvt_pk_bf16_f32 v251, v110, v111\n\tv_cndmask_b32 v90, v76, v88, vcc\n\tv_cndmask_b32 v91, v77, v89, vcc\n\tv_cndmask_b32 v102, v88, v100, vcc\n\tv_cndmask_b32 v103, v89, v101, vcc\n\t"
            "v_mfma_f32_16x16x32_bf16 v[234:237], v[88:91], v[84:87], 0\n\tv_cndmask_b32 v114, v100, v112, vcc\n\tv_cndmask_b32 v115, v101, v113, vcc\n\tv_mfma_f32_16x16x32_bf16 v[246:249], v[100:103], v[96:99], 0\n\tv_and_b32 v108, v250, v18\n\tv_and_b32 v109, v251, v18\n\t"
            "v_mfma_f32_16x16x32_bf16 v[242:245], v[88:91], v[238:241], 0\n\tv_and_b32 v110, v250, v19\n\tv_and_b32 v111, v251, v19\n\tv_mfma_f32_16x16x32_bf16 v[222:225], v[100:103], v[218:221], 0\n\tv_cndmask_b32 v84, v236, v234, s[14:15]\n\tv_cndmask_b32 v85, v237, v235, s[16:17]\n\t"
            "ds_write_b64 v28, v[84:85] offset:38912\n\tv_mfma_f32_16x16x32_bf16 v[226:229], v[112:115], v[108:111], 0\n\tv_cndmask_b32 v96, v248, v246, s[14:15]\n\tv_cndmask_b32 v97, v249, v247, s[16:17]\n\tv_cndmask_b32 v86, v244, v242, s[14:15]\n\tds_write_b64 v28, v[96:97] offset:39424\n\t"
            "v_cndmask_b32 v87, v245, v243, s[16:17]\n\tv_cndmask_b32 v98, v224, v222, s[14:15]\n\tv_cndmask_b32 v99, v225, v223, s[16:17]\n\ts_nop 0\n\tv_cndmask_b32 v108, v228, v226, s[14:15]\n\tv_cndmask_b32 v109, v229, v227, s[16:17]\n\t"
            "ds_write_b64 v29, v[86:87] offset:38912\n\ts_cbranch_scc0 1f\n\tv_cvt_pk_bf16_f32 v253, v84, v85\n\tv_min_u32 v253, -2, v253\n\tv_cvt_pk_bf16_f32 v255, v96, v97\n\tv_min_u32 v255, -2, v255\n\t"
            "buffer_store_dwordx4 v[252:255], v17, s[44:47], s22 offen sc1\n\ts_nop 1\n\tv_cvt_pk_bf16_f32 v253, v108, v109\n\tv_min_u32 v253, -2, v253\n\tbuffer_store_dwordx4 v[252:255], v17, s[44:47], s23 offen sc1\n\t1:\n"
            "v_and_b32 v230, v250, v20\n\tv_and_b32 v231, v251, v20\n\tv_and_b32 v232, v250, v21\n\tv_and_b32 v233, v251, v21\n\tds_write_b64 v29, v[98:99] offset:39424\n\tv_mfma_f32_16x16x32_bf16 v[44:47], v[120:123], v[184:187], v[44:47]\n\t"
            "v_mov_b32 v218, 0\n\tv_mov_b32 v219, 0\n\tv_mfma_f32_16x16x32_bf16 v[234:237], v[112:115], v[230:233], 0\n\tds_write_b64 v28, v[108:109] offset:39936\n\tv_mov_b32 v188, 0\n\tv_mfma_f32_16x16x32_bf16 v[56:59], v[124:127], v[184:187], v[56:59]\n\t"
            "v_mov_b32 v192, 0\n\tds_read_b128 v[120:123], v6\n\ts_nop 2\n\tv_cndmask_b32 v110, v236, v234, s[14:15]\n\tv_cndmask_b32 v111, v237, v235, s[16:17]\n\tv_mfma_f32_16x16x32_bf16 v[68:71], v[128:131], v[184:187], v[68:71]\n\t"
            "ds_write_b64 v29, v[110:111] offset:39936\n\tv_mov_b32 v234, 0\n\tv_mfma_f32_16x16x32_bf16 v[80:83], v[132:135], v[184:187], v[80:83]\n\tv_mov_b32 v235, 0\n\tds_write_b32 v33, v33\n\t"
            SA(3)
            "v_mfma_f32_16x16x32_bf16 v[92:95], v[136:139], v[184:187], v[92:95]\n\tds_read_b128 v[124:127], v2 offset:2048\n\tv_mfma_f32_16x16x32_bf16 v[104:107], v[140:143], v[184:187], v[104:107]\n\tds_read_b128 v[128:131], v2 offset:4096\n\tv_mfma_f32_16x16x32_bf16 v[116:119], v[144:147], v[184:187], v[116:119]\n\tds_read_b64_tr_b16 v[184:185], v12 offset:2048\n\t"
            "v_mfma_f32_16x16x32_bf16 v[44:47], v[148:151], v[196:199], v[44:47]\n\tds_read_b64_tr_b16 v[186:187], v12 offset:2560\n\tds_read_b128 v[148:151], v7\n\tv_mfma_f32_16x16x32_bf16 v[56:59], v[152:155], v[196:199], v[56:59]\n\tds_read_b128 v[152:155], v3 offset:2048\n\tv_mfma_f32_16x16x32_bf16 v[68:71], v[156:159], v[196:199], v[68:71]\n\t"
            "ds_read_b128 v[132:135], v2 offset:6144\n\tv_mfma_f32_16x16x32_bf16 v[80:83], v[160:163], v[196:199], v[80:83]\n\tds_read_b128 v[156:159], v3 offset:4096\n\tv_mfma_f32_16x16x32_bf16 v[92:95], v[164:167], v[196:199], v[92:95]\n\tds_read_b128 v[160:163], v3 offset:6144\n\tv_mfma_f32_16x16x32_bf16 v[104:107], v[168:171], v[196:199], v[104:107]\n\t"
            "ds_read_b128 v[136:139], v2 offset:8192\n\tv_mfma_f32_16x16x32_bf16 v[116:119], v[172:175], v[196:199], v[116:119]\n\tds_read_b64_tr_b16 v[196:197], v12 offset:14336\n\tds_read_b64_tr_b16 v[198:199], v12 offset:14848\n\ts_waitcnt lgkmcnt(8)\n\tv_mfma_f32_16x16x32_bf16 v[44:47], v[120:123], v[184:187], v[44:47]\n\t"
            "ds_read_b128 v[140:143], v2 offset:10240\n\tv_mfma_f32_16x16x32_bf16 v[56:59], v[124:127], v[184:187], v[56:59]\n\tds_read_b128 v[144:147], v2 offset:12288\n\tv_mfma_f32_16x16x32_bf16 v[68:71], v[128:131], v[184:187], v[68:71]\n\tds_read_b128 v[164:167], v3 offset:8192\n\ts_waitcnt lgkmcnt(8)\n\t"
            "v_mfma_f32_16x16x32_bf16 v[80:83], v[132:135], v[184:187], v[80:83]\n\tds_read_b128 v[168:171], v3 offset:10240\n\tds_read_b128 v[172:175], v3 offset:12288\n\ts_waitcnt lgkmcnt(7)\n\tv_mfma_f32_16x16x32_bf16 v[92:95], v[136:139], v[184:187], v[92:95]\n\tds_read_b128 v[84:87], v15 offset:256\n\t"
            "s_waitcnt lgkmcnt(6)\n\tv_mfma_f32_16x16x32_bf16 v[44:47], v[148:151], v[196:199], v[44:47]\n\tds_read_b128 v[96:99], v15 offset:320\n\tv_mfma_f32_16x16x32_bf16 v[56:59], v[152:155], v[196:199], v[56:59]\n\tds_read_b128 v[108:111], v15 offset:384\n\tv_mfma_f32_16x16x32_bf16 v[68:71], v[156:159], v[196:199], v[68:71]\n\t"
            "s_nop 3\n\tv_mov_b32 v216, v44\n\tv_mov_b32 v217, v44\n\tv_mfma_f32_16x16x32_bf16 v[80:83], v[160:163], v[196:199], v[80:83]\n\ts_nop 0\n\tv_permlane32_swap_b32 v216, v217\n\t"
            "v_mov_b32 v217, v216\n\ts_waitcnt lgkmcnt(5)\n\tv_mfma_f32_16x16x32_bf16 v[104:107], v[140:143], v[184:187], v[104:107]\n\tv_permlane16_swap_b32 v216, v217\n\tv_fmac_f32 v44, v36, v216\n\tv_mfma_f32_16x16x32_bf16 v[116:119], v[144:147], v[184:187], v[116:119]\n\t"
            "v_fmac_f32 v45, v37, v216\n\tv_fmac_f32 v46, v38, v216\n\tv_fmac_f32 v47, v39, v216\n\tv_cvt_pk_bf16_f32 v44, v44, v45\n\tv_cvt_pk_bf16_f32 v45, v46, v47\n\tv_mfma_f32_16x16x32_bf16 v[92:95], v[164:167], v[196:199], v[92:95]\n\t"
            "v_and_b32 v220, v44, v22\n\tv_and_b32 v221, v45, v22\n\ts_waitcnt lgkmcnt(0)\n\tv_mfma_f32_16x16x32_bf16 v[104:107], v[168:171], v[196:199], v[104:107]\n\tv_and_b32 v236, v44, v23\n\tv_and_b32 v237, v45, v23\n\t"
            "v_mfma_f32_16x16x32_bf16 v[116:119], v[172:175], v[196:199], v[116:119]\n\tv_fmac_f32 v56, v48, v216\n\tv_fmac_f32 v57, v49, v216\n\tv_fmac_f32 v58, v50, v216\n\tv_fmac_f32 v59, v51, v216\n\tv_cvt_pk_bf16_f32 v56, v56, v57\n\t"
            "v_cvt_pk_bf16_f32 v57, v58, v59\n\tv_and_b32 v222, v56, v22\n\tv_and_b32 v223, v57, v22\n\tv_and_b32 v238, v56, v23\n\tv_and_b32 v239, v57, v23\n\tv_fmac_f32 v68, v60, v216\n\t"
            "v_fmac_f32 v69, v61, v216\n\tv_fmac_f32 v70, v62, v216\n\tv_fmac_f32 v71, v63, v216\n\tds_read_b32 v27, v32\n\tds_read2st64_b32 v[42:43], v31 offset0:0 offset1:1\n\tds_read2st64_b32 v[40:41], v30 offset0:0 offset1:1\n\t"
            "ds_read2st64_b32 v[66:67], v31 offset0:2 offset1:3\n\tds_read2st64_b32 v[64:65], v30 offset0:2 offset1:3\n\tv_cvt_pk_bf16_f32 v68, v68, v69\n\tv_cvt_pk_bf16_f32 v69, v70, v71\n\tv_and_b32 v224, v68, v22\n\tv_and_b32 v225, v69, v22\n\t"
            "v_and_b32 v240, v68, v23\n\tv_and_b32 v241, v69, v23\n\tv_fmac_f32 v80, v72, v216\n\tv_fmac_f32 v81, v73, v216\n\tv_fmac_f32 v82, v74, v216\n\tv_fmac_f32 v83, v75, v216\n\t"
            "v_cvt_pk_bf16_f32 v80, v80, v81\n\tv_cvt_pk_bf16_f32 v81, v82, v83\n\tv_and_b32 v226, v80, v22\n\tv_and_b32 v227, v81, v22\n\tv_and_b32 v242, v80, v23\n\tv_and_b32 v243, v81, v23\n\t"
            SA(6)
            "s_waitcnt lgkmcnt(2)\n\tv_cmp_ge_u32 vcc, 0, v27\n\ts_cbranch_vccnz 10f\n\t20:\nv_and_b32 v177, v42, v24\n\tv_and_b32 v178, v42, v25\n\t"
            "v_and_b32 v179, v42, v26\n\tv_and_b32 v189, v43, v24\n\tv_and_b32 v190, v43, v25\n\tv_and_b32 v191, v43, v26\n\tv_mfma_f32_16x16x32_bf16 v[44:47], v[234:237], v[176:179], 0\n\tv_and_b32 v181, v40, v24\n\t"
            "v_and_b32 v182, v40, v25\n\tv_mfma_f32_16x16x32_bf16 v[56:59], v[236:239], v[188:191], 0\n\tv_and_b32 v183, v40, v26\n\tv_and_b32 v193, v41, v24\n\tv_and_b32 v194, v41, v25\n\tv_and_b32 v195, v41, v26\n\t"
            "v_mfma_f32_16x16x32_bf16 v[44:47], v[218:221], v[180:183], v[44:47]\n\ts_waitcnt lgkmcnt(0)\n\tv_and_b32 v177, v66, v24\n\tv_and_b32 v178, v66, v25\n\tv_mfma_f32_16x16x32_bf16 v[56:59], v[220:223], v[192:195], v[56:59]\n\tv_and_b32 v179, v66, v26\n\t"
            "v_and_b32 v189, v67, v24\n\tv_and_b32 v190, v67, v25\n\ts_nop 0\n\tv_cvt_pk_bf16_f32 v36, v44, v45\n\tv_cvt_pk_bf16_f32 v37, v46, v47\n\tv_min_u32 v36, -2, v36\n\t"
            "v_and_b32 v191, v67, v26\n\tv_cvt_pk_bf16_f32 v38, v56, v57\n\tv_cvt_pk_bf16_f32 v39, v58, v59\n\tv_min_u32 v38, -2, v38\n\tv_mfma_f32_16x16x32_bf16 v[68:71], v[238:241], v[176:179], 0\n\ts_nop 0\n\t"
            "v_permlane16_swap_b32 v36, v38\n\tv_permlane16_swap_b32 v37, v39\n\tbuffer_store_dwordx4 v[36:39], v16, s[44:47], s20 offen sc1\n\tv_mfma_f32_16x16x32_bf16 v[80:83], v[240:243], v[188:191], 0\n\tv_and_b32 v181, v64, v24\n\tv_and_b32 v182, v64, v25\n\t"
            "v_and_b32 v183, v64, v26\n\tv_and_b32 v193, v65, v24\n\tv_and_b32 v194, v65, v25\n\tv_and_b32 v195, v65, v26\n\tv_mfma_f32_16x16x32_bf16 v[68:71], v[222:225], v[180:183], v[68:71]\n\tv_fmac_f32 v92, v84, v216\n\t"
            "v_fmac_f32 v93, v85, v216\n\tv_mfma_f32_16x16x32_bf16 v[80:83], v[224:227], v[192:195], v[80:83]\n\tv_fmac_f32 v94, v86, v216\n\tv_fmac_f32 v95, v87, v216\n\tv_cvt_pk_bf16_f32 v92, v92, v93\n\ts_nop 1\n\t"
            "v_cvt_pk_bf16_f32 v60, v68, v69\n\tv_cvt_pk_bf16_f32 v61, v70, v71\n\tv_min_u32 v60, -2, v60\n\tv_fmac_f32 v104, v96, v216\n\tv_cvt_pk_bf16_f32 v62, v80, v81\n\tv_cvt_pk_bf16_f32 v63, v82, v83\n\t"
            "v_min_u32 v62, -2, v62\n\tv_fmac_f32 v105, v97, v216\n\tv_permlane16_swap_b32 v61, v63\n\tv_permlane16_swap_b32 v60, v62\n\tbuffer_store_dwordx4 v[60:63], v16, s[44:47], s21 offen sc1\n\tds_write_b32 v34, v35\n\t"
            SA(4)
            "v_cvt_pk_bf16_f32 v93, v94, v95\n\tv_fmac_f32 v106, v98, v216\n\tv_fmac_f32 v107, v99, v216\n\tv_cvt_pk_bf16_f32 v104, v104, v105\n\tv_fmac_f32 v116, v108, v216\n\tv_fmac_f32 v117, v109, v216\n\t"
            "v_and_b32 v228, v92, v22\n\tds_read_b32 v27, v32\n\tds_read2st64_b32 v[102:103], v31 offset0:152 offset1:153\n\tds_read2st64_b32 v[100:101], v30 offset0:152 offset1:153\n\tds_read2st64_b32 v[78:79], v31 offset0:154 offset1:155\n\tds_read2st64_b32 v[76:77], v30 offset0:154 offset1:155\n\t"
            "v_and_b32 v229, v93, v22\n\tv_and_b32 v244, v92, v23\n\tv_and_b32 v245, v93, v23\n\tv_cvt_pk_bf16_f32 v105, v106, v107\n\tv_fmac_f32 v118, v110, v216\n\tv_fmac_f32 v119, v111, v216\n\t"
            "v_cvt_pk_bf16_f32 v116, v116, v117\n\tv_and_b32 v230, v104, v22\n\tv_and_b32 v231, v105, v22\n\tv_and_b32 v246, v104, v23\n\tv_and_b32 v247, v105, v23\n\tv_cvt_pk_bf16_f32 v117, v118, v119\n\t"
            "v_and_b32 v232, v116, v22\n\tv_and_b32 v233, v117, v22\n\tv_and_b32 v248, v116, v23\n\tv_and_b32 v249, v117, v23\n\ts_waitcnt lgkmcnt(3)\n\tv_cmp_ge_u32 vcc, 1, v27\n\t"
            "s_cbranch_vccnz 11f\n\t21:\nv_and_b32 v177, v102, v24\n\tv_and_b32 v178, v102, v25\n\tv_and_b32 v179, v102, v26\n\tv_and_b32 v189, v103, v24\n\t"
            "v_and_b32 v190, v103, v25\n\tv_and_b32 v191, v103, v26\n\tv_mfma_f32_16x16x32_bf16 v[92:95], v[242:245], v[176:179], 0\n\ts_waitcnt lgkmcnt(0)\n\tv_and_b32 v181, v100, v24\n\tv_and_b32 v182, v100, v25\n\t"
            "v_mfma_f32_16x16x32_bf16 v[104:107], v[244:247], v[188:191], 0\n\tv_and_b32 v183, v100, v26\n\tv_and_b32 v193, v101, v24\n\tv_and_b32 v194, v101, v25\n\tv_and_b32 v195, v101, v26\n\tv_mfma_f32_16x16x32_bf16 v[92:95], v[226:229], v[180:183], v[92:95]\n\t"
            "v_and_b32 v177, v78, v24\n\tv_and_b32 v178, v78, v25\n\tv_mfma_f32_16x16x32_bf16 v[104:107], v[228:231], v[192:195], v[104:107]\n\tv_and_b32 v179, v78, v26\n\tv_and_b32 v181, v76, v24\n\tv_and_b32 v182, v76, v25\n\t"
            "v_mfma_f32_16x16x32_bf16 v[116:119], v[246:249], v[176:179], 0\n\tv_and_b32 v183, v76, v26\n\tv_cvt_pk_bf16_f32 v84, v92, v93\n\ts_nop 1\n\tv_cvt_pk_bf16_f32 v86, v104, v105\n\tv_mfma_f32_16x16x32_bf16 v[116:119], v[230:233], v[180:183], v[116:119]\n\t"
            "v_cvt_pk_bf16_f32 v85, v94, v95\n\tv_min_u32 v84, -2, v84\n\tv_cvt_pk_bf16_f32 v87, v106, v107\n\tv_min_u32 v86, -2, v86\n\ts_nop 0\n\tv_permlane16_swap_b32 v85, v87\n\t"
            "v_permlane16_swap_b32 v84, v86\n\tbuffer_store_dwordx4 v[84:87], v16, s[44:47], s22 offen sc1\n\tv_cvt_pk_bf16_f32 v108, v116, v117\n\tv_cvt_pk_bf16_f32 v109, v118, v119\n\tv_min_u32 v108, -2, v108\n\ts_nop 0\n\t"
            "v_permlane16_swap_b32 v109, v111\n\tv_permlane16_swap_b32 v108, v110\n\tbuffer_store_dwordx4 v[108:111], v215, s[44:47], s23 offen sc1\n\tds_write_b32 v34, v34\n\t"
            SA(5)
            "s_setprio 0\n\t"
            DUMP
            "s_cmp_eq_u32 s69, 1\n\ts_cbranch_scc1 .Lout\n\ts_endpgm\n\t10:\nds_read_b32 v27, v32\n\tds_read2st64_b32 v[42:43], v31 offset0:0 offset1:1\n\t"
            "ds_read2st64_b32 v[40:41], v30 offset0:0 offset1:1\n\tds_read2st64_b32 v[66:67], v31 offset0:2 offset1:3\n\tds_read2st64_b32 v[64:65], v30 offset0:2 offset1:3\n\ts_waitcnt lgkmcnt(0)\n\tv_cmp_ge_u32 vcc, 0, v27\n\ts_cbranch_vccz 20b\n\t"
            ASLP
            "s_sub_u32 s68, s68, 1\n\ts_cbranch_scc0 10b\n\ts_branch 20b\n\t11:\nds_read_b32 v27, v32\n\tds_read2st64_b32 v[102:103], v31 offset0:152 offset1:153\n\t"
            "ds_read2st64_b32 v[100:101], v30 offset0:152 offset1:153\n\tds_read2st64_b32 v[78:79], v31 offset0:154 offset1:155\n\tds_read2st64_b32 v[76:77], v30 offset0:154 offset1:155\n\ts_waitcnt lgkmcnt(0)\n\tv_cmp_ge_u32 vcc, 1, v27\n\ts_cbranch_vccz 21b\n\t"
            ASLP
            "s_sub_u32 s68, s68, 1\n\ts_cbranch_scc0 11b\n\ts_branch 21b\n\t.LB:\ns_and_b32 s72, s56, 1\n\ts_lshr_b32 s73, s56, 1\n\t"
            "v_and_b32 v156, 2, v51\n\tv_lshlrev_b32 v156, 1, v156\n\tv_and_b32 v157, 7, v52\n\tv_xor_b32 v157, v156, v157\n\tv_mul_u32_u24 v156, 0x500, v51\n\tv_lshl_add_u32 v156, v157, 4, v156\n\t"
            "s_mul_i32 s74, s72, 0x2800\n\ts_mul_i32 s75, s73, 0xf000\n\ts_add_i32 s75, s75, s74\n\tv_add_u32 v154, s75, v156\n\ts_mul_i32 s75, s73, 0x5000\n\ts_add_i32 s75, s75, s74\n\t"
            "s_addk_i32 s75, 0x5000\n\tv_add_u32 v156, s75, v156\n\ts_cmp_lg_u32 s63, 0\n\ts_cselect_b32 s74, 81920, 0\n\ts_max_u32 s74, s74, 65536\n\tv_add_u32 v158, s62, v52\n\t"
            "v_min_u32 v159, 47, v52\n\ts_add_i32 s75, s62, 64\n\tv_add_u32 v159, s75, v159\n\tv_max_i32 v158, 0, v158\n\tv_min_i32 v159, 0x3ff, v159\n\tv_lshl_add_u32 v158, v158, 4, s74\n\t"
            "v_lshl_add_u32 v159, v159, 4, s74\n\tv_lshl_add_u32 v59, v53, 4, v57\n\ts_cmp_lg_u32 s73, 0\n\ts_cselect_b64 vcc, -1, 0\n\tv_cndmask_b32 v97, v59, v52, vcc\n\ts_lshl_b32 s75, s72, 6\n\t"
            "s_add_i32 s75, s75, s62\n\tv_add_u32 v97, s75, v97\n\tv_mov_b32 v60, s62\n\tv_add_u32 v60, 0x6f, v60\n\tv_min_i32 v60, 0x3ff, v60\n\tv_med3_i32 v97, v97, 0, v60\n\t"
            "s_lshl3_add_u32 s74, s73, s74\n\tv_lshl_add_u32 v97, v97, 4, s74\n\ts_mul_i32 s30, s73, 576\n\ts_lshl_b32 s75, s72, 8\n\ts_add_i32 s30, s30, s75\n\ts_add_i32 s30, s30, 160000\n\t"
            "s_lshl_b32 s74, s59, 7\n\ts_add_i32 s74, s74, 8192\n\tv_min_u32 v96, 9, v52\n\tv_lshl_add_u32 v96, v96, 2, s74\n\ts_lshr_b32 s75, s58, 5\n\ts_add_i32 s74, s74, s75\n\t"
            "s_cmp_eq_u32 s56, 3\n\ts_cselect_b32 s74, s74, 0x40000000\n\tv_lshl_add_u32 v98, v52, 24, s74\n\ts_cselect_b32 s75, 1, 0\n\ts_lshl2_add_u32 s74, s59, s56\n\ts_mul_i32 s74, s74, 0xa000\n\t"
            "s_add_i32 s74, s74, 155648\n\tv_lshl_add_u32 v101, v52, 4, s74\n\tv_and_b32 v60, s75, v53\n\tv_lshl_add_u32 v2, v60, 30, v101\n\ts_cmp_ge_u32 s56, s57\n\ts_addc_u32 s76, s56, 0\n\t"
            "s_and_b32 s16, s76, 3\n\ts_lshl_b32 s77, s76, 1\n\ts_max_u32 s78, s77, 1\n\ts_sub_u32 s78, s78, 1\n\ts_min_u32 s79, s77, 7\n\tv_lshlrev_b32 v60, 9, v53\n\t"
            "v_lshl_add_u32 v60, v54, 3, v60\n\ts_mul_i32 s80, s78, 0xc00\n\ts_mul_i32 s81, s79, 0xc00\n\ts_addk_i32 s80, 14336\n\ts_addk_i32 s81, 14336\n\tv_add_u32 v24, s80, v60\n\t"
            "v_add_u32 v26, s81, v60\n\tv_add_u32 v25, 38912, v24\n\tv_add_u32 v27, 38912, v26\n\tv_lshl_add_u32 v29, v52, 4, s80\n\ts_lshr_b32 s78, s78, 1\n\ts_lshr_b32 s79, s79, 1\n\t"
            "s_sub_u32 s79, s79, s78\n\ts_lshl2_add_u32 s78, s78, 159744\n\tv_and_b32 v28, s79, v52\n\tv_lshl_add_u32 v28, v28, 2, s78\n\ts_lshl2_add_u32 s78, s56, 159744\n\tv_mov_b32 v30, s78\n\t"
            "s_mov_b32 s78, 160576\n\tv_lshl_add_u32 v153, v59, 2, s78\n\ts_mul_i32 s79, s59, 163840\n\ts_lshl_b32 s80, s58, 5\n\ts_add_i32 s79, s79, s80\n\ts_cmp_eq_u32 s76, 0\n\t"
            "s_cselect_b32 s80, 0, 0xc00\n\ts_add_i32 s79, s79, s80\n\ts_add_i32 s79, s79, 155652\n\tv_lshl_add_u32 v31, v54, 4, s79\n\tv_lshrrev_b32 v60, 1, v53\n\tv_mul_u32_u24 v60, 0xa000, v60\n\t"
            "v_add_u32 v31, v60, v31\n\tv_and_b32 v60, 1, v53\n\tv_lshl_add_u32 v31, v60, 3, v31\n\ts_mov_b32 s31, 0x14000\n\ts_cmp_lt_i32 s62, 0\n\ts_cselect_b32 s79, 8, -16\n\t"
            "v_mov_b32 v61, 0xff800000\n\tv_mov_b32 v142, 1\n\tv_add_u32 v60, 0, v59\n\tv_and_b32 v60, -8, v60\n\tv_add_u32 v60, 0, v60\n\tv_cmp_gt_i32 vcc, s79, v60\n\t"
            "v_lshl_add_u32 v160, v60, 2, s78\n\ts_nop 0\n\tv_cndmask_b32 v128, 0, v61, vcc\n\tv_add_u32 v60, 4, v59\n\tv_and_b32 v60, -8, v60\n\tv_add_u32 v60, -4, v60\n\t"
            "v_cmp_gt_i32 vcc, s79, v60\n\tv_lshl_add_u32 v161, v60, 2, s78\n\ts_nop 0\n\tv_cndmask_b32 v129, 0, v61, vcc\n\tv_add_u32 v60, -1, v59\n\tv_and_b32 v60, -8, v60\n\t"
            "v_add_u32 v60, 1, v60\n\tv_cmp_gt_i32 vcc, s79, v60\n\tv_lshl_add_u32 v162, v60, 2, s78\n\ts_nop 0\n\tv_cndmask_b32 v130, 0, v61, vcc\n\tv_add_u32 v60, 3, v59\n\t"
            "v_and_b32 v60, -8, v60\n\tv_add_u32 v60, -3, v60\n\tv_cmp_gt_i32 vcc, s79, v60\n\tv_lshl_add_u32 v163, v60, 2, s78\n\ts_nop 0\n\tv_cndmask_b32 v131, 0, v61, vcc\n\t"
            "v_add_u32 v60, -2, v59\n\tv_and_b32 v60, -8, v60\n\tv_add_u32 v60, 2, v60\n\tv_cmp_gt_i32 vcc, s79, v60\n\tv_lshl_add_u32 v164, v60, 2, s78\n\ts_nop 0\n\t"
            "v_cndmask_b32 v132, 0, v61, vcc\n\tv_add_u32 v60, 2, v59\n\tv_and_b32 v60, -8, v60\n\tv_add_u32 v60, -2, v60\n\tv_cmp_gt_i32 vcc, s79, v60\n\tv_lshl_add_u32 v165, v60, 2, s78\n\t"
            "s_nop 0\n\tv_cndmask_b32 v133, 0, v61, vcc\n\tv_add_u32 v60, -3, v59\n\tv_and_b32 v60, -8, v60\n\tv_add_u32 v60, 3, v60\n\tv_cmp_gt_i32 vcc, s79, v60\n\t"
            "v_lshl_add_u32 v166, v60, 2, s78\n\ts_nop 0\n\tv_cndmask_b32 v134, 0, v61, vcc\n\tv_add_u32 v60, 1, v59\n\tv_and_b32 v60, -8, v60\n\tv_add_u32 v60, -1, v60\n\t"
            "v_cmp_gt_i32 vcc, s79, v60\n\tv_lshl_add_u32 v167, v60, 2, s78\n\ts_nop 0\n\tv_cndmask_b32 v135, 0, v61, vcc\n\ts_lshl2_add_u32 s80, s71, s56\n\ts_lshl_b32 s80, s80, 5\n\t"
            "v_add_u32 v100, s80, v52\n\tv_mul_u32_u24 v100, 0x500, v100\n\ts_add_i32 s80, s58, 3072\n\tv_add_u32 v100, s80, v100\n\tv_min_u32 v104, 51, v52\n\tv_lshlrev_b32 v104, 4, v104\n\t"
            "v_add_u32 v104, 0x1000, v104\n\ts_lshl_b32 s4, s64, 12\n\ts_add_u32 s5, s4, 0x1000\n\ts_cmpk_eq_u32 s5, 0x5000\n\ts_cselect_b32 s5, 0, s5\n\ts_add_u32 s6, s5, 0x1000\n\t"
            "s_cmpk_eq_u32 s6, 0x5000\n\ts_cselect_b32 s6, 0, s6\n\ts_add_u32 s7, s6, 0x1000\n\ts_cmpk_eq_u32 s7, 0x5000\n\ts_cselect_b32 s7, 0, s7\n\ts_add_u32 s8, s7, 0x1000\n\t"
            "s_cmpk_eq_u32 s8, 0x5000\n\ts_cselect_b32 s8, 0, s8\n\ts_cmpk_lt_u32 s58, 0x280\n\ts_cselect_b32 s80, 0, 0x5000\n\ts_xor_b32 s81, s80, 0x5000\n\ts_add_u32 s9, s4, s81\n\t"
            "s_add_u32 s4, s4, s80\n\ts_add_u32 s10, s5, s81\n\ts_add_u32 s5, s5, s80\n\ts_add_u32 s11, s6, s81\n\ts_add_u32 s6, s6, s80\n\ts_add_u32 s12, s7, s81\n\t"
            "s_add_u32 s7, s7, s80\n\ts_add_u32 s13, s8, s81\n\ts_add_u32 s8, s8, s80\n\ts_mul_i32 s20, s4, 20\n\ts_add_i32 s20, s20, s58\n\ts_addk_i32 s20, 3072\n\t"
            "s_mul_i32 s21, s5, 20\n\ts_add_i32 s21, s21, s58\n\ts_addk_i32 s21, 2048\n\ts_mul_i32 s22, s6, 20\n\ts_add_i32 s22, s22, s58\n\ts_addk_i32 s22, 1024\n\t"
            "s_mul_i32 s23, s7, 20\n\ts_add_i32 s23, s23, s58\n\ts_mul_i32 s24, s8, 20\n\ts_add_i32 s24, s24, s58\n\ts_addk_i32 s24, 3072\n\ts_mul_i32 s25, s9, 20\n\t"
            "s_add_i32 s25, s25, s58\n\ts_addk_i32 s25, 2048\n\ts_mul_i32 s26, s10, 20\n\ts_add_i32 s26, s26, s58\n\ts_addk_i32 s26, 1024\n\ts_mul_i32 s27, s11, 20\n\t"
            "s_add_i32 s27, s27, s58\n\ts_mul_i32 s28, s12, 20\n\ts_add_i32 s28, s28, s58\n\ts_addk_i32 s28, 3072\n\ts_mul_i32 s29, s13, 20\n\ts_add_i32 s29, s29, s58\n\t"
            "s_addk_i32 s29, 2048\n\ts_mul_i32 s72, s60, 1920\n\ts_sub_u32 s14, s61, s72\n\ts_mul_i32 s15, s56, 0x2800\n\ts_add_u32 s15, s15, 77824\n\tv_lshl_add_u32 v152, v52, 4, s15\n\t"
            "s_lshl_b32 s18, s58, 5\n\ts_mov_b32 s50, 822272\n\ts_mov_b32 s51, 0x20000\n\ts_waitcnt lgkmcnt(0)\n\ts_load_dword s67, s[44:45], 0x0\n\ts_sub_u32 s48, s48, 3072\n\t"
            "s_subb_u32 s49, s49, 0\n\ts_lshl_b32 s78, s56, 5\n\tv_add_u32 v12, s78, v57\n\tv_add_u32 v3, s62, v12\n\tv_and_b32 v13, 1, v53\n\tv_lshrrev_b32 v14, 1, v53\n\t"
            "v_lshl_add_u32 v15, v13, 1, v14\n\ts_add_u32 s79, s63, s58\n\tv_lshl_add_u32 v15, v15, 4, s79\n\ts_movk_i32 s79, 0xa00\n\tv_mad_i32_i24 v3, v3, s79, v15\n\tv_cmp_gt_u32 vcc, 8, v12\n\t"
            "s_cmp_eq_u32 s56, 3\n\ts_cselect_b32 s79, 2.0, 0xa000\n\tv_add_u32 v11, s79, v3\n\tv_cndmask_b32 v3, v3, 2.0, vcc\n\tv_mov_b32 v8, 159744\n\tv_and_b32 v9, 31, v52\n\t"
            "v_add_u32 v9, s78, v9\n\tv_lshl_add_u32 v9, v9, 2, v8\n\tv_lshl_add_u32 v10, v13, 4, v12\n\tv_lshl_add_u32 v10, v10, 2, v8\n\tv_mul_u32_u24 v4, 0x2800, v14\n\tv_lshl_add_u32 v4, v56, 7, v4\n\t"
            "v_lshl_add_u32 v4, v55, 3, v4\n\tv_lshl_add_u32 v4, v13, 5, v4\n\tv_bfe_u32 v5, v52, 3, 1\n\tv_lshl_add_u32 v4, v5, 6, v4\n\tv_add_u32 v4, 77824, v4\n\tv_xor_b32 v5, 64, v4\n\t"
            "v_add_u32 v6, 0x5000, v4\n\tv_add_u32 v7, 0x5000, v5\n\ts_add_u32 s78, s63, 0x500\n\ts_mov_b32 s32, s60\n\ts_mov_b32 s83, 0\n\ts_add_u32 s33, s32, 256\n\t"
            "s_cmp_eq_u32 s33, s78\n\ts_cselect_b32 s33, s63, s33\n\ts_add_u32 s34, s33, 256\n\ts_cmp_eq_u32 s34, s78\n\ts_cselect_b32 s34, s63, s34\n\ts_add_u32 s35, s34, 256\n\t"
            "s_cmp_eq_u32 s35, s78\n\ts_cselect_b32 s35, s63, s35\n\ts_add_u32 s17, s35, 256\n\ts_cmp_eq_u32 s17, s78\n\ts_cselect_b32 s17, s63, s17\n\ts_add_u32 s80, s32, 128\n\t"
            "s_bitcmp1_b32 31, s64\n\ts_cselect_b32 s82, s34, s33\n\ts_bitcmp1_b32 0, s64\n\ts_cselect_b32 s82, s80, s82\n\ts_bitcmp1_b32 30, s64\n\ts_cselect_b32 s81, s35, s34\n\t"
            "s_bitcmp1_b32 1, s64\n\ts_cselect_b32 s81, s80, s81\n\ts_bitcmp1_b32 28, s64\n\ts_cselect_b32 s79, s17, s35\n\ts_bitcmp1_b32 2, s64\n\ts_cselect_b32 s79, s80, s79\n\t"
            "s_bitcmp1_b32 28, s64\n\ts_cselect_b32 s78, s80, s17\n\ts_waitcnt vmcnt(0) lgkmcnt(0)\n\ts_barrier\n\tv_mov_b32 v136, -1\n\tv_mov_b32 v137, -1\n\t"
            "v_mov_b32 v138, -1\n\tv_mov_b32 v139, -1\n\ts_mov_b32 s60, s82\n\ts_mul_i32 s61, s60, 1920\n\ts_add_u32 m0, s66, 0\n\ts_add_u32 s61, s61, s14\n\t"
            "buffer_load_dwordx4 v40, s[40:43], s61 offen lds\n\tbuffer_load_dwordx4 v41, s[40:43], s61 offen offset:1024 lds\n\tbuffer_load_dwordx4 v42, s[40:43], s61 offen offset:2048 lds\n\tbuffer_load_dwordx4 v43, s[40:43], s61 offen offset:3072 lds\n\ts_addk_i32 m0, 0x1000\n\ts_nop 0\n\t"
            "buffer_load_dwordx4 v44, s[40:43], s61 offen lds\n\tbuffer_load_dwordx4 v45, s[40:43], s61 offen offset:1024 lds\n\ts_add_u32 m0, s65, 0\n\ts_mov_b32 exec_hi, 0\n\tbuffer_load_dwordx4 v39, s[36:39], s60 offen offset:3072 lds\n\ts_mov_b32 exec_hi, -1\n\t"
            "buffer_load_dwordx4 v36, s[36:39], s60 offen lds\n\tbuffer_load_dwordx4 v37, s[36:39], s60 offen offset:1024 lds\n\tbuffer_load_dwordx4 v38, s[36:39], s60 offen offset:2048 lds\n\ts_mul_i32 s67, s67, 0x19660d\n\ts_add_u32 s67, s67, 0x3c6ef35f\n\ts_barrier\n\t"
            "s_mov_b32 s60, s81\n\ts_mul_i32 s61, s60, 1920\n\ts_add_u32 m0, s66, 38912\n\ts_add_u32 s61, s61, s14\n\tbuffer_load_dwordx4 v40, s[40:43], s61 offen lds\n\tbuffer_load_dwordx4 v41, s[40:43], s61 offen offset:1024 lds\n\t"
            "buffer_load_dwordx4 v42, s[40:43], s61 offen offset:2048 lds\n\tbuffer_load_dwordx4 v43, s[40:43], s61 offen offset:3072 lds\n\ts_addk_i32 m0, 0x1000\n\ts_nop 0\n\tbuffer_load_dwordx4 v44, s[40:43], s61 offen lds\n\tbuffer_load_dwordx4 v45, s[40:43], s61 offen offset:1024 lds\n\t"
            "s_add_u32 m0, s65, 38912\n\ts_mov_b32 exec_hi, 0\n\tbuffer_load_dwordx4 v39, s[36:39], s60 offen offset:3072 lds\n\ts_mov_b32 exec_hi, -1\n\tbuffer_load_dwordx4 v36, s[36:39], s60 offen lds\n\tbuffer_load_dwordx4 v37, s[36:39], s60 offen offset:1024 lds\n\t"
            "buffer_load_dwordx4 v38, s[36:39], s60 offen offset:2048 lds\n\ts_bitcmp1_b32 13, s64\n\ts_cbranch_scc0 1f\n\tbuffer_store_dwordx4 v[136:139], v101, s[44:47], s18 offen offset:0 sc1\n\tbuffer_store_dwordx4 v[136:139], v2, s[44:47], s18 offen offset:1024 sc1\n\tbuffer_store_dwordx4 v[136:139], v2, s[44:47], s18 offen offset:2048 sc1\n\t"
            "buffer_store_dwordx4 v[136:139], v101, s[44:47], s18 offen offset:3072 sc1\n\t1:\ns_bitcmp1_b32 13, s64\n\ts_cbranch_scc1 1f\n\ts_waitcnt vmcnt(10)\n\t1:\n"
            "s_waitcnt vmcnt(14)\n\ts_barrier\n\ts_mov_b32 s60, s79\n\ts_mul_i32 s61, s60, 1920\n\ts_add_u32 m0, s66, 77824\n\ts_add_u32 s61, s61, s14\n\t"
            "buffer_load_dwordx4 v40, s[40:43], s61 offen lds\n\tbuffer_load_dwordx4 v41, s[40:43], s61 offen offset:1024 lds\n\tbuffer_load_dwordx4 v42, s[40:43], s61 offen offset:2048 lds\n\tbuffer_load_dwordx4 v43, s[40:43], s61 offen offset:3072 lds\n\ts_addk_i32 m0, 0x1000\n\ts_nop 0\n\t"
            "buffer_load_dwordx4 v44, s[40:43], s61 offen lds\n\tbuffer_load_dwordx4 v45, s[40:43], s61 offen offset:1024 lds\n\ts_add_u32 m0, s65, 77824\n\ts_mov_b32 exec_hi, 0\n\tbuffer_load_dwordx4 v39, s[36:39], s60 offen offset:3072 lds\n\ts_mov_b32 exec_hi, -1\n\t"
            "buffer_load_dwordx4 v36, s[36:39], s60 offen lds\n\tbuffer_load_dwordx4 v37, s[36:39], s60 offen offset:1024 lds\n\tbuffer_load_dwordx4 v38, s[36:39], s60 offen offset:2048 lds\n\ts_bitcmp1_b32 18, s64\n\ts_cbranch_scc0 1f\n\tbuffer_store_dwordx4 v[136:139], v101, s[44:47], s18 offen offset:0 sc1\n\t"
            "buffer_store_dwordx4 v[136:139], v2, s[44:47], s18 offen offset:1024 sc1\n\tbuffer_store_dwordx4 v[136:139], v2, s[44:47], s18 offen offset:2048 sc1\n\tbuffer_store_dwordx4 v[136:139], v101, s[44:47], s18 offen offset:3072 sc1\n\t1:\ns_bitcmp1_b32 31, s64\n\ts_cbranch_scc1 1f\n\t"
            "s_waitcnt vmcnt(10)\n\t1:\ns_waitcnt vmcnt(14)\n\ts_barrier\n\ts_mov_b32 s60, s78\n\ts_mul_i32 s61, s60, 1920\n\t"
            "s_add_u32 m0, s66, 0\n\ts_add_u32 s61, s61, s14\n\tbuffer_load_dwordx4 v40, s[40:43], s61 offen lds\n\tbuffer_load_dwordx4 v41, s[40:43], s61 offen offset:1024 lds\n\tbuffer_load_dwordx4 v42, s[40:43], s61 offen offset:2048 lds\n\tbuffer_load_dwordx4 v43, s[40:43], s61 offen offset:3072 lds\n\t"
            "s_addk_i32 m0, 0x1000\n\ts_nop 0\n\tbuffer_load_dwordx4 v44, s[40:43], s61 offen lds\n\tbuffer_load_dwordx4 v45, s[40:43], s61 offen offset:1024 lds\n\ts_add_u32 m0, s65, 0\n\ts_mov_b32 exec_hi, 0\n\t"
            "buffer_load_dwordx4 v39, s[36:39], s60 offen offset:3072 lds\n\ts_mov_b32 exec_hi, -1\n\tbuffer_load_dwordx4 v36, s[36:39], s60 offen lds\n\tbuffer_load_dwordx4 v37, s[36:39], s60 offen offset:1024 lds\n\tbuffer_load_dwordx4 v38, s[36:39], s60 offen offset:2048 lds\n\ts_waitcnt vmcnt(10)\n\t"
            "s_barrier\n\ts_add_u32 s60, s33, 128\n\ts_mul_i32 s61, s60, 1920\n\ts_add_u32 m0, s66, 38912\n\ts_add_u32 s61, s61, s14\n\tbuffer_load_dwordx4 v40, s[40:43], s61 offen lds\n\t"
            "buffer_load_dwordx4 v41, s[40:43], s61 offen offset:1024 lds\n\tbuffer_load_dwordx4 v42, s[40:43], s61 offen offset:2048 lds\n\tbuffer_load_dwordx4 v43, s[40:43], s61 offen offset:3072 lds\n\ts_addk_i32 m0, 0x1000\n\ts_nop 0\n\tbuffer_load_dwordx4 v44, s[40:43], s61 offen lds\n\t"
            "buffer_load_dwordx4 v45, s[40:43], s61 offen offset:1024 lds\n\ts_add_u32 m0, s65, 38912\n\ts_mov_b32 exec_hi, 0\n\tbuffer_load_dwordx4 v39, s[36:39], s60 offen offset:3072 lds\n\ts_mov_b32 exec_hi, -1\n\tbuffer_load_dwordx4 v36, s[36:39], s60 offen lds\n\t"
            "buffer_load_dwordx4 v37, s[36:39], s60 offen offset:1024 lds\n\tbuffer_load_dwordx4 v38, s[36:39], s60 offen offset:2048 lds\n\tv_mov_b32 v140, s67\n\tbuffer_store_dword v140, v98, s[44:47], 0 offen sc1\n\ts_waitcnt vmcnt(11)\n\ts_barrier\n\t"
            "s_add_u32 s60, s34, 128\n\ts_mul_i32 s61, s60, 1920\n\ts_add_u32 m0, s66, 77824\n\ts_add_u32 s61, s61, s14\n\tbuffer_load_dwordx4 v40, s[40:43], s61 offen lds\n\tbuffer_load_dwordx4 v41, s[40:43], s61 offen offset:1024 lds\n\t"
            "buffer_load_dwordx4 v42, s[40:43], s61 offen offset:2048 lds\n\tbuffer_load_dwordx4 v43, s[40:43], s61 offen offset:3072 lds\n\ts_addk_i32 m0, 0x1000\n\ts_nop 0\n\tbuffer_load_dwordx4 v44, s[40:43], s61 offen lds\n\tbuffer_load_dwordx4 v45, s[40:43], s61 offen offset:1024 lds\n\t"
            "s_add_u32 m0, s65, 77824\n\ts_mov_b32 exec_hi, 0\n\tbuffer_load_dwordx4 v39, s[36:39], s60 offen offset:3072 lds\n\ts_mov_b32 exec_hi, -1\n\tbuffer_load_dwordx4 v36, s[36:39], s60 offen lds\n\tbuffer_load_dwordx4 v37, s[36:39], s60 offen offset:1024 lds\n\t"
            "buffer_load_dwordx4 v38, s[36:39], s60 offen offset:2048 lds\n\ts_waitcnt vmcnt(11)\n\ts_barrier\n\ts_cmp_lg_u32 s56, 3\n\ts_cbranch_scc1 1f\n\tbuffer_load_dwordx4 v[106:109], v158, s[44:47], 0 offen sc1\n\t"
            "buffer_load_dwordx4 v[110:113], v159, s[44:47], 0 offen sc1\n\tbuffer_load_dwordx4 v[114:117], v104, s[44:47], 0 offen sc1\n\tbuffer_load_dword v118, v96, s[44:47], 0 offen sc1\n\t1:\ns_add_u32 s60, s35, 128\n\ts_mul_i32 s61, s60, 1920\n\t"
            "s_add_u32 m0, s66, 0\n\ts_add_u32 s61, s61, s14\n\tbuffer_load_dwordx4 v40, s[40:43], s61 offen lds\n\tbuffer_load_dwordx4 v41, s[40:43], s61 offen offset:1024 lds\n\tbuffer_load_dwordx4 v42, s[40:43], s61 offen offset:2048 lds\n\tbuffer_load_dwordx4 v43, s[40:43], s61 offen offset:3072 lds\n\t"
            "s_addk_i32 m0, 0x1000\n\ts_nop 0\n\tbuffer_load_dwordx4 v44, s[40:43], s61 offen lds\n\tbuffer_load_dwordx4 v45, s[40:43], s61 offen offset:1024 lds\n\ts_add_u32 m0, s65, 0\n\ts_mov_b32 exec_hi, 0\n\t"
            "buffer_load_dwordx4 v39, s[36:39], s60 offen offset:3072 lds\n\ts_mov_b32 exec_hi, -1\n\tbuffer_load_dwordx4 v36, s[36:39], s60 offen lds\n\tbuffer_load_dwordx4 v37, s[36:39], s60 offen offset:1024 lds\n\tbuffer_load_dwordx4 v38, s[36:39], s60 offen offset:2048 lds\n\ts_waitcnt vmcnt(10)\n\t"
            "s_cmp_lg_u32 s56, 3\n\ts_cbranch_scc1 2f\n\ts_mov_b32 s68, 0x200000\n\tv_xor_b32 v106, s67, v107\n\tv_xor_b32 v108, s67, v109\n\tv_xor_b32 v110, s67, v111\n\t"
            "v_xor_b32 v112, s67, v113\n\tv_xor_b32 v114, s67, v115\n\tv_xor_b32 v116, s67, v117\n\tv_xor_b32 v118, s67, v118\n\tv_or3_b32 v106, v106, v108, v110\n\tv_or3_b32 v118, v118, v114, v116\n\t"
            "v_or3_b32 v106, v106, v112, v118\n\tv_cmp_ne_u32 vcc, 0, v106\n\ts_cbranch_vccnz 30f\n\t31:\n2:\ns_waitcnt vmcnt(10)\n\t"
            "s_barrier\n\ts_add_u32 s60, s17, 128\n\ts_mul_i32 s61, s60, 1920\n\ts_add_u32 m0, s66, 38912\n\ts_add_u32 s61, s61, s14\n\tbuffer_load_dwordx4 v40, s[40:43], s61 offen lds\n\t"
            "buffer_load_dwordx4 v41, s[40:43], s61 offen offset:1024 lds\n\tbuffer_load_dwordx4 v42, s[40:43], s61 offen offset:2048 lds\n\tbuffer_load_dwordx4 v43, s[40:43], s61 offen offset:3072 lds\n\ts_addk_i32 m0, 0x1000\n\ts_nop 0\n\tbuffer_load_dwordx4 v44, s[40:43], s61 offen lds\n\t"
            "buffer_load_dwordx4 v45, s[40:43], s61 offen offset:1024 lds\n\ts_add_u32 m0, s65, 38912\n\ts_mov_b32 exec_hi, 0\n\tbuffer_load_dwordx4 v39, s[36:39], s60 offen offset:3072 lds\n\ts_mov_b32 exec_hi, -1\n\tbuffer_load_dwordx4 v36, s[36:39], s60 offen lds\n\t"
            "buffer_load_dwordx4 v37, s[36:39], s60 offen offset:1024 lds\n\tbuffer_load_dwordx4 v38, s[36:39], s60 offen offset:2048 lds\n\ts_mov_b32 m0, s30\n\ts_nop 0\n\tbuffer_load_dword v97, s[44:47], 0 offen sc1 lds\n\ts_waitcnt vmcnt(11)\n\t"
            "s_barrier\n\ts_add_u32 m0, s15, 40960\n\ts_nop 0\n\tbuffer_load_dwordx4 v156, s[48:51], s20 offen lds\n\tbuffer_load_dwordx4 v156, s[48:51], s21 offen offset:1024 lds\n\tbuffer_load_dwordx4 v156, s[48:51], s22 offen offset:2048 lds\n\t"
            "buffer_load_dwordx4 v156, s[48:51], s23 offen offset:3072 lds\n\ts_add_u32 m0, s15, 45056\n\ts_nop 0\n\tbuffer_load_dwordx4 v156, s[48:51], s24 offen lds\n\tbuffer_load_dwordx4 v156, s[48:51], s25 offen offset:1024 lds\n\tbuffer_load_dwordx4 v156, s[48:51], s26 offen offset:2048 lds\n\t"
            "s_waitcnt vmcnt(7)\n\ts_barrier\n\ts_add_u32 m0, s15, 45056\n\ts_nop 0\n\tbuffer_load_dwordx4 v156, s[48:51], s27 offen offset:3072 lds\n\ts_add_u32 m0, s15, 49152\n\t"
            "s_nop 0\n\tbuffer_load_dwordx4 v156, s[48:51], s28 offen lds\n\tbuffer_load_dwordx4 v156, s[48:51], s29 offen offset:1024 lds\n\ts_add_u32 m0, s15, 0\n\ts_nop 0\n\tbuffer_load_dwordx4 v154, s[48:51], s20 offen lds\n\t"
            "buffer_load_dwordx4 v154, s[48:51], s21 offen offset:1024 lds\n\tbuffer_load_dwordx4 v154, s[48:51], s22 offen offset:2048 lds\n\tbuffer_load_dwordx4 v154, s[48:51], s23 offen offset:3072 lds\n\ts_add_u32 m0, s15, 4096\n\ts_nop 0\n\tbuffer_load_dwordx4 v154, s[48:51], s24 offen lds\n\t"
            "buffer_load_dwordx4 v154, s[48:51], s25 offen offset:1024 lds\n\tbuffer_load_dwordx4 v154, s[48:51], s26 offen offset:2048 lds\n\tbuffer_load_dwordx4 v154, s[48:51], s27 offen offset:3072 lds\n\ts_add_u32 m0, s15, 8192\n\ts_nop 0\n\tbuffer_load_dwordx4 v154, s[48:51], s28 offen lds\n\t"
            "buffer_load_dwordx4 v154, s[48:51], s29 offen offset:1024 lds\n\ts_cmp_lg_u32 s56, 0\n\ts_cbranch_scc1 1f\n\ts_mov_b32 m0, 162624\n\ts_nop 0\n\tbuffer_load_dwordx4 v104, s[44:47], 0 offen sc1 lds\n\t"
            "1:\nbuffer_load_dwordx4 v[240:243], v3, s[36:39], 0 offen offset:0\n\tbuffer_load_dwordx4 v[244:247], v11, s[36:39], 0 offen offset:0\n\tbuffer_load_dwordx4 v[248:251], v3, s[36:39], 0 offen offset:64\n\tbuffer_load_dwordx4 v[252:255], v11, s[36:39], 0 offen offset:64\n\tds_read_b32 v20, v9 offset:256\n\t"
            "ds_read_b32 v21, v10 offset:832\n\tds_read2st64_b32 v[138:139], v153 offset1:1\n\tds_read2st64_b32 v[176:177], v160 offset1:1\n\tds_read2st64_b32 v[178:179], v161 offset1:1\n\tds_read2st64_b32 v[180:181], v162 offset1:1\n\tds_read2st64_b32 v[182:183], v163 offset1:1\n\t"
            "ds_read2st64_b32 v[184:185], v164 offset1:1\n\tds_read2st64_b32 v[186:187], v165 offset1:1\n\tds_read2st64_b32 v[188:189], v166 offset1:1\n\tds_read2st64_b32 v[190:191], v167 offset1:1\n\ts_mov_b32 s68, 0x200000\n\ts_waitcnt lgkmcnt(8)\n\t"
            "v_mul_f32 v138, 0x3ea52b5c, v138\n\tv_mul_f32 v139, 0x3ea52b5c, v139\n\ts_waitcnt lgkmcnt(7)\n\tv_mul_f32 v192, v176, v138\n\tv_mul_f32 v200, v177, v139\n\ts_waitcnt lgkmcnt(6)\n\t"
            "v_mul_f32 v193, v178, v138\n\tv_mul_f32 v201, v179, v139\n\ts_waitcnt lgkmcnt(5)\n\tv_mul_f32 v194, v180, v138\n\tv_mul_f32 v202, v181, v139\n\ts_waitcnt lgkmcnt(4)\n\t"
            "v_mul_f32 v195, v182, v138\n\tv_mul_f32 v203, v183, v139\n\ts_waitcnt lgkmcnt(3)\n\tv_mul_f32 v196, v184, v138\n\tv_mul_f32 v204, v185, v139\n\ts_waitcnt lgkmcnt(2)\n\t"
            "v_mul_f32 v197, v186, v138\n\tv_mul_f32 v205, v187, v139\n\ts_waitcnt lgkmcnt(1)\n\tv_mul_f32 v198, v188, v138\n\tv_mul_f32 v206, v189, v139\n\ts_waitcnt lgkmcnt(0)\n\t"
            "v_mul_f32 v199, v190, v138\n\tv_mul_f32 v207, v191, v139\n\ts_barrier\n\t"
            SB_(1) PRIS
            "50:\nds_read2_b64 v[208:211], v24 offset0:0 offset1:16\n\tds_read2_b64 v[212:215], v24 offset0:32 offset1:48\n\tds_read2_b64 v[216:219], v26 offset0:0 offset1:16\n\tds_read2_b64 v[220:223], v26 offset0:32 offset1:48\n\ts_cmp_eq_u32 s16, 0\n\t"
            "s_cbranch_scc1 56f\n\t58:\ns_cbranch_scc0 1f\n\tbuffer_load_dword v232, v31, s[44:47], s31 offen offset:0 sc1\n\tbuffer_load_dword v233, v31, s[44:47], s31 offen offset:256 sc1\n\tbuffer_load_dword v234, v31, s[44:47], s31 offen offset:512 sc1\n\t"
            "buffer_load_dword v235, v31, s[44:47], s31 offen offset:768 sc1\n\t1:\ns_waitcnt lgkmcnt(1)\n\tv_add_f32 v208, v208, v216\n\tv_add_f32 v209, v209, v217\n\tv_add_f32 v210, v210, v218\n\t"
            "v_add_f32 v211, v211, v219\n\ts_waitcnt lgkmcnt(0)\n\tv_add_f32 v212, v212, v220\n\tv_add_f32 v213, v213, v221\n\tv_add_f32 v214, v214, v222\n\tv_add_f32 v215, v215, v223\n\t"
            "v_fma_f32 v208, v208, v192, v128\n\tv_fma_f32 v209, v209, v193, v129\n\tv_fma_f32 v210, v210, v194, v130\n\tv_fma_f32 v211, v211, v195, v131\n\tv_fma_f32 v212, v212, v196, v132\n\tv_fma_f32 v213, v213, v197, v133\n\t"
            "v_fma_f32 v214, v214, v198, v134\n\tv_fma_f32 v215, v215, v199, v135\n\tv_max3_f32 v136, v208, v209, v210\n\tv_max3_f32 v137, v211, v212, v213\n\tv_max3_f32 v136, v136, v214, v215\n\tv_max_f32 v136, v136, v137\n\t"
            "v_sub_f32 v208, v208, v136\n\tv_sub_f32 v209, v209, v136\n\tv_sub_f32 v210, v210, v136\n\tv_sub_f32 v211, v211, v136\n\tv_sub_f32 v212, v212, v136\n\tv_sub_f32 v213, v213, v136\n\t"
            "v_sub_f32 v214, v214, v136\n\tv_sub_f32 v215, v215, v136\n\tv_exp_f32 v208, v208\n\tv_exp_f32 v209, v209\n\tv_exp_f32 v210, v210\n\tv_exp_f32 v211, v211\n\t"
            "v_exp_f32 v212, v212\n\tv_exp_f32 v213, v213\n\tv_exp_f32 v214, v214\n\tv_exp_f32 v215, v215\n\tv_add_f32 v216, v208, v209\n\tv_add_f32 v217, v210, v211\n\t"
            "v_add_f32 v218, v212, v213\n\tv_add_f32 v219, v214, v215\n\tv_add_f32 v216, v216, v217\n\tv_add_f32 v218, v218, v219\n\tv_mul_f32 v208, v208, v176\n\tv_add_f32 v216, v216, v218\n\t"
            "v_mul_f32 v216, 0xbfb8aa3b, v216\n\tv_rcp_f32 v216, v216\n\tv_mul_f32 v209, v209, v178\n\tv_mul_f32 v210, v210, v180\n\tv_mul_f32 v211, v211, v182\n\tv_mul_f32 v212, v212, v184\n\t"
            "v_mul_f32 v213, v213, v186\n\tv_mul_f32 v214, v214, v188\n\tv_mul_f32 v215, v215, v190\n\tv_mul_f32 v208, v208, v216\n\tv_mul_f32 v209, v209, v216\n\tv_mul_f32 v210, v210, v216\n\t"
            "v_mul_f32 v211, v211, v216\n\tv_mul_f32 v212, v212, v216\n\tv_mul_f32 v213, v213, v216\n\tv_mul_f32 v214, v214, v216\n\tv_mul_f32 v215, v215, v216\n\tv_cvt_pk_bf16_f32 v220, v208, v209\n\t"
            "v_cvt_pk_bf16_f32 v221, v210, v211\n\tv_cvt_pk_bf16_f32 v222, v212, v213\n\tv_cvt_pk_bf16_f32 v223, v214, v215\n\tds_write_b128 v29, v[220:223] offset:0\n\tds_write_b32 v30, v142 offset:16\n\t"
            SP0 PRIW
            "51:\nds_read_b32 v136, v28\n\tds_read2_b64 v[208:211], v25 offset0:0 offset1:16\n\tds_read2_b64 v[212:215], v25 offset0:32 offset1:48\n\tds_read2_b64 v[216:219], v27 offset0:0 offset1:16\n\tds_read2_b64 v[220:223], v27 offset0:32 offset1:48\n\t"
            "s_waitcnt lgkmcnt(4)\n\tv_cmp_eq_u32 vcc, 0, v136\n\ts_cbranch_vccnz 53f\n\t55:\n"
            PRI1
            "s_cmp_eq_u32 s16, 0\n\ts_cbranch_scc1 57f\n\t59:\ns_waitcnt lgkmcnt(1)\n\tv_add_f32 v208, v208, v216\n\tv_add_f32 v209, v209, v217\n\t"
            "v_add_f32 v210, v210, v218\n\tv_add_f32 v211, v211, v219\n\ts_waitcnt lgkmcnt(0)\n\tv_add_f32 v212, v212, v220\n\tv_add_f32 v213, v213, v221\n\tv_add_f32 v214, v214, v222\n\t"
            "v_add_f32 v215, v215, v223\n\tv_mul_f32 v208, v208, v200\n\tv_mul_f32 v209, v209, v201\n\tv_mul_f32 v210, v210, v202\n\tv_mul_f32 v211, v211, v203\n\tv_mul_f32 v212, v212, v204\n\t"
            "v_mul_f32 v213, v213, v205\n\tv_mul_f32 v214, v214, v206\n\tv_mul_f32 v215, v215, v207\n\tv_max3_f32 v136, v208, v209, v210\n\tv_max3_f32 v137, v211, v212, v213\n\tv_max3_f32 v136, v136, v214, v215\n\t"
            "v_max_f32 v136, v136, v137\n\tv_sub_f32 v208, v208, v136\n\tv_sub_f32 v209, v209, v136\n\tv_sub_f32 v210, v210, v136\n\tv_sub_f32 v211, v211, v136\n\tv_sub_f32 v212, v212, v136\n\t"
            "v_sub_f32 v213, v213, v136\n\tv_sub_f32 v214, v214, v136\n\tv_sub_f32 v215, v215, v136\n\tv_exp_f32 v208, v208\n\tv_exp_f32 v209, v209\n\tv_exp_f32 v210, v210\n\t"
            "v_exp_f32 v211, v211\n\tv_exp_f32 v212, v212\n\tv_exp_f32 v213, v213\n\tv_exp_f32 v214, v214\n\tv_exp_f32 v215, v215\n\tv_add_f32 v216, v208, v209\n\t"
            "v_add_f32 v217, v210, v211\n\tv_add_f32 v218, v212, v213\n\tv_add_f32 v219, v214, v215\n\tv_add_f32 v216, v216, v217\n\tv_add_f32 v218, v218, v219\n\tv_mul_f32 v208, v208, v177\n\t"
            "v_add_f32 v216, v216, v218\n\tv_mul_f32 v216, 0xbfb8aa3b, v216\n\tv_rcp_f32 v216, v216\n\tv_mul_f32 v209, v209, v179\n\tv_mul_f32 v210, v210, v181\n\tv_mul_f32 v211, v211, v183\n\t"
            "v_mul_f32 v212, v212, v185\n\tv_mul_f32 v213, v213, v187\n\tv_mul_f32 v214, v214, v189\n\tv_mul_f32 v215, v215, v191\n\tv_mul_f32 v208, v208, v216\n\tv_mul_f32 v209, v209, v216\n\t"
            "v_mul_f32 v210, v210, v216\n\tv_mul_f32 v211, v211, v216\n\tv_mul_f32 v212, v212, v216\n\tv_mul_f32 v213, v213, v216\n\tv_mul_f32 v214, v214, v216\n\tv_mul_f32 v215, v215, v216\n\t"
            "v_cvt_pk_bf16_f32 v220, v208, v209\n\tv_cvt_pk_bf16_f32 v221, v210, v211\n\tv_cvt_pk_bf16_f32 v222, v212, v213\n\tv_cvt_pk_bf16_f32 v223, v214, v215\n\tds_write_b128 v29, v[220:223] offset:38912\n\tds_write_b32 v30, v30 offset:16\n\t"
            SB_(2) PRIG
            "s_waitcnt vmcnt(0)\n\tds_write_b32 v30, v30 offset:48\n\ts_branch .LOL\n\t30:\nbuffer_load_dwordx4 v[106:109], v158, s[44:47], 0 offen sc1\n\tbuffer_load_dwordx4 v[110:113], v159, s[44:47], 0 offen sc1\n\t"
            "buffer_load_dwordx4 v[114:117], v104, s[44:47], 0 offen sc1\n\tbuffer_load_dword v118, v96, s[44:47], 0 offen sc1\n\t"
            GINC
            "s_waitcnt vmcnt(3)\n\tv_xor_b32 v106, s67, v107\n\tv_xor_b32 v108, s67, v109\n\ts_waitcnt vmcnt(2)\n\tv_xor_b32 v110, s67, v111\n\tv_xor_b32 v112, s67, v113\n\t"
            "s_waitcnt vmcnt(1)\n\tv_xor_b32 v114, s67, v115\n\tv_xor_b32 v116, s67, v117\n\ts_waitcnt vmcnt(0)\n\tv_xor_b32 v118, s67, v118\n\tv_or3_b32 v106, v106, v108, v110\n\t"
            "v_or3_b32 v118, v118, v114, v116\n\tv_or3_b32 v106, v106, v112, v118\n\tv_cmp_ne_u32 vcc, 0, v106\n\ts_cbranch_vccz 31b\n\ts_sub_u32 s68, s68, 1\n\ts_cbranch_scc0 30b\n\t"
            "s_branch 31b\n\t53:\n"
            PSLP
            "s_sub_u32 s68, s68, 1\n\ts_cbranch_scc0 51b\n\ts_branch 55b\n\t56:\n"
            RSL0
            "buffer_load_dword v224, v31, s[44:47], 0 offen offset:0 sc1\n\tbuffer_load_dword v225, v31, s[44:47], 0 offen offset:256 sc1\n\tbuffer_load_dword v226, v31, s[44:47], 0 offen offset:512 sc1\n\tbuffer_load_dword v227, v31, s[44:47], 0 offen offset:768 sc1\n\ts_waitcnt vmcnt(4)\n\t"
            PRIR
            "s_call_b64 s[76:77], .LG0\n\t"
            PRIX
            "s_branch 2f\n\t1:\ns_waitcnt vmcnt(3)\n\tbuffer_load_dword v224, v31, s[44:47], 0 offen offset:0 sc1\n\ts_waitcnt vmcnt(3)\n\tbuffer_load_dword v225, v31, s[44:47], 0 offen offset:256 sc1\n\t"
            "s_waitcnt vmcnt(3)\n\tbuffer_load_dword v226, v31, s[44:47], 0 offen offset:512 sc1\n\ts_waitcnt vmcnt(3)\n\tbuffer_load_dword v227, v31, s[44:47], 0 offen offset:768 sc1\n\ts_cmp_lg_u32 s83, 0\n\ts_cbranch_scc1 3f\n\t"
            "s_call_b64 s[76:77], .LG1\n\t3:\ns_cmp_eq_u32 s83, 2\n\ts_cbranch_scc1 2f\n\ts_call_b64 s[76:77], .LG2\n\t2:\n"
            "s_waitcnt vmcnt(0) lgkmcnt(0)\n\t4:\nds_write_b32 v30, v30 offset:48\n\tv_max3_u32 v136, v224, v225, v226\n\tv_max_u32 v136, v136, v227\n\tv_cmp_eq_u32 vcc, -1, v136\n\t"
            "v_lshlrev_b32 v216, 16, v224\n\tv_and_b32 v217, 0xffff0000, v224\n\tv_lshlrev_b32 v218, 16, v225\n\tv_and_b32 v219, 0xffff0000, v225\n\tv_lshlrev_b32 v220, 16, v226\n\tv_and_b32 v221, 0xffff0000, v226\n\t"
            "v_lshlrev_b32 v222, 16, v227\n\tv_and_b32 v223, 0xffff0000, v227\n\t"
            LINC
            "s_cmp_eq_u32 0, 0\n\ts_cbranch_vccz 58b\n\ts_sub_u32 s68, s68, 1\n\ts_cbranch_scc0 1b\n\ts_branch 58b\n\t57:\n"
            "s_branch 2f\n\t1:\nbuffer_load_dword v232, v31, s[44:47], s31 offen offset:0 sc1\n\tbuffer_load_dword v233, v31, s[44:47], s31 offen offset:256 sc1\n\tbuffer_load_dword v234, v31, s[44:47], s31 offen offset:512 sc1\n\tbuffer_load_dword v235, v31, s[44:47], s31 offen offset:768 sc1\n\t"
            "s_cmp_lg_u32 s83, 0\n\ts_cbranch_scc1 3f\n\ts_call_b64 s[76:77], .LG1\n\t3:\ns_cmp_eq_u32 s83, 2\n\ts_cbranch_scc1 2f\n\t"
            "s_call_b64 s[76:77], .LG2\n\t2:\ns_waitcnt vmcnt(0) lgkmcnt(0)\n\t4:\nv_max3_u32 v136, v232, v233, v234\n\tv_max_u32 v136, v136, v235\n\t"
            "v_cmp_eq_u32 vcc, -1, v136\n\tv_lshlrev_b32 v216, 16, v232\n\tv_and_b32 v217, 0xffff0000, v232\n\tv_lshlrev_b32 v218, 16, v233\n\tv_and_b32 v219, 0xffff0000, v233\n\tv_lshlrev_b32 v220, 16, v234\n\t"
            "v_and_b32 v221, 0xffff0000, v234\n\tv_lshlrev_b32 v222, 16, v235\n\tv_and_b32 v223, 0xffff0000, v235\n\t"
            LINC
            "s_cmp_eq_u32 0, 0\n\ts_cbranch_vccz 59b\n\ts_sub_u32 s68, s68, 1\n\ts_cbranch_scc0 1b\n\ts_branch 59b\n\t.LOL:\n"
            "s_lshr_b32 s71, s56, 1\n\ts_mov_b32 s68, 0x200000\n\ts_cmp_eq_u32 s16, 0\n\ts_cbranch_scc1 3f\n\ts_call_b64 s[76:77], .LG0\n\t3:\n"
            "ds_read_b64 v[14:15], v8 offset:32\n\ts_waitcnt lgkmcnt(0)\n\tv_min_u32 v12, v14, v15\n\tv_cmp_lt_u32 vcc, s71, v12\n\ts_cbranch_vccnz 4f\n\ts_sleep 1\n\t"
            "s_sub_u32 s68, s68, 1\n\ts_cbranch_scc0 3b\n\t4:\ns_cmp_eq_u32 s16, 0\n\ts_cbranch_scc1 1f\n\ts_call_b64 s[76:77], .LG1\n\t"
            "1:\ns_cmp_lt_u32 s56, 2\n\ts_cbranch_scc1 1f\n\ts_sleep 3\n\t1:\n"
            SO(4) RZ
            "buffer_load_dwordx4 v[176:179], v2, s[44:47], s4 offen offset:1024 sc1\n\tbuffer_load_dwordx4 v[180:183], v2, s[44:47], s4 offen offset:2048 sc1\n\tbuffer_load_dwordx4 v[184:187], v2, s[44:47], s5 offen offset:1024 sc1\n\tbuffer_load_dwordx4 v[188:191], v2, s[44:47], s5 offen offset:2048 sc1\n\tbuffer_load_dwordx4 v[192:195], v2, s[44:47], s6 offen offset:1024 sc1\n\tbuffer_load_dwordx4 v[196:199], v2, s[44:47], s6 offen offset:2048 sc1\n\t"
            "buffer_load_dwordx4 v[200:203], v2, s[44:47], s7 offen offset:1024 sc1\n\tbuffer_load_dwordx4 v[204:207], v2, s[44:47], s7 offen offset:2048 sc1\n\tbuffer_load_dwordx4 v[208:211], v2, s[44:47], s8 offen offset:1024 sc1\n\tbuffer_load_dwordx4 v[212:215], v2, s[44:47], s8 offen offset:2048 sc1\n\ts_cmp_lg_u32 s83, 0\n\ts_cbranch_scc1 1f\n\t"
            "s_call_b64 s[76:77], .LG1\n\t1:\ns_cmp_eq_u32 s83, 2\n\ts_cbranch_scc1 1f\n\ts_call_b64 s[76:77], .LG2\n\t1:\n"
            "ds_read_b128 v[16:19], v8 offset:48\n\tds_read_b64_tr_b16 v[128:129], v4 offset:40960\n\tds_read_b64_tr_b16 v[130:131], v4 offset:41472\n\tds_read_b64_tr_b16 v[132:133], v5 offset:40960\n\tds_read_b64_tr_b16 v[134:135], v5 offset:41472\n\tds_read_b64_tr_b16 v[136:137], v6 offset:40960\n\t"
            "ds_read_b64_tr_b16 v[138:139], v6 offset:41472\n\tds_read_b64_tr_b16 v[140:141], v7 offset:40960\n\tds_read_b64_tr_b16 v[142:143], v7 offset:41472\n\tds_read_b64_tr_b16 v[144:145], v4 offset:41984\n\tds_read_b64_tr_b16 v[146:147], v4 offset:42496\n\tds_read_b64_tr_b16 v[148:149], v5 offset:41984\n\t"
            "ds_read_b64_tr_b16 v[150:151], v5 offset:42496\n\tds_read_b64_tr_b16 v[152:153], v6 offset:41984\n\tds_read_b64_tr_b16 v[154:155], v6 offset:42496\n\tds_read_b64_tr_b16 v[156:157], v7 offset:41984\n\tds_read_b64_tr_b16 v[158:159], v7 offset:42496\n\tv_mul_f32 v48, v48, v112\n\t"
            "v_mul_f32 v49, v49, v113\n\tv_mul_f32 v50, v50, v114\n\tv_mul_f32 v51, v51, v115\n\tv_mul_f32 v52, v52, v116\n\tv_mul_f32 v53, v53, v117\n\tv_mul_f32 v54, v54, v118\n\t"
            "v_mul_f32 v55, v55, v119\n\tv_mul_f32 v56, v56, v120\n\tv_mul_f32 v57, v57, v121\n\tv_mul_f32 v58, v58, v122\n\tv_mul_f32 v59, v59, v123\n\tv_mul_f32 v60, v60, v124\n\t"
            "v_mul_f32 v61, v61, v125\n\tv_mul_f32 v62, v62, v126\n\tv_mul_f32 v63, v63, v127\n\t"
            SO(3)
            "s_waitcnt lgkmcnt(15)\n\tv_min3_u32 v16, v16, v17, v18\n\tv_min_u32 v16, v16, v19\n\tv_cmp_eq_u32 vcc, 0, v16\n\ts_cbranch_vccnz 62f\n\t63:\n"
            PRIO
            "s_waitcnt vmcnt(8)\n\tv_max3_u32 v12, v176, v178, v180\n\tv_max_u32 v12, v12, v182\n\tv_cmp_eq_u32 vcc, -1, v12\n\ts_cbranch_vccnz 100f\n\t200:\n"
            SO(5)
            "s_waitcnt lgkmcnt(8)\n\tv_mfma_f32_32x32x16_bf16 v[96:111], v[128:131], v[176:179], 0\n\tds_read_b64_tr_b16 v[160:161], v4 offset:43008\n\tds_read_b64_tr_b16 v[162:163], v4 offset:43520\n\tbuffer_load_dwordx4 v[216:219], v2, s[44:47], s9 offen offset:1024 sc1\n\tv_mfma_f32_32x32x16_bf16 v[112:127], v[132:135], v[176:179], 0\n\t"
            "ds_read_b64_tr_b16 v[164:165], v5 offset:43008\n\tds_read_b64_tr_b16 v[166:167], v5 offset:43520\n\tbuffer_load_dwordx4 v[220:223], v2, s[44:47], s9 offen offset:2048 sc1\n\tv_mfma_f32_32x32x16_bf16 v[96:111], v[136:139], v[180:183], v[96:111]\n\tds_read_b64_tr_b16 v[168:169], v6 offset:43008\n\tds_read_b64_tr_b16 v[170:171], v6 offset:43520\n\t"
            "s_waitcnt vmcnt(8)\n\tv_max3_u32 v12, v184, v186, v188\n\tv_max_u32 v12, v12, v190\n\tv_cmp_eq_u32 vcc, -1, v12\n\tv_mfma_f32_32x32x16_bf16 v[112:127], v[140:143], v[180:183], v[112:127]\n\tds_read_b64_tr_b16 v[172:173], v7 offset:43008\n\t"
            "ds_read_b64_tr_b16 v[174:175], v7 offset:43520\n\ts_cbranch_vccnz 101f\n\t201:\ns_waitcnt lgkmcnt(8)\n\tv_mfma_f32_32x32x16_bf16 v[96:111], v[144:147], v[184:187], v[96:111]\n\tds_read_b64_tr_b16 v[128:129], v4 offset:44032\n\t"
            "ds_read_b64_tr_b16 v[130:131], v4 offset:44544\n\tbuffer_load_dwordx4 v[224:227], v2, s[44:47], s10 offen offset:1024 sc1\n\tv_mfma_f32_32x32x16_bf16 v[112:127], v[148:151], v[184:187], v[112:127]\n\tds_read_b64_tr_b16 v[132:133], v5 offset:44032\n\tds_read_b64_tr_b16 v[134:135], v5 offset:44544\n\tbuffer_load_dwordx4 v[228:231], v2, s[44:47], s10 offen offset:2048 sc1\n\t"
            "v_mfma_f32_32x32x16_bf16 v[96:111], v[152:155], v[188:191], v[96:111]\n\tds_read_b64_tr_b16 v[136:137], v6 offset:44032\n\tds_read_b64_tr_b16 v[138:139], v6 offset:44544\n\ts_waitcnt vmcnt(8)\n\tv_max3_u32 v12, v192, v194, v196\n\tv_max_u32 v12, v12, v198\n\t"
            "v_cmp_eq_u32 vcc, -1, v12\n\tv_mfma_f32_32x32x16_bf16 v[112:127], v[156:159], v[188:191], v[112:127]\n\tds_read_b64_tr_b16 v[140:141], v7 offset:44032\n\tds_read_b64_tr_b16 v[142:143], v7 offset:44544\n\ts_cbranch_vccnz 102f\n\t202:\n"
            "s_waitcnt lgkmcnt(8)\n\tv_mfma_f32_32x32x16_bf16 v[96:111], v[160:163], v[192:195], v[96:111]\n\tds_read_b64_tr_b16 v[144:145], v4 offset:45056\n\tds_read_b64_tr_b16 v[146:147], v4 offset:45568\n\tbuffer_load_dwordx4 v[232:235], v2, s[44:47], s11 offen offset:1024 sc1\n\tv_mfma_f32_32x32x16_bf16 v[112:127], v[164:167], v[192:195], v[112:127]\n\t"
            "ds_read_b64_tr_b16 v[148:149], v5 offset:45056\n\tds_read_b64_tr_b16 v[150:151], v5 offset:45568\n\tbuffer_load_dwordx4 v[236:239], v2, s[44:47], s11 offen offset:2048 sc1\n\tv_mfma_f32_32x32x16_bf16 v[96:111], v[168:171], v[196:199], v[96:111]\n\tds_read_b64_tr_b16 v[152:153], v6 offset:45056\n\tds_read_b64_tr_b16 v[154:155], v6 offset:45568\n\t"
            "s_waitcnt vmcnt(8)\n\tv_max3_u32 v12, v200, v202, v204\n\tv_max_u32 v12, v12, v206\n\tv_cmp_eq_u32 vcc, -1, v12\n\tv_mfma_f32_32x32x16_bf16 v[112:127], v[172:175], v[196:199], v[112:127]\n\tds_read_b64_tr_b16 v[156:157], v7 offset:45056\n\t"
            "ds_read_b64_tr_b16 v[158:159], v7 offset:45568\n\ts_cbranch_vccnz 103f\n\t203:\ns_waitcnt lgkmcnt(8)\n\tv_mfma_f32_32x32x16_bf16 v[96:111], v[128:131], v[200:203], v[96:111]\n\tds_read_b64_tr_b16 v[160:161], v4 offset:46080\n\t"
            "ds_read_b64_tr_b16 v[162:163], v4 offset:46592\n\tbuffer_load_dwordx4 v[176:179], v2, s[44:47], s12 offen offset:1024 sc1\n\tv_mfma_f32_32x32x16_bf16 v[112:127], v[132:135], v[200:203], v[112:127]\n\tds_read_b64_tr_b16 v[164:165], v5 offset:46080\n\tds_read_b64_tr_b16 v[166:167], v5 offset:46592\n\tds_read_b64 v[14:15], v8 offset:40\n\t"
            "buffer_load_dwordx4 v[180:183], v2, s[44:47], s12 offen offset:2048 sc1\n\tv_mfma_f32_32x32x16_bf16 v[96:111], v[136:139], v[204:207], v[96:111]\n\tds_read_b64_tr_b16 v[168:169], v6 offset:46080\n\tds_read_b64_tr_b16 v[170:171], v6 offset:46592\n\ts_waitcnt vmcnt(8)\n\tv_max3_u32 v12, v208, v210, v212\n\t"
            "v_max_u32 v12, v12, v214\n\tv_cmp_eq_u32 vcc, -1, v12\n\tv_mfma_f32_32x32x16_bf16 v[112:127], v[140:143], v[204:207], v[112:127]\n\tds_read_b64_tr_b16 v[172:173], v7 offset:46080\n\tds_read_b64_tr_b16 v[174:175], v7 offset:46592\n\ts_cbranch_vccnz 104f\n\t"
            "204:\ns_waitcnt lgkmcnt(9)\n\tv_mfma_f32_32x32x16_bf16 v[96:111], v[144:147], v[208:211], v[96:111]\n\tds_read_b64_tr_b16 v[128:129], v4 offset:47104\n\tds_read_b64_tr_b16 v[130:131], v4 offset:47616\n\tbuffer_load_dwordx4 v[184:187], v2, s[44:47], s13 offen offset:1024 sc1\n\t"
            "v_mfma_f32_32x32x16_bf16 v[112:127], v[148:151], v[208:211], v[112:127]\n\tds_read_b64_tr_b16 v[132:133], v5 offset:47104\n\tds_read_b64_tr_b16 v[134:135], v5 offset:47616\n\tbuffer_load_dwordx4 v[188:191], v2, s[44:47], s13 offen offset:2048 sc1\n\tv_mfma_f32_32x32x16_bf16 v[96:111], v[152:155], v[212:215], v[96:111]\n\tds_read_b64_tr_b16 v[136:137], v6 offset:47104\n\t"
            "ds_read_b64_tr_b16 v[138:139], v6 offset:47616\n\ts_waitcnt vmcnt(8)\n\tv_max3_u32 v12, v216, v218, v220\n\tv_max_u32 v12, v12, v222\n\tv_cmp_eq_u32 vcc, -1, v12\n\tv_mfma_f32_32x32x16_bf16 v[112:127], v[156:159], v[212:215], v[112:127]\n\t"
            "ds_read_b64_tr_b16 v[140:141], v7 offset:47104\n\tds_read_b64_tr_b16 v[142:143], v7 offset:47616\n\ts_cbranch_vccnz 105f\n\t205:\ns_waitcnt lgkmcnt(8)\n\tv_mfma_f32_32x32x16_bf16 v[96:111], v[160:163], v[216:219], v[96:111]\n\t"
            "v_min_u32 v14, v14, v15\n\tv_cmp_ge_u32 vcc, s71, v14\n\tds_read_b64_tr_b16 v[144:145], v4 offset:48128\n\tds_read_b64_tr_b16 v[146:147], v4 offset:48640\n\tv_mfma_f32_32x32x16_bf16 v[112:127], v[164:167], v[216:219], v[112:127]\n\ts_cbranch_vccnz 5f\n\t"
            "6:\nds_read_b64_tr_b16 v[148:149], v5 offset:48128\n\tds_read_b64_tr_b16 v[150:151], v5 offset:48640\n\tbuffer_load_dwordx4 v[192:195], v2, s[44:47], s4 offen offset:0 sc1\n\tbuffer_load_dwordx4 v[196:199], v2, s[44:47], s4 offen offset:3072 sc1\n\tv_mfma_f32_32x32x16_bf16 v[96:111], v[168:171], v[220:223], v[96:111]\n\t"
            "ds_read_b64_tr_b16 v[152:153], v6 offset:48128\n\tds_read_b64_tr_b16 v[154:155], v6 offset:48640\n\ts_waitcnt vmcnt(8)\n\tv_max3_u32 v12, v224, v226, v228\n\tv_max_u32 v12, v12, v230\n\tv_cmp_eq_u32 vcc, -1, v12\n\t"
            "v_mfma_f32_32x32x16_bf16 v[112:127], v[172:175], v[220:223], v[112:127]\n\tds_read_b64_tr_b16 v[156:157], v7 offset:48128\n\tds_read_b64_tr_b16 v[158:159], v7 offset:48640\n\ts_cbranch_vccnz 106f\n\t206:\ns_waitcnt lgkmcnt(8)\n\t"
            "v_mfma_f32_32x32x16_bf16 v[96:111], v[128:131], v[224:227], v[96:111]\n\tds_read_b64_tr_b16 v[160:161], v4 offset:49152\n\tds_read_b64_tr_b16 v[162:163], v4 offset:49664\n\tbuffer_load_dwordx4 v[200:203], v2, s[44:47], s5 offen offset:0 sc1\n\tv_mfma_f32_32x32x16_bf16 v[112:127], v[132:135], v[224:227], v[112:127]\n\tds_read_b64_tr_b16 v[164:165], v5 offset:49152\n\t"
            "ds_read_b64_tr_b16 v[166:167], v5 offset:49664\n\tbuffer_load_dwordx4 v[204:207], v2, s[44:47], s5 offen offset:3072 sc1\n\tv_mfma_f32_32x32x16_bf16 v[96:111], v[136:139], v[228:231], v[96:111]\n\tds_read_b64_tr_b16 v[168:169], v6 offset:49152\n\tds_read_b64_tr_b16 v[170:171], v6 offset:49664\n\ts_waitcnt vmcnt(8)\n\t"
            "v_max3_u32 v12, v232, v234, v236\n\tv_max_u32 v12, v12, v238\n\tv_cmp_eq_u32 vcc, -1, v12\n\tv_mfma_f32_32x32x16_bf16 v[112:127], v[140:143], v[228:231], v[112:127]\n\tds_read_b64_tr_b16 v[172:173], v7 offset:49152\n\tds_read_b64_tr_b16 v[174:175], v7 offset:49664\n\t"
            "s_cbranch_vccnz 107f\n\t207:\ns_waitcnt lgkmcnt(8)\n\tv_mfma_f32_32x32x16_bf16 v[96:111], v[144:147], v[232:235], v[96:111]\n\tds_read_b64_tr_b16 v[128:129], v4 offset:50176\n\tds_read_b64_tr_b16 v[130:131], v4 offset:50688\n\t"
            "buffer_load_dwordx4 v[208:211], v2, s[44:47], s6 offen offset:0 sc1\n\tv_mfma_f32_32x32x16_bf16 v[112:127], v[148:151], v[232:235], v[112:127]\n\tds_read_b64_tr_b16 v[132:133], v5 offset:50176\n\tds_read_b64_tr_b16 v[134:135], v5 offset:50688\n\tbuffer_load_dwordx4 v[212:215], v2, s[44:47], s6 offen offset:3072 sc1\n\tv_mfma_f32_32x32x16_bf16 v[96:111], v[152:155], v[236:239], v[96:111]\n\t"
            "ds_read_b64_tr_b16 v[136:137], v6 offset:50176\n\tds_read_b64_tr_b16 v[138:139], v6 offset:50688\n\ts_waitcnt vmcnt(8)\n\tv_max3_u32 v12, v176, v178, v180\n\tv_max_u32 v12, v12, v182\n\tv_cmp_eq_u32 vcc, -1, v12\n\t"
            "v_mfma_f32_32x32x16_bf16 v[112:127], v[156:159], v[236:239], v[112:127]\n\tds_read_b64_tr_b16 v[140:141], v7 offset:50176\n\tds_read_b64_tr_b16 v[142:143], v7 offset:50688\n\ts_cbranch_vccnz 108f\n\t208:\ns_waitcnt lgkmcnt(8)\n\t"
            "v_mfma_f32_32x32x16_bf16 v[96:111], v[160:163], v[176:179], v[96:111]\n\tds_read_b64_tr_b16 v[144:145], v4 offset:0\n\tds_read_b64_tr_b16 v[146:147], v4 offset:512\n\tbuffer_load_dwordx4 v[216:219], v2, s[44:47], s7 offen offset:0 sc1\n\tv_mfma_f32_32x32x16_bf16 v[112:127], v[164:167], v[176:179], v[112:127]\n\tds_read_b64_tr_b16 v[148:149], v5 offset:0\n\t"
            "ds_read_b64_tr_b16 v[150:151], v5 offset:512\n\tbuffer_load_dwordx4 v[220:223], v2, s[44:47], s7 offen offset:3072 sc1\n\tv_mfma_f32_32x32x16_bf16 v[96:111], v[168:171], v[180:183], v[96:111]\n\tds_read_b64_tr_b16 v[152:153], v6 offset:0\n\tds_read_b64_tr_b16 v[154:155], v6 offset:512\n\ts_waitcnt vmcnt(8)\n\t"
            "v_max3_u32 v12, v184, v186, v188\n\tv_max_u32 v12, v12, v190\n\tv_cmp_eq_u32 vcc, -1, v12\n\tv_mfma_f32_32x32x16_bf16 v[112:127], v[172:175], v[180:183], v[112:127]\n\tds_read_b64_tr_b16 v[156:157], v7 offset:0\n\tds_read_b64_tr_b16 v[158:159], v7 offset:512\n\t"
            "s_cbranch_vccnz 109f\n\t209:\ns_waitcnt lgkmcnt(8)\n\tv_mfma_f32_32x32x16_bf16 v[96:111], v[128:131], v[184:187], v[96:111]\n\tds_read_b64_tr_b16 v[160:161], v4 offset:1024\n\tds_read_b64_tr_b16 v[162:163], v4 offset:1536\n\t"
            "buffer_load_dwordx4 v[224:227], v2, s[44:47], s8 offen offset:0 sc1\n\tv_mfma_f32_32x32x16_bf16 v[112:127], v[132:135], v[184:187], v[112:127]\n\tds_read_b64_tr_b16 v[164:165], v5 offset:1024\n\tds_read_b64_tr_b16 v[166:167], v5 offset:1536\n\tbuffer_load_dwordx4 v[228:231], v2, s[44:47], s8 offen offset:3072 sc1\n\tv_mfma_f32_32x32x16_bf16 v[96:111], v[136:139], v[188:191], v[96:111]\n\t"
            "ds_read_b64_tr_b16 v[168:169], v6 offset:1024\n\tds_read_b64_tr_b16 v[170:171], v6 offset:1536\n\ts_waitcnt vmcnt(8)\n\tv_max3_u32 v12, v192, v194, v196\n\tv_max_u32 v12, v12, v198\n\tv_cmp_eq_u32 vcc, -1, v12\n\t"
            "v_mfma_f32_32x32x16_bf16 v[112:127], v[140:143], v[188:191], v[112:127]\n\tds_read_b64_tr_b16 v[172:173], v7 offset:1024\n\tds_read_b64_tr_b16 v[174:175], v7 offset:1536\n\ts_cbranch_vccnz 110f\n\t210:\ns_waitcnt lgkmcnt(8)\n\t"
            "v_mfma_f32_32x32x16_bf16 v[96:111], v[144:147], v[192:195], v[96:111]\n\tds_read_b64_tr_b16 v[128:129], v4 offset:2048\n\tds_read_b64_tr_b16 v[130:131], v4 offset:2560\n\tbuffer_load_dwordx4 v[232:235], v2, s[44:47], s9 offen offset:0 sc1\n\tv_mfma_f32_32x32x16_bf16 v[112:127], v[148:151], v[192:195], v[112:127]\n\tds_read_b64_tr_b16 v[132:133], v5 offset:2048\n\t"
            "ds_read_b64_tr_b16 v[134:135], v5 offset:2560\n\tbuffer_load_dwordx4 v[236:239], v2, s[44:47], s9 offen offset:3072 sc1\n\tv_mfma_f32_32x32x16_bf16 v[96:111], v[152:155], v[196:199], v[96:111]\n\tds_read_b64_tr_b16 v[136:137], v6 offset:2048\n\tds_read_b64_tr_b16 v[138:139], v6 offset:2560\n\ts_waitcnt vmcnt(8)\n\t"
            "v_max3_u32 v12, v200, v202, v204\n\tv_max_u32 v12, v12, v206\n\tv_cmp_eq_u32 vcc, -1, v12\n\tv_mfma_f32_32x32x16_bf16 v[112:127], v[156:159], v[196:199], v[112:127]\n\tds_read_b64_tr_b16 v[140:141], v7 offset:2048\n\tds_read_b64_tr_b16 v[142:143], v7 offset:2560\n\t"
            "s_cbranch_vccnz 111f\n\t211:\ns_waitcnt lgkmcnt(8)\n\tv_mfma_f32_32x32x16_bf16 v[96:111], v[160:163], v[200:203], v[96:111]\n\tds_read_b64_tr_b16 v[144:145], v4 offset:3072\n\tds_read_b64_tr_b16 v[146:147], v4 offset:3584\n\t"
            "buffer_load_dwordx4 v[176:179], v2, s[44:47], s10 offen offset:0 sc1\n\tv_mfma_f32_32x32x16_bf16 v[112:127], v[164:167], v[200:203], v[112:127]\n\tds_read_b64_tr_b16 v[148:149], v5 offset:3072\n\tds_read_b64_tr_b16 v[150:151], v5 offset:3584\n\tbuffer_load_dwordx4 v[180:183], v2, s[44:47], s10 offen offset:3072 sc1\n\tv_mfma_f32_32x32x16_bf16 v[96:111], v[168:171], v[204:207], v[96:111]\n\t"
            "ds_read_b64_tr_b16 v[152:153], v6 offset:3072\n\tds_read_b64_tr_b16 v[154:155], v6 offset:3584\n\ts_waitcnt vmcnt(8)\n\tv_max3_u32 v12, v208, v210, v212\n\tv_max_u32 v12, v12, v214\n\tv_cmp_eq_u32 vcc, -1, v12\n\t"
            "v_mfma_f32_32x32x16_bf16 v[112:127], v[172:175], v[204:207], v[112:127]\n\tds_read_b64_tr_b16 v[156:157], v7 offset:3072\n\tds_read_b64_tr_b16 v[158:159], v7 offset:3584\n\ts_cbranch_vccnz 112f\n\t212:\ns_waitcnt lgkmcnt(8)\n\t"
            "v_mfma_f32_32x32x16_bf16 v[96:111], v[128:131], v[208:211], v[96:111]\n\tds_read_b64_tr_b16 v[160:161], v4 offset:4096\n\tds_read_b64_tr_b16 v[162:163], v4 offset:4608\n\tbuffer_load_dwordx4 v[184:187], v2, s[44:47], s11 offen offset:0 sc1\n\tv_mfma_f32_32x32x16_bf16 v[112:127], v[132:135], v[208:211], v[112:127]\n\tds_read_b64_tr_b16 v[164:165], v5 offset:4096\n\t"
            "ds_read_b64_tr_b16 v[166:167], v5 offset:4608\n\tbuffer_load_dwordx4 v[188:191], v2, s[44:47], s11 offen offset:3072 sc1\n\tv_mfma_f32_32x32x16_bf16 v[96:111], v[136:139], v[212:215], v[96:111]\n\tds_read_b64_tr_b16 v[168:169], v6 offset:4096\n\tds_read_b64_tr_b16 v[170:171], v6 offset:4608\n\ts_waitcnt vmcnt(8)\n\t"
            "v_max3_u32 v12, v216, v218, v220\n\tv_max_u32 v12, v12, v222\n\tv_cmp_eq_u32 vcc, -1, v12\n\tv_mfma_f32_32x32x16_bf16 v[112:127], v[140:143], v[212:215], v[112:127]\n\tds_read_b64_tr_b16 v[172:173], v7 offset:4096\n\tds_read_b64_tr_b16 v[174:175], v7 offset:4608\n\t"
            "s_cbranch_vccnz 113f\n\t213:\ns_waitcnt lgkmcnt(8)\n\tv_mfma_f32_32x32x16_bf16 v[96:111], v[144:147], v[216:219], v[96:111]\n\tds_read_b64_tr_b16 v[128:129], v4 offset:5120\n\tds_read_b64_tr_b16 v[130:131], v4 offset:5632\n\t"
            "buffer_load_dwordx4 v[192:195], v2, s[44:47], s12 offen offset:0 sc1\n\tv_mfma_f32_32x32x16_bf16 v[112:127], v[148:151], v[216:219], v[112:127]\n\tds_read_b64_tr_b16 v[132:133], v5 offset:5120\n\tds_read_b64_tr_b16 v[134:135], v5 offset:5632\n\tbuffer_load_dwordx4 v[196:199], v2, s[44:47], s12 offen offset:3072 sc1\n\tv_mfma_f32_32x32x16_bf16 v[96:111], v[152:155], v[220:223], v[96:111]\n\t"
            "ds_read_b64_tr_b16 v[136:137], v6 offset:5120\n\tds_read_b64_tr_b16 v[138:139], v6 offset:5632\n\ts_waitcnt vmcnt(8)\n\tv_max3_u32 v12, v224, v226, v228\n\tv_max_u32 v12, v12, v230\n\tv_cmp_eq_u32 vcc, -1, v12\n\t"
            "v_mfma_f32_32x32x16_bf16 v[112:127], v[156:159], v[220:223], v[112:127]\n\tds_read_b64_tr_b16 v[140:141], v7 offset:5120\n\tds_read_b64_tr_b16 v[142:143], v7 offset:5632\n\ts_cbranch_vccnz 114f\n\t214:\ns_waitcnt lgkmcnt(8)\n\t"
            "v_mfma_f32_32x32x16_bf16 v[96:111], v[160:163], v[224:227], v[96:111]\n\tds_read_b64_tr_b16 v[144:145], v4 offset:6144\n\tds_read_b64_tr_b16 v[146:147], v4 offset:6656\n\tbuffer_load_dwordx4 v[200:203], v2, s[44:47], s13 offen offset:0 sc1\n\tv_mfma_f32_32x32x16_bf16 v[112:127], v[164:167], v[224:227], v[112:127]\n\tds_read_b64_tr_b16 v[148:149], v5 offset:6144\n\t"
            "ds_read_b64_tr_b16 v[150:151], v5 offset:6656\n\tbuffer_load_dwordx4 v[204:207], v2, s[44:47], s13 offen offset:3072 sc1\n\tv_mfma_f32_32x32x16_bf16 v[96:111], v[168:171], v[228:231], v[96:111]\n\tds_read_b64_tr_b16 v[152:153], v6 offset:6144\n\tds_read_b64_tr_b16 v[154:155], v6 offset:6656\n\ts_waitcnt vmcnt(8)\n\t"
            "v_max3_u32 v12, v232, v234, v236\n\tv_max_u32 v12, v12, v238\n\tv_cmp_eq_u32 vcc, -1, v12\n\tv_mfma_f32_32x32x16_bf16 v[112:127], v[172:175], v[228:231], v[112:127]\n\tds_read_b64_tr_b16 v[156:157], v7 offset:6144\n\tds_read_b64_tr_b16 v[158:159], v7 offset:6656\n\t"
            "s_cbranch_vccnz 115f\n\t215:\ns_waitcnt lgkmcnt(8)\n\tv_mfma_f32_32x32x16_bf16 v[96:111], v[128:131], v[232:235], v[96:111]\n\tds_read_b64_tr_b16 v[160:161], v4 offset:7168\n\tds_read_b64_tr_b16 v[162:163], v4 offset:7680\n\t"
            "v_mfma_f32_32x32x16_bf16 v[112:127], v[132:135], v[232:235], v[112:127]\n\tds_read_b64_tr_b16 v[164:165], v5 offset:7168\n\tds_read_b64_tr_b16 v[166:167], v5 offset:7680\n\tv_mfma_f32_32x32x16_bf16 v[96:111], v[136:139], v[236:239], v[96:111]\n\tds_read_b64_tr_b16 v[168:169], v6 offset:7168\n\tds_read_b64_tr_b16 v[170:171], v6 offset:7680\n\t"
            "s_waitcnt vmcnt(6)\n\tv_max3_u32 v12, v176, v178, v180\n\tv_max_u32 v12, v12, v182\n\tv_cmp_eq_u32 vcc, -1, v12\n\tv_mfma_f32_32x32x16_bf16 v[112:127], v[140:143], v[236:239], v[112:127]\n\tds_read_b64_tr_b16 v[172:173], v7 offset:7168\n\t"
            "ds_read_b64_tr_b16 v[174:175], v7 offset:7680\n\ts_cbranch_vccnz 116f\n\t216:\ns_waitcnt lgkmcnt(8)\n\tv_mfma_f32_32x32x16_bf16 v[96:111], v[144:147], v[176:179], v[96:111]\n\tds_read_b64_tr_b16 v[128:129], v4 offset:8192\n\t"
            "ds_read_b64_tr_b16 v[130:131], v4 offset:8704\n\tv_mfma_f32_32x32x16_bf16 v[112:127], v[148:151], v[176:179], v[112:127]\n\tds_read_b64_tr_b16 v[132:133], v5 offset:8192\n\tds_read_b64_tr_b16 v[134:135], v5 offset:8704\n\tv_mfma_f32_32x32x16_bf16 v[96:111], v[152:155], v[180:183], v[96:111]\n\tds_read_b64_tr_b16 v[136:137], v6 offset:8192\n\t"
            "ds_read_b64_tr_b16 v[138:139], v6 offset:8704\n\ts_waitcnt vmcnt(4)\n\tv_max3_u32 v12, v184, v186, v188\n\tv_max_u32 v12, v12, v190\n\tv_cmp_eq_u32 vcc, -1, v12\n\tv_mfma_f32_32x32x16_bf16 v[112:127], v[156:159], v[180:183], v[112:127]\n\t"
            "ds_read_b64_tr_b16 v[140:141], v7 offset:8192\n\tds_read_b64_tr_b16 v[142:143], v7 offset:8704\n\ts_cbranch_vccnz 117f\n\t217:\nds_read_b32 v16, v8 offset:64\n\tds_read_b32 v17, v9 offset:1344\n\t"
            "s_waitcnt lgkmcnt(10)\n\tv_mfma_f32_32x32x16_bf16 v[96:111], v[160:163], v[184:187], v[96:111]\n\tds_read_b64_tr_b16 v[144:145], v4 offset:9216\n\tds_read_b64_tr_b16 v[146:147], v4 offset:9728\n\tds_read_b64_tr_b16 v[148:149], v5 offset:9216\n\tds_read_b64_tr_b16 v[150:151], v5 offset:9728\n\t"
            "ds_read_b64_tr_b16 v[152:153], v6 offset:9216\n\tds_read_b64_tr_b16 v[154:155], v6 offset:9728\n\ts_waitcnt vmcnt(2)\n\tv_max3_u32 v12, v192, v194, v196\n\tv_max_u32 v12, v12, v198\n\tv_cmp_eq_u32 vcc, -1, v12\n\t"
            "v_mfma_f32_32x32x16_bf16 v[96:111], v[168:171], v[188:191], v[96:111]\n\tds_read_b64_tr_b16 v[156:157], v7 offset:9216\n\tds_read_b64_tr_b16 v[158:159], v7 offset:9728\n\ts_cbranch_vccnz 118f\n\t218:\ns_waitcnt lgkmcnt(10)\n\t"
            "v_mfma_f32_32x32x16_bf16 v[96:111], v[128:131], v[192:195], v[96:111]\n\ts_waitcnt vmcnt(0)\n\tv_max3_u32 v12, v200, v202, v204\n\tv_max_u32 v12, v12, v206\n\tv_cmp_eq_u32 vcc, -1, v12\n\tv_mfma_f32_32x32x16_bf16 v[96:111], v[136:139], v[196:199], v[96:111]\n\t"
            "s_cbranch_vccnz 119f\n\t219:\ns_waitcnt lgkmcnt(0)\n\tv_mfma_f32_32x32x16_bf16 v[96:111], v[144:147], v[200:203], v[96:111]\n\tv_cmp_ne_u32 s[74:75], 0, v17\n\tv_cmp_eq_u32 vcc, 0, v16\n\t"
            "v_mfma_f32_32x32x16_bf16 v[96:111], v[152:155], v[204:207], v[96:111]\n\ts_or_b64 vcc, vcc, s[74:75]\n\tv_mfma_f32_32x32x16_bf16 v[112:127], v[164:167], v[184:187], v[112:127]\n\ts_cbranch_scc1 7f\n\t8:\ns_nop 5\n\t"
            "v_mfma_f32_32x32x16_bf16 v[112:127], v[172:175], v[188:191], v[112:127]\n\ts_nop 1\n\tv_fmac_f32 v64, v96, v32\n\tv_fmac_f32 v65, v97, v33\n\tv_fmac_f32 v66, v98, v34\n\tv_fmac_f32 v67, v99, v35\n\t"
            "v_fmac_f32 v68, v100, v36\n\tv_fmac_f32 v69, v101, v37\n\tv_fmac_f32 v70, v102, v38\n\tv_mfma_f32_32x32x16_bf16 v[112:127], v[132:135], v[192:195], v[112:127]\n\tv_fmac_f32 v71, v103, v39\n\tv_fmac_f32 v72, v104, v40\n\t"
            "v_fmac_f32 v73, v105, v41\n\tv_fmac_f32 v74, v106, v42\n\tv_fmac_f32 v75, v107, v43\n\tv_fmac_f32 v76, v108, v44\n\tv_fmac_f32 v77, v109, v45\n\tv_mfma_f32_32x32x16_bf16 v[112:127], v[140:143], v[196:199], v[112:127]\n\t"
            "v_fmac_f32 v78, v110, v46\n\tv_fmac_f32 v79, v111, v47\n\tv_cvt_pk_bf16_f32 v240, v64, v65\n\tv_cvt_pk_bf16_f32 v242, v68, v69\n\tv_cvt_pk_bf16_f32 v241, v66, v67\n\tv_cvt_pk_bf16_f32 v243, v70, v71\n\t"
            "v_cvt_pk_bf16_f32 v244, v72, v73\n\tv_mfma_f32_32x32x16_bf16 v[112:127], v[148:151], v[200:203], v[112:127]\n\tv_cvt_pk_bf16_f32 v246, v76, v77\n\tv_cvt_pk_bf16_f32 v245, v74, v75\n\tv_cvt_pk_bf16_f32 v247, v78, v79\n\tv_permlane32_swap_b32 v240, v242\n\t"
            "v_permlane32_swap_b32 v241, v243\n\tv_permlane32_swap_b32 v244, v246\n\tv_permlane32_swap_b32 v245, v247\n\tv_mfma_f32_32x32x16_bf16 v[112:127], v[156:159], v[204:207], v[112:127]\n\tv_permlane16_swap_b32 v240, v244\n\tv_permlane16_swap_b32 v242, v246\n\t"
            "v_permlane16_swap_b32 v241, v245\n\tv_permlane16_swap_b32 v243, v247\n\tbuffer_store_dwordx4 v[240:243], v3, s[52:55], 0 offen offset:0 sc1\n\tbuffer_store_dwordx4 v[244:247], v11, s[52:55], 0 offen offset:0 sc1\n\t"
            SO(6)
            "s_nop 5\n\tv_fmac_f32 v80, v112, v48\n\tv_fmac_f32 v81, v113, v49\n\tv_fmac_f32 v82, v114, v50\n\tv_fmac_f32 v83, v115, v51\n\tv_fmac_f32 v84, v116, v52\n\t"
            "v_fmac_f32 v85, v117, v53\n\tv_fmac_f32 v86, v118, v54\n\tv_fmac_f32 v87, v119, v55\n\tv_fmac_f32 v88, v120, v56\n\tv_fmac_f32 v89, v121, v57\n\tv_fmac_f32 v90, v122, v58\n\t"
            "v_fmac_f32 v91, v123, v59\n\tv_fmac_f32 v92, v124, v60\n\tv_fmac_f32 v93, v125, v61\n\tv_fmac_f32 v94, v126, v62\n\tv_fmac_f32 v95, v127, v63\n\tv_cvt_pk_bf16_f32 v248, v80, v81\n\t"
            "v_cvt_pk_bf16_f32 v250, v84, v85\n\tv_cvt_pk_bf16_f32 v249, v82, v83\n\tv_cvt_pk_bf16_f32 v251, v86, v87\n\tv_cvt_pk_bf16_f32 v252, v88, v89\n\tv_cvt_pk_bf16_f32 v254, v92, v93\n\tv_cvt_pk_bf16_f32 v253, v90, v91\n\t"
            "v_cvt_pk_bf16_f32 v255, v94, v95\n\tv_permlane32_swap_b32 v248, v250\n\tv_permlane32_swap_b32 v249, v251\n\tv_permlane32_swap_b32 v252, v254\n\tv_permlane32_swap_b32 v253, v255\n\ts_nop 0\n\t"
            "v_permlane16_swap_b32 v248, v252\n\tv_permlane16_swap_b32 v250, v254\n\tv_permlane16_swap_b32 v249, v253\n\tv_permlane16_swap_b32 v251, v255\n\tbuffer_store_dwordx4 v[248:251], v3, s[52:55], 0 offen offset:64 sc1\n\tbuffer_store_dwordx4 v[252:255], v11, s[52:55], 0 offen offset:64 sc1\n\t"
            "9:\n"
            RCNT DUMP
            "s_endpgm\n\t.LG0:\nv_mul_f32 v21, 0xbfb8aa3b, v21\n\tv_mul_f32 v20, v20, v21\n\tv_permlane16_swap_b32 v240, v244\n\tv_permlane16_swap_b32 v241, v245\n\t"
            "v_permlane16_swap_b32 v242, v246\n\tv_permlane16_swap_b32 v243, v247\n\tv_permlane16_swap_b32 v248, v252\n\tv_permlane16_swap_b32 v249, v253\n\tv_permlane16_swap_b32 v250, v254\n\tv_permlane16_swap_b32 v251, v255\n\t"
            "v_permlane32_swap_b32 v240, v242\n\tv_permlane32_swap_b32 v241, v243\n\tv_permlane32_swap_b32 v244, v246\n\tv_permlane32_swap_b32 v245, v247\n\tv_permlane32_swap_b32 v248, v250\n\tv_permlane32_swap_b32 v249, v251\n\t"
            "v_permlane32_swap_b32 v252, v254\n\tv_permlane32_swap_b32 v253, v255\n\tv_lshlrev_b32 v64, 16, v240\n\tv_and_b32 v65, 0xffff0000, v240\n\tv_lshlrev_b32 v66, 16, v241\n\tv_and_b32 v67, 0xffff0000, v241\n\t"
            "v_lshlrev_b32 v68, 16, v242\n\tv_and_b32 v69, 0xffff0000, v242\n\tv_lshlrev_b32 v70, 16, v243\n\tv_and_b32 v71, 0xffff0000, v243\n\tv_lshlrev_b32 v72, 16, v244\n\tv_and_b32 v73, 0xffff0000, v244\n\t"
            "v_lshlrev_b32 v74, 16, v245\n\tv_and_b32 v75, 0xffff0000, v245\n\tv_lshlrev_b32 v76, 16, v246\n\tv_and_b32 v77, 0xffff0000, v246\n\tv_lshlrev_b32 v78, 16, v247\n\tv_and_b32 v79, 0xffff0000, v247\n\t"
            "v_lshlrev_b32 v80, 16, v248\n\tv_and_b32 v81, 0xffff0000, v248\n\tv_lshlrev_b32 v82, 16, v249\n\tv_and_b32 v83, 0xffff0000, v249\n\tv_lshlrev_b32 v84, 16, v250\n\tv_and_b32 v85, 0xffff0000, v250\n\t"
            "v_lshlrev_b32 v86, 16, v251\n\tv_and_b32 v87, 0xffff0000, v251\n\tv_lshlrev_b32 v88, 16, v252\n\tv_and_b32 v89, 0xffff0000, v252\n\tv_lshlrev_b32 v90, 16, v253\n\tv_and_b32 v91, 0xffff0000, v253\n\t"
            "v_lshlrev_b32 v92, 16, v254\n\tv_and_b32 v93, 0xffff0000, v254\n\tv_lshlrev_b32 v94, 16, v255\n\tv_and_b32 v95, 0xffff0000, v255\n\tv_fma_f32 v96, v64, v21, v20\n\tv_fma_f32 v97, v65, v21, v20\n\t"
            "v_fma_f32 v98, v66, v21, v20\n\tv_fma_f32 v99, v67, v21, v20\n\tv_fma_f32 v100, v68, v21, v20\n\tv_fma_f32 v101, v69, v21, v20\n\tv_fma_f32 v102, v70, v21, v20\n\tv_fma_f32 v103, v71, v21, v20\n\t"
            "v_fma_f32 v104, v72, v21, v20\n\tv_fma_f32 v105, v73, v21, v20\n\tv_fma_f32 v106, v74, v21, v20\n\tv_fma_f32 v107, v75, v21, v20\n\tv_fma_f32 v108, v76, v21, v20\n\tv_fma_f32 v109, v77, v21, v20\n\t"
            "v_fma_f32 v110, v78, v21, v20\n\tv_fma_f32 v111, v79, v21, v20\n\tv_fma_f32 v112, v80, v21, v20\n\tv_fma_f32 v113, v81, v21, v20\n\tv_fma_f32 v114, v82, v21, v20\n\tv_fma_f32 v115, v83, v21, v20\n\t"
            "v_fma_f32 v116, v84, v21, v20\n\tv_fma_f32 v117, v85, v21, v20\n\tv_fma_f32 v118, v86, v21, v20\n\tv_fma_f32 v119, v87, v21, v20\n\tv_fma_f32 v120, v88, v21, v20\n\tv_fma_f32 v121, v89, v21, v20\n\t"
            "v_fma_f32 v122, v90, v21, v20\n\tv_fma_f32 v123, v91, v21, v20\n\tv_fma_f32 v124, v92, v21, v20\n\tv_fma_f32 v125, v93, v21, v20\n\tv_fma_f32 v126, v94, v21, v20\n\tv_fma_f32 v127, v95, v21, v20\n\t"
            "s_setpc_b64 s[76:77]\n\t.LG1:\nv_exp_f32 v32, v96\n\tv_exp_f32 v33, v97\n\tv_exp_f32 v34, v98\n\tv_exp_f32 v35, v99\n\t"
            "v_exp_f32 v36, v100\n\tv_exp_f32 v37, v101\n\tv_exp_f32 v38, v102\n\tv_exp_f32 v39, v103\n\tv_exp_f32 v40, v104\n\tv_exp_f32 v41, v105\n\t"
            "v_exp_f32 v42, v106\n\tv_exp_f32 v43, v107\n\tv_exp_f32 v44, v108\n\tv_exp_f32 v45, v109\n\tv_exp_f32 v46, v110\n\tv_exp_f32 v47, v111\n\t"
            "v_exp_f32 v48, v112\n\tv_exp_f32 v49, v113\n\tv_exp_f32 v50, v114\n\tv_exp_f32 v51, v115\n\tv_exp_f32 v52, v116\n\tv_exp_f32 v53, v117\n\t"
            "v_exp_f32 v54, v118\n\tv_exp_f32 v55, v119\n\tv_exp_f32 v56, v120\n\tv_exp_f32 v57, v121\n\tv_exp_f32 v58, v122\n\tv_exp_f32 v59, v123\n\t"
            "v_exp_f32 v60, v124\n\tv_exp_f32 v61, v125\n\tv_exp_f32 v62, v126\n\tv_exp_f32 v63, v127\n\tv_add_f32 v32, 1.0, v32\n\tv_add_f32 v33, 1.0, v33\n\t"
            "v_add_f32 v34, 1.0, v34\n\tv_add_f32 v35, 1.0, v35\n\tv_add_f32 v36, 1.0, v36\n\tv_add_f32 v37, 1.0, v37\n\tv_add_f32 v38, 1.0, v38\n\tv_add_f32 v39, 1.0, v39\n\t"
            "v_add_f32 v40, 1.0, v40\n\tv_add_f32 v41, 1.0, v41\n\tv_add_f32 v42, 1.0, v42\n\tv_add_f32 v43, 1.0, v43\n\tv_add_f32 v44, 1.0, v44\n\tv_add_f32 v45, 1.0, v45\n\t"
            "v_add_f32 v46, 1.0, v46\n\tv_add_f32 v47, 1.0, v47\n\tv_add_f32 v48, 1.0, v48\n\tv_add_f32 v49, 1.0, v49\n\tv_add_f32 v50, 1.0, v50\n\tv_add_f32 v51, 1.0, v51\n\t"
            "v_add_f32 v52, 1.0, v52\n\tv_add_f32 v53, 1.0, v53\n\tv_add_f32 v54, 1.0, v54\n\tv_add_f32 v55, 1.0, v55\n\tv_add_f32 v56, 1.0, v56\n\tv_add_f32 v57, 1.0, v57\n\t"
            "v_add_f32 v58, 1.0, v58\n\tv_add_f32 v59, 1.0, v59\n\tv_add_f32 v60, 1.0, v60\n\tv_add_f32 v61, 1.0, v61\n\tv_add_f32 v62, 1.0, v62\n\tv_add_f32 v63, 1.0, v63\n\t"
            "v_rcp_f32 v32, v32\n\tv_rcp_f32 v33, v33\n\tv_rcp_f32 v34, v34\n\tv_rcp_f32 v35, v35\n\ts_mov_b32 s83, 1\n\ts_setpc_b64 s[76:77]\n\t"
            ".LG2:\nv_rcp_f32 v36, v36\n\tv_rcp_f32 v37, v37\n\tv_rcp_f32 v38, v38\n\tv_rcp_f32 v39, v39\n\tv_rcp_f32 v40, v40\n\t"
            "v_rcp_f32 v41, v41\n\tv_rcp_f32 v42, v42\n\tv_rcp_f32 v43, v43\n\tv_rcp_f32 v44, v44\n\tv_rcp_f32 v45, v45\n\tv_rcp_f32 v46, v46\n\t"
            "v_rcp_f32 v47, v47\n\tv_rcp_f32 v48, v48\n\tv_rcp_f32 v49, v49\n\tv_rcp_f32 v50, v50\n\tv_rcp_f32 v51, v51\n\tv_rcp_f32 v52, v52\n\t"
            "v_rcp_f32 v53, v53\n\tv_rcp_f32 v54, v54\n\tv_rcp_f32 v55, v55\n\tv_rcp_f32 v56, v56\n\tv_rcp_f32 v57, v57\n\tv_rcp_f32 v58, v58\n\t"
            "v_rcp_f32 v59, v59\n\tv_rcp_f32 v60, v60\n\tv_rcp_f32 v61, v61\n\tv_rcp_f32 v62, v62\n\tv_rcp_f32 v63, v63\n\tv_mul_f32 v32, v32, v96\n\t"
            "v_mul_f32 v33, v33, v97\n\tv_mul_f32 v34, v34, v98\n\tv_mul_f32 v35, v35, v99\n\tv_mul_f32 v36, v36, v100\n\tv_mul_f32 v37, v37, v101\n\tv_mul_f32 v38, v38, v102\n\t"
            "v_mul_f32 v39, v39, v103\n\tv_mul_f32 v40, v40, v104\n\tv_mul_f32 v41, v41, v105\n\tv_mul_f32 v42, v42, v106\n\tv_mul_f32 v43, v43, v107\n\tv_mul_f32 v44, v44, v108\n\t"
            "v_mul_f32 v45, v45, v109\n\tv_mul_f32 v46, v46, v110\n\tv_mul_f32 v47, v47, v111\n\ts_mov_b32 s83, 2\n\ts_setpc_b64 s[76:77]\n\t7:\n"
            "ds_read_b32 v16, v8 offset:64\n\tds_read_b32 v17, v9 offset:1344\n\ts_waitcnt lgkmcnt(0)\n\tv_cmp_ne_u32 s[74:75], 0, v17\n\tv_cmp_eq_u32 vcc, 0, v16\n\ts_cbranch_vccz 71f\n\t"
            "s_sleep 1\n\ts_sub_u32 s68, s68, 1\n\ts_cbranch_scc0 7b\n\ts_branch 9b\n\t71:\nv_lshrrev_b32 v12, 5, v2\n\t"
            "v_and_b32 v12, 16, v12\n\tv_add_u32 v12, v12, v17\n\ts_mov_b64 exec, s[74:75]\n\ts_cbranch_execz 72f\n\tds_read_b128 v[32:35], v12 offset:0\n\tds_read_b128 v[36:39], v12 offset:32\n\t"
            "ds_read_b128 v[40:43], v12 offset:64\n\tds_read_b128 v[44:47], v12 offset:96\n\tds_read_b128 v[48:51], v12 offset:128\n\tds_read_b128 v[52:55], v12 offset:160\n\tds_read_b128 v[56:59], v12 offset:192\n\tds_read_b128 v[60:63], v12 offset:224\n\t"
            "72:\ns_mov_b64 exec, -1\n\ts_waitcnt lgkmcnt(0)\n\ts_branch 8b\n\t5:\ns_sleep 1\n\t"
            "ds_read_b64 v[14:15], v8 offset:40\n\ts_waitcnt lgkmcnt(0)\n\tv_min_u32 v14, v14, v15\n\tv_cmp_ge_u32 vcc, s71, v14\n\ts_cbranch_vccz 61f\n\ts_sub_u32 s68, s68, 1\n\t"
            "s_cbranch_scc0 5b\n\ts_branch 9b\n\t61:\n"
            TRG2D
            "s_branch 6b\n\t62:\nds_read_b128 v[16:19], v8 offset:48\n\tds_read_b64_tr_b16 v[128:129], v4 offset:40960\n\tds_read_b64_tr_b16 v[130:131], v4 offset:41472\n\tds_read_b64_tr_b16 v[132:133], v5 offset:40960\n\t"
            "ds_read_b64_tr_b16 v[134:135], v5 offset:41472\n\tds_read_b64_tr_b16 v[136:137], v6 offset:40960\n\tds_read_b64_tr_b16 v[138:139], v6 offset:41472\n\tds_read_b64_tr_b16 v[140:141], v7 offset:40960\n\tds_read_b64_tr_b16 v[142:143], v7 offset:41472\n\tds_read_b64_tr_b16 v[144:145], v4 offset:41984\n\t"
            "ds_read_b64_tr_b16 v[146:147], v4 offset:42496\n\tds_read_b64_tr_b16 v[148:149], v5 offset:41984\n\tds_read_b64_tr_b16 v[150:151], v5 offset:42496\n\tds_read_b64_tr_b16 v[152:153], v6 offset:41984\n\tds_read_b64_tr_b16 v[154:155], v6 offset:42496\n\tds_read_b64_tr_b16 v[156:157], v7 offset:41984\n\t"
            "ds_read_b64_tr_b16 v[158:159], v7 offset:42496\n\ts_waitcnt lgkmcnt(15)\n\tv_min3_u32 v16, v16, v17, v18\n\tv_min_u32 v16, v16, v19\n\tv_cmp_eq_u32 vcc, 0, v16\n\ts_cbranch_vccz 63b\n\t"
            "s_sub_u32 s68, s68, 1\n\ts_cbranch_scc0 62b\n\ts_branch 9b\n\t100:\ns_mov_b32 s70, 4\n\ts_call_b64 s[72:73], 300f\n\t"
            "v_max3_u32 v12, v176, v178, v180\n\tv_max_u32 v12, v12, v182\n\tv_cmp_eq_u32 vcc, -1, v12\n\ts_cbranch_vccnz 100b\n\ts_branch 200b\n\t101:\n"
            "s_mov_b32 s70, 5\n\ts_call_b64 s[72:73], 301f\n\tv_max3_u32 v12, v184, v186, v188\n\tv_max_u32 v12, v12, v190\n\tv_cmp_eq_u32 vcc, -1, v12\n\ts_cbranch_vccnz 101b\n\t"
            "s_branch 201b\n\t102:\ns_mov_b32 s70, 6\n\ts_call_b64 s[72:73], 302f\n\tv_max3_u32 v12, v192, v194, v196\n\tv_max_u32 v12, v12, v198\n\t"
            "v_cmp_eq_u32 vcc, -1, v12\n\ts_cbranch_vccnz 102b\n\ts_branch 202b\n\t103:\ns_mov_b32 s70, 7\n\ts_call_b64 s[72:73], 303f\n\t"
            "v_max3_u32 v12, v200, v202, v204\n\tv_max_u32 v12, v12, v206\n\tv_cmp_eq_u32 vcc, -1, v12\n\ts_cbranch_vccnz 103b\n\ts_branch 203b\n\t104:\n"
            "s_mov_b32 s70, 8\n\ts_call_b64 s[72:73], 304f\n\tv_max3_u32 v12, v208, v210, v212\n\tv_max_u32 v12, v12, v214\n\tv_cmp_eq_u32 vcc, -1, v12\n\ts_cbranch_vccnz 104b\n\t"
            "s_branch 204b\n\t105:\ns_mov_b32 s70, 9\n\ts_call_b64 s[72:73], 305f\n\tv_max3_u32 v12, v216, v218, v220\n\tv_max_u32 v12, v12, v222\n\t"
            "v_cmp_eq_u32 vcc, -1, v12\n\ts_cbranch_vccnz 105b\n\ts_branch 205b\n\t106:\ns_mov_b32 s70, 10\n\ts_call_b64 s[72:73], 306f\n\t"
            "v_max3_u32 v12, v224, v226, v228\n\tv_max_u32 v12, v12, v230\n\tv_cmp_eq_u32 vcc, -1, v12\n\ts_cbranch_vccnz 106b\n\ts_branch 206b\n\t107:\n"
            "s_mov_b32 s70, 11\n\ts_call_b64 s[72:73], 307f\n\tv_max3_u32 v12, v232, v234, v236\n\tv_max_u32 v12, v12, v238\n\tv_cmp_eq_u32 vcc, -1, v12\n\ts_cbranch_vccnz 107b\n\t"
            "s_branch 207b\n\t108:\ns_mov_b32 s70, 12\n\ts_call_b64 s[72:73], 308f\n\tv_max3_u32 v12, v176, v178, v180\n\tv_max_u32 v12, v12, v182\n\t"
            "v_cmp_eq_u32 vcc, -1, v12\n\ts_cbranch_vccnz 108b\n\ts_branch 208b\n\t109:\ns_mov_b32 s70, 13\n\ts_call_b64 s[72:73], 309f\n\t"
            "v_max3_u32 v12, v184, v186, v188\n\tv_max_u32 v12, v12, v190\n\tv_cmp_eq_u32 vcc, -1, v12\n\ts_cbranch_vccnz 109b\n\ts_branch 209b\n\t110:\n"
            "s_mov_b32 s70, 14\n\ts_call_b64 s[72:73], 310f\n\tv_max3_u32 v12, v192, v194, v196\n\tv_max_u32 v12, v12, v198\n\tv_cmp_eq_u32 vcc, -1, v12\n\ts_cbranch_vccnz 110b\n\t"
            "s_branch 210b\n\t111:\ns_mov_b32 s70, 15\n\ts_call_b64 s[72:73], 311f\n\tv_max3_u32 v12, v200, v202, v204\n\tv_max_u32 v12, v12, v206\n\t"
            "v_cmp_eq_u32 vcc, -1, v12\n\ts_cbranch_vccnz 111b\n\ts_branch 211b\n\t112:\ns_mov_b32 s70, 16\n\ts_call_b64 s[72:73], 312f\n\t"
            "v_max3_u32 v12, v208, v210, v212\n\tv_max_u32 v12, v12, v214\n\tv_cmp_eq_u32 vcc, -1, v12\n\ts_cbranch_vccnz 112b\n\ts_branch 212b\n\t113:\n"
            "s_mov_b32 s70, 17\n\ts_call_b64 s[72:73], 313f\n\tv_max3_u32 v12, v216, v218, v220\n\tv_max_u32 v12, v12, v222\n\tv_cmp_eq_u32 vcc, -1, v12\n\ts_cbranch_vccnz 113b\n\t"
            "s_branch 213b\n\t114:\ns_mov_b32 s70, 18\n\ts_call_b64 s[72:73], 314f\n\tv_max3_u32 v12, v224, v226, v228\n\tv_max_u32 v12, v12, v230\n\t"
            "v_cmp_eq_u32 vcc, -1, v12\n\ts_cbranch_vccnz 114b\n\ts_branch 214b\n\t115:\ns_mov_b32 s70, 19\n\ts_call_b64 s[72:73], 315f\n\t"
            "v_max3_u32 v12, v232, v234, v236\n\tv_max_u32 v12, v12, v238\n\tv_cmp_eq_u32 vcc, -1, v12\n\ts_cbranch_vccnz 115b\n\ts_branch 215b\n\t116:\n"
            "s_mov_b32 s70, 19\n\ts_call_b64 s[72:73], 316f\n\tv_max3_u32 v12, v176, v178, v180\n\tv_max_u32 v12, v12, v182\n\tv_cmp_eq_u32 vcc, -1, v12\n\ts_cbranch_vccnz 116b\n\t"
            "s_branch 216b\n\t117:\ns_mov_b32 s70, 19\n\ts_call_b64 s[72:73], 317f\n\tv_max3_u32 v12, v184, v186, v188\n\tv_max_u32 v12, v12, v190\n\t"
            "v_cmp_eq_u32 vcc, -1, v12\n\ts_cbranch_vccnz 117b\n\ts_branch 217b\n\t118:\ns_mov_b32 s70, 19\n\ts_call_b64 s[72:73], 318f\n\t"
            "v_max3_u32 v12, v192, v194, v196\n\tv_max_u32 v12, v12, v198\n\tv_cmp_eq_u32 vcc, -1, v12\n\ts_cbranch_vccnz 118b\n\ts_branch 218b\n\t119:\n"
            "s_mov_b32 s70, 19\n\ts_call_b64 s[72:73], 319f\n\tv_max3_u32 v12, v200, v202, v204\n\tv_max_u32 v12, v12, v206\n\tv_cmp_eq_u32 vcc, -1, v12\n\ts_cbranch_vccnz 119b\n\t"
            "s_branch 219b\n\t400:\nv_max3_u32 v12, v176, v178, v180\n\tv_max_u32 v12, v12, v182\n\tv_cmp_eq_u32 vcc, -1, v12\n\ts_cbranch_vccz 11f\n\t"
            "buffer_load_dwordx4 v[176:179], v2, s[44:47], s4 offen offset:1024 sc1\n\tbuffer_load_dwordx4 v[180:183], v2, s[44:47], s4 offen offset:2048 sc1\n\ts_branch 11f\n\t300:\nbuffer_load_dwordx4 v[176:179], v2, s[44:47], s4 offen offset:1024 sc1\n\tbuffer_load_dwordx4 v[180:183], v2, s[44:47], s4 offen offset:2048 sc1\n\t"
            "s_waitcnt vmcnt(2)\n\t11:\ns_cmp_eq_u32 s70, 0\n\ts_cbranch_scc0 401f\n\ts_branch 12f\n\t401:\n"
            "v_max3_u32 v12, v184, v186, v188\n\tv_max_u32 v12, v12, v190\n\tv_cmp_eq_u32 vcc, -1, v12\n\ts_cbranch_vccz 11f\n\tbuffer_load_dwordx4 v[184:187], v2, s[44:47], s5 offen offset:1024 sc1\n\tbuffer_load_dwordx4 v[188:191], v2, s[44:47], s5 offen offset:2048 sc1\n\t"
            "s_branch 11f\n\t301:\nbuffer_load_dwordx4 v[184:187], v2, s[44:47], s5 offen offset:1024 sc1\n\tbuffer_load_dwordx4 v[188:191], v2, s[44:47], s5 offen offset:2048 sc1\n\ts_waitcnt vmcnt(2)\n\t11:\n"
            "s_cmp_eq_u32 s70, 1\n\ts_cbranch_scc0 402f\n\ts_branch 12f\n\t402:\nv_max3_u32 v12, v192, v194, v196\n\tv_max_u32 v12, v12, v198\n\t"
            "v_cmp_eq_u32 vcc, -1, v12\n\ts_cbranch_vccz 11f\n\tbuffer_load_dwordx4 v[192:195], v2, s[44:47], s6 offen offset:1024 sc1\n\tbuffer_load_dwordx4 v[196:199], v2, s[44:47], s6 offen offset:2048 sc1\n\ts_branch 11f\n\t302:\n"
            "buffer_load_dwordx4 v[192:195], v2, s[44:47], s6 offen offset:1024 sc1\n\tbuffer_load_dwordx4 v[196:199], v2, s[44:47], s6 offen offset:2048 sc1\n\ts_waitcnt vmcnt(2)\n\t11:\ns_cmp_eq_u32 s70, 2\n\ts_cbranch_scc0 403f\n\t"
            "s_branch 12f\n\t403:\nv_max3_u32 v12, v200, v202, v204\n\tv_max_u32 v12, v12, v206\n\tv_cmp_eq_u32 vcc, -1, v12\n\ts_cbranch_vccz 11f\n\t"
            "buffer_load_dwordx4 v[200:203], v2, s[44:47], s7 offen offset:1024 sc1\n\tbuffer_load_dwordx4 v[204:207], v2, s[44:47], s7 offen offset:2048 sc1\n\ts_branch 11f\n\t303:\nbuffer_load_dwordx4 v[200:203], v2, s[44:47], s7 offen offset:1024 sc1\n\tbuffer_load_dwordx4 v[204:207], v2, s[44:47], s7 offen offset:2048 sc1\n\t"
            "s_waitcnt vmcnt(2)\n\t11:\ns_cmp_eq_u32 s70, 3\n\ts_cbranch_scc0 404f\n\ts_branch 12f\n\t404:\n"
            "v_max3_u32 v12, v208, v210, v212\n\tv_max_u32 v12, v12, v214\n\tv_cmp_eq_u32 vcc, -1, v12\n\ts_cbranch_vccz 11f\n\tbuffer_load_dwordx4 v[208:211], v2, s[44:47], s8 offen offset:1024 sc1\n\tbuffer_load_dwordx4 v[212:215], v2, s[44:47], s8 offen offset:2048 sc1\n\t"
            "s_branch 11f\n\t304:\nbuffer_load_dwordx4 v[208:211], v2, s[44:47], s8 offen offset:1024 sc1\n\tbuffer_load_dwordx4 v[212:215], v2, s[44:47], s8 offen offset:2048 sc1\n\ts_waitcnt vmcnt(2)\n\t11:\n"
            "s_cmp_eq_u32 s70, 4\n\ts_cbranch_scc0 405f\n\ts_branch 12f\n\t405:\nv_max3_u32 v12, v216, v218, v220\n\tv_max_u32 v12, v12, v222\n\t"
            "v_cmp_eq_u32 vcc, -1, v12\n\ts_cbranch_vccz 11f\n\tbuffer_load_dwordx4 v[216:219], v2, s[44:47], s9 offen offset:1024 sc1\n\tbuffer_load_dwordx4 v[220:223], v2, s[44:47], s9 offen offset:2048 sc1\n\ts_branch 11f\n\t305:\n"
            "buffer_load_dwordx4 v[216:219], v2, s[44:47], s9 offen offset:1024 sc1\n\tbuffer_load_dwordx4 v[220:223], v2, s[44:47], s9 offen offset:2048 sc1\n\ts_waitcnt vmcnt(2)\n\t11:\ns_cmp_eq_u32 s70, 5\n\ts_cbranch_scc0 406f\n\t"
            "s_branch 12f\n\t406:\nv_max3_u32 v12, v224, v226, v228\n\tv_max_u32 v12, v12, v230\n\tv_cmp_eq_u32 vcc, -1, v12\n\ts_cbranch_vccz 11f\n\t"
            "buffer_load_dwordx4 v[224:227], v2, s[44:47], s10 offen offset:1024 sc1\n\tbuffer_load_dwordx4 v[228:231], v2, s[44:47], s10 offen offset:2048 sc1\n\ts_branch 11f\n\t306:\nbuffer_load_dwordx4 v[224:227], v2, s[44:47], s10 offen offset:1024 sc1\n\tbuffer_load_dwordx4 v[228:231], v2, s[44:47], s10 offen offset:2048 sc1\n\t"
            "s_waitcnt vmcnt(2)\n\t11:\ns_cmp_eq_u32 s70, 6\n\ts_cbranch_scc0 407f\n\ts_branch 12f\n\t407:\n"
            "v_max3_u32 v12, v232, v234, v236\n\tv_max_u32 v12, v12, v238\n\tv_cmp_eq_u32 vcc, -1, v12\n\ts_cbranch_vccz 11f\n\tbuffer_load_dwordx4 v[232:235], v2, s[44:47], s11 offen offset:1024 sc1\n\tbuffer_load_dwordx4 v[236:239], v2, s[44:47], s11 offen offset:2048 sc1\n\t"
            "s_branch 11f\n\t307:\nbuffer_load_dwordx4 v[232:235], v2, s[44:47], s11 offen offset:1024 sc1\n\tbuffer_load_dwordx4 v[236:239], v2, s[44:47], s11 offen offset:2048 sc1\n\ts_waitcnt vmcnt(2)\n\t11:\n"
            "s_cmp_eq_u32 s70, 7\n\ts_cbranch_scc0 408f\n\ts_branch 12f\n\t408:\nv_max3_u32 v12, v176, v178, v180\n\tv_max_u32 v12, v12, v182\n\t"
            "v_cmp_eq_u32 vcc, -1, v12\n\ts_cbranch_vccz 11f\n\tbuffer_load_dwordx4 v[176:179], v2, s[44:47], s12 offen offset:1024 sc1\n\tbuffer_load_dwordx4 v[180:183], v2, s[44:47], s12 offen offset:2048 sc1\n\ts_branch 11f\n\t308:\n"
            "buffer_load_dwordx4 v[176:179], v2, s[44:47], s12 offen offset:1024 sc1\n\tbuffer_load_dwordx4 v[180:183], v2, s[44:47], s12 offen offset:2048 sc1\n\ts_waitcnt vmcnt(2)\n\t11:\ns_cmp_eq_u32 s70, 8\n\ts_cbranch_scc0 409f\n\t"
            "s_branch 12f\n\t409:\nv_max3_u32 v12, v184, v186, v188\n\tv_max_u32 v12, v12, v190\n\tv_cmp_eq_u32 vcc, -1, v12\n\ts_cbranch_vccz 11f\n\t"
            "buffer_load_dwordx4 v[184:187], v2, s[44:47], s13 offen offset:1024 sc1\n\tbuffer_load_dwordx4 v[188:191], v2, s[44:47], s13 offen offset:2048 sc1\n\ts_branch 11f\n\t309:\nbuffer_load_dwordx4 v[184:187], v2, s[44:47], s13 offen offset:1024 sc1\n\tbuffer_load_dwordx4 v[188:191], v2, s[44:47], s13 offen offset:2048 sc1\n\t"
            "s_waitcnt vmcnt(2)\n\t11:\ns_cmp_eq_u32 s70, 9\n\ts_cbranch_scc0 410f\n\ts_branch 12f\n\t410:\n"
            "v_max3_u32 v12, v192, v194, v196\n\tv_max_u32 v12, v12, v198\n\tv_cmp_eq_u32 vcc, -1, v12\n\ts_cbranch_vccz 11f\n\tbuffer_load_dwordx4 v[192:195], v2, s[44:47], s4 offen offset:0 sc1\n\tbuffer_load_dwordx4 v[196:199], v2, s[44:47], s4 offen offset:3072 sc1\n\t"
            "s_branch 11f\n\t310:\nbuffer_load_dwordx4 v[192:195], v2, s[44:47], s4 offen offset:0 sc1\n\tbuffer_load_dwordx4 v[196:199], v2, s[44:47], s4 offen offset:3072 sc1\n\ts_waitcnt vmcnt(2)\n\t11:\n"
            "s_cmp_eq_u32 s70, 10\n\ts_cbranch_scc0 411f\n\ts_branch 12f\n\t411:\nv_max3_u32 v12, v200, v202, v204\n\tv_max_u32 v12, v12, v206\n\t"
            "v_cmp_eq_u32 vcc, -1, v12\n\ts_cbranch_vccz 11f\n\tbuffer_load_dwordx4 v[200:203], v2, s[44:47], s5 offen offset:0 sc1\n\tbuffer_load_dwordx4 v[204:207], v2, s[44:47], s5 offen offset:3072 sc1\n\ts_branch 11f\n\t311:\n"
            "buffer_load_dwordx4 v[200:203], v2, s[44:47], s5 offen offset:0 sc1\n\tbuffer_load_dwordx4 v[204:207], v2, s[44:47], s5 offen offset:3072 sc1\n\ts_waitcnt vmcnt(2)\n\t11:\ns_cmp_eq_u32 s70, 11\n\ts_cbranch_scc0 412f\n\t"
            "s_branch 12f\n\t412:\nv_max3_u32 v12, v208, v210, v212\n\tv_max_u32 v12, v12, v214\n\tv_cmp_eq_u32 vcc, -1, v12\n\ts_cbranch_vccz 11f\n\t"
            "buffer_load_dwordx4 v[208:211], v2, s[44:47], s6 offen offset:0 sc1\n\tbuffer_load_dwordx4 v[212:215], v2, s[44:47], s6 offen offset:3072 sc1\n\ts_branch 11f\n\t312:\nbuffer_load_dwordx4 v[208:211], v2, s[44:47], s6 offen offset:0 sc1\n\tbuffer_load_dwordx4 v[212:215], v2, s[44:47], s6 offen offset:3072 sc1\n\t"
            "s_waitcnt vmcnt(2)\n\t11:\ns_cmp_eq_u32 s70, 12\n\ts_cbranch_scc0 413f\n\ts_branch 12f\n\t413:\n"
            "v_max3_u32 v12, v216, v218, v220\n\tv_max_u32 v12, v12, v222\n\tv_cmp_eq_u32 vcc, -1, v12\n\ts_cbranch_vccz 11f\n\tbuffer_load_dwordx4 v[216:219], v2, s[44:47], s7 offen offset:0 sc1\n\tbuffer_load_dwordx4 v[220:223], v2, s[44:47], s7 offen offset:3072 sc1\n\t"
            "s_branch 11f\n\t313:\nbuffer_load_dwordx4 v[216:219], v2, s[44:47], s7 offen offset:0 sc1\n\tbuffer_load_dwordx4 v[220:223], v2, s[44:47], s7 offen offset:3072 sc1\n\ts_waitcnt vmcnt(2)\n\t11:\n"
            "s_cmp_eq_u32 s70, 13\n\ts_cbranch_scc0 414f\n\ts_branch 12f\n\t414:\nv_max3_u32 v12, v224, v226, v228\n\tv_max_u32 v12, v12, v230\n\t"
            "v_cmp_eq_u32 vcc, -1, v12\n\ts_cbranch_vccz 11f\n\tbuffer_load_dwordx4 v[224:227], v2, s[44:47], s8 offen offset:0 sc1\n\tbuffer_load_dwordx4 v[228:231], v2, s[44:47], s8 offen offset:3072 sc1\n\ts_branch 11f\n\t314:\n"
            "buffer_load_dwordx4 v[224:227], v2, s[44:47], s8 offen offset:0 sc1\n\tbuffer_load_dwordx4 v[228:231], v2, s[44:47], s8 offen offset:3072 sc1\n\ts_waitcnt vmcnt(2)\n\t11:\ns_cmp_eq_u32 s70, 14\n\ts_cbranch_scc0 415f\n\t"
            "s_branch 12f\n\t415:\nv_max3_u32 v12, v232, v234, v236\n\tv_max_u32 v12, v12, v238\n\tv_cmp_eq_u32 vcc, -1, v12\n\ts_cbranch_vccz 11f\n\t"
            "buffer_load_dwordx4 v[232:235], v2, s[44:47], s9 offen offset:0 sc1\n\tbuffer_load_dwordx4 v[236:239], v2, s[44:47], s9 offen offset:3072 sc1\n\ts_branch 11f\n\t315:\nbuffer_load_dwordx4 v[232:235], v2, s[44:47], s9 offen offset:0 sc1\n\tbuffer_load_dwordx4 v[236:239], v2, s[44:47], s9 offen offset:3072 sc1\n\t"
            "s_waitcnt vmcnt(2)\n\t11:\ns_cmp_eq_u32 s70, 15\n\ts_cbranch_scc0 416f\n\ts_branch 12f\n\t416:\n"
            "v_max3_u32 v12, v176, v178, v180\n\tv_max_u32 v12, v12, v182\n\tv_cmp_eq_u32 vcc, -1, v12\n\ts_cbranch_vccz 11f\n\tbuffer_load_dwordx4 v[176:179], v2, s[44:47], s10 offen offset:0 sc1\n\tbuffer_load_dwordx4 v[180:183], v2, s[44:47], s10 offen offset:3072 sc1\n\t"
            "s_branch 11f\n\t316:\nbuffer_load_dwordx4 v[176:179], v2, s[44:47], s10 offen offset:0 sc1\n\tbuffer_load_dwordx4 v[180:183], v2, s[44:47], s10 offen offset:3072 sc1\n\ts_waitcnt vmcnt(2)\n\t11:\n"
            "s_cmp_eq_u32 s70, 16\n\ts_cbranch_scc0 417f\n\ts_branch 12f\n\t417:\nv_max3_u32 v12, v184, v186, v188\n\tv_max_u32 v12, v12, v190\n\t"
            "v_cmp_eq_u32 vcc, -1, v12\n\ts_cbranch_vccz 11f\n\tbuffer_load_dwordx4 v[184:187], v2, s[44:47], s11 offen offset:0 sc1\n\tbuffer_load_dwordx4 v[188:191], v2, s[44:47], s11 offen offset:3072 sc1\n\ts_branch 11f\n\t317:\n"
            "buffer_load_dwordx4 v[184:187], v2, s[44:47], s11 offen offset:0 sc1\n\tbuffer_load_dwordx4 v[188:191], v2, s[44:47], s11 offen offset:3072 sc1\n\ts_waitcnt vmcnt(2)\n\t11:\ns_cmp_eq_u32 s70, 17\n\ts_cbranch_scc0 418f\n\t"
            "s_branch 12f\n\t418:\nv_max3_u32 v12, v192, v194, v196\n\tv_max_u32 v12, v12, v198\n\tv_cmp_eq_u32 vcc, -1, v12\n\ts_cbranch_vccz 11f\n\t"
            "buffer_load_dwordx4 v[192:195], v2, s[44:47], s12 offen offset:0 sc1\n\tbuffer_load_dwordx4 v[196:199], v2, s[44:47], s12 offen offset:3072 sc1\n\ts_branch 11f\n\t318:\nbuffer_load_dwordx4 v[192:195], v2, s[44:47], s12 offen offset:0 sc1\n\tbuffer_load_dwordx4 v[196:199], v2, s[44:47], s12 offen offset:3072 sc1\n\t"
            "s_waitcnt vmcnt(2)\n\t11:\ns_cmp_eq_u32 s70, 18\n\ts_cbranch_scc0 419f\n\ts_branch 12f\n\t419:\n"
            "v_max3_u32 v12, v200, v202, v204\n\tv_max_u32 v12, v12, v206\n\tv_cmp_eq_u32 vcc, -1, v12\n\ts_cbranch_vccz 11f\n\tbuffer_load_dwordx4 v[200:203], v2, s[44:47], s13 offen offset:0 sc1\n\tbuffer_load_dwordx4 v[204:207], v2, s[44:47], s13 offen offset:3072 sc1\n\t"
            "s_branch 11f\n\t319:\nbuffer_load_dwordx4 v[200:203], v2, s[44:47], s13 offen offset:0 sc1\n\tbuffer_load_dwordx4 v[204:207], v2, s[44:47], s13 offen offset:3072 sc1\n\ts_waitcnt vmcnt(2)\n\t11:\n"
            "12:\n"
            RINC
            "s_sub_u32 s68, s68, 1\n\ts_cbranch_scc1 9b\n\ts_waitcnt vmcnt(0)\n\ts_setpc_b64 s[72:73]\n\t.subsection 1\n\t.Lce:\n"
            ".subsection 0\n\t.Lout:\ns_waitcnt lgkmcnt(0)\n\t"
            : "=&{s67}"(tag), "=&{v[52:83]}"(sa), "=&{v[84:115]}"(sb), "=&{v[116:123]}"(sc) : [t] "v"(tid), [b] "s"(bid), [k] "s"(ka)
            : "memory", "scc", "vcc", "s4", "s5", "s6", "s7", "s8", "s9", "s10", "s11", "s12", "s13", "s14", "s15", "s16", "s17", "s18", "s19", "s20", "s21", "s22", "s23", "s24", "s25", "s26", "s27", "s28", "s29", "s30", "s31", "s32", "s33", "s34", "s35", "s36", "s37", "s38", "s39", "s40", "s41", "s42", "s43", "s44", "s45", "s46", "s47", "s48", "s49", "s50", "s51", "s52", "s53", "s54", "s55", "s56", "s57", "s58", "s59", "s60", "s61", "s62", "s63", "s64", "s65", "s66", "s68", "s69", "s70", "s71", "s72", "s73", "s74", "s75", "s76", "s77", "s78", "s79", "s80", "s81", "s82", "s83", "s84", "s85", "s86", "s87", "s88", "s89", "s90", "s91", "s92", "s93", "s94", "s95", "s96", "s97", "s98", "s99", "s100", "s101", "v2", "v3", "v4", "v5", "v6", "v7", "v8", "v9", "v10", "v11", "v12", "v13", "v14", "v15", "v16", "v17", "v18", "v19", "v20", "v21", "v22", "v23", "v24", "v25", "v26", "v27", "v28", "v29", "v30", "v31", "v32", "v33", "v34", "v35", "v36", "v37", "v38", "v39", "v40", "v41", "v42", "v43", "v44", "v45", "v46", "v47", "v48", "v49", "v50", "v51", "v124", "v125", "v126", "v127", "v128", "v129", "v130", "v131", "v132", "v133", "v134", "v135", "v136", "v137", "v138", "v139", "v140", "v141", "v142", "v143", "v144", "v145", "v146", "v147", "v148", "v149", "v150", "v151", "v152", "v153", "v154", "v155", "v156", "v157", "v158", "v159", "v160", "v161", "v162", "v163", "v164", "v165", "v166", "v167", "v168", "v169", "v170", "v171", "v172", "v173", "v174", "v175", "v176", "v177", "v178", "v179", "v180", "v181", "v182", "v183", "v184", "v185", "v186", "v187", "v188", "v189", "v190", "v191", "v192", "v193", "v194", "v195", "v196", "v197", "v198", "v199", "v200", "v201", "v202", "v203", "v204", "v205", "v206", "v207", "v208", "v209", "v210", "v211", "v212", "v213", "v214", "v215", "v216", "v217", "v218", "v219", "v220", "v221", "v222", "v223", "v224", "v225", "v226", "v227", "v228", "v229", "v230", "v231", "v232", "v233", "v234", "v235", "v236", "v237", "v238", "v239", "v240", "v241", "v242", "v243", "v244", "v245", "v246", "v247", "v248", "v249", "v250", "v251", "v252", "v253", "v254", "v255");
    tag = RFL(tag);
    asm volatile("" : "+s"(bid), "+s"(ka)); asm volatile("" : "+v"(tid));
    const u16 *x = (const u16*)ka[0], *wo = (const u16*)ka[2], *w_r = (const u16*)ka[3], *wg = (const u16*)ka[4], *wu = (const u16*)ka[5], *wd = (const u16*)ka[6]; char* ws = (char*)ka[7];
    const int wv = __builtin_amdgcn_readfirstlane(tid >> 6);
    const unsigned lds0 = (unsigned)(size_t)(lchar*)smem;
#define LANE() const int lane = lane_id(); const int g = lane >> 4, li = lane & 15, jq = 4 * (li & 3) + (li >> 2); (void)jq
    const int xcd = bid & 7, j = ((bid >> 3) + 25) & 31;
    const bool gemm = j < 25;
    const int fp = xcd >> 1, bl = (j * 13) >> 6, cl = j - bl * 5;
    const int rb = fp * 5 + bl, cg = (xcd & 1) * 5 + cl;
    const int par = fp >> 1, I0 = ((fp & 1) * 5 + bl) * 104 - 8;
    const __amdgpu_buffer_rsrc_t rs = __builtin_amdgcn_make_buffer_rsrc((void*)ws, 0, W_END, 0x00020000);
    auto wait_flag = [&](unsigned a) { SPIN { if (RFL(ldsrv<unsigned>(a))) break; __builtin_amdgcn_s_sleep(1); } MEMB(); };
    auto all4 = [&](u32x4 c) { return RFL(min(min(c.x, c.y), min(c.z, c.w))); };
    unsigned ep = 0;
    auto epld = [&]() { asm volatile("s_load_dword %0, %1, 0x0" : "=s"(ep) : "s"(ws) : "memory"); };
    auto mktag = [&]() { asm volatile("s_waitcnt lgkmcnt(0)" : "+s"(ep) :: "memory"); tag = ep * 1664525u + 1013904223u; };
        if (gemm) {
            LANE();
            if (RP0) __builtin_amdgcn_s_setprio(RP0);
            wait_flag(lds0 + FL_W);
            const u32x4 kk = ldsrv<u32x4>(lds0 + L_KEY + lane * 16);
            const uint2 t2 = top2wave(lane < 52 ? kk.x : 0u, lane < 52 ? kk.z : 0u);
            bool first = true;
            _Pragma("nounroll") for (int k = 0; k < 2; ++k) {
                const unsigned key = k ? t2.y : t2.x, rr = (0xffffu - (key & 0xffffu)) & 2047u; const int pos = (int)(rr >> 1) - I0;
                if (__builtin_expect((int)(rr & 1) == par && pos >= 8 && pos < 112, 0)) {
                    const float sg = rbf(__builtin_amdgcn_rcpf(1.f + __builtin_amdgcn_exp2f(keyscore(key) * -1.4426950408889634f)));
                    const int m = (pos & ~15) + 4 * (pos & 3) + ((pos >> 2) & 3);
                    const float xr_ = lo2f(x[rr * 640 + cg * 64 + lane]), nm = ldsrv<float>(lds0 + L_NM + m * 4), rsj = ldsrv<float>(lds0 + L_RS + pos * 4);
                    u32x2 y[5];
                    if (RP1) __builtin_amdgcn_s_setprio(RP1);
                    float xn = rbf(rbf(xr_ + nm) * rsj), sg_ = sg; asm volatile("" : "+v"(xn), "+v"(sg_));
                    if (YSL && first) { __builtin_amdgcn_s_sleep(YSL); first = false; }
                    SPIN {
                        MEMB();
                        bool old = false;
#pragma unroll
                        for (int i = 0; i < 5; ++i) y[i] = __builtin_amdgcn_raw_buffer_load_b64(rs, W_Y + (cg * 64 + lane) * 16 + k * 8, i * 10240, SC1);
#pragma unroll
                        for (int i = 0; i < 5; ++i) old = old || y[i].y != tag;
                        if (__builtin_amdgcn_ballot_w64(old) == 0) break;
                        __builtin_amdgcn_s_sleep(YS2);
                    }
                    const float ys = ((__uint_as_float(y[0].x) + __uint_as_float(y[1].x)) + (__uint_as_float(y[2].x) + __uint_as_float(y[3].x))) + __uint_as_float(y[4].x);
                    const float u = rbf(rbf(ys) * sg_);
                    ldsw<float>(lds0 + L_RG + k * 256 + lane * 4, silu_b(rbf(xn + u)) * -1.4426950408889634f);
                    ldsw<unsigned>(lds0 + L_RM + m * 4, L_RG + k * 256);
                }
            }
            ldsw<unsigned>(lds0 + FL_R, 1u);
            END();
        }
    {
        TL(0);
        LANE();
        const int fq = (j - 25) * 2 + (xcd & 1), f = fp * 14 + fq;
        const int n0 = fq * 40 + wv * 4 + g;
        epld();
        if (wv == 7 && lane < 16) ldsw<u32x4>(lds0 + MISC + lane * 16, (u32x4){0u, 0u, 0u, 0u});
        const int u2 = max(f - 6, 0), kr = (u2 * 26) >> 8, fc = u2 - kr * 10, n1 = wv < 4 ? 3 : 2;
        const int d0 = kr * 11 + wv, d1 = kr * 11 + 4 + wv; const bool two = wv >= 4 && wv < 7 && d1 < 54;
        const char* w1 = (const char*)(f & 1 ? wu : wg) + min(f >> 1, 26) * 128; const char* w2 = (const char*)wd + fc * 128;
        unsigned fv[2][4];
#pragma unroll
        for (int i = 0; i < 4; ++i) { const unsigned c = (((lane & 7) ^ (2 * ((lane >> 4) & 1) + 4 * (i & 1))) * 16) - i * 1024; fv[0][i] = ((lane >> 3) + i * 8) * (F_ * 2) + c; fv[1][i] = ((lane >> 3) + i * 8) * 1280 + c; }
        auto issue = [&](int m, int ks, int slot) {
            asm volatile("s_mov_b32 m0, %[l]\n\ts_nop 0\n\tglobal_load_lds_dwordx4 %0, %[b]" FPOL "\n\tglobal_load_lds_dwordx4 %1, %[b] offset:1024" FPOL "\n\tglobal_load_lds_dwordx4 %2, %[b] offset:2048" FPOL "\n\tglobal_load_lds_dwordx4 %3, %[b] offset:3072" FPOL
                         :: "v"(fv[m][0]), "v"(fv[m][1]), "v"(fv[m][2]), "v"(fv[m][3]), [b] "s"((m ? w2 : w1) + (unsigned)(ks * 32 * (m ? 1280 : F_ * 2))), [l] "s"(lds0 + wv * 16384 + slot * 4096) : "memory");
        };
        if (TCH && wv == 4) {
            const int l = min(f * 14 + lane, 770), o = l < 280 ? W_KEY + l * 128 : l < 536 ? W_ST + (l - 280) * 128 : W_Y + (l - 536) * 256;
            if (lane < 14) asm volatile("s_mov_b32 m0, %[l]\n\ts_nop 0\n\tbuffer_load_dword %0, %1, 0 offen lds" :: "v"(o), "s"(rs), [l] "s"(lds0 + L_DUM) : "memory");
        }
        if (QPF && wv >= 8 - QPF) {
            const int i = min(f * 172 + (7 - wv) * 64 + lane, 9599), rw = (i * 34953) >> 20;
            asm volatile("s_mov_b32 m0, %[l]\n\ts_nop 0\n\tglobal_load_lds_dword %0, %[b]" :: "v"(i * 128 + ((rw >> 6) + 1) * 245760), [b] "s"(ka[1]), [l] "s"(lds0 + L_DUM) : "memory");
        }
        BARN();
        auto wopf = [&]() {
            const int i = min(((j - 25) * 8 + wv) * 58 + lane, 3199), rw = (i * 13108) >> 16;
            asm volatile("s_mov_b32 m0, %[l]\n\ts_nop 0\n\tglobal_load_lds_dword %0, %[b]" :: "v"(rw * 640 + i * 128 + (xcd & 1) * 640), [b] "s"(wo), [l] "s"(lds0 + L_DUM) : "memory"); };
        if (WPF == 1) wopf();
        if (EARLYK) issue(0, wv, 0);
        asm volatile("s_waitcnt vmcnt(%2)" : "+{v[84:115]}"(sb), "+{v[116:123]}"(sc) : "n"(5 + 4 * EARLYK + (WPF == 1)) : "memory");
#define SXR(i) ((i) < 20 ? sa[(12 + (i)) & 31] : (i) < 52 ? sb[((i) - 20) & 31] : sc[((i) - 52) & 7])
        mktag();
        {
            const unsigned one2 = 0x3F803F80u;
            float wsum = 0.f;
#pragma unroll
            for (int e = 0; e < 20; ++e) wsum = dot2(SXR(40 + e), one2, wsum);
            wsum = sum16(wsum);
            auto stat = [&](int p) {
                float s1 = 0.f, s2 = 0.f, sr = 0.f;
#pragma unroll
                for (int e = 0; e < 20; ++e) { const unsigned u = SXR(20 * p + e); s1 = dot2(u, one2, s1); s2 = dot2(u, u, s2); sr = dot2(u, SXR(40 + e), sr); }
                const float mean = sum16(s1) * (1.f / 640.f);
                const unsigned mb2 = cvtpk(mean, mean); const float mb = lo2f(mb2);
                const float q = fmaf(40.f * mb, mb, fmaf(-2.f * mb, s1, s2));
                const float dm = mean - mb;
                const float var = fmaxf(sum16(q) * (1.f / 640.f) - dm * dm, 0.f);
                const float sd = rbf(__builtin_amdgcn_sqrtf(rbf(rbf(var) + 1e-5f)));
                const float rsc = rbf(fmaf(-mb, wsum, sum16(sr)) / sd);
                const int n = n0 + 32 * p, i = (fp & 1) * 512 + n;
                if (li == 0) {
                    asm volatile("buffer_store_dwordx4 %0, %1, %2, 0 offen sc1\n\ts_nop 1" :: "v"((u32x4){(mb2 << 16) ^ 0x80000000u, tag, __float_as_uint(__builtin_amdgcn_rcpf(sd)), tag}), "v"(n < 512 ? W_ST + (par * 1024 + i) * 16 : 0x40000000), "s"(rs) : "memory");
                    ldsw<unsigned>(lds0 + LS_ROW + (p * 32 + wv * 4 + g) * 4, n < 512 ? rkey(rsc, 2 * i + par) : 0u);
                }
            };
            asm volatile("" : "+v"(wsum));
            if (wv < 2) stat(1);
            asm volatile("s_bitcmp1_b32 " QW ", %2\n\ts_cbranch_scc1 1f\n\ts_waitcnt vmcnt(%1)\n1:\n\ts_waitcnt vmcnt(%1 + 1)" : "+{v[52:83]}"(sa) : "n"(4 * EARLYK + (WPF == 1)), "s"(wv) : "memory", "scc");
            TL(1);
            if (EK2 && wv < EK2) issue(0, wv, 0);
            stat(0);
            if (E2 && wv < 7) issue(0, wv + 8, 1);
        }
        BAR();
        if (wv == 6) asm volatile("buffer_store_dword %0, %1, %2, 0 offen sc1" :: "v"(tag), "v"(lane ? 0x40000000 : W_ACK + (f < 40 ? (f >> 1) * 128 + 40 + (f & 1) * 4 : 2560 + (f - 40) * 4)), "s"(rs) : "memory");
        if (wv == 7) {
            const unsigned k = ldsrv<unsigned>(lds0 + LS_ROW + min(lane, 39) * 4);
            const uint2 t2 = top2wave(lane < 40 ? k : 0u, 0u);
            if (lane == 0 && fq < 13) asm volatile("buffer_store_dwordx4 %0, %1, %2, 0 offen sc1\n\ts_nop 1" :: "v"((u32x4){t2.x, tag, t2.y, tag}), "v"(W_KEY + (fp * 13 + fq) * 16), "s"(rs) : "memory");
        }
        BARN();
        TL(2);
        if (WPF == 2) wopf();
        if (!EARLYK && wv >= EK2) issue(0, wv, 0);
        if (!E2 || wv == 7) issue(0, wv + 8, 1);
        auto w3 = [&]() {
        if (wv < 4) issue(0, wv + 16, 2);
        else if (!WPF && (!KE || wv < 7)) asm volatile("s_mov_b32 m0, %[l]\n\ts_mov_b32 exec_hi, " PFH "\n\tglobal_load_lds_dword %0, %[b]\n\ts_mov_b32 exec_hi, -1" :: "v"(min(f * 128 + (wv - 4) * (KE ? 43 : 32) + lane, 6399) * 128), [b] "s"(wo), [l] "s"(lds0 + L_DUM) : "memory");
        };
        if (!KE) w3();
        if (wv >= 4 && wv < 4 + P2A) issue(1, d0, 3);
        u32x4 kk;
        BARN();
        asm volatile("s_cmp_lg_u32 %3, 7\n\ts_cbranch_scc1 1f\n\ts_sleep " KSL "\n\tbuffer_load_dwordx4 %0, %1, %2, 0 offen sc1\n1:\n\ts_barrier" : "=&v"(kk) : "v"(W_KEY + min(lane, 51) * 16), "s"(rs), "s"(wv) : "memory", "scc");
        if (KE) w3();
        if (!KE && wv != 7) issue(1, d0, 3);
        if (P2A && two) issue(1, d1, 2);
        asm volatile("s_cmp_lg_u32 %1, 7\n\ts_cbranch_scc1 1f\n\ts_waitcnt vmcnt(0)\n1:" : "+v"(kk) : "s"(wv) : "memory", "scc");
        if (wv == 7) {
            SPIN {
                if (__builtin_amdgcn_ballot_w64(kk.y != tag || kk.w != tag) == 0) break;
                asm volatile("buffer_load_dwordx4 %0, %1, %2, 0 offen sc1\n\ts_waitcnt vmcnt(0)" : "=&v"(kk) : "v"(W_KEY + min(lane, 51) * 16), "s"(rs) : "memory");
            }
            const uint2 t2 = top2wave(lane < 52 ? kk.x : 0u, lane < 52 ? kk.z : 0u);
            if (lane < 2) ldsw<unsigned>(lds0 + ROUT + lane * 8, (0xffffu - ((lane ? t2.y : t2.x) & 0xffffu)) & 2047u);
        }
        BAR();
        TL(3);
        const unsigned row = ldsrv<unsigned>(lds0 + ROUT + (li & 1) * 8);
        u32x4 af[3], st;
        asm volatile("global_load_dwordx4 %0, %4, %5\n\tglobal_load_dwordx4 %1, %4, %5 offset:512\n\tglobal_load_dwordx4 %2, %6, %5 offset:1024\n\tbuffer_load_dwordx4 %3, %7, %8, 0 offen sc1"
                     : "=&v"(af[0]), "=&v"(af[1]), "=&v"(af[2]), "=&v"(st) : "v"(row * 1280u + wv * 64 + g * 16), "s"(x), "v"(row * 1280u + (wv & 3) * 64 + g * 16), "v"(wv ? 0x40000000 : W_ST + ((row & 1) * 1024 + (row >> 1)) * 16), "s"(rs) : "memory");
        bf16x8 wf[3][4];
        unsigned ra[4];
#pragma unroll
        for (int q = 0; q < 4; ++q) ra[q] = lds0 + wv * 16384 + (unsigned)((8 * g + (li >> 2)) * 128 + (((q ^ (((li >> 3) & 1) + 2 * (g & 1))) * 32) + (li & 3) * 8));
        auto rdf = [&](int k, int slot) {
#pragma unroll
            for (int q = 0; q < 4; ++q) wf[k][q] = cat8(trread(ra[q] + slot * 4096), trread(ra[q] + slot * 4096 + 512));
        };
        if (KE) { BARN(); if (wv) issue(1, d0, 3); if (wv) wvm<8>(); else wvm<4>(); }
        else {
        if (wv == 7) issue(1, d0, 3);
        if (two) issue(1, d1, 2);
        if (two) wvm<12>(); else wvm<8>(); }
        rdf(0, 0); rdf(1, 1); if (wv < 4) rdf(2, 2);
        asm volatile("s_cmp_eq_u32 %4, 0\n\ts_cbranch_scc0 1f\n\ts_waitcnt vmcnt(0)\n1:\n\ts_waitcnt vmcnt(4)" : "+v"(af[0]), "+v"(af[1]), "+v"(af[2]), "+v"(st) : "s"(KE ? wv : RFL(two || wv == 7)) : "memory", "scc");
        f32x4 c[4];
#pragma unroll
        for (int q = 0; q < 4; ++q) c[q] = (f32x4){0.f, 0.f, 0.f, 0.f};
#pragma unroll
        for (int k = 0; k < 3; ++k) if (k < n1) {
#pragma unroll
            for (int e = 0; e < 4; ++e) af[k][e] = li & 2 ? 0x3F803F80u : af[k][e];
#pragma unroll
            for (int q = 0; q < 4; ++q) c[q] = mfma32(__builtin_bit_cast(bf16x8, af[k]), wf[k][q], c[q]);
        }
        auto total = [&](int off) {
            ldsw<f32x4>(lds0 + wv * 16384 + off + lane * 16, g == 0 ? c[0] : g == 1 ? c[1] : g == 2 ? c[2] : c[3]);
            BAR();
            if (wv) return c[0];
            f32x4 pr[8];
#pragma unroll
            for (int w = 0; w < 8; ++w) pr[w] = ldsrv<f32x4>(lds0 + w * 16384 + off + lane * 16);
            return ((pr[0] + pr[1]) + (pr[2] + pr[3])) + ((pr[4] + pr[5]) + (pr[6] + pr[7]));
        };
        TL(4);
        {
            const f32x4 s = total(0);
            if (wv == 0 && f < 54) {
                SPIN {
                    if (__builtin_amdgcn_ballot_w64(st.y != tag || st.w != tag) == 0) break;
                    asm volatile("buffer_load_dwordx4 %0, %1, %2, 0 offen sc1\n\ts_waitcnt vmcnt(0)" : "=&v"(st) : "v"(W_ST + ((row & 1) * 1024 + (row >> 1)) * 16), "s"(rs) : "memory");
                }
                const float n0_ = __uint_as_float(__builtin_amdgcn_readlane(st.x, 0)), n1_ = __uint_as_float(__builtin_amdgcn_readlane(st.x, 1)), i0 = __uint_as_float(__builtin_amdgcn_readlane(st.z, 0)), i1 = __uint_as_float(__builtin_amdgcn_readlane(st.z, 1));
                float v0 = rbf(fmaf(n0_, s.z, s.x) * i0), v1 = rbf(fmaf(n1_, s.z, s.y) * i1);
                if (!(f & 1)) { v0 = silu_b(v0); v1 = silu_b(v1); }
                const unsigned hb = cvtpk(v0, v1), nb = dppu<0xB1>(hb);
                const unsigned pay = (lane & 1) ? __builtin_amdgcn_perm(hb, nb, 0x07060302u) : __builtin_amdgcn_perm(nb, hb, 0x05040100u);
                asm volatile("buffer_store_dwordx2 %0, %1, %2, 0 offen sc1" :: "v"((u32x2){pay, tag}), "v"(W_H + ((f & 1) * 2 + (lane & 1)) * 6912 + ((f >> 1) * 32 + (lane >> 1)) * 8), "s"(rs) : "memory");
            }
        }
        TL(5);
        if (KE) { if (two) issue(1, d1, 2); if (wv == 0) issue(1, d0, 3); }
        if (f == 0 && wv == 1) {
            SPIN {
                u32x4 a_;
                asm volatile("buffer_load_dwordx4 %0, %1, %2, 0 offen sc1\n\ts_waitcnt vmcnt(0)" : "=&v"(a_) : "v"(W_ACK + (lane < 60 ? ((lane * 43) >> 7) * 80 + lane * 16 : 1600 + lane * 16)), "s"(rs) : "memory");
                if (__builtin_amdgcn_ballot_w64(a_.x != tag || a_.y != tag || a_.z != tag || a_.w != tag) == 0) break;
                __builtin_amdgcn_s_sleep(8);
            }
            if (lane == 0) __builtin_amdgcn_raw_buffer_store_b32(tag, rs, W_EP, 0, SC1);
        }
        if (f >= 6) {
            wvm<0>();
            rdf(0, 3); rdf(1, 2);
            const int ho = W_H + ((lane >> 4) & 1) * 6912 + ((lane >> 5 && two ? d1 : d0) * 16 + li) * 8;
            u32x2 hg, hu;
            __builtin_amdgcn_s_sleep(HSL);
            SPIN {
                asm volatile("buffer_load_dwordx2 %0, %2, %3, 0 offen sc1\n\tbuffer_load_dwordx2 %1, %2, %3, %4 offen sc1\n\ts_waitcnt vmcnt(0)" : "=&v"(hg), "=&v"(hu) : "v"(ho), "s"(rs), "s"(13824) : "memory");
                if (__builtin_amdgcn_ballot_w64(hg.y != tag || hu.y != tag) == 0) break;
                __builtin_amdgcn_s_sleep(HS2);
            }
            ldsw<unsigned>(lds0 + wv * 16384 + 4096 + lane * 4, cvtpk(lo2f(hg.x) * lo2f(hu.x), hi2f(hg.x) * hi2f(hu.x)));
#pragma unroll
            for (int q = 0; q < 4; ++q) c[q] = (f32x4){0.f, 0.f, 0.f, 0.f};
#pragma unroll
            for (int k = 0; k < 2; ++k) if (k == 0 || two) {
                const u32x4 h = ldsrv<u32x4>(lds0 + wv * 16384 + 4096 + k * 128 + (li & 1) * 64 + g * 16);
#pragma unroll
                for (int q = 0; q < 4; ++q) c[q] = mfma32(__builtin_bit_cast(bf16x8, h), wf[k][q], c[q]);
            }
            const f32x4 ys = total(1024);
            if (wv == 0) __builtin_amdgcn_raw_buffer_store_b128((u32x4){__float_as_uint(ys.x), tag, __float_as_uint(ys.y), tag}, rs, W_Y + kr * 10240 + (fc * 64 + lane) * 16, 0, SC1);
        }
        TL(6);
        TLEND();
        wvm<0>();
    }
}

extern "C" void kernel_launch(void* const* d_in, const int* in_sizes, int n_in, void* d_out, int out_size, void* d_ws,
                              size_t ws_size, hipStream_t stream) {
    (void)hipFuncSetAttribute((const void*)fused, hipFuncAttributeMaxDynamicSharedMemorySize, LDSZ);
    fused<<<256, 512, LDSZ, stream>>>((const u16*)d_in[0], (const u16*)d_in[1], (const u16*)d_in[2], (const u16*)d_in[4],
                                      (const u16*)d_in[5], (const u16*)d_in[6], (const u16*)d_in[7], (char*)d_ws, (u16*)d_out);
}
```

```cpp
#include <hip/hip_runtime.h>
#include <type_traits>

typedef __attribute__((ext_vector_type(8))) short bf16x8;
typedef __attribute__((ext_vector_type(4))) short bf16x4;
typedef __attribute__((ext_vector_type(4))) float f32x4;
typedef __attribute__((ext_vector_type(2))) float f32x2;
typedef __attribute__((ext_vector_type(4))) unsigned u32x4;
typedef __attribute__((ext_vector_type(2))) unsigned u32x2;
typedef __attribute__((ext_vector_type(2))) __bf16 bf16x2v;
typedef __attribute__((ext_vector_type(8))) unsigned u32x8;
typedef __attribute__((ext_vector_type(16))) unsigned u32x16;
typedef unsigned short u16;
typedef __attribute__((address_space(3))) char lchar;

#define F_ 1728
constexpr int W_EP = 0;
constexpr int W_KEY = 4096;
constexpr int W_ACK = 8192;
constexpr int W_H = 12288;
constexpr int W_ST = 65536;
constexpr int W_Y = 98304;
constexpr int W_O = 155648;
constexpr int W_END = W_O + 80 * 40960;
#define SENT 0xffffffffu
constexpr int XS = 14336, SLOT = XS + 24576;
constexpr int WO0 = 2 * SLOT;
constexpr int MISC = WO0 + 81920;
constexpr int FL_S = MISC, FL_P = MISC + 16, FL_E = MISC + 32, FL_W = MISC + 48, FL_R = MISC + 64, FL_AUX = MISC + 80, ONES = MISC + 128, ROUT = MISC + 144, LTAG = MISC + 160;
constexpr int L_NM = MISC + 256, L_RS = L_NM + 512 + 64, L_RM = L_RS + 512, L_RG = L_RM + 512, L_DUM = L_RG + 512, L_KEY = L_DUM + 512;
constexpr int LS_ROW = L_NM;
constexpr int LDSZ = 163840;
static_assert(L_KEY + 1024 <= LDSZ, "lds");
#define SPIN_MAX (1 << 21)
#define SPIN _Pragma("nounroll") for (int it = 0; it < SPIN_MAX; ++it)

__device__ __forceinline__ unsigned cvtpk(float lo, float hi) { return __builtin_bit_cast(unsigned, __builtin_convertvector((f32x2){lo, hi}, bf16x2v)); }
__device__ __forceinline__ unsigned nosent(unsigned u) { return u < 0xfffffffeu ? u : 0xfffffffeu; }
__device__ __forceinline__ float lo2f(unsigned u) { return __uint_as_float(u << 16); }
__device__ __forceinline__ float hi2f(unsigned u) { return __uint_as_float(u & 0xffff0000u); }
__device__ __forceinline__ float rbf(float f) { return lo2f(cvtpk(f, f)); }
template <int CTRL> __device__ __forceinline__ float dppf(float x) { return __builtin_bit_cast(float, __builtin_amdgcn_mov_dpp(__builtin_bit_cast(int, x), CTRL, 0xf, 0xf, true)); }
template <int CTRL> __device__ __forceinline__ unsigned dppu(unsigned x) { return (unsigned)__builtin_amdgcn_mov_dpp((int)x, CTRL, 0xf, 0xf, true); }
__device__ __forceinline__ void sw16(unsigned& a, unsigned& b) { const auto r = __builtin_amdgcn_permlane16_swap(a, b, false, false); a = r[0]; b = r[1]; }
__device__ __forceinline__ void sw32(unsigned& a, unsigned& b) { const auto r = __builtin_amdgcn_permlane32_swap(a, b, false, false); a = r[0]; b = r[1]; }
__device__ __forceinline__ void sw16f(float& a, float& b) { unsigned x = __float_as_uint(a), y = __float_as_uint(b); sw16(x, y); a = __uint_as_float(x); b = __uint_as_float(y); }
__device__ __forceinline__ void sw32f(float& a, float& b) { unsigned x = __float_as_uint(a), y = __float_as_uint(b); sw32(x, y); a = __uint_as_float(x); b = __uint_as_float(y); }
__device__ __forceinline__ float vmax(float a, float b) { return __builtin_elementwise_maximum(a, b); }
__device__ __forceinline__ float sum16(float x) { x += dppf<0xB1>(x); x += dppf<0x4E>(x); x += dppf<0x141>(x); x += dppf<0x140>(x); return x; }
__device__ __forceinline__ float dot2(unsigned a, unsigned b, float c) { return __builtin_amdgcn_fdot2_f32_bf16(__builtin_bit_cast(bf16x2v, a), __builtin_bit_cast(bf16x2v, b), c, false); }
__device__ __forceinline__ float silu_b(float v) { return v * __builtin_amdgcn_rcpf(__builtin_amdgcn_exp2f(v * -1.4426950408889634f) + 1.f); }
__device__ __forceinline__ unsigned rkey(float rb, int row) {
    unsigned b = __float_as_uint(rb) >> 16; b = (b & 0x8000u) ? (~b & 0xffffu) : (b | 0x8000u);
    return (b << 16) | (unsigned)(0xffff - row);
}
__device__ __forceinline__ float keyscore(unsigned k) { const unsigned b = k >> 16; return __uint_as_float(((b & 0x8000u) ? (b & 0x7fffu) : (~b & 0xffffu)) << 16); }
#define T2M(b0, b1) { const unsigned _b0 = (b0), _b1 = (b1); const unsigned _m = min(k0, _b0); k0 = max(k0, _b0); k1 = max(_m, max(k1, _b1)); }
__device__ __forceinline__ uint2 top2wave(unsigned k0, unsigned k1) {
    T2M(dppu<0xB1>(k0), dppu<0xB1>(k1)); T2M(dppu<0x4E>(k0), dppu<0x4E>(k1));
    T2M(dppu<0x141>(k0), dppu<0x141>(k1)); T2M(dppu<0x140>(k0), dppu<0x140>(k1));
    const unsigned a0 = __builtin_amdgcn_readlane(k0, 0), a1 = __builtin_amdgcn_readlane(k1, 0);
    const unsigned c0 = __builtin_amdgcn_readlane(k0, 16), c1 = __builtin_amdgcn_readlane(k1, 16);
    const unsigned d0 = __builtin_amdgcn_readlane(k0, 32), d1 = __builtin_amdgcn_readlane(k1, 32);
    const unsigned e0 = __builtin_amdgcn_readlane(k0, 48), e1 = __builtin_amdgcn_readlane(k1, 48);
    k0 = a0; k1 = a1; T2M(c0, c1); T2M(d0, d1); T2M(e0, e1);
    return make_uint2(k0, k1);
}
template <int N> __device__ __forceinline__ void wvm() { asm volatile("" ::: "memory"); __builtin_amdgcn_s_waitcnt((N & 15) | ((N >> 4) << 14) | 0x0F70); asm volatile("" ::: "memory"); }
__device__ __forceinline__ bf16x4 trread(unsigned a) { return __builtin_amdgcn_ds_read_tr16_b64_v4i16((__attribute__((address_space(3))) bf16x4*)(size_t)a); }
template <class T> __device__ __forceinline__ T ldsr(unsigned a) { return *(const __attribute__((address_space(3))) T*)(size_t)a; }
template <class T> __device__ __forceinline__ T ldsrv(unsigned a) { return *(volatile __attribute__((address_space(3))) T*)(size_t)a; }
template <class T> __device__ __forceinline__ void ldsw(unsigned a, T v) { *(volatile __attribute__((address_space(3))) T*)(size_t)a = v; }
__device__ __forceinline__ bf16x8 cat8(bf16x4 lo, bf16x4 hi) { return (bf16x8){lo[0], lo[1], lo[2], lo[3], hi[0], hi[1], hi[2], hi[3]}; }
__device__ __forceinline__ bf16x8 frag4(unsigned a, unsigned b, unsigned c, unsigned d) { return __builtin_bit_cast(bf16x8, (u32x4){a, b, c, d}); }
__device__ __forceinline__ f32x4 mfma32(bf16x8 a, bf16x8 b, f32x4 c) { return __builtin_amdgcn_mfma_f32_16x16x32_bf16(a, b, c, 0, 0, 0); }
__device__ __forceinline__ int lane_id() { int l; asm volatile("v_mbcnt_lo_u32_b32 %0, -1, 0\n\tv_mbcnt_hi_u32_b32 %0, -1, %0" : "=v"(l)); __builtin_assume(l >= 0 && l < 64); return l; }
#define SB() __builtin_amdgcn_sched_barrier(0)
#define BAR() asm volatile("s_waitcnt lgkmcnt(0)\n\ts_barrier" ::: "memory")
#define MEMB() asm volatile("" ::: "memory")
#define RFL(x) ((unsigned)__builtin_amdgcn_readfirstlane((int)(x)))
#define SC1 16

#define WOPF 1
#define DSEL "0"
#define TRG1D
#define TRG2D
#define PRIA "s_setprio 2\n\t"
#define TCHD
#define RSLP "s_sleep 3\n\t"
#define PRIR "s_setprio 0\n\t"
#define PRI1 "s_setprio 3\n\t"
#define PRIW
#define PRIX "s_setprio 3\n\t"
#define PRIO
#define RSL0 "s_sleep 6\n\t"
#define RSL1 "s_sleep 3\n\t"
#define ASLP "s_sleep 1\n\t"
#define PRIS
#define PRIG "s_setprio 0\n\t"
#define PSLP "s_sleep 1\n\t"
#define HSL 26
#define HS2 10
#define TCH 1
#define KSL "0"
#define E2 0
#define P2A 0
#define FPOL " nt"
#define EK2 5
#define YS2 0
#define QPF 3
#define QW "0xf3"
#define YSL 18
#define KE 0
#define PFH "0"
#define RP0 0
#define RP1 3
#define HF 0
#define QH 1
#define WH 1
#define Q2 0
#define NOR 0
#define WPF 0
#define EARLYK 0
#define NOPK(v) asm("" : "+v"(v))
#define OPQ(v) asm volatile("" : "+v"(v))
#define IB(m) __builtin_amdgcn_inverse_ballot_w64(m)
#define END() do { asm volatile("s_endpgm"); __builtin_amdgcn_s_waitcnt(0); return; } while (0)
#define BARN() asm volatile("s_barrier" ::: "memory")
#define TL(i)
#define TA(i)
#define TLEND()
__device__ __forceinline__ constexpr int phys(int S) { return (S + 1) % 3; }

__global__ __launch_bounds__(512, 2) void fused(const u16* x_, const u16* wqkv_, const u16* wo_, const u16* w_r_, const u16* wg_, const u16* wu_, const u16* wd_, char* ws_, u16* out_) {
    extern __shared__ __attribute__((aligned(16))) char smem[];
#define SL(k)
#define DUMP
#define SZ
#define SB_(k)
#define SO(k)
#define RINC
#define RZ
#define SA(k)
#define ST(h)
#define SP0
#define LINC
#define GINC
#define RCNT
    typedef __attribute__((ext_vector_type(32))) unsigned u32x32;
    u32x32 sa, sb; u32x8 sc;
    unsigned tag, bid = blockIdx.x; int tid = threadIdx.x;
    typedef __attribute__((address_space(1))) char gchar;
    typedef gchar* const __attribute__((address_space(4)))* karg_t;
    karg_t ka = (karg_t)__builtin_amdgcn_kernarg_segment_ptr();
    asm volatile(
            SL(0) SZ
            "s_load_dwordx2 s[40:41], %[k], 0x8\n\ts_load_dwordx2 s[36:37], %[k], 0x0\n\tv_readfirstlane_b32 s69, %[t]\n\ts_lshr_b32 s70, %[b], 3\n\ts_cmpk_lg_u32 s69, 0xc0\n\ts_cbranch_scc1 1f\n\t"
            "s_getpc_b64 s[72:73]\n\t.Lch:\ns_lshl3_add_u32 s71, s70, 0xffffff40\n\tv_add_lshl_u32 v36, s71, %[t], 7\n\tv_min_u32 v36, .Lce-.Lch-4, v36\n\ts_mov_b32 m0, 162368\n\t"
            "s_mov_b64 exec, 0xff\n\tglobal_load_lds_dword v36, s[72:73]\n\ts_mov_b64 exec, -1\n\t1:\ns_add_i32 s70, s70, 25\n\ts_and_b32 s70, s70, 31\n\t"
            "s_cmp_lt_u32 s70, 25\n\ts_cbranch_scc0 .LF\n\tv_bfe_u32 v36, %[t], 3, 3\n\tv_and_b32 v37, 7, %[t]\n\ts_mul_i32 s71, s70, 13\n\ts_lshr_b32 s71, s71, 6\n\t"
            "v_and_b32 v38, 2, v36\n\tv_xor_b32 v46, v38, v37\n\ts_lshl_b32 s64, s71, 2\n\ts_sub_i32 s64, s70, s64\n\ts_sub_i32 s57, s64, s71\n\tv_mul_u32_u24 v40, 0xf00, v36\n\t"
            "s_sub_i32 s72, s64, 5\n\ts_min_u32 s64, s64, s72\n\tv_lshl_add_u32 v40, v46, 4, v40\n\ts_bfe_u32 s56, s69, 0x20006\n\ts_and_b32 s72, %[b], 1\n\ts_mul_i32 s72, s72, 5\n\t"
            "s_add_i32 s58, s57, s72\n\ts_lshl_b32 s58, s58, 7\n\ts_mul_i32 s61, s64, 491520\n\ts_mul_i32 s73, s56, 0xf000\n\ts_add_i32 s73, s73, s58\n\ts_add_i32 s61, s61, s73\n\t"
            "s_mul_i32 s66, s56, 0x1800\n\ts_add_i32 m0, s66, 53248\n\ts_mov_b32 s42, 0x258000\n\ts_mov_b32 s43, 0x20000\n\tv_add_u32 v41, 0x100, v40\n\tv_add_u32 v42, 0x200, v40\n\t"
            "v_xor_b32 v43, 64, v40\n\tv_add_u32 v43, 0x6c00, v43\n\tv_add_u32 v44, 0x1100, v43\n\tv_add_u32 v45, 0x1200, v43\n\ts_cmpk_lt_u32 s69, 0x100\n\ts_cbranch_scc1 1f\n\t"
            "s_waitcnt lgkmcnt(0)\n\tbuffer_load_dwordx4 v40, s[40:43], s61 offen lds\n\tbuffer_load_dwordx4 v41, s[40:43], s61 offen offset:1024 lds\n\tbuffer_load_dwordx4 v42, s[40:43], s61 offen offset:2048 lds\n\tbuffer_load_dwordx4 v43, s[40:43], s61 offen offset:3072 lds\n\ts_addk_i32 m0, 0x1000\n\t"
            "s_nop 0\n\tbuffer_load_dwordx4 v44, s[40:43], s61 offen lds\n\tbuffer_load_dwordx4 v45, s[40:43], s61 offen offset:1024 lds\n\t1:\ns_mul_i32 s72, s56, 28\n\tv_add_u32 v39, s72, v36\n\t"
            "s_bfe_u32 s62, %[b], 0x10001\n\ts_mul_i32 s62, s62, 5\n\ts_add_i32 s62, s62, s71\n\ts_mul_i32 s62, s62, 104\n\ts_sub_i32 s62, s62, 8\n\tv_and_b32 v47, 3, v39\n\t"
            "v_lshl_add_u32 v47, v47, 2, s62\n\tv_and_b32 v48, 0x70, v39\n\tv_bfe_u32 v49, v39, 2, 2\n\tv_add3_u32 v48, v48, v49, v47\n\tv_bfe_u32 v49, v39, 1, 3\n\tv_xor_b32 v50, v49, v37\n\t"
            "v_mul_i32_i24 v48, 0xa00, v48\n\ts_bfe_u32 s63, %[b], 0x10002\n\ts_mul_i32 s63, s63, 0x500\n\ts_lshl_b32 s60, s64, 8\n\tv_mov_b32 v51, v36\n\tv_lshl_add_u32 v36, v50, 4, v48\n\t"
            "s_add_i32 s60, s60, s63\n\ts_mul_i32 s65, s56, 0xe00\n\ts_add_i32 m0, s65, 38912\n\ts_mov_b32 s38, 0x280000\n\ts_mov_b32 s39, 0x20000\n\tv_add_u32 v38, 0x9800, v36\n\t"
            "s_cmpk_lt_u32 s69, 0x100\n\ts_cbranch_scc1 1f\n\tbuffer_load_dwordx4 v36, s[36:39], s60 offen lds\n\tbuffer_load_dwordx4 v38, s[36:39], s60 offen offset:2048 lds\n\ts_barrier\n\t1:\n"
            "s_load_dwordx2 s[44:45], %[k], 0x38\n\ts_load_dwordx2 s[48:49], %[k], 0x10\n\ts_load_dwordx2 s[52:53], %[k], 0x40\n\tv_add_u32 v39, 8, v39\n\tv_and_b32 v48, 0x70, v39\n\tv_bfe_u32 v49, v39, 2, 2\n\t"
            "v_add3_u32 v48, v48, v49, v47\n\tv_xor_b32 v50, 4, v50\n\tv_mul_i32_i24 v48, 0xa00, v48\n\tv_lshl_add_u32 v37, v50, 4, v48\n\tv_subrev_u32 v37, 0x400, v37\n\tv_add_u32 v39, 0x9800, v37\n\t"
            "s_addk_i32 s66, 14336\n\ts_bfe_u32 s59, %[b], 0x20001\n\ts_mul_i32 s59, s59, 5\n\ts_add_i32 s59, s59, s71\n\ts_mov_b32 s46, 3432448\n\ts_mov_b32 s47, 0x20000\n\t"
            "s_mov_b32 s54, 0x280000\n\ts_mov_b32 s55, 0x20000\n\tv_and_b32 v52, 63, %[t]\n\tv_bfe_u32 v53, %[t], 4, 2\n\tv_and_b32 v54, 15, %[t]\n\tv_and_b32 v55, 3, %[t]\n\t"
            "v_bfe_u32 v56, %[t], 2, 2\n\tv_lshl_add_u32 v57, v55, 2, v56\n\ts_lshr_b32 s69, s69, 6\n\ts_cmp_lt_u32 s69, 4\n\ts_cbranch_scc0 .LB\n\ts_branch .LA\n\t"
            ".LF:\ns_load_dwordx2 s[72:73], %[k], 0x18\n\ts_and_b32 s71, %[b], 1\n\ts_lshl1_add_u32 s71, s70, s71\n\ts_mul_i32 s71, s71, 40\n\ts_sub_u32 s71, s71, 2000\n\t"
            "v_lshrrev_b32 v2, 4, %[t]\n\tv_add_u32 v2, s71, v2\n\tv_add_u32 v3, 32, v2\n\tv_min_u32 v2, 0x1ff, v2\n\tv_min_u32 v3, 0x1ff, v3\n\tv_and_b32 v4, 15, %[t]\n\t"
            "v_lshlrev_b32 v4, 4, v4\n\ts_bfe_u32 s71, %[b], 0x10001\n\ts_mul_i32 s71, s71, 0x140000\n\ts_bfe_u32 s74, %[b], 0x10002\n\ts_mul_i32 s74, s74, 0x500\n\ts_add_u32 s71, s71, s74\n\t"
            "v_add_u32 v5, s71, v4\n\ts_movk_i32 s71, 0xa00\n\tv_mad_u32_u24 v2, v2, s71, v5\n\tv_mad_u32_u24 v3, v3, s71, v5\n\ts_waitcnt lgkmcnt(0)\n\tglobal_load_dwordx4 v[104:107], v4, s[72:73] offset:0\n\t"
            "global_load_dwordx4 v[108:111], v4, s[72:73] offset:256\n\tglobal_load_dwordx4 v[112:115], v4, s[72:73] offset:512\n\tglobal_load_dwordx4 v[116:119], v4, s[72:73] offset:768\n\tglobal_load_dwordx4 v[120:123], v4, s[72:73] offset:1024\n\ts_cmpk_ge_u32 s69, 0x80\n\ts_cbranch_scc1 1f\n\t"
            "global_load_dwordx4 v[84:87], v3, s[36:37] offset:0\n\tglobal_load_dwordx4 v[88:91], v3, s[36:37] offset:256\n\tglobal_load_dwordx4 v[92:95], v3, s[36:37] offset:512\n\tglobal_load_dwordx4 v[96:99], v3, s[36:37] offset:768\n\tglobal_load_dwordx4 v[100:103], v3, s[36:37] offset:1024\n\t1:\n"
            "global_load_dwordx4 v[64:67], v2, s[36:37] offset:0\n\tglobal_load_dwordx4 v[68:71], v2, s[36:37] offset:256\n\tglobal_load_dwordx4 v[72:75], v2, s[36:37] offset:512\n\tglobal_load_dwordx4 v[76:79], v2, s[36:37] offset:768\n\tglobal_load_dwordx4 v[80:83], v2, s[36:37] offset:1024\n\ts_branch .Lout\n\t"
            ".LA:\nv_lshrrev_b32 v2, 1, v54\n\tv_xor_b32 v2, v2, v53\n\tv_lshlrev_b32 v3, 7, v54\n\tv_lshl_add_u32 v2, v2, 4, v3\n\tv_xor_b32 v3, 64, v2\n\t"
            "v_add_u32 v4, 38912, v2\n\tv_add_u32 v5, 38912, v3\n\tv_add_u32 v10, 77824, v2\n\tv_add_u32 v11, 77824, v3\n\tv_mov_b32 v12, 159872\n\tv_cmp_eq_u32 vcc, 0, v54\n\t"
            "v_mov_b32 v60, 0\n\tv_mov_b32 v61, 0\n\tv_mov_b32 v62, 0x3f803f80\n\tv_lshlrev_b32 v63, 3, v52\n\tv_cndmask_b32 v6, v2, v12, vcc\n\tv_cndmask_b32 v7, v3, v12, vcc\n\t"
            "v_cndmask_b32 v8, v4, v12, vcc\n\tv_cndmask_b32 v9, v5, v12, vcc\n\tv_cndmask_b32 v10, v10, v12, vcc\n\tv_cndmask_b32 v11, v11, v12, vcc\n\tv_lshlrev_b32 v64, 2, v52\n\tv_lshrrev_b32 v65, 2, v52\n\t"
            "v_cmp_eq_u32 vcc, 8, v65\n\tv_add_u32 v66, 161088, v63\n\tv_add_u32 v64, 159744, v64\n\tv_cndmask_b32 v62, 0, v62, vcc\n\ts_cmp_lg_u32 s69, 1\n\ts_cbranch_scc1 1f\n\t"
            "ds_write_b32 v64, v62\n\tds_write_b64 v66, v[60:61]\n\tds_write_b64 v66, v[60:61] offset:576\n\t1:\nv_bfe_u32 v12, v52, 3, 2\n\tv_xor_b32 v12, s69, v12\n\t"
            "v_lshlrev_b32 v13, 7, v56\n\tv_lshl_add_u32 v13, v55, 3, v13\n\tv_lshl_add_u32 v12, v12, 5, v13\n\tv_mul_u32_u24 v13, 0xc00, v53\n\ts_movk_i32 s72, 14336\n\tv_add3_u32 v12, v12, v13, s72\n\t"
            "v_add_u32 v13, 38912, v12\n\ts_mov_b32 s72, 160000\n\tv_lshl_add_u32 v14, v54, 2, s72\n\tv_lshl_add_u32 v15, v53, 4, s72\n\ts_cmp_eq_u32 s69, 3\n\ts_cselect_b32 s74, 1, 0\n\t"
            "s_cselect_b64 s[76:77], -1, 0\n\ts_cselect_b32 s75, 4, 0\n\ts_add_i32 s72, s69, 1\n\ts_bfe_u32 s72, s72, 0x10001\n\ts_xor_b32 s72, s72, 1\n\ts_lshr_b32 s73, s69, 1\n\t"
            "s_lshl1_add_u32 s78, s72, s73\n\ts_lshl2_add_u32 s78, s78, 159776\n\tv_mov_b32 v34, s78\n\ts_lshl2_add_u32 s78, s69, 159744\n\tv_mov_b32 v33, s78\n\ts_mul_i32 s78, s59, 163840\n\t"
            "s_lshl_b32 s79, s58, 5\n\ts_add_i32 s78, s78, s79\n\ts_lshl_b32 s79, s69, 10\n\ts_add_i32 s78, s78, s79\n\ts_add_i32 s78, s78, 155648\n\tv_lshl_add_u32 v16, v52, 4, s78\n\t"
            "v_and_b32 v215, 1, v53\n\tv_lshl_add_u32 v215, v215, 30, v16\n\ts_lshl_b32 s79, s74, 11\n\ts_add_i32 s78, s78, s79\n\ts_sub_i32 s78, s78, 0x400\n\ts_cmp_lg_u32 s57, s75\n\t"
            "s_cselect_b32 s79, s72, 0\n\ts_cmp_lg_u32 s79, 0\n\ts_cselect_b32 s78, s78, 0x40000000\n\tv_lshl_add_u32 v17, v52, 4, s78\n\ts_sub_i32 s78, s69, s57\n\ts_add_i32 s79, s78, 5\n\t"
            "s_min_u32 s78, s78, s79\n\tv_cmp_gt_u32 s[80:81], s78, v53\n\tv_cmp_gt_u32 s[82:83], s78, v56\n\ts_mov_b32 s14, 0x33363333\n\ts_mov_b32 s15, 0x36663366\n\ts_mov_b32 s16, 0x666c6666\n\t"
            "s_mov_b32 s17, 0x6ccc66cc\n\ts_mov_b32 s18, 0x33333333\n\ts_mov_b32 s19, 0x33333333\n\ts_xor_b64 s[80:81], s[80:81], s[76:77]\n\ts_xor_b64 s[82:83], s[82:83], s[76:77]\n\ts_andn2_b64 vcc, s[80:81], s[18:19]\n\t"
            "v_cndmask_b32 v18, 0, -1, vcc\n\ts_and_b64 vcc, s[80:81], s[18:19]\n\tv_cndmask_b32 v19, 0, -1, vcc\n\ts_nor_b64 vcc, s[80:81], s[18:19]\n\tv_cndmask_b32 v20, 0, -1, vcc\n\ts_andn2_b64 vcc, s[18:19], s[80:81]\n\t"
            "v_cndmask_b32 v21, 0, -1, vcc\n\tv_cndmask_b32 v22, 0, -1, s[82:83]\n\tv_cndmask_b32 v23, -1, 0, s[82:83]\n\tv_sub_u32 v58, v57, v53\n\tv_add_u32 v58, 4, v58\n\tv_and_b32 v58, 28, v58\n\t"
            "v_lshlrev_b32 v58, 1, v58\n\tv_mov_b32 v24, -1\n\tv_mov_b32 v25, 0\n\tv_lshlrev_b64 v[24:25], v58, v[24:25]\n\tv_lshlrev_b64 v[24:25], v58, v[24:25]\n\tv_sub_u32 v58, 32, v58\n\t"
            "v_min_u32 v58, 16, v58\n\tv_lshrrev_b32 v26, v58, -1\n\tv_lshrrev_b32 v26, v58, v26\n\ts_lshl1_add_u32 s78, s69, s74\n\ts_xor_b32 s79, s78, 1\n\ts_mul_i32 s78, s78, 0xc00\n\t"
            "s_mul_i32 s79, s79, 0xc00\n\ts_addk_i32 s78, 14336\n\ts_addk_i32 s79, 14336\n\tv_add_u32 v28, s78, v63\n\tv_add_u32 v29, s79, v63\n\ts_cmp_eq_u32 s69, s57\n\t"
            "s_addc_u32 s78, s69, 0\n\ts_add_i32 s79, s69, 1\n\ts_cmp_eq_u32 s79, s57\n\ts_subb_u32 s79, s79, 0\n\ts_cmp_eq_u32 s74, 1\n\ts_cselect_b32 s80, s79, s78\n\t"
            "s_cselect_b32 s81, s78, s79\n\tv_lshlrev_b32 v58, 2, v53\n\tv_lshl_add_u32 v58, v54, 4, v58\n\ts_cmp_gt_u32 s80, s57\n\ts_subb_u32 s82, s80, 0\n\ts_lshl2_add_u32 s82, s82, 159760\n\t"
            "v_mov_b32 v59, s82\n\ts_lshl_b32 s82, s80, 1\n\ts_max_u32 s82, s82, 1\n\ts_mul_i32 s82, s82, 0xc00\n\ts_addk_i32 s82, 11264\n\tv_add_u32 v30, s82, v58\n\t"
            "s_cmp_gt_u32 s81, s57\n\ts_subb_u32 s82, s81, 0\n\ts_lshl2_add_u32 s82, s82, 159760\n\tv_mov_b32 v60, s82\n\ts_lshl_b32 s82, s81, 1\n\ts_max_u32 s82, s82, 1\n\t"
            "s_mul_i32 s82, s82, 0xc00\n\ts_addk_i32 s82, 11264\n\tv_add_u32 v31, s82, v58\n\tv_cndmask_b32 v32, v59, v60, s[18:19]\n\tv_mov_b32 v35, 1\n\ts_waitcnt lgkmcnt(0)\n\t"
            "s_barrier\n\ts_load_dword s67, s[44:45], 0x0\n\ts_mov_b32 s20, 0\n\ts_mov_b32 s21, 40960\n\ts_mov_b32 s22, 81920\n\ts_mov_b32 s23, 122880\n\t"
            "s_add_u32 m0, s65, 38912\n\ts_nop 0\n\tbuffer_load_dwordx4 v37, s[36:39], s60 offen offset:1024 lds\n\ts_mov_b32 exec_hi, 0\n\tbuffer_load_dwordx4 v39, s[36:39], s60 offen offset:3072 lds\n\ts_mov_b32 exec_hi, -1\n\t"
            "s_cmp_eq_u32 s64, 4\n\ts_cselect_b32 s72, -1280, 0\n\ts_add_i32 s72, s72, 256\n\ts_bitcmp1_b32 31, s64\n\ts_cselect_b32 s72, s72, 128\n\ts_mul_i32 s73, s72, 1920\n\t"
            "s_add_u32 s60, s60, s72\n\ts_add_u32 m0, s66, 77824\n\ts_add_u32 s61, s61, s73\n\tbuffer_load_dwordx4 v40, s[40:43], s61 offen lds\n\tbuffer_load_dwordx4 v41, s[40:43], s61 offen offset:1024 lds\n\tbuffer_load_dwordx4 v42, s[40:43], s61 offen offset:2048 lds\n\t"
            "buffer_load_dwordx4 v43, s[40:43], s61 offen offset:3072 lds\n\ts_addk_i32 m0, 0x1000\n\ts_nop 0\n\tbuffer_load_dwordx4 v44, s[40:43], s61 offen lds\n\tbuffer_load_dwordx4 v45, s[40:43], s61 offen offset:1024 lds\n\ts_add_u32 m0, s65, 77824\n\t"
            "s_nop 0\n\tbuffer_load_dwordx4 v36, s[36:39], s60 offen lds\n\tbuffer_load_dwordx4 v37, s[36:39], s60 offen offset:1024 lds\n\tbuffer_load_dwordx4 v38, s[36:39], s60 offen offset:2048 lds\n\ts_mov_b32 exec_hi, 0\n\tbuffer_load_dwordx4 v39, s[36:39], s60 offen offset:3072 lds\n\t"
            "s_mov_b32 exec_hi, -1\n\ts_mov_b32 s68, 0x200000\n\ts_waitcnt lgkmcnt(0)\n\ts_mul_i32 s67, s67, 0x19660d\n\ts_add_u32 s67, s67, 0x3c6ef35f\n\ts_waitcnt vmcnt(10)\n\t"
            "s_barrier\n\t"
            SA(1) ST(-1)
            "ds_read_b64_tr_b16 v[176:177], v13 offset:0\n\tds_read_b64_tr_b16 v[178:179], v13 offset:512\n\tds_read_b128 v[120:123], v8\n\tds_read_b128 v[124:127], v4 offset:2048\n\tds_read_b128 v[128:131], v4 offset:4096\n\tds_read_b128 v[132:135], v4 offset:6144\n\t"
            "ds_read_b128 v[136:139], v4 offset:8192\n\tds_read_b128 v[140:143], v4 offset:10240\n\tds_read_b128 v[144:147], v4 offset:12288\n\tds_read_b64_tr_b16 v[180:181], v13 offset:1024\n\tds_read_b64_tr_b16 v[182:183], v13 offset:1536\n\tds_read_b64_tr_b16 v[184:185], v13 offset:2048\n\t"
            "ds_read_b64_tr_b16 v[186:187], v13 offset:2560\n\ts_waitcnt lgkmcnt(10)\n\tv_mfma_f32_16x16x32_bf16 v[36:39], v[176:179], v[120:123], 0\n\ts_waitcnt lgkmcnt(9)\n\tv_mfma_f32_16x16x32_bf16 v[48:51], v[176:179], v[124:127], 0\n\ts_waitcnt lgkmcnt(8)\n\t"
            "v_mfma_f32_16x16x32_bf16 v[60:63], v[176:179], v[128:131], 0\n\tds_read_b64_tr_b16 v[188:189], v13 offset:12288\n\tds_read_b64_tr_b16 v[190:191], v13 offset:12800\n\ts_waitcnt lgkmcnt(9)\n\tv_mfma_f32_16x16x32_bf16 v[72:75], v[176:179], v[132:135], 0\n\tds_read_b128 v[148:151], v9\n\t"
            "s_waitcnt lgkmcnt(9)\n\tv_mfma_f32_16x16x32_bf16 v[84:87], v[176:179], v[136:139], 0\n\tds_read_b128 v[152:155], v5 offset:2048\n\ts_waitcnt lgkmcnt(9)\n\tv_mfma_f32_16x16x32_bf16 v[96:99], v[176:179], v[140:143], 0\n\tds_read_b128 v[156:159], v5 offset:4096\n\t"
            "s_waitcnt lgkmcnt(9)\n\tv_mfma_f32_16x16x32_bf16 v[108:111], v[176:179], v[144:147], 0\n\tds_read_b128 v[160:163], v5 offset:6144\n\ts_waitcnt lgkmcnt(8)\n\tv_mfma_f32_16x16x32_bf16 v[40:43], v[180:183], v[120:123], 0\n\tds_read_b128 v[164:167], v5 offset:8192\n\t"
            "v_mfma_f32_16x16x32_bf16 v[52:55], v[180:183], v[124:127], 0\n\tds_read_b128 v[168:171], v5 offset:10240\n\tv_mfma_f32_16x16x32_bf16 v[64:67], v[180:183], v[128:131], 0\n\tds_read_b128 v[172:175], v5 offset:12288\n\tv_mfma_f32_16x16x32_bf16 v[76:79], v[180:183], v[132:135], 0\n\tds_read_b64_tr_b16 v[192:193], v13 offset:13312\n\t"
            "ds_read_b64_tr_b16 v[194:195], v13 offset:13824\n\tv_mfma_f32_16x16x32_bf16 v[88:91], v[180:183], v[136:139], 0\n\tds_read_b64_tr_b16 v[196:197], v13 offset:14336\n\tds_read_b64_tr_b16 v[198:199], v13 offset:14848\n\tv_mfma_f32_16x16x32_bf16 v[100:103], v[180:183], v[140:143], 0\n\tv_mfma_f32_16x16x32_bf16 v[112:115], v[180:183], v[144:147], 0\n\t"
            "s_waitcnt lgkmcnt(13)\n\tv_mfma_f32_16x16x32_bf16 v[44:47], v[120:123], v[184:187], 0\n\tv_mfma_f32_16x16x32_bf16 v[56:59], v[124:127], v[184:187], 0\n\tv_mfma_f32_16x16x32_bf16 v[68:71], v[128:131], v[184:187], 0\n\tv_mfma_f32_16x16x32_bf16 v[80:83], v[132:135], v[184:187], 0\n\tv_mfma_f32_16x16x32_bf16 v[92:95], v[136:139], v[184:187], 0\n\t"
            "v_mfma_f32_16x16x32_bf16 v[104:107], v[140:143], v[184:187], 0\n\tv_mfma_f32_16x16x32_bf16 v[116:119], v[144:147], v[184:187], 0\n\ts_waitcnt lgkmcnt(10)\n\tv_mfma_f32_16x16x32_bf16 v[36:39], v[188:191], v[148:151], v[36:39]\n\ts_waitcnt lgkmcnt(9)\n\tv_mfma_f32_16x16x32_bf16 v[48:51], v[188:191], v[152:155], v[48:51]\n\t"
            "s_waitcnt lgkmcnt(8)\n\tv_mfma_f32_16x16x32_bf16 v[60:63], v[188:191], v[156:159], v[60:63]\n\ts_waitcnt lgkmcnt(7)\n\tv_mfma_f32_16x16x32_bf16 v[72:75], v[188:191], v[160:163], v[72:75]\n\ts_waitcnt lgkmcnt(6)\n\tv_mfma_f32_16x16x32_bf16 v[84:87], v[188:191], v[164:167], v[84:87]\n\t"
            "s_waitcnt lgkmcnt(5)\n\tv_mfma_f32_16x16x32_bf16 v[96:99], v[188:191], v[168:171], v[96:99]\n\ts_waitcnt lgkmcnt(4)\n\tv_mfma_f32_16x16x32_bf16 v[108:111], v[188:191], v[172:175], v[108:111]\n\ts_waitcnt lgkmcnt(2)\n\tv_mfma_f32_16x16x32_bf16 v[40:43], v[192:195], v[148:151], v[40:43]\n\t"
            "v_mfma_f32_16x16x32_bf16 v[52:55], v[192:195], v[152:155], v[52:55]\n\tv_mfma_f32_16x16x32_bf16 v[64:67], v[192:195], v[156:159], v[64:67]\n\tv_mfma_f32_16x16x32_bf16 v[76:79], v[192:195], v[160:163], v[76:79]\n\ts_waitcnt vmcnt(0) lgkmcnt(0)\n\ts_barrier\n\t"
            ST(1)
            "ds_read_b64_tr_b16 v[176:177], v13 offset:38912\n\tds_read_b64_tr_b16 v[178:179], v13 offset:39424\n\tv_mfma_f32_16x16x32_bf16 v[88:91], v[192:195], v[164:167], v[88:91]\n\tds_read_b128 v[120:123], v10\n\tv_mfma_f32_16x16x32_bf16 v[100:103], v[192:195], v[168:171], v[100:103]\n\tds_read_b128 v[124:127], v4 offset:40960\n\t"
            "v_mfma_f32_16x16x32_bf16 v[112:115], v[192:195], v[172:175], v[112:115]\n\tds_read_b128 v[128:131], v4 offset:43008\n\tv_mfma_f32_16x16x32_bf16 v[44:47], v[148:151], v[196:199], v[44:47]\n\tds_read_b128 v[132:135], v4 offset:45056\n\tv_mfma_f32_16x16x32_bf16 v[56:59], v[152:155], v[196:199], v[56:59]\n\tds_read_b128 v[136:139], v4 offset:47104\n\t"
            "v_mfma_f32_16x16x32_bf16 v[68:71], v[156:159], v[196:199], v[68:71]\n\tds_read_b128 v[140:143], v4 offset:49152\n\tv_mfma_f32_16x16x32_bf16 v[80:83], v[160:163], v[196:199], v[80:83]\n\tds_read_b128 v[144:147], v4 offset:51200\n\tv_mfma_f32_16x16x32_bf16 v[92:95], v[164:167], v[196:199], v[92:95]\n\tds_read_b64_tr_b16 v[180:181], v13 offset:39936\n\t"
            "ds_read_b64_tr_b16 v[182:183], v13 offset:40448\n\tv_mfma_f32_16x16x32_bf16 v[104:107], v[168:171], v[196:199], v[104:107]\n\tds_read_b64_tr_b16 v[184:185], v13 offset:40960\n\tds_read_b64_tr_b16 v[186:187], v13 offset:41472\n\tv_mfma_f32_16x16x32_bf16 v[116:119], v[172:175], v[196:199], v[116:119]\n\ts_waitcnt lgkmcnt(10)\n\t"
            "v_mfma_f32_16x16x32_bf16 v[36:39], v[176:179], v[120:123], v[36:39]\n\ts_waitcnt lgkmcnt(9)\n\tv_mfma_f32_16x16x32_bf16 v[48:51], v[176:179], v[124:127], v[48:51]\n\ts_waitcnt lgkmcnt(8)\n\tv_mfma_f32_16x16x32_bf16 v[60:63], v[176:179], v[128:131], v[60:63]\n\tds_read_b64_tr_b16 v[188:189], v13 offset:51200\n\t"
            "ds_read_b64_tr_b16 v[190:191], v13 offset:51712\n\ts_waitcnt lgkmcnt(9)\n\tv_mfma_f32_16x16x32_bf16 v[72:75], v[176:179], v[132:135], v[72:75]\n\tds_read_b128 v[148:151], v11\n\ts_waitcnt lgkmcnt(9)\n\tv_mfma_f32_16x16x32_bf16 v[84:87], v[176:179], v[136:139], v[84:87]\n\t"
            "ds_read_b128 v[152:155], v5 offset:40960\n\ts_waitcnt lgkmcnt(9)\n\tv_mfma_f32_16x16x32_bf16 v[96:99], v[176:179], v[140:143], v[96:99]\n\tds_read_b128 v[156:159], v5 offset:43008\n\ts_waitcnt lgkmcnt(9)\n\tv_mfma_f32_16x16x32_bf16 v[108:111], v[176:179], v[144:147], v[108:111]\n\t"
            "ds_read_b128 v[160:163], v5 offset:45056\n\ts_waitcnt lgkmcnt(8)\n\tv_mfma_f32_16x16x32_bf16 v[40:43], v[180:183], v[120:123], v[40:43]\n\tds_read_b128 v[164:167], v5 offset:47104\n\tv_mfma_f32_16x16x32_bf16 v[52:55], v[180:183], v[124:127], v[52:55]\n\tds_read_b128 v[168:171], v5 offset:49152\n\t"
            "v_mfma_f32_16x16x32_bf16 v[64:67], v[180:183], v[128:131], v[64:67]\n\tds_read_b128 v[172:175], v5 offset:51200\n\tv_mfma_f32_16x16x32_bf16 v[76:79], v[180:183], v[132:135], v[76:79]\n\tds_read_b64_tr_b16 v[192:193], v13 offset:52224\n\tds_read_b64_tr_b16 v[194:195], v13 offset:52736\n\tv_mfma_f32_16x16x32_bf16 v[88:91], v[180:183], v[136:139], v[88:91]\n\t"
            "ds_read_b64_tr_b16 v[196:197], v13 offset:53248\n\tds_read_b64_tr_b16 v[198:199], v13 offset:53760\n\tv_mfma_f32_16x16x32_bf16 v[100:103], v[180:183], v[140:143], v[100:103]\n\tv_mfma_f32_16x16x32_bf16 v[112:115], v[180:183], v[144:147], v[112:115]\n\ts_waitcnt lgkmcnt(13)\n\tv_mfma_f32_16x16x32_bf16 v[44:47], v[120:123], v[184:187], v[44:47]\n\t"
            "v_mfma_f32_16x16x32_bf16 v[56:59], v[124:127], v[184:187], v[56:59]\n\tv_mfma_f32_16x16x32_bf16 v[68:71], v[128:131], v[184:187], v[68:71]\n\tv_mfma_f32_16x16x32_bf16 v[80:83], v[132:135], v[184:187], v[80:83]\n\tv_mfma_f32_16x16x32_bf16 v[92:95], v[136:139], v[184:187], v[92:95]\n\tv_mfma_f32_16x16x32_bf16 v[104:107], v[140:143], v[184:187], v[104:107]\n\tv_mfma_f32_16x16x32_bf16 v[116:119], v[144:147], v[184:187], v[116:119]\n\t"
            "s_waitcnt lgkmcnt(10)\n\tv_mfma_f32_16x16x32_bf16 v[36:39], v[188:191], v[148:151], v[36:39]\n\ts_waitcnt lgkmcnt(9)\n\tv_mfma_f32_16x16x32_bf16 v[48:51], v[188:191], v[152:155], v[48:51]\n\ts_waitcnt lgkmcnt(8)\n\tv_mfma_f32_16x16x32_bf16 v[60:63], v[188:191], v[156:159], v[60:63]\n\t"
            "s_waitcnt lgkmcnt(7)\n\tv_mfma_f32_16x16x32_bf16 v[72:75], v[188:191], v[160:163], v[72:75]\n\ts_waitcnt lgkmcnt(6)\n\tv_mfma_f32_16x16x32_bf16 v[84:87], v[188:191], v[164:167], v[84:87]\n\ts_waitcnt lgkmcnt(5)\n\tv_mfma_f32_16x16x32_bf16 v[96:99], v[188:191], v[168:171], v[96:99]\n\t"
            "s_waitcnt lgkmcnt(4)\n\tv_mfma_f32_16x16x32_bf16 v[108:111], v[188:191], v[172:175], v[108:111]\n\ts_waitcnt lgkmcnt(2)\n\tv_mfma_f32_16x16x32_bf16 v[40:43], v[192:195], v[148:151], v[40:43]\n\tv_mfma_f32_16x16x32_bf16 v[52:55], v[192:195], v[152:155], v[52:55]\n\tv_mfma_f32_16x16x32_bf16 v[64:67], v[192:195], v[156:159], v[64:67]\n\t"
            "v_mfma_f32_16x16x32_bf16 v[76:79], v[192:195], v[160:163], v[76:79]\n\ts_waitcnt lgkmcnt(0)\n\ts_barrier\n\t"
            ST(3)
            "ds_read_b64_tr_b16 v[176:177], v12 offset:0\n\tds_read_b64_tr_b16 v[178:179], v12 offset:512\n\tv_mfma_f32_16x16x32_bf16 v[88:91], v[192:195], v[164:167], v[88:91]\n\tds_read_b128 v[120:123], v6\n\tv_mfma_f32_16x16x32_bf16 v[100:103], v[192:195], v[168:171], v[100:103]\n\tds_read_b128 v[124:127], v2 offset:2048\n\t"
            "v_mfma_f32_16x16x32_bf16 v[112:115], v[192:195], v[172:175], v[112:115]\n\tds_read_b128 v[128:131], v2 offset:4096\n\tv_mfma_f32_16x16x32_bf16 v[44:47], v[148:151], v[196:199], v[44:47]\n\tds_read_b128 v[132:135], v2 offset:6144\n\tv_mfma_f32_16x16x32_bf16 v[56:59], v[152:155], v[196:199], v[56:59]\n\tds_read_b128 v[136:139], v2 offset:8192\n\t"
            "v_mfma_f32_16x16x32_bf16 v[68:71], v[156:159], v[196:199], v[68:71]\n\tds_read_b128 v[140:143], v2 offset:10240\n\tv_mfma_f32_16x16x32_bf16 v[80:83], v[160:163], v[196:199], v[80:83]\n\tds_read_b128 v[144:147], v2 offset:12288\n\tv_mfma_f32_16x16x32_bf16 v[92:95], v[164:167], v[196:199], v[92:95]\n\tds_read_b64_tr_b16 v[180:181], v12 offset:1024\n\t"
            "ds_read_b64_tr_b16 v[182:183], v12 offset:1536\n\tv_mfma_f32_16x16x32_bf16 v[104:107], v[168:171], v[196:199], v[104:107]\n\tds_read_b64_tr_b16 v[184:185], v12 offset:2048\n\tds_read_b64_tr_b16 v[186:187], v12 offset:2560\n\tv_mfma_f32_16x16x32_bf16 v[116:119], v[172:175], v[196:199], v[116:119]\n\ts_waitcnt lgkmcnt(10)\n\t"
            "v_mfma_f32_16x16x32_bf16 v[36:39], v[176:179], v[120:123], v[36:39]\n\ts_waitcnt lgkmcnt(9)\n\tv_mfma_f32_16x16x32_bf16 v[48:51], v[176:179], v[124:127], v[48:51]\n\ts_waitcnt lgkmcnt(8)\n\tv_mfma_f32_16x16x32_bf16 v[60:63], v[176:179], v[128:131], v[60:63]\n\tds_read_b64_tr_b16 v[188:189], v12 offset:12288\n\t"
            "ds_read_b64_tr_b16 v[190:191], v12 offset:12800\n\ts_waitcnt lgkmcnt(9)\n\tv_mfma_f32_16x16x32_bf16 v[72:75], v[176:179], v[132:135], v[72:75]\n\tds_read_b128 v[148:151], v7\n\ts_waitcnt lgkmcnt(9)\n\tv_mfma_f32_16x16x32_bf16 v[84:87], v[176:179], v[136:139], v[84:87]\n\t"
            "ds_read_b128 v[152:155], v3 offset:2048\n\ts_waitcnt lgkmcnt(9)\n\tv_mfma_f32_16x16x32_bf16 v[96:99], v[176:179], v[140:143], v[96:99]\n\tds_read_b128 v[156:159], v3 offset:4096\n\ts_waitcnt lgkmcnt(9)\n\tv_mfma_f32_16x16x32_bf16 v[108:111], v[176:179], v[144:147], v[108:111]\n\t"
            "ds_read_b128 v[160:163], v3 offset:6144\n\ts_waitcnt lgkmcnt(8)\n\tv_mfma_f32_16x16x32_bf16 v[40:43], v[180:183], v[120:123], v[40:43]\n\tds_read_b128 v[164:167], v3 offset:8192\n\tv_mfma_f32_16x16x32_bf16 v[52:55], v[180:183], v[124:127], v[52:55]\n\tds_read_b128 v[168:171], v3 offset:10240\n\t"
            "v_mfma_f32_16x16x32_bf16 v[64:67], v[180:183], v[128:131], v[64:67]\n\tds_read_b128 v[172:175], v3 offset:12288\n\tv_mfma_f32_16x16x32_bf16 v[76:79], v[180:183], v[132:135], v[76:79]\n\tds_read_b64_tr_b16 v[192:193], v12 offset:13312\n\tds_read_b64_tr_b16 v[194:195], v12 offset:13824\n\tv_mfma_f32_16x16x32_bf16 v[88:91], v[180:183], v[136:139], v[88:91]\n\t"
            "ds_read_b64_tr_b16 v[196:197], v12 offset:14336\n\tds_read_b64_tr_b16 v[198:199], v12 offset:14848\n\tv_mfma_f32_16x16x32_bf16 v[100:103], v[180:183], v[140:143], v[100:103]\n\tv_mfma_f32_16x16x32_bf16 v[112:115], v[180:183], v[144:147], v[112:115]\n\ts_waitcnt lgkmcnt(13)\n\tv_mfma_f32_16x16x32_bf16 v[44:47], v[120:123], v[184:187], v[44:47]\n\t"
            "v_mfma_f32_16x16x32_bf16 v[56:59], v[124:127], v[184:187], v[56:59]\n\tv_mfma_f32_16x16x32_bf16 v[68:71], v[128:131], v[184:187], v[68:71]\n\tv_mfma_f32_16x16x32_bf16 v[80:83], v[132:135], v[184:187], v[80:83]\n\tv_mfma_f32_16x16x32_bf16 v[92:95], v[136:139], v[184:187], v[92:95]\n\tv_mfma_f32_16x16x32_bf16 v[104:107], v[140:143], v[184:187], v[104:107]\n\tv_mfma_f32_16x16x32_bf16 v[116:119], v[144:147], v[184:187], v[116:119]\n\t"
            "s_waitcnt lgkmcnt(10)\n\tv_mfma_f32_16x16x32_bf16 v[36:39], v[188:191], v[148:151], v[36:39]\n\ts_waitcnt lgkmcnt(9)\n\tv_mfma_f32_16x16x32_bf16 v[48:51], v[188:191], v[152:155], v[48:51]\n\ts_waitcnt lgkmcnt(8)\n\tv_mfma_f32_16x16x32_bf16 v[60:63], v[188:191], v[156:159], v[60:63]\n\t"
            "s_waitcnt lgkmcnt(7)\n\tv_mfma_f32_16x16x32_bf16 v[72:75], v[188:191], v[160:163], v[72:75]\n\ts_waitcnt lgkmcnt(6)\n\tv_mfma_f32_16x16x32_bf16 v[84:87], v[188:191], v[164:167], v[84:87]\n\ts_waitcnt lgkmcnt(5)\n\tv_mfma_f32_16x16x32_bf16 v[96:99], v[188:191], v[168:171], v[96:99]\n\t"
            "s_waitcnt lgkmcnt(4)\n\tv_mfma_f32_16x16x32_bf16 v[108:111], v[188:191], v[172:175], v[108:111]\n\ts_waitcnt lgkmcnt(2)\n\tv_mfma_f32_16x16x32_bf16 v[40:43], v[192:195], v[148:151], v[40:43]\n\tv_mfma_f32_16x16x32_bf16 v[52:55], v[192:195], v[152:155], v[52:55]\n\tv_mfma_f32_16x16x32_bf16 v[64:67], v[192:195], v[156:159], v[64:67]\n\t"
            "v_mfma_f32_16x16x32_bf16 v[76:79], v[192:195], v[160:163], v[76:79]\n\ts_waitcnt lgkmcnt(0)\n\ts_barrier\n\t"
            ST(5)
            "ds_read_b64_tr_b16 v[176:177], v13 offset:0\n\tds_read_b64_tr_b16 v[178:179], v13 offset:512\n\tv_mfma_f32_16x16x32_bf16 v[88:91], v[192:195], v[164:167], v[88:91]\n\tds_read_b128 v[120:123], v8\n\tv_mfma_f32_16x16x32_bf16 v[100:103], v[192:195], v[168:171], v[100:103]\n\tds_read_b128 v[124:127], v4 offset:2048\n\t"
            "v_mfma_f32_16x16x32_bf16 v[112:115], v[192:195], v[172:175], v[112:115]\n\tds_read_b128 v[128:131], v4 offset:4096\n\tv_mfma_f32_16x16x32_bf16 v[44:47], v[148:151], v[196:199], v[44:47]\n\tds_read_b128 v[132:135], v4 offset:6144\n\tv_mfma_f32_16x16x32_bf16 v[56:59], v[152:155], v[196:199], v[56:59]\n\tds_read_b128 v[136:139], v4 offset:8192\n\t"
            "v_mfma_f32_16x16x32_bf16 v[68:71], v[156:159], v[196:199], v[68:71]\n\tds_read_b128 v[140:143], v4 offset:10240\n\tv_mfma_f32_16x16x32_bf16 v[80:83], v[160:163], v[196:199], v[80:83]\n\tds_read_b128 v[144:147], v4 offset:12288\n\tv_mfma_f32_16x16x32_bf16 v[92:95], v[164:167], v[196:199], v[92:95]\n\tds_read_b64_tr_b16 v[180:181], v13 offset:1024\n\t"
            "ds_read_b64_tr_b16 v[182:183], v13 offset:1536\n\tv_mfma_f32_16x16x32_bf16 v[104:107], v[168:171], v[196:199], v[104:107]\n\tds_read_b64_tr_b16 v[184:185], v13 offset:2048\n\tds_read_b64_tr_b16 v[186:187], v13 offset:2560\n\tv_mfma_f32_16x16x32_bf16 v[116:119], v[172:175], v[196:199], v[116:119]\n\ts_waitcnt lgkmcnt(10)\n\t"
            "v_mfma_f32_16x16x32_bf16 v[36:39], v[176:179], v[120:123], v[36:39]\n\ts_waitcnt lgkmcnt(9)\n\tv_mfma_f32_16x16x32_bf16 v[48:51], v[176:179], v[124:127], v[48:51]\n\ts_waitcnt lgkmcnt(8)\n\tv_mfma_f32_16x16x32_bf16 v[60:63], v[176:179], v[128:131], v[60:63]\n\tds_read_b64_tr_b16 v[188:189], v13 offset:12288\n\t"
            "ds_read_b64_tr_b16 v[190:191], v13 offset:12800\n\ts_waitcnt lgkmcnt(9)\n\tv_mfma_f32_16x16x32_bf16 v[72:75], v[176:179], v[132:135], v[72:75]\n\tds_read_b128 v[148:151], v9\n\ts_waitcnt lgkmcnt(9)\n\tv_mfma_f32_16x16x32_bf16 v[84:87], v[176:179], v[136:139], v[84:87]\n\t"
            "ds_read_b128 v[152:155], v5 offset:2048\n\ts_waitcnt lgkmcnt(9)\n\tv_mfma_f32_16x16x32_bf16 v[96:99], v[176:179], v[140:143], v[96:99]\n\tds_read_b128 v[156:159], v5 offset:4096\n\ts_waitcnt lgkmcnt(9)\n\tv_mfma_f32_16x16x32_bf16 v[108:111], v[176:179], v[144:147], v[108:111]\n\t"
            "ds_read_b128 v[160:163], v5 offset:6144\n\ts_waitcnt lgkmcnt(8)\n\tv_mfma_f32_16x16x32_bf16 v[40:43], v[180:183], v[120:123], v[40:43]\n\tds_read_b128 v[164:167], v5 offset:8192\n\tv_mfma_f32_16x16x32_bf16 v[52:55], v[180:183], v[124:127], v[52:55]\n\tds_read_b128 v[168:171], v5 offset:10240\n\t"
            "v_mfma_f32_16x16x32_bf16 v[64:67], v[180:183], v[128:131], v[64:67]\n\tds_read_b128 v[172:175], v5 offset:12288\n\tv_mfma_f32_16x16x32_bf16 v[76:79], v[180:183], v[132:135], v[76:79]\n\tds_read_b64_tr_b16 v[192:193], v13 offset:13312\n\tds_read_b64_tr_b16 v[194:195], v13 offset:13824\n\tv_mfma_f32_16x16x32_bf16 v[88:91], v[180:183], v[136:139], v[88:91]\n\t"
            "ds_read_b64_tr_b16 v[196:197], v13 offset:14336\n\tds_read_b64_tr_b16 v[198:199], v13 offset:14848\n\tv_mfma_f32_16x16x32_bf16 v[100:103], v[180:183], v[140:143], v[100:103]\n\tv_mfma_f32_16x16x32_bf16 v[112:115], v[180:183], v[144:147], v[112:115]\n\ts_waitcnt lgkmcnt(13)\n\tv_mfma_f32_16x16x32_bf16 v[44:47], v[120:123], v[184:187], v[44:47]\n\t"
            "v_mfma_f32_16x16x32_bf16 v[56:59], v[124:127], v[184:187], v[56:59]\n\tv_mfma_f32_16x16x32_bf16 v[68:71], v[128:131], v[184:187], v[68:71]\n\tv_mfma_f32_16x16x32_bf16 v[80:83], v[132:135], v[184:187], v[80:83]\n\tv_mfma_f32_16x16x32_bf16 v[92:95], v[136:139], v[184:187], v[92:95]\n\tv_mfma_f32_16x16x32_bf16 v[104:107], v[140:143], v[184:187], v[104:107]\n\tv_mfma_f32_16x16x32_bf16 v[116:119], v[144:147], v[184:187], v[116:119]\n\t"
            "s_waitcnt lgkmcnt(10)\n\tv_mfma_f32_16x16x32_bf16 v[36:39], v[188:191], v[148:151], v[36:39]\n\ts_waitcnt lgkmcnt(9)\n\tv_mfma_f32_16x16x32_bf16 v[48:51], v[188:191], v[152:155], v[48:51]\n\ts_waitcnt lgkmcnt(8)\n\tv_mfma_f32_16x16x32_bf16 v[60:63], v[188:191], v[156:159], v[60:63]\n\t"
            "s_waitcnt lgkmcnt(7)\n\tv_mfma_f32_16x16x32_bf16 v[72:75], v[188:191], v[160:163], v[72:75]\n\ts_waitcnt lgkmcnt(6)\n\tv_mfma_f32_16x16x32_bf16 v[84:87], v[188:191], v[164:167], v[84:87]\n\ts_waitcnt lgkmcnt(5)\n\tv_mfma_f32_16x16x32_bf16 v[96:99], v[188:191], v[168:171], v[96:99]\n\t"
            "s_waitcnt lgkmcnt(4)\n\tv_mfma_f32_16x16x32_bf16 v[108:111], v[188:191], v[172:175], v[108:111]\n\ts_waitcnt lgkmcnt(2)\n\tv_mfma_f32_16x16x32_bf16 v[40:43], v[192:195], v[148:151], v[40:43]\n\tv_mfma_f32_16x16x32_bf16 v[52:55], v[192:195], v[152:155], v[52:55]\n\tv_mfma_f32_16x16x32_bf16 v[64:67], v[192:195], v[156:159], v[64:67]\n\t"
            "v_mfma_f32_16x16x32_bf16 v[76:79], v[192:195], v[160:163], v[76:79]\n\ts_waitcnt lgkmcnt(0)\n\ts_barrier\n\t"
            ST(7)
            "ds_read_b64_tr_b16 v[176:177], v13 offset:38912\n\tds_read_b64_tr_b16 v[178:179], v13 offset:39424\n\tv_mfma_f32_16x16x32_bf16 v[88:91], v[192:195], v[164:167], v[88:91]\n\tds_read_b128 v[120:123], v10\n\tv_mfma_f32_16x16x32_bf16 v[100:103], v[192:195], v[168:171], v[100:103]\n\tds_read_b128 v[124:127], v4 offset:40960\n\t"
            "v_mfma_f32_16x16x32_bf16 v[112:115], v[192:195], v[172:175], v[112:115]\n\tds_read_b128 v[128:131], v4 offset:43008\n\tv_mfma_f32_16x16x32_bf16 v[44:47], v[148:151], v[196:199], v[44:47]\n\tds_read_b128 v[132:135], v4 offset:45056\n\tv_mfma_f32_16x16x32_bf16 v[56:59], v[152:155], v[196:199], v[56:59]\n\tds_read_b128 v[136:139], v4 offset:47104\n\t"
            "v_mfma_f32_16x16x32_bf16 v[68:71], v[156:159], v[196:199], v[68:71]\n\tds_read_b128 v[140:143], v4 offset:49152\n\tv_mfma_f32_16x16x32_bf16 v[80:83], v[160:163], v[196:199], v[80:83]\n\tds_read_b128 v[144:147], v4 offset:51200\n\tv_mfma_f32_16x16x32_bf16 v[92:95], v[164:167], v[196:199], v[92:95]\n\tds_read_b64_tr_b16 v[180:181], v13 offset:39936\n\t"
            "ds_read_b64_tr_b16 v[182:183], v13 offset:40448\n\tv_mfma_f32_16x16x32_bf16 v[104:107], v[168:171], v[196:199], v[104:107]\n\tds_read_b64_tr_b16 v[184:185], v13 offset:40960\n\tds_read_b64_tr_b16 v[186:187], v13 offset:41472\n\tv_mfma_f32_16x16x32_bf16 v[116:119], v[172:175], v[196:199], v[116:119]\n\ts_waitcnt lgkmcnt(10)\n\t"
            "v_mfma_f32_16x16x32_bf16 v[36:39], v[176:179], v[120:123], v[36:39]\n\ts_waitcnt lgkmcnt(9)\n\tv_mfma_f32_16x16x32_bf16 v[48:51], v[176:179], v[124:127], v[48:51]\n\ts_waitcnt lgkmcnt(8)\n\tv_mfma_f32_16x16x32_bf16 v[60:63], v[176:179], v[128:131], v[60:63]\n\tds_read_b64_tr_b16 v[188:189], v13 offset:51200\n\t"
            "ds_read_b64_tr_b16 v[190:191], v13 offset:51712\n\ts_waitcnt lgkmcnt(9)\n\tv_mfma_f32_16x16x32_bf16 v[72:75], v[176:179], v[132:135], v[72:75]\n\tds_read_b128 v[148:151], v11\n\ts_waitcnt lgkmcnt(9)\n\tv_mfma_f32_16x16x32_bf16 v[84:87], v[176:179], v[136:139], v[84:87]\n\t"
            "ds_read_b128 v[152:155], v5 offset:40960\n\ts_waitcnt lgkmcnt(9)\n\tv_mfma_f32_16x16x32_bf16 v[96:99], v[176:179], v[140:143], v[96:99]\n\tds_read_b128 v[156:159], v5 offset:43008\n\ts_waitcnt lgkmcnt(9)\n\tv_mfma_f32_16x16x32_bf16 v[108:111], v[176:179], v[144:147], v[108:111]\n\t"
            "ds_read_b128 v[160:163], v5 offset:45056\n\ts_waitcnt lgkmcnt(8)\n\tv_mfma_f32_16x16x32_bf16 v[40:43], v[180:183], v[120:123], v[40:43]\n\tds_read_b128 v[164:167], v5 offset:47104\n\tv_mfma_f32_16x16x32_bf16 v[52:55], v[180:183], v[124:127], v[52:55]\n\tds_read_b128 v[168:171], v5 offset:49152\n\t"
            "v_mfma_f32_16x16x32_bf16 v[64:67], v[180:183], v[128:131], v[64:67]\n\tds_read_b128 v[172:175], v5 offset:51200\n\tv_mfma_f32_16x16x32_bf16 v[76:79], v[180:183], v[132:135], v[76:79]\n\tds_read_b64_tr_b16 v[192:193], v13 offset:52224\n\tds_read_b64_tr_b16 v[194:195], v13 offset:52736\n\tv_mfma_f32_16x16x32_bf16 v[88:91], v[180:183], v[136:139], v[88:91]\n\t"
            "ds_read_b64_tr_b16 v[196:197], v13 offset:53248\n\tds_read_b64_tr_b16 v[198:199], v13 offset:53760\n\tv_mfma_f32_16x16x32_bf16 v[100:103], v[180:183], v[140:143], v[100:103]\n\tv_mfma_f32_16x16x32_bf16 v[112:115], v[180:183], v[144:147], v[112:115]\n\ts_waitcnt lgkmcnt(13)\n\tv_mfma_f32_16x16x32_bf16 v[44:47], v[120:123], v[184:187], v[44:47]\n\t"
            "v_mfma_f32_16x16x32_bf16 v[56:59], v[124:127], v[184:187], v[56:59]\n\tv_mfma_f32_16x16x32_bf16 v[68:71], v[128:131], v[184:187], v[68:71]\n\tv_mfma_f32_16x16x32_bf16 v[80:83], v[132:135], v[184:187], v[80:83]\n\tv_mfma_f32_16x16x32_bf16 v[92:95], v[136:139], v[184:187], v[92:95]\n\tv_mfma_f32_16x16x32_bf16 v[104:107], v[140:143], v[184:187], v[104:107]\n\tv_mfma_f32_16x16x32_bf16 v[116:119], v[144:147], v[184:187], v[116:119]\n\t"
            "s_waitcnt lgkmcnt(10)\n\tv_mfma_f32_16x16x32_bf16 v[36:39], v[188:191], v[148:151], v[36:39]\n\ts_waitcnt lgkmcnt(9)\n\tv_mfma_f32_16x16x32_bf16 v[48:51], v[188:191], v[152:155], v[48:51]\n\ts_waitcnt lgkmcnt(8)\n\tv_mfma_f32_16x16x32_bf16 v[60:63], v[188:191], v[156:159], v[60:63]\n\t"
            "s_waitcnt lgkmcnt(7)\n\tv_mfma_f32_16x16x32_bf16 v[72:75], v[188:191], v[160:163], v[72:75]\n\ts_waitcnt lgkmcnt(6)\n\tv_mfma_f32_16x16x32_bf16 v[84:87], v[188:191], v[164:167], v[84:87]\n\ts_waitcnt lgkmcnt(5)\n\tv_mfma_f32_16x16x32_bf16 v[96:99], v[188:191], v[168:171], v[96:99]\n\t"
            "s_waitcnt lgkmcnt(4)\n\tv_mfma_f32_16x16x32_bf16 v[108:111], v[188:191], v[172:175], v[108:111]\n\ts_waitcnt lgkmcnt(2)\n\tv_mfma_f32_16x16x32_bf16 v[40:43], v[192:195], v[148:151], v[40:43]\n\tv_mfma_f32_16x16x32_bf16 v[52:55], v[192:195], v[152:155], v[52:55]\n\tv_mfma_f32_16x16x32_bf16 v[64:67], v[192:195], v[156:159], v[64:67]\n\t"
            "v_mfma_f32_16x16x32_bf16 v[76:79], v[192:195], v[160:163], v[76:79]\n\ts_waitcnt lgkmcnt(0)\n\ts_barrier\n\t"
            ST(9)
            "ds_read_b64_tr_b16 v[176:177], v12 offset:0\n\tds_read_b64_tr_b16 v[178:179], v12 offset:512\n\tv_mfma_f32_16x16x32_bf16 v[88:91], v[192:195], v[164:167], v[88:91]\n\tds_read_b128 v[120:123], v6\n\tv_mfma_f32_16x16x32_bf16 v[100:103], v[192:195], v[168:171], v[100:103]\n\tds_read_b128 v[124:127], v2 offset:2048\n\t"
            "v_mfma_f32_16x16x32_bf16 v[112:115], v[192:195], v[172:175], v[112:115]\n\tds_read_b128 v[128:131], v2 offset:4096\n\tv_mfma_f32_16x16x32_bf16 v[44:47], v[148:151], v[196:199], v[44:47]\n\tds_read_b128 v[132:135], v2 offset:6144\n\tv_mfma_f32_16x16x32_bf16 v[56:59], v[152:155], v[196:199], v[56:59]\n\tds_read_b128 v[136:139], v2 offset:8192\n\t"
            "v_mfma_f32_16x16x32_bf16 v[68:71], v[156:159], v[196:199], v[68:71]\n\tds_read_b128 v[140:143], v2 offset:10240\n\tv_mfma_f32_16x16x32_bf16 v[80:83], v[160:163], v[196:199], v[80:83]\n\tds_read_b128 v[144:147], v2 offset:12288\n\tv_mfma_f32_16x16x32_bf16 v[92:95], v[164:167], v[196:199], v[92:95]\n\tds_read_b64_tr_b16 v[180:181], v12 offset:1024\n\t"
            "ds_read_b64_tr_b16 v[182:183], v12 offset:1536\n\tv_mfma_f32_16x16x32_bf16 v[104:107], v[168:171], v[196:199], v[104:107]\n\tds_read_b64_tr_b16 v[184:185], v12 offset:2048\n\tds_read_b64_tr_b16 v[186:187], v12 offset:2560\n\tv_mfma_f32_16x16x32_bf16 v[116:119], v[172:175], v[196:199], v[116:119]\n\ts_waitcnt lgkmcnt(10)\n\t"
            "v_mfma_f32_16x16x32_bf16 v[36:39], v[176:179], v[120:123], v[36:39]\n\ts_waitcnt lgkmcnt(9)\n\tv_mfma_f32_16x16x32_bf16 v[48:51], v[176:179], v[124:127], v[48:51]\n\ts_waitcnt lgkmcnt(8)\n\tv_mfma_f32_16x16x32_bf16 v[60:63], v[176:179], v[128:131], v[60:63]\n\tds_read_b64_tr_b16 v[188:189], v12 offset:12288\n\t"
            "ds_read_b64_tr_b16 v[190:191], v12 offset:12800\n\ts_waitcnt lgkmcnt(9)\n\tv_mfma_f32_16x16x32_bf16 v[72:75], v[176:179], v[132:135], v[72:75]\n\tds_read_b128 v[148:151], v7\n\ts_waitcnt lgkmcnt(9)\n\tv_mfma_f32_16x16x32_bf16 v[84:87], v[176:179], v[136:139], v[84:87]\n\t"
            "ds_read_b128 v[152:155], v3 offset:2048\n\ts_waitcnt lgkmcnt(9)\n\tv_mfma_f32_16x16x32_bf16 v[96:99], v[176:179], v[140:143], v[96:99]\n\tds_read_b128 v[156:159], v3 offset:4096\n\ts_waitcnt lgkmcnt(9)\n\tv_mfma_f32_16x16x32_bf16 v[108:111], v[176:179], v[144:147], v[108:111]\n\t"
            "ds_read_b128 v[160:163], v3 offset:6144\n\ts_waitcnt lgkmcnt(8)\n\tv_mfma_f32_16x16x32_bf16 v[40:43], v[180:183], v[120:123], v[40:43]\n\tds_read_b128 v[164:167], v3 offset:8192\n\tv_mfma_f32_16x16x32_bf16 v[52:55], v[180:183], v[124:127], v[52:55]\n\tds_read_b128 v[168:171], v3 offset:10240\n\t"
            "v_mfma_f32_16x16x32_bf16 v[64:67], v[180:183], v[128:131], v[64:67]\n\tds_read_b128 v[172:175], v3 offset:12288\n\tv_mfma_f32_16x16x32_bf16 v[76:79], v[180:183], v[132:135], v[76:79]\n\tds_read_b64_tr_b16 v[192:193], v12 offset:13312\n\tds_read_b64_tr_b16 v[194:195], v12 offset:13824\n\tv_mfma_f32_16x16x32_bf16 v[88:91], v[180:183], v[136:139], v[88:91]\n\t"
            "ds_read_b64_tr_b16 v[196:197], v12 offset:14336\n\tds_read_b64_tr_b16 v[198:199], v12 offset:14848\n\tv_mfma_f32_16x16x32_bf16 v[100:103], v[180:183], v[140:143], v[100:103]\n\tv_mfma_f32_16x16x32_bf16 v[112:115], v[180:183], v[144:147], v[112:115]\n\ts_waitcnt lgkmcnt(13)\n\tv_mfma_f32_16x16x32_bf16 v[44:47], v[120:123], v[184:187], v[44:47]\n\t"
            "v_mfma_f32_16x16x32_bf16 v[56:59], v[124:127], v[184:187], v[56:59]\n\tv_mfma_f32_16x16x32_bf16 v[68:71], v[128:131], v[184:187], v[68:71]\n\tv_mfma_f32_16x16x32_bf16 v[80:83], v[132:135], v[184:187], v[80:83]\n\tv_mfma_f32_16x16x32_bf16 v[92:95], v[136:139], v[184:187], v[92:95]\n\tv_mfma_f32_16x16x32_bf16 v[104:107], v[140:143], v[184:187], v[104:107]\n\tv_mfma_f32_16x16x32_bf16 v[116:119], v[144:147], v[184:187], v[116:119]\n\t"
            "s_waitcnt lgkmcnt(10)\n\tv_mfma_f32_16x16x32_bf16 v[36:39], v[188:191], v[148:151], v[36:39]\n\ts_waitcnt lgkmcnt(9)\n\tv_mfma_f32_16x16x32_bf16 v[48:51], v[188:191], v[152:155], v[48:51]\n\ts_waitcnt lgkmcnt(8)\n\tv_mfma_f32_16x16x32_bf16 v[60:63], v[188:191], v[156:159], v[60:63]\n\t"
            "s_waitcnt lgkmcnt(7)\n\tv_mfma_f32_16x16x32_bf16 v[72:75], v[188:191], v[160:163], v[72:75]\n\ts_waitcnt lgkmcnt(6)\n\tv_mfma_f32_16x16x32_bf16 v[84:87], v[188:191], v[164:167], v[84:87]\n\ts_waitcnt lgkmcnt(5)\n\tv_mfma_f32_16x16x32_bf16 v[96:99], v[188:191], v[168:171], v[96:99]\n\t"
            "s_waitcnt lgkmcnt(4)\n\tv_mfma_f32_16x16x32_bf16 v[108:111], v[188:191], v[172:175], v[108:111]\n\ts_waitcnt lgkmcnt(2)\n\tv_mfma_f32_16x16x32_bf16 v[40:43], v[192:195], v[148:151], v[40:43]\n\tv_mfma_f32_16x16x32_bf16 v[52:55], v[192:195], v[152:155], v[52:55]\n\tv_mfma_f32_16x16x32_bf16 v[64:67], v[192:195], v[156:159], v[64:67]\n\t"
            "v_mfma_f32_16x16x32_bf16 v[76:79], v[192:195], v[160:163], v[76:79]\n\ts_waitcnt lgkmcnt(0)\n\ts_barrier\n\t"
            ST(11)
            "ds_read_b64_tr_b16 v[176:177], v13 offset:0\n\tds_read_b64_tr_b16 v[178:179], v13 offset:512\n\tv_mfma_f32_16x16x32_bf16 v[88:91], v[192:195], v[164:167], v[88:91]\n\tds_read_b128 v[120:123], v8\n\tv_mfma_f32_16x16x32_bf16 v[100:103], v[192:195], v[168:171], v[100:103]\n\tds_read_b128 v[124:127], v4 offset:2048\n\t"
            "v_mfma_f32_16x16x32_bf16 v[112:115], v[192:195], v[172:175], v[112:115]\n\tds_read_b128 v[128:131], v4 offset:4096\n\tv_mfma_f32_16x16x32_bf16 v[44:47], v[148:151], v[196:199], v[44:47]\n\tds_read_b128 v[132:135], v4 offset:6144\n\tv_mfma_f32_16x16x32_bf16 v[56:59], v[152:155], v[196:199], v[56:59]\n\tds_read_b128 v[136:139], v4 offset:8192\n\t"
            "v_mfma_f32_16x16x32_bf16 v[68:71], v[156:159], v[196:199], v[68:71]\n\tds_read_b128 v[140:143], v4 offset:10240\n\tv_mfma_f32_16x16x32_bf16 v[80:83], v[160:163], v[196:199], v[80:83]\n\tds_read_b128 v[144:147], v4 offset:12288\n\tv_mfma_f32_16x16x32_bf16 v[92:95], v[164:167], v[196:199], v[92:95]\n\tds_read_b64_tr_b16 v[180:181], v13 offset:1024\n\t"
            "ds_read_b64_tr_b16 v[182:183], v13 offset:1536\n\tv_mfma_f32_16x16x32_bf16 v[104:107], v[168:171], v[196:199], v[104:107]\n\tds_read_b64_tr_b16 v[184:185], v13 offset:2048\n\tds_read_b64_tr_b16 v[186:187], v13 offset:2560\n\tv_mfma_f32_16x16x32_bf16 v[116:119], v[172:175], v[196:199], v[116:119]\n\ts_waitcnt lgkmcnt(10)\n\t"
            "v_mfma_f32_16x16x32_bf16 v[36:39], v[176:179], v[120:123], v[36:39]\n\ts_waitcnt lgkmcnt(9)\n\tv_mfma_f32_16x16x32_bf16 v[48:51], v[176:179], v[124:127], v[48:51]\n\ts_waitcnt lgkmcnt(8)\n\tv_mfma_f32_16x16x32_bf16 v[60:63], v[176:179], v[128:131], v[60:63]\n\tds_read_b64_tr_b16 v[188:189], v13 offset:12288\n\t"
            "ds_read_b64_tr_b16 v[190:191], v13 offset:12800\n\ts_waitcnt lgkmcnt(9)\n\tv_mfma_f32_16x16x32_bf16 v[72:75], v[176:179], v[132:135], v[72:75]\n\tds_read_b128 v[148:151], v9\n\ts_waitcnt lgkmcnt(9)\n\tv_mfma_f32_16x16x32_bf16 v[84:87], v[176:179], v[136:139], v[84:87]\n\t"
            "ds_read_b128 v[152:155], v5 offset:2048\n\ts_waitcnt lgkmcnt(9)\n\tv_mfma_f32_16x16x32_bf16 v[96:99], v[176:179], v[140:143], v[96:99]\n\tds_read_b128 v[156:159], v5 offset:4096\n\ts_waitcnt lgkmcnt(9)\n\tv_mfma_f32_16x16x32_bf16 v[108:111], v[176:179], v[144:147], v[108:111]\n\t"
            "ds_read_b128 v[160:163], v5 offset:6144\n\ts_waitcnt lgkmcnt(8)\n\tv_mfma_f32_16x16x32_bf16 v[40:43], v[180:183], v[120:123], v[40:43]\n\tds_read_b128 v[164:167], v5 offset:8192\n\tv_mfma_f32_16x16x32_bf16 v[52:55], v[180:183], v[124:127], v[52:55]\n\tds_read_b128 v[168:171], v5 offset:10240\n\t"
            "v_mfma_f32_16x16x32_bf16 v[64:67], v[180:183], v[128:131], v[64:67]\n\tds_read_b128 v[172:175], v5 offset:12288\n\tv_mfma_f32_16x16x32_bf16 v[76:79], v[180:183], v[132:135], v[76:79]\n\tds_read_b64_tr_b16 v[192:193], v13 offset:13312\n\tds_read_b64_tr_b16 v[194:195], v13 offset:13824\n\tv_mfma_f32_16x16x32_bf16 v[88:91], v[180:183], v[136:139], v[88:91]\n\t"
            "ds_read_b64_tr_b16 v[196:197], v13 offset:14336\n\tds_read_b64_tr_b16 v[198:199], v13 offset:14848\n\tv_mfma_f32_16x16x32_bf16 v[100:103], v[180:183], v[140:143], v[100:103]\n\tv_mfma_f32_16x16x32_bf16 v[112:115], v[180:183], v[144:147], v[112:115]\n\ts_waitcnt lgkmcnt(13)\n\tv_mfma_f32_16x16x32_bf16 v[44:47], v[120:123], v[184:187], v[44:47]\n\t"
            "v_mfma_f32_16x16x32_bf16 v[56:59], v[124:127], v[184:187], v[56:59]\n\tv_mfma_f32_16x16x32_bf16 v[68:71], v[128:131], v[184:187], v[68:71]\n\tv_mfma_f32_16x16x32_bf16 v[80:83], v[132:135], v[184:187], v[80:83]\n\tv_mfma_f32_16x16x32_bf16 v[92:95], v[136:139], v[184:187], v[92:95]\n\tv_mfma_f32_16x16x32_bf16 v[104:107], v[140:143], v[184:187], v[104:107]\n\tv_mfma_f32_16x16x32_bf16 v[116:119], v[144:147], v[184:187], v[116:119]\n\t"
            "s_waitcnt lgkmcnt(10)\n\tv_mfma_f32_16x16x32_bf16 v[36:39], v[188:191], v[148:151], v[36:39]\n\ts_waitcnt lgkmcnt(9)\n\tv_mfma_f32_16x16x32_bf16 v[48:51], v[188:191], v[152:155], v[48:51]\n\ts_waitcnt lgkmcnt(8)\n\tv_mfma_f32_16x16x32_bf16 v[60:63], v[188:191], v[156:159], v[60:63]\n\t"
            "s_waitcnt lgkmcnt(7)\n\tv_mfma_f32_16x16x32_bf16 v[72:75], v[188:191], v[160:163], v[72:75]\n\ts_waitcnt lgkmcnt(6)\n\tv_mfma_f32_16x16x32_bf16 v[84:87], v[188:191], v[164:167], v[84:87]\n\ts_waitcnt lgkmcnt(5)\n\tv_mfma_f32_16x16x32_bf16 v[96:99], v[188:191], v[168:171], v[96:99]\n\t"
            "s_waitcnt lgkmcnt(4)\n\tv_mfma_f32_16x16x32_bf16 v[108:111], v[188:191], v[172:175], v[108:111]\n\ts_waitcnt lgkmcnt(2)\n\tv_mfma_f32_16x16x32_bf16 v[40:43], v[192:195], v[148:151], v[40:43]\n\tv_mfma_f32_16x16x32_bf16 v[52:55], v[192:195], v[152:155], v[52:55]\n\tv_mfma_f32_16x16x32_bf16 v[64:67], v[192:195], v[156:159], v[64:67]\n\t"
            "v_mfma_f32_16x16x32_bf16 v[76:79], v[192:195], v[160:163], v[76:79]\n\ts_waitcnt lgkmcnt(0)\n\ts_barrier\n\t"
            ST(13)
            "ds_read_b64_tr_b16 v[176:177], v13 offset:38912\n\tds_read_b64_tr_b16 v[178:179], v13 offset:39424\n\tv_mfma_f32_16x16x32_bf16 v[88:91], v[192:195], v[164:167], v[88:91]\n\tds_read_b128 v[120:123], v10\n\tv_mfma_f32_16x16x32_bf16 v[100:103], v[192:195], v[168:171], v[100:103]\n\tds_read_b128 v[124:127], v4 offset:40960\n\t"
            "v_mfma_f32_16x16x32_bf16 v[112:115], v[192:195], v[172:175], v[112:115]\n\tds_read_b128 v[128:131], v4 offset:43008\n\tv_mfma_f32_16x16x32_bf16 v[44:47], v[148:151], v[196:199], v[44:47]\n\tds_read_b128 v[132:135], v4 offset:45056\n\tv_mfma_f32_16x16x32_bf16 v[56:59], v[152:155], v[196:199], v[56:59]\n\tds_read_b128 v[136:139], v4 offset:47104\n\t"
            "v_mfma_f32_16x16x32_bf16 v[68:71], v[156:159], v[196:199], v[68:71]\n\tds_read_b128 v[140:143], v4 offset:49152\n\tv_mfma_f32_16x16x32_bf16 v[80:83], v[160:163], v[196:199], v[80:83]\n\tds_read_b128 v[144:147], v4 offset:51200\n\tv_mfma_f32_16x16x32_bf16 v[92:95], v[164:167], v[196:199], v[92:95]\n\tds_read_b64_tr_b16 v[180:181], v13 offset:39936\n\t"
            "ds_read_b64_tr_b16 v[182:183], v13 offset:40448\n\tv_mfma_f32_16x16x32_bf16 v[104:107], v[168:171], v[196:199], v[104:107]\n\tds_read_b64_tr_b16 v[184:185], v13 offset:40960\n\tds_read_b64_tr_b16 v[186:187], v13 offset:41472\n\tv_mfma_f32_16x16x32_bf16 v[116:119], v[172:175], v[196:199], v[116:119]\n\ts_waitcnt lgkmcnt(10)\n\t"
            "v_mfma_f32_16x16x32_bf16 v[36:39], v[176:179], v[120:123], v[36:39]\n\ts_waitcnt lgkmcnt(9)\n\tv_mfma_f32_16x16x32_bf16 v[48:51], v[176:179], v[124:127], v[48:51]\n\ts_waitcnt lgkmcnt(8)\n\tv_mfma_f32_16x16x32_bf16 v[60:63], v[176:179], v[128:131], v[60:63]\n\tds_read_b64_tr_b16 v[188:189], v13 offset:51200\n\t"
            "ds_read_b64_tr_b16 v[190:191], v13 offset:51712\n\ts_waitcnt lgkmcnt(9)\n\tv_mfma_f32_16x16x32_bf16 v[72:75], v[176:179], v[132:135], v[72:75]\n\tds_read_b128 v[148:151], v11\n\ts_waitcnt lgkmcnt(9)\n\tv_mfma_f32_16x16x32_bf16 v[84:87], v[176:179], v[136:139], v[84:87]\n\t"
            "ds_read_b128 v[152:155], v5 offset:40960\n\ts_waitcnt lgkmcnt(9)\n\tv_mfma_f32_16x16x32_bf16 v[96:99], v[176:179], v[140:143], v[96:99]\n\tds_read_b128 v[156:159], v5 offset:43008\n\ts_waitcnt lgkmcnt(9)\n\tv_mfma_f32_16x16x32_bf16 v[108:111], v[176:179], v[144:147], v[108:111]\n\t"
            "ds_read_b128 v[160:163], v5 offset:45056\n\ts_waitcnt lgkmcnt(8)\n\tv_mfma_f32_16x16x32_bf16 v[40:43], v[180:183], v[120:123], v[40:43]\n\tds_read_b128 v[164:167], v5 offset:47104\n\tv_mfma_f32_16x16x32_bf16 v[52:55], v[180:183], v[124:127], v[52:55]\n\tds_read_b128 v[168:171], v5 offset:49152\n\t"
            "v_mfma_f32_16x16x32_bf16 v[64:67], v[180:183], v[128:131], v[64:67]\n\tds_read_b128 v[172:175], v5 offset:51200\n\tv_mfma_f32_16x16x32_bf16 v[76:79], v[180:183], v[132:135], v[76:79]\n\tds_read_b64_tr_b16 v[192:193], v13 offset:52224\n\tds_read_b64_tr_b16 v[194:195], v13 offset:52736\n\tv_mfma_f32_16x16x32_bf16 v[88:91], v[180:183], v[136:139], v[88:91]\n\t"
            "ds_read_b64_tr_b16 v[196:197], v13 offset:53248\n\tds_read_b64_tr_b16 v[198:199], v13 offset:53760\n\tv_mfma_f32_16x16x32_bf16 v[100:103], v[180:183], v[140:143], v[100:103]\n\tv_mfma_f32_16x16x32_bf16 v[112:115], v[180:183], v[144:147], v[112:115]\n\ts_waitcnt lgkmcnt(13)\n\tv_mfma_f32_16x16x32_bf16 v[44:47], v[120:123], v[184:187], v[44:47]\n\t"
            "v_mfma_f32_16x16x32_bf16 v[56:59], v[124:127], v[184:187], v[56:59]\n\tv_mfma_f32_16x16x32_bf16 v[68:71], v[128:131], v[184:187], v[68:71]\n\tv_mfma_f32_16x16x32_bf16 v[80:83], v[132:135], v[184:187], v[80:83]\n\tv_mfma_f32_16x16x32_bf16 v[92:95], v[136:139], v[184:187], v[92:95]\n\tv_mfma_f32_16x16x32_bf16 v[104:107], v[140:143], v[184:187], v[104:107]\n\tv_mfma_f32_16x16x32_bf16 v[116:119], v[144:147], v[184:187], v[116:119]\n\t"
            "s_waitcnt lgkmcnt(10)\n\tv_mfma_f32_16x16x32_bf16 v[36:39], v[188:191], v[148:151], v[36:39]\n\ts_waitcnt lgkmcnt(9)\n\tv_mfma_f32_16x16x32_bf16 v[48:51], v[188:191], v[152:155], v[48:51]\n\ts_waitcnt lgkmcnt(8)\n\tv_mfma_f32_16x16x32_bf16 v[60:63], v[188:191], v[156:159], v[60:63]\n\t"
            "s_waitcnt lgkmcnt(7)\n\tv_mfma_f32_16x16x32_bf16 v[72:75], v[188:191], v[160:163], v[72:75]\n\ts_waitcnt lgkmcnt(6)\n\tv_mfma_f32_16x16x32_bf16 v[84:87], v[188:191], v[164:167], v[84:87]\n\ts_waitcnt lgkmcnt(5)\n\tv_mfma_f32_16x16x32_bf16 v[96:99], v[188:191], v[168:171], v[96:99]\n\t"
            "s_waitcnt lgkmcnt(4)\n\tv_mfma_f32_16x16x32_bf16 v[108:111], v[188:191], v[172:175], v[108:111]\n\ts_waitcnt lgkmcnt(2)\n\tv_mfma_f32_16x16x32_bf16 v[40:43], v[192:195], v[148:151], v[40:43]\n\tv_mfma_f32_16x16x32_bf16 v[52:55], v[192:195], v[152:155], v[52:55]\n\tv_mfma_f32_16x16x32_bf16 v[64:67], v[192:195], v[156:159], v[64:67]\n\t"
            "v_mfma_f32_16x16x32_bf16 v[76:79], v[192:195], v[160:163], v[76:79]\n\ts_waitcnt lgkmcnt(0)\n\ts_barrier\n\t"
            ST(15)
            "ds_read_b64_tr_b16 v[176:177], v12 offset:0\n\tds_read_b64_tr_b16 v[178:179], v12 offset:512\n\tv_mfma_f32_16x16x32_bf16 v[88:91], v[192:195], v[164:167], v[88:91]\n\tds_read_b128 v[120:123], v6\n\tv_mfma_f32_16x16x32_bf16 v[100:103], v[192:195], v[168:171], v[100:103]\n\tds_read_b128 v[124:127], v2 offset:2048\n\t"
            "v_mfma_f32_16x16x32_bf16 v[112:115], v[192:195], v[172:175], v[112:115]\n\tds_read_b128 v[128:131], v2 offset:4096\n\tv_mfma_f32_16x16x32_bf16 v[44:47], v[148:151], v[196:199], v[44:47]\n\tds_read_b128 v[132:135], v2 offset:6144\n\tv_mfma_f32_16x16x32_bf16 v[56:59], v[152:155], v[196:199], v[56:59]\n\tds_read_b128 v[136:139], v2 offset:8192\n\t"
            "v_mfma_f32_16x16x32_bf16 v[68:71], v[156:159], v[196:199], v[68:71]\n\tds_read_b128 v[140:143], v2 offset:10240\n\tv_mfma_f32_16x16x32_bf16 v[80:83], v[160:163], v[196:199], v[80:83]\n\tds_read_b128 v[144:147], v2 offset:12288\n\tv_mfma_f32_16x16x32_bf16 v[92:95], v[164:167], v[196:199], v[92:95]\n\tds_read_b64_tr_b16 v[180:181], v12 offset:1024\n\t"
            "ds_read_b64_tr_b16 v[182:183], v12 offset:1536\n\tv_mfma_f32_16x16x32_bf16 v[104:107], v[168:171], v[196:199], v[104:107]\n\tv_mfma_f32_16x16x32_bf16 v[116:119], v[172:175], v[196:199], v[116:119]\n\ts_waitcnt lgkmcnt(8)\n\tv_mfma_f32_16x16x32_bf16 v[36:39], v[176:179], v[120:123], v[36:39]\n\ts_waitcnt lgkmcnt(7)\n\t"
            "v_mfma_f32_16x16x32_bf16 v[48:51], v[176:179], v[124:127], v[48:51]\n\ts_waitcnt lgkmcnt(6)\n\tv_mfma_f32_16x16x32_bf16 v[60:63], v[176:179], v[128:131], v[60:63]\n\tds_read_b64_tr_b16 v[188:189], v12 offset:12288\n\tds_read_b64_tr_b16 v[190:191], v12 offset:12800\n\ts_waitcnt lgkmcnt(7)\n\t"
            "v_mfma_f32_16x16x32_bf16 v[72:75], v[176:179], v[132:135], v[72:75]\n\tds_read_b128 v[148:151], v7\n\ts_waitcnt lgkmcnt(7)\n\tv_mfma_f32_16x16x32_bf16 v[84:87], v[176:179], v[136:139], v[84:87]\n\tds_read_b128 v[152:155], v3 offset:2048\n\ts_waitcnt lgkmcnt(7)\n\t"
            "v_mfma_f32_16x16x32_bf16 v[96:99], v[176:179], v[140:143], v[96:99]\n\tds_read_b128 v[156:159], v3 offset:4096\n\ts_waitcnt lgkmcnt(7)\n\tv_mfma_f32_16x16x32_bf16 v[108:111], v[176:179], v[144:147], v[108:111]\n\tds_read_b128 v[160:163], v3 offset:6144\n\ts_waitcnt lgkmcnt(6)\n\t"
            "v_mfma_f32_16x16x32_bf16 v[40:43], v[180:183], v[120:123], v[40:43]\n\tds_read_b128 v[164:167], v3 offset:8192\n\tv_mfma_f32_16x16x32_bf16 v[52:55], v[180:183], v[124:127], v[52:55]\n\tds_read_b128 v[168:171], v3 offset:10240\n\tv_mfma_f32_16x16x32_bf16 v[64:67], v[180:183], v[128:131], v[64:67]\n\tds_read_b128 v[172:175], v3 offset:12288\n\t"
            "v_mfma_f32_16x16x32_bf16 v[76:79], v[180:183], v[132:135], v[76:79]\n\tds_read_b64_tr_b16 v[192:193], v12 offset:13312\n\tds_read_b64_tr_b16 v[194:195], v12 offset:13824\n\tv_mfma_f32_16x16x32_bf16 v[88:91], v[180:183], v[136:139], v[88:91]\n\tv_mfma_f32_16x16x32_bf16 v[100:103], v[180:183], v[140:143], v[100:103]\n\tv_mfma_f32_16x16x32_bf16 v[112:115], v[180:183], v[144:147], v[112:115]\n\t"
            "s_waitcnt lgkmcnt(8)\n\tv_mfma_f32_16x16x32_bf16 v[36:39], v[188:191], v[148:151], v[36:39]\n\ts_waitcnt lgkmcnt(7)\n\tv_mfma_f32_16x16x32_bf16 v[48:51], v[188:191], v[152:155], v[48:51]\n\ts_waitcnt lgkmcnt(6)\n\tv_mfma_f32_16x16x32_bf16 v[60:63], v[188:191], v[156:159], v[60:63]\n\t"
            "s_waitcnt lgkmcnt(5)\n\tv_mfma_f32_16x16x32_bf16 v[72:75], v[188:191], v[160:163], v[72:75]\n\ts_waitcnt lgkmcnt(0)\n\ts_barrier\n\t"
            ST(17)
            "ds_read_b64_tr_b16 v[176:177], v13 offset:0\n\tds_read_b64_tr_b16 v[178:179], v13 offset:512\n\tv_mfma_f32_16x16x32_bf16 v[84:87], v[188:191], v[164:167], v[84:87]\n\tds_read_b128 v[120:123], v8\n\tv_mfma_f32_16x16x32_bf16 v[96:99], v[188:191], v[168:171], v[96:99]\n\tds_read_b128 v[124:127], v4 offset:2048\n\t"
            "v_mfma_f32_16x16x32_bf16 v[108:111], v[188:191], v[172:175], v[108:111]\n\tds_read_b128 v[128:131], v4 offset:4096\n\tv_mfma_f32_16x16x32_bf16 v[40:43], v[192:195], v[148:151], v[40:43]\n\tds_read_b128 v[132:135], v4 offset:6144\n\tv_mfma_f32_16x16x32_bf16 v[52:55], v[192:195], v[152:155], v[52:55]\n\tds_read_b128 v[136:139], v4 offset:8192\n\t"
            "v_mfma_f32_16x16x32_bf16 v[64:67], v[192:195], v[156:159], v[64:67]\n\tds_read_b128 v[140:143], v4 offset:10240\n\tv_mfma_f32_16x16x32_bf16 v[76:79], v[192:195], v[160:163], v[76:79]\n\tds_read_b128 v[144:147], v4 offset:12288\n\tv_mfma_f32_16x16x32_bf16 v[88:91], v[192:195], v[164:167], v[88:91]\n\tds_read_b64_tr_b16 v[180:181], v13 offset:1024\n\t"
            "ds_read_b64_tr_b16 v[182:183], v13 offset:1536\n\tv_mfma_f32_16x16x32_bf16 v[100:103], v[192:195], v[168:171], v[100:103]\n\tds_read_b64_tr_b16 v[184:185], v13 offset:2048\n\tds_read_b64_tr_b16 v[186:187], v13 offset:2560\n\tv_mfma_f32_16x16x32_bf16 v[112:115], v[192:195], v[172:175], v[112:115]\n\tds_read2_b32 v[208:209], v14 offset1:16\n\t"
            SA(2) PRIA
            "s_mov_b64 vcc, s[18:19]\n\ts_bitcmp1_b32 9, s69\n\tds_read_b64_tr_b16 v[188:189], v13 offset:12288\n\tv_mov_b32 v252, -1\n\tds_read_b64_tr_b16 v[190:191], v13 offset:12800\n\tv_mov_b32 v254, -1\n\t"
            "ds_read_b128 v[148:151], v9\n\ts_waitcnt lgkmcnt(6)\n\tv_mfma_f32_16x16x32_bf16 v[40:43], v[180:183], v[120:123], v[40:43]\n\tds_read_b128 v[152:155], v5 offset:2048\n\tv_mfma_f32_16x16x32_bf16 v[36:39], v[176:179], v[120:123], v[36:39]\n\tds_read_b128 v[156:159], v5 offset:4096\n\t"
            "v_mfma_f32_16x16x32_bf16 v[48:51], v[176:179], v[124:127], v[48:51]\n\tds_read_b128 v[160:163], v5 offset:6144\n\tv_mfma_f32_16x16x32_bf16 v[60:63], v[176:179], v[128:131], v[60:63]\n\tds_read_b128 v[164:167], v5 offset:8192\n\tv_mfma_f32_16x16x32_bf16 v[52:55], v[180:183], v[124:127], v[52:55]\n\tds_read_b128 v[168:171], v5 offset:10240\n\t"
            "v_mfma_f32_16x16x32_bf16 v[64:67], v[180:183], v[128:131], v[64:67]\n\tds_read_b128 v[172:175], v5 offset:12288\n\tv_mfma_f32_16x16x32_bf16 v[76:79], v[180:183], v[132:135], v[76:79]\n\tds_read_b64_tr_b16 v[192:193], v13 offset:13312\n\ts_waitcnt lgkmcnt(7)\n\tv_mfma_f32_16x16x32_bf16 v[36:39], v[188:191], v[148:151], v[36:39]\n\t"
            "ds_read_b64_tr_b16 v[194:195], v13 offset:13824\n\tds_read_b64_tr_b16 v[196:197], v13 offset:14336\n\ts_waitcnt lgkmcnt(8)\n\tv_mfma_f32_16x16x32_bf16 v[48:51], v[188:191], v[152:155], v[48:51]\n\tds_read_b64_tr_b16 v[198:199], v13 offset:14848\n\tds_read2_b32 v[210:211], v14 offset0:32 offset1:48\n\t"
            "v_mfma_f32_16x16x32_bf16 v[72:75], v[176:179], v[132:135], v[72:75]\n\ts_nop 0\n\tv_mov_b32_dpp v200, v36 row_newbcast:0 row_mask:0xf bank_mask:0xf bound_ctrl:1\n\tds_read2_b32 v[212:213], v14 offset0:64 offset1:80\n\ts_waitcnt lgkmcnt(9)\n\tv_mfma_f32_16x16x32_bf16 v[60:63], v[188:191], v[156:159], v[60:63]\n\t"
            "v_mov_b32_dpp v201, v37 row_newbcast:0 row_mask:0xf bank_mask:0xf bound_ctrl:1\n\tds_read_b32 v214, v14 offset:384\n\tv_mfma_f32_16x16x32_bf16 v[72:75], v[188:191], v[160:163], v[72:75]\n\tv_mov_b32_dpp v202, v38 row_newbcast:0 row_mask:0xf bank_mask:0xf bound_ctrl:1\n\tv_mov_b32_dpp v203, v39 row_newbcast:0 row_mask:0xf bank_mask:0xf bound_ctrl:1\n\tv_fmac_f32 v36, v208, v200\n\t"
            "v_fmac_f32 v37, v208, v201\n\tv_fmac_f32 v38, v208, v202\n\tv_fmac_f32 v39, v208, v203\n\tv_cvt_pk_bf16_f32 v250, v36, v37\n\tv_cvt_pk_bf16_f32 v251, v38, v39\n\tv_and_b32 v36, v250, v18\n\t"
            "v_and_b32 v37, v251, v18\n\ts_waitcnt lgkmcnt(2)\n\tv_mfma_f32_16x16x32_bf16 v[40:43], v[192:195], v[148:151], v[40:43]\n\tv_and_b32 v38, v250, v19\n\tv_and_b32 v39, v251, v19\n\tv_mfma_f32_16x16x32_bf16 v[52:55], v[192:195], v[152:155], v[52:55]\n\t"
            "v_and_b32 v222, v250, v20\n\tv_and_b32 v223, v251, v20\n\tv_mfma_f32_16x16x32_bf16 v[64:67], v[192:195], v[156:159], v[64:67]\n\ts_nop 1\n\tv_mov_b32_dpp v204, v40 row_newbcast:0 row_mask:0xf bank_mask:0xf bound_ctrl:1\n\tv_mov_b32_dpp v205, v41 row_newbcast:0 row_mask:0xf bank_mask:0xf bound_ctrl:1\n\t"
            "v_mfma_f32_16x16x32_bf16 v[76:79], v[192:195], v[160:163], v[76:79]\n\tv_mov_b32_dpp v206, v42 row_newbcast:0 row_mask:0xf bank_mask:0xf bound_ctrl:1\n\tv_mov_b32_dpp v207, v43 row_newbcast:0 row_mask:0xf bank_mask:0xf bound_ctrl:1\n\tv_fmac_f32 v40, v208, v204\n\tv_fmac_f32 v41, v208, v205\n\tv_fmac_f32 v42, v208, v206\n\t"
            "v_fmac_f32 v43, v208, v207\n\tv_cvt_pk_bf16_f32 v40, v40, v41\n\tv_cvt_pk_bf16_f32 v41, v42, v43\n\tv_cndmask_b32 v42, v40, v40, vcc\n\tv_cndmask_b32 v43, v41, v41, vcc\n\tv_and_b32 v224, v250, v21\n\t"
            "v_and_b32 v225, v251, v21\n\tv_mfma_f32_16x16x32_bf16 v[218:221], v[40:43], v[36:39], 0\n\tv_fmac_f32 v48, v209, v200\n\tv_fmac_f32 v49, v209, v201\n\tv_mfma_f32_16x16x32_bf16 v[226:229], v[40:43], v[222:225], 0\n\tv_fmac_f32 v50, v209, v202\n\t"
            "v_fmac_f32 v51, v209, v203\n\tv_cvt_pk_bf16_f32 v250, v48, v49\n\ts_nop 1\n\tv_cndmask_b32 v36, v220, v218, s[14:15]\n\tv_cndmask_b32 v37, v221, v219, s[16:17]\n\tv_cvt_pk_bf16_f32 v251, v50, v51\n\t"
            "v_and_b32 v48, v250, v18\n\tv_cndmask_b32 v38, v228, v226, s[14:15]\n\tv_cndmask_b32 v39, v229, v227, s[16:17]\n\tv_and_b32 v49, v251, v18\n\tv_and_b32 v50, v250, v19\n\tv_and_b32 v51, v251, v19\n\t"
            "v_and_b32 v234, v250, v20\n\tv_and_b32 v235, v251, v20\n\tv_and_b32 v236, v250, v21\n\tv_and_b32 v237, v251, v21\n\tv_fmac_f32 v60, v210, v200\n\tv_fmac_f32 v61, v210, v201\n\t"
            "v_fmac_f32 v62, v210, v202\n\tv_fmac_f32 v63, v210, v203\n\tv_fmac_f32 v52, v209, v204\n\tv_fmac_f32 v53, v209, v205\n\tv_fmac_f32 v64, v210, v204\n\tv_fmac_f32 v65, v210, v205\n\t"
            "v_cvt_pk_bf16_f32 v250, v60, v61\n\tv_cvt_pk_bf16_f32 v251, v62, v63\n\tv_fmac_f32 v76, v211, v204\n\tv_fmac_f32 v77, v211, v205\n\tv_fmac_f32 v54, v209, v206\n\tv_fmac_f32 v55, v209, v207\n\t"
            "v_cvt_pk_bf16_f32 v52, v52, v53\n\tv_fmac_f32 v66, v210, v206\n\tv_fmac_f32 v67, v210, v207\n\tv_cvt_pk_bf16_f32 v64, v64, v65\n\tv_and_b32 v60, v250, v18\n\tv_and_b32 v61, v251, v18\n\t"
            "v_and_b32 v62, v250, v19\n\tv_and_b32 v63, v251, v19\n\tv_and_b32 v246, v250, v20\n\tv_and_b32 v247, v251, v20\n\tv_and_b32 v248, v250, v21\n\tv_and_b32 v249, v251, v21\n\t"
            "v_fmac_f32 v78, v211, v206\n\tv_fmac_f32 v79, v211, v207\n\tv_fmac_f32 v72, v211, v200\n\tv_fmac_f32 v73, v211, v201\n\tv_fmac_f32 v74, v211, v202\n\tv_fmac_f32 v75, v211, v203\n\t"
            "v_cvt_pk_bf16_f32 v76, v76, v77\n\tv_cvt_pk_bf16_f32 v53, v54, v55\n\tv_cvt_pk_bf16_f32 v65, v66, v67\n\tv_cvt_pk_bf16_f32 v77, v78, v79\n\tv_cvt_pk_bf16_f32 v250, v72, v73\n\tv_cvt_pk_bf16_f32 v251, v74, v75\n\t"
            "v_cndmask_b32 v54, v40, v52, vcc\n\tv_cndmask_b32 v55, v41, v53, vcc\n\tv_cndmask_b32 v66, v52, v64, vcc\n\tv_cndmask_b32 v67, v53, v65, vcc\n\tv_mfma_f32_16x16x32_bf16 v[230:233], v[52:55], v[48:51], 0\n\tv_cndmask_b32 v78, v64, v76, vcc\n\t"
            "v_cndmask_b32 v79, v65, v77, vcc\n\tv_mfma_f32_16x16x32_bf16 v[242:245], v[64:67], v[60:63], 0\n\tv_and_b32 v72, v250, v18\n\tv_and_b32 v73, v251, v18\n\tv_mfma_f32_16x16x32_bf16 v[238:241], v[52:55], v[234:237], 0\n\tv_and_b32 v74, v250, v19\n\t"
            "v_and_b32 v75, v251, v19\n\tv_mfma_f32_16x16x32_bf16 v[218:221], v[64:67], v[246:249], 0\n\tv_cndmask_b32 v48, v232, v230, s[14:15]\n\tv_cndmask_b32 v49, v233, v231, s[16:17]\n\tv_mfma_f32_16x16x32_bf16 v[222:225], v[76:79], v[72:75], 0\n\tv_cndmask_b32 v60, v244, v242, s[14:15]\n\t"
            "v_cndmask_b32 v61, v245, v243, s[16:17]\n\tv_mfma_f32_16x16x32_bf16 v[84:87], v[176:179], v[136:139], v[84:87]\n\tv_cndmask_b32 v50, v240, v238, s[14:15]\n\tv_cndmask_b32 v51, v241, v239, s[16:17]\n\tv_mfma_f32_16x16x32_bf16 v[96:99], v[176:179], v[140:143], v[96:99]\n\ts_nop 1\n\t"
            "v_cndmask_b32 v72, v224, v222, s[14:15]\n\tv_cndmask_b32 v73, v225, v223, s[16:17]\n\tv_mfma_f32_16x16x32_bf16 v[88:91], v[180:183], v[136:139], v[88:91]\n\ts_cbranch_scc0 1f\n\tv_cvt_pk_bf16_f32 v253, v36, v37\n\tv_min_u32 v253, -2, v253\n\t"
            "v_cvt_pk_bf16_f32 v255, v48, v49\n\tv_min_u32 v255, -2, v255\n\tbuffer_store_dwordx4 v[252:255], v17, s[44:47], s20 offen sc1\n\ts_nop 1\n\tv_cvt_pk_bf16_f32 v253, v60, v61\n\tv_min_u32 v253, -2, v253\n\t"
            "v_cvt_pk_bf16_f32 v255, v72, v73\n\tv_min_u32 v255, -2, v255\n\tbuffer_store_dwordx4 v[252:255], v17, s[44:47], s21 offen sc1\n\t1:\nv_cndmask_b32 v62, v220, v218, s[14:15]\n\tv_mfma_f32_16x16x32_bf16 v[100:103], v[180:183], v[140:143], v[100:103]\n\t"
            "v_cndmask_b32 v63, v221, v219, s[16:17]\n\tv_and_b32 v226, v250, v20\n\tv_mfma_f32_16x16x32_bf16 v[112:115], v[180:183], v[144:147], v[112:115]\n\tv_and_b32 v227, v251, v20\n\tv_and_b32 v228, v250, v21\n\tv_mfma_f32_16x16x32_bf16 v[84:87], v[188:191], v[164:167], v[84:87]\n\t"
            "v_and_b32 v229, v251, v21\n\tv_mov_b32 v180, 0\n\tv_mfma_f32_16x16x32_bf16 v[108:111], v[176:179], v[144:147], v[108:111]\n\tv_mov_b32 v176, 0\n\tds_write_b64 v28, v[36:37] offset:0\n\tv_mfma_f32_16x16x32_bf16 v[230:233], v[76:79], v[226:229], 0\n\t"
            "s_waitcnt lgkmcnt(1)\n\ts_nop 0\n\tv_fmac_f32 v84, v212, v200\n\tv_fmac_f32 v85, v212, v201\n\tv_mfma_f32_16x16x32_bf16 v[96:99], v[188:191], v[168:171], v[96:99]\n\tds_write_b64 v29, v[38:39] offset:0\n\t"
            "v_fmac_f32 v86, v212, v202\n\tv_mfma_f32_16x16x32_bf16 v[88:91], v[192:195], v[164:167], v[88:91]\n\tv_cndmask_b32 v74, v232, v230, s[14:15]\n\tv_cndmask_b32 v75, v233, v231, s[16:17]\n\tds_write_b64 v28, v[48:49] offset:512\n\tv_mfma_f32_16x16x32_bf16 v[100:103], v[192:195], v[168:171], v[100:103]\n\t"
            "v_fmac_f32 v87, v212, v203\n\tv_cvt_pk_bf16_f32 v250, v84, v85\n\tv_mfma_f32_16x16x32_bf16 v[112:115], v[192:195], v[172:175], v[112:115]\n\tds_write_b64 v29, v[50:51] offset:512\n\tv_cvt_pk_bf16_f32 v251, v86, v87\n\tv_mfma_f32_16x16x32_bf16 v[108:111], v[188:191], v[172:175], v[108:111]\n\t"
            "v_and_b32 v84, v250, v18\n\tv_and_b32 v85, v251, v18\n\tds_write_b64 v28, v[60:61] offset:1024\n\tv_and_b32 v86, v250, v19\n\tv_and_b32 v87, v251, v19\n\tv_and_b32 v238, v250, v20\n\t"
            "v_and_b32 v239, v251, v20\n\tv_and_b32 v240, v250, v21\n\tds_write_b64 v29, v[62:63] offset:1024\n\tv_and_b32 v241, v251, v21\n\tv_fmac_f32 v96, v213, v200\n\tv_fmac_f32 v97, v213, v201\n\t"
            "v_fmac_f32 v98, v213, v202\n\tv_fmac_f32 v99, v213, v203\n\tds_write_b64 v28, v[72:73] offset:1536\n\tv_fmac_f32 v88, v212, v204\n\tv_fmac_f32 v89, v212, v205\n\tv_fmac_f32 v100, v213, v204\n\t"
            "v_fmac_f32 v101, v213, v205\n\tv_cvt_pk_bf16_f32 v250, v96, v97\n\tds_write_b64 v29, v[74:75] offset:1536\n\tv_cvt_pk_bf16_f32 v251, v98, v99\n\tv_fmac_f32 v112, v214, v204\n\tv_fmac_f32 v113, v214, v205\n\t"
            "v_fmac_f32 v90, v212, v206\n\tv_fmac_f32 v91, v212, v207\n\tds_read_b128 v[36:39], v15 offset:0\n\tv_cvt_pk_bf16_f32 v88, v88, v89\n\tv_fmac_f32 v102, v213, v206\n\tv_fmac_f32 v103, v213, v207\n\t"
            "ds_read_b128 v[48:51], v15 offset:64\n\tv_cvt_pk_bf16_f32 v100, v100, v101\n\ts_waitcnt lgkmcnt(0)\n\ts_barrier\n\tv_and_b32 v96, v250, v18\n\tds_read_b128 v[60:63], v15 offset:128\n\t"
            "v_and_b32 v97, v251, v18\n\tv_and_b32 v98, v250, v19\n\tv_and_b32 v99, v251, v19\n\tds_read_b128 v[72:75], v15 offset:192\n\tv_and_b32 v218, v250, v20\n\tv_and_b32 v219, v251, v20\n\t"
            "v_and_b32 v220, v250, v21\n\tv_and_b32 v221, v251, v21\n\tv_fmac_f32 v114, v214, v206\n\tv_fmac_f32 v115, v214, v207\n\tv_fmac_f32 v108, v214, v200\n\tv_fmac_f32 v109, v214, v201\n\t"
            "v_fmac_f32 v110, v214, v202\n\tv_fmac_f32 v111, v214, v203\n\tv_cvt_pk_bf16_f32 v112, v112, v113\n\tv_cvt_pk_bf16_f32 v89, v90, v91\n\tv_cvt_pk_bf16_f32 v101, v102, v103\n\tv_cvt_pk_bf16_f32 v113, v114, v115\n\t"
            "v_cvt_pk_bf16_f32 v250, v108, v109\n\tv_cvt_pk_bf16_f32 v251, v110, v111\n\tv_cndmask_b32 v90, v76, v88, vcc\n\tv_cndmask_b32 v91, v77, v89, vcc\n\tv_cndmask_b32 v102, v88, v100, vcc\n\tv_cndmask_b32 v103, v89, v101, vcc\n\t"
            "v_mfma_f32_16x16x32_bf16 v[234:237], v[88:91], v[84:87], 0\n\tv_cndmask_b32 v114, v100, v112, vcc\n\tv_cndmask_b32 v115, v101, v113, vcc\n\tv_mfma_f32_16x16x32_bf16 v[246:249], v[100:103], v[96:99], 0\n\tv_and_b32 v108, v250, v18\n\tv_and_b32 v109, v251, v18\n\t"
            "v_mfma_f32_16x16x32_bf16 v[242:245], v[88:91], v[238:241], 0\n\tv_and_b32 v110, v250, v19\n\tv_and_b32 v111, v251, v19\n\tv_mfma_f32_16x16x32_bf16 v[222:225], v[100:103], v[218:221], 0\n\tv_cndmask_b32 v84, v236, v234, s[14:15]\n\tv_cndmask_b32 v85, v237, v235, s[16:17]\n\t"
            "ds_write_b64 v28, v[84:85] offset:38912\n\tv_mfma_f32_16x16x32_bf16 v[226:229], v[112:115], v[108:111], 0\n\tv_cndmask_b32 v96, v248, v246, s[14:15]\n\tv_cndmask_b32 v97, v249, v247, s[16:17]\n\tv_cndmask_b32 v86, v244, v242, s[14:15]\n\tds_write_b64 v28, v[96:97] offset:39424\n\t"
            "v_cndmask_b32 v87, v245, v243, s[16:17]\n\tv_cndmask_b32 v98, v224, v222, s[14:15]\n\tv_cndmask_b32 v99, v225, v223, s[16:17]\n\ts_nop 0\n\tv_cndmask_b32 v108, v228, v226, s[14:15]\n\tv_cndmask_b32 v109, v229, v227, s[16:17]\n\t"
            "ds_write_b64 v29, v[86:87] offset:38912\n\ts_cbranch_scc0 1f\n\tv_cvt_pk_bf16_f32 v253, v84, v85\n\tv_min_u32 v253, -2, v253\n\tv_cvt_pk_bf16_f32 v255, v96, v97\n\tv_min_u32 v255, -2, v255\n\t"
            "buffer_store_dwordx4 v[252:255], v17, s[44:47], s22 offen sc1\n\ts_nop 1\n\tv_cvt_pk_bf16_f32 v253, v108, v109\n\tv_min_u32 v253, -2, v253\n\tbuffer_store_dwordx4 v[252:255], v17, s[44:47], s23 offen sc1\n\t1:\n"
            "v_and_b32 v230, v250, v20\n\tv_and_b32 v231, v251, v20\n\tv_and_b32 v232, v250, v21\n\tv_and_b32 v233, v251, v21\n\tds_write_b64 v29, v[98:99] offset:39424\n\tv_mfma_f32_16x16x32_bf16 v[44:47], v[120:123], v[184:187], v[44:47]\n\t"
            "v_mov_b32 v218, 0\n\tv_mov_b32 v219, 0\n\tv_mfma_f32_16x16x32_bf16 v[234:237], v[112:115], v[230:233], 0\n\tds_write_b64 v28, v[108:109] offset:39936\n\tv_mov_b32 v188, 0\n\tv_mfma_f32_16x16x32_bf16 v[56:59], v[124:127], v[184:187], v[56:59]\n\t"
            "v_mov_b32 v192, 0\n\tds_read_b128 v[120:123], v6\n\ts_nop 2\n\tv_cndmask_b32 v110, v236, v234, s[14:15]\n\tv_cndmask_b32 v111, v237, v235, s[16:17]\n\tv_mfma_f32_16x16x32_bf16 v[68:71], v[128:131], v[184:187], v[68:71]\n\t"
            "ds_write_b64 v29, v[110:111] offset:39936\n\tv_mov_b32 v234, 0\n\tv_mfma_f32_16x16x32_bf16 v[80:83], v[132:135], v[184:187], v[80:83]\n\tv_mov_b32 v235, 0\n\tds_write_b32 v33, v33\n\t"
            SA(3)
            "v_mfma_f32_16x16x32_bf16 v[92:95], v[136:139], v[184:187], v[92:95]\n\tds_read_b128 v[124:127], v2 offset:2048\n\tv_mfma_f32_16x16x32_bf16 v[104:107], v[140:143], v[184:187], v[104:107]\n\tds_read_b128 v[128:131], v2 offset:4096\n\tv_mfma_f32_16x16x32_bf16 v[116:119], v[144:147], v[184:187], v[116:119]\n\tds_read_b64_tr_b16 v[184:185], v12 offset:2048\n\t"
            "v_mfma_f32_16x16x32_bf16 v[44:47], v[148:151], v[196:199], v[44:47]\n\tds_read_b64_tr_b16 v[186:187], v12 offset:2560\n\tds_read_b128 v[148:151], v7\n\tv_mfma_f32_16x16x32_bf16 v[56:59], v[152:155], v[196:199], v[56:59]\n\tds_read_b128 v[152:155], v3 offset:2048\n\tv_mfma_f32_16x16x32_bf16 v[68:71], v[156:159], v[196:199], v[68:71]\n\t"
            "ds_read_b128 v[132:135], v2 offset:6144\n\tv_mfma_f32_16x16x32_bf16 v[80:83], v[160:163], v[196:199], v[80:83]\n\tds_read_b128 v[156:159], v3 offset:4096\n\tv_mfma_f32_16x16x32_bf16 v[92:95], v[164:167], v[196:199], v[92:95]\n\tds_read_b128 v[160:163], v3 offset:6144\n\tv_mfma_f32_16x16x32_bf16 v[104:107], v[168:171], v[196:199], v[104:107]\n\t"
            "ds_read_b128 v[136:139], v2 offset:8192\n\tv_mfma_f32_16x16x32_bf16 v[116:119], v[172:175], v[196:199], v[116:119]\n\tds_read_b64_tr_b16 v[196:197], v12 offset:14336\n\tds_read_b64_tr_b16 v[198:199], v12 offset:14848\n\ts_waitcnt lgkmcnt(8)\n\tv_mfma_f32_16x16x32_bf16 v[44:47], v[120:123], v[184:187], v[44:47]\n\t"
            "ds_read_b128 v[140:143], v2 offset:10240\n\tv_mfma_f32_16x16x32_bf16 v[56:59], v[124:127], v[184:187], v[56:59]\n\tds_read_b128 v[144:147], v2 offset:12288\n\tv_mfma_f32_16x16x32_bf16 v[68:71], v[128:131], v[184:187], v[68:71]\n\tds_read_b128 v[164:167], v3 offset:8192\n\ts_waitcnt lgkmcnt(8)\n\t"
            "v_mfma_f32_16x16x32_bf16 v[80:83], v[132:135], v[184:187], v[80:83]\n\tds_read_b128 v[168:171], v3 offset:10240\n\tds_read_b128 v[172:175], v3 offset:12288\n\ts_waitcnt lgkmcnt(7)\n\tv_mfma_f32_16x16x32_bf16 v[92:95], v[136:139], v[184:187], v[92:95]\n\tds_read_b128 v[84:87], v15 offset:256\n\t"
            "s_waitcnt lgkmcnt(6)\n\tv_mfma_f32_16x16x32_bf16 v[44:47], v[148:151], v[196:199], v[44:47]\n\tds_read_b128 v[96:99], v15 offset:320\n\tv_mfma_f32_16x16x32_bf16 v[56:59], v[152:155], v[196:199], v[56:59]\n\tds_read_b128 v[108:111], v15 offset:384\n\tv_mfma_f32_16x16x32_bf16 v[68:71], v[156:159], v[196:199], v[68:71]\n\t"
            "s_nop 3\n\tv_mov_b32 v216, v44\n\tv_mov_b32 v217, v44\n\tv_mfma_f32_16x16x32_bf16 v[80:83], v[160:163], v[196:199], v[80:83]\n\ts_nop 0\n\tv_permlane32_swap_b32 v216, v217\n\t"
            "v_mov_b32 v217, v216\n\ts_waitcnt lgkmcnt(5)\n\tv_mfma_f32_16x16x32_bf16 v[104:107], v[140:143], v[184:187], v[104:107]\n\tv_permlane16_swap_b32 v216, v217\n\tv_fmac_f32 v44, v36, v216\n\tv_mfma_f32_16x16x32_bf16 v[116:119], v[144:147], v[184:187], v[116:119]\n\t"
            "v_fmac_f32 v45, v37, v216\n\tv_fmac_f32 v46, v38, v216\n\tv_fmac_f32 v47, v39, v216\n\tv_cvt_pk_bf16_f32 v44, v44, v45\n\tv_cvt_pk_bf16_f32 v45, v46, v47\n\tv_mfma_f32_16x16x32_bf16 v[92:95], v[164:167], v[196:199], v[92:95]\n\t"
            "v_and_b32 v220, v44, v22\n\tv_and_b32 v221, v45, v22\n\ts_waitcnt lgkmcnt(0)\n\tv_mfma_f32_16x16x32_bf16 v[104:107], v[168:171], v[196:199], v[104:107]\n\tv_and_b32 v236, v44, v23\n\tv_and_b32 v237, v45, v23\n\t"
            "v_mfma_f32_16x16x32_bf16 v[116:119], v[172:175], v[196:199], v[116:119]\n\tv_fmac_f32 v56, v48, v216\n\tv_fmac_f32 v57, v49, v216\n\tv_fmac_f32 v58, v50, v216\n\tv_fmac_f32 v59, v51, v216\n\tv_cvt_pk_bf16_f32 v56, v56, v57\n\t"
            "v_cvt_pk_bf16_f32 v57, v58, v59\n\tv_and_b32 v222, v56, v22\n\tv_and_b32 v223, v57, v22\n\tv_and_b32 v238, v56, v23\n\tv_and_b32 v239, v57, v23\n\tv_fmac_f32 v68, v60, v216\n\t"
            "v_fmac_f32 v69, v61, v216\n\tv_fmac_f32 v70, v62, v216\n\tv_fmac_f32 v71, v63, v216\n\tds_read_b32 v27, v32\n\tds_read2st64_b32 v[42:43], v31 offset0:0 offset1:1\n\tds_read2st64_b32 v[40:41], v30 offset0:0 offset1:1\n\t"
            "ds_read2st64_b32 v[66:67], v31 offset0:2 offset1:3\n\tds_read2st64_b32 v[64:65], v30 offset0:2 offset1:3\n\tv_cvt_pk_bf16_f32 v68, v68, v69\n\tv_cvt_pk_bf16_f32 v69, v70, v71\n\tv_and_b32 v224, v68, v22\n\tv_and_b32 v225, v69, v22\n\t"
            "v_and_b32 v240, v68, v23\n\tv_and_b32 v241, v69, v23\n\tv_fmac_f32 v80, v72, v216\n\tv_fmac_f32 v81, v73, v216\n\tv_fmac_f32 v82, v74, v216\n\tv_fmac_f32 v83, v75, v216\n\t"
            "v_cvt_pk_bf16_f32 v80, v80, v81\n\tv_cvt_pk_bf16_f32 v81, v82, v83\n\tv_and_b32 v226, v80, v22\n\tv_and_b32 v227, v81, v22\n\tv_and_b32 v242, v80, v23\n\tv_and_b32 v243, v81, v23\n\t"
            SA(6)
            "s_waitcnt lgkmcnt(2)\n\tv_cmp_ge_u32 vcc, 0, v27\n\ts_cbranch_vccnz 10f\n\t20:\nv_and_b32 v177, v42, v24\n\tv_and_b32 v178, v42, v25\n\t"
            "v_and_b32 v179, v42, v26\n\tv_and_b32 v189, v43, v24\n\tv_and_b32 v190, v43, v25\n\tv_and_b32 v191, v43, v26\n\tv_mfma_f32_16x16x32_bf16 v[44:47], v[234:237], v[176:179], 0\n\tv_and_b32 v181, v40, v24\n\t"
            "v_and_b32 v182, v40, v25\n\tv_mfma_f32_16x16x32_bf16 v[56:59], v[236:239], v[188:191], 0\n\tv_and_b32 v183, v40, v26\n\tv_and_b32 v193, v41, v24\n\tv_and_b32 v194, v41, v25\n\tv_and_b32 v195, v41, v26\n\t"
            "v_mfma_f32_16x16x32_bf16 v[44:47], v[218:221], v[180:183], v[44:47]\n\ts_waitcnt lgkmcnt(0)\n\tv_and_b32 v177, v66, v24\n\tv_and_b32 v178, v66, v25\n\tv_mfma_f32_16x16x32_bf16 v[56:59], v[220:223], v[192:195], v[56:59]\n\tv_and_b32 v179, v66, v26\n\t"
            "v_and_b32 v189, v67, v24\n\tv_and_b32 v190, v67, v25\n\ts_nop 0\n\tv_cvt_pk_bf16_f32 v36, v44, v45\n\tv_cvt_pk_bf16_f32 v37, v46, v47\n\tv_min_u32 v36, -2, v36\n\t"
            "v_and_b32 v191, v67, v26\n\tv_cvt_pk_bf16_f32 v38, v56, v57\n\tv_cvt_pk_bf16_f32 v39, v58, v59\n\tv_min_u32 v38, -2, v38\n\tv_mfma_f32_16x16x32_bf16 v[68:71], v[238:241], v[176:179], 0\n\ts_nop 0\n\t"
            "v_permlane16_swap_b32 v36, v38\n\tv_permlane16_swap_b32 v37, v39\n\tbuffer_store_dwordx4 v[36:39], v16, s[44:47], s20 offen sc1\n\tv_mfma_f32_16x16x32_bf16 v[80:83], v[240:243], v[188:191], 0\n\tv_and_b32 v181, v64, v24\n\tv_and_b32 v182, v64, v25\n\t"
            "v_and_b32 v183, v64, v26\n\tv_and_b32 v193, v65, v24\n\tv_and_b32 v194, v65, v25\n\tv_and_b32 v195, v65, v26\n\tv_mfma_f32_16x16x32_bf16 v[68:71], v[222:225], v[180:183], v[68:71]\n\tv_fmac_f32 v92, v84, v216\n\t"
            "v_fmac_f32 v93, v85, v216\n\tv_mfma_f32_16x16x32_bf16 v[80:83], v[224:227], v[192:195], v[80:83]\n\tv_fmac_f32 v94, v86, v216\n\tv_fmac_f32 v95, v87, v216\n\tv_cvt_pk_bf16_f32 v92, v92, v93\n\ts_nop 1\n\t"
            "v_cvt_pk_bf16_f32 v60, v68, v69\n\tv_cvt_pk_bf16_f32 v61, v70, v71\n\tv_min_u32 v60, -2, v60\n\tv_fmac_f32 v104, v96, v216\n\tv_cvt_pk_bf16_f32 v62, v80, v81\n\tv_cvt_pk_bf16_f32 v63, v82, v83\n\t"
            "v_min_u32 v62, -2, v62\n\tv_fmac_f32 v105, v97, v216\n\tv_permlane16_swap_b32 v61, v63\n\tv_permlane16_swap_b32 v60, v62\n\tbuffer_store_dwordx4 v[60:63], v16, s[44:47], s21 offen sc1\n\tds_write_b32 v34, v35\n\t"
            SA(4)
            "v_cvt_pk_bf16_f32 v93, v94, v95\n\tv_fmac_f32 v106, v98, v216\n\tv_fmac_f32 v107, v99, v216\n\tv_cvt_pk_bf16_f32 v104, v104, v105\n\tv_fmac_f32 v116, v108, v216\n\tv_fmac_f32 v117, v109, v216\n\t"
            "v_and_b32 v228, v92, v22\n\tds_read_b32 v27, v32\n\tds_read2st64_b32 v[102:103], v31 offset0:152 offset1:153\n\tds_read2st64_b32 v[100:101], v30 offset0:152 offset1:153\n\tds_read2st64_b32 v[78:79], v31 offset0:154 offset1:155\n\tds_read2st64_b32 v[76:77], v30 offset0:154 offset1:155\n\t"
            "v_and_b32 v229, v93, v22\n\tv_and_b32 v244, v92, v23\n\tv_and_b32 v245, v93, v23\n\tv_cvt_pk_bf16_f32 v105, v106, v107\n\tv_fmac_f32 v118, v110, v216\n\tv_fmac_f32 v119, v111, v216\n\t"
            "v_cvt_pk_bf16_f32 v116, v116, v117\n\tv_and_b32 v230, v104, v22\n\tv_and_b32 v231, v105, v22\n\tv_and_b32 v246, v104, v23\n\tv_and_b32 v247, v105, v23\n\tv_cvt_pk_bf16_f32 v117, v118, v119\n\t"
            "v_and_b32 v232, v116, v22\n\tv_and_b32 v233, v117, v22\n\tv_and_b32 v248, v116, v23\n\tv_and_b32 v249, v117, v23\n\ts_waitcnt lgkmcnt(3)\n\tv_cmp_ge_u32 vcc, 1, v27\n\t"
            "s_cbranch_vccnz 11f\n\t21:\nv_and_b32 v177, v102, v24\n\tv_and_b32 v178, v102, v25\n\tv_and_b32 v179, v102, v26\n\tv_and_b32 v189, v103, v24\n\t"
            "v_and_b32 v190, v103, v25\n\tv_and_b32 v191, v103, v26\n\tv_mfma_f32_16x16x32_bf16 v[92:95], v[242:245], v[176:179], 0\n\ts_waitcnt lgkmcnt(0)\n\tv_and_b32 v181, v100, v24\n\tv_and_b32 v182, v100, v25\n\t"
            "v_mfma_f32_16x16x32_bf16 v[104:107], v[244:247], v[188:191], 0\n\tv_and_b32 v183, v100, v26\n\tv_and_b32 v193, v101, v24\n\tv_and_b32 v194, v101, v25\n\tv_and_b32 v195, v101, v26\n\tv_mfma_f32_16x16x32_bf16 v[92:95], v[226:229], v[180:183], v[92:95]\n\t"
            "v_and_b32 v177, v78, v24\n\tv_and_b32 v178, v78, v25\n\tv_mfma_f32_16x16x32_bf16 v[104:107], v[228:231], v[192:195], v[104:107]\n\tv_and_b32 v179, v78, v26\n\tv_and_b32 v181, v76, v24\n\tv_and_b32 v182, v76, v25\n\t"
            "v_mfma_f32_16x16x32_bf16 v[116:119], v[246:249], v[176:179], 0\n\tv_and_b32 v183, v76, v26\n\tv_cvt_pk_bf16_f32 v84, v92, v93\n\ts_nop 1\n\tv_cvt_pk_bf16_f32 v86, v104, v105\n\tv_mfma_f32_16x16x32_bf16 v[116:119], v[230:233], v[180:183], v[116:119]\n\t"
            "v_cvt_pk_bf16_f32 v85, v94, v95\n\tv_min_u32 v84, -2, v84\n\tv_cvt_pk_bf16_f32 v87, v106, v107\n\tv_min_u32 v86, -2, v86\n\ts_nop 0\n\tv_permlane16_swap_b32 v85, v87\n\t"
            "v_permlane16_swap_b32 v84, v86\n\tbuffer_store_dwordx4 v[84:87], v16, s[44:47], s22 offen sc1\n\tv_cvt_pk_bf16_f32 v108, v116, v117\n\tv_cvt_pk_bf16_f32 v109, v118, v119\n\tv_min_u32 v108, -2, v108\n\ts_nop 0\n\t"
            "v_permlane16_swap_b32 v109, v111\n\tv_permlane16_swap_b32 v108, v110\n\tbuffer_store_dwordx4 v[108:111], v215, s[44:47], s23 offen sc1\n\tds_write_b32 v34, v34\n\t"
            SA(5)
            "s_setprio 0\n\t"
            DUMP
            "s_cmp_eq_u32 s69, 1\n\ts_cbranch_scc1 .Lout\n\ts_endpgm\n\t10:\nds_read_b32 v27, v32\n\tds_read2st64_b32 v[42:43], v31 offset0:0 offset1:1\n\t"
            "ds_read2st64_b32 v[40:41], v30 offset0:0 offset1:1\n\tds_read2st64_b32 v[66:67], v31 offset0:2 offset1:3\n\tds_read2st64_b32 v[64:65], v30 offset0:2 offset1:3\n\ts_waitcnt lgkmcnt(0)\n\tv_cmp_ge_u32 vcc, 0, v27\n\ts_cbranch_vccz 20b\n\t"
            ASLP
            "s_sub_u32 s68, s68, 1\n\ts_cbranch_scc0 10b\n\ts_branch 20b\n\t11:\nds_read_b32 v27, v32\n\tds_read2st64_b32 v[102:103], v31 offset0:152 offset1:153\n\t"
            "ds_read2st64_b32 v[100:101], v30 offset0:152 offset1:153\n\tds_read2st64_b32 v[78:79], v31 offset0:154 offset1:155\n\tds_read2st64_b32 v[76:77], v30 offset0:154 offset1:155\n\ts_waitcnt lgkmcnt(0)\n\tv_cmp_ge_u32 vcc, 1, v27\n\ts_cbranch_vccz 21b\n\t"
            ASLP
            "s_sub_u32 s68, s68, 1\n\ts_cbranch_scc0 11b\n\ts_branch 21b\n\t.LB:\ns_and_b32 s72, s56, 1\n\ts_lshr_b32 s73, s56, 1\n\t"
            "v_and_b32 v156, 2, v51\n\tv_lshlrev_b32 v156, 1, v156\n\tv_and_b32 v157, 7, v52\n\tv_xor_b32 v157, v156, v157\n\tv_mul_u32_u24 v156, 0x500, v51\n\tv_lshl_add_u32 v156, v157, 4, v156\n\t"
            "s_mul_i32 s74, s72, 0x2800\n\ts_mul_i32 s75, s73, 0xf000\n\ts_add_i32 s75, s75, s74\n\tv_add_u32 v154, s75, v156\n\ts_mul_i32 s75, s73, 0x5000\n\ts_add_i32 s75, s75, s74\n\t"
            "s_addk_i32 s75, 0x5000\n\tv_add_u32 v156, s75, v156\n\ts_cmp_lg_u32 s63, 0\n\ts_cselect_b32 s74, 81920, 0\n\ts_max_u32 s74, s74, 65536\n\tv_add_u32 v158, s62, v52\n\t"
            "v_min_u32 v159, 47, v52\n\ts_add_i32 s75, s62, 64\n\tv_add_u32 v159, s75, v159\n\tv_max_i32 v158, 0, v158\n\tv_min_i32 v159, 0x3ff, v159\n\tv_lshl_add_u32 v158, v158, 4, s74\n\t"
            "v_lshl_add_u32 v159, v159, 4, s74\n\tv_lshl_add_u32 v59, v53, 4, v57\n\ts_cmp_lg_u32 s73, 0\n\ts_cselect_b64 vcc, -1, 0\n\tv_cndmask_b32 v97, v59, v52, vcc\n\ts_lshl_b32 s75, s72, 6\n\t"
            "s_add_i32 s75, s75, s62\n\tv_add_u32 v97, s75, v97\n\tv_mov_b32 v60, s62\n\tv_add_u32 v60, 0x6f, v60\n\tv_min_i32 v60, 0x3ff, v60\n\tv_med3_i32 v97, v97, 0, v60\n\t"
            "s_lshl3_add_u32 s74, s73, s74\n\tv_lshl_add_u32 v97, v97, 4, s74\n\ts_mul_i32 s30, s73, 576\n\ts_lshl_b32 s75, s72, 8\n\ts_add_i32 s30, s30, s75\n\ts_add_i32 s30, s30, 160000\n\t"
            "s_lshl_b32 s74, s59, 7\n\ts_add_i32 s74, s74, 8192\n\tv_min_u32 v96, 9, v52\n\tv_lshl_add_u32 v96, v96, 2, s74\n\ts_lshr_b32 s75, s58, 5\n\ts_add_i32 s74, s74, s75\n\t"
            "s_cmp_eq_u32 s56, 3\n\ts_cselect_b32 s74, s74, 0x40000000\n\tv_lshl_add_u32 v98, v52, 24, s74\n\ts_cselect_b32 s75, 0, 0x40000000\n\tv_add_u32 v96, s75, v96\n\ts_cselect_b32 s75, 1, 0\n\t"
            "s_lshl2_add_u32 s74, s59, s56\n\ts_mul_i32 s74, s74, 0xa000\n\ts_add_i32 s74, s74, 155648\n\tv_lshl_add_u32 v101, v52, 4, s74\n\tv_and_b32 v60, s75, v53\n\tv_lshl_add_u32 v2, v60, 30, v101\n\t"
            "s_cmp_ge_u32 s56, s57\n\ts_addc_u32 s76, s56, 0\n\ts_and_b32 s16, s76, 3\n\ts_lshl_b32 s77, s76, 1\n\ts_max_u32 s78, s77, 1\n\ts_sub_u32 s78, s78, 1\n\t"
            "s_min_u32 s79, s77, 7\n\tv_lshlrev_b32 v60, 9, v53\n\tv_lshl_add_u32 v60, v54, 3, v60\n\ts_mul_i32 s80, s78, 0xc00\n\ts_mul_i32 s81, s79, 0xc00\n\ts_addk_i32 s80, 14336\n\t"
            "s_addk_i32 s81, 14336\n\tv_add_u32 v24, s80, v60\n\tv_add_u32 v26, s81, v60\n\tv_add_u32 v25, 38912, v24\n\tv_add_u32 v27, 38912, v26\n\tv_lshl_add_u32 v29, v52, 4, s80\n\t"
            "s_lshr_b32 s78, s78, 1\n\ts_lshr_b32 s79, s79, 1\n\ts_sub_u32 s79, s79, s78\n\ts_lshl2_add_u32 s78, s78, 159744\n\tv_and_b32 v28, s79, v52\n\tv_lshl_add_u32 v28, v28, 2, s78\n\t"
            "s_lshl2_add_u32 s78, s56, 159744\n\tv_mov_b32 v30, s78\n\ts_mov_b32 s78, 160576\n\tv_lshl_add_u32 v153, v59, 2, s78\n\ts_mul_i32 s79, s59, 163840\n\ts_lshl_b32 s80, s58, 5\n\t"
            "s_add_i32 s79, s79, s80\n\ts_cmp_eq_u32 s76, 0\n\ts_cselect_b32 s80, 0, 0xc00\n\ts_add_i32 s79, s79, s80\n\ts_add_i32 s79, s79, 155652\n\tv_lshl_add_u32 v31, v54, 4, s79\n\t"
            "v_lshrrev_b32 v60, 1, v53\n\tv_mul_u32_u24 v60, 0xa000, v60\n\tv_add_u32 v31, v60, v31\n\tv_and_b32 v60, 1, v53\n\tv_lshl_add_u32 v31, v60, 3, v31\n\ts_mov_b32 s31, 0x14000\n\t"
            "s_cmp_lt_i32 s62, 0\n\ts_cselect_b32 s79, 8, -16\n\tv_mov_b32 v61, 0xff800000\n\tv_mov_b32 v142, 1\n\tv_add_u32 v60, 0, v59\n\tv_and_b32 v60, -8, v60\n\t"
            "v_add_u32 v60, 0, v60\n\tv_cmp_gt_i32 vcc, s79, v60\n\tv_lshl_add_u32 v160, v60, 2, s78\n\ts_nop 0\n\tv_cndmask_b32 v128, 0, v61, vcc\n\tv_add_u32 v60, 4, v59\n\t"
            "v_and_b32 v60, -8, v60\n\tv_add_u32 v60, -4, v60\n\tv_cmp_gt_i32 vcc, s79, v60\n\tv_lshl_add_u32 v161, v60, 2, s78\n\ts_nop 0\n\tv_cndmask_b32 v129, 0, v61, vcc\n\t"
            "v_add_u32 v60, -1, v59\n\tv_and_b32 v60, -8, v60\n\tv_add_u32 v60, 1, v60\n\tv_cmp_gt_i32 vcc, s79, v60\n\tv_lshl_add_u32 v162, v60, 2, s78\n\ts_nop 0\n\t"
            "v_cndmask_b32 v130, 0, v61, vcc\n\tv_add_u32 v60, 3, v59\n\tv_and_b32 v60, -8, v60\n\tv_add_u32 v60, -3, v60\n\tv_cmp_gt_i32 vcc, s79, v60\n\tv_lshl_add_u32 v163, v60, 2, s78\n\t"
            "s_nop 0\n\tv_cndmask_b32 v131, 0, v61, vcc\n\tv_add_u32 v60, -2, v59\n\tv_and_b32 v60, -8, v60\n\tv_add_u32 v60, 2, v60\n\tv_cmp_gt_i32 vcc, s79, v60\n\t"
            "v_lshl_add_u32 v164, v60, 2, s78\n\ts_nop 0\n\tv_cndmask_b32 v132, 0, v61, vcc\n\tv_add_u32 v60, 2, v59\n\tv_and_b32 v60, -8, v60\n\tv_add_u32 v60, -2, v60\n\t"
            "v_cmp_gt_i32 vcc, s79, v60\n\tv_lshl_add_u32 v165, v60, 2, s78\n\ts_nop 0\n\tv_cndmask_b32 v133, 0, v61, vcc\n\tv_add_u32 v60, -3, v59\n\tv_and_b32 v60, -8, v60\n\t"
            "v_add_u32 v60, 3, v60\n\tv_cmp_gt_i32 vcc, s79, v60\n\tv_lshl_add_u32 v166, v60, 2, s78\n\ts_nop 0\n\tv_cndmask_b32 v134, 0, v61, vcc\n\tv_add_u32 v60, 1, v59\n\t"
            "v_and_b32 v60, -8, v60\n\tv_add_u32 v60, -1, v60\n\tv_cmp_gt_i32 vcc, s79, v60\n\tv_lshl_add_u32 v167, v60, 2, s78\n\ts_nop 0\n\tv_cndmask_b32 v135, 0, v61, vcc\n\t"
            "s_lshl2_add_u32 s80, s71, s56\n\ts_lshl_b32 s80, s80, 5\n\tv_add_u32 v100, s80, v52\n\tv_mul_u32_u24 v100, 0x500, v100\n\ts_add_i32 s80, s58, 3072\n\tv_add_u32 v100, s80, v100\n\t"
            "v_min_u32 v104, 51, v52\n\tv_lshlrev_b32 v104, 4, v104\n\tv_add_u32 v104, 0x1000, v104\n\ts_lshl_b32 s4, s64, 12\n\ts_add_u32 s5, s4, 0x1000\n\ts_cmpk_eq_u32 s5, 0x5000\n\t"
            "s_cselect_b32 s5, 0, s5\n\ts_add_u32 s6, s5, 0x1000\n\ts_cmpk_eq_u32 s6, 0x5000\n\ts_cselect_b32 s6, 0, s6\n\ts_add_u32 s7, s6, 0x1000\n\ts_cmpk_eq_u32 s7, 0x5000\n\t"
            "s_cselect_b32 s7, 0, s7\n\ts_add_u32 s8, s7, 0x1000\n\ts_cmpk_eq_u32 s8, 0x5000\n\ts_cselect_b32 s8, 0, s8\n\ts_cmpk_lt_u32 s58, 0x280\n\ts_cselect_b32 s80, 0, 0x5000\n\t"
            "s_xor_b32 s81, s80, 0x5000\n\ts_add_u32 s9, s4, s81\n\ts_add_u32 s4, s4, s80\n\ts_add_u32 s10, s5, s81\n\ts_add_u32 s5, s5, s80\n\ts_add_u32 s11, s6, s81\n\t"
            "s_add_u32 s6, s6, s80\n\ts_add_u32 s12, s7, s81\n\ts_add_u32 s7, s7, s80\n\ts_add_u32 s13, s8, s81\n\ts_add_u32 s8, s8, s80\n\ts_mul_i32 s20, s4, 20\n\t"
            "s_add_i32 s20, s20, s58\n\ts_addk_i32 s20, 3072\n\ts_mul_i32 s21, s5, 20\n\ts_add_i32 s21, s21, s58\n\ts_addk_i32 s21, 2048\n\ts_mul_i32 s22, s6, 20\n\t"
            "s_add_i32 s22, s22, s58\n\ts_addk_i32 s22, 1024\n\ts_mul_i32 s23, s7, 20\n\ts_add_i32 s23, s23, s58\n\ts_mul_i32 s24, s8, 20\n\ts_add_i32 s24, s24, s58\n\t"
            "s_addk_i32 s24, 3072\n\ts_mul_i32 s25, s9, 20\n\ts_add_i32 s25, s25, s58\n\ts_addk_i32 s25, 2048\n\ts_mul_i32 s26, s10, 20\n\ts_add_i32 s26, s26, s58\n\t"
            "s_addk_i32 s26, 1024\n\ts_mul_i32 s27, s11, 20\n\ts_add_i32 s27, s27, s58\n\ts_mul_i32 s28, s12, 20\n\ts_add_i32 s28, s28, s58\n\ts_addk_i32 s28, 3072\n\t"
            "s_mul_i32 s29, s13, 20\n\ts_add_i32 s29, s29, s58\n\ts_addk_i32 s29, 2048\n\ts_mul_i32 s72, s60, 1920\n\ts_sub_u32 s14, s61, s72\n\ts_mul_i32 s15, s56, 0x2800\n\t"
            "s_add_u32 s15, s15, 77824\n\tv_lshl_add_u32 v152, v52, 4, s15\n\ts_lshl_b32 s18, s58, 5\n\ts_mov_b32 s50, 822272\n\ts_mov_b32 s51, 0x20000\n\ts_waitcnt lgkmcnt(0)\n\t"
            "s_load_dword s67, s[44:45], 0x0\n\ts_sub_u32 s48, s48, 3072\n\ts_subb_u32 s49, s49, 0\n\ts_lshl_b32 s78, s56, 5\n\tv_add_u32 v12, s78, v57\n\tv_add_u32 v3, s62, v12\n\t"
            "v_and_b32 v13, 1, v53\n\tv_lshrrev_b32 v14, 1, v53\n\tv_lshl_add_u32 v15, v13, 1, v14\n\ts_add_u32 s79, s63, s58\n\tv_lshl_add_u32 v15, v15, 4, s79\n\ts_movk_i32 s79, 0xa00\n\t"
            "v_mad_i32_i24 v3, v3, s79, v15\n\tv_cmp_gt_u32 vcc, 8, v12\n\ts_cmp_eq_u32 s56, 3\n\ts_cselect_b32 s79, 2.0, 0xa000\n\tv_add_u32 v11, s79, v3\n\tv_cndmask_b32 v3, v3, 2.0, vcc\n\t"
            "v_mov_b32 v8, 159744\n\tv_and_b32 v9, 31, v52\n\tv_add_u32 v9, s78, v9\n\tv_lshl_add_u32 v9, v9, 2, v8\n\tv_lshl_add_u32 v10, v13, 4, v12\n\tv_lshl_add_u32 v10, v10, 2, v8\n\t"
            "v_mul_u32_u24 v4, 0x2800, v14\n\tv_lshl_add_u32 v4, v56, 7, v4\n\tv_lshl_add_u32 v4, v55, 3, v4\n\tv_lshl_add_u32 v4, v13, 5, v4\n\tv_bfe_u32 v5, v52, 3, 1\n\tv_lshl_add_u32 v4, v5, 6, v4\n\t"
            "v_add_u32 v4, 77824, v4\n\tv_xor_b32 v5, 64, v4\n\tv_add_u32 v6, 0x5000, v4\n\tv_add_u32 v7, 0x5000, v5\n\ts_add_u32 s78, s63, 1280\n\ts_mov_b32 s32, s60\n\t"
            "s_mov_b32 s83, 0\n\ts_add_u32 s33, s32, 256\n\ts_cmp_eq_u32 s33, s78\n\ts_cselect_b32 s33, s63, s33\n\ts_add_u32 s34, s33, 256\n\ts_cmp_eq_u32 s34, s78\n\t"
            "s_cselect_b32 s34, s63, s34\n\ts_add_u32 s35, s34, 256\n\ts_cmp_eq_u32 s35, s78\n\ts_cselect_b32 s35, s63, s35\n\ts_add_u32 s17, s35, 256\n\ts_cmp_eq_u32 s17, s78\n\t"
            "s_cselect_b32 s17, s63, s17\n\ts_add_u32 s80, s32, 128\n\ts_bitcmp1_b32 31, s64\n\ts_cselect_b32 s82, s34, s33\n\ts_bitcmp1_b32 0, s64\n\ts_cselect_b32 s82, s80, s82\n\t"
            "s_bitcmp1_b32 30, s64\n\ts_cselect_b32 s81, s35, s34\n\ts_bitcmp1_b32 1, s64\n\ts_cselect_b32 s81, s80, s81\n\ts_bitcmp1_b32 28, s64\n\ts_cselect_b32 s79, s17, s35\n\t"
            "s_bitcmp1_b32 2, s64\n\ts_cselect_b32 s79, s80, s79\n\ts_bitcmp1_b32 28, s64\n\ts_cselect_b32 s78, s80, s17\n\ts_waitcnt vmcnt(0) lgkmcnt(0)\n\ts_barrier\n\t"
            "v_mov_b32 v136, -1\n\tv_mov_b32 v137, -1\n\tv_mov_b32 v138, -1\n\tv_mov_b32 v139, -1\n\ts_mov_b32 s60, s82\n\ts_mul_i32 s61, s60, 1920\n\t"
            "s_add_u32 m0, s66, 0\n\ts_add_u32 s61, s61, s14\n\tbuffer_load_dwordx4 v40, s[40:43], s61 offen lds\n\tbuffer_load_dwordx4 v41, s[40:43], s61 offen offset:1024 lds\n\tbuffer_load_dwordx4 v42, s[40:43], s61 offen offset:2048 lds\n\tbuffer_load_dwordx4 v43, s[40:43], s61 offen offset:3072 lds\n\t"
            "s_addk_i32 m0, 0x1000\n\ts_nop 0\n\tbuffer_load_dwordx4 v44, s[40:43], s61 offen lds\n\tbuffer_load_dwordx4 v45, s[40:43], s61 offen offset:1024 lds\n\ts_add_u32 m0, s65, 0\n\ts_mov_b32 exec_hi, 0\n\t"
            "buffer_load_dwordx4 v39, s[36:39], s60 offen offset:3072 lds\n\ts_mov_b32 exec_hi, -1\n\tbuffer_load_dwordx4 v36, s[36:39], s60 offen lds\n\tbuffer_load_dwordx4 v37, s[36:39], s60 offen offset:1024 lds\n\tbuffer_load_dwordx4 v38, s[36:39], s60 offen offset:2048 lds\n\ts_mul_i32 s67, s67, 0x19660d\n\t"
            "s_add_u32 s67, s67, 0x3c6ef35f\n\ts_barrier\n\ts_mov_b32 s60, s81\n\ts_mul_i32 s61, s60, 1920\n\ts_add_u32 m0, s66, 38912\n\ts_add_u32 s61, s61, s14\n\t"
            "buffer_load_dwordx4 v40, s[40:43], s61 offen lds\n\tbuffer_load_dwordx4 v41, s[40:43], s61 offen offset:1024 lds\n\tbuffer_load_dwordx4 v42, s[40:43], s61 offen offset:2048 lds\n\tbuffer_load_dwordx4 v43, s[40:43], s61 offen offset:3072 lds\n\ts_addk_i32 m0, 0x1000\n\ts_nop 0\n\t"
            "buffer_load_dwordx4 v44, s[40:43], s61 offen lds\n\tbuffer_load_dwordx4 v45, s[40:43], s61 offen offset:1024 lds\n\ts_add_u32 m0, s65, 38912\n\ts_mov_b32 exec_hi, 0\n\tbuffer_load_dwordx4 v39, s[36:39], s60 offen offset:3072 lds\n\ts_mov_b32 exec_hi, -1\n\t"
            "buffer_load_dwordx4 v36, s[36:39], s60 offen lds\n\tbuffer_load_dwordx4 v37, s[36:39], s60 offen offset:1024 lds\n\tbuffer_load_dwordx4 v38, s[36:39], s60 offen offset:2048 lds\n\ts_bitcmp1_b32 13, s64\n\ts_cbranch_scc0 1f\n\tbuffer_store_dwordx4 v[136:139], v101, s[44:47], s18 offen offset:0 sc1\n\t"
            "buffer_store_dwordx4 v[136:139], v2, s[44:47], s18 offen offset:1024 sc1\n\tbuffer_store_dwordx4 v[136:139], v2, s[44:47], s18 offen offset:2048 sc1\n\tbuffer_store_dwordx4 v[136:139], v101, s[44:47], s18 offen offset:3072 sc1\n\t1:\ns_bitcmp1_b32 13, s64\n\ts_cbranch_scc1 1f\n\t"
            "s_waitcnt vmcnt(10)\n\t1:\ns_waitcnt vmcnt(14)\n\ts_barrier\n\ts_mov_b32 s60, s79\n\ts_mul_i32 s61, s60, 1920\n\t"
            "s_add_u32 m0, s66, 77824\n\ts_add_u32 s61, s61, s14\n\tbuffer_load_dwordx4 v40, s[40:43], s61 offen lds\n\tbuffer_load_dwordx4 v41, s[40:43], s61 offen offset:1024 lds\n\tbuffer_load_dwordx4 v42, s[40:43], s61 offen offset:2048 lds\n\tbuffer_load_dwordx4 v43, s[40:43], s61 offen offset:3072 lds\n\t"
            "s_addk_i32 m0, 0x1000\n\ts_nop 0\n\tbuffer_load_dwordx4 v44, s[40:43], s61 offen lds\n\tbuffer_load_dwordx4 v45, s[40:43], s61 offen offset:1024 lds\n\ts_add_u32 m0, s65, 77824\n\ts_mov_b32 exec_hi, 0\n\t"
            "buffer_load_dwordx4 v39, s[36:39], s60 offen offset:3072 lds\n\ts_mov_b32 exec_hi, -1\n\tbuffer_load_dwordx4 v36, s[36:39], s60 offen lds\n\tbuffer_load_dwordx4 v37, s[36:39], s60 offen offset:1024 lds\n\tbuffer_load_dwordx4 v38, s[36:39], s60 offen offset:2048 lds\n\ts_bitcmp1_b32 18, s64\n\t"
            "s_cbranch_scc0 1f\n\tbuffer_store_dwordx4 v[136:139], v101, s[44:47], s18 offen offset:0 sc1\n\tbuffer_store_dwordx4 v[136:139], v2, s[44:47], s18 offen offset:1024 sc1\n\tbuffer_store_dwordx4 v[136:139], v2, s[44:47], s18 offen offset:2048 sc1\n\tbuffer_store_dwordx4 v[136:139], v101, s[44:47], s18 offen offset:3072 sc1\n\t1:\n"
            "s_bitcmp1_b32 31, s64\n\ts_cbranch_scc1 1f\n\ts_waitcnt vmcnt(10)\n\t1:\ns_waitcnt vmcnt(14)\n\ts_barrier\n\t"
            "s_mov_b32 s60, s78\n\ts_mul_i32 s61, s60, 1920\n\ts_add_u32 m0, s66, 0\n\ts_add_u32 s61, s61, s14\n\tbuffer_load_dwordx4 v40, s[40:43], s61 offen lds\n\tbuffer_load_dwordx4 v41, s[40:43], s61 offen offset:1024 lds\n\t"
            "buffer_load_dwordx4 v42, s[40:43], s61 offen offset:2048 lds\n\tbuffer_load_dwordx4 v43, s[40:43], s61 offen offset:3072 lds\n\ts_addk_i32 m0, 0x1000\n\ts_nop 0\n\tbuffer_load_dwordx4 v44, s[40:43], s61 offen lds\n\tbuffer_load_dwordx4 v45, s[40:43], s61 offen offset:1024 lds\n\t"
            "s_add_u32 m0, s65, 0\n\ts_mov_b32 exec_hi, 0\n\tbuffer_load_dwordx4 v39, s[36:39], s60 offen offset:3072 lds\n\ts_mov_b32 exec_hi, -1\n\tbuffer_load_dwordx4 v36, s[36:39], s60 offen lds\n\tbuffer_load_dwordx4 v37, s[36:39], s60 offen offset:1024 lds\n\t"
            "buffer_load_dwordx4 v38, s[36:39], s60 offen offset:2048 lds\n\ts_waitcnt vmcnt(10)\n\ts_barrier\n\ts_add_u32 s60, s33, 128\n\ts_mul_i32 s61, s60, 1920\n\ts_add_u32 m0, s66, 38912\n\t"
            "s_add_u32 s61, s61, s14\n\tbuffer_load_dwordx4 v40, s[40:43], s61 offen lds\n\tbuffer_load_dwordx4 v41, s[40:43], s61 offen offset:1024 lds\n\tbuffer_load_dwordx4 v42, s[40:43], s61 offen offset:2048 lds\n\tbuffer_load_dwordx4 v43, s[40:43], s61 offen offset:3072 lds\n\ts_addk_i32 m0, 0x1000\n\t"
            "s_nop 0\n\tbuffer_load_dwordx4 v44, s[40:43], s61 offen lds\n\tbuffer_load_dwordx4 v45, s[40:43], s61 offen offset:1024 lds\n\ts_add_u32 m0, s65, 38912\n\ts_mov_b32 exec_hi, 0\n\tbuffer_load_dwordx4 v39, s[36:39], s60 offen offset:3072 lds\n\t"
            "s_mov_b32 exec_hi, -1\n\tbuffer_load_dwordx4 v36, s[36:39], s60 offen lds\n\tbuffer_load_dwordx4 v37, s[36:39], s60 offen offset:1024 lds\n\tbuffer_load_dwordx4 v38, s[36:39], s60 offen offset:2048 lds\n\tv_mov_b32 v140, s67\n\tbuffer_store_dword v140, v98, s[44:47], 0 offen sc1\n\t"
            "s_waitcnt vmcnt(11)\n\ts_barrier\n\ts_add_u32 s60, s34, 128\n\ts_mul_i32 s61, s60, 1920\n\ts_add_u32 m0, s66, 77824\n\ts_add_u32 s61, s61, s14\n\t"
            "buffer_load_dwordx4 v40, s[40:43], s61 offen lds\n\tbuffer_load_dwordx4 v41, s[40:43], s61 offen offset:1024 lds\n\tbuffer_load_dwordx4 v42, s[40:43], s61 offen offset:2048 lds\n\tbuffer_load_dwordx4 v43, s[40:43], s61 offen offset:3072 lds\n\ts_addk_i32 m0, 0x1000\n\ts_nop 0\n\t"
            "buffer_load_dwordx4 v44, s[40:43], s61 offen lds\n\tbuffer_load_dwordx4 v45, s[40:43], s61 offen offset:1024 lds\n\ts_add_u32 m0, s65, 77824\n\ts_mov_b32 exec_hi, 0\n\tbuffer_load_dwordx4 v39, s[36:39], s60 offen offset:3072 lds\n\ts_mov_b32 exec_hi, -1\n\t"
            "buffer_load_dwordx4 v36, s[36:39], s60 offen lds\n\tbuffer_load_dwordx4 v37, s[36:39], s60 offen offset:1024 lds\n\tbuffer_load_dwordx4 v38, s[36:39], s60 offen offset:2048 lds\n\ts_waitcnt vmcnt(11)\n\ts_cmp_lg_u32 s56, 3\n\ts_cbranch_scc1 1f\n\t"
            "buffer_load_dwordx4 v[106:109], v158, s[44:47], 0 offen sc1\n\tbuffer_load_dwordx4 v[110:113], v159, s[44:47], 0 offen sc1\n\tbuffer_load_dwordx4 v[114:117], v104, s[44:47], 0 offen sc1\n\t1:\ns_barrier\n\ts_add_u32 s60, s35, 128\n\t"
            "s_mul_i32 s61, s60, 1920\n\ts_add_u32 m0, s66, 0\n\ts_add_u32 s61, s61, s14\n\tbuffer_load_dwordx4 v40, s[40:43], s61 offen lds\n\tbuffer_load_dwordx4 v41, s[40:43], s61 offen offset:1024 lds\n\tbuffer_load_dwordx4 v42, s[40:43], s61 offen offset:2048 lds\n\t"
            "buffer_load_dwordx4 v43, s[40:43], s61 offen offset:3072 lds\n\ts_addk_i32 m0, 0x1000\n\ts_nop 0\n\tbuffer_load_dwordx4 v44, s[40:43], s61 offen lds\n\tbuffer_load_dwordx4 v45, s[40:43], s61 offen offset:1024 lds\n\ts_add_u32 m0, s65, 0\n\t"
            "s_mov_b32 exec_hi, 0\n\tbuffer_load_dwordx4 v39, s[36:39], s60 offen offset:3072 lds\n\ts_mov_b32 exec_hi, -1\n\tbuffer_load_dwordx4 v36, s[36:39], s60 offen lds\n\tbuffer_load_dwordx4 v37, s[36:39], s60 offen offset:1024 lds\n\tbuffer_load_dwordx4 v38, s[36:39], s60 offen offset:2048 lds\n\t"
            "s_cmp_lg_u32 s56, 3\n\ts_cbranch_scc1 2f\n\ts_waitcnt vmcnt(10)\n\ts_mov_b32 s68, 0x200000\n\tv_xor_b32 v106, s67, v107\n\tv_xor_b32 v108, s67, v109\n\t"
            "v_xor_b32 v110, s67, v111\n\tv_xor_b32 v112, s67, v113\n\tv_xor_b32 v114, s67, v115\n\tv_xor_b32 v116, s67, v117\n\tv_or3_b32 v106, v106, v108, v110\n\tv_or3_b32 v112, v112, v114, v116\n\t"
            "v_or_b32 v106, v106, v112\n\tv_cmp_ne_u32 vcc, 0, v106\n\ts_cbranch_vccnz 30f\n\t31:\n2:\ns_waitcnt vmcnt(10)\n\t"
            "s_barrier\n\ts_add_u32 s60, s17, 128\n\ts_mul_i32 s61, s60, 1920\n\ts_add_u32 m0, s66, 38912\n\ts_add_u32 s61, s61, s14\n\tbuffer_load_dwordx4 v40, s[40:43], s61 offen lds\n\t"
            "buffer_load_dwordx4 v41, s[40:43], s61 offen offset:1024 lds\n\tbuffer_load_dwordx4 v42, s[40:43], s61 offen offset:2048 lds\n\tbuffer_load_dwordx4 v43, s[40:43], s61 offen offset:3072 lds\n\ts_addk_i32 m0, 0x1000\n\ts_nop 0\n\tbuffer_load_dwordx4 v44, s[40:43], s61 offen lds\n\t"
            "buffer_load_dwordx4 v45, s[40:43], s61 offen offset:1024 lds\n\ts_add_u32 m0, s65, 38912\n\ts_mov_b32 exec_hi, 0\n\tbuffer_load_dwordx4 v39, s[36:39], s60 offen offset:3072 lds\n\ts_mov_b32 exec_hi, -1\n\tbuffer_load_dwordx4 v36, s[36:39], s60 offen lds\n\t"
            "buffer_load_dwordx4 v37, s[36:39], s60 offen offset:1024 lds\n\tbuffer_load_dwordx4 v38, s[36:39], s60 offen offset:2048 lds\n\ts_mov_b32 m0, s30\n\ts_nop 0\n\tbuffer_load_dword v97, s[44:47], 0 offen sc1 lds\n\tbuffer_load_dword v118, v96, s[44:47], 0 offen sc1\n\t"
            "s_waitcnt vmcnt(12)\n\ts_barrier\n\ts_add_u32 m0, s15, 40960\n\ts_nop 0\n\tbuffer_load_dwordx4 v156, s[48:51], s20 offen lds\n\tbuffer_load_dwordx4 v156, s[48:51], s21 offen offset:1024 lds\n\t"
            "buffer_load_dwordx4 v156, s[48:51], s22 offen offset:2048 lds\n\tbuffer_load_dwordx4 v156, s[48:51], s23 offen offset:3072 lds\n\ts_add_u32 m0, s15, 45056\n\ts_nop 0\n\tbuffer_load_dwordx4 v156, s[48:51], s24 offen lds\n\tbuffer_load_dwordx4 v156, s[48:51], s25 offen offset:1024 lds\n\t"
            "buffer_load_dwordx4 v156, s[48:51], s26 offen offset:2048 lds\n\ts_waitcnt vmcnt(7)\n\tv_cmp_ne_u32 vcc, s67, v118\n\ts_cmp_lg_u32 s56, 3\n\ts_cbranch_scc1 33f\n\ts_cbranch_vccnz 32f\n\t"
            "33:\ns_barrier\n\ts_add_u32 m0, s15, 45056\n\ts_nop 0\n\tbuffer_load_dwordx4 v156, s[48:51], s27 offen offset:3072 lds\n\ts_add_u32 m0, s15, 49152\n\t"
            "s_nop 0\n\tbuffer_load_dwordx4 v156, s[48:51], s28 offen lds\n\tbuffer_load_dwordx4 v156, s[48:51], s29 offen offset:1024 lds\n\ts_add_u32 m0, s15, 0\n\ts_nop 0\n\tbuffer_load_dwordx4 v154, s[48:51], s20 offen lds\n\t"
            "buffer_load_dwordx4 v154, s[48:51], s21 offen offset:1024 lds\n\tbuffer_load_dwordx4 v154, s[48:51], s22 offen offset:2048 lds\n\tbuffer_load_dwordx4 v154, s[48:51], s23 offen offset:3072 lds\n\ts_add_u32 m0, s15, 4096\n\ts_nop 0\n\tbuffer_load_dwordx4 v154, s[48:51], s24 offen lds\n\t"
            "buffer_load_dwordx4 v154, s[48:51], s25 offen offset:1024 lds\n\tbuffer_load_dwordx4 v154, s[48:51], s26 offen offset:2048 lds\n\tbuffer_load_dwordx4 v154, s[48:51], s27 offen offset:3072 lds\n\ts_add_u32 m0, s15, 8192\n\ts_nop 0\n\tbuffer_load_dwordx4 v154, s[48:51], s28 offen lds\n\t"
            "buffer_load_dwordx4 v154, s[48:51], s29 offen offset:1024 lds\n\ts_cmp_lg_u32 s56, 0\n\ts_cbranch_scc1 1f\n\ts_mov_b32 m0, 162624\n\ts_nop 0\n\tbuffer_load_dwordx4 v104, s[44:47], 0 offen sc1 lds\n\t"
            "1:\nbuffer_load_dwordx4 v[240:243], v3, s[36:39], 0 offen offset:0\n\tbuffer_load_dwordx4 v[244:247], v11, s[36:39], 0 offen offset:0\n\tbuffer_load_dwordx4 v[248:251], v3, s[36:39], 0 offen offset:64\n\tbuffer_load_dwordx4 v[252:255], v11, s[36:39], 0 offen offset:64\n\tds_read_b32 v20, v9 offset:256\n\t"
            "ds_read_b32 v21, v10 offset:832\n\tds_read2st64_b32 v[138:139], v153 offset1:1\n\tds_read2st64_b32 v[176:177], v160 offset1:1\n\tds_read2st64_b32 v[178:179], v161 offset1:1\n\tds_read2st64_b32 v[180:181], v162 offset1:1\n\tds_read2st64_b32 v[182:183], v163 offset1:1\n\t"
            "ds_read2st64_b32 v[184:185], v164 offset1:1\n\tds_read2st64_b32 v[186:187], v165 offset1:1\n\tds_read2st64_b32 v[188:189], v166 offset1:1\n\tds_read2st64_b32 v[190:191], v167 offset1:1\n\ts_mov_b32 s68, 0x200000\n\ts_waitcnt lgkmcnt(8)\n\t"
            "v_mul_f32 v138, 0x3ea52b5c, v138\n\tv_mul_f32 v139, 0x3ea52b5c, v139\n\ts_waitcnt lgkmcnt(7)\n\tv_mul_f32 v192, v176, v138\n\tv_mul_f32 v200, v177, v139\n\ts_waitcnt lgkmcnt(6)\n\t"
            "v_mul_f32 v193, v178, v138\n\tv_mul_f32 v201, v179, v139\n\ts_waitcnt lgkmcnt(5)\n\tv_mul_f32 v194, v180, v138\n\tv_mul_f32 v202, v181, v139\n\ts_waitcnt lgkmcnt(4)\n\t"
            "v_mul_f32 v195, v182, v138\n\tv_mul_f32 v203, v183, v139\n\ts_waitcnt lgkmcnt(3)\n\tv_mul_f32 v196, v184, v138\n\tv_mul_f32 v204, v185, v139\n\ts_waitcnt lgkmcnt(2)\n\t"
            "v_mul_f32 v197, v186, v138\n\tv_mul_f32 v205, v187, v139\n\ts_waitcnt lgkmcnt(1)\n\tv_mul_f32 v198, v188, v138\n\tv_mul_f32 v206, v189, v139\n\ts_waitcnt lgkmcnt(0)\n\t"
            "v_mul_f32 v199, v190, v138\n\tv_mul_f32 v207, v191, v139\n\ts_barrier\n\t"
            SB_(1) PRIS
            "50:\nds_read2_b64 v[208:211], v24 offset0:0 offset1:16\n\tds_read2_b64 v[212:215], v24 offset0:32 offset1:48\n\tds_read2_b64 v[216:219], v26 offset0:0 offset1:16\n\tds_read2_b64 v[220:223], v26 offset0:32 offset1:48\n\ts_cmp_eq_u32 s16, 0\n\t"
            "s_cbranch_scc1 56f\n\t58:\ns_cbranch_scc0 1f\n\tbuffer_load_dword v232, v31, s[44:47], s31 offen offset:0 sc1\n\tbuffer_load_dword v233, v31, s[44:47], s31 offen offset:256 sc1\n\tbuffer_load_dword v234, v31, s[44:47], s31 offen offset:512 sc1\n\t"
            "buffer_load_dword v235, v31, s[44:47], s31 offen offset:768 sc1\n\t1:\ns_waitcnt lgkmcnt(1)\n\tv_add_f32 v208, v208, v216\n\tv_add_f32 v209, v209, v217\n\tv_add_f32 v210, v210, v218\n\t"
            "v_add_f32 v211, v211, v219\n\ts_waitcnt lgkmcnt(0)\n\tv_add_f32 v212, v212, v220\n\tv_add_f32 v213, v213, v221\n\tv_add_f32 v214, v214, v222\n\tv_add_f32 v215, v215, v223\n\t"
            "v_fma_f32 v208, v208, v192, v128\n\tv_fma_f32 v209, v209, v193, v129\n\tv_fma_f32 v210, v210, v194, v130\n\tv_fma_f32 v211, v211, v195, v131\n\tv_fma_f32 v212, v212, v196, v132\n\tv_fma_f32 v213, v213, v197, v133\n\t"
            "v_fma_f32 v214, v214, v198, v134\n\tv_fma_f32 v215, v215, v199, v135\n\tv_max3_f32 v136, v208, v209, v210\n\tv_max3_f32 v137, v211, v212, v213\n\tv_max3_f32 v136, v136, v214, v215\n\tv_max_f32 v136, v136, v137\n\t"
            "v_sub_f32 v208, v208, v136\n\tv_sub_f32 v209, v209, v136\n\tv_sub_f32 v210, v210, v136\n\tv_sub_f32 v211, v211, v136\n\tv_sub_f32 v212, v212, v136\n\tv_sub_f32 v213, v213, v136\n\t"
            "v_sub_f32 v214, v214, v136\n\tv_sub_f32 v215, v215, v136\n\tv_exp_f32 v208, v208\n\tv_exp_f32 v209, v209\n\tv_exp_f32 v210, v210\n\tv_exp_f32 v211, v211\n\t"
            "v_exp_f32 v212, v212\n\tv_exp_f32 v213, v213\n\tv_exp_f32 v214, v214\n\tv_exp_f32 v215, v215\n\tv_add_f32 v216, v208, v209\n\tv_add_f32 v217, v210, v211\n\t"
            "v_add_f32 v218, v212, v213\n\tv_add_f32 v219, v214, v215\n\tv_add_f32 v216, v216, v217\n\tv_add_f32 v218, v218, v219\n\tv_mul_f32 v208, v208, v176\n\tv_add_f32 v216, v216, v218\n\t"
            "v_mul_f32 v216, 0xbfb8aa3b, v216\n\tv_rcp_f32 v216, v216\n\tv_mul_f32 v209, v209, v178\n\tv_mul_f32 v210, v210, v180\n\tv_mul_f32 v211, v211, v182\n\tv_mul_f32 v212, v212, v184\n\t"
            "v_mul_f32 v213, v213, v186\n\tv_mul_f32 v214, v214, v188\n\tv_mul_f32 v215, v215, v190\n\tv_mul_f32 v208, v208, v216\n\tv_mul_f32 v209, v209, v216\n\tv_mul_f32 v210, v210, v216\n\t"
            "v_mul_f32 v211, v211, v216\n\tv_mul_f32 v212, v212, v216\n\tv_mul_f32 v213, v213, v216\n\tv_mul_f32 v214, v214, v216\n\tv_mul_f32 v215, v215, v216\n\tv_cvt_pk_bf16_f32 v220, v208, v209\n\t"
            "v_cvt_pk_bf16_f32 v221, v210, v211\n\tv_cvt_pk_bf16_f32 v222, v212, v213\n\tv_cvt_pk_bf16_f32 v223, v214, v215\n\tds_write_b128 v29, v[220:223] offset:0\n\tds_write_b32 v30, v142 offset:16\n\t"
            SP0 PRIW
            "51:\nds_read_b32 v136, v28\n\tds_read2_b64 v[208:211], v25 offset0:0 offset1:16\n\tds_read2_b64 v[212:215], v25 offset0:32 offset1:48\n\tds_read2_b64 v[216:219], v27 offset0:0 offset1:16\n\tds_read2_b64 v[220:223], v27 offset0:32 offset1:48\n\t"
            "s_waitcnt lgkmcnt(4)\n\tv_cmp_eq_u32 vcc, 0, v136\n\ts_cbranch_vccnz 53f\n\t55:\n"
            PRI1
            "s_cmp_eq_u32 s16, 0\n\ts_cbranch_scc1 57f\n\t59:\ns_waitcnt lgkmcnt(1)\n\tv_add_f32 v208, v208, v216\n\tv_add_f32 v209, v209, v217\n\t"
            "v_add_f32 v210, v210, v218\n\tv_add_f32 v211, v211, v219\n\ts_waitcnt lgkmcnt(0)\n\tv_add_f32 v212, v212, v220\n\tv_add_f32 v213, v213, v221\n\tv_add_f32 v214, v214, v222\n\t"
            "v_add_f32 v215, v215, v223\n\tv_mul_f32 v208, v208, v200\n\tv_mul_f32 v209, v209, v201\n\tv_mul_f32 v210, v210, v202\n\tv_mul_f32 v211, v211, v203\n\tv_mul_f32 v212, v212, v204\n\t"
            "v_mul_f32 v213, v213, v205\n\tv_mul_f32 v214, v214, v206\n\tv_mul_f32 v215, v215, v207\n\tv_max3_f32 v136, v208, v209, v210\n\tv_max3_f32 v137, v211, v212, v213\n\tv_max3_f32 v136, v136, v214, v215\n\t"
            "v_max_f32 v136, v136, v137\n\tv_sub_f32 v208, v208, v136\n\tv_sub_f32 v209, v209, v136\n\tv_sub_f32 v210, v210, v136\n\tv_sub_f32 v211, v211, v136\n\tv_sub_f32 v212, v212, v136\n\t"
            "v_sub_f32 v213, v213, v136\n\tv_sub_f32 v214, v214, v136\n\tv_sub_f32 v215, v215, v136\n\tv_exp_f32 v208, v208\n\tv_exp_f32 v209, v209\n\tv_exp_f32 v210, v210\n\t"
            "v_exp_f32 v211, v211\n\tv_exp_f32 v212, v212\n\tv_exp_f32 v213, v213\n\tv_exp_f32 v214, v214\n\tv_exp_f32 v215, v215\n\tv_add_f32 v216, v208, v209\n\t"
            "v_add_f32 v217, v210, v211\n\tv_add_f32 v218, v212, v213\n\tv_add_f32 v219, v214, v215\n\tv_add_f32 v216, v216, v217\n\tv_add_f32 v218, v218, v219\n\tv_mul_f32 v208, v208, v177\n\t"
            "v_add_f32 v216, v216, v218\n\tv_mul_f32 v216, 0xbfb8aa3b, v216\n\tv_rcp_f32 v216, v216\n\tv_mul_f32 v209, v209, v179\n\tv_mul_f32 v210, v210, v181\n\tv_mul_f32 v211, v211, v183\n\t"
            "v_mul_f32 v212, v212, v185\n\tv_mul_f32 v213, v213, v187\n\tv_mul_f32 v214, v214, v189\n\tv_mul_f32 v215, v215, v191\n\tv_mul_f32 v208, v208, v216\n\tv_mul_f32 v209, v209, v216\n\t"
            "v_mul_f32 v210, v210, v216\n\tv_mul_f32 v211, v211, v216\n\tv_mul_f32 v212, v212, v216\n\tv_mul_f32 v213, v213, v216\n\tv_mul_f32 v214, v214, v216\n\tv_mul_f32 v215, v215, v216\n\t"
            "v_cvt_pk_bf16_f32 v220, v208, v209\n\tv_cvt_pk_bf16_f32 v221, v210, v211\n\tv_cvt_pk_bf16_f32 v222, v212, v213\n\tv_cvt_pk_bf16_f32 v223, v214, v215\n\tds_write_b128 v29, v[220:223] offset:38912\n\tds_write_b32 v30, v30 offset:16\n\t"
            SB_(2) PRIG
            "s_waitcnt vmcnt(0)\n\tds_write_b32 v30, v30 offset:48\n\ts_branch .LOL\n\t32:\nbuffer_load_dword v118, v96, s[44:47], 0 offen sc1\n\t"
            GINC
            "s_waitcnt vmcnt(0)\n\tv_cmp_ne_u32 vcc, s67, v118\n\ts_cbranch_vccz 33b\n\ts_sub_u32 s68, s68, 1\n\ts_cbranch_scc0 32b\n\ts_branch 33b\n\t"
            "30:\nbuffer_load_dwordx4 v[106:109], v158, s[44:47], 0 offen sc1\n\tbuffer_load_dwordx4 v[110:113], v159, s[44:47], 0 offen sc1\n\tbuffer_load_dwordx4 v[114:117], v104, s[44:47], 0 offen sc1\n\t"
            GINC
            "s_waitcnt vmcnt(2)\n\tv_xor_b32 v106, s67, v107\n\tv_xor_b32 v108, s67, v109\n\ts_waitcnt vmcnt(1)\n\tv_xor_b32 v110, s67, v111\n\tv_xor_b32 v112, s67, v113\n\t"
            "s_waitcnt vmcnt(0)\n\tv_xor_b32 v114, s67, v115\n\tv_xor_b32 v116, s67, v117\n\tv_or3_b32 v106, v106, v108, v110\n\tv_or3_b32 v112, v112, v114, v116\n\tv_or_b32 v106, v106, v112\n\t"
            "v_cmp_ne_u32 vcc, 0, v106\n\ts_cbranch_vccz 31b\n\ts_sub_u32 s68, s68, 1\n\ts_cbranch_scc0 30b\n\ts_branch 31b\n\t53:\n"
            PSLP
            "s_sub_u32 s68, s68, 1\n\ts_cbranch_scc0 51b\n\ts_branch 55b\n\t56:\n"
            RSL0
            "buffer_load_dword v224, v31, s[44:47], 0 offen offset:0 sc1\n\tbuffer_load_dword v225, v31, s[44:47], 0 offen offset:256 sc1\n\tbuffer_load_dword v226, v31, s[44:47], 0 offen offset:512 sc1\n\tbuffer_load_dword v227, v31, s[44:47], 0 offen offset:768 sc1\n\ts_waitcnt vmcnt(4)\n\t"
            PRIR
            "s_call_b64 s[76:77], .LG0\n\t"
            PRIX
            "s_branch 2f\n\t1:\ns_waitcnt vmcnt(3)\n\tbuffer_load_dword v224, v31, s[44:47], 0 offen offset:0 sc1\n\ts_waitcnt vmcnt(3)\n\tbuffer_load_dword v225, v31, s[44:47], 0 offen offset:256 sc1\n\t"
            "s_waitcnt vmcnt(3)\n\tbuffer_load_dword v226, v31, s[44:47], 0 offen offset:512 sc1\n\ts_waitcnt vmcnt(3)\n\tbuffer_load_dword v227, v31, s[44:47], 0 offen offset:768 sc1\n\ts_cmp_lg_u32 s83, 0\n\ts_cbranch_scc1 3f\n\t"
            "s_call_b64 s[76:77], .LG1\n\t3:\ns_cmp_eq_u32 s83, 2\n\ts_cbranch_scc1 2f\n\ts_call_b64 s[76:77], .LG2\n\t2:\n"
            "s_waitcnt vmcnt(0) lgkmcnt(0)\n\t4:\nds_write_b32 v30, v30 offset:48\n\tv_max3_u32 v136, v224, v225, v226\n\tv_max_u32 v136, v136, v227\n\tv_cmp_eq_u32 vcc, -1, v136\n\t"
            "v_lshlrev_b32 v216, 16, v224\n\tv_and_b32 v217, 0xffff0000, v224\n\tv_lshlrev_b32 v218, 16, v225\n\tv_and_b32 v219, 0xffff0000, v225\n\tv_lshlrev_b32 v220, 16, v226\n\tv_and_b32 v221, 0xffff0000, v226\n\t"
            "v_lshlrev_b32 v222, 16, v227\n\tv_and_b32 v223, 0xffff0000, v227\n\t"
            LINC
            "s_cmp_eq_u32 0, 0\n\ts_cbranch_vccz 58b\n\ts_sub_u32 s68, s68, 1\n\ts_cbranch_scc0 1b\n\ts_branch 58b\n\t57:\n"
            "s_branch 2f\n\t1:\nbuffer_load_dword v232, v31, s[44:47], s31 offen offset:0 sc1\n\tbuffer_load_dword v233, v31, s[44:47], s31 offen offset:256 sc1\n\tbuffer_load_dword v234, v31, s[44:47], s31 offen offset:512 sc1\n\tbuffer_load_dword v235, v31, s[44:47], s31 offen offset:768 sc1\n\t"
            "s_cmp_lg_u32 s83, 0\n\ts_cbranch_scc1 3f\n\ts_call_b64 s[76:77], .LG1\n\t3:\ns_cmp_eq_u32 s83, 2\n\ts_cbranch_scc1 2f\n\t"
            "s_call_b64 s[76:77], .LG2\n\t2:\ns_waitcnt vmcnt(0) lgkmcnt(0)\n\t4:\nv_max3_u32 v136, v232, v233, v234\n\tv_max_u32 v136, v136, v235\n\t"
            "v_cmp_eq_u32 vcc, -1, v136\n\tv_lshlrev_b32 v216, 16, v232\n\tv_and_b32 v217, 0xffff0000, v232\n\tv_lshlrev_b32 v218, 16, v233\n\tv_and_b32 v219, 0xffff0000, v233\n\tv_lshlrev_b32 v220, 16, v234\n\t"
            "v_and_b32 v221, 0xffff0000, v234\n\tv_lshlrev_b32 v222, 16, v235\n\tv_and_b32 v223, 0xffff0000, v235\n\t"
            LINC
            "s_cmp_eq_u32 0, 0\n\ts_cbranch_vccz 59b\n\ts_sub_u32 s68, s68, 1\n\ts_cbranch_scc0 1b\n\ts_branch 59b\n\t.LOL:\n"
            "s_lshr_b32 s71, s56, 1\n\ts_mov_b32 s68, 0x200000\n\ts_cmp_eq_u32 s16, 0\n\ts_cbranch_scc1 3f\n\ts_call_b64 s[76:77], .LG0\n\t3:\n"
            "ds_read_b64 v[14:15], v8 offset:32\n\ts_waitcnt lgkmcnt(0)\n\tv_min_u32 v12, v14, v15\n\tv_cmp_lt_u32 vcc, s71, v12\n\ts_cbranch_vccnz 4f\n\ts_sleep 1\n\t"
            "s_sub_u32 s68, s68, 1\n\ts_cbranch_scc0 3b\n\t4:\ns_cmp_eq_u32 s16, 0\n\ts_cbranch_scc1 1f\n\ts_call_b64 s[76:77], .LG1\n\t"
            "1:\ns_cmp_lt_u32 s56, 2\n\ts_cbranch_scc1 1f\n\ts_sleep 3\n\t1:\n"
            SO(4) RZ
            "buffer_load_dwordx4 v[176:179], v2, s[44:47], s4 offen offset:1024 sc1\n\tbuffer_load_dwordx4 v[180:183], v2, s[44:47], s4 offen offset:2048 sc1\n\tbuffer_load_dwordx4 v[184:187], v2, s[44:47], s5 offen offset:1024 sc1\n\tbuffer_load_dwordx4 v[188:191], v2, s[44:47], s5 offen offset:2048 sc1\n\tbuffer_load_dwordx4 v[192:195], v2, s[44:47], s6 offen offset:1024 sc1\n\tbuffer_load_dwordx4 v[196:199], v2, s[44:47], s6 offen offset:2048 sc1\n\t"
            "buffer_load_dwordx4 v[200:203], v2, s[44:47], s7 offen offset:1024 sc1\n\tbuffer_load_dwordx4 v[204:207], v2, s[44:47], s7 offen offset:2048 sc1\n\tbuffer_load_dwordx4 v[208:211], v2, s[44:47], s8 offen offset:1024 sc1\n\tbuffer_load_dwordx4 v[212:215], v2, s[44:47], s8 offen offset:2048 sc1\n\ts_cmp_lg_u32 s83, 0\n\ts_cbranch_scc1 1f\n\t"
            "s_call_b64 s[76:77], .LG1\n\t1:\ns_cmp_eq_u32 s83, 2\n\ts_cbranch_scc1 1f\n\ts_call_b64 s[76:77], .LG2\n\t1:\n"
            "ds_read_b128 v[16:19], v8 offset:48\n\tds_read_b64_tr_b16 v[128:129], v4 offset:40960\n\tds_read_b64_tr_b16 v[130:131], v4 offset:41472\n\tds_read_b64_tr_b16 v[132:133], v5 offset:40960\n\tds_read_b64_tr_b16 v[134:135], v5 offset:41472\n\tds_read_b64_tr_b16 v[136:137], v6 offset:40960\n\t"
            "ds_read_b64_tr_b16 v[138:139], v6 offset:41472\n\tds_read_b64_tr_b16 v[140:141], v7 offset:40960\n\tds_read_b64_tr_b16 v[142:143], v7 offset:41472\n\tds_read_b64_tr_b16 v[144:145], v4 offset:41984\n\tds_read_b64_tr_b16 v[146:147], v4 offset:42496\n\tds_read_b64_tr_b16 v[148:149], v5 offset:41984\n\t"
            "ds_read_b64_tr_b16 v[150:151], v5 offset:42496\n\tds_read_b64_tr_b16 v[152:153], v6 offset:41984\n\tds_read_b64_tr_b16 v[154:155], v6 offset:42496\n\tds_read_b64_tr_b16 v[156:157], v7 offset:41984\n\tds_read_b64_tr_b16 v[158:159], v7 offset:42496\n\tv_mul_f32 v48, v48, v112\n\t"
            "v_mul_f32 v49, v49, v113\n\tv_mul_f32 v50, v50, v114\n\tv_mul_f32 v51, v51, v115\n\tv_mul_f32 v52, v52, v116\n\tv_mul_f32 v53, v53, v117\n\tv_mul_f32 v54, v54, v118\n\t"
            "v_mul_f32 v55, v55, v119\n\tv_mul_f32 v56, v56, v120\n\tv_mul_f32 v57, v57, v121\n\tv_mul_f32 v58, v58, v122\n\tv_mul_f32 v59, v59, v123\n\tv_mul_f32 v60, v60, v124\n\t"
            "v_mul_f32 v61, v61, v125\n\tv_mul_f32 v62, v62, v126\n\tv_mul_f32 v63, v63, v127\n\t"
            SO(3)
            "s_waitcnt lgkmcnt(15)\n\tv_min3_u32 v16, v16, v17, v18\n\tv_min_u32 v16, v16, v19\n\tv_cmp_eq_u32 vcc, 0, v16\n\ts_cbranch_vccnz 62f\n\t63:\n"
            PRIO
            "s_waitcnt vmcnt(8)\n\tv_max3_u32 v12, v176, v178, v180\n\tv_max_u32 v12, v12, v182\n\tv_cmp_eq_u32 vcc, -1, v12\n\ts_cbranch_vccnz 100f\n\t200:\n"
            SO(5)
            "s_waitcnt lgkmcnt(8)\n\tv_mfma_f32_32x32x16_bf16 v[96:111], v[128:131], v[176:179], 0\n\tds_read_b64_tr_b16 v[160:161], v4 offset:43008\n\tds_read_b64_tr_b16 v[162:163], v4 offset:43520\n\tbuffer_load_dwordx4 v[216:219], v2, s[44:47], s9 offen offset:1024 sc1\n\tv_mfma_f32_32x32x16_bf16 v[112:127], v[132:135], v[176:179], 0\n\t"
            "ds_read_b64_tr_b16 v[164:165], v5 offset:43008\n\tds_read_b64_tr_b16 v[166:167], v5 offset:43520\n\tbuffer_load_dwordx4 v[220:223], v2, s[44:47], s9 offen offset:2048 sc1\n\tv_mfma_f32_32x32x16_bf16 v[96:111], v[136:139], v[180:183], v[96:111]\n\tds_read_b64_tr_b16 v[168:169], v6 offset:43008\n\tds_read_b64_tr_b16 v[170:171], v6 offset:43520\n\t"
            "s_waitcnt vmcnt(8)\n\tv_max3_u32 v12, v184, v186, v188\n\tv_max_u32 v12, v12, v190\n\tv_cmp_eq_u32 vcc, -1, v12\n\tv_mfma_f32_32x32x16_bf16 v[112:127], v[140:143], v[180:183], v[112:127]\n\tds_read_b64_tr_b16 v[172:173], v7 offset:43008\n\t"
            "ds_read_b64_tr_b16 v[174:175], v7 offset:43520\n\ts_cbranch_vccnz 101f\n\t201:\ns_waitcnt lgkmcnt(8)\n\tv_mfma_f32_32x32x16_bf16 v[96:111], v[144:147], v[184:187], v[96:111]\n\tds_read_b64_tr_b16 v[128:129], v4 offset:44032\n\t"
            "ds_read_b64_tr_b16 v[130:131], v4 offset:44544\n\tbuffer_load_dwordx4 v[224:227], v2, s[44:47], s10 offen offset:1024 sc1\n\tv_mfma_f32_32x32x16_bf16 v[112:127], v[148:151], v[184:187], v[112:127]\n\tds_read_b64_tr_b16 v[132:133], v5 offset:44032\n\tds_read_b64_tr_b16 v[134:135], v5 offset:44544\n\tbuffer_load_dwordx4 v[228:231], v2, s[44:47], s10 offen offset:2048 sc1\n\t"
            "v_mfma_f32_32x32x16_bf16 v[96:111], v[152:155], v[188:191], v[96:111]\n\tds_read_b64_tr_b16 v[136:137], v6 offset:44032\n\tds_read_b64_tr_b16 v[138:139], v6 offset:44544\n\ts_waitcnt vmcnt(8)\n\tv_max3_u32 v12, v192, v194, v196\n\tv_max_u32 v12, v12, v198\n\t"
            "v_cmp_eq_u32 vcc, -1, v12\n\tv_mfma_f32_32x32x16_bf16 v[112:127], v[156:159], v[188:191], v[112:127]\n\tds_read_b64_tr_b16 v[140:141], v7 offset:44032\n\tds_read_b64_tr_b16 v[142:143], v7 offset:44544\n\ts_cbranch_vccnz 102f\n\t202:\n"
            "s_waitcnt lgkmcnt(8)\n\tv_mfma_f32_32x32x16_bf16 v[96:111], v[160:163], v[192:195], v[96:111]\n\tds_read_b64_tr_b16 v[144:145], v4 offset:45056\n\tds_read_b64_tr_b16 v[146:147], v4 offset:45568\n\tbuffer_load_dwordx4 v[232:235], v2, s[44:47], s11 offen offset:1024 sc1\n\tv_mfma_f32_32x32x16_bf16 v[112:127], v[164:167], v[192:195], v[112:127]\n\t"
            "ds_read_b64_tr_b16 v[148:149], v5 offset:45056\n\tds_read_b64_tr_b16 v[150:151], v5 offset:45568\n\tbuffer_load_dwordx4 v[236:239], v2, s[44:47], s11 offen offset:2048 sc1\n\tv_mfma_f32_32x32x16_bf16 v[96:111], v[168:171], v[196:199], v[96:111]\n\tds_read_b64_tr_b16 v[152:153], v6 offset:45056\n\tds_read_b64_tr_b16 v[154:155], v6 offset:45568\n\t"
            "s_waitcnt vmcnt(8)\n\tv_max3_u32 v12, v200, v202, v204\n\tv_max_u32 v12, v12, v206\n\tv_cmp_eq_u32 vcc, -1, v12\n\tv_mfma_f32_32x32x16_bf16 v[112:127], v[172:175], v[196:199], v[112:127]\n\tds_read_b64_tr_b16 v[156:157], v7 offset:45056\n\t"
            "ds_read_b64_tr_b16 v[158:159], v7 offset:45568\n\ts_cbranch_vccnz 103f\n\t203:\ns_waitcnt lgkmcnt(8)\n\tv_mfma_f32_32x32x16_bf16 v[96:111], v[128:131], v[200:203], v[96:111]\n\tds_read_b64_tr_b16 v[160:161], v4 offset:46080\n\t"
            "ds_read_b64_tr_b16 v[162:163], v4 offset:46592\n\tbuffer_load_dwordx4 v[176:179], v2, s[44:47], s12 offen offset:1024 sc1\n\tv_mfma_f32_32x32x16_bf16 v[112:127], v[132:135], v[200:203], v[112:127]\n\tds_read_b64_tr_b16 v[164:165], v5 offset:46080\n\tds_read_b64_tr_b16 v[166:167], v5 offset:46592\n\tds_read_b64 v[14:15], v8 offset:40\n\t"
            "buffer_load_dwordx4 v[180:183], v2, s[44:47], s12 offen offset:2048 sc1\n\tv_mfma_f32_32x32x16_bf16 v[96:111], v[136:139], v[204:207], v[96:111]\n\tds_read_b64_tr_b16 v[168:169], v6 offset:46080\n\tds_read_b64_tr_b16 v[170:171], v6 offset:46592\n\ts_waitcnt vmcnt(8)\n\tv_max3_u32 v12, v208, v210, v212\n\t"
            "v_max_u32 v12, v12, v214\n\tv_cmp_eq_u32 vcc, -1, v12\n\tv_mfma_f32_32x32x16_bf16 v[112:127], v[140:143], v[204:207], v[112:127]\n\tds_read_b64_tr_b16 v[172:173], v7 offset:46080\n\tds_read_b64_tr_b16 v[174:175], v7 offset:46592\n\ts_cbranch_vccnz 104f\n\t"
            "204:\ns_waitcnt lgkmcnt(9)\n\tv_mfma_f32_32x32x16_bf16 v[96:111], v[144:147], v[208:211], v[96:111]\n\tds_read_b64_tr_b16 v[128:129], v4 offset:47104\n\tds_read_b64_tr_b16 v[130:131], v4 offset:47616\n\tbuffer_load_dwordx4 v[184:187], v2, s[44:47], s13 offen offset:1024 sc1\n\t"
            "v_mfma_f32_32x32x16_bf16 v[112:127], v[148:151], v[208:211], v[112:127]\n\tds_read_b64_tr_b16 v[132:133], v5 offset:47104\n\tds_read_b64_tr_b16 v[134:135], v5 offset:47616\n\tbuffer_load_dwordx4 v[188:191], v2, s[44:47], s13 offen offset:2048 sc1\n\tv_mfma_f32_32x32x16_bf16 v[96:111], v[152:155], v[212:215], v[96:111]\n\tds_read_b64_tr_b16 v[136:137], v6 offset:47104\n\t"
            "ds_read_b64_tr_b16 v[138:139], v6 offset:47616\n\ts_waitcnt vmcnt(8)\n\tv_max3_u32 v12, v216, v218, v220\n\tv_max_u32 v12, v12, v222\n\tv_cmp_eq_u32 vcc, -1, v12\n\tv_mfma_f32_32x32x16_bf16 v[112:127], v[156:159], v[212:215], v[112:127]\n\t"
            "ds_read_b64_tr_b16 v[140:141], v7 offset:47104\n\tds_read_b64_tr_b16 v[142:143], v7 offset:47616\n\ts_cbranch_vccnz 105f\n\t205:\ns_waitcnt lgkmcnt(8)\n\tv_mfma_f32_32x32x16_bf16 v[96:111], v[160:163], v[216:219], v[96:111]\n\t"
            "v_min_u32 v14, v14, v15\n\tv_cmp_ge_u32 vcc, s71, v14\n\tds_read_b64_tr_b16 v[144:145], v4 offset:48128\n\tds_read_b64_tr_b16 v[146:147], v4 offset:48640\n\tv_mfma_f32_32x32x16_bf16 v[112:127], v[164:167], v[216:219], v[112:127]\n\ts_cbranch_vccnz 5f\n\t"
            "6:\nds_read_b64_tr_b16 v[148:149], v5 offset:48128\n\tds_read_b64_tr_b16 v[150:151], v5 offset:48640\n\tbuffer_load_dwordx4 v[192:195], v2, s[44:47], s4 offen offset:0 sc1\n\tbuffer_load_dwordx4 v[196:199], v2, s[44:47], s4 offen offset:3072 sc1\n\tv_mfma_f32_32x32x16_bf16 v[96:111], v[168:171], v[220:223], v[96:111]\n\t"
            "ds_read_b64_tr_b16 v[152:153], v6 offset:48128\n\tds_read_b64_tr_b16 v[154:155], v6 offset:48640\n\ts_waitcnt vmcnt(8)\n\tv_max3_u32 v12, v224, v226, v228\n\tv_max_u32 v12, v12, v230\n\tv_cmp_eq_u32 vcc, -1, v12\n\t"
            "v_mfma_f32_32x32x16_bf16 v[112:127], v[172:175], v[220:223], v[112:127]\n\tds_read_b64_tr_b16 v[156:157], v7 offset:48128\n\tds_read_b64_tr_b16 v[158:159], v7 offset:48640\n\ts_cbranch_vccnz 106f\n\t206:\ns_waitcnt lgkmcnt(8)\n\t"
            "v_mfma_f32_32x32x16_bf16 v[96:111], v[128:131], v[224:227], v[96:111]\n\tds_read_b64_tr_b16 v[160:161], v4 offset:49152\n\tds_read_b64_tr_b16 v[162:163], v4 offset:49664\n\tbuffer_load_dwordx4 v[200:203], v2, s[44:47], s5 offen offset:0 sc1\n\tv_mfma_f32_32x32x16_bf16 v[112:127], v[132:135], v[224:227], v[112:127]\n\tds_read_b64_tr_b16 v[164:165], v5 offset:49152\n\t"
            "ds_read_b64_tr_b16 v[166:167], v5 offset:49664\n\tbuffer_load_dwordx4 v[204:207], v2, s[44:47], s5 offen offset:3072 sc1\n\tv_mfma_f32_32x32x16_bf16 v[96:111], v[136:139], v[228:231], v[96:111]\n\tds_read_b64_tr_b16 v[168:169], v6 offset:49152\n\tds_read_b64_tr_b16 v[170:171], v6 offset:49664\n\ts_waitcnt vmcnt(8)\n\t"
            "v_max3_u32 v12, v232, v234, v236\n\tv_max_u32 v12, v12, v238\n\tv_cmp_eq_u32 vcc, -1, v12\n\tv_mfma_f32_32x32x16_bf16 v[112:127], v[140:143], v[228:231], v[112:127]\n\tds_read_b64_tr_b16 v[172:173], v7 offset:49152\n\tds_read_b64_tr_b16 v[174:175], v7 offset:49664\n\t"
            "s_cbranch_vccnz 107f\n\t207:\ns_waitcnt lgkmcnt(8)\n\tv_mfma_f32_32x32x16_bf16 v[96:111], v[144:147], v[232:235], v[96:111]\n\tds_read_b64_tr_b16 v[128:129], v4 offset:50176\n\tds_read_b64_tr_b16 v[130:131], v4 offset:50688\n\t"
            "buffer_load_dwordx4 v[208:211], v2, s[44:47], s6 offen offset:0 sc1\n\tv_mfma_f32_32x32x16_bf16 v[112:127], v[148:151], v[232:235], v[112:127]\n\tds_read_b64_tr_b16 v[132:133], v5 offset:50176\n\tds_read_b64_tr_b16 v[134:135], v5 offset:50688\n\tbuffer_load_dwordx4 v[212:215], v2, s[44:47], s6 offen offset:3072 sc1\n\tv_mfma_f32_32x32x16_bf16 v[96:111], v[152:155], v[236:239], v[96:111]\n\t"
            "ds_read_b64_tr_b16 v[136:137], v6 offset:50176\n\tds_read_b64_tr_b16 v[138:139], v6 offset:50688\n\ts_waitcnt vmcnt(8)\n\tv_max3_u32 v12, v176, v178, v180\n\tv_max_u32 v12, v12, v182\n\tv_cmp_eq_u32 vcc, -1, v12\n\t"
            "v_mfma_f32_32x32x16_bf16 v[112:127], v[156:159], v[236:239], v[112:127]\n\tds_read_b64_tr_b16 v[140:141], v7 offset:50176\n\tds_read_b64_tr_b16 v[142:143], v7 offset:50688\n\ts_cbranch_vccnz 108f\n\t208:\ns_waitcnt lgkmcnt(8)\n\t"
            "v_mfma_f32_32x32x16_bf16 v[96:111], v[160:163], v[176:179], v[96:111]\n\tds_read_b64_tr_b16 v[144:145], v4 offset:0\n\tds_read_b64_tr_b16 v[146:147], v4 offset:512\n\tbuffer_load_dwordx4 v[216:219], v2, s[44:47], s7 offen offset:0 sc1\n\tv_mfma_f32_32x32x16_bf16 v[112:127], v[164:167], v[176:179], v[112:127]\n\tds_read_b64_tr_b16 v[148:149], v5 offset:0\n\t"
            "ds_read_b64_tr_b16 v[150:151], v5 offset:512\n\tbuffer_load_dwordx4 v[220:223], v2, s[44:47], s7 offen offset:3072 sc1\n\tv_mfma_f32_32x32x16_bf16 v[96:111], v[168:171], v[180:183], v[96:111]\n\tds_read_b64_tr_b16 v[152:153], v6 offset:0\n\tds_read_b64_tr_b16 v[154:155], v6 offset:512\n\ts_waitcnt vmcnt(8)\n\t"
            "v_max3_u32 v12, v184, v186, v188\n\tv_max_u32 v12, v12, v190\n\tv_cmp_eq_u32 vcc, -1, v12\n\tv_mfma_f32_32x32x16_bf16 v[112:127], v[172:175], v[180:183], v[112:127]\n\tds_read_b64_tr_b16 v[156:157], v7 offset:0\n\tds_read_b64_tr_b16 v[158:159], v7 offset:512\n\t"
            "s_cbranch_vccnz 109f\n\t209:\ns_waitcnt lgkmcnt(8)\n\tv_mfma_f32_32x32x16_bf16 v[96:111], v[128:131], v[184:187], v[96:111]\n\tds_read_b64_tr_b16 v[160:161], v4 offset:1024\n\tds_read_b64_tr_b16 v[162:163], v4 offset:1536\n\t"
            "buffer_load_dwordx4 v[224:227], v2, s[44:47], s8 offen offset:0 sc1\n\tv_mfma_f32_32x32x16_bf16 v[112:127], v[132:135], v[184:187], v[112:127]\n\tds_read_b64_tr_b16 v[164:165], v5 offset:1024\n\tds_read_b64_tr_b16 v[166:167], v5 offset:1536\n\tbuffer_load_dwordx4 v[228:231], v2, s[44:47], s8 offen offset:3072 sc1\n\tv_mfma_f32_32x32x16_bf16 v[96:111], v[136:139], v[188:191], v[96:111]\n\t"
            "ds_read_b64_tr_b16 v[168:169], v6 offset:1024\n\tds_read_b64_tr_b16 v[170:171], v6 offset:1536\n\ts_waitcnt vmcnt(8)\n\tv_max3_u32 v12, v192, v194, v196\n\tv_max_u32 v12, v12, v198\n\tv_cmp_eq_u32 vcc, -1, v12\n\t"
            "v_mfma_f32_32x32x16_bf16 v[112:127], v[140:143], v[188:191], v[112:127]\n\tds_read_b64_tr_b16 v[172:173], v7 offset:1024\n\tds_read_b64_tr_b16 v[174:175], v7 offset:1536\n\ts_cbranch_vccnz 110f\n\t210:\ns_waitcnt lgkmcnt(8)\n\t"
            "v_mfma_f32_32x32x16_bf16 v[96:111], v[144:147], v[192:195], v[96:111]\n\tds_read_b64_tr_b16 v[128:129], v4 offset:2048\n\tds_read_b64_tr_b16 v[130:131], v4 offset:2560\n\tbuffer_load_dwordx4 v[232:235], v2, s[44:47], s9 offen offset:0 sc1\n\tv_mfma_f32_32x32x16_bf16 v[112:127], v[148:151], v[192:195], v[112:127]\n\tds_read_b64_tr_b16 v[132:133], v5 offset:2048\n\t"
            "ds_read_b64_tr_b16 v[134:135], v5 offset:2560\n\tbuffer_load_dwordx4 v[236:239], v2, s[44:47], s9 offen offset:3072 sc1\n\tv_mfma_f32_32x32x16_bf16 v[96:111], v[152:155], v[196:199], v[96:111]\n\tds_read_b64_tr_b16 v[136:137], v6 offset:2048\n\tds_read_b64_tr_b16 v[138:139], v6 offset:2560\n\ts_waitcnt vmcnt(8)\n\t"
            "v_max3_u32 v12, v200, v202, v204\n\tv_max_u32 v12, v12, v206\n\tv_cmp_eq_u32 vcc, -1, v12\n\tv_mfma_f32_32x32x16_bf16 v[112:127], v[156:159], v[196:199], v[112:127]\n\tds_read_b64_tr_b16 v[140:141], v7 offset:2048\n\tds_read_b64_tr_b16 v[142:143], v7 offset:2560\n\t"
            "s_cbranch_vccnz 111f\n\t211:\ns_waitcnt lgkmcnt(8)\n\tv_mfma_f32_32x32x16_bf16 v[96:111], v[160:163], v[200:203], v[96:111]\n\tds_read_b64_tr_b16 v[144:145], v4 offset:3072\n\tds_read_b64_tr_b16 v[146:147], v4 offset:3584\n\t"
            "buffer_load_dwordx4 v[176:179], v2, s[44:47], s10 offen offset:0 sc1\n\tv_mfma_f32_32x32x16_bf16 v[112:127], v[164:167], v[200:203], v[112:127]\n\tds_read_b64_tr_b16 v[148:149], v5 offset:3072\n\tds_read_b64_tr_b16 v[150:151], v5 offset:3584\n\tbuffer_load_dwordx4 v[180:183], v2, s[44:47], s10 offen offset:3072 sc1\n\tv_mfma_f32_32x32x16_bf16 v[96:111], v[168:171], v[204:207], v[96:111]\n\t"
            "ds_read_b64_tr_b16 v[152:153], v6 offset:3072\n\tds_read_b64_tr_b16 v[154:155], v6 offset:3584\n\ts_waitcnt vmcnt(8)\n\tv_max3_u32 v12, v208, v210, v212\n\tv_max_u32 v12, v12, v214\n\tv_cmp_eq_u32 vcc, -1, v12\n\t"
            "v_mfma_f32_32x32x16_bf16 v[112:127], v[172:175], v[204:207], v[112:127]\n\tds_read_b64_tr_b16 v[156:157], v7 offset:3072\n\tds_read_b64_tr_b16 v[158:159], v7 offset:3584\n\ts_cbranch_vccnz 112f\n\t212:\ns_waitcnt lgkmcnt(8)\n\t"
            "v_mfma_f32_32x32x16_bf16 v[96:111], v[128:131], v[208:211], v[96:111]\n\tds_read_b64_tr_b16 v[160:161], v4 offset:4096\n\tds_read_b64_tr_b16 v[162:163], v4 offset:4608\n\tbuffer_load_dwordx4 v[184:187], v2, s[44:47], s11 offen offset:0 sc1\n\tv_mfma_f32_32x32x16_bf16 v[112:127], v[132:135], v[208:211], v[112:127]\n\tds_read_b64_tr_b16 v[164:165], v5 offset:4096\n\t"
            "ds_read_b64_tr_b16 v[166:167], v5 offset:4608\n\tbuffer_load_dwordx4 v[188:191], v2, s[44:47], s11 offen offset:3072 sc1\n\tv_mfma_f32_32x32x16_bf16 v[96:111], v[136:139], v[212:215], v[96:111]\n\tds_read_b64_tr_b16 v[168:169], v6 offset:4096\n\tds_read_b64_tr_b16 v[170:171], v6 offset:4608\n\ts_waitcnt vmcnt(8)\n\t"
            "v_max3_u32 v12, v216, v218, v220\n\tv_max_u32 v12, v12, v222\n\tv_cmp_eq_u32 vcc, -1, v12\n\tv_mfma_f32_32x32x16_bf16 v[112:127], v[140:143], v[212:215], v[112:127]\n\tds_read_b64_tr_b16 v[172:173], v7 offset:4096\n\tds_read_b64_tr_b16 v[174:175], v7 offset:4608\n\t"
            "s_cbranch_vccnz 113f\n\t213:\ns_waitcnt lgkmcnt(8)\n\tv_mfma_f32_32x32x16_bf16 v[96:111], v[144:147], v[216:219], v[96:111]\n\tds_read_b64_tr_b16 v[128:129], v4 offset:5120\n\tds_read_b64_tr_b16 v[130:131], v4 offset:5632\n\t"
            "buffer_load_dwordx4 v[192:195], v2, s[44:47], s12 offen offset:0 sc1\n\tv_mfma_f32_32x32x16_bf16 v[112:127], v[148:151], v[216:219], v[112:127]\n\tds_read_b64_tr_b16 v[132:133], v5 offset:5120\n\tds_read_b64_tr_b16 v[134:135], v5 offset:5632\n\tbuffer_load_dwordx4 v[196:199], v2, s[44:47], s12 offen offset:3072 sc1\n\tv_mfma_f32_32x32x16_bf16 v[96:111], v[152:155], v[220:223], v[96:111]\n\t"
            "ds_read_b64_tr_b16 v[136:137], v6 offset:5120\n\tds_read_b64_tr_b16 v[138:139], v6 offset:5632\n\ts_waitcnt vmcnt(8)\n\tv_max3_u32 v12, v224, v226, v228\n\tv_max_u32 v12, v12, v230\n\tv_cmp_eq_u32 vcc, -1, v12\n\t"
            "v_mfma_f32_32x32x16_bf16 v[112:127], v[156:159], v[220:223], v[112:127]\n\tds_read_b64_tr_b16 v[140:141], v7 offset:5120\n\tds_read_b64_tr_b16 v[142:143], v7 offset:5632\n\ts_cbranch_vccnz 114f\n\t214:\ns_waitcnt lgkmcnt(8)\n\t"
            "v_mfma_f32_32x32x16_bf16 v[96:111], v[160:163], v[224:227], v[96:111]\n\tds_read_b64_tr_b16 v[144:145], v4 offset:6144\n\tds_read_b64_tr_b16 v[146:147], v4 offset:6656\n\tbuffer_load_dwordx4 v[200:203], v2, s[44:47], s13 offen offset:0 sc1\n\tv_mfma_f32_32x32x16_bf16 v[112:127], v[164:167], v[224:227], v[112:127]\n\tds_read_b64_tr_b16 v[148:149], v5 offset:6144\n\t"
            "ds_read_b64_tr_b16 v[150:151], v5 offset:6656\n\tbuffer_load_dwordx4 v[204:207], v2, s[44:47], s13 offen offset:3072 sc1\n\tv_mfma_f32_32x32x16_bf16 v[96:111], v[168:171], v[228:231], v[96:111]\n\tds_read_b64_tr_b16 v[152:153], v6 offset:6144\n\tds_read_b64_tr_b16 v[154:155], v6 offset:6656\n\ts_waitcnt vmcnt(8)\n\t"
            "v_max3_u32 v12, v232, v234, v236\n\tv_max_u32 v12, v12, v238\n\tv_cmp_eq_u32 vcc, -1, v12\n\tv_mfma_f32_32x32x16_bf16 v[112:127], v[172:175], v[228:231], v[112:127]\n\tds_read_b64_tr_b16 v[156:157], v7 offset:6144\n\tds_read_b64_tr_b16 v[158:159], v7 offset:6656\n\t"
            "s_cbranch_vccnz 115f\n\t215:\ns_waitcnt lgkmcnt(8)\n\tv_mfma_f32_32x32x16_bf16 v[96:111], v[128:131], v[232:235], v[96:111]\n\tds_read_b64_tr_b16 v[160:161], v4 offset:7168\n\tds_read_b64_tr_b16 v[162:163], v4 offset:7680\n\t"
            "v_mfma_f32_32x32x16_bf16 v[112:127], v[132:135], v[232:235], v[112:127]\n\tds_read_b64_tr_b16 v[164:165], v5 offset:7168\n\tds_read_b64_tr_b16 v[166:167], v5 offset:7680\n\tv_mfma_f32_32x32x16_bf16 v[96:111], v[136:139], v[236:239], v[96:111]\n\tds_read_b64_tr_b16 v[168:169], v6 offset:7168\n\tds_read_b64_tr_b16 v[170:171], v6 offset:7680\n\t"
            "s_waitcnt vmcnt(6)\n\tv_max3_u32 v12, v176, v178, v180\n\tv_max_u32 v12, v12, v182\n\tv_cmp_eq_u32 vcc, -1, v12\n\tv_mfma_f32_32x32x16_bf16 v[112:127], v[140:143], v[236:239], v[112:127]\n\tds_read_b64_tr_b16 v[172:173], v7 offset:7168\n\t"
            "ds_read_b64_tr_b16 v[174:175], v7 offset:7680\n\ts_cbranch_vccnz 116f\n\t216:\ns_waitcnt lgkmcnt(8)\n\tv_mfma_f32_32x32x16_bf16 v[96:111], v[144:147], v[176:179], v[96:111]\n\tds_read_b64_tr_b16 v[128:129], v4 offset:8192\n\t"
            "ds_read_b64_tr_b16 v[130:131], v4 offset:8704\n\tv_mfma_f32_32x32x16_bf16 v[112:127], v[148:151], v[176:179], v[112:127]\n\tds_read_b64_tr_b16 v[132:133], v5 offset:8192\n\tds_read_b64_tr_b16 v[134:135], v5 offset:8704\n\tv_mfma_f32_32x32x16_bf16 v[96:111], v[152:155], v[180:183], v[96:111]\n\tds_read_b64_tr_b16 v[136:137], v6 offset:8192\n\t"
            "ds_read_b64_tr_b16 v[138:139], v6 offset:8704\n\ts_waitcnt vmcnt(4)\n\tv_max3_u32 v12, v184, v186, v188\n\tv_max_u32 v12, v12, v190\n\tv_cmp_eq_u32 vcc, -1, v12\n\tv_mfma_f32_32x32x16_bf16 v[112:127], v[156:159], v[180:183], v[112:127]\n\t"
            "ds_read_b64_tr_b16 v[140:141], v7 offset:8192\n\tds_read_b64_tr_b16 v[142:143], v7 offset:8704\n\ts_cbranch_vccnz 117f\n\t217:\nds_read_b32 v16, v8 offset:64\n\tds_read_b32 v17, v9 offset:1344\n\t"
            "s_waitcnt lgkmcnt(10)\n\tv_mfma_f32_32x32x16_bf16 v[96:111], v[160:163], v[184:187], v[96:111]\n\tds_read_b64_tr_b16 v[144:145], v4 offset:9216\n\tds_read_b64_tr_b16 v[146:147], v4 offset:9728\n\tds_read_b64_tr_b16 v[148:149], v5 offset:9216\n\tds_read_b64_tr_b16 v[150:151], v5 offset:9728\n\t"
            "ds_read_b64_tr_b16 v[152:153], v6 offset:9216\n\tds_read_b64_tr_b16 v[154:155], v6 offset:9728\n\ts_waitcnt vmcnt(2)\n\tv_max3_u32 v12, v192, v194, v196\n\tv_max_u32 v12, v12, v198\n\tv_cmp_eq_u32 vcc, -1, v12\n\t"
            "v_mfma_f32_32x32x16_bf16 v[96:111], v[168:171], v[188:191], v[96:111]\n\tds_read_b64_tr_b16 v[156:157], v7 offset:9216\n\tds_read_b64_tr_b16 v[158:159], v7 offset:9728\n\ts_cbranch_vccnz 118f\n\t218:\ns_waitcnt lgkmcnt(10)\n\t"
            "v_mfma_f32_32x32x16_bf16 v[96:111], v[128:131], v[192:195], v[96:111]\n\ts_waitcnt vmcnt(0)\n\tv_max3_u32 v12, v200, v202, v204\n\tv_max_u32 v12, v12, v206\n\tv_cmp_eq_u32 vcc, -1, v12\n\tv_mfma_f32_32x32x16_bf16 v[96:111], v[136:139], v[196:199], v[96:111]\n\t"
            "s_cbranch_vccnz 119f\n\t219:\ns_waitcnt lgkmcnt(0)\n\tv_mfma_f32_32x32x16_bf16 v[96:111], v[144:147], v[200:203], v[96:111]\n\tv_cmp_ne_u32 s[74:75], 0, v17\n\tv_cmp_eq_u32 vcc, 0, v16\n\t"
            "v_mfma_f32_32x32x16_bf16 v[96:111], v[152:155], v[204:207], v[96:111]\n\ts_or_b64 vcc, vcc, s[74:75]\n\tv_mfma_f32_32x32x16_bf16 v[112:127], v[164:167], v[184:187], v[112:127]\n\ts_cbranch_scc1 7f\n\t8:\ns_nop 5\n\t"
            "v_mfma_f32_32x32x16_bf16 v[112:127], v[172:175], v[188:191], v[112:127]\n\ts_nop 1\n\tv_fmac_f32 v64, v96, v32\n\tv_fmac_f32 v65, v97, v33\n\tv_fmac_f32 v66, v98, v34\n\tv_fmac_f32 v67, v99, v35\n\t"
            "v_fmac_f32 v68, v100, v36\n\tv_fmac_f32 v69, v101, v37\n\tv_fmac_f32 v70, v102, v38\n\tv_mfma_f32_32x32x16_bf16 v[112:127], v[132:135], v[192:195], v[112:127]\n\tv_fmac_f32 v71, v103, v39\n\tv_fmac_f32 v72, v104, v40\n\t"
            "v_fmac_f32 v73, v105, v41\n\tv_fmac_f32 v74, v106, v42\n\tv_fmac_f32 v75, v107, v43\n\tv_fmac_f32 v76, v108, v44\n\tv_fmac_f32 v77, v109, v45\n\tv_mfma_f32_32x32x16_bf16 v[112:127], v[140:143], v[196:199], v[112:127]\n\t"
            "v_fmac_f32 v78, v110, v46\n\tv_fmac_f32 v79, v111, v47\n\tv_cvt_pk_bf16_f32 v240, v64, v65\n\tv_cvt_pk_bf16_f32 v242, v68, v69\n\tv_cvt_pk_bf16_f32 v241, v66, v67\n\tv_cvt_pk_bf16_f32 v243, v70, v71\n\t"
            "v_cvt_pk_bf16_f32 v244, v72, v73\n\tv_mfma_f32_32x32x16_bf16 v[112:127], v[148:151], v[200:203], v[112:127]\n\tv_cvt_pk_bf16_f32 v246, v76, v77\n\tv_cvt_pk_bf16_f32 v245, v74, v75\n\tv_cvt_pk_bf16_f32 v247, v78, v79\n\tv_permlane32_swap_b32 v240, v242\n\t"
            "v_permlane32_swap_b32 v241, v243\n\tv_permlane32_swap_b32 v244, v246\n\tv_permlane32_swap_b32 v245, v247\n\tv_mfma_f32_32x32x16_bf16 v[112:127], v[156:159], v[204:207], v[112:127]\n\tv_permlane16_swap_b32 v240, v244\n\tv_permlane16_swap_b32 v242, v246\n\t"
            "v_permlane16_swap_b32 v241, v245\n\tv_permlane16_swap_b32 v243, v247\n\tbuffer_store_dwordx4 v[240:243], v3, s[52:55], 0 offen offset:0 sc1\n\tbuffer_store_dwordx4 v[244:247], v11, s[52:55], 0 offen offset:0 sc1\n\t"
            SO(6)
            "s_cmp_lg_u32 s74, 0\n\ts_cbranch_scc1 73f\n\t74:\ns_nop 3\n\tv_fmac_f32 v80, v112, v48\n\tv_fmac_f32 v81, v113, v49\n\t"
            "v_fmac_f32 v82, v114, v50\n\tv_fmac_f32 v83, v115, v51\n\tv_fmac_f32 v84, v116, v52\n\tv_fmac_f32 v85, v117, v53\n\tv_fmac_f32 v86, v118, v54\n\tv_fmac_f32 v87, v119, v55\n\t"
            "v_fmac_f32 v88, v120, v56\n\tv_fmac_f32 v89, v121, v57\n\tv_fmac_f32 v90, v122, v58\n\tv_fmac_f32 v91, v123, v59\n\tv_fmac_f32 v92, v124, v60\n\tv_fmac_f32 v93, v125, v61\n\t"
            "v_fmac_f32 v94, v126, v62\n\tv_fmac_f32 v95, v127, v63\n\tv_cvt_pk_bf16_f32 v248, v80, v81\n\tv_cvt_pk_bf16_f32 v250, v84, v85\n\tv_cvt_pk_bf16_f32 v249, v82, v83\n\tv_cvt_pk_bf16_f32 v251, v86, v87\n\t"
            "v_cvt_pk_bf16_f32 v252, v88, v89\n\tv_cvt_pk_bf16_f32 v254, v92, v93\n\tv_cvt_pk_bf16_f32 v253, v90, v91\n\tv_cvt_pk_bf16_f32 v255, v94, v95\n\tv_permlane32_swap_b32 v248, v250\n\tv_permlane32_swap_b32 v249, v251\n\t"
            "v_permlane32_swap_b32 v252, v254\n\tv_permlane32_swap_b32 v253, v255\n\ts_nop 0\n\tv_permlane16_swap_b32 v248, v252\n\tv_permlane16_swap_b32 v250, v254\n\tv_permlane16_swap_b32 v249, v253\n\t"
            "v_permlane16_swap_b32 v251, v255\n\tbuffer_store_dwordx4 v[248:251], v3, s[52:55], 0 offen offset:64 sc1\n\tbuffer_store_dwordx4 v[252:255], v11, s[52:55], 0 offen offset:64 sc1\n\t9:\n"
            RCNT DUMP
            "s_endpgm\n\t.LG0:\nv_mul_f32 v21, 0xbfb8aa3b, v21\n\tv_mul_f32 v20, v20, v21\n\tv_permlane16_swap_b32 v240, v244\n\tv_permlane16_swap_b32 v241, v245\n\t"
            "v_permlane16_swap_b32 v242, v246\n\tv_permlane16_swap_b32 v243, v247\n\tv_permlane16_swap_b32 v248, v252\n\tv_permlane16_swap_b32 v249, v253\n\tv_permlane16_swap_b32 v250, v254\n\tv_permlane16_swap_b32 v251, v255\n\t"
            "v_permlane32_swap_b32 v240, v242\n\tv_permlane32_swap_b32 v241, v243\n\tv_permlane32_swap_b32 v244, v246\n\tv_permlane32_swap_b32 v245, v247\n\tv_permlane32_swap_b32 v248, v250\n\tv_permlane32_swap_b32 v249, v251\n\t"
            "v_permlane32_swap_b32 v252, v254\n\tv_permlane32_swap_b32 v253, v255\n\tv_lshlrev_b32 v64, 16, v240\n\tv_and_b32 v65, 0xffff0000, v240\n\tv_lshlrev_b32 v66, 16, v241\n\tv_and_b32 v67, 0xffff0000, v241\n\t"
            "v_lshlrev_b32 v68, 16, v242\n\tv_and_b32 v69, 0xffff0000, v242\n\tv_lshlrev_b32 v70, 16, v243\n\tv_and_b32 v71, 0xffff0000, v243\n\tv_lshlrev_b32 v72, 16, v244\n\tv_and_b32 v73, 0xffff0000, v244\n\t"
            "v_lshlrev_b32 v74, 16, v245\n\tv_and_b32 v75, 0xffff0000, v245\n\tv_lshlrev_b32 v76, 16, v246\n\tv_and_b32 v77, 0xffff0000, v246\n\tv_lshlrev_b32 v78, 16, v247\n\tv_and_b32 v79, 0xffff0000, v247\n\t"
            "v_lshlrev_b32 v80, 16, v248\n\tv_and_b32 v81, 0xffff0000, v248\n\tv_lshlrev_b32 v82, 16, v249\n\tv_and_b32 v83, 0xffff0000, v249\n\tv_lshlrev_b32 v84, 16, v250\n\tv_and_b32 v85, 0xffff0000, v250\n\t"
            "v_lshlrev_b32 v86, 16, v251\n\tv_and_b32 v87, 0xffff0000, v251\n\tv_lshlrev_b32 v88, 16, v252\n\tv_and_b32 v89, 0xffff0000, v252\n\tv_lshlrev_b32 v90, 16, v253\n\tv_and_b32 v91, 0xffff0000, v253\n\t"
            "v_lshlrev_b32 v92, 16, v254\n\tv_and_b32 v93, 0xffff0000, v254\n\tv_lshlrev_b32 v94, 16, v255\n\tv_and_b32 v95, 0xffff0000, v255\n\tv_fma_f32 v96, v64, v21, v20\n\tv_fma_f32 v97, v65, v21, v20\n\t"
            "v_fma_f32 v98, v66, v21, v20\n\tv_fma_f32 v99, v67, v21, v20\n\tv_fma_f32 v100, v68, v21, v20\n\tv_fma_f32 v101, v69, v21, v20\n\tv_fma_f32 v102, v70, v21, v20\n\tv_fma_f32 v103, v71, v21, v20\n\t"
            "v_fma_f32 v104, v72, v21, v20\n\tv_fma_f32 v105, v73, v21, v20\n\tv_fma_f32 v106, v74, v21, v20\n\tv_fma_f32 v107, v75, v21, v20\n\tv_fma_f32 v108, v76, v21, v20\n\tv_fma_f32 v109, v77, v21, v20\n\t"
            "v_fma_f32 v110, v78, v21, v20\n\tv_fma_f32 v111, v79, v21, v20\n\tv_fma_f32 v112, v80, v21, v20\n\tv_fma_f32 v113, v81, v21, v20\n\tv_fma_f32 v114, v82, v21, v20\n\tv_fma_f32 v115, v83, v21, v20\n\t"
            "v_fma_f32 v116, v84, v21, v20\n\tv_fma_f32 v117, v85, v21, v20\n\tv_fma_f32 v118, v86, v21, v20\n\tv_fma_f32 v119, v87, v21, v20\n\tv_fma_f32 v120, v88, v21, v20\n\tv_fma_f32 v121, v89, v21, v20\n\t"
            "v_fma_f32 v122, v90, v21, v20\n\tv_fma_f32 v123, v91, v21, v20\n\tv_fma_f32 v124, v92, v21, v20\n\tv_fma_f32 v125, v93, v21, v20\n\tv_fma_f32 v126, v94, v21, v20\n\tv_fma_f32 v127, v95, v21, v20\n\t"
            "s_setpc_b64 s[76:77]\n\t.LG1:\nv_exp_f32 v32, v96\n\tv_exp_f32 v33, v97\n\tv_exp_f32 v34, v98\n\tv_exp_f32 v35, v99\n\t"
            "v_exp_f32 v36, v100\n\tv_exp_f32 v37, v101\n\tv_exp_f32 v38, v102\n\tv_exp_f32 v39, v103\n\tv_exp_f32 v40, v104\n\tv_exp_f32 v41, v105\n\t"
            "v_exp_f32 v42, v106\n\tv_exp_f32 v43, v107\n\tv_exp_f32 v44, v108\n\tv_exp_f32 v45, v109\n\tv_exp_f32 v46, v110\n\tv_exp_f32 v47, v111\n\t"
            "v_exp_f32 v48, v112\n\tv_exp_f32 v49, v113\n\tv_exp_f32 v50, v114\n\tv_exp_f32 v51, v115\n\tv_exp_f32 v52, v116\n\tv_exp_f32 v53, v117\n\t"
            "v_exp_f32 v54, v118\n\tv_exp_f32 v55, v119\n\tv_exp_f32 v56, v120\n\tv_exp_f32 v57, v121\n\tv_exp_f32 v58, v122\n\tv_exp_f32 v59, v123\n\t"
            "v_exp_f32 v60, v124\n\tv_exp_f32 v61, v125\n\tv_exp_f32 v62, v126\n\tv_exp_f32 v63, v127\n\tv_add_f32 v32, 1.0, v32\n\tv_add_f32 v33, 1.0, v33\n\t"
            "v_add_f32 v34, 1.0, v34\n\tv_add_f32 v35, 1.0, v35\n\tv_add_f32 v36, 1.0, v36\n\tv_add_f32 v37, 1.0, v37\n\tv_add_f32 v38, 1.0, v38\n\tv_add_f32 v39, 1.0, v39\n\t"
            "v_add_f32 v40, 1.0, v40\n\tv_add_f32 v41, 1.0, v41\n\tv_add_f32 v42, 1.0, v42\n\tv_add_f32 v43, 1.0, v43\n\tv_add_f32 v44, 1.0, v44\n\tv_add_f32 v45, 1.0, v45\n\t"
            "v_add_f32 v46, 1.0, v46\n\tv_add_f32 v47, 1.0, v47\n\tv_add_f32 v48, 1.0, v48\n\tv_add_f32 v49, 1.0, v49\n\tv_add_f32 v50, 1.0, v50\n\tv_add_f32 v51, 1.0, v51\n\t"
            "v_add_f32 v52, 1.0, v52\n\tv_add_f32 v53, 1.0, v53\n\tv_add_f32 v54, 1.0, v54\n\tv_add_f32 v55, 1.0, v55\n\tv_add_f32 v56, 1.0, v56\n\tv_add_f32 v57, 1.0, v57\n\t"
            "v_add_f32 v58, 1.0, v58\n\tv_add_f32 v59, 1.0, v59\n\tv_add_f32 v60, 1.0, v60\n\tv_add_f32 v61, 1.0, v61\n\tv_add_f32 v62, 1.0, v62\n\tv_add_f32 v63, 1.0, v63\n\t"
            "v_rcp_f32 v32, v32\n\tv_rcp_f32 v33, v33\n\tv_rcp_f32 v34, v34\n\tv_rcp_f32 v35, v35\n\ts_mov_b32 s83, 1\n\ts_setpc_b64 s[76:77]\n\t"
            ".LG2:\nv_rcp_f32 v36, v36\n\tv_rcp_f32 v37, v37\n\tv_rcp_f32 v38, v38\n\tv_rcp_f32 v39, v39\n\tv_rcp_f32 v40, v40\n\t"
            "v_rcp_f32 v41, v41\n\tv_rcp_f32 v42, v42\n\tv_rcp_f32 v43, v43\n\tv_rcp_f32 v44, v44\n\tv_rcp_f32 v45, v45\n\tv_rcp_f32 v46, v46\n\t"
            "v_rcp_f32 v47, v47\n\tv_rcp_f32 v48, v48\n\tv_rcp_f32 v49, v49\n\tv_rcp_f32 v50, v50\n\tv_rcp_f32 v51, v51\n\tv_rcp_f32 v52, v52\n\t"
            "v_rcp_f32 v53, v53\n\tv_rcp_f32 v54, v54\n\tv_rcp_f32 v55, v55\n\tv_rcp_f32 v56, v56\n\tv_rcp_f32 v57, v57\n\tv_rcp_f32 v58, v58\n\t"
            "v_rcp_f32 v59, v59\n\tv_rcp_f32 v60, v60\n\tv_rcp_f32 v61, v61\n\tv_rcp_f32 v62, v62\n\tv_rcp_f32 v63, v63\n\tv_mul_f32 v32, v32, v96\n\t"
            "v_mul_f32 v33, v33, v97\n\tv_mul_f32 v34, v34, v98\n\tv_mul_f32 v35, v35, v99\n\tv_mul_f32 v36, v36, v100\n\tv_mul_f32 v37, v37, v101\n\tv_mul_f32 v38, v38, v102\n\t"
            "v_mul_f32 v39, v39, v103\n\tv_mul_f32 v40, v40, v104\n\tv_mul_f32 v41, v41, v105\n\tv_mul_f32 v42, v42, v106\n\tv_mul_f32 v43, v43, v107\n\tv_mul_f32 v44, v44, v108\n\t"
            "v_mul_f32 v45, v45, v109\n\tv_mul_f32 v46, v46, v110\n\tv_mul_f32 v47, v47, v111\n\ts_mov_b32 s83, 2\n\ts_setpc_b64 s[76:77]\n\t7:\n"
            "ds_read_b32 v16, v8 offset:64\n\tds_read_b32 v17, v9 offset:1344\n\ts_waitcnt lgkmcnt(0)\n\tv_cmp_ne_u32 s[74:75], 0, v17\n\tv_cmp_eq_u32 vcc, 0, v16\n\ts_cbranch_vccz 71f\n\t"
            "s_sleep 1\n\ts_sub_u32 s68, s68, 1\n\ts_cbranch_scc0 7b\n\ts_branch 9b\n\t71:\nv_lshrrev_b32 v12, 5, v2\n\t"
            "v_and_b32 v12, 16, v12\n\tv_add_u32 v12, v12, v17\n\ts_pack_ll_b32_b16 s78, s74, s74\n\ts_mov_b32 s79, s78\n\ts_pack_hh_b32_b16 s80, s74, s74\n\ts_mov_b32 s81, s80\n\t"
            "v_cndmask_b32 v3, v3, 2.0, s[78:79]\n\tv_cndmask_b32 v11, v11, 2.0, s[80:81]\n\ts_mov_b64 exec, s[74:75]\n\ts_cbranch_execz 72f\n\tds_write_b128 v12, v[96:99] offset:0\n\tds_write_b128 v12, v[100:103] offset:32\n\t"
            "ds_write_b128 v12, v[104:107] offset:64\n\tds_write_b128 v12, v[108:111] offset:96\n\t72:\ns_mov_b64 exec, -1\n\ts_branch 8b\n\t73:\n"
            "s_nop 7\n\ts_nop 3\n\ts_mov_b64 exec, s[74:75]\n\tds_write_b128 v12, v[112:115] offset:128\n\tds_write_b128 v12, v[116:119] offset:160\n\tds_write_b128 v12, v[120:123] offset:192\n\t"
            "ds_write_b128 v12, v[124:127] offset:224\n\tds_write_b32 v17, v17 offset:256\n\ts_mov_b64 exec, -1\n\ts_branch 74b\n\t5:\ns_sleep 1\n\t"
            "ds_read_b64 v[14:15], v8 offset:40\n\ts_waitcnt lgkmcnt(0)\n\tv_min_u32 v14, v14, v15\n\tv_cmp_ge_u32 vcc, s71, v14\n\ts_cbranch_vccz 61f\n\ts_sub_u32 s68, s68, 1\n\t"
            "s_cbranch_scc0 5b\n\ts_branch 9b\n\t61:\n"
            TRG2D
            "s_branch 6b\n\t62:\nds_read_b128 v[16:19], v8 offset:48\n\tds_read_b64_tr_b16 v[128:129], v4 offset:40960\n\tds_read_b64_tr_b16 v[130:131], v4 offset:41472\n\tds_read_b64_tr_b16 v[132:133], v5 offset:40960\n\t"
            "ds_read_b64_tr_b16 v[134:135], v5 offset:41472\n\tds_read_b64_tr_b16 v[136:137], v6 offset:40960\n\tds_read_b64_tr_b16 v[138:139], v6 offset:41472\n\tds_read_b64_tr_b16 v[140:141], v7 offset:40960\n\tds_read_b64_tr_b16 v[142:143], v7 offset:41472\n\tds_read_b64_tr_b16 v[144:145], v4 offset:41984\n\t"
            "ds_read_b64_tr_b16 v[146:147], v4 offset:42496\n\tds_read_b64_tr_b16 v[148:149], v5 offset:41984\n\tds_read_b64_tr_b16 v[150:151], v5 offset:42496\n\tds_read_b64_tr_b16 v[152:153], v6 offset:41984\n\tds_read_b64_tr_b16 v[154:155], v6 offset:42496\n\tds_read_b64_tr_b16 v[156:157], v7 offset:41984\n\t"
            "ds_read_b64_tr_b16 v[158:159], v7 offset:42496\n\ts_waitcnt lgkmcnt(15)\n\tv_min3_u32 v16, v16, v17, v18\n\tv_min_u32 v16, v16, v19\n\tv_cmp_eq_u32 vcc, 0, v16\n\ts_cbranch_vccz 63b\n\t"
            "s_sub_u32 s68, s68, 1\n\ts_cbranch_scc0 62b\n\ts_branch 9b\n\t100:\ns_mov_b32 s70, 4\n\ts_call_b64 s[72:73], 300f\n\t"
            "v_max3_u32 v12, v176, v178, v180\n\tv_max_u32 v12, v12, v182\n\tv_cmp_eq_u32 vcc, -1, v12\n\ts_cbranch_vccnz 100b\n\ts_branch 200b\n\t101:\n"
            "s_mov_b32 s70, 5\n\ts_call_b64 s[72:73], 301f\n\tv_max3_u32 v12, v184, v186, v188\n\tv_max_u32 v12, v12, v190\n\tv_cmp_eq_u32 vcc, -1, v12\n\ts_cbranch_vccnz 101b\n\t"
            "s_branch 201b\n\t102:\ns_mov_b32 s70, 6\n\ts_call_b64 s[72:73], 302f\n\tv_max3_u32 v12, v192, v194, v196\n\tv_max_u32 v12, v12, v198\n\t"
            "v_cmp_eq_u32 vcc, -1, v12\n\ts_cbranch_vccnz 102b\n\ts_branch 202b\n\t103:\ns_mov_b32 s70, 7\n\ts_call_b64 s[72:73], 303f\n\t"
            "v_max3_u32 v12, v200, v202, v204\n\tv_max_u32 v12, v12, v206\n\tv_cmp_eq_u32 vcc, -1, v12\n\ts_cbranch_vccnz 103b\n\ts_branch 203b\n\t104:\n"
            "s_mov_b32 s70, 8\n\ts_call_b64 s[72:73], 304f\n\tv_max3_u32 v12, v208, v210, v212\n\tv_max_u32 v12, v12, v214\n\tv_cmp_eq_u32 vcc, -1, v12\n\ts_cbranch_vccnz 104b\n\t"
            "s_branch 204b\n\t105:\ns_mov_b32 s70, 9\n\ts_call_b64 s[72:73], 305f\n\tv_max3_u32 v12, v216, v218, v220\n\tv_max_u32 v12, v12, v222\n\t"
            "v_cmp_eq_u32 vcc, -1, v12\n\ts_cbranch_vccnz 105b\n\ts_branch 205b\n\t106:\ns_mov_b32 s70, 10\n\ts_call_b64 s[72:73], 306f\n\t"
            "v_max3_u32 v12, v224, v226, v228\n\tv_max_u32 v12, v12, v230\n\tv_cmp_eq_u32 vcc, -1, v12\n\ts_cbranch_vccnz 106b\n\ts_branch 206b\n\t107:\n"
            "s_mov_b32 s70, 11\n\ts_call_b64 s[72:73], 307f\n\tv_max3_u32 v12, v232, v234, v236\n\tv_max_u32 v12, v12, v238\n\tv_cmp_eq_u32 vcc, -1, v12\n\ts_cbranch_vccnz 107b\n\t"
            "s_branch 207b\n\t108:\ns_mov_b32 s70, 12\n\ts_call_b64 s[72:73], 308f\n\tv_max3_u32 v12, v176, v178, v180\n\tv_max_u32 v12, v12, v182\n\t"
            "v_cmp_eq_u32 vcc, -1, v12\n\ts_cbranch_vccnz 108b\n\ts_branch 208b\n\t109:\ns_mov_b32 s70, 13\n\ts_call_b64 s[72:73], 309f\n\t"
            "v_max3_u32 v12, v184, v186, v188\n\tv_max_u32 v12, v12, v190\n\tv_cmp_eq_u32 vcc, -1, v12\n\ts_cbranch_vccnz 109b\n\ts_branch 209b\n\t110:\n"
            "s_mov_b32 s70, 14\n\ts_call_b64 s[72:73], 310f\n\tv_max3_u32 v12, v192, v194, v196\n\tv_max_u32 v12, v12, v198\n\tv_cmp_eq_u32 vcc, -1, v12\n\ts_cbranch_vccnz 110b\n\t"
            "s_branch 210b\n\t111:\ns_mov_b32 s70, 15\n\ts_call_b64 s[72:73], 311f\n\tv_max3_u32 v12, v200, v202, v204\n\tv_max_u32 v12, v12, v206\n\t"
            "v_cmp_eq_u32 vcc, -1, v12\n\ts_cbranch_vccnz 111b\n\ts_branch 211b\n\t112:\ns_mov_b32 s70, 16\n\ts_call_b64 s[72:73], 312f\n\t"
            "v_max3_u32 v12, v208, v210, v212\n\tv_max_u32 v12, v12, v214\n\tv_cmp_eq_u32 vcc, -1, v12\n\ts_cbranch_vccnz 112b\n\ts_branch 212b\n\t113:\n"
            "s_mov_b32 s70, 17\n\ts_call_b64 s[72:73], 313f\n\tv_max3_u32 v12, v216, v218, v220\n\tv_max_u32 v12, v12, v222\n\tv_cmp_eq_u32 vcc, -1, v12\n\ts_cbranch_vccnz 113b\n\t"
            "s_branch 213b\n\t114:\ns_mov_b32 s70, 18\n\ts_call_b64 s[72:73], 314f\n\tv_max3_u32 v12, v224, v226, v228\n\tv_max_u32 v12, v12, v230\n\t"
            "v_cmp_eq_u32 vcc, -1, v12\n\ts_cbranch_vccnz 114b\n\ts_branch 214b\n\t115:\ns_mov_b32 s70, 19\n\ts_call_b64 s[72:73], 315f\n\t"
            "v_max3_u32 v12, v232, v234, v236\n\tv_max_u32 v12, v12, v238\n\tv_cmp_eq_u32 vcc, -1, v12\n\ts_cbranch_vccnz 115b\n\ts_branch 215b\n\t116:\n"
            "s_mov_b32 s70, 19\n\ts_call_b64 s[72:73], 316f\n\tv_max3_u32 v12, v176, v178, v180\n\tv_max_u32 v12, v12, v182\n\tv_cmp_eq_u32 vcc, -1, v12\n\ts_cbranch_vccnz 116b\n\t"
            "s_branch 216b\n\t117:\ns_mov_b32 s70, 19\n\ts_call_b64 s[72:73], 317f\n\tv_max3_u32 v12, v184, v186, v188\n\tv_max_u32 v12, v12, v190\n\t"
            "v_cmp_eq_u32 vcc, -1, v12\n\ts_cbranch_vccnz 117b\n\ts_branch 217b\n\t118:\ns_mov_b32 s70, 19\n\ts_call_b64 s[72:73], 318f\n\t"
            "v_max3_u32 v12, v192, v194, v196\n\tv_max_u32 v12, v12, v198\n\tv_cmp_eq_u32 vcc, -1, v12\n\ts_cbranch_vccnz 118b\n\ts_branch 218b\n\t119:\n"
            "s_mov_b32 s70, 19\n\ts_call_b64 s[72:73], 319f\n\tv_max3_u32 v12, v200, v202, v204\n\tv_max_u32 v12, v12, v206\n\tv_cmp_eq_u32 vcc, -1, v12\n\ts_cbranch_vccnz 119b\n\t"
            "s_branch 219b\n\t400:\nv_max3_u32 v12, v176, v178, v180\n\tv_max_u32 v12, v12, v182\n\tv_cmp_eq_u32 vcc, -1, v12\n\ts_cbranch_vccz 11f\n\t"
            "buffer_load_dwordx4 v[176:179], v2, s[44:47], s4 offen offset:1024 sc1\n\tbuffer_load_dwordx4 v[180:183], v2, s[44:47], s4 offen offset:2048 sc1\n\ts_branch 11f\n\t300:\nbuffer_load_dwordx4 v[176:179], v2, s[44:47], s4 offen offset:1024 sc1\n\tbuffer_load_dwordx4 v[180:183], v2, s[44:47], s4 offen offset:2048 sc1\n\t"
            "s_waitcnt vmcnt(2)\n\t11:\ns_cmp_eq_u32 s70, 0\n\ts_cbranch_scc0 401f\n\ts_branch 12f\n\t401:\n"
            "v_max3_u32 v12, v184, v186, v188\n\tv_max_u32 v12, v12, v190\n\tv_cmp_eq_u32 vcc, -1, v12\n\ts_cbranch_vccz 11f\n\tbuffer_load_dwordx4 v[184:187], v2, s[44:47], s5 offen offset:1024 sc1\n\tbuffer_load_dwordx4 v[188:191], v2, s[44:47], s5 offen offset:2048 sc1\n\t"
            "s_branch 11f\n\t301:\nbuffer_load_dwordx4 v[184:187], v2, s[44:47], s5 offen offset:1024 sc1\n\tbuffer_load_dwordx4 v[188:191], v2, s[44:47], s5 offen offset:2048 sc1\n\ts_waitcnt vmcnt(2)\n\t11:\n"
            "s_cmp_eq_u32 s70, 1\n\ts_cbranch_scc0 402f\n\ts_branch 12f\n\t402:\nv_max3_u32 v12, v192, v194, v196\n\tv_max_u32 v12, v12, v198\n\t"
            "v_cmp_eq_u32 vcc, -1, v12\n\ts_cbranch_vccz 11f\n\tbuffer_load_dwordx4 v[192:195], v2, s[44:47], s6 offen offset:1024 sc1\n\tbuffer_load_dwordx4 v[196:199], v2, s[44:47], s6 offen offset:2048 sc1\n\ts_branch 11f\n\t302:\n"
            "buffer_load_dwordx4 v[192:195], v2, s[44:47], s6 offen offset:1024 sc1\n\tbuffer_load_dwordx4 v[196:199], v2, s[44:47], s6 offen offset:2048 sc1\n\ts_waitcnt vmcnt(2)\n\t11:\ns_cmp_eq_u32 s70, 2\n\ts_cbranch_scc0 403f\n\t"
            "s_branch 12f\n\t403:\nv_max3_u32 v12, v200, v202, v204\n\tv_max_u32 v12, v12, v206\n\tv_cmp_eq_u32 vcc, -1, v12\n\ts_cbranch_vccz 11f\n\t"
            "buffer_load_dwordx4 v[200:203], v2, s[44:47], s7 offen offset:1024 sc1\n\tbuffer_load_dwordx4 v[204:207], v2, s[44:47], s7 offen offset:2048 sc1\n\ts_branch 11f\n\t303:\nbuffer_load_dwordx4 v[200:203], v2, s[44:47], s7 offen offset:1024 sc1\n\tbuffer_load_dwordx4 v[204:207], v2, s[44:47], s7 offen offset:2048 sc1\n\t"
            "s_waitcnt vmcnt(2)\n\t11:\ns_cmp_eq_u32 s70, 3\n\ts_cbranch_scc0 404f\n\ts_branch 12f\n\t404:\n"
            "v_max3_u32 v12, v208, v210, v212\n\tv_max_u32 v12, v12, v214\n\tv_cmp_eq_u32 vcc, -1, v12\n\ts_cbranch_vccz 11f\n\tbuffer_load_dwordx4 v[208:211], v2, s[44:47], s8 offen offset:1024 sc1\n\tbuffer_load_dwordx4 v[212:215], v2, s[44:47], s8 offen offset:2048 sc1\n\t"
            "s_branch 11f\n\t304:\nbuffer_load_dwordx4 v[208:211], v2, s[44:47], s8 offen offset:1024 sc1\n\tbuffer_load_dwordx4 v[212:215], v2, s[44:47], s8 offen offset:2048 sc1\n\ts_waitcnt vmcnt(2)\n\t11:\n"
            "s_cmp_eq_u32 s70, 4\n\ts_cbranch_scc0 405f\n\ts_branch 12f\n\t405:\nv_max3_u32 v12, v216, v218, v220\n\tv_max_u32 v12, v12, v222\n\t"
            "v_cmp_eq_u32 vcc, -1, v12\n\ts_cbranch_vccz 11f\n\tbuffer_load_dwordx4 v[216:219], v2, s[44:47], s9 offen offset:1024 sc1\n\tbuffer_load_dwordx4 v[220:223], v2, s[44:47], s9 offen offset:2048 sc1\n\ts_branch 11f\n\t305:\n"
            "buffer_load_dwordx4 v[216:219], v2, s[44:47], s9 offen offset:1024 sc1\n\tbuffer_load_dwordx4 v[220:223], v2, s[44:47], s9 offen offset:2048 sc1\n\ts_waitcnt vmcnt(2)\n\t11:\ns_cmp_eq_u32 s70, 5\n\ts_cbranch_scc0 406f\n\t"
            "s_branch 12f\n\t406:\nv_max3_u32 v12, v224, v226, v228\n\tv_max_u32 v12, v12, v230\n\tv_cmp_eq_u32 vcc, -1, v12\n\ts_cbranch_vccz 11f\n\t"
            "buffer_load_dwordx4 v[224:227], v2, s[44:47], s10 offen offset:1024 sc1\n\tbuffer_load_dwordx4 v[228:231], v2, s[44:47], s10 offen offset:2048 sc1\n\ts_branch 11f\n\t306:\nbuffer_load_dwordx4 v[224:227], v2, s[44:47], s10 offen offset:1024 sc1\n\tbuffer_load_dwordx4 v[228:231], v2, s[44:47], s10 offen offset:2048 sc1\n\t"
            "s_waitcnt vmcnt(2)\n\t11:\ns_cmp_eq_u32 s70, 6\n\ts_cbranch_scc0 407f\n\ts_branch 12f\n\t407:\n"
            "v_max3_u32 v12, v232, v234, v236\n\tv_max_u32 v12, v12, v238\n\tv_cmp_eq_u32 vcc, -1, v12\n\ts_cbranch_vccz 11f\n\tbuffer_load_dwordx4 v[232:235], v2, s[44:47], s11 offen offset:1024 sc1\n\tbuffer_load_dwordx4 v[236:239], v2, s[44:47], s11 offen offset:2048 sc1\n\t"
            "s_branch 11f\n\t307:\nbuffer_load_dwordx4 v[232:235], v2, s[44:47], s11 offen offset:1024 sc1\n\tbuffer_load_dwordx4 v[236:239], v2, s[44:47], s11 offen offset:2048 sc1\n\ts_waitcnt vmcnt(2)\n\t11:\n"
            "s_cmp_eq_u32 s70, 7\n\ts_cbranch_scc0 408f\n\ts_branch 12f\n\t408:\nv_max3_u32 v12, v176, v178, v180\n\tv_max_u32 v12, v12, v182\n\t"
            "v_cmp_eq_u32 vcc, -1, v12\n\ts_cbranch_vccz 11f\n\tbuffer_load_dwordx4 v[176:179], v2, s[44:47], s12 offen offset:1024 sc1\n\tbuffer_load_dwordx4 v[180:183], v2, s[44:47], s12 offen offset:2048 sc1\n\ts_branch 11f\n\t308:\n"
            "buffer_load_dwordx4 v[176:179], v2, s[44:47], s12 offen offset:1024 sc1\n\tbuffer_load_dwordx4 v[180:183], v2, s[44:47], s12 offen offset:2048 sc1\n\ts_waitcnt vmcnt(2)\n\t11:\ns_cmp_eq_u32 s70, 8\n\ts_cbranch_scc0 409f\n\t"
            "s_branch 12f\n\t409:\nv_max3_u32 v12, v184, v186, v188\n\tv_max_u32 v12, v12, v190\n\tv_cmp_eq_u32 vcc, -1, v12\n\ts_cbranch_vccz 11f\n\t"
            "buffer_load_dwordx4 v[184:187], v2, s[44:47], s13 offen offset:1024 sc1\n\tbuffer_load_dwordx4 v[188:191], v2, s[44:47], s13 offen offset:2048 sc1\n\ts_branch 11f\n\t309:\nbuffer_load_dwordx4 v[184:187], v2, s[44:47], s13 offen offset:1024 sc1\n\tbuffer_load_dwordx4 v[188:191], v2, s[44:47], s13 offen offset:2048 sc1\n\t"
            "s_waitcnt vmcnt(2)\n\t11:\ns_cmp_eq_u32 s70, 9\n\ts_cbranch_scc0 410f\n\ts_branch 12f\n\t410:\n"
            "v_max3_u32 v12, v192, v194, v196\n\tv_max_u32 v12, v12, v198\n\tv_cmp_eq_u32 vcc, -1, v12\n\ts_cbranch_vccz 11f\n\tbuffer_load_dwordx4 v[192:195], v2, s[44:47], s4 offen offset:0 sc1\n\tbuffer_load_dwordx4 v[196:199], v2, s[44:47], s4 offen offset:3072 sc1\n\t"
            "s_branch 11f\n\t310:\nbuffer_load_dwordx4 v[192:195], v2, s[44:47], s4 offen offset:0 sc1\n\tbuffer_load_dwordx4 v[196:199], v2, s[44:47], s4 offen offset:3072 sc1\n\ts_waitcnt vmcnt(2)\n\t11:\n"
            "s_cmp_eq_u32 s70, 10\n\ts_cbranch_scc0 411f\n\ts_branch 12f\n\t411:\nv_max3_u32 v12, v200, v202, v204\n\tv_max_u32 v12, v12, v206\n\t"
            "v_cmp_eq_u32 vcc, -1, v12\n\ts_cbranch_vccz 11f\n\tbuffer_load_dwordx4 v[200:203], v2, s[44:47], s5 offen offset:0 sc1\n\tbuffer_load_dwordx4 v[204:207], v2, s[44:47], s5 offen offset:3072 sc1\n\ts_branch 11f\n\t311:\n"
            "buffer_load_dwordx4 v[200:203], v2, s[44:47], s5 offen offset:0 sc1\n\tbuffer_load_dwordx4 v[204:207], v2, s[44:47], s5 offen offset:3072 sc1\n\ts_waitcnt vmcnt(2)\n\t11:\ns_cmp_eq_u32 s70, 11\n\ts_cbranch_scc0 412f\n\t"
            "s_branch 12f\n\t412:\nv_max3_u32 v12, v208, v210, v212\n\tv_max_u32 v12, v12, v214\n\tv_cmp_eq_u32 vcc, -1, v12\n\ts_cbranch_vccz 11f\n\t"
            "buffer_load_dwordx4 v[208:211], v2, s[44:47], s6 offen offset:0 sc1\n\tbuffer_load_dwordx4 v[212:215], v2, s[44:47], s6 offen offset:3072 sc1\n\ts_branch 11f\n\t312:\nbuffer_load_dwordx4 v[208:211], v2, s[44:47], s6 offen offset:0 sc1\n\tbuffer_load_dwordx4 v[212:215], v2, s[44:47], s6 offen offset:3072 sc1\n\t"
            "s_waitcnt vmcnt(2)\n\t11:\ns_cmp_eq_u32 s70, 12\n\ts_cbranch_scc0 413f\n\ts_branch 12f\n\t413:\n"
            "v_max3_u32 v12, v216, v218, v220\n\tv_max_u32 v12, v12, v222\n\tv_cmp_eq_u32 vcc, -1, v12\n\ts_cbranch_vccz 11f\n\tbuffer_load_dwordx4 v[216:219], v2, s[44:47], s7 offen offset:0 sc1\n\tbuffer_load_dwordx4 v[220:223], v2, s[44:47], s7 offen offset:3072 sc1\n\t"
            "s_branch 11f\n\t313:\nbuffer_load_dwordx4 v[216:219], v2, s[44:47], s7 offen offset:0 sc1\n\tbuffer_load_dwordx4 v[220:223], v2, s[44:47], s7 offen offset:3072 sc1\n\ts_waitcnt vmcnt(2)\n\t11:\n"
            "s_cmp_eq_u32 s70, 13\n\ts_cbranch_scc0 414f\n\ts_branch 12f\n\t414:\nv_max3_u32 v12, v224, v226, v228\n\tv_max_u32 v12, v12, v230\n\t"
            "v_cmp_eq_u32 vcc, -1, v12\n\ts_cbranch_vccz 11f\n\tbuffer_load_dwordx4 v[224:227], v2, s[44:47], s8 offen offset:0 sc1\n\tbuffer_load_dwordx4 v[228:231], v2, s[44:47], s8 offen offset:3072 sc1\n\ts_branch 11f\n\t314:\n"
            "buffer_load_dwordx4 v[224:227], v2, s[44:47], s8 offen offset:0 sc1\n\tbuffer_load_dwordx4 v[228:231], v2, s[44:47], s8 offen offset:3072 sc1\n\ts_waitcnt vmcnt(2)\n\t11:\ns_cmp_eq_u32 s70, 14\n\ts_cbranch_scc0 415f\n\t"
            "s_branch 12f\n\t415:\nv_max3_u32 v12, v232, v234, v236\n\tv_max_u32 v12, v12, v238\n\tv_cmp_eq_u32 vcc, -1, v12\n\ts_cbranch_vccz 11f\n\t"
            "buffer_load_dwordx4 v[232:235], v2, s[44:47], s9 offen offset:0 sc1\n\tbuffer_load_dwordx4 v[236:239], v2, s[44:47], s9 offen offset:3072 sc1\n\ts_branch 11f\n\t315:\nbuffer_load_dwordx4 v[232:235], v2, s[44:47], s9 offen offset:0 sc1\n\tbuffer_load_dwordx4 v[236:239], v2, s[44:47], s9 offen offset:3072 sc1\n\t"
            "s_waitcnt vmcnt(2)\n\t11:\ns_cmp_eq_u32 s70, 15\n\ts_cbranch_scc0 416f\n\ts_branch 12f\n\t416:\n"
            "v_max3_u32 v12, v176, v178, v180\n\tv_max_u32 v12, v12, v182\n\tv_cmp_eq_u32 vcc, -1, v12\n\ts_cbranch_vccz 11f\n\tbuffer_load_dwordx4 v[176:179], v2, s[44:47], s10 offen offset:0 sc1\n\tbuffer_load_dwordx4 v[180:183], v2, s[44:47], s10 offen offset:3072 sc1\n\t"
            "s_branch 11f\n\t316:\nbuffer_load_dwordx4 v[176:179], v2, s[44:47], s10 offen offset:0 sc1\n\tbuffer_load_dwordx4 v[180:183], v2, s[44:47], s10 offen offset:3072 sc1\n\ts_waitcnt vmcnt(2)\n\t11:\n"
            "s_cmp_eq_u32 s70, 16\n\ts_cbranch_scc0 417f\n\ts_branch 12f\n\t417:\nv_max3_u32 v12, v184, v186, v188\n\tv_max_u32 v12, v12, v190\n\t"
            "v_cmp_eq_u32 vcc, -1, v12\n\ts_cbranch_vccz 11f\n\tbuffer_load_dwordx4 v[184:187], v2, s[44:47], s11 offen offset:0 sc1\n\tbuffer_load_dwordx4 v[188:191], v2, s[44:47], s11 offen offset:3072 sc1\n\ts_branch 11f\n\t317:\n"
            "buffer_load_dwordx4 v[184:187], v2, s[44:47], s11 offen offset:0 sc1\n\tbuffer_load_dwordx4 v[188:191], v2, s[44:47], s11 offen offset:3072 sc1\n\ts_waitcnt vmcnt(2)\n\t11:\ns_cmp_eq_u32 s70, 17\n\ts_cbranch_scc0 418f\n\t"
            "s_branch 12f\n\t418:\nv_max3_u32 v12, v192, v194, v196\n\tv_max_u32 v12, v12, v198\n\tv_cmp_eq_u32 vcc, -1, v12\n\ts_cbranch_vccz 11f\n\t"
            "buffer_load_dwordx4 v[192:195], v2, s[44:47], s12 offen offset:0 sc1\n\tbuffer_load_dwordx4 v[196:199], v2, s[44:47], s12 offen offset:3072 sc1\n\ts_branch 11f\n\t318:\nbuffer_load_dwordx4 v[192:195], v2, s[44:47], s12 offen offset:0 sc1\n\tbuffer_load_dwordx4 v[196:199], v2, s[44:47], s12 offen offset:3072 sc1\n\t"
            "s_waitcnt vmcnt(2)\n\t11:\ns_cmp_eq_u32 s70, 18\n\ts_cbranch_scc0 419f\n\ts_branch 12f\n\t419:\n"
            "v_max3_u32 v12, v200, v202, v204\n\tv_max_u32 v12, v12, v206\n\tv_cmp_eq_u32 vcc, -1, v12\n\ts_cbranch_vccz 11f\n\tbuffer_load_dwordx4 v[200:203], v2, s[44:47], s13 offen offset:0 sc1\n\tbuffer_load_dwordx4 v[204:207], v2, s[44:47], s13 offen offset:3072 sc1\n\t"
            "s_branch 11f\n\t319:\nbuffer_load_dwordx4 v[200:203], v2, s[44:47], s13 offen offset:0 sc1\n\tbuffer_load_dwordx4 v[204:207], v2, s[44:47], s13 offen offset:3072 sc1\n\ts_waitcnt vmcnt(2)\n\t11:\n"
            "12:\n"
            RINC
            "s_sub_u32 s68, s68, 1\n\ts_cbranch_scc1 9b\n\ts_waitcnt vmcnt(0)\n\ts_setpc_b64 s[72:73]\n\t.subsection 1\n\t.Lce:\n"
            ".subsection 0\n\t.Lout:\ns_waitcnt lgkmcnt(0)\n\t"
            : "=&{s67}"(tag), "=&{v[52:83]}"(sa), "=&{v[84:115]}"(sb), "=&{v[116:123]}"(sc) : [t] "v"(tid), [b] "s"(bid), [k] "s"(ka)
            : "memory", "scc", "vcc", "s4", "s5", "s6", "s7", "s8", "s9", "s10", "s11", "s12", "s13", "s14", "s15", "s16", "s17", "s18", "s19", "s20", "s21", "s22", "s23", "s24", "s25", "s26", "s27", "s28", "s29", "s30", "s31", "s32", "s33", "s34", "s35", "s36", "s37", "s38", "s39", "s40", "s41", "s42", "s43", "s44", "s45", "s46", "s47", "s48", "s49", "s50", "s51", "s52", "s53", "s54", "s55", "s56", "s57", "s58", "s59", "s60", "s61", "s62", "s63", "s64", "s65", "s66", "s68", "s69", "s70", "s71", "s72", "s73", "s74", "s75", "s76", "s77", "s78", "s79", "s80", "s81", "s82", "s83", "s84", "s85", "s86", "s87", "s88", "s89", "s90", "s91", "s92", "s93", "s94", "s95", "s96", "s97", "s98", "s99", "s100", "s101", "v2", "v3", "v4", "v5", "v6", "v7", "v8", "v9", "v10", "v11", "v12", "v13", "v14", "v15", "v16", "v17", "v18", "v19", "v20", "v21", "v22", "v23", "v24", "v25", "v26", "v27", "v28", "v29", "v30", "v31", "v32", "v33", "v34", "v35", "v36", "v37", "v38", "v39", "v40", "v41", "v42", "v43", "v44", "v45", "v46", "v47", "v48", "v49", "v50", "v51", "v124", "v125", "v126", "v127", "v128", "v129", "v130", "v131", "v132", "v133", "v134", "v135", "v136", "v137", "v138", "v139", "v140", "v141", "v142", "v143", "v144", "v145", "v146", "v147", "v148", "v149", "v150", "v151", "v152", "v153", "v154", "v155", "v156", "v157", "v158", "v159", "v160", "v161", "v162", "v163", "v164", "v165", "v166", "v167", "v168", "v169", "v170", "v171", "v172", "v173", "v174", "v175", "v176", "v177", "v178", "v179", "v180", "v181", "v182", "v183", "v184", "v185", "v186", "v187", "v188", "v189", "v190", "v191", "v192", "v193", "v194", "v195", "v196", "v197", "v198", "v199", "v200", "v201", "v202", "v203", "v204", "v205", "v206", "v207", "v208", "v209", "v210", "v211", "v212", "v213", "v214", "v215", "v216", "v217", "v218", "v219", "v220", "v221", "v222", "v223", "v224", "v225", "v226", "v227", "v228", "v229", "v230", "v231", "v232", "v233", "v234", "v235", "v236", "v237", "v238", "v239", "v240", "v241", "v242", "v243", "v244", "v245", "v246", "v247", "v248", "v249", "v250", "v251", "v252", "v253", "v254", "v255");
    tag = RFL(tag);
    asm volatile("" : "+s"(bid), "+s"(ka)); asm volatile("" : "+v"(tid));
    const u16 *x = (const u16*)ka[0], *wo = (const u16*)ka[2], *w_r = (const u16*)ka[3], *wg = (const u16*)ka[4], *wu = (const u16*)ka[5], *wd = (const u16*)ka[6]; char* ws = (char*)ka[7];
    const int wv = __builtin_amdgcn_readfirstlane(tid >> 6);
    const unsigned lds0 = (unsigned)(size_t)(lchar*)smem;
#define LANE() const int lane = lane_id(); const int g = lane >> 4, li = lane & 15, jq = 4 * (li & 3) + (li >> 2); (void)jq
    const int xcd = bid & 7, j = ((bid >> 3) + 25) & 31;
    const bool gemm = j < 25;
    const int fp = xcd >> 1, bl = (j * 13) >> 6, cl = j - bl * 5;
    const int rb = fp * 5 + bl, cg = (xcd & 1) * 5 + cl;
    const int par = fp >> 1, I0 = ((fp & 1) * 5 + bl) * 104 - 8;
    const __amdgpu_buffer_rsrc_t rs = __builtin_amdgcn_make_buffer_rsrc((void*)ws, 0, W_END, 0x00020000);
    auto wait_flag = [&](unsigned a) { SPIN { if (RFL(ldsrv<unsigned>(a))) break; __builtin_amdgcn_s_sleep(1); } MEMB(); };
    auto all4 = [&](u32x4 c) { return RFL(min(min(c.x, c.y), min(c.z, c.w))); };
    unsigned ep = 0;
    auto epld = [&]() { asm volatile("s_load_dword %0, %1, 0x0" : "=s"(ep) : "s"(ws) : "memory"); };
    auto mktag = [&]() { asm volatile("s_waitcnt lgkmcnt(0)" : "+s"(ep) :: "memory"); tag = ep * 1664525u + 1013904223u; };
        if (gemm) {
            LANE();
            if (RP0) __builtin_amdgcn_s_setprio(RP0);
            wait_flag(lds0 + FL_W);
            const u32x4 kk = ldsrv<u32x4>(lds0 + L_KEY + lane * 16);
            const uint2 t2 = top2wave(lane < 52 ? kk.x : 0u, lane < 52 ? kk.z : 0u);
            {
                const unsigned rr = (0xffffu - ((lane ? t2.y : t2.x) & 0xffffu)) & 2047u; const int pos = (int)(rr >> 1) - I0;
                if (lane < 2 && (int)(rr & 1) == par && pos >= 8 && pos < 112) ldsw<unsigned>(lds0 + L_RM + ((pos & ~15) + 4 * (pos & 3) + ((pos >> 2) & 3)) * 4, L_RG + lane * 288);
                ldsw<unsigned>(lds0 + FL_R, 1u);
            }
            unsigned rr[2]; bool own[2]; float xr_[2], xn[2], sg[2];
#pragma unroll
            for (int k = 0; k < 2; ++k) { rr[k] = (0xffffu - ((k ? t2.y : t2.x) & 0xffffu)) & 2047u; const int pos = (int)(rr[k] >> 1) - I0; own[k] = (int)(rr[k] & 1) == par && pos >= 8 && pos < 112; }
            if (__builtin_expect(own[0] || own[1], 0)) {
                __builtin_amdgcn_s_setprio(RP1);
#pragma unroll
                for (int k = 0; k < 2; ++k) {
                    const int pos = own[k] ? (int)(rr[k] >> 1) - I0 : 8, m = (pos & ~15) + 4 * (pos & 3) + ((pos >> 2) & 3);
                    xr_[k] = lo2f(x[rr[k] * 640 + cg * 64 + lane]);
                    sg[k] = rbf(__builtin_amdgcn_rcpf(1.f + __builtin_amdgcn_exp2f(keyscore(k ? t2.y : t2.x) * -1.4426950408889634f))); NOPK(sg[k]);
                    xn[k] = rbf(xr_[k] + ldsrv<float>(lds0 + L_NM + m * 4)); NOPK(xn[k]); xn[k] = rbf(xn[k] * ldsrv<float>(lds0 + L_RS + pos * 4)); OPQ(xn[k]);
                }
                __builtin_amdgcn_s_sleep(YSL);
                u32x4 y[5];
                SPIN {
                    MEMB();
                    bool old = false;
#pragma unroll
                    for (int i = 0; i < 5; ++i) y[i] = __builtin_amdgcn_raw_buffer_load_b128(rs, W_Y + (cg * 64 + lane) * 16, i * 10240, SC1);
#pragma unroll
                    for (int i = 0; i < 5; ++i) old = old || y[i].y != tag || y[i].w != tag;
                    if (__builtin_amdgcn_ballot_w64(old) == 0) break;
                }
#pragma unroll
                for (int k = 0; k < 2; ++k) if (own[k]) {
                    float ya = __uint_as_float(y[0][2 * k]) + __uint_as_float(y[1][2 * k]); NOPK(ya);
                    const float ys = (ya + (__uint_as_float(y[2][2 * k]) + __uint_as_float(y[3][2 * k]))) + __uint_as_float(y[4][2 * k]);
                    float gt = silu_b(rbf(xn[k] + rbf(rbf(ys) * sg[k]))) * -1.4426950408889634f, sv; OPQ(gt);
                    SPIN {
                        const unsigned fl = ldsrv<unsigned>(lds0 + L_RG + k * 288 + 256); sv = ldsrv<float>(lds0 + L_RG + k * 288 + lane * 4);
                        if (RFL(fl)) break;
                        __builtin_amdgcn_s_sleep(1);
                    }
                    const float o = fmaf(sv, gt, xr_[k]);
                    asm volatile("global_store_short %0, %1, %2 sc1" :: "v"((rr[k] * 640 + cg * 64 + lane) * 2), "v"(cvtpk(o, o)), "s"(ka[8]) : "memory");
                }
            }
            END();
        }
    {
        TL(0);
        LANE();
        const int fq = (j - 25) * 2 + (xcd & 1), f = fp * 14 + fq;
        const int n0 = fq * 40 + wv * 4 + g;
        epld();
        if (wv == 7 && lane < 16) ldsw<u32x4>(lds0 + MISC + lane * 16, (u32x4){0u, 0u, 0u, 0u});
        const int u2 = max(f - 6, 0), kr = (u2 * 26) >> 8, fc = u2 - kr * 10, n1 = wv < 4 ? 3 : 2;
        const int d0 = kr * 11 + wv, d1 = kr * 11 + 4 + wv; const bool two = wv >= 4 && wv < 7 && d1 < 54;
        const char* w1 = (const char*)(f & 1 ? wu : wg) + min(f >> 1, 26) * 128; const char* w2 = (const char*)wd + fc * 128;
        unsigned fv[2][4];
#pragma unroll
        for (int i = 0; i < 4; ++i) { const unsigned c = (((lane & 7) ^ (2 * ((lane >> 4) & 1) + 4 * (i & 1))) * 16) - i * 1024; fv[0][i] = ((lane >> 3) + i * 8) * (F_ * 2) + c; fv[1][i] = ((lane >> 3) + i * 8) * 1280 + c; }
        auto issue = [&](int m, int ks, int slot) {
            asm volatile("s_mov_b32 m0, %[l]\n\ts_nop 0\n\tglobal_load_lds_dwordx4 %0, %[b]" FPOL "\n\tglobal_load_lds_dwordx4 %1, %[b] offset:1024" FPOL "\n\tglobal_load_lds_dwordx4 %2, %[b] offset:2048" FPOL "\n\tglobal_load_lds_dwordx4 %3, %[b] offset:3072" FPOL
                         :: "v"(fv[m][0]), "v"(fv[m][1]), "v"(fv[m][2]), "v"(fv[m][3]), [b] "s"((m ? w2 : w1) + (unsigned)(ks * 32 * (m ? 1280 : F_ * 2))), [l] "s"(lds0 + wv * 16384 + slot * 4096) : "memory");
        };
        if (TCH && wv == 4) {
            const int l = min(f * 14 + lane, 770), o = l < 280 ? W_KEY + l * 128 : l < 536 ? W_ST + (l - 280) * 128 : W_Y + (l - 536) * 256;
            if (lane < 14) asm volatile("s_mov_b32 m0, %[l]\n\ts_nop 0\n\tbuffer_load_dword %0, %1, 0 offen lds" :: "v"(o), "s"(rs), [l] "s"(lds0 + L_DUM) : "memory");
        }
        if (QPF && wv >= 8 - QPF) {
            const int w_ = 7 - wv, i = min((j - 25) * 172 + (w_ >= 3 ? w_ - 3 : w_) * 64 + lane, 1199), n = (i * 4370) >> 16, e = i - n * 15, m = (e * 52) >> 8, rw = 4 * n + (w_ >= 3 ? fp ^ 2 : fp);
            asm volatile("s_mov_b32 m0, %[l]\n\ts_nop 0\n\tglobal_load_lds_dword %0, %[b]" :: "v"(rw * 3840 + ((rw >> 6) + 1) * 245760 + e * 128 + m * 640 + (xcd & 1) * 640), [b] "s"(ka[1]), [l] "s"(lds0 + L_DUM) : "memory");
        }
        BARN();
        auto wopf = [&]() {
            const int i = min(((j - 25) * 8 + wv) * 58 + lane, 3199), rw = (i * 13108) >> 16;
            asm volatile("s_mov_b32 m0, %[l]\n\ts_nop 0\n\tglobal_load_lds_dword %0, %[b]" :: "v"(rw * 640 + i * 128 + (xcd & 1) * 640), [b] "s"(wo), [l] "s"(lds0 + L_DUM) : "memory"); };
        if (WPF == 1) wopf();
        if (EARLYK) issue(0, wv, 0);
        asm volatile("s_waitcnt vmcnt(%2)" : "+{v[84:115]}"(sb), "+{v[116:123]}"(sc) : "n"(5 + 4 * EARLYK + (WPF == 1)) : "memory");
#define SXR(i) ((i) < 20 ? sa[(12 + (i)) & 31] : (i) < 52 ? sb[((i) - 20) & 31] : sc[((i) - 52) & 7])
        mktag();
        {
            const unsigned one2 = 0x3F803F80u;
            float wsum = 0.f;
#pragma unroll
            for (int e = 0; e < 20; ++e) wsum = dot2(SXR(40 + e), one2, wsum);
            wsum = sum16(wsum);
            auto stat = [&](int p) {
                float s1 = 0.f, s2 = 0.f, sr = 0.f;
#pragma unroll
                for (int e = 0; e < 20; ++e) { const unsigned u = SXR(20 * p + e); s1 = dot2(u, one2, s1); s2 = dot2(u, u, s2); sr = dot2(u, SXR(40 + e), sr); }
                const float mean = sum16(s1) * (1.f / 640.f);
                const unsigned mb2 = cvtpk(mean, mean); const float mb = lo2f(mb2);
                const float q = fmaf(40.f * mb, mb, fmaf(-2.f * mb, s1, s2));
                float dm = mean - mb; NOPK(dm);
                dm *= dm; NOPK(dm);
                const float var = fmaxf(sum16(q) * (1.f / 640.f) - dm, 0.f);
                const float sd = rbf(__builtin_amdgcn_sqrtf(rbf(rbf(var) + 1e-5f)));
                const float rsc = rbf(fmaf(-mb, wsum, sum16(sr)) / sd);
                const int n = n0 + 32 * p, i = (fp & 1) * 512 + n;
                if (li == 0) {
                    asm volatile("buffer_store_dwordx4 %0, %1, %2, 0 offen sc1\n\ts_nop 1" :: "v"((u32x4){(mb2 << 16) ^ 0x80000000u, tag, __float_as_uint(__builtin_amdgcn_rcpf(sd)), tag}), "v"(n < 512 ? W_ST + (par * 1024 + i) * 16 : 0x40000000), "s"(rs) : "memory");
                    ldsw<unsigned>(lds0 + LS_ROW + (p * 32 + wv * 4 + g) * 4, n < 512 ? rkey(rsc, 2 * i + par) : 0u);
                }
            };
            asm volatile("" : "+v"(wsum));
            if (wv < 2) stat(1);
            asm volatile("s_bitcmp1_b32 " QW ", %2\n\ts_cbranch_scc1 1f\n\ts_waitcnt vmcnt(%1)\n1:\n\ts_waitcnt vmcnt(%1 + 1)" : "+{v[52:83]}"(sa) : "n"(4 * EARLYK + (WPF == 1)), "s"(wv) : "memory", "scc");
            TL(1);
            if (EK2 && wv < EK2) issue(0, wv, 0);
            stat(0);
            if (E2 && wv < 7) issue(0, wv + 8, 1);
        }
        BAR();
        if (wv == 6) asm volatile("buffer_store_dword %0, %1, %2, 0 offen sc1" :: "v"(tag), "v"(lane ? 0x40000000 : W_ACK + (f < 40 ? (f >> 1) * 128 + 40 + (f & 1) * 4 : 2560 + (f - 40) * 4)), "s"(rs) : "memory");
        if (wv == 7) {
            const unsigned k = ldsrv<unsigned>(lds0 + LS_ROW + min(lane, 39) * 4);
            const uint2 t2 = top2wave(lane < 40 ? k : 0u, 0u);
            if (lane == 0 && fq < 13) asm volatile("buffer_store_dwordx4 %0, %1, %2, 0 offen sc1\n\ts_nop 1" :: "v"((u32x4){t2.x, tag, t2.y, tag}), "v"(W_KEY + (fp * 13 + fq) * 16), "s"(rs) : "memory");
        }
        BARN();
        TL(2);
        if (Q2 && wv < 3) {
            const int i = min((j - 25) * 172 + wv * 64 + lane, 1199), n = (i * 4370) >> 16, e = i - n * 15, m = (e * 52) >> 8, rw = 4 * n + (fp ^ 2);
            asm volatile("s_mov_b32 m0, %[l]\n\ts_nop 0\n\tglobal_load_lds_dword %0, %[b]" :: "v"(rw * 3840 + ((rw >> 6) + 1) * 245760 + e * 128 + m * 640 + (xcd & 1) * 640), [b] "s"(ka[1]), [l] "s"(lds0 + L_DUM) : "memory");
        }
        if (WPF == 2) wopf();
        if (!EARLYK && wv >= EK2) issue(0, wv, 0);
        if (!E2 || wv == 7) issue(0, wv + 8, 1);
        auto wh = [&]() { const int i = min((j - 25) * 116 + (wv - 4) * 29 + lane, 799), n = (i * 13108) >> 16; return (4 * n + fp) * 1280 + (i - 5 * n) * 128 + (xcd & 1) * 640; };
        auto w3 = [&]() {
        if (wv < 4) issue(0, wv + 16, 2);
        else if (!WPF && (!KE || wv < 7)) asm volatile("s_mov_b32 m0, %[l]\n\ts_mov_b32 exec_hi, " PFH "\n\tglobal_load_lds_dword %0, %[b]\n\ts_mov_b32 exec_hi, -1" :: "v"(WH ? wh() : min(f * 128 + (wv - 4) * (KE ? 43 : 32) + lane, 6399) * 128), [b] "s"(wo), [l] "s"(lds0 + L_DUM) : "memory");
        };
        if (!KE) w3();
        if (wv >= 4 && wv < 4 + P2A) issue(1, d0, 3);
        u32x4 kk;
        BARN();
        asm volatile("s_cmp_lg_u32 %3, 7\n\ts_cbranch_scc1 1f\n\ts_sleep " KSL "\n\tbuffer_load_dwordx4 %0, %1, %2, 0 offen sc1\n1:\n\ts_barrier" : "=&v"(kk) : "v"(W_KEY + min(lane, 51) * 16), "s"(rs), "s"(wv) : "memory", "scc");
        if (KE) w3();
        if (!KE && wv != 7) issue(1, d0, 3);
        if (P2A && two) issue(1, d1, 2);
        asm volatile("s_cmp_lg_u32 %1, 7\n\ts_cbranch_scc1 1f\n\ts_waitcnt vmcnt(0)\n1:" : "+v"(kk) : "s"(wv) : "memory", "scc");
        if (wv == 7) {
            SPIN {
                if (__builtin_amdgcn_ballot_w64(kk.y != tag || kk.w != tag) == 0) break;
                asm volatile("buffer_load_dwordx4 %0, %1, %2, 0 offen sc1\n\ts_waitcnt vmcnt(0)" : "=&v"(kk) : "v"(W_KEY + min(lane, 51) * 16), "s"(rs) : "memory");
            }
            const uint2 t2 = top2wave(lane < 52 ? kk.x : 0u, lane < 52 ? kk.z : 0u);
            if (lane < 2) ldsw<unsigned>(lds0 + ROUT + lane * 8, (0xffffu - ((lane ? t2.y : t2.x) & 0xffffu)) & 2047u);
        }
        BAR();
        TL(3);
        const unsigned row = ldsrv<unsigned>(lds0 + ROUT + (li & 1) * 8);
        u32x4 af[3], st;
        asm volatile("global_load_dwordx4 %0, %4, %5\n\tglobal_load_dwordx4 %1, %4, %5 offset:512\n\tglobal_load_dwordx4 %2, %6, %5 offset:1024\n\tbuffer_load_dwordx4 %3, %7, %8, 0 offen sc1"
                     : "=&v"(af[0]), "=&v"(af[1]), "=&v"(af[2]), "=&v"(st) : "v"(row * 1280u + wv * 64 + g * 16), "s"(x), "v"(row * 1280u + (wv & 3) * 64 + g * 16), "v"(wv ? 0x40000000 : W_ST + ((row & 1) * 1024 + (row >> 1)) * 16), "s"(rs) : "memory");
        bf16x8 wf[3][4];
        unsigned ra[4];
#pragma unroll
        for (int q = 0; q < 4; ++q) ra[q] = lds0 + wv * 16384 + (unsigned)((8 * g + (li >> 2)) * 128 + (((q ^ (((li >> 3) & 1) + 2 * (g & 1))) * 32) + (li & 3) * 8));
        auto rdf = [&](int k, int slot) {
#pragma unroll
            for (int q = 0; q < 4; ++q) wf[k][q] = cat8(trread(ra[q] + slot * 4096), trread(ra[q] + slot * 4096 + 512));
        };
        if (KE) { BARN(); if (wv) issue(1, d0, 3); if (wv) wvm<8>(); else wvm<4>(); }
        else {
        if (wv == 7) issue(1, d0, 3);
        if (two) issue(1, d1, 2);
        if (two) wvm<12>(); else wvm<8>(); }
        rdf(0, 0); rdf(1, 1); if (wv < 4) rdf(2, 2);
        asm volatile("s_cmp_eq_u32 %4, 0\n\ts_cbranch_scc0 1f\n\ts_waitcnt vmcnt(0)\n1:\n\ts_waitcnt vmcnt(4)" : "+v"(af[0]), "+v"(af[1]), "+v"(af[2]), "+v"(st) : "s"(KE ? wv : RFL(two || wv == 7)) : "memory", "scc");
        f32x4 c[4];
#pragma unroll
        for (int q = 0; q < 4; ++q) c[q] = (f32x4){0.f, 0.f, 0.f, 0.f};
#pragma unroll
        for (int k = 0; k < 3; ++k) if (k < n1) {
#pragma unroll
            for (int e = 0; e < 4; ++e) af[k][e] = li & 2 ? 0x3F803F80u : af[k][e];
#pragma unroll
            for (int q = 0; q < 4; ++q) c[q] = mfma32(__builtin_bit_cast(bf16x8, af[k]), wf[k][q], c[q]);
        }
        auto total = [&](int off) {
            f32x4 m;
#pragma unroll
            for (int e = 0; e < 4; ++e) { float a_ = g & 1 ? c[1][e] : c[0][e], b_ = g & 1 ? c[3][e] : c[2][e]; NOPK(a_); NOPK(b_); m[e] = g & 2 ? b_ : a_; }
            ldsw<f32x4>(lds0 + wv * 16384 + off + lane * 16, m);
            BAR();
            if (wv) return m;
            f32x4 pr[8];
#pragma unroll
            for (int w = 0; w < 8; ++w) pr[w] = ldsrv<f32x4>(lds0 + w * 16384 + off + lane * 16);
#pragma unroll
            for (int e = 0; e < 3; ++e) { float t[4];
#pragma unroll
                for (int w = 0; w < 4; ++w) { t[w] = pr[2 * w][e] + pr[2 * w + 1][e]; NOPK(t[w]); }
                float u = t[0] + t[1], v = t[2] + t[3]; NOPK(u); NOPK(v); u += v; NOPK(u); m[e] = u; }
            return m;
        };
        TL(4);
        {
            const f32x4 s = total(0);
            if (wv == 0 && f < 54) {
                SPIN {
                    if (__builtin_amdgcn_ballot_w64(st.y != tag || st.w != tag) == 0) break;
                    asm volatile("buffer_load_dwordx4 %0, %1, %2, 0 offen sc1\n\ts_waitcnt vmcnt(0)" : "=&v"(st) : "v"(W_ST + ((row & 1) * 1024 + (row >> 1)) * 16), "s"(rs) : "memory");
                }
                const float n0_ = __uint_as_float(__builtin_amdgcn_readlane(st.x, 0)), n1_ = __uint_as_float(__builtin_amdgcn_readlane(st.x, 1)), i0 = __uint_as_float(__builtin_amdgcn_readlane(st.z, 0)), i1 = __uint_as_float(__builtin_amdgcn_readlane(st.z, 1));
                float v0 = rbf(fmaf(n0_, s.z, s.x) * i0), v1 = rbf(fmaf(n1_, s.z, s.y) * i1);
                if (!(f & 1)) { v0 = silu_b(v0); NOPK(v0); v1 = silu_b(v1); }
                const unsigned hb = cvtpk(v0, v1), nb = dppu<0xB1>(hb);
                const unsigned pay = (lane & 1) ? __builtin_amdgcn_perm(hb, nb, 0x07060302u) : __builtin_amdgcn_perm(nb, hb, 0x05040100u);
                asm volatile("buffer_store_dwordx2 %0, %1, %2, 0 offen sc1" :: "v"((u32x2){pay, tag}), "v"(W_H + ((f & 1) * 2 + (lane & 1)) * 6912 + ((f >> 1) * 32 + (lane >> 1)) * 8), "s"(rs) : "memory");
            }
        }
        TL(5);
        if (KE) { if (two) issue(1, d1, 2); if (wv == 0) issue(1, d0, 3); }
        if (f == 0 && wv == 1) {
            SPIN {
                u32x4 a_;
                asm volatile("buffer_load_dwordx4 %0, %1, %2, 0 offen sc1\n\ts_waitcnt vmcnt(0)" : "=&v"(a_) : "v"(W_ACK + (lane < 60 ? ((lane * 43) >> 7) * 80 + lane * 16 : 1600 + lane * 16)), "s"(rs) : "memory");
                if (__builtin_amdgcn_ballot_w64(a_.x != tag || a_.y != tag || a_.z != tag || a_.w != tag) == 0) break;
                __builtin_amdgcn_s_sleep(8);
            }
            if (lane == 0) __builtin_amdgcn_raw_buffer_store_b32(tag, rs, W_EP, 0, SC1);
        }
        if (f >= 6) {
            wvm<0>();
            rdf(0, 3); rdf(1, 2);
            const int ho = W_H + ((lane >> 4) & 1) * 6912 + ((lane >> 5 && two ? d1 : d0) * 16 + li) * 8;
            u32x2 hg, hu;
            __builtin_amdgcn_s_sleep(HSL);
            SPIN {
                asm volatile("buffer_load_dwordx2 %0, %2, %3, 0 offen sc1\n\tbuffer_load_dwordx2 %1, %2, %3, %4 offen sc1\n\ts_waitcnt vmcnt(0)" : "=&v"(hg), "=&v"(hu) : "v"(ho), "s"(rs), "s"(13824) : "memory");
                if (__builtin_amdgcn_ballot_w64(hg.y != tag || hu.y != tag) == 0) break;
                __builtin_amdgcn_s_sleep(HS2);
            }
            float h0 = lo2f(hg.x) * lo2f(hu.x), h1 = hi2f(hg.x); NOPK(h0); NOPK(h1); h1 *= hi2f(hu.x); NOPK(h1);
            ldsw<unsigned>(lds0 + wv * 16384 + 4096 + lane * 4, cvtpk(h0, h1));
#pragma unroll
            for (int q = 0; q < 4; ++q) c[q] = (f32x4){0.f, 0.f, 0.f, 0.f};
#pragma unroll
            for (int k = 0; k < 2; ++k) if (k == 0 || two) {
                const u32x4 h = ldsrv<u32x4>(lds0 + wv * 16384 + 4096 + k * 128 + (li & 1) * 64 + g * 16);
#pragma unroll
                for (int q = 0; q < 4; ++q) c[q] = mfma32(__builtin_bit_cast(bf16x8, h), wf[k][q], c[q]);
            }
            const f32x4 ys = total(1024);
            if (wv == 0) __builtin_amdgcn_raw_buffer_store_b128((u32x4){__float_as_uint(ys.x), tag, __float_as_uint(ys.y), tag}, rs, W_Y + kr * 10240 + (fc * 64 + lane) * 16, 0, SC1);
        }
        TL(6);
        TLEND();
        wvm<0>();
    }
}

extern "C" void kernel_launch(void* const* d_in, const int* in_sizes, int n_in, void* d_out, int out_size, void* d_ws,
                              size_t ws_size, hipStream_t stream) {
    (void)hipFuncSetAttribute((const void*)fused, hipFuncAttributeMaxDynamicSharedMemorySize, LDSZ);
    fused<<<256, 512, LDSZ, stream>>>((const u16*)d_in[0], (const u16*)d_in[1], (const u16*)d_in[2], (const u16*)d_in[4],
                                      (const u16*)d_in[5], (const u16*)d_in[6], (const u16*)d_in[7], (char*)d_ws, (u16*)d_out);
}
```

```cpp
#include <hip/hip_runtime.h>

typedef __attribute__((ext_vector_type(8))) short bf16x8;
typedef __attribute__((ext_vector_type(4))) short bf16x4;
typedef __attribute__((ext_vector_type(4))) float f32x4;
typedef __attribute__((ext_vector_type(2))) float f32x2;
typedef __attribute__((ext_vector_type(4))) unsigned u32x4;
typedef __attribute__((ext_vector_type(2))) unsigned u32x2;
typedef __attribute__((ext_vector_type(2))) __bf16 bf16x2v;
typedef __attribute__((ext_vector_type(8))) unsigned u32x8;
typedef unsigned short u16;
typedef __attribute__((address_space(3))) char lchar;


#define F_ 1728
constexpr int W_EP = 0;
constexpr int W_KEY = 4096;
constexpr int W_ACK = 8192;
constexpr int W_H = 12288;
constexpr int W_ST = 65536;
constexpr int W_Y = 98304;
constexpr int W_O = 155648;
constexpr int W_END = W_O + 80 * 40960;
constexpr int XS = 14336, SLOT = XS + 24576;
constexpr int WO0 = 2 * SLOT;
constexpr int MISC = WO0 + 81920;
constexpr int FL_W = MISC + 48, FL_R = MISC + 64, ROUT = MISC + 144;
constexpr int L_NM = MISC + 256, L_RS = L_NM + 512 + 64, L_RM = L_RS + 512, L_RG = L_RM + 512, L_DUM = L_RG + 512, L_KEY = L_DUM + 512;
constexpr int LS_ROW = L_NM;
constexpr int LDSZ = 163840;
static_assert(L_KEY + 1024 <= LDSZ, "lds");
#define SPIN_MAX (1 << 21)
#define SPIN _Pragma("nounroll") for (int it = 0; it < SPIN_MAX; ++it)

__device__ __forceinline__ unsigned cvtpk(float lo, float hi) { return __builtin_bit_cast(unsigned, __builtin_convertvector((f32x2){lo, hi}, bf16x2v)); }
__device__ __forceinline__ float lo2f(unsigned u) { return __uint_as_float(u << 16); }
__device__ __forceinline__ float hi2f(unsigned u) { return __uint_as_float(u & 0xffff0000u); }
__device__ __forceinline__ float rbf(float f) { return lo2f(cvtpk(f, f)); }
template <int CTRL> __device__ __forceinline__ float dppf(float x) { return __builtin_bit_cast(float, __builtin_amdgcn_mov_dpp(__builtin_bit_cast(int, x), CTRL, 0xf, 0xf, true)); }
template <int CTRL> __device__ __forceinline__ unsigned dppu(unsigned x) { return (unsigned)__builtin_amdgcn_mov_dpp((int)x, CTRL, 0xf, 0xf, true); }
__device__ __forceinline__ float sum16(float x) { x += dppf<0xB1>(x); x += dppf<0x4E>(x); x += dppf<0x141>(x); x += dppf<0x140>(x); return x; }
__device__ __forceinline__ float dot2(unsigned a, unsigned b, float c) { return __builtin_amdgcn_fdot2_f32_bf16(__builtin_bit_cast(bf16x2v, a), __builtin_bit_cast(bf16x2v, b), c, false); }
__device__ __forceinline__ float silu_b(float v) { return v * __builtin_amdgcn_rcpf(__builtin_amdgcn_exp2f(v * -1.4426950408889634f) + 1.f); }
__device__ __forceinline__ unsigned rkey(float rb, int row) {
    unsigned b = __float_as_uint(rb) >> 16; b = (b & 0x8000u) ? (~b & 0xffffu) : (b | 0x8000u);
    return (b << 16) | (unsigned)(0xffff - row);
}
__device__ __forceinline__ float keyscore(unsigned k) { const unsigned b = k >> 16; return __uint_as_float(((b & 0x8000u) ? (b & 0x7fffu) : (~b & 0xffffu)) << 16); }
#define T2M(b0, b1) { const unsigned _b0 = (b0), _b1 = (b1); const unsigned _m = min(k0, _b0); k0 = max(k0, _b0); k1 = max(_m, max(k1, _b1)); }
__device__ __forceinline__ uint2 top2wave(unsigned k0, unsigned k1) {
    T2M(dppu<0xB1>(k0), dppu<0xB1>(k1)); T2M(dppu<0x4E>(k0), dppu<0x4E>(k1));
    T2M(dppu<0x141>(k0), dppu<0x141>(k1)); T2M(dppu<0x140>(k0), dppu<0x140>(k1));
    const unsigned a0 = __builtin_amdgcn_readlane(k0, 0), a1 = __builtin_amdgcn_readlane(k1, 0);
    const unsigned c0 = __builtin_amdgcn_readlane(k0, 16), c1 = __builtin_amdgcn_readlane(k1, 16);
    const unsigned d0 = __builtin_amdgcn_readlane(k0, 32), d1 = __builtin_amdgcn_readlane(k1, 32);
    const unsigned e0 = __builtin_amdgcn_readlane(k0, 48), e1 = __builtin_amdgcn_readlane(k1, 48);
    k0 = a0; k1 = a1; T2M(c0, c1); T2M(d0, d1); T2M(e0, e1);
    return make_uint2(k0, k1);
}
template <int N> __device__ __forceinline__ void wvm() { asm volatile("" ::: "memory"); __builtin_amdgcn_s_waitcnt((N & 15) | ((N >> 4) << 14) | 0x0F70); asm volatile("" ::: "memory"); }
__device__ __forceinline__ bf16x4 trread(unsigned a) { return __builtin_amdgcn_ds_read_tr16_b64_v4i16((__attribute__((address_space(3))) bf16x4*)(size_t)a); }
template <class T> __device__ __forceinline__ T ldsrv(unsigned a) { return *(volatile __attribute__((address_space(3))) T*)(size_t)a; }
template <class T> __device__ __forceinline__ void ldsw(unsigned a, T v) { *(volatile __attribute__((address_space(3))) T*)(size_t)a = v; }
__device__ __forceinline__ bf16x8 cat8(bf16x4 lo, bf16x4 hi) { return (bf16x8){lo[0], lo[1], lo[2], lo[3], hi[0], hi[1], hi[2], hi[3]}; }
__device__ __forceinline__ f32x4 mfma32(bf16x8 a, bf16x8 b, f32x4 c) { return __builtin_amdgcn_mfma_f32_16x16x32_bf16(a, b, c, 0, 0, 0); }
__device__ __forceinline__ int lane_id() { int l; asm volatile("v_mbcnt_lo_u32_b32 %0, -1, 0\n\tv_mbcnt_hi_u32_b32 %0, -1, %0" : "=v"(l)); __builtin_assume(l >= 0 && l < 64); return l; }
#define BAR() asm volatile("s_waitcnt lgkmcnt(0)\n\ts_barrier" ::: "memory")
#define MEMB() asm volatile("" ::: "memory")
#define RFL(x) ((unsigned)__builtin_amdgcn_readfirstlane((int)(x)))
#define SC1 16

#define NOPK(v) asm("" : "+v"(v))
#define OPQ(v) asm volatile("" : "+v"(v))
#define END() do { asm volatile("s_endpgm"); __builtin_amdgcn_s_waitcnt(0); return; } while (0)
#define BARN() asm volatile("s_barrier" ::: "memory")

__global__ __launch_bounds__(512, 2) void fused(const u16* x_, const u16* wqkv_, const u16* wo_, const u16* w_r_, const u16* wg_, const u16* wu_, const u16* wd_, char* ws_, u16* out_) {
    extern __shared__ __attribute__((aligned(16))) char smem[];
    typedef __attribute__((ext_vector_type(32))) unsigned u32x32;
    u32x32 sa, sb; u32x8 sc;
    unsigned tag, bid = blockIdx.x; int tid = threadIdx.x;
    typedef __attribute__((address_space(1))) char gchar;
    typedef gchar* const __attribute__((address_space(4)))* karg_t;
    karg_t ka = (karg_t)__builtin_amdgcn_kernarg_segment_ptr();
    asm volatile(
            "s_load_dwordx2 s[40:41], %[k], 0x8\n\ts_load_dwordx2 s[36:37], %[k], 0x0\n\tv_readfirstlane_b32 s69, %[t]\n\ts_lshr_b32 s70, %[b], 3\n\ts_cmpk_lg_u32 s69, 0xc0\n\ts_cbranch_scc1 1f\n\t"
            "s_getpc_b64 s[72:73]\n\t.Lch:\ns_lshl3_add_u32 s71, s70, 0xffffff40\n\tv_add_lshl_u32 v36, s71, %[t], 7\n\tv_min_u32 v36, .Lce-.Lch-4, v36\n\ts_mov_b32 m0, 162368\n\t"
            "s_mov_b64 exec, 0xff\n\tglobal_load_lds_dword v36, s[72:73]\n\ts_mov_b64 exec, -1\n\t1:\ns_add_i32 s70, s70, 25\n\ts_and_b32 s70, s70, 31\n\t"
            "s_cmp_lt_u32 s70, 25\n\ts_cbranch_scc0 .LF\n\tv_bfe_u32 v36, %[t], 3, 3\n\tv_and_b32 v37, 7, %[t]\n\ts_mul_i32 s71, s70, 13\n\ts_lshr_b32 s71, s71, 6\n\t"
            "v_and_b32 v38, 2, v36\n\tv_xor_b32 v46, v38, v37\n\ts_lshl_b32 s64, s71, 2\n\ts_sub_i32 s64, s70, s64\n\ts_sub_i32 s57, s64, s71\n\tv_mul_u32_u24 v40, 0xf00, v36\n\t"
            "s_sub_i32 s72, s64, 5\n\ts_min_u32 s64, s64, s72\n\tv_lshl_add_u32 v40, v46, 4, v40\n\ts_bfe_u32 s56, s69, 0x20006\n\ts_and_b32 s72, %[b], 1\n\ts_mul_i32 s72, s72, 5\n\t"
            "s_add_i32 s58, s57, s72\n\ts_lshl_b32 s58, s58, 7\n\ts_mul_i32 s61, s64, 491520\n\ts_mul_i32 s73, s56, 0xf000\n\ts_add_i32 s73, s73, s58\n\ts_add_i32 s61, s61, s73\n\t"
            "s_mul_i32 s66, s56, 0x1800\n\ts_add_i32 m0, s66, 53248\n\ts_mov_b32 s42, 0x258000\n\ts_mov_b32 s43, 0x20000\n\tv_add_u32 v41, 0x100, v40\n\tv_add_u32 v42, 0x200, v40\n\t"
            "v_xor_b32 v43, 64, v40\n\tv_add_u32 v43, 0x6c00, v43\n\tv_add_u32 v44, 0x1100, v43\n\tv_add_u32 v45, 0x1200, v43\n\ts_cmpk_lt_u32 s69, 0x100\n\ts_cbranch_scc1 1f\n\t"
            "s_waitcnt lgkmcnt(0)\n\tbuffer_load_dwordx4 v40, s[40:43], s61 offen lds\n\tbuffer_load_dwordx4 v41, s[40:43], s61 offen offset:1024 lds\n\tbuffer_load_dwordx4 v42, s[40:43], s61 offen offset:2048 lds\n\tbuffer_load_dwordx4 v43, s[40:43], s61 offen offset:3072 lds\n\ts_addk_i32 m0, 0x1000\n\t"
            "s_nop 0\n\tbuffer_load_dwordx4 v44, s[40:43], s61 offen lds\n\tbuffer_load_dwordx4 v45, s[40:43], s61 offen offset:1024 lds\n\t1:\ns_lshl_b32 s72, s56, 5\n\ts_min_u32 s72, s72, 88\n\t"
            "v_add_u32 v39, s72, v36\n\ts_bfe_u32 s62, %[b], 0x10001\n\ts_mul_i32 s62, s62, 5\n\ts_add_i32 s62, s62, s71\n\ts_mul_i32 s62, s62, 104\n\ts_sub_i32 s62, s62, 8\n\t"
            "v_and_b32 v47, 3, v39\n\tv_lshl_add_u32 v47, v47, 2, s62\n\tv_and_b32 v48, 0x70, v39\n\tv_bfe_u32 v49, v39, 2, 2\n\tv_add3_u32 v48, v48, v49, v47\n\tv_bfe_u32 v49, v39, 1, 3\n\t"
            "v_xor_b32 v50, v49, v37\n\tv_mul_i32_i24 v48, 0xa00, v48\n\ts_bfe_u32 s63, %[b], 0x10002\n\ts_mul_i32 s63, s63, 0x500\n\ts_lshl_b32 s60, s64, 8\n\tv_mov_b32 v51, v36\n\t"
            "v_lshl_add_u32 v36, v50, 4, v48\n\ts_add_i32 s60, s60, s63\n\ts_lshl_b32 s65, s72, 7\n\ts_add_i32 m0, s65, 38912\n\ts_mov_b32 s38, 0x280000\n\ts_mov_b32 s39, 0x20000\n\t"
            "v_add_u32 v38, 0x9800, v36\n\ts_cmpk_lt_u32 s69, 0x100\n\ts_cbranch_scc1 1f\n\tbuffer_load_dwordx4 v36, s[36:39], s60 offen lds\n\tbuffer_load_dwordx4 v38, s[36:39], s60 offen offset:2048 lds\n\ts_barrier\n\t"
            "1:\ns_load_dwordx2 s[44:45], %[k], 0x38\n\ts_load_dwordx2 s[48:49], %[k], 0x10\n\ts_load_dwordx2 s[52:53], %[k], 0x40\n\tv_add_u32 v39, 8, v39\n\tv_and_b32 v48, 0x70, v39\n\t"
            "v_bfe_u32 v49, v39, 2, 2\n\tv_add3_u32 v48, v48, v49, v47\n\tv_xor_b32 v50, 4, v50\n\tv_mul_i32_i24 v48, 0xa00, v48\n\tv_lshl_add_u32 v37, v50, 4, v48\n\tv_subrev_u32 v37, 0x400, v37\n\t"
            "v_add_u32 v39, 0x9800, v37\n\ts_addk_i32 s66, 14336\n\ts_bfe_u32 s59, %[b], 0x20001\n\ts_mul_i32 s59, s59, 5\n\ts_add_i32 s59, s59, s71\n\ts_mov_b32 s46, 3432448\n\t"
            "s_mov_b32 s47, 0x20000\n\ts_mov_b32 s54, 0x280000\n\ts_mov_b32 s55, 0x20000\n\tv_and_b32 v52, 63, %[t]\n\tv_bfe_u32 v53, %[t], 4, 2\n\tv_and_b32 v54, 15, %[t]\n\t"
            "v_and_b32 v55, 3, %[t]\n\tv_bfe_u32 v56, %[t], 2, 2\n\tv_lshl_add_u32 v57, v55, 2, v56\n\ts_lshr_b32 s69, s69, 6\n\ts_cmp_lt_u32 s69, 4\n\ts_cbranch_scc0 .LB\n\t"
            "s_branch .LA\n\t.LF:\ns_load_dwordx2 s[72:73], %[k], 0x18\n\ts_and_b32 s71, %[b], 1\n\ts_lshl1_add_u32 s71, s70, s71\n\ts_mul_i32 s71, s71, 40\n\t"
            "s_sub_u32 s71, s71, 2000\n\tv_lshrrev_b32 v2, 4, %[t]\n\tv_add_u32 v2, s71, v2\n\tv_add_u32 v3, 32, v2\n\tv_min_u32 v2, 0x1ff, v2\n\tv_min_u32 v3, 0x1ff, v3\n\t"
            "v_and_b32 v4, 15, %[t]\n\tv_lshlrev_b32 v4, 4, v4\n\ts_bfe_u32 s71, %[b], 0x10001\n\ts_mul_i32 s71, s71, 0x140000\n\ts_bfe_u32 s74, %[b], 0x10002\n\ts_mul_i32 s74, s74, 0x500\n\t"
            "s_add_u32 s71, s71, s74\n\tv_add_u32 v5, s71, v4\n\ts_movk_i32 s71, 0xa00\n\tv_mad_u32_u24 v2, v2, s71, v5\n\tv_mad_u32_u24 v3, v3, s71, v5\n\ts_waitcnt lgkmcnt(0)\n\t"
            "global_load_dwordx4 v[104:107], v4, s[72:73] offset:0\n\tglobal_load_dwordx4 v[108:111], v4, s[72:73] offset:256\n\tglobal_load_dwordx4 v[112:115], v4, s[72:73] offset:512\n\tglobal_load_dwordx4 v[116:119], v4, s[72:73] offset:768\n\tglobal_load_dwordx4 v[120:123], v4, s[72:73] offset:1024\n\ts_cmpk_ge_u32 s69, 0x80\n\t"
            "s_cbranch_scc1 1f\n\tglobal_load_dwordx4 v[84:87], v3, s[36:37] offset:0\n\tglobal_load_dwordx4 v[88:91], v3, s[36:37] offset:256\n\tglobal_load_dwordx4 v[92:95], v3, s[36:37] offset:512\n\tglobal_load_dwordx4 v[96:99], v3, s[36:37] offset:768\n\tglobal_load_dwordx4 v[100:103], v3, s[36:37] offset:1024\n\t"
            "1:\nglobal_load_dwordx4 v[64:67], v2, s[36:37] offset:0\n\tglobal_load_dwordx4 v[68:71], v2, s[36:37] offset:256\n\tglobal_load_dwordx4 v[72:75], v2, s[36:37] offset:512\n\tglobal_load_dwordx4 v[76:79], v2, s[36:37] offset:768\n\tglobal_load_dwordx4 v[80:83], v2, s[36:37] offset:1024\n\t"
            "s_branch .Lout\n\t.LA:\n"
            "v_lshrrev_b32 v2, 1, v54\n\tv_xor_b32 v2, v2, v53\n\tv_lshlrev_b32 v3, 7, v54\n\tv_lshl_add_u32 v2, v2, 4, v3\n\tv_xor_b32 v3, 64, v2\n\tv_add_u32 v4, 38912, v2\n\t"
            "v_add_u32 v5, 38912, v3\n\tv_add_u32 v10, 77824, v2\n\tv_add_u32 v11, 77824, v3\n\tv_mov_b32 v12, 159872\n\tv_cmp_eq_u32 vcc, 0, v54\n\tv_mov_b32 v60, 0\n\t"
            "v_mov_b32 v61, 0\n\tv_mov_b32 v62, 0x3f803f80\n\tv_lshlrev_b32 v63, 3, v52\n\tv_cndmask_b32 v6, v2, v12, vcc\n\tv_cndmask_b32 v7, v3, v12, vcc\n\tv_cndmask_b32 v8, v4, v12, vcc\n\t"
            "v_cndmask_b32 v9, v5, v12, vcc\n\tv_cndmask_b32 v10, v10, v12, vcc\n\tv_cndmask_b32 v11, v11, v12, vcc\n\tv_lshlrev_b32 v64, 2, v52\n\tv_lshrrev_b32 v65, 2, v52\n\tv_cmp_eq_u32 vcc, 8, v65\n\t"
            "v_add_u32 v66, 161088, v63\n\tv_add_u32 v64, 159744, v64\n\tv_cndmask_b32 v62, 0, v62, vcc\n\ts_cmp_lg_u32 s69, 1\n\ts_cbranch_scc1 1f\n\tds_write_b32 v64, v62\n\t"
            "ds_write_b64 v66, v[60:61]\n\tds_write_b64 v66, v[60:61] offset:576\n\t1:\nv_bfe_u32 v12, v52, 3, 2\n\tv_xor_b32 v12, s69, v12\n\tv_lshlrev_b32 v13, 7, v56\n\t"
            "v_lshl_add_u32 v13, v55, 3, v13\n\tv_lshl_add_u32 v12, v12, 5, v13\n\tv_mul_u32_u24 v13, 0xc00, v53\n\ts_movk_i32 s72, 14336\n\tv_add3_u32 v12, v12, v13, s72\n\tv_add_u32 v13, 38912, v12\n\t"
            "s_mov_b32 s72, 160000\n\tv_lshl_add_u32 v14, v54, 2, s72\n\tv_lshl_add_u32 v15, v53, 4, s72\n\ts_cmp_eq_u32 s69, 3\n\ts_cselect_b32 s74, 1, 0\n\ts_cselect_b64 s[76:77], -1, 0\n\t"
            "s_cselect_b32 s75, 4, 0\n\ts_add_i32 s72, s69, 1\n\ts_bfe_u32 s72, s72, 0x10001\n\ts_xor_b32 s72, s72, 1\n\ts_lshr_b32 s73, s69, 1\n\ts_lshl1_add_u32 s78, s72, s73\n\t"
            "s_lshl2_add_u32 s78, s78, 159776\n\tv_mov_b32 v34, s78\n\ts_lshl2_add_u32 s78, s69, 159744\n\tv_mov_b32 v33, s78\n\ts_mul_i32 s78, s59, 163840\n\ts_lshl_b32 s79, s58, 5\n\t"
            "s_add_i32 s78, s78, s79\n\ts_lshl_b32 s79, s69, 10\n\ts_add_i32 s78, s78, s79\n\ts_add_i32 s78, s78, 155648\n\tv_lshl_add_u32 v16, v52, 4, s78\n\tv_and_b32 v215, 1, v53\n\t"
            "v_lshl_add_u32 v215, v215, 30, v16\n\ts_lshl_b32 s79, s74, 11\n\ts_add_i32 s78, s78, s79\n\ts_sub_i32 s78, s78, 0x400\n\ts_cmp_lg_u32 s57, s75\n\ts_cselect_b32 s79, s72, 0\n\t"
            "s_cmp_lg_u32 s79, 0\n\ts_cselect_b32 s78, s78, 0x40000000\n\tv_lshl_add_u32 v17, v52, 4, s78\n\ts_sub_i32 s78, s69, s57\n\ts_add_i32 s79, s78, 5\n\ts_min_u32 s78, s78, s79\n\t"
            "v_cmp_gt_u32 s[80:81], s78, v53\n\tv_cmp_gt_u32 s[82:83], s78, v56\n\ts_mov_b32 s14, 0x33363333\n\ts_mov_b32 s15, 0x36663366\n\ts_mov_b32 s16, 0x666c6666\n\ts_mov_b32 s17, 0x6ccc66cc\n\t"
            "s_mov_b32 s18, 0x33333333\n\ts_mov_b32 s19, 0x33333333\n\ts_xor_b64 s[80:81], s[80:81], s[76:77]\n\ts_xor_b64 s[82:83], s[82:83], s[76:77]\n\ts_andn2_b64 vcc, s[80:81], s[18:19]\n\tv_cndmask_b32 v18, 0, -1, vcc\n\t"
            "s_and_b64 vcc, s[80:81], s[18:19]\n\tv_cndmask_b32 v19, 0, -1, vcc\n\ts_nor_b64 vcc, s[80:81], s[18:19]\n\tv_cndmask_b32 v20, 0, -1, vcc\n\ts_andn2_b64 vcc, s[18:19], s[80:81]\n\tv_cndmask_b32 v21, 0, -1, vcc\n\t"
            "v_cndmask_b32 v22, 0, -1, s[82:83]\n\tv_cndmask_b32 v23, -1, 0, s[82:83]\n\tv_sub_u32 v58, v57, v53\n\tv_add_u32 v58, 4, v58\n\tv_and_b32 v58, 28, v58\n\tv_lshlrev_b32 v58, 1, v58\n\t"
            "v_mov_b32 v24, -1\n\tv_mov_b32 v25, 0\n\tv_lshlrev_b64 v[24:25], v58, v[24:25]\n\tv_lshlrev_b64 v[24:25], v58, v[24:25]\n\tv_sub_u32 v58, 32, v58\n\tv_min_u32 v58, 16, v58\n\t"
            "v_lshrrev_b32 v26, v58, -1\n\tv_lshrrev_b32 v26, v58, v26\n\ts_lshl1_add_u32 s78, s69, s74\n\ts_xor_b32 s79, s78, 1\n\ts_mul_i32 s78, s78, 0xc00\n\ts_mul_i32 s79, s79, 0xc00\n\t"
            "s_addk_i32 s78, 14336\n\ts_addk_i32 s79, 14336\n\tv_add_u32 v28, s78, v63\n\tv_add_u32 v29, s79, v63\n\ts_cmp_eq_u32 s69, s57\n\ts_addc_u32 s78, s69, 0\n\t"
            "s_add_i32 s79, s69, 1\n\ts_cmp_eq_u32 s79, s57\n\ts_subb_u32 s79, s79, 0\n\ts_cmp_eq_u32 s74, 1\n\ts_cselect_b32 s80, s79, s78\n\ts_cselect_b32 s81, s78, s79\n\t"
            "v_lshlrev_b32 v58, 2, v53\n\tv_lshl_add_u32 v58, v54, 4, v58\n\ts_cmp_gt_u32 s80, s57\n\ts_subb_u32 s82, s80, 0\n\ts_lshl2_add_u32 s82, s82, 159760\n\tv_mov_b32 v59, s82\n\t"
            "s_lshl_b32 s82, s80, 1\n\ts_max_u32 s82, s82, 1\n\ts_mul_i32 s82, s82, 0xc00\n\ts_addk_i32 s82, 11264\n\tv_add_u32 v30, s82, v58\n\ts_cmp_gt_u32 s81, s57\n\t"
            "s_subb_u32 s82, s81, 0\n\ts_lshl2_add_u32 s82, s82, 159760\n\tv_mov_b32 v60, s82\n\ts_lshl_b32 s82, s81, 1\n\ts_max_u32 s82, s82, 1\n\ts_mul_i32 s82, s82, 0xc00\n\t"
            "s_addk_i32 s82, 11264\n\tv_add_u32 v31, s82, v58\n\tv_cndmask_b32 v32, v59, v60, s[18:19]\n\tv_mov_b32 v35, 1\n\ts_waitcnt lgkmcnt(0)\n\ts_barrier\n\t"
            "s_load_dword s67, s[44:45], 0x0\n\ts_mov_b32 s20, 0\n\ts_mov_b32 s21, 40960\n\ts_mov_b32 s22, 81920\n\ts_mov_b32 s23, 122880\n\ts_cmp_ge_u32 s56, 2\n\t"
            "s_cselect_b64 vcc, -1, 0\n\ts_add_u32 m0, s65, 38912\n\ts_nop 0\n\tbuffer_load_dwordx4 v37, s[36:39], s60 offen offset:1024 lds\n\ts_cbranch_vccnz 1f\n\tbuffer_load_dwordx4 v39, s[36:39], s60 offen offset:3072 lds\n\t"
            "1:\ns_cmp_eq_u32 s64, 4\n\ts_cselect_b32 s72, -1280, 0\n\ts_add_i32 s72, s72, 256\n\ts_bitcmp1_b32 31, s64\n\ts_cselect_b32 s72, s72, 128\n\t"
            "s_mul_i32 s73, s72, 1920\n\ts_add_u32 s60, s60, s72\n\ts_add_u32 m0, s66, 77824\n\ts_add_u32 s61, s61, s73\n\tbuffer_load_dwordx4 v40, s[40:43], s61 offen lds\n\tbuffer_load_dwordx4 v41, s[40:43], s61 offen offset:1024 lds\n\t"
            "buffer_load_dwordx4 v42, s[40:43], s61 offen offset:2048 lds\n\tbuffer_load_dwordx4 v43, s[40:43], s61 offen offset:3072 lds\n\ts_addk_i32 m0, 0x1000\n\ts_nop 0\n\tbuffer_load_dwordx4 v44, s[40:43], s61 offen lds\n\tbuffer_load_dwordx4 v45, s[40:43], s61 offen offset:1024 lds\n\t"
            "s_add_u32 m0, s65, 77824\n\ts_nop 0\n\tbuffer_load_dwordx4 v36, s[36:39], s60 offen lds\n\tbuffer_load_dwordx4 v37, s[36:39], s60 offen offset:1024 lds\n\tbuffer_load_dwordx4 v38, s[36:39], s60 offen offset:2048 lds\n\ts_cbranch_vccnz 1f\n\t"
            "buffer_load_dwordx4 v39, s[36:39], s60 offen offset:3072 lds\n\t1:\ns_mov_b32 s68, 0x200000\n\ts_waitcnt lgkmcnt(0)\n\ts_mul_i32 s67, s67, 0x19660d\n\ts_add_u32 s67, s67, 0x3c6ef35f\n\t"
            "s_cbranch_vccz 1f\n\ts_waitcnt vmcnt(9)\n\t1:\ns_waitcnt vmcnt(10)\n\ts_barrier\n\t"
            "ds_read_b64_tr_b16 v[176:177], v13 offset:0\n\tds_read_b64_tr_b16 v[178:179], v13 offset:512\n\tds_read_b128 v[120:123], v8\n\tds_read_b128 v[124:127], v4 offset:2048\n\tds_read_b128 v[128:131], v4 offset:4096\n\tds_read_b128 v[132:135], v4 offset:6144\n\t"
            "ds_read_b128 v[136:139], v4 offset:8192\n\tds_read_b128 v[140:143], v4 offset:10240\n\tds_read_b128 v[144:147], v4 offset:12288\n\tds_read_b64_tr_b16 v[180:181], v13 offset:1024\n\tds_read_b64_tr_b16 v[182:183], v13 offset:1536\n\tds_read_b64_tr_b16 v[184:185], v13 offset:2048\n\t"
            "ds_read_b64_tr_b16 v[186:187], v13 offset:2560\n\ts_waitcnt lgkmcnt(10)\n\tv_mfma_f32_16x16x32_bf16 v[36:39], v[176:179], v[120:123], 0\n\ts_waitcnt lgkmcnt(9)\n\tv_mfma_f32_16x16x32_bf16 v[48:51], v[176:179], v[124:127], 0\n\ts_waitcnt lgkmcnt(8)\n\t"
            "v_mfma_f32_16x16x32_bf16 v[60:63], v[176:179], v[128:131], 0\n\tds_read_b64_tr_b16 v[188:189], v13 offset:12288\n\tds_read_b64_tr_b16 v[190:191], v13 offset:12800\n\ts_waitcnt lgkmcnt(9)\n\tv_mfma_f32_16x16x32_bf16 v[72:75], v[176:179], v[132:135], 0\n\tds_read_b128 v[148:151], v9\n\t"
            "s_waitcnt lgkmcnt(9)\n\tv_mfma_f32_16x16x32_bf16 v[84:87], v[176:179], v[136:139], 0\n\tds_read_b128 v[152:155], v5 offset:2048\n\ts_waitcnt lgkmcnt(9)\n\tv_mfma_f32_16x16x32_bf16 v[96:99], v[176:179], v[140:143], 0\n\tds_read_b128 v[156:159], v5 offset:4096\n\t"
            "s_waitcnt lgkmcnt(9)\n\tv_mfma_f32_16x16x32_bf16 v[108:111], v[176:179], v[144:147], 0\n\tds_read_b128 v[160:163], v5 offset:6144\n\ts_waitcnt lgkmcnt(8)\n\tv_mfma_f32_16x16x32_bf16 v[40:43], v[180:183], v[120:123], 0\n\tds_read_b128 v[164:167], v5 offset:8192\n\t"
            "v_mfma_f32_16x16x32_bf16 v[52:55], v[180:183], v[124:127], 0\n\tds_read_b128 v[168:171], v5 offset:10240\n\tv_mfma_f32_16x16x32_bf16 v[64:67], v[180:183], v[128:131], 0\n\tds_read_b128 v[172:175], v5 offset:12288\n\tv_mfma_f32_16x16x32_bf16 v[76:79], v[180:183], v[132:135], 0\n\tds_read_b64_tr_b16 v[192:193], v13 offset:13312\n\t"
            "ds_read_b64_tr_b16 v[194:195], v13 offset:13824\n\tv_mfma_f32_16x16x32_bf16 v[88:91], v[180:183], v[136:139], 0\n\tds_read_b64_tr_b16 v[196:197], v13 offset:14336\n\tds_read_b64_tr_b16 v[198:199], v13 offset:14848\n\tv_mfma_f32_16x16x32_bf16 v[100:103], v[180:183], v[140:143], 0\n\tv_mfma_f32_16x16x32_bf16 v[112:115], v[180:183], v[144:147], 0\n\t"
            "s_waitcnt lgkmcnt(13)\n\tv_mfma_f32_16x16x32_bf16 v[44:47], v[120:123], v[184:187], 0\n\tv_mfma_f32_16x16x32_bf16 v[56:59], v[124:127], v[184:187], 0\n\tv_mfma_f32_16x16x32_bf16 v[68:71], v[128:131], v[184:187], 0\n\tv_mfma_f32_16x16x32_bf16 v[80:83], v[132:135], v[184:187], 0\n\tv_mfma_f32_16x16x32_bf16 v[92:95], v[136:139], v[184:187], 0\n\t"
            "v_mfma_f32_16x16x32_bf16 v[104:107], v[140:143], v[184:187], 0\n\tv_mfma_f32_16x16x32_bf16 v[116:119], v[144:147], v[184:187], 0\n\ts_waitcnt lgkmcnt(10)\n\tv_mfma_f32_16x16x32_bf16 v[36:39], v[188:191], v[148:151], v[36:39]\n\ts_waitcnt lgkmcnt(9)\n\tv_mfma_f32_16x16x32_bf16 v[48:51], v[188:191], v[152:155], v[48:51]\n\t"
            "s_waitcnt lgkmcnt(8)\n\tv_mfma_f32_16x16x32_bf16 v[60:63], v[188:191], v[156:159], v[60:63]\n\ts_waitcnt lgkmcnt(7)\n\tv_mfma_f32_16x16x32_bf16 v[72:75], v[188:191], v[160:163], v[72:75]\n\ts_waitcnt lgkmcnt(6)\n\tv_mfma_f32_16x16x32_bf16 v[84:87], v[188:191], v[164:167], v[84:87]\n\t"
            "s_waitcnt lgkmcnt(5)\n\tv_mfma_f32_16x16x32_bf16 v[96:99], v[188:191], v[168:171], v[96:99]\n\ts_waitcnt lgkmcnt(4)\n\tv_mfma_f32_16x16x32_bf16 v[108:111], v[188:191], v[172:175], v[108:111]\n\ts_waitcnt lgkmcnt(2)\n\tv_mfma_f32_16x16x32_bf16 v[40:43], v[192:195], v[148:151], v[40:43]\n\t"
            "v_mfma_f32_16x16x32_bf16 v[52:55], v[192:195], v[152:155], v[52:55]\n\tv_mfma_f32_16x16x32_bf16 v[64:67], v[192:195], v[156:159], v[64:67]\n\tv_mfma_f32_16x16x32_bf16 v[76:79], v[192:195], v[160:163], v[76:79]\n\ts_waitcnt vmcnt(0) lgkmcnt(0)\n\ts_barrier\n\t"
            "ds_read_b64_tr_b16 v[176:177], v13 offset:38912\n\tds_read_b64_tr_b16 v[178:179], v13 offset:39424\n\tv_mfma_f32_16x16x32_bf16 v[88:91], v[192:195], v[164:167], v[88:91]\n\tds_read_b128 v[120:123], v10\n\tv_mfma_f32_16x16x32_bf16 v[100:103], v[192:195], v[168:171], v[100:103]\n\tds_read_b128 v[124:127], v4 offset:40960\n\t"
            "v_mfma_f32_16x16x32_bf16 v[112:115], v[192:195], v[172:175], v[112:115]\n\tds_read_b128 v[128:131], v4 offset:43008\n\tv_mfma_f32_16x16x32_bf16 v[44:47], v[148:151], v[196:199], v[44:47]\n\tds_read_b128 v[132:135], v4 offset:45056\n\tv_mfma_f32_16x16x32_bf16 v[56:59], v[152:155], v[196:199], v[56:59]\n\tds_read_b128 v[136:139], v4 offset:47104\n\t"
            "v_mfma_f32_16x16x32_bf16 v[68:71], v[156:159], v[196:199], v[68:71]\n\tds_read_b128 v[140:143], v4 offset:49152\n\tv_mfma_f32_16x16x32_bf16 v[80:83], v[160:163], v[196:199], v[80:83]\n\tds_read_b128 v[144:147], v4 offset:51200\n\tv_mfma_f32_16x16x32_bf16 v[92:95], v[164:167], v[196:199], v[92:95]\n\tds_read_b64_tr_b16 v[180:181], v13 offset:39936\n\t"
            "ds_read_b64_tr_b16 v[182:183], v13 offset:40448\n\tv_mfma_f32_16x16x32_bf16 v[104:107], v[168:171], v[196:199], v[104:107]\n\tds_read_b64_tr_b16 v[184:185], v13 offset:40960\n\tds_read_b64_tr_b16 v[186:187], v13 offset:41472\n\tv_mfma_f32_16x16x32_bf16 v[116:119], v[172:175], v[196:199], v[116:119]\n\ts_waitcnt lgkmcnt(10)\n\t"
            "v_mfma_f32_16x16x32_bf16 v[36:39], v[176:179], v[120:123], v[36:39]\n\ts_waitcnt lgkmcnt(9)\n\tv_mfma_f32_16x16x32_bf16 v[48:51], v[176:179], v[124:127], v[48:51]\n\ts_waitcnt lgkmcnt(8)\n\tv_mfma_f32_16x16x32_bf16 v[60:63], v[176:179], v[128:131], v[60:63]\n\tds_read_b64_tr_b16 v[188:189], v13 offset:51200\n\t"
            "ds_read_b64_tr_b16 v[190:191], v13 offset:51712\n\ts_waitcnt lgkmcnt(9)\n\tv_mfma_f32_16x16x32_bf16 v[72:75], v[176:179], v[132:135], v[72:75]\n\tds_read_b128 v[148:151], v11\n\ts_waitcnt lgkmcnt(9)\n\tv_mfma_f32_16x16x32_bf16 v[84:87], v[176:179], v[136:139], v[84:87]\n\t"
            "ds_read_b128 v[152:155], v5 offset:40960\n\ts_waitcnt lgkmcnt(9)\n\tv_mfma_f32_16x16x32_bf16 v[96:99], v[176:179], v[140:143], v[96:99]\n\tds_read_b128 v[156:159], v5 offset:43008\n\ts_waitcnt lgkmcnt(9)\n\tv_mfma_f32_16x16x32_bf16 v[108:111], v[176:179], v[144:147], v[108:111]\n\t"
            "ds_read_b128 v[160:163], v5 offset:45056\n\ts_waitcnt lgkmcnt(8)\n\tv_mfma_f32_16x16x32_bf16 v[40:43], v[180:183], v[120:123], v[40:43]\n\tds_read_b128 v[164:167], v5 offset:47104\n\tv_mfma_f32_16x16x32_bf16 v[52:55], v[180:183], v[124:127], v[52:55]\n\tds_read_b128 v[168:171], v5 offset:49152\n\t"
            "v_mfma_f32_16x16x32_bf16 v[64:67], v[180:183], v[128:131], v[64:67]\n\tds_read_b128 v[172:175], v5 offset:51200\n\tv_mfma_f32_16x16x32_bf16 v[76:79], v[180:183], v[132:135], v[76:79]\n\tds_read_b64_tr_b16 v[192:193], v13 offset:52224\n\tds_read_b64_tr_b16 v[194:195], v13 offset:52736\n\tv_mfma_f32_16x16x32_bf16 v[88:91], v[180:183], v[136:139], v[88:91]\n\t"
            "ds_read_b64_tr_b16 v[196:197], v13 offset:53248\n\tds_read_b64_tr_b16 v[198:199], v13 offset:53760\n\tv_mfma_f32_16x16x32_bf16 v[100:103], v[180:183], v[140:143], v[100:103]\n\tv_mfma_f32_16x16x32_bf16 v[112:115], v[180:183], v[144:147], v[112:115]\n\ts_waitcnt lgkmcnt(13)\n\tv_mfma_f32_16x16x32_bf16 v[44:47], v[120:123], v[184:187], v[44:47]\n\t"
            "v_mfma_f32_16x16x32_bf16 v[56:59], v[124:127], v[184:187], v[56:59]\n\tv_mfma_f32_16x16x32_bf16 v[68:71], v[128:131], v[184:187], v[68:71]\n\tv_mfma_f32_16x16x32_bf16 v[80:83], v[132:135], v[184:187], v[80:83]\n\tv_mfma_f32_16x16x32_bf16 v[92:95], v[136:139], v[184:187], v[92:95]\n\tv_mfma_f32_16x16x32_bf16 v[104:107], v[140:143], v[184:187], v[104:107]\n\tv_mfma_f32_16x16x32_bf16 v[116:119], v[144:147], v[184:187], v[116:119]\n\t"
            "s_waitcnt lgkmcnt(10)\n\tv_mfma_f32_16x16x32_bf16 v[36:39], v[188:191], v[148:151], v[36:39]\n\ts_waitcnt lgkmcnt(9)\n\tv_mfma_f32_16x16x32_bf16 v[48:51], v[188:191], v[152:155], v[48:51]\n\ts_waitcnt lgkmcnt(8)\n\tv_mfma_f32_16x16x32_bf16 v[60:63], v[188:191], v[156:159], v[60:63]\n\t"
            "s_waitcnt lgkmcnt(7)\n\tv_mfma_f32_16x16x32_bf16 v[72:75], v[188:191], v[160:163], v[72:75]\n\ts_waitcnt lgkmcnt(6)\n\tv_mfma_f32_16x16x32_bf16 v[84:87], v[188:191], v[164:167], v[84:87]\n\ts_waitcnt lgkmcnt(5)\n\tv_mfma_f32_16x16x32_bf16 v[96:99], v[188:191], v[168:171], v[96:99]\n\t"
            "s_waitcnt lgkmcnt(4)\n\tv_mfma_f32_16x16x32_bf16 v[108:111], v[188:191], v[172:175], v[108:111]\n\ts_waitcnt lgkmcnt(2)\n\tv_mfma_f32_16x16x32_bf16 v[40:43], v[192:195], v[148:151], v[40:43]\n\tv_mfma_f32_16x16x32_bf16 v[52:55], v[192:195], v[152:155], v[52:55]\n\tv_mfma_f32_16x16x32_bf16 v[64:67], v[192:195], v[156:159], v[64:67]\n\t"
            "v_mfma_f32_16x16x32_bf16 v[76:79], v[192:195], v[160:163], v[76:79]\n\ts_waitcnt lgkmcnt(0)\n\ts_barrier\n\t"
            "ds_read_b64_tr_b16 v[176:177], v12 offset:0\n\tds_read_b64_tr_b16 v[178:179], v12 offset:512\n\tv_mfma_f32_16x16x32_bf16 v[88:91], v[192:195], v[164:167], v[88:91]\n\tds_read_b128 v[120:123], v6\n\tv_mfma_f32_16x16x32_bf16 v[100:103], v[192:195], v[168:171], v[100:103]\n\tds_read_b128 v[124:127], v2 offset:2048\n\t"
            "v_mfma_f32_16x16x32_bf16 v[112:115], v[192:195], v[172:175], v[112:115]\n\tds_read_b128 v[128:131], v2 offset:4096\n\tv_mfma_f32_16x16x32_bf16 v[44:47], v[148:151], v[196:199], v[44:47]\n\tds_read_b128 v[132:135], v2 offset:6144\n\tv_mfma_f32_16x16x32_bf16 v[56:59], v[152:155], v[196:199], v[56:59]\n\tds_read_b128 v[136:139], v2 offset:8192\n\t"
            "v_mfma_f32_16x16x32_bf16 v[68:71], v[156:159], v[196:199], v[68:71]\n\tds_read_b128 v[140:143], v2 offset:10240\n\tv_mfma_f32_16x16x32_bf16 v[80:83], v[160:163], v[196:199], v[80:83]\n\tds_read_b128 v[144:147], v2 offset:12288\n\tv_mfma_f32_16x16x32_bf16 v[92:95], v[164:167], v[196:199], v[92:95]\n\tds_read_b64_tr_b16 v[180:181], v12 offset:1024\n\t"
            "ds_read_b64_tr_b16 v[182:183], v12 offset:1536\n\tv_mfma_f32_16x16x32_bf16 v[104:107], v[168:171], v[196:199], v[104:107]\n\tds_read_b64_tr_b16 v[184:185], v12 offset:2048\n\tds_read_b64_tr_b16 v[186:187], v12 offset:2560\n\tv_mfma_f32_16x16x32_bf16 v[116:119], v[172:175], v[196:199], v[116:119]\n\ts_waitcnt lgkmcnt(10)\n\t"
            "v_mfma_f32_16x16x32_bf16 v[36:39], v[176:179], v[120:123], v[36:39]\n\ts_waitcnt lgkmcnt(9)\n\tv_mfma_f32_16x16x32_bf16 v[48:51], v[176:179], v[124:127], v[48:51]\n\ts_waitcnt lgkmcnt(8)\n\tv_mfma_f32_16x16x32_bf16 v[60:63], v[176:179], v[128:131], v[60:63]\n\tds_read_b64_tr_b16 v[188:189], v12 offset:12288\n\t"
            "ds_read_b64_tr_b16 v[190:191], v12 offset:12800\n\ts_waitcnt lgkmcnt(9)\n\tv_mfma_f32_16x16x32_bf16 v[72:75], v[176:179], v[132:135], v[72:75]\n\tds_read_b128 v[148:151], v7\n\ts_waitcnt lgkmcnt(9)\n\tv_mfma_f32_16x16x32_bf16 v[84:87], v[176:179], v[136:139], v[84:87]\n\t"
            "ds_read_b128 v[152:155], v3 offset:2048\n\ts_waitcnt lgkmcnt(9)\n\tv_mfma_f32_16x16x32_bf16 v[96:99], v[176:179], v[140:143], v[96:99]\n\tds_read_b128 v[156:159], v3 offset:4096\n\ts_waitcnt lgkmcnt(9)\n\tv_mfma_f32_16x16x32_bf16 v[108:111], v[176:179], v[144:147], v[108:111]\n\t"
            "ds_read_b128 v[160:163], v3 offset:6144\n\ts_waitcnt lgkmcnt(8)\n\tv_mfma_f32_16x16x32_bf16 v[40:43], v[180:183], v[120:123], v[40:43]\n\tds_read_b128 v[164:167], v3 offset:8192\n\tv_mfma_f32_16x16x32_bf16 v[52:55], v[180:183], v[124:127], v[52:55]\n\tds_read_b128 v[168:171], v3 offset:10240\n\t"
            "v_mfma_f32_16x16x32_bf16 v[64:67], v[180:183], v[128:131], v[64:67]\n\tds_read_b128 v[172:175], v3 offset:12288\n\tv_mfma_f32_16x16x32_bf16 v[76:79], v[180:183], v[132:135], v[76:79]\n\tds_read_b64_tr_b16 v[192:193], v12 offset:13312\n\tds_read_b64_tr_b16 v[194:195], v12 offset:13824\n\tv_mfma_f32_16x16x32_bf16 v[88:91], v[180:183], v[136:139], v[88:91]\n\t"
            "ds_read_b64_tr_b16 v[196:197], v12 offset:14336\n\tds_read_b64_tr_b16 v[198:199], v12 offset:14848\n\tv_mfma_f32_16x16x32_bf16 v[100:103], v[180:183], v[140:143], v[100:103]\n\tv_mfma_f32_16x16x32_bf16 v[112:115], v[180:183], v[144:147], v[112:115]\n\ts_waitcnt lgkmcnt(13)\n\tv_mfma_f32_16x16x32_bf16 v[44:47], v[120:123], v[184:187], v[44:47]\n\t"
            "v_mfma_f32_16x16x32_bf16 v[56:59], v[124:127], v[184:187], v[56:59]\n\tv_mfma_f32_16x16x32_bf16 v[68:71], v[128:131], v[184:187], v[68:71]\n\tv_mfma_f32_16x16x32_bf16 v[80:83], v[132:135], v[184:187], v[80:83]\n\tv_mfma_f32_16x16x32_bf16 v[92:95], v[136:139], v[184:187], v[92:95]\n\tv_mfma_f32_16x16x32_bf16 v[104:107], v[140:143], v[184:187], v[104:107]\n\tv_mfma_f32_16x16x32_bf16 v[116:119], v[144:147], v[184:187], v[116:119]\n\t"
            "s_waitcnt lgkmcnt(10)\n\tv_mfma_f32_16x16x32_bf16 v[36:39], v[188:191], v[148:151], v[36:39]\n\ts_waitcnt lgkmcnt(9)\n\tv_mfma_f32_16x16x32_bf16 v[48:51], v[188:191], v[152:155], v[48:51]\n\ts_waitcnt lgkmcnt(8)\n\tv_mfma_f32_16x16x32_bf16 v[60:63], v[188:191], v[156:159], v[60:63]\n\t"
            "s_waitcnt lgkmcnt(7)\n\tv_mfma_f32_16x16x32_bf16 v[72:75], v[188:191], v[160:163], v[72:75]\n\ts_waitcnt lgkmcnt(6)\n\tv_mfma_f32_16x16x32_bf16 v[84:87], v[188:191], v[164:167], v[84:87]\n\ts_waitcnt lgkmcnt(5)\n\tv_mfma_f32_16x16x32_bf16 v[96:99], v[188:191], v[168:171], v[96:99]\n\t"
            "s_waitcnt lgkmcnt(4)\n\tv_mfma_f32_16x16x32_bf16 v[108:111], v[188:191], v[172:175], v[108:111]\n\ts_waitcnt lgkmcnt(2)\n\tv_mfma_f32_16x16x32_bf16 v[40:43], v[192:195], v[148:151], v[40:43]\n\tv_mfma_f32_16x16x32_bf16 v[52:55], v[192:195], v[152:155], v[52:55]\n\tv_mfma_f32_16x16x32_bf16 v[64:67], v[192:195], v[156:159], v[64:67]\n\t"
            "v_mfma_f32_16x16x32_bf16 v[76:79], v[192:195], v[160:163], v[76:79]\n\ts_waitcnt lgkmcnt(0)\n\ts_barrier\n\t"
            "ds_read_b64_tr_b16 v[176:177], v13 offset:0\n\tds_read_b64_tr_b16 v[178:179], v13 offset:512\n\tv_mfma_f32_16x16x32_bf16 v[88:91], v[192:195], v[164:167], v[88:91]\n\tds_read_b128 v[120:123], v8\n\tv_mfma_f32_16x16x32_bf16 v[100:103], v[192:195], v[168:171], v[100:103]\n\tds_read_b128 v[124:127], v4 offset:2048\n\t"
            "v_mfma_f32_16x16x32_bf16 v[112:115], v[192:195], v[172:175], v[112:115]\n\tds_read_b128 v[128:131], v4 offset:4096\n\tv_mfma_f32_16x16x32_bf16 v[44:47], v[148:151], v[196:199], v[44:47]\n\tds_read_b128 v[132:135], v4 offset:6144\n\tv_mfma_f32_16x16x32_bf16 v[56:59], v[152:155], v[196:199], v[56:59]\n\tds_read_b128 v[136:139], v4 offset:8192\n\t"
            "v_mfma_f32_16x16x32_bf16 v[68:71], v[156:159], v[196:199], v[68:71]\n\tds_read_b128 v[140:143], v4 offset:10240\n\tv_mfma_f32_16x16x32_bf16 v[80:83], v[160:163], v[196:199], v[80:83]\n\tds_read_b128 v[144:147], v4 offset:12288\n\tv_mfma_f32_16x16x32_bf16 v[92:95], v[164:167], v[196:199], v[92:95]\n\tds_read_b64_tr_b16 v[180:181], v13 offset:1024\n\t"
            "ds_read_b64_tr_b16 v[182:183], v13 offset:1536\n\tv_mfma_f32_16x16x32_bf16 v[104:107], v[168:171], v[196:199], v[104:107]\n\tds_read_b64_tr_b16 v[184:185], v13 offset:2048\n\tds_read_b64_tr_b16 v[186:187], v13 offset:2560\n\tv_mfma_f32_16x16x32_bf16 v[116:119], v[172:175], v[196:199], v[116:119]\n\ts_waitcnt lgkmcnt(10)\n\t"
            "v_mfma_f32_16x16x32_bf16 v[36:39], v[176:179], v[120:123], v[36:39]\n\ts_waitcnt lgkmcnt(9)\n\tv_mfma_f32_16x16x32_bf16 v[48:51], v[176:179], v[124:127], v[48:51]\n\ts_waitcnt lgkmcnt(8)\n\tv_mfma_f32_16x16x32_bf16 v[60:63], v[176:179], v[128:131], v[60:63]\n\tds_read_b64_tr_b16 v[188:189], v13 offset:12288\n\t"
            "ds_read_b64_tr_b16 v[190:191], v13 offset:12800\n\ts_waitcnt lgkmcnt(9)\n\tv_mfma_f32_16x16x32_bf16 v[72:75], v[176:179], v[132:135], v[72:75]\n\tds_read_b128 v[148:151], v9\n\ts_waitcnt lgkmcnt(9)\n\tv_mfma_f32_16x16x32_bf16 v[84:87], v[176:179], v[136:139], v[84:87]\n\t"
            "ds_read_b128 v[152:155], v5 offset:2048\n\ts_waitcnt lgkmcnt(9)\n\tv_mfma_f32_16x16x32_bf16 v[96:99], v[176:179], v[140:143], v[96:99]\n\tds_read_b128 v[156:159], v5 offset:4096\n\ts_waitcnt lgkmcnt(9)\n\tv_mfma_f32_16x16x32_bf16 v[108:111], v[176:179], v[144:147], v[108:111]\n\t"
            "ds_read_b128 v[160:163], v5 offset:6144\n\ts_waitcnt lgkmcnt(8)\n\tv_mfma_f32_16x16x32_bf16 v[40:43], v[180:183], v[120:123], v[40:43]\n\tds_read_b128 v[164:167], v5 offset:8192\n\tv_mfma_f32_16x16x32_bf16 v[52:55], v[180:183], v[124:127], v[52:55]\n\tds_read_b128 v[168:171], v5 offset:10240\n\t"
            "v_mfma_f32_16x16x32_bf16 v[64:67], v[180:183], v[128:131], v[64:67]\n\tds_read_b128 v[172:175], v5 offset:12288\n\tv_mfma_f32_16x16x32_bf16 v[76:79], v[180:183], v[132:135], v[76:79]\n\tds_read_b64_tr_b16 v[192:193], v13 offset:13312\n\tds_read_b64_tr_b16 v[194:195], v13 offset:13824\n\tv_mfma_f32_16x16x32_bf16 v[88:91], v[180:183], v[136:139], v[88:91]\n\t"
            "ds_read_b64_tr_b16 v[196:197], v13 offset:14336\n\tds_read_b64_tr_b16 v[198:199], v13 offset:14848\n\tv_mfma_f32_16x16x32_bf16 v[100:103], v[180:183], v[140:143], v[100:103]\n\tv_mfma_f32_16x16x32_bf16 v[112:115], v[180:183], v[144:147], v[112:115]\n\ts_waitcnt lgkmcnt(13)\n\tv_mfma_f32_16x16x32_bf16 v[44:47], v[120:123], v[184:187], v[44:47]\n\t"
            "v_mfma_f32_16x16x32_bf16 v[56:59], v[124:127], v[184:187], v[56:59]\n\tv_mfma_f32_16x16x32_bf16 v[68:71], v[128:131], v[184:187], v[68:71]\n\tv_mfma_f32_16x16x32_bf16 v[80:83], v[132:135], v[184:187], v[80:83]\n\tv_mfma_f32_16x16x32_bf16 v[92:95], v[136:139], v[184:187], v[92:95]\n\tv_mfma_f32_16x16x32_bf16 v[104:107], v[140:143], v[184:187], v[104:107]\n\tv_mfma_f32_16x16x32_bf16 v[116:119], v[144:147], v[184:187], v[116:119]\n\t"
            "s_waitcnt lgkmcnt(10)\n\tv_mfma_f32_16x16x32_bf16 v[36:39], v[188:191], v[148:151], v[36:39]\n\ts_waitcnt lgkmcnt(9)\n\tv_mfma_f32_16x16x32_bf16 v[48:51], v[188:191], v[152:155], v[48:51]\n\ts_waitcnt lgkmcnt(8)\n\tv_mfma_f32_16x16x32_bf16 v[60:63], v[188:191], v[156:159], v[60:63]\n\t"
            "s_waitcnt lgkmcnt(7)\n\tv_mfma_f32_16x16x32_bf16 v[72:75], v[188:191], v[160:163], v[72:75]\n\ts_waitcnt lgkmcnt(6)\n\tv_mfma_f32_16x16x32_bf16 v[84:87], v[188:191], v[164:167], v[84:87]\n\ts_waitcnt lgkmcnt(5)\n\tv_mfma_f32_16x16x32_bf16 v[96:99], v[188:191], v[168:171], v[96:99]\n\t"
            "s_waitcnt lgkmcnt(4)\n\tv_mfma_f32_16x16x32_bf16 v[108:111], v[188:191], v[172:175], v[108:111]\n\ts_waitcnt lgkmcnt(2)\n\tv_mfma_f32_16x16x32_bf16 v[40:43], v[192:195], v[148:151], v[40:43]\n\tv_mfma_f32_16x16x32_bf16 v[52:55], v[192:195], v[152:155], v[52:55]\n\tv_mfma_f32_16x16x32_bf16 v[64:67], v[192:195], v[156:159], v[64:67]\n\t"
            "v_mfma_f32_16x16x32_bf16 v[76:79], v[192:195], v[160:163], v[76:79]\n\ts_waitcnt lgkmcnt(0)\n\ts_barrier\n\t"
            "ds_read_b64_tr_b16 v[176:177], v13 offset:38912\n\tds_read_b64_tr_b16 v[178:179], v13 offset:39424\n\tv_mfma_f32_16x16x32_bf16 v[88:91], v[192:195], v[164:167], v[88:91]\n\tds_read_b128 v[120:123], v10\n\tv_mfma_f32_16x16x32_bf16 v[100:103], v[192:195], v[168:171], v[100:103]\n\tds_read_b128 v[124:127], v4 offset:40960\n\t"
            "v_mfma_f32_16x16x32_bf16 v[112:115], v[192:195], v[172:175], v[112:115]\n\tds_read_b128 v[128:131], v4 offset:43008\n\tv_mfma_f32_16x16x32_bf16 v[44:47], v[148:151], v[196:199], v[44:47]\n\tds_read_b128 v[132:135], v4 offset:45056\n\tv_mfma_f32_16x16x32_bf16 v[56:59], v[152:155], v[196:199], v[56:59]\n\tds_read_b128 v[136:139], v4 offset:47104\n\t"
            "v_mfma_f32_16x16x32_bf16 v[68:71], v[156:159], v[196:199], v[68:71]\n\tds_read_b128 v[140:143], v4 offset:49152\n\tv_mfma_f32_16x16x32_bf16 v[80:83], v[160:163], v[196:199], v[80:83]\n\tds_read_b128 v[144:147], v4 offset:51200\n\tv_mfma_f32_16x16x32_bf16 v[92:95], v[164:167], v[196:199], v[92:95]\n\tds_read_b64_tr_b16 v[180:181], v13 offset:39936\n\t"
            "ds_read_b64_tr_b16 v[182:183], v13 offset:40448\n\tv_mfma_f32_16x16x32_bf16 v[104:107], v[168:171], v[196:199], v[104:107]\n\tds_read_b64_tr_b16 v[184:185], v13 offset:40960\n\tds_read_b64_tr_b16 v[186:187], v13 offset:41472\n\tv_mfma_f32_16x16x32_bf16 v[116:119], v[172:175], v[196:199], v[116:119]\n\ts_waitcnt lgkmcnt(10)\n\t"
            "v_mfma_f32_16x16x32_bf16 v[36:39], v[176:179], v[120:123], v[36:39]\n\ts_waitcnt lgkmcnt(9)\n\tv_mfma_f32_16x16x32_bf16 v[48:51], v[176:179], v[124:127], v[48:51]\n\ts_waitcnt lgkmcnt(8)\n\tv_mfma_f32_16x16x32_bf16 v[60:63], v[176:179], v[128:131], v[60:63]\n\tds_read_b64_tr_b16 v[188:189], v13 offset:51200\n\t"
            "ds_read_b64_tr_b16 v[190:191], v13 offset:51712\n\ts_waitcnt lgkmcnt(9)\n\tv_mfma_f32_16x16x32_bf16 v[72:75], v[176:179], v[132:135], v[72:75]\n\tds_read_b128 v[148:151], v11\n\ts_waitcnt lgkmcnt(9)\n\tv_mfma_f32_16x16x32_bf16 v[84:87], v[176:179], v[136:139], v[84:87]\n\t"
            "ds_read_b128 v[152:155], v5 offset:40960\n\ts_waitcnt lgkmcnt(9)\n\tv_mfma_f32_16x16x32_bf16 v[96:99], v[176:179], v[140:143], v[96:99]\n\tds_read_b128 v[156:159], v5 offset:43008\n\ts_waitcnt lgkmcnt(9)\n\tv_mfma_f32_16x16x32_bf16 v[108:111], v[176:179], v[144:147], v[108:111]\n\t"
            "ds_read_b128 v[160:163], v5 offset:45056\n\ts_waitcnt lgkmcnt(8)\n\tv_mfma_f32_16x16x32_bf16 v[40:43], v[180:183], v[120:123], v[40:43]\n\tds_read_b128 v[164:167], v5 offset:47104\n\tv_mfma_f32_16x16x32_bf16 v[52:55], v[180:183], v[124:127], v[52:55]\n\tds_read_b128 v[168:171], v5 offset:49152\n\t"
            "v_mfma_f32_16x16x32_bf16 v[64:67], v[180:183], v[128:131], v[64:67]\n\tds_read_b128 v[172:175], v5 offset:51200\n\tv_mfma_f32_16x16x32_bf16 v[76:79], v[180:183], v[132:135], v[76:79]\n\tds_read_b64_tr_b16 v[192:193], v13 offset:52224\n\tds_read_b64_tr_b16 v[194:195], v13 offset:52736\n\tv_mfma_f32_16x16x32_bf16 v[88:91], v[180:183], v[136:139], v[88:91]\n\t"
            "ds_read_b64_tr_b16 v[196:197], v13 offset:53248\n\tds_read_b64_tr_b16 v[198:199], v13 offset:53760\n\tv_mfma_f32_16x16x32_bf16 v[100:103], v[180:183], v[140:143], v[100:103]\n\tv_mfma_f32_16x16x32_bf16 v[112:115], v[180:183], v[144:147], v[112:115]\n\ts_waitcnt lgkmcnt(13)\n\tv_mfma_f32_16x16x32_bf16 v[44:47], v[120:123], v[184:187], v[44:47]\n\t"
            "v_mfma_f32_16x16x32_bf16 v[56:59], v[124:127], v[184:187], v[56:59]\n\tv_mfma_f32_16x16x32_bf16 v[68:71], v[128:131], v[184:187], v[68:71]\n\tv_mfma_f32_16x16x32_bf16 v[80:83], v[132:135], v[184:187], v[80:83]\n\tv_mfma_f32_16x16x32_bf16 v[92:95], v[136:139], v[184:187], v[92:95]\n\tv_mfma_f32_16x16x32_bf16 v[104:107], v[140:143], v[184:187], v[104:107]\n\tv_mfma_f32_16x16x32_bf16 v[116:119], v[144:147], v[184:187], v[116:119]\n\t"
            "s_waitcnt lgkmcnt(10)\n\tv_mfma_f32_16x16x32_bf16 v[36:39], v[188:191], v[148:151], v[36:39]\n\ts_waitcnt lgkmcnt(9)\n\tv_mfma_f32_16x16x32_bf16 v[48:51], v[188:191], v[152:155], v[48:51]\n\ts_waitcnt lgkmcnt(8)\n\tv_mfma_f32_16x16x32_bf16 v[60:63], v[188:191], v[156:159], v[60:63]\n\t"
            "s_waitcnt lgkmcnt(7)\n\tv_mfma_f32_16x16x32_bf16 v[72:75], v[188:191], v[160:163], v[72:75]\n\ts_waitcnt lgkmcnt(6)\n\tv_mfma_f32_16x16x32_bf16 v[84:87], v[188:191], v[164:167], v[84:87]\n\ts_waitcnt lgkmcnt(5)\n\tv_mfma_f32_16x16x32_bf16 v[96:99], v[188:191], v[168:171], v[96:99]\n\t"
            "s_waitcnt lgkmcnt(4)\n\tv_mfma_f32_16x16x32_bf16 v[108:111], v[188:191], v[172:175], v[108:111]\n\ts_waitcnt lgkmcnt(2)\n\tv_mfma_f32_16x16x32_bf16 v[40:43], v[192:195], v[148:151], v[40:43]\n\tv_mfma_f32_16x16x32_bf16 v[52:55], v[192:195], v[152:155], v[52:55]\n\tv_mfma_f32_16x16x32_bf16 v[64:67], v[192:195], v[156:159], v[64:67]\n\t"
            "v_mfma_f32_16x16x32_bf16 v[76:79], v[192:195], v[160:163], v[76:79]\n\ts_waitcnt lgkmcnt(0)\n\ts_barrier\n\t"
            "ds_read_b64_tr_b16 v[176:177], v12 offset:0\n\tds_read_b64_tr_b16 v[178:179], v12 offset:512\n\tv_mfma_f32_16x16x32_bf16 v[88:91], v[192:195], v[164:167], v[88:91]\n\tds_read_b128 v[120:123], v6\n\tv_mfma_f32_16x16x32_bf16 v[100:103], v[192:195], v[168:171], v[100:103]\n\tds_read_b128 v[124:127], v2 offset:2048\n\t"
            "v_mfma_f32_16x16x32_bf16 v[112:115], v[192:195], v[172:175], v[112:115]\n\tds_read_b128 v[128:131], v2 offset:4096\n\tv_mfma_f32_16x16x32_bf16 v[44:47], v[148:151], v[196:199], v[44:47]\n\tds_read_b128 v[132:135], v2 offset:6144\n\tv_mfma_f32_16x16x32_bf16 v[56:59], v[152:155], v[196:199], v[56:59]\n\tds_read_b128 v[136:139], v2 offset:8192\n\t"
            "v_mfma_f32_16x16x32_bf16 v[68:71], v[156:159], v[196:199], v[68:71]\n\tds_read_b128 v[140:143], v2 offset:10240\n\tv_mfma_f32_16x16x32_bf16 v[80:83], v[160:163], v[196:199], v[80:83]\n\tds_read_b128 v[144:147], v2 offset:12288\n\tv_mfma_f32_16x16x32_bf16 v[92:95], v[164:167], v[196:199], v[92:95]\n\tds_read_b64_tr_b16 v[180:181], v12 offset:1024\n\t"
            "ds_read_b64_tr_b16 v[182:183], v12 offset:1536\n\tv_mfma_f32_16x16x32_bf16 v[104:107], v[168:171], v[196:199], v[104:107]\n\tds_read_b64_tr_b16 v[184:185], v12 offset:2048\n\tds_read_b64_tr_b16 v[186:187], v12 offset:2560\n\tv_mfma_f32_16x16x32_bf16 v[116:119], v[172:175], v[196:199], v[116:119]\n\ts_waitcnt lgkmcnt(10)\n\t"
            "v_mfma_f32_16x16x32_bf16 v[36:39], v[176:179], v[120:123], v[36:39]\n\ts_waitcnt lgkmcnt(9)\n\tv_mfma_f32_16x16x32_bf16 v[48:51], v[176:179], v[124:127], v[48:51]\n\ts_waitcnt lgkmcnt(8)\n\tv_mfma_f32_16x16x32_bf16 v[60:63], v[176:179], v[128:131], v[60:63]\n\tds_read_b64_tr_b16 v[188:189], v12 offset:12288\n\t"
            "ds_read_b64_tr_b16 v[190:191], v12 offset:12800\n\ts_waitcnt lgkmcnt(9)\n\tv_mfma_f32_16x16x32_bf16 v[72:75], v[176:179], v[132:135], v[72:75]\n\tds_read_b128 v[148:151], v7\n\ts_waitcnt lgkmcnt(9)\n\tv_mfma_f32_16x16x32_bf16 v[84:87], v[176:179], v[136:139], v[84:87]\n\t"
            "ds_read_b128 v[152:155], v3 offset:2048\n\ts_waitcnt lgkmcnt(9)\n\tv_mfma_f32_16x16x32_bf16 v[96:99], v[176:179], v[140:143], v[96:99]\n\tds_read_b128 v[156:159], v3 offset:4096\n\ts_waitcnt lgkmcnt(9)\n\tv_mfma_f32_16x16x32_bf16 v[108:111], v[176:179], v[144:147], v[108:111]\n\t"
            "ds_read_b128 v[160:163], v3 offset:6144\n\ts_waitcnt lgkmcnt(8)\n\tv_mfma_f32_16x16x32_bf16 v[40:43], v[180:183], v[120:123], v[40:43]\n\tds_read_b128 v[164:167], v3 offset:8192\n\tv_mfma_f32_16x16x32_bf16 v[52:55], v[180:183], v[124:127], v[52:55]\n\tds_read_b128 v[168:171], v3 offset:10240\n\t"
            "v_mfma_f32_16x16x32_bf16 v[64:67], v[180:183], v[128:131], v[64:67]\n\tds_read_b128 v[172:175], v3 offset:12288\n\tv_mfma_f32_16x16x32_bf16 v[76:79], v[180:183], v[132:135], v[76:79]\n\tds_read_b64_tr_b16 v[192:193], v12 offset:13312\n\tds_read_b64_tr_b16 v[194:195], v12 offset:13824\n\tv_mfma_f32_16x16x32_bf16 v[88:91], v[180:183], v[136:139], v[88:91]\n\t"
            "ds_read_b64_tr_b16 v[196:197], v12 offset:14336\n\tds_read_b64_tr_b16 v[198:199], v12 offset:14848\n\tv_mfma_f32_16x16x32_bf16 v[100:103], v[180:183], v[140:143], v[100:103]\n\tv_mfma_f32_16x16x32_bf16 v[112:115], v[180:183], v[144:147], v[112:115]\n\ts_waitcnt lgkmcnt(13)\n\tv_mfma_f32_16x16x32_bf16 v[44:47], v[120:123], v[184:187], v[44:47]\n\t"
            "v_mfma_f32_16x16x32_bf16 v[56:59], v[124:127], v[184:187], v[56:59]\n\tv_mfma_f32_16x16x32_bf16 v[68:71], v[128:131], v[184:187], v[68:71]\n\tv_mfma_f32_16x16x32_bf16 v[80:83], v[132:135], v[184:187], v[80:83]\n\tv_mfma_f32_16x16x32_bf16 v[92:95], v[136:139], v[184:187], v[92:95]\n\tv_mfma_f32_16x16x32_bf16 v[104:107], v[140:143], v[184:187], v[104:107]\n\tv_mfma_f32_16x16x32_bf16 v[116:119], v[144:147], v[184:187], v[116:119]\n\t"
            "s_waitcnt lgkmcnt(10)\n\tv_mfma_f32_16x16x32_bf16 v[36:39], v[188:191], v[148:151], v[36:39]\n\ts_waitcnt lgkmcnt(9)\n\tv_mfma_f32_16x16x32_bf16 v[48:51], v[188:191], v[152:155], v[48:51]\n\ts_waitcnt lgkmcnt(8)\n\tv_mfma_f32_16x16x32_bf16 v[60:63], v[188:191], v[156:159], v[60:63]\n\t"
            "s_waitcnt lgkmcnt(7)\n\tv_mfma_f32_16x16x32_bf16 v[72:75], v[188:191], v[160:163], v[72:75]\n\ts_waitcnt lgkmcnt(6)\n\tv_mfma_f32_16x16x32_bf16 v[84:87], v[188:191], v[164:167], v[84:87]\n\ts_waitcnt lgkmcnt(5)\n\tv_mfma_f32_16x16x32_bf16 v[96:99], v[188:191], v[168:171], v[96:99]\n\t"
            "s_waitcnt lgkmcnt(4)\n\tv_mfma_f32_16x16x32_bf16 v[108:111], v[188:191], v[172:175], v[108:111]\n\ts_waitcnt lgkmcnt(2)\n\tv_mfma_f32_16x16x32_bf16 v[40:43], v[192:195], v[148:151], v[40:43]\n\tv_mfma_f32_16x16x32_bf16 v[52:55], v[192:195], v[152:155], v[52:55]\n\tv_mfma_f32_16x16x32_bf16 v[64:67], v[192:195], v[156:159], v[64:67]\n\t"
            "v_mfma_f32_16x16x32_bf16 v[76:79], v[192:195], v[160:163], v[76:79]\n\ts_waitcnt lgkmcnt(0)\n\ts_barrier\n\t"
            "ds_read_b64_tr_b16 v[176:177], v13 offset:0\n\tds_read_b64_tr_b16 v[178:179], v13 offset:512\n\tv_mfma_f32_16x16x32_bf16 v[88:91], v[192:195], v[164:167], v[88:91]\n\tds_read_b128 v[120:123], v8\n\tv_mfma_f32_16x16x32_bf16 v[100:103], v[192:195], v[168:171], v[100:103]\n\tds_read_b128 v[124:127], v4 offset:2048\n\t"
            "v_mfma_f32_16x16x32_bf16 v[112:115], v[192:195], v[172:175], v[112:115]\n\tds_read_b128 v[128:131], v4 offset:4096\n\tv_mfma_f32_16x16x32_bf16 v[44:47], v[148:151], v[196:199], v[44:47]\n\tds_read_b128 v[132:135], v4 offset:6144\n\tv_mfma_f32_16x16x32_bf16 v[56:59], v[152:155], v[196:199], v[56:59]\n\tds_read_b128 v[136:139], v4 offset:8192\n\t"
            "v_mfma_f32_16x16x32_bf16 v[68:71], v[156:159], v[196:199], v[68:71]\n\tds_read_b128 v[140:143], v4 offset:10240\n\tv_mfma_f32_16x16x32_bf16 v[80:83], v[160:163], v[196:199], v[80:83]\n\tds_read_b128 v[144:147], v4 offset:12288\n\tv_mfma_f32_16x16x32_bf16 v[92:95], v[164:167], v[196:199], v[92:95]\n\tds_read_b64_tr_b16 v[180:181], v13 offset:1024\n\t"
            "ds_read_b64_tr_b16 v[182:183], v13 offset:1536\n\tv_mfma_f32_16x16x32_bf16 v[104:107], v[168:171], v[196:199], v[104:107]\n\tds_read_b64_tr_b16 v[184:185], v13 offset:2048\n\tds_read_b64_tr_b16 v[186:187], v13 offset:2560\n\tv_mfma_f32_16x16x32_bf16 v[116:119], v[172:175], v[196:199], v[116:119]\n\ts_waitcnt lgkmcnt(10)\n\t"
            "v_mfma_f32_16x16x32_bf16 v[36:39], v[176:179], v[120:123], v[36:39]\n\ts_waitcnt lgkmcnt(9)\n\tv_mfma_f32_16x16x32_bf16 v[48:51], v[176:179], v[124:127], v[48:51]\n\ts_waitcnt lgkmcnt(8)\n\tv_mfma_f32_16x16x32_bf16 v[60:63], v[176:179], v[128:131], v[60:63]\n\tds_read_b64_tr_b16 v[188:189], v13 offset:12288\n\t"
            "ds_read_b64_tr_b16 v[190:191], v13 offset:12800\n\ts_waitcnt lgkmcnt(9)\n\tv_mfma_f32_16x16x32_bf16 v[72:75], v[176:179], v[132:135], v[72:75]\n\tds_read_b128 v[148:151], v9\n\ts_waitcnt lgkmcnt(9)\n\tv_mfma_f32_16x16x32_bf16 v[84:87], v[176:179], v[136:139], v[84:87]\n\t"
            "ds_read_b128 v[152:155], v5 offset:2048\n\ts_waitcnt lgkmcnt(9)\n\tv_mfma_f32_16x16x32_bf16 v[96:99], v[176:179], v[140:143], v[96:99]\n\tds_read_b128 v[156:159], v5 offset:4096\n\ts_waitcnt lgkmcnt(9)\n\tv_mfma_f32_16x16x32_bf16 v[108:111], v[176:179], v[144:147], v[108:111]\n\t"
            "ds_read_b128 v[160:163], v5 offset:6144\n\ts_waitcnt lgkmcnt(8)\n\tv_mfma_f32_16x16x32_bf16 v[40:43], v[180:183], v[120:123], v[40:43]\n\tds_read_b128 v[164:167], v5 offset:8192\n\tv_mfma_f32_16x16x32_bf16 v[52:55], v[180:183], v[124:127], v[52:55]\n\tds_read_b128 v[168:171], v5 offset:10240\n\t"
            "v_mfma_f32_16x16x32_bf16 v[64:67], v[180:183], v[128:131], v[64:67]\n\tds_read_b128 v[172:175], v5 offset:12288\n\tv_mfma_f32_16x16x32_bf16 v[76:79], v[180:183], v[132:135], v[76:79]\n\tds_read_b64_tr_b16 v[192:193], v13 offset:13312\n\tds_read_b64_tr_b16 v[194:195], v13 offset:13824\n\tv_mfma_f32_16x16x32_bf16 v[88:91], v[180:183], v[136:139], v[88:91]\n\t"
            "ds_read_b64_tr_b16 v[196:197], v13 offset:14336\n\tds_read_b64_tr_b16 v[198:199], v13 offset:14848\n\tv_mfma_f32_16x16x32_bf16 v[100:103], v[180:183], v[140:143], v[100:103]\n\tv_mfma_f32_16x16x32_bf16 v[112:115], v[180:183], v[144:147], v[112:115]\n\ts_waitcnt lgkmcnt(13)\n\tv_mfma_f32_16x16x32_bf16 v[44:47], v[120:123], v[184:187], v[44:47]\n\t"
            "v_mfma_f32_16x16x32_bf16 v[56:59], v[124:127], v[184:187], v[56:59]\n\tv_mfma_f32_16x16x32_bf16 v[68:71], v[128:131], v[184:187], v[68:71]\n\tv_mfma_f32_16x16x32_bf16 v[80:83], v[132:135], v[184:187], v[80:83]\n\tv_mfma_f32_16x16x32_bf16 v[92:95], v[136:139], v[184:187], v[92:95]\n\tv_mfma_f32_16x16x32_bf16 v[104:107], v[140:143], v[184:187], v[104:107]\n\tv_mfma_f32_16x16x32_bf16 v[116:119], v[144:147], v[184:187], v[116:119]\n\t"
            "s_waitcnt lgkmcnt(10)\n\tv_mfma_f32_16x16x32_bf16 v[36:39], v[188:191], v[148:151], v[36:39]\n\ts_waitcnt lgkmcnt(9)\n\tv_mfma_f32_16x16x32_bf16 v[48:51], v[188:191], v[152:155], v[48:51]\n\ts_waitcnt lgkmcnt(8)\n\tv_mfma_f32_16x16x32_bf16 v[60:63], v[188:191], v[156:159], v[60:63]\n\t"
            "s_waitcnt lgkmcnt(7)\n\tv_mfma_f32_16x16x32_bf16 v[72:75], v[188:191], v[160:163], v[72:75]\n\ts_waitcnt lgkmcnt(6)\n\tv_mfma_f32_16x16x32_bf16 v[84:87], v[188:191], v[164:167], v[84:87]\n\ts_waitcnt lgkmcnt(5)\n\tv_mfma_f32_16x16x32_bf16 v[96:99], v[188:191], v[168:171], v[96:99]\n\t"
            "s_waitcnt lgkmcnt(4)\n\tv_mfma_f32_16x16x32_bf16 v[108:111], v[188:191], v[172:175], v[108:111]\n\ts_waitcnt lgkmcnt(2)\n\tv_mfma_f32_16x16x32_bf16 v[40:43], v[192:195], v[148:151], v[40:43]\n\tv_mfma_f32_16x16x32_bf16 v[52:55], v[192:195], v[152:155], v[52:55]\n\tv_mfma_f32_16x16x32_bf16 v[64:67], v[192:195], v[156:159], v[64:67]\n\t"
            "v_mfma_f32_16x16x32_bf16 v[76:79], v[192:195], v[160:163], v[76:79]\n\ts_waitcnt lgkmcnt(0)\n\ts_barrier\n\t"
            "ds_read_b64_tr_b16 v[176:177], v13 offset:38912\n\tds_read_b64_tr_b16 v[178:179], v13 offset:39424\n\tv_mfma_f32_16x16x32_bf16 v[88:91], v[192:195], v[164:167], v[88:91]\n\tds_read_b128 v[120:123], v10\n\tv_mfma_f32_16x16x32_bf16 v[100:103], v[192:195], v[168:171], v[100:103]\n\tds_read_b128 v[124:127], v4 offset:40960\n\t"
            "v_mfma_f32_16x16x32_bf16 v[112:115], v[192:195], v[172:175], v[112:115]\n\tds_read_b128 v[128:131], v4 offset:43008\n\tv_mfma_f32_16x16x32_bf16 v[44:47], v[148:151], v[196:199], v[44:47]\n\tds_read_b128 v[132:135], v4 offset:45056\n\tv_mfma_f32_16x16x32_bf16 v[56:59], v[152:155], v[196:199], v[56:59]\n\tds_read_b128 v[136:139], v4 offset:47104\n\t"
            "v_mfma_f32_16x16x32_bf16 v[68:71], v[156:159], v[196:199], v[68:71]\n\tds_read_b128 v[140:143], v4 offset:49152\n\tv_mfma_f32_16x16x32_bf16 v[80:83], v[160:163], v[196:199], v[80:83]\n\tds_read_b128 v[144:147], v4 offset:51200\n\tv_mfma_f32_16x16x32_bf16 v[92:95], v[164:167], v[196:199], v[92:95]\n\tds_read_b64_tr_b16 v[180:181], v13 offset:39936\n\t"
            "ds_read_b64_tr_b16 v[182:183], v13 offset:40448\n\tv_mfma_f32_16x16x32_bf16 v[104:107], v[168:171], v[196:199], v[104:107]\n\tds_read_b64_tr_b16 v[184:185], v13 offset:40960\n\tds_read_b64_tr_b16 v[186:187], v13 offset:41472\n\tv_mfma_f32_16x16x32_bf16 v[116:119], v[172:175], v[196:199], v[116:119]\n\ts_waitcnt lgkmcnt(10)\n\t"
            "v_mfma_f32_16x16x32_bf16 v[36:39], v[176:179], v[120:123], v[36:39]\n\ts_waitcnt lgkmcnt(9)\n\tv_mfma_f32_16x16x32_bf16 v[48:51], v[176:179], v[124:127], v[48:51]\n\ts_waitcnt lgkmcnt(8)\n\tv_mfma_f32_16x16x32_bf16 v[60:63], v[176:179], v[128:131], v[60:63]\n\tds_read_b64_tr_b16 v[188:189], v13 offset:51200\n\t"
            "ds_read_b64_tr_b16 v[190:191], v13 offset:51712\n\ts_waitcnt lgkmcnt(9)\n\tv_mfma_f32_16x16x32_bf16 v[72:75], v[176:179], v[132:135], v[72:75]\n\tds_read_b128 v[148:151], v11\n\ts_waitcnt lgkmcnt(9)\n\tv_mfma_f32_16x16x32_bf16 v[84:87], v[176:179], v[136:139], v[84:87]\n\t"
            "ds_read_b128 v[152:155], v5 offset:40960\n\ts_waitcnt lgkmcnt(9)\n\tv_mfma_f32_16x16x32_bf16 v[96:99], v[176:179], v[140:143], v[96:99]\n\tds_read_b128 v[156:159], v5 offset:43008\n\ts_waitcnt lgkmcnt(9)\n\tv_mfma_f32_16x16x32_bf16 v[108:111], v[176:179], v[144:147], v[108:111]\n\t"
            "ds_read_b128 v[160:163], v5 offset:45056\n\ts_waitcnt lgkmcnt(8)\n\tv_mfma_f32_16x16x32_bf16 v[40:43], v[180:183], v[120:123], v[40:43]\n\tds_read_b128 v[164:167], v5 offset:47104\n\tv_mfma_f32_16x16x32_bf16 v[52:55], v[180:183], v[124:127], v[52:55]\n\tds_read_b128 v[168:171], v5 offset:49152\n\t"
            "v_mfma_f32_16x16x32_bf16 v[64:67], v[180:183], v[128:131], v[64:67]\n\tds_read_b128 v[172:175], v5 offset:51200\n\tv_mfma_f32_16x16x32_bf16 v[76:79], v[180:183], v[132:135], v[76:79]\n\tds_read_b64_tr_b16 v[192:193], v13 offset:52224\n\tds_read_b64_tr_b16 v[194:195], v13 offset:52736\n\tv_mfma_f32_16x16x32_bf16 v[88:91], v[180:183], v[136:139], v[88:91]\n\t"
            "ds_read_b64_tr_b16 v[196:197], v13 offset:53248\n\tds_read_b64_tr_b16 v[198:199], v13 offset:53760\n\tv_mfma_f32_16x16x32_bf16 v[100:103], v[180:183], v[140:143], v[100:103]\n\tv_mfma_f32_16x16x32_bf16 v[112:115], v[180:183], v[144:147], v[112:115]\n\ts_waitcnt lgkmcnt(13)\n\tv_mfma_f32_16x16x32_bf16 v[44:47], v[120:123], v[184:187], v[44:47]\n\t"
            "v_mfma_f32_16x16x32_bf16 v[56:59], v[124:127], v[184:187], v[56:59]\n\tv_mfma_f32_16x16x32_bf16 v[68:71], v[128:131], v[184:187], v[68:71]\n\tv_mfma_f32_16x16x32_bf16 v[80:83], v[132:135], v[184:187], v[80:83]\n\tv_mfma_f32_16x16x32_bf16 v[92:95], v[136:139], v[184:187], v[92:95]\n\tv_mfma_f32_16x16x32_bf16 v[104:107], v[140:143], v[184:187], v[104:107]\n\tv_mfma_f32_16x16x32_bf16 v[116:119], v[144:147], v[184:187], v[116:119]\n\t"
            "s_waitcnt lgkmcnt(10)\n\tv_mfma_f32_16x16x32_bf16 v[36:39], v[188:191], v[148:151], v[36:39]\n\ts_waitcnt lgkmcnt(9)\n\tv_mfma_f32_16x16x32_bf16 v[48:51], v[188:191], v[152:155], v[48:51]\n\ts_waitcnt lgkmcnt(8)\n\tv_mfma_f32_16x16x32_bf16 v[60:63], v[188:191], v[156:159], v[60:63]\n\t"
            "s_waitcnt lgkmcnt(7)\n\tv_mfma_f32_16x16x32_bf16 v[72:75], v[188:191], v[160:163], v[72:75]\n\ts_waitcnt lgkmcnt(6)\n\tv_mfma_f32_16x16x32_bf16 v[84:87], v[188:191], v[164:167], v[84:87]\n\ts_waitcnt lgkmcnt(5)\n\tv_mfma_f32_16x16x32_bf16 v[96:99], v[188:191], v[168:171], v[96:99]\n\t"
            "s_waitcnt lgkmcnt(4)\n\tv_mfma_f32_16x16x32_bf16 v[108:111], v[188:191], v[172:175], v[108:111]\n\ts_waitcnt lgkmcnt(2)\n\tv_mfma_f32_16x16x32_bf16 v[40:43], v[192:195], v[148:151], v[40:43]\n\tv_mfma_f32_16x16x32_bf16 v[52:55], v[192:195], v[152:155], v[52:55]\n\tv_mfma_f32_16x16x32_bf16 v[64:67], v[192:195], v[156:159], v[64:67]\n\t"
            "v_mfma_f32_16x16x32_bf16 v[76:79], v[192:195], v[160:163], v[76:79]\n\ts_waitcnt lgkmcnt(0)\n\ts_barrier\n\t"
            "ds_read_b64_tr_b16 v[176:177], v12 offset:0\n\tds_read_b64_tr_b16 v[178:179], v12 offset:512\n\tv_mfma_f32_16x16x32_bf16 v[88:91], v[192:195], v[164:167], v[88:91]\n\tds_read_b128 v[120:123], v6\n\tv_mfma_f32_16x16x32_bf16 v[100:103], v[192:195], v[168:171], v[100:103]\n\tds_read_b128 v[124:127], v2 offset:2048\n\t"
            "v_mfma_f32_16x16x32_bf16 v[112:115], v[192:195], v[172:175], v[112:115]\n\tds_read_b128 v[128:131], v2 offset:4096\n\tv_mfma_f32_16x16x32_bf16 v[44:47], v[148:151], v[196:199], v[44:47]\n\tds_read_b128 v[132:135], v2 offset:6144\n\tv_mfma_f32_16x16x32_bf16 v[56:59], v[152:155], v[196:199], v[56:59]\n\tds_read_b128 v[136:139], v2 offset:8192\n\t"
            "v_mfma_f32_16x16x32_bf16 v[68:71], v[156:159], v[196:199], v[68:71]\n\tds_read_b128 v[140:143], v2 offset:10240\n\tv_mfma_f32_16x16x32_bf16 v[80:83], v[160:163], v[196:199], v[80:83]\n\tds_read_b128 v[144:147], v2 offset:12288\n\tv_mfma_f32_16x16x32_bf16 v[92:95], v[164:167], v[196:199], v[92:95]\n\tds_read_b64_tr_b16 v[180:181], v12 offset:1024\n\t"
            "ds_read_b64_tr_b16 v[182:183], v12 offset:1536\n\tv_mfma_f32_16x16x32_bf16 v[104:107], v[168:171], v[196:199], v[104:107]\n\tv_mfma_f32_16x16x32_bf16 v[116:119], v[172:175], v[196:199], v[116:119]\n\ts_waitcnt lgkmcnt(8)\n\tv_mfma_f32_16x16x32_bf16 v[36:39], v[176:179], v[120:123], v[36:39]\n\ts_waitcnt lgkmcnt(7)\n\t"
            "v_mfma_f32_16x16x32_bf16 v[48:51], v[176:179], v[124:127], v[48:51]\n\ts_waitcnt lgkmcnt(6)\n\tv_mfma_f32_16x16x32_bf16 v[60:63], v[176:179], v[128:131], v[60:63]\n\tds_read_b64_tr_b16 v[188:189], v12 offset:12288\n\tds_read_b64_tr_b16 v[190:191], v12 offset:12800\n\ts_waitcnt lgkmcnt(7)\n\t"
            "v_mfma_f32_16x16x32_bf16 v[72:75], v[176:179], v[132:135], v[72:75]\n\tds_read_b128 v[148:151], v7\n\ts_waitcnt lgkmcnt(7)\n\tv_mfma_f32_16x16x32_bf16 v[84:87], v[176:179], v[136:139], v[84:87]\n\tds_read_b128 v[152:155], v3 offset:2048\n\ts_waitcnt lgkmcnt(7)\n\t"
            "v_mfma_f32_16x16x32_bf16 v[96:99], v[176:179], v[140:143], v[96:99]\n\tds_read_b128 v[156:159], v3 offset:4096\n\ts_waitcnt lgkmcnt(7)\n\tv_mfma_f32_16x16x32_bf16 v[108:111], v[176:179], v[144:147], v[108:111]\n\tds_read_b128 v[160:163], v3 offset:6144\n\ts_waitcnt lgkmcnt(6)\n\t"
            "v_mfma_f32_16x16x32_bf16 v[40:43], v[180:183], v[120:123], v[40:43]\n\tds_read_b128 v[164:167], v3 offset:8192\n\tv_mfma_f32_16x16x32_bf16 v[52:55], v[180:183], v[124:127], v[52:55]\n\tds_read_b128 v[168:171], v3 offset:10240\n\tv_mfma_f32_16x16x32_bf16 v[64:67], v[180:183], v[128:131], v[64:67]\n\tds_read_b128 v[172:175], v3 offset:12288\n\t"
            "v_mfma_f32_16x16x32_bf16 v[76:79], v[180:183], v[132:135], v[76:79]\n\tds_read_b64_tr_b16 v[192:193], v12 offset:13312\n\tds_read_b64_tr_b16 v[194:195], v12 offset:13824\n\tv_mfma_f32_16x16x32_bf16 v[88:91], v[180:183], v[136:139], v[88:91]\n\tv_mfma_f32_16x16x32_bf16 v[100:103], v[180:183], v[140:143], v[100:103]\n\tv_mfma_f32_16x16x32_bf16 v[112:115], v[180:183], v[144:147], v[112:115]\n\t"
            "s_waitcnt lgkmcnt(8)\n\tv_mfma_f32_16x16x32_bf16 v[36:39], v[188:191], v[148:151], v[36:39]\n\ts_waitcnt lgkmcnt(7)\n\tv_mfma_f32_16x16x32_bf16 v[48:51], v[188:191], v[152:155], v[48:51]\n\ts_waitcnt lgkmcnt(6)\n\tv_mfma_f32_16x16x32_bf16 v[60:63], v[188:191], v[156:159], v[60:63]\n\t"
            "s_waitcnt lgkmcnt(5)\n\tv_mfma_f32_16x16x32_bf16 v[72:75], v[188:191], v[160:163], v[72:75]\n\ts_waitcnt lgkmcnt(0)\n\ts_barrier\n\t"
            "ds_read_b64_tr_b16 v[176:177], v13 offset:0\n\tds_read_b64_tr_b16 v[178:179], v13 offset:512\n\tv_mfma_f32_16x16x32_bf16 v[84:87], v[188:191], v[164:167], v[84:87]\n\tds_read_b128 v[120:123], v8\n\tv_mfma_f32_16x16x32_bf16 v[96:99], v[188:191], v[168:171], v[96:99]\n\tds_read_b128 v[124:127], v4 offset:2048\n\t"
            "v_mfma_f32_16x16x32_bf16 v[108:111], v[188:191], v[172:175], v[108:111]\n\tds_read_b128 v[128:131], v4 offset:4096\n\tv_mfma_f32_16x16x32_bf16 v[40:43], v[192:195], v[148:151], v[40:43]\n\tds_read_b128 v[132:135], v4 offset:6144\n\tv_mfma_f32_16x16x32_bf16 v[52:55], v[192:195], v[152:155], v[52:55]\n\tds_read_b128 v[136:139], v4 offset:8192\n\t"
            "v_mfma_f32_16x16x32_bf16 v[64:67], v[192:195], v[156:159], v[64:67]\n\tds_read_b128 v[140:143], v4 offset:10240\n\tv_mfma_f32_16x16x32_bf16 v[76:79], v[192:195], v[160:163], v[76:79]\n\tds_read_b128 v[144:147], v4 offset:12288\n\tv_mfma_f32_16x16x32_bf16 v[88:91], v[192:195], v[164:167], v[88:91]\n\tds_read_b64_tr_b16 v[180:181], v13 offset:1024\n\t"
            "ds_read_b64_tr_b16 v[182:183], v13 offset:1536\n\tv_mfma_f32_16x16x32_bf16 v[100:103], v[192:195], v[168:171], v[100:103]\n\tds_read_b64_tr_b16 v[184:185], v13 offset:2048\n\tds_read_b64_tr_b16 v[186:187], v13 offset:2560\n\tv_mfma_f32_16x16x32_bf16 v[112:115], v[192:195], v[172:175], v[112:115]\n\tds_read2_b32 v[208:209], v14 offset1:16\n\t"
            "s_setprio 2\n\t"
            "s_mov_b64 vcc, s[18:19]\n\ts_bitcmp1_b32 9, s69\n\tds_read_b64_tr_b16 v[188:189], v13 offset:12288\n\tv_mov_b32 v252, -1\n\tds_read_b64_tr_b16 v[190:191], v13 offset:12800\n\tv_mov_b32 v254, -1\n\t"
            "ds_read_b128 v[148:151], v9\n\ts_waitcnt lgkmcnt(6)\n\tv_mfma_f32_16x16x32_bf16 v[40:43], v[180:183], v[120:123], v[40:43]\n\tds_read_b128 v[152:155], v5 offset:2048\n\tv_mfma_f32_16x16x32_bf16 v[36:39], v[176:179], v[120:123], v[36:39]\n\tds_read_b128 v[156:159], v5 offset:4096\n\t"
            "v_mfma_f32_16x16x32_bf16 v[48:51], v[176:179], v[124:127], v[48:51]\n\tds_read_b128 v[160:163], v5 offset:6144\n\tv_mfma_f32_16x16x32_bf16 v[60:63], v[176:179], v[128:131], v[60:63]\n\tds_read_b128 v[164:167], v5 offset:8192\n\tv_mfma_f32_16x16x32_bf16 v[52:55], v[180:183], v[124:127], v[52:55]\n\tds_read_b128 v[168:171], v5 offset:10240\n\t"
            "v_mfma_f32_16x16x32_bf16 v[64:67], v[180:183], v[128:131], v[64:67]\n\tds_read_b128 v[172:175], v5 offset:12288\n\tv_mfma_f32_16x16x32_bf16 v[76:79], v[180:183], v[132:135], v[76:79]\n\tds_read_b64_tr_b16 v[192:193], v13 offset:13312\n\ts_waitcnt lgkmcnt(7)\n\tv_mfma_f32_16x16x32_bf16 v[36:39], v[188:191], v[148:151], v[36:39]\n\t"
            "ds_read_b64_tr_b16 v[194:195], v13 offset:13824\n\tds_read_b64_tr_b16 v[196:197], v13 offset:14336\n\ts_waitcnt lgkmcnt(8)\n\tv_mfma_f32_16x16x32_bf16 v[48:51], v[188:191], v[152:155], v[48:51]\n\tds_read_b64_tr_b16 v[198:199], v13 offset:14848\n\tds_read2_b32 v[210:211], v14 offset0:32 offset1:48\n\t"
            "v_mfma_f32_16x16x32_bf16 v[72:75], v[176:179], v[132:135], v[72:75]\n\ts_nop 0\n\tv_mov_b32_dpp v200, v36 row_newbcast:0 row_mask:0xf bank_mask:0xf bound_ctrl:1\n\tds_read2_b32 v[212:213], v14 offset0:64 offset1:80\n\ts_waitcnt lgkmcnt(9)\n\tv_mfma_f32_16x16x32_bf16 v[60:63], v[188:191], v[156:159], v[60:63]\n\t"
            "v_mov_b32_dpp v201, v37 row_newbcast:0 row_mask:0xf bank_mask:0xf bound_ctrl:1\n\tds_read_b32 v214, v14 offset:384\n\tv_mfma_f32_16x16x32_bf16 v[72:75], v[188:191], v[160:163], v[72:75]\n\tv_mov_b32_dpp v202, v38 row_newbcast:0 row_mask:0xf bank_mask:0xf bound_ctrl:1\n\tv_mov_b32_dpp v203, v39 row_newbcast:0 row_mask:0xf bank_mask:0xf bound_ctrl:1\n\tv_fmac_f32 v36, v208, v200\n\t"
            "v_fmac_f32 v37, v208, v201\n\tv_fmac_f32 v38, v208, v202\n\tv_fmac_f32 v39, v208, v203\n\tv_cvt_pk_bf16_f32 v250, v36, v37\n\tv_cvt_pk_bf16_f32 v251, v38, v39\n\tv_and_b32 v36, v250, v18\n\t"
            "v_and_b32 v37, v251, v18\n\ts_waitcnt lgkmcnt(2)\n\tv_mfma_f32_16x16x32_bf16 v[40:43], v[192:195], v[148:151], v[40:43]\n\tv_and_b32 v38, v250, v19\n\tv_and_b32 v39, v251, v19\n\tv_mfma_f32_16x16x32_bf16 v[52:55], v[192:195], v[152:155], v[52:55]\n\t"
            "v_and_b32 v222, v250, v20\n\tv_and_b32 v223, v251, v20\n\tv_mfma_f32_16x16x32_bf16 v[64:67], v[192:195], v[156:159], v[64:67]\n\ts_nop 1\n\tv_mov_b32_dpp v204, v40 row_newbcast:0 row_mask:0xf bank_mask:0xf bound_ctrl:1\n\tv_mov_b32_dpp v205, v41 row_newbcast:0 row_mask:0xf bank_mask:0xf bound_ctrl:1\n\t"
            "v_mfma_f32_16x16x32_bf16 v[76:79], v[192:195], v[160:163], v[76:79]\n\tv_mov_b32_dpp v206, v42 row_newbcast:0 row_mask:0xf bank_mask:0xf bound_ctrl:1\n\tv_mov_b32_dpp v207, v43 row_newbcast:0 row_mask:0xf bank_mask:0xf bound_ctrl:1\n\tv_fmac_f32 v40, v208, v204\n\tv_fmac_f32 v41, v208, v205\n\tv_fmac_f32 v42, v208, v206\n\t"
            "v_fmac_f32 v43, v208, v207\n\tv_cvt_pk_bf16_f32 v40, v40, v41\n\tv_cvt_pk_bf16_f32 v41, v42, v43\n\tv_cndmask_b32 v42, v40, v40, vcc\n\tv_cndmask_b32 v43, v41, v41, vcc\n\tv_and_b32 v224, v250, v21\n\t"
            "v_and_b32 v225, v251, v21\n\tv_mfma_f32_16x16x32_bf16 v[218:221], v[40:43], v[36:39], 0\n\tv_fmac_f32 v48, v209, v200\n\tv_fmac_f32 v49, v209, v201\n\tv_mfma_f32_16x16x32_bf16 v[226:229], v[40:43], v[222:225], 0\n\tv_fmac_f32 v50, v209, v202\n\t"
            "v_fmac_f32 v51, v209, v203\n\tv_cvt_pk_bf16_f32 v250, v48, v49\n\ts_nop 1\n\tv_cndmask_b32 v36, v220, v218, s[14:15]\n\tv_cndmask_b32 v37, v221, v219, s[16:17]\n\tv_cvt_pk_bf16_f32 v251, v50, v51\n\t"
            "v_and_b32 v48, v250, v18\n\tv_cndmask_b32 v38, v228, v226, s[14:15]\n\tv_cndmask_b32 v39, v229, v227, s[16:17]\n\tv_and_b32 v49, v251, v18\n\tv_and_b32 v50, v250, v19\n\tv_and_b32 v51, v251, v19\n\t"
            "v_and_b32 v234, v250, v20\n\tv_and_b32 v235, v251, v20\n\tv_and_b32 v236, v250, v21\n\tv_and_b32 v237, v251, v21\n\tv_fmac_f32 v60, v210, v200\n\tv_fmac_f32 v61, v210, v201\n\t"
            "v_fmac_f32 v62, v210, v202\n\tv_fmac_f32 v63, v210, v203\n\tv_fmac_f32 v52, v209, v204\n\tv_fmac_f32 v53, v209, v205\n\tv_fmac_f32 v64, v210, v204\n\tv_fmac_f32 v65, v210, v205\n\t"
            "v_cvt_pk_bf16_f32 v250, v60, v61\n\tv_cvt_pk_bf16_f32 v251, v62, v63\n\tv_fmac_f32 v76, v211, v204\n\tv_fmac_f32 v77, v211, v205\n\tv_fmac_f32 v54, v209, v206\n\tv_fmac_f32 v55, v209, v207\n\t"
            "v_cvt_pk_bf16_f32 v52, v52, v53\n\tv_fmac_f32 v66, v210, v206\n\tv_fmac_f32 v67, v210, v207\n\tv_cvt_pk_bf16_f32 v64, v64, v65\n\tv_and_b32 v60, v250, v18\n\tv_and_b32 v61, v251, v18\n\t"
            "v_and_b32 v62, v250, v19\n\tv_and_b32 v63, v251, v19\n\tv_and_b32 v246, v250, v20\n\tv_and_b32 v247, v251, v20\n\tv_and_b32 v248, v250, v21\n\tv_and_b32 v249, v251, v21\n\t"
            "v_fmac_f32 v78, v211, v206\n\tv_fmac_f32 v79, v211, v207\n\tv_fmac_f32 v72, v211, v200\n\tv_fmac_f32 v73, v211, v201\n\tv_fmac_f32 v74, v211, v202\n\tv_fmac_f32 v75, v211, v203\n\t"
            "v_cvt_pk_bf16_f32 v76, v76, v77\n\tv_cvt_pk_bf16_f32 v53, v54, v55\n\tv_cvt_pk_bf16_f32 v65, v66, v67\n\tv_cvt_pk_bf16_f32 v77, v78, v79\n\tv_cvt_pk_bf16_f32 v250, v72, v73\n\tv_cvt_pk_bf16_f32 v251, v74, v75\n\t"
            "v_cndmask_b32 v54, v40, v52, vcc\n\tv_cndmask_b32 v55, v41, v53, vcc\n\tv_cndmask_b32 v66, v52, v64, vcc\n\tv_cndmask_b32 v67, v53, v65, vcc\n\tv_mfma_f32_16x16x32_bf16 v[230:233], v[52:55], v[48:51], 0\n\tv_cndmask_b32 v78, v64, v76, vcc\n\t"
            "v_cndmask_b32 v79, v65, v77, vcc\n\tv_mfma_f32_16x16x32_bf16 v[242:245], v[64:67], v[60:63], 0\n\tv_and_b32 v72, v250, v18\n\tv_and_b32 v73, v251, v18\n\tv_mfma_f32_16x16x32_bf16 v[238:241], v[52:55], v[234:237], 0\n\tv_and_b32 v74, v250, v19\n\t"
            "v_and_b32 v75, v251, v19\n\tv_mfma_f32_16x16x32_bf16 v[218:221], v[64:67], v[246:249], 0\n\tv_cndmask_b32 v48, v232, v230, s[14:15]\n\tv_cndmask_b32 v49, v233, v231, s[16:17]\n\tv_mfma_f32_16x16x32_bf16 v[222:225], v[76:79], v[72:75], 0\n\tv_cndmask_b32 v60, v244, v242, s[14:15]\n\t"
            "v_cndmask_b32 v61, v245, v243, s[16:17]\n\tv_mfma_f32_16x16x32_bf16 v[84:87], v[176:179], v[136:139], v[84:87]\n\tv_cndmask_b32 v50, v240, v238, s[14:15]\n\tv_cndmask_b32 v51, v241, v239, s[16:17]\n\tv_mfma_f32_16x16x32_bf16 v[96:99], v[176:179], v[140:143], v[96:99]\n\ts_nop 1\n\t"
            "v_cndmask_b32 v72, v224, v222, s[14:15]\n\tv_cndmask_b32 v73, v225, v223, s[16:17]\n\tv_mfma_f32_16x16x32_bf16 v[88:91], v[180:183], v[136:139], v[88:91]\n\ts_cbranch_scc0 1f\n\tv_cvt_pk_bf16_f32 v253, v36, v37\n\tv_min_u32 v253, -2, v253\n\t"
            "v_cvt_pk_bf16_f32 v255, v48, v49\n\tv_min_u32 v255, -2, v255\n\tbuffer_store_dwordx4 v[252:255], v17, s[44:47], s20 offen sc1\n\ts_nop 1\n\tv_cvt_pk_bf16_f32 v253, v60, v61\n\tv_min_u32 v253, -2, v253\n\t"
            "v_cvt_pk_bf16_f32 v255, v72, v73\n\tv_min_u32 v255, -2, v255\n\tbuffer_store_dwordx4 v[252:255], v17, s[44:47], s21 offen sc1\n\t1:\nv_cndmask_b32 v62, v220, v218, s[14:15]\n\tv_mfma_f32_16x16x32_bf16 v[100:103], v[180:183], v[140:143], v[100:103]\n\t"
            "v_cndmask_b32 v63, v221, v219, s[16:17]\n\tv_and_b32 v226, v250, v20\n\tv_mfma_f32_16x16x32_bf16 v[112:115], v[180:183], v[144:147], v[112:115]\n\tv_and_b32 v227, v251, v20\n\tv_and_b32 v228, v250, v21\n\tv_mfma_f32_16x16x32_bf16 v[84:87], v[188:191], v[164:167], v[84:87]\n\t"
            "v_and_b32 v229, v251, v21\n\tv_mov_b32 v180, 0\n\tv_mfma_f32_16x16x32_bf16 v[108:111], v[176:179], v[144:147], v[108:111]\n\tv_mov_b32 v176, 0\n\tds_write_b64 v28, v[36:37] offset:0\n\tv_mfma_f32_16x16x32_bf16 v[230:233], v[76:79], v[226:229], 0\n\t"
            "s_waitcnt lgkmcnt(1)\n\ts_nop 0\n\tv_fmac_f32 v84, v212, v200\n\tv_fmac_f32 v85, v212, v201\n\tv_mfma_f32_16x16x32_bf16 v[96:99], v[188:191], v[168:171], v[96:99]\n\tds_write_b64 v29, v[38:39] offset:0\n\t"
            "v_fmac_f32 v86, v212, v202\n\tv_mfma_f32_16x16x32_bf16 v[88:91], v[192:195], v[164:167], v[88:91]\n\tv_cndmask_b32 v74, v232, v230, s[14:15]\n\tv_cndmask_b32 v75, v233, v231, s[16:17]\n\tds_write_b64 v28, v[48:49] offset:512\n\tv_mfma_f32_16x16x32_bf16 v[100:103], v[192:195], v[168:171], v[100:103]\n\t"
            "v_fmac_f32 v87, v212, v203\n\tv_cvt_pk_bf16_f32 v250, v84, v85\n\tv_mfma_f32_16x16x32_bf16 v[112:115], v[192:195], v[172:175], v[112:115]\n\tds_write_b64 v29, v[50:51] offset:512\n\tv_cvt_pk_bf16_f32 v251, v86, v87\n\tv_mfma_f32_16x16x32_bf16 v[108:111], v[188:191], v[172:175], v[108:111]\n\t"
            "v_and_b32 v84, v250, v18\n\tv_and_b32 v85, v251, v18\n\tds_write_b64 v28, v[60:61] offset:1024\n\tv_and_b32 v86, v250, v19\n\tv_and_b32 v87, v251, v19\n\tv_and_b32 v238, v250, v20\n\t"
            "v_and_b32 v239, v251, v20\n\tv_and_b32 v240, v250, v21\n\tds_write_b64 v29, v[62:63] offset:1024\n\tv_and_b32 v241, v251, v21\n\tv_fmac_f32 v96, v213, v200\n\tv_fmac_f32 v97, v213, v201\n\t"
            "v_fmac_f32 v98, v213, v202\n\tv_fmac_f32 v99, v213, v203\n\tds_write_b64 v28, v[72:73] offset:1536\n\tv_fmac_f32 v88, v212, v204\n\tv_fmac_f32 v89, v212, v205\n\tv_fmac_f32 v100, v213, v204\n\t"
            "v_fmac_f32 v101, v213, v205\n\tv_cvt_pk_bf16_f32 v250, v96, v97\n\tds_write_b64 v29, v[74:75] offset:1536\n\tv_cvt_pk_bf16_f32 v251, v98, v99\n\tv_fmac_f32 v112, v214, v204\n\tv_fmac_f32 v113, v214, v205\n\t"
            "v_fmac_f32 v90, v212, v206\n\tv_fmac_f32 v91, v212, v207\n\tds_read_b128 v[36:39], v15 offset:0\n\tv_cvt_pk_bf16_f32 v88, v88, v89\n\tv_fmac_f32 v102, v213, v206\n\tv_fmac_f32 v103, v213, v207\n\t"
            "ds_read_b128 v[48:51], v15 offset:64\n\tv_cvt_pk_bf16_f32 v100, v100, v101\n\ts_waitcnt lgkmcnt(0)\n\ts_barrier\n\tv_and_b32 v96, v250, v18\n\tds_read_b128 v[60:63], v15 offset:128\n\t"
            "v_and_b32 v97, v251, v18\n\tv_and_b32 v98, v250, v19\n\tv_and_b32 v99, v251, v19\n\tds_read_b128 v[72:75], v15 offset:192\n\tv_and_b32 v218, v250, v20\n\tv_and_b32 v219, v251, v20\n\t"
            "v_and_b32 v220, v250, v21\n\tv_and_b32 v221, v251, v21\n\tv_fmac_f32 v114, v214, v206\n\tv_fmac_f32 v115, v214, v207\n\tv_fmac_f32 v108, v214, v200\n\tv_fmac_f32 v109, v214, v201\n\t"
            "v_fmac_f32 v110, v214, v202\n\tv_fmac_f32 v111, v214, v203\n\tv_cvt_pk_bf16_f32 v112, v112, v113\n\tv_cvt_pk_bf16_f32 v89, v90, v91\n\tv_cvt_pk_bf16_f32 v101, v102, v103\n\tv_cvt_pk_bf16_f32 v113, v114, v115\n\t"
            "v_cvt_pk_bf16_f32 v250, v108, v109\n\tv_cvt_pk_bf16_f32 v251, v110, v111\n\tv_cndmask_b32 v90, v76, v88, vcc\n\tv_cndmask_b32 v91, v77, v89, vcc\n\tv_cndmask_b32 v102, v88, v100, vcc\n\tv_cndmask_b32 v103, v89, v101, vcc\n\t"
            "v_mfma_f32_16x16x32_bf16 v[234:237], v[88:91], v[84:87], 0\n\tv_cndmask_b32 v114, v100, v112, vcc\n\tv_cndmask_b32 v115, v101, v113, vcc\n\tv_mfma_f32_16x16x32_bf16 v[246:249], v[100:103], v[96:99], 0\n\tv_and_b32 v108, v250, v18\n\tv_and_b32 v109, v251, v18\n\t"
            "v_mfma_f32_16x16x32_bf16 v[242:245], v[88:91], v[238:241], 0\n\tv_and_b32 v110, v250, v19\n\tv_and_b32 v111, v251, v19\n\tv_mfma_f32_16x16x32_bf16 v[222:225], v[100:103], v[218:221], 0\n\tv_cndmask_b32 v84, v236, v234, s[14:15]\n\tv_cndmask_b32 v85, v237, v235, s[16:17]\n\t"
            "ds_write_b64 v28, v[84:85] offset:38912\n\tv_mfma_f32_16x16x32_bf16 v[226:229], v[112:115], v[108:111], 0\n\tv_cndmask_b32 v96, v248, v246, s[14:15]\n\tv_cndmask_b32 v97, v249, v247, s[16:17]\n\tv_cndmask_b32 v86, v244, v242, s[14:15]\n\tds_write_b64 v28, v[96:97] offset:39424\n\t"
            "v_cndmask_b32 v87, v245, v243, s[16:17]\n\tv_cndmask_b32 v98, v224, v222, s[14:15]\n\tv_cndmask_b32 v99, v225, v223, s[16:17]\n\ts_nop 0\n\tv_cndmask_b32 v108, v228, v226, s[14:15]\n\tv_cndmask_b32 v109, v229, v227, s[16:17]\n\t"
            "ds_write_b64 v29, v[86:87] offset:38912\n\ts_cbranch_scc0 1f\n\tv_cvt_pk_bf16_f32 v253, v84, v85\n\tv_min_u32 v253, -2, v253\n\tv_cvt_pk_bf16_f32 v255, v96, v97\n\tv_min_u32 v255, -2, v255\n\t"
            "buffer_store_dwordx4 v[252:255], v17, s[44:47], s22 offen sc1\n\ts_nop 1\n\tv_cvt_pk_bf16_f32 v253, v108, v109\n\tv_min_u32 v253, -2, v253\n\tbuffer_store_dwordx4 v[252:255], v17, s[44:47], s23 offen sc1\n\t1:\n"
            "v_and_b32 v230, v250, v20\n\tv_and_b32 v231, v251, v20\n\tv_and_b32 v232, v250, v21\n\tv_and_b32 v233, v251, v21\n\tds_write_b64 v29, v[98:99] offset:39424\n\tv_mfma_f32_16x16x32_bf16 v[44:47], v[120:123], v[184:187], v[44:47]\n\t"
            "v_mov_b32 v218, 0\n\tv_mov_b32 v219, 0\n\tv_mfma_f32_16x16x32_bf16 v[234:237], v[112:115], v[230:233], 0\n\tds_write_b64 v28, v[108:109] offset:39936\n\tv_mov_b32 v188, 0\n\tv_mfma_f32_16x16x32_bf16 v[56:59], v[124:127], v[184:187], v[56:59]\n\t"
            "v_mov_b32 v192, 0\n\tds_read_b128 v[120:123], v6\n\ts_nop 2\n\tv_cndmask_b32 v110, v236, v234, s[14:15]\n\tv_cndmask_b32 v111, v237, v235, s[16:17]\n\tv_mfma_f32_16x16x32_bf16 v[68:71], v[128:131], v[184:187], v[68:71]\n\t"
            "ds_write_b64 v29, v[110:111] offset:39936\n\tv_mov_b32 v234, 0\n\tv_mfma_f32_16x16x32_bf16 v[80:83], v[132:135], v[184:187], v[80:83]\n\tv_mov_b32 v235, 0\n\tds_write_b32 v33, v33\n\t"
            "v_mfma_f32_16x16x32_bf16 v[92:95], v[136:139], v[184:187], v[92:95]\n\tds_read_b128 v[124:127], v2 offset:2048\n\tv_mfma_f32_16x16x32_bf16 v[104:107], v[140:143], v[184:187], v[104:107]\n\tds_read_b128 v[128:131], v2 offset:4096\n\tv_mfma_f32_16x16x32_bf16 v[116:119], v[144:147], v[184:187], v[116:119]\n\tds_read_b64_tr_b16 v[184:185], v12 offset:2048\n\t"
            "v_mfma_f32_16x16x32_bf16 v[44:47], v[148:151], v[196:199], v[44:47]\n\tds_read_b64_tr_b16 v[186:187], v12 offset:2560\n\tds_read_b128 v[148:151], v7\n\tv_mfma_f32_16x16x32_bf16 v[56:59], v[152:155], v[196:199], v[56:59]\n\tds_read_b128 v[152:155], v3 offset:2048\n\tv_mfma_f32_16x16x32_bf16 v[68:71], v[156:159], v[196:199], v[68:71]\n\t"
            "ds_read_b128 v[132:135], v2 offset:6144\n\tv_mfma_f32_16x16x32_bf16 v[80:83], v[160:163], v[196:199], v[80:83]\n\tds_read_b128 v[156:159], v3 offset:4096\n\tv_mfma_f32_16x16x32_bf16 v[92:95], v[164:167], v[196:199], v[92:95]\n\tds_read_b128 v[160:163], v3 offset:6144\n\tv_mfma_f32_16x16x32_bf16 v[104:107], v[168:171], v[196:199], v[104:107]\n\t"
            "ds_read_b128 v[136:139], v2 offset:8192\n\tv_mfma_f32_16x16x32_bf16 v[116:119], v[172:175], v[196:199], v[116:119]\n\tds_read_b64_tr_b16 v[196:197], v12 offset:14336\n\tds_read_b64_tr_b16 v[198:199], v12 offset:14848\n\ts_waitcnt lgkmcnt(8)\n\tv_mfma_f32_16x16x32_bf16 v[44:47], v[120:123], v[184:187], v[44:47]\n\t"
            "ds_read_b128 v[140:143], v2 offset:10240\n\tv_mfma_f32_16x16x32_bf16 v[56:59], v[124:127], v[184:187], v[56:59]\n\tds_read_b128 v[144:147], v2 offset:12288\n\tv_mfma_f32_16x16x32_bf16 v[68:71], v[128:131], v[184:187], v[68:71]\n\tds_read_b128 v[164:167], v3 offset:8192\n\ts_waitcnt lgkmcnt(8)\n\t"
            "v_mfma_f32_16x16x32_bf16 v[80:83], v[132:135], v[184:187], v[80:83]\n\tds_read_b128 v[168:171], v3 offset:10240\n\tds_read_b128 v[172:175], v3 offset:12288\n\ts_waitcnt lgkmcnt(7)\n\tv_mfma_f32_16x16x32_bf16 v[92:95], v[136:139], v[184:187], v[92:95]\n\tds_read_b128 v[84:87], v15 offset:256\n\t"
            "s_waitcnt lgkmcnt(6)\n\tv_mfma_f32_16x16x32_bf16 v[44:47], v[148:151], v[196:199], v[44:47]\n\tds_read_b128 v[96:99], v15 offset:320\n\tv_mfma_f32_16x16x32_bf16 v[56:59], v[152:155], v[196:199], v[56:59]\n\tds_read_b128 v[108:111], v15 offset:384\n\tv_mfma_f32_16x16x32_bf16 v[68:71], v[156:159], v[196:199], v[68:71]\n\t"
            "s_nop 3\n\tv_mov_b32 v216, v44\n\tv_mov_b32 v217, v44\n\tv_mfma_f32_16x16x32_bf16 v[80:83], v[160:163], v[196:199], v[80:83]\n\ts_nop 0\n\tv_permlane32_swap_b32 v216, v217\n\t"
            "v_mov_b32 v217, v216\n\ts_waitcnt lgkmcnt(5)\n\tv_mfma_f32_16x16x32_bf16 v[104:107], v[140:143], v[184:187], v[104:107]\n\tv_permlane16_swap_b32 v216, v217\n\tv_fmac_f32 v44, v36, v216\n\tv_mfma_f32_16x16x32_bf16 v[116:119], v[144:147], v[184:187], v[116:119]\n\t"
            "v_fmac_f32 v45, v37, v216\n\tv_fmac_f32 v46, v38, v216\n\tv_fmac_f32 v47, v39, v216\n\tv_cvt_pk_bf16_f32 v44, v44, v45\n\tv_cvt_pk_bf16_f32 v45, v46, v47\n\tv_mfma_f32_16x16x32_bf16 v[92:95], v[164:167], v[196:199], v[92:95]\n\t"
            "v_and_b32 v220, v44, v22\n\tv_and_b32 v221, v45, v22\n\ts_waitcnt lgkmcnt(0)\n\tv_mfma_f32_16x16x32_bf16 v[104:107], v[168:171], v[196:199], v[104:107]\n\tv_and_b32 v236, v44, v23\n\tv_and_b32 v237, v45, v23\n\t"
            "v_mfma_f32_16x16x32_bf16 v[116:119], v[172:175], v[196:199], v[116:119]\n\tv_fmac_f32 v56, v48, v216\n\tv_fmac_f32 v57, v49, v216\n\tv_fmac_f32 v58, v50, v216\n\tv_fmac_f32 v59, v51, v216\n\tv_cvt_pk_bf16_f32 v56, v56, v57\n\t"
            "v_cvt_pk_bf16_f32 v57, v58, v59\n\tv_and_b32 v222, v56, v22\n\tv_and_b32 v223, v57, v22\n\tv_and_b32 v238, v56, v23\n\tv_and_b32 v239, v57, v23\n\tv_fmac_f32 v68, v60, v216\n\t"
            "v_fmac_f32 v69, v61, v216\n\tv_fmac_f32 v70, v62, v216\n\tv_fmac_f32 v71, v63, v216\n\tds_read_b32 v27, v32\n\tds_read2st64_b32 v[42:43], v31 offset0:0 offset1:1\n\tds_read2st64_b32 v[40:41], v30 offset0:0 offset1:1\n\t"
            "ds_read2st64_b32 v[66:67], v31 offset0:2 offset1:3\n\tds_read2st64_b32 v[64:65], v30 offset0:2 offset1:3\n\tv_cvt_pk_bf16_f32 v68, v68, v69\n\tv_cvt_pk_bf16_f32 v69, v70, v71\n\tv_and_b32 v224, v68, v22\n\tv_and_b32 v225, v69, v22\n\t"
            "v_and_b32 v240, v68, v23\n\tv_and_b32 v241, v69, v23\n\tv_fmac_f32 v80, v72, v216\n\tv_fmac_f32 v81, v73, v216\n\tv_fmac_f32 v82, v74, v216\n\tv_fmac_f32 v83, v75, v216\n\t"
            "v_cvt_pk_bf16_f32 v80, v80, v81\n\tv_cvt_pk_bf16_f32 v81, v82, v83\n\tv_and_b32 v226, v80, v22\n\tv_and_b32 v227, v81, v22\n\tv_and_b32 v242, v80, v23\n\tv_and_b32 v243, v81, v23\n\t"
            "s_waitcnt lgkmcnt(1)\n\tv_cmp_ge_u32 vcc, 0, v27\n\ts_cbranch_vccnz 10f\n\t20:\nv_and_b32 v177, v42, v24\n\tv_and_b32 v178, v42, v25\n\t"
            "v_and_b32 v179, v42, v26\n\tv_and_b32 v189, v43, v24\n\tv_and_b32 v190, v43, v25\n\tv_and_b32 v191, v43, v26\n\tv_mfma_f32_16x16x32_bf16 v[44:47], v[234:237], v[176:179], 0\n\tv_and_b32 v181, v40, v24\n\t"
            "v_and_b32 v182, v40, v25\n\tv_mfma_f32_16x16x32_bf16 v[56:59], v[236:239], v[188:191], 0\n\tv_and_b32 v183, v40, v26\n\tv_and_b32 v193, v41, v24\n\tv_and_b32 v194, v41, v25\n\tv_and_b32 v195, v41, v26\n\t"
            "v_mfma_f32_16x16x32_bf16 v[44:47], v[218:221], v[180:183], v[44:47]\n\tv_and_b32 v177, v66, v24\n\tv_and_b32 v178, v66, v25\n\tv_mfma_f32_16x16x32_bf16 v[56:59], v[220:223], v[192:195], v[56:59]\n\tv_and_b32 v179, v66, v26\n\tv_and_b32 v189, v67, v24\n\t"
            "v_and_b32 v190, v67, v25\n\ts_nop 1\n\tv_cvt_pk_bf16_f32 v36, v44, v45\n\tv_cvt_pk_bf16_f32 v37, v46, v47\n\tv_min_u32 v36, -2, v36\n\tv_and_b32 v191, v67, v26\n\t"
            "v_cvt_pk_bf16_f32 v38, v56, v57\n\tv_cvt_pk_bf16_f32 v39, v58, v59\n\tv_min_u32 v38, -2, v38\n\tv_mfma_f32_16x16x32_bf16 v[68:71], v[238:241], v[176:179], 0\n\ts_nop 0\n\tv_permlane16_swap_b32 v36, v38\n\t"
            "v_permlane16_swap_b32 v37, v39\n\tbuffer_store_dwordx4 v[36:39], v16, s[44:47], s20 offen sc1\n\tv_mfma_f32_16x16x32_bf16 v[80:83], v[240:243], v[188:191], 0\n\ts_waitcnt lgkmcnt(0)\n\tv_and_b32 v181, v64, v24\n\tv_and_b32 v182, v64, v25\n\t"
            "v_and_b32 v183, v64, v26\n\tv_and_b32 v193, v65, v24\n\tv_and_b32 v194, v65, v25\n\tv_and_b32 v195, v65, v26\n\tv_mfma_f32_16x16x32_bf16 v[68:71], v[222:225], v[180:183], v[68:71]\n\tv_fmac_f32 v92, v84, v216\n\t"
            "v_fmac_f32 v93, v85, v216\n\tv_mfma_f32_16x16x32_bf16 v[80:83], v[224:227], v[192:195], v[80:83]\n\tv_fmac_f32 v94, v86, v216\n\tv_fmac_f32 v95, v87, v216\n\tv_cvt_pk_bf16_f32 v92, v92, v93\n\ts_nop 1\n\t"
            "v_cvt_pk_bf16_f32 v60, v68, v69\n\tv_cvt_pk_bf16_f32 v61, v70, v71\n\tv_min_u32 v60, -2, v60\n\tv_fmac_f32 v104, v96, v216\n\tv_cvt_pk_bf16_f32 v62, v80, v81\n\tv_cvt_pk_bf16_f32 v63, v82, v83\n\t"
            "v_min_u32 v62, -2, v62\n\tv_fmac_f32 v105, v97, v216\n\tv_permlane16_swap_b32 v61, v63\n\tv_permlane16_swap_b32 v60, v62\n\tbuffer_store_dwordx4 v[60:63], v16, s[44:47], s21 offen sc1\n\tds_write_b32 v34, v35\n\t"
            "v_cvt_pk_bf16_f32 v93, v94, v95\n\tv_fmac_f32 v106, v98, v216\n\tv_fmac_f32 v107, v99, v216\n\tv_cvt_pk_bf16_f32 v104, v104, v105\n\tv_fmac_f32 v116, v108, v216\n\tv_fmac_f32 v117, v109, v216\n\t"
            "v_and_b32 v228, v92, v22\n\tds_read_b32 v27, v32\n\tds_read2st64_b32 v[102:103], v31 offset0:152 offset1:153\n\tds_read2st64_b32 v[100:101], v30 offset0:152 offset1:153\n\tds_read2st64_b32 v[78:79], v31 offset0:154 offset1:155\n\tds_read2st64_b32 v[76:77], v30 offset0:154 offset1:155\n\t"
            "v_and_b32 v229, v93, v22\n\tv_and_b32 v244, v92, v23\n\tv_and_b32 v245, v93, v23\n\tv_cvt_pk_bf16_f32 v105, v106, v107\n\tv_fmac_f32 v118, v110, v216\n\tv_fmac_f32 v119, v111, v216\n\t"
            "v_cvt_pk_bf16_f32 v116, v116, v117\n\tv_and_b32 v230, v104, v22\n\tv_and_b32 v231, v105, v22\n\tv_and_b32 v246, v104, v23\n\tv_and_b32 v247, v105, v23\n\tv_cvt_pk_bf16_f32 v117, v118, v119\n\t"
            "v_and_b32 v232, v116, v22\n\tv_and_b32 v233, v117, v22\n\tv_and_b32 v248, v116, v23\n\tv_and_b32 v249, v117, v23\n\ts_waitcnt lgkmcnt(3)\n\tv_cmp_ge_u32 vcc, 1, v27\n\t"
            "s_cbranch_vccnz 11f\n\t21:\nv_and_b32 v177, v102, v24\n\tv_and_b32 v178, v102, v25\n\tv_and_b32 v179, v102, v26\n\tv_and_b32 v189, v103, v24\n\t"
            "v_and_b32 v190, v103, v25\n\tv_and_b32 v191, v103, v26\n\tv_mfma_f32_16x16x32_bf16 v[92:95], v[242:245], v[176:179], 0\n\ts_waitcnt lgkmcnt(0)\n\tv_and_b32 v181, v100, v24\n\tv_and_b32 v182, v100, v25\n\t"
            "v_mfma_f32_16x16x32_bf16 v[104:107], v[244:247], v[188:191], 0\n\tv_and_b32 v183, v100, v26\n\tv_and_b32 v193, v101, v24\n\tv_and_b32 v194, v101, v25\n\tv_and_b32 v195, v101, v26\n\tv_mfma_f32_16x16x32_bf16 v[92:95], v[226:229], v[180:183], v[92:95]\n\t"
            "v_and_b32 v177, v78, v24\n\tv_and_b32 v178, v78, v25\n\tv_mfma_f32_16x16x32_bf16 v[104:107], v[228:231], v[192:195], v[104:107]\n\tv_and_b32 v179, v78, v26\n\tv_and_b32 v181, v76, v24\n\tv_and_b32 v182, v76, v25\n\t"
            "v_mfma_f32_16x16x32_bf16 v[116:119], v[246:249], v[176:179], 0\n\tv_and_b32 v183, v76, v26\n\tv_cvt_pk_bf16_f32 v84, v92, v93\n\ts_nop 1\n\tv_cvt_pk_bf16_f32 v86, v104, v105\n\tv_mfma_f32_16x16x32_bf16 v[116:119], v[230:233], v[180:183], v[116:119]\n\t"
            "v_cvt_pk_bf16_f32 v85, v94, v95\n\tv_min_u32 v84, -2, v84\n\tv_cvt_pk_bf16_f32 v87, v106, v107\n\tv_min_u32 v86, -2, v86\n\ts_nop 0\n\tv_permlane16_swap_b32 v85, v87\n\t"
            "v_permlane16_swap_b32 v84, v86\n\tbuffer_store_dwordx4 v[84:87], v16, s[44:47], s22 offen sc1\n\tv_cvt_pk_bf16_f32 v108, v116, v117\n\tv_cvt_pk_bf16_f32 v109, v118, v119\n\tv_min_u32 v108, -2, v108\n\ts_nop 0\n\t"
            "v_permlane16_swap_b32 v109, v111\n\tv_permlane16_swap_b32 v108, v110\n\tbuffer_store_dwordx4 v[108:111], v215, s[44:47], s23 offen sc1\n\tds_write_b32 v34, v34\n\t"
            "s_setprio 0\n\t"
            "s_cmp_eq_u32 s69, 1\n\ts_cbranch_scc1 .Lout\n\ts_endpgm\n\t10:\nds_read_b32 v27, v32\n\tds_read2st64_b32 v[42:43], v31 offset0:0 offset1:1\n\t"
            "ds_read2st64_b32 v[40:41], v30 offset0:0 offset1:1\n\tds_read2st64_b32 v[66:67], v31 offset0:2 offset1:3\n\tds_read2st64_b32 v[64:65], v30 offset0:2 offset1:3\n\ts_waitcnt lgkmcnt(0)\n\tv_cmp_ge_u32 vcc, 0, v27\n\ts_cbranch_vccz 20b\n\t"
            "s_sleep 1\n\t"
            "s_sub_u32 s68, s68, 1\n\ts_cbranch_scc0 10b\n\ts_branch 20b\n\t11:\nds_read_b32 v27, v32\n\tds_read2st64_b32 v[102:103], v31 offset0:152 offset1:153\n\t"
            "ds_read2st64_b32 v[100:101], v30 offset0:152 offset1:153\n\tds_read2st64_b32 v[78:79], v31 offset0:154 offset1:155\n\tds_read2st64_b32 v[76:77], v30 offset0:154 offset1:155\n\ts_waitcnt lgkmcnt(0)\n\tv_cmp_ge_u32 vcc, 1, v27\n\ts_cbranch_vccz 21b\n\t"
            "s_sleep 1\n\t"
            "s_sub_u32 s68, s68, 1\n\ts_cbranch_scc0 11b\n\ts_branch 21b\n\t"
            ".LB:\ns_and_b32 s72, s56, 1\n\ts_lshr_b32 s73, s56, 1\n\tv_and_b32 v156, 2, v51\n\tv_lshlrev_b32 v156, 1, v156\n\tv_and_b32 v157, 7, v52\n\t"
            "v_xor_b32 v157, v156, v157\n\tv_mul_u32_u24 v156, 0x500, v51\n\tv_lshl_add_u32 v156, v157, 4, v156\n\ts_mul_i32 s74, s72, 0x2800\n\ts_mul_i32 s75, s73, 0xf000\n\ts_add_i32 s75, s75, s74\n\t"
            "v_add_u32 v154, s75, v156\n\ts_mul_i32 s75, s73, 0x5000\n\ts_add_i32 s75, s75, s74\n\ts_addk_i32 s75, 0x5000\n\tv_add_u32 v156, s75, v156\n\ts_cmp_lg_u32 s63, 0\n\t"
            "s_cselect_b32 s74, 81920, 0\n\ts_max_u32 s74, s74, 65536\n\tv_add_u32 v158, s62, v52\n\tv_min_u32 v159, 47, v52\n\ts_add_i32 s75, s62, 64\n\tv_add_u32 v159, s75, v159\n\t"
            "v_max_i32 v158, 0, v158\n\tv_min_i32 v159, 0x3ff, v159\n\tv_lshl_add_u32 v158, v158, 4, s74\n\tv_lshl_add_u32 v159, v159, 4, s74\n\tv_lshl_add_u32 v59, v53, 4, v57\n\ts_cmp_lg_u32 s73, 0\n\t"
            "s_cselect_b64 vcc, -1, 0\n\tv_cndmask_b32 v97, v59, v52, vcc\n\ts_lshl_b32 s75, s72, 6\n\ts_add_i32 s75, s75, s62\n\tv_add_u32 v97, s75, v97\n\tv_mov_b32 v60, s62\n\t"
            "v_add_u32 v60, 0x6f, v60\n\tv_min_i32 v60, 0x3ff, v60\n\tv_med3_i32 v97, v97, 0, v60\n\ts_lshl3_add_u32 s74, s73, s74\n\tv_lshl_add_u32 v97, v97, 4, s74\n\ts_mul_i32 s30, s73, 576\n\t"
            "s_lshl_b32 s75, s72, 8\n\ts_add_i32 s30, s30, s75\n\ts_add_i32 s30, s30, 160000\n\ts_lshl_b32 s74, s59, 7\n\ts_add_i32 s74, s74, 8192\n\tv_min_u32 v96, 9, v52\n\t"
            "v_lshl_add_u32 v96, v96, 2, s74\n\ts_lshr_b32 s75, s58, 5\n\ts_add_i32 s74, s74, s75\n\ts_cmp_eq_u32 s56, 3\n\ts_cselect_b32 s74, s74, 0x40000000\n\tv_lshl_add_u32 v98, v52, 24, s74\n\t"
            "s_cselect_b32 s75, 0, 0x40000000\n\tv_add_u32 v96, s75, v96\n\ts_cselect_b32 s75, 1, 0\n\ts_lshl2_add_u32 s74, s59, s56\n\ts_mul_i32 s74, s74, 0xa000\n\ts_add_i32 s74, s74, 155648\n\t"
            "v_lshl_add_u32 v101, v52, 4, s74\n\tv_and_b32 v60, s75, v53\n\tv_lshl_add_u32 v2, v60, 30, v101\n\ts_xor_b32 s82, s56, 0\n\ts_cmp_ge_u32 s82, s57\n\ts_addc_u32 s76, s82, 0\n\t"
            "s_and_b32 s16, s76, 3\n\ts_lshl_b32 s77, s76, 1\n\ts_max_u32 s78, s77, 1\n\ts_sub_u32 s78, s78, 1\n\ts_min_u32 s79, s77, 7\n\tv_lshlrev_b32 v60, 9, v53\n\t"
            "v_lshl_add_u32 v60, v54, 3, v60\n\ts_mul_i32 s80, s78, 0xc00\n\ts_mul_i32 s81, s79, 0xc00\n\ts_addk_i32 s80, 14336\n\ts_addk_i32 s81, 14336\n\tv_add_u32 v24, s80, v60\n\t"
            "v_add_u32 v26, s81, v60\n\tv_add_u32 v25, 38912, v24\n\tv_add_u32 v27, 38912, v26\n\tv_lshl_add_u32 v29, v52, 4, s80\n\ts_lshr_b32 s78, s78, 1\n\ts_lshr_b32 s79, s79, 1\n\t"
            "s_sub_u32 s79, s79, s78\n\ts_lshl2_add_u32 s78, s78, 159744\n\tv_and_b32 v28, s79, v52\n\tv_lshl_add_u32 v28, v28, 2, s78\n\ts_lshl2_add_u32 s78, s82, 159744\n\tv_mov_b32 v30, s78\n\t"
            "s_mov_b32 s78, 160576\n\tv_lshl_add_u32 v153, v59, 2, s78\n\ts_mul_i32 s79, s59, 163840\n\ts_lshl_b32 s80, s58, 5\n\ts_add_i32 s79, s79, s80\n\ts_cmp_eq_u32 s76, 0\n\t"
            "s_cselect_b32 s80, 0, 0xc00\n\ts_add_i32 s79, s79, s80\n\ts_add_i32 s79, s79, 155652\n\tv_lshl_add_u32 v31, v54, 4, s79\n\tv_lshrrev_b32 v60, 1, v53\n\tv_mul_u32_u24 v60, 0xa000, v60\n\t"
            "v_add_u32 v31, v60, v31\n\tv_and_b32 v60, 1, v53\n\tv_lshl_add_u32 v31, v60, 3, v31\n\ts_mov_b32 s31, 0x14000\n\ts_cmp_lt_i32 s62, 0\n\ts_cselect_b32 s79, 8, -16\n\t"
            "v_mov_b32 v61, 0xff800000\n\tv_mov_b32 v142, 1\n\tv_add_u32 v60, 0, v59\n\tv_and_b32 v60, -8, v60\n\tv_add_u32 v60, 0, v60\n\tv_cmp_gt_i32 vcc, s79, v60\n\t"
            "v_lshl_add_u32 v160, v60, 2, s78\n\ts_nop 0\n\tv_cndmask_b32 v128, 0, v61, vcc\n\tv_add_u32 v60, 4, v59\n\tv_and_b32 v60, -8, v60\n\tv_add_u32 v60, -4, v60\n\t"
            "v_cmp_gt_i32 vcc, s79, v60\n\tv_lshl_add_u32 v161, v60, 2, s78\n\ts_nop 0\n\tv_cndmask_b32 v129, 0, v61, vcc\n\tv_add_u32 v60, -1, v59\n\tv_and_b32 v60, -8, v60\n\t"
            "v_add_u32 v60, 1, v60\n\tv_cmp_gt_i32 vcc, s79, v60\n\tv_lshl_add_u32 v162, v60, 2, s78\n\ts_nop 0\n\tv_cndmask_b32 v130, 0, v61, vcc\n\tv_add_u32 v60, 3, v59\n\t"
            "v_and_b32 v60, -8, v60\n\tv_add_u32 v60, -3, v60\n\tv_cmp_gt_i32 vcc, s79, v60\n\tv_lshl_add_u32 v163, v60, 2, s78\n\ts_nop 0\n\tv_cndmask_b32 v131, 0, v61, vcc\n\t"
            "v_add_u32 v60, -2, v59\n\tv_and_b32 v60, -8, v60\n\tv_add_u32 v60, 2, v60\n\tv_cmp_gt_i32 vcc, s79, v60\n\tv_lshl_add_u32 v164, v60, 2, s78\n\ts_nop 0\n\t"
            "v_cndmask_b32 v132, 0, v61, vcc\n\tv_add_u32 v60, 2, v59\n\tv_and_b32 v60, -8, v60\n\tv_add_u32 v60, -2, v60\n\tv_cmp_gt_i32 vcc, s79, v60\n\tv_lshl_add_u32 v165, v60, 2, s78\n\t"
            "s_nop 0\n\tv_cndmask_b32 v133, 0, v61, vcc\n\tv_add_u32 v60, -3, v59\n\tv_and_b32 v60, -8, v60\n\tv_add_u32 v60, 3, v60\n\tv_cmp_gt_i32 vcc, s79, v60\n\t"
            "v_lshl_add_u32 v166, v60, 2, s78\n\ts_nop 0\n\tv_cndmask_b32 v134, 0, v61, vcc\n\tv_add_u32 v60, 1, v59\n\tv_and_b32 v60, -8, v60\n\tv_add_u32 v60, -1, v60\n\t"
            "v_cmp_gt_i32 vcc, s79, v60\n\tv_lshl_add_u32 v167, v60, 2, s78\n\ts_nop 0\n\tv_cndmask_b32 v135, 0, v61, vcc\n\ts_lshl2_add_u32 s80, s71, s56\n\ts_lshl_b32 s80, s80, 5\n\t"
            "v_add_u32 v100, s80, v52\n\tv_mul_u32_u24 v100, 0x500, v100\n\ts_add_i32 s80, s58, 3072\n\tv_add_u32 v100, s80, v100\n\tv_min_u32 v104, 51, v52\n\tv_lshlrev_b32 v104, 4, v104\n\t"
            "v_add_u32 v104, 0x1000, v104\n\ts_lshl_b32 s4, s64, 12\n\ts_add_u32 s5, s4, 0x1000\n\ts_cmpk_eq_u32 s5, 0x5000\n\ts_cselect_b32 s5, 0, s5\n\ts_add_u32 s6, s5, 0x1000\n\t"
            "s_cmpk_eq_u32 s6, 0x5000\n\ts_cselect_b32 s6, 0, s6\n\ts_add_u32 s7, s6, 0x1000\n\ts_cmpk_eq_u32 s7, 0x5000\n\ts_cselect_b32 s7, 0, s7\n\ts_add_u32 s8, s7, 0x1000\n\t"
            "s_cmpk_eq_u32 s8, 0x5000\n\ts_cselect_b32 s8, 0, s8\n\ts_cmpk_lt_u32 s58, 0x280\n\ts_cselect_b32 s80, 0, 0x5000\n\ts_xor_b32 s81, s80, 0x5000\n\ts_add_u32 s9, s4, s81\n\t"
            "s_add_u32 s4, s4, s80\n\ts_add_u32 s10, s5, s81\n\ts_add_u32 s5, s5, s80\n\ts_add_u32 s11, s6, s81\n\ts_add_u32 s6, s6, s80\n\ts_add_u32 s12, s7, s81\n\t"
            "s_add_u32 s7, s7, s80\n\ts_add_u32 s13, s8, s81\n\ts_add_u32 s8, s8, s80\n\ts_mul_i32 s20, s4, 20\n\ts_add_i32 s20, s20, s58\n\ts_addk_i32 s20, 3072\n\t"
            "s_mul_i32 s21, s5, 20\n\ts_add_i32 s21, s21, s58\n\ts_addk_i32 s21, 2048\n\ts_mul_i32 s22, s6, 20\n\ts_add_i32 s22, s22, s58\n\ts_addk_i32 s22, 1024\n\t"
            "s_mul_i32 s23, s7, 20\n\ts_add_i32 s23, s23, s58\n\ts_mul_i32 s24, s8, 20\n\ts_add_i32 s24, s24, s58\n\ts_addk_i32 s24, 3072\n\ts_mul_i32 s25, s9, 20\n\t"
            "s_add_i32 s25, s25, s58\n\ts_addk_i32 s25, 2048\n\ts_mul_i32 s26, s10, 20\n\ts_add_i32 s26, s26, s58\n\ts_addk_i32 s26, 1024\n\ts_mul_i32 s27, s11, 20\n\t"
            "s_add_i32 s27, s27, s58\n\ts_mul_i32 s28, s12, 20\n\ts_add_i32 s28, s28, s58\n\ts_addk_i32 s28, 3072\n\ts_mul_i32 s29, s13, 20\n\ts_add_i32 s29, s29, s58\n\t"
            "s_addk_i32 s29, 2048\n\ts_mul_i32 s72, s60, 1920\n\ts_sub_u32 s14, s61, s72\n\ts_mul_i32 s15, s56, 0x2800\n\ts_add_u32 s15, s15, 77824\n\tv_lshl_add_u32 v152, v52, 4, s15\n\t"
            "s_lshl_b32 s18, s58, 5\n\ts_mov_b32 s50, 822272\n\ts_mov_b32 s51, 0x20000\n\ts_waitcnt lgkmcnt(0)\n\ts_load_dword s67, s[44:45], 0x0\n\ts_sub_u32 s48, s48, 3072\n\t"
            "s_subb_u32 s49, s49, 0\n\ts_lshl_b32 s78, s56, 5\n\tv_add_u32 v12, s78, v57\n\tv_add_u32 v3, s62, v12\n\tv_and_b32 v13, 1, v53\n\tv_lshrrev_b32 v14, 1, v53\n\t"
            "v_lshl_add_u32 v15, v13, 1, v14\n\ts_add_u32 s79, s63, s58\n\tv_lshl_add_u32 v15, v15, 4, s79\n\ts_movk_i32 s79, 0xa00\n\tv_mad_i32_i24 v3, v3, s79, v15\n\tv_cmp_gt_u32 vcc, 8, v12\n\t"
            "s_cmp_eq_u32 s56, 3\n\ts_cselect_b32 s79, 2.0, 0xa000\n\tv_add_u32 v11, s79, v3\n\tv_cndmask_b32 v3, v3, 2.0, vcc\n\tv_mov_b32 v8, 159744\n\tv_and_b32 v9, 31, v52\n\t"
            "v_add_u32 v9, s78, v9\n\tv_lshl_add_u32 v9, v9, 2, v8\n\tv_lshl_add_u32 v10, v13, 4, v12\n\tv_lshl_add_u32 v10, v10, 2, v8\n\tv_mul_u32_u24 v4, 0x2800, v14\n\tv_lshl_add_u32 v4, v56, 7, v4\n\t"
            "v_lshl_add_u32 v4, v55, 3, v4\n\tv_lshl_add_u32 v4, v13, 5, v4\n\tv_bfe_u32 v5, v52, 3, 1\n\tv_lshl_add_u32 v4, v5, 6, v4\n\tv_add_u32 v4, 77824, v4\n\tv_xor_b32 v5, 64, v4\n\t"
            "v_add_u32 v6, 0x5000, v4\n\tv_add_u32 v7, 0x5000, v5\n\ts_add_u32 s78, s63, 1280\n\ts_mov_b32 s32, s60\n\ts_mov_b32 s83, 0\n\ts_add_u32 s33, s32, 256\n\t"
            "s_cmp_eq_u32 s33, s78\n\ts_cselect_b32 s33, s63, s33\n\ts_add_u32 s34, s33, 256\n\ts_cmp_eq_u32 s34, s78\n\ts_cselect_b32 s34, s63, s34\n\ts_add_u32 s35, s34, 256\n\t"
            "s_cmp_eq_u32 s35, s78\n\ts_cselect_b32 s35, s63, s35\n\ts_add_u32 s17, s35, 256\n\ts_cmp_eq_u32 s17, s78\n\ts_cselect_b32 s17, s63, s17\n\ts_add_u32 s80, s32, 128\n\t"
            "s_bitcmp1_b32 31, s64\n\ts_cselect_b32 s82, s34, s33\n\ts_bitcmp1_b32 0, s64\n\ts_cselect_b32 s82, s80, s82\n\ts_bitcmp1_b32 30, s64\n\ts_cselect_b32 s81, s35, s34\n\t"
            "s_bitcmp1_b32 1, s64\n\ts_cselect_b32 s81, s80, s81\n\ts_bitcmp1_b32 28, s64\n\ts_cselect_b32 s79, s17, s35\n\ts_bitcmp1_b32 2, s64\n\ts_cselect_b32 s79, s80, s79\n\t"
            "s_bitcmp1_b32 28, s64\n\ts_cselect_b32 s78, s80, s17\n\ts_waitcnt vmcnt(0) lgkmcnt(0)\n\ts_barrier\n\t"
            "v_mov_b32 v136, -1\n\tv_mov_b32 v137, -1\n\tv_mov_b32 v138, -1\n\tv_mov_b32 v139, -1\n\ts_cmp_ge_u32 s56, 2\n\ts_cselect_b64 vcc, -1, 0\n\t"
            "s_mov_b32 s60, s82\n\ts_mul_i32 s61, s60, 1920\n\ts_add_u32 m0, s66, 0\n\ts_add_u32 s61, s61, s14\n\tbuffer_load_dwordx4 v40, s[40:43], s61 offen lds\n\tbuffer_load_dwordx4 v41, s[40:43], s61 offen offset:1024 lds\n\t"
            "buffer_load_dwordx4 v42, s[40:43], s61 offen offset:2048 lds\n\tbuffer_load_dwordx4 v43, s[40:43], s61 offen offset:3072 lds\n\ts_addk_i32 m0, 0x1000\n\ts_nop 0\n\tbuffer_load_dwordx4 v44, s[40:43], s61 offen lds\n\tbuffer_load_dwordx4 v45, s[40:43], s61 offen offset:1024 lds\n\t"
            "s_add_u32 m0, s65, 0\n\ts_cbranch_vccnz 1f\n\tbuffer_load_dwordx4 v39, s[36:39], s60 offen offset:3072 lds\n\t1:\nbuffer_load_dwordx4 v36, s[36:39], s60 offen lds\n\tbuffer_load_dwordx4 v37, s[36:39], s60 offen offset:1024 lds\n\t"
            "buffer_load_dwordx4 v38, s[36:39], s60 offen offset:2048 lds\n\ts_mul_i32 s67, s67, 0x19660d\n\ts_add_u32 s67, s67, 0x3c6ef35f\n\ts_barrier\n\ts_mov_b32 s60, s81\n\ts_mul_i32 s61, s60, 1920\n\t"
            "s_add_u32 m0, s66, 38912\n\ts_add_u32 s61, s61, s14\n\tbuffer_load_dwordx4 v40, s[40:43], s61 offen lds\n\tbuffer_load_dwordx4 v41, s[40:43], s61 offen offset:1024 lds\n\tbuffer_load_dwordx4 v42, s[40:43], s61 offen offset:2048 lds\n\tbuffer_load_dwordx4 v43, s[40:43], s61 offen offset:3072 lds\n\t"
            "s_addk_i32 m0, 0x1000\n\ts_nop 0\n\tbuffer_load_dwordx4 v44, s[40:43], s61 offen lds\n\tbuffer_load_dwordx4 v45, s[40:43], s61 offen offset:1024 lds\n\ts_add_u32 m0, s65, 38912\n\ts_cbranch_vccnz 1f\n\t"
            "buffer_load_dwordx4 v39, s[36:39], s60 offen offset:3072 lds\n\t1:\nbuffer_load_dwordx4 v36, s[36:39], s60 offen lds\n\tbuffer_load_dwordx4 v37, s[36:39], s60 offen offset:1024 lds\n\tbuffer_load_dwordx4 v38, s[36:39], s60 offen offset:2048 lds\n\ts_bitcmp1_b32 13, s64\n\t"
            "s_cbranch_scc0 1f\n\tbuffer_store_dwordx4 v[136:139], v101, s[44:47], s18 offen offset:0 sc1\n\tbuffer_store_dwordx4 v[136:139], v2, s[44:47], s18 offen offset:1024 sc1\n\tbuffer_store_dwordx4 v[136:139], v2, s[44:47], s18 offen offset:2048 sc1\n\tbuffer_store_dwordx4 v[136:139], v101, s[44:47], s18 offen offset:3072 sc1\n\t1:\n"
            "s_bitcmp1_b32 13, s64\n\ts_cbranch_scc1 2f\n\ts_cbranch_vccz 1f\n\ts_waitcnt vmcnt(9)\n\t1:\ns_waitcnt vmcnt(10)\n\t"
            "2:\ns_cbranch_vccz 1f\n\ts_waitcnt vmcnt(13)\n\t1:\ns_waitcnt vmcnt(14)\n\ts_barrier\n\t"
            "s_mov_b32 s60, s79\n\ts_mul_i32 s61, s60, 1920\n\ts_add_u32 m0, s66, 77824\n\ts_add_u32 s61, s61, s14\n\tbuffer_load_dwordx4 v40, s[40:43], s61 offen lds\n\tbuffer_load_dwordx4 v41, s[40:43], s61 offen offset:1024 lds\n\t"
            "buffer_load_dwordx4 v42, s[40:43], s61 offen offset:2048 lds\n\tbuffer_load_dwordx4 v43, s[40:43], s61 offen offset:3072 lds\n\ts_addk_i32 m0, 0x1000\n\ts_nop 0\n\tbuffer_load_dwordx4 v44, s[40:43], s61 offen lds\n\tbuffer_load_dwordx4 v45, s[40:43], s61 offen offset:1024 lds\n\t"
            "s_add_u32 m0, s65, 77824\n\ts_cbranch_vccnz 1f\n\tbuffer_load_dwordx4 v39, s[36:39], s60 offen offset:3072 lds\n\t1:\nbuffer_load_dwordx4 v36, s[36:39], s60 offen lds\n\tbuffer_load_dwordx4 v37, s[36:39], s60 offen offset:1024 lds\n\t"
            "buffer_load_dwordx4 v38, s[36:39], s60 offen offset:2048 lds\n\ts_bitcmp1_b32 18, s64\n\ts_cbranch_scc0 1f\n\tbuffer_store_dwordx4 v[136:139], v101, s[44:47], s18 offen offset:0 sc1\n\tbuffer_store_dwordx4 v[136:139], v2, s[44:47], s18 offen offset:1024 sc1\n\tbuffer_store_dwordx4 v[136:139], v2, s[44:47], s18 offen offset:2048 sc1\n\t"
            "buffer_store_dwordx4 v[136:139], v101, s[44:47], s18 offen offset:3072 sc1\n\t1:\ns_bitcmp1_b32 31, s64\n\ts_cbranch_scc1 2f\n\ts_cbranch_vccz 1f\n\ts_waitcnt vmcnt(9)\n\t"
            "1:\ns_waitcnt vmcnt(10)\n\t2:\ns_cbranch_vccz 1f\n\ts_waitcnt vmcnt(13)\n\t1:\n"
            "s_waitcnt vmcnt(14)\n\ts_barrier\n\ts_mov_b32 s60, s78\n\ts_mul_i32 s61, s60, 1920\n\ts_add_u32 m0, s66, 0\n\ts_add_u32 s61, s61, s14\n\t"
            "buffer_load_dwordx4 v40, s[40:43], s61 offen lds\n\tbuffer_load_dwordx4 v41, s[40:43], s61 offen offset:1024 lds\n\tbuffer_load_dwordx4 v42, s[40:43], s61 offen offset:2048 lds\n\tbuffer_load_dwordx4 v43, s[40:43], s61 offen offset:3072 lds\n\ts_addk_i32 m0, 0x1000\n\ts_nop 0\n\t"
            "buffer_load_dwordx4 v44, s[40:43], s61 offen lds\n\tbuffer_load_dwordx4 v45, s[40:43], s61 offen offset:1024 lds\n\ts_add_u32 m0, s65, 0\n\ts_cbranch_vccnz 1f\n\tbuffer_load_dwordx4 v39, s[36:39], s60 offen offset:3072 lds\n\t1:\n"
            "buffer_load_dwordx4 v36, s[36:39], s60 offen lds\n\tbuffer_load_dwordx4 v37, s[36:39], s60 offen offset:1024 lds\n\tbuffer_load_dwordx4 v38, s[36:39], s60 offen offset:2048 lds\n\ts_cbranch_vccz 1f\n\ts_waitcnt vmcnt(9)\n\t1:\n"
            "s_waitcnt vmcnt(10)\n\ts_barrier\n\ts_add_u32 s60, s33, 128\n\ts_mul_i32 s61, s60, 1920\n\ts_add_u32 m0, s66, 38912\n\ts_add_u32 s61, s61, s14\n\t"
            "buffer_load_dwordx4 v40, s[40:43], s61 offen lds\n\tbuffer_load_dwordx4 v41, s[40:43], s61 offen offset:1024 lds\n\tbuffer_load_dwordx4 v42, s[40:43], s61 offen offset:2048 lds\n\tbuffer_load_dwordx4 v43, s[40:43], s61 offen offset:3072 lds\n\ts_addk_i32 m0, 0x1000\n\ts_nop 0\n\t"
            "buffer_load_dwordx4 v44, s[40:43], s61 offen lds\n\tbuffer_load_dwordx4 v45, s[40:43], s61 offen offset:1024 lds\n\ts_add_u32 m0, s65, 38912\n\ts_cbranch_vccnz 1f\n\tbuffer_load_dwordx4 v39, s[36:39], s60 offen offset:3072 lds\n\t1:\n"
            "buffer_load_dwordx4 v36, s[36:39], s60 offen lds\n\tbuffer_load_dwordx4 v37, s[36:39], s60 offen offset:1024 lds\n\tbuffer_load_dwordx4 v38, s[36:39], s60 offen offset:2048 lds\n\tv_mov_b32 v140, s67\n\tbuffer_store_dword v140, v98, s[44:47], 0 offen sc1\n\ts_cbranch_vccz 1f\n\t"
            "s_waitcnt vmcnt(10)\n\t1:\ns_waitcnt vmcnt(11)\n\ts_barrier\n\ts_add_u32 s60, s34, 128\n\ts_mul_i32 s61, s60, 1920\n\t"
            "s_add_u32 m0, s66, 77824\n\ts_add_u32 s61, s61, s14\n\tbuffer_load_dwordx4 v40, s[40:43], s61 offen lds\n\tbuffer_load_dwordx4 v41, s[40:43], s61 offen offset:1024 lds\n\tbuffer_load_dwordx4 v42, s[40:43], s61 offen offset:2048 lds\n\tbuffer_load_dwordx4 v43, s[40:43], s61 offen offset:3072 lds\n\t"
            "s_addk_i32 m0, 0x1000\n\ts_nop 0\n\tbuffer_load_dwordx4 v44, s[40:43], s61 offen lds\n\tbuffer_load_dwordx4 v45, s[40:43], s61 offen offset:1024 lds\n\ts_add_u32 m0, s65, 77824\n\ts_cbranch_vccnz 1f\n\t"
            "buffer_load_dwordx4 v39, s[36:39], s60 offen offset:3072 lds\n\t1:\nbuffer_load_dwordx4 v36, s[36:39], s60 offen lds\n\tbuffer_load_dwordx4 v37, s[36:39], s60 offen offset:1024 lds\n\tbuffer_load_dwordx4 v38, s[36:39], s60 offen offset:2048 lds\n\ts_cbranch_vccz 1f\n\t"
            "s_waitcnt vmcnt(10)\n\t1:\ns_waitcnt vmcnt(11)\n\ts_cmp_lg_u32 s56, 3\n\ts_cbranch_scc1 1f\n\tbuffer_load_dwordx4 v[106:109], v158, s[44:47], 0 offen sc1\n\t"
            "buffer_load_dwordx4 v[110:113], v159, s[44:47], 0 offen sc1\n\tbuffer_load_dwordx4 v[114:117], v104, s[44:47], 0 offen sc1\n\t1:\ns_barrier\n\ts_add_u32 s60, s35, 128\n\ts_mul_i32 s61, s60, 1920\n\t"
            "s_add_u32 m0, s66, 0\n\ts_add_u32 s61, s61, s14\n\tbuffer_load_dwordx4 v40, s[40:43], s61 offen lds\n\tbuffer_load_dwordx4 v41, s[40:43], s61 offen offset:1024 lds\n\tbuffer_load_dwordx4 v42, s[40:43], s61 offen offset:2048 lds\n\tbuffer_load_dwordx4 v43, s[40:43], s61 offen offset:3072 lds\n\t"
            "s_addk_i32 m0, 0x1000\n\ts_nop 0\n\tbuffer_load_dwordx4 v44, s[40:43], s61 offen lds\n\tbuffer_load_dwordx4 v45, s[40:43], s61 offen offset:1024 lds\n\ts_add_u32 m0, s65, 0\n\ts_cbranch_vccnz 1f\n\t"
            "buffer_load_dwordx4 v39, s[36:39], s60 offen offset:3072 lds\n\t1:\nbuffer_load_dwordx4 v36, s[36:39], s60 offen lds\n\tbuffer_load_dwordx4 v37, s[36:39], s60 offen offset:1024 lds\n\tbuffer_load_dwordx4 v38, s[36:39], s60 offen offset:2048 lds\n\ts_add_u32 m0, s15, 40960\n\t"
            "s_nop 0\n\tbuffer_load_dwordx4 v156, s[48:51], s20 offen lds\n\ts_cmp_lg_u32 s56, 3\n\ts_cbranch_scc1 2f\n\ts_waitcnt vmcnt(10)\n\ts_mov_b32 s68, 0x200000\n\t"
            "v_xor_b32 v106, s67, v107\n\tv_xor_b32 v108, s67, v109\n\tv_xor_b32 v110, s67, v111\n\tv_xor_b32 v112, s67, v113\n\tv_xor_b32 v114, s67, v115\n\tv_xor_b32 v116, s67, v117\n\t"
            "v_or3_b32 v106, v106, v108, v110\n\tv_or3_b32 v112, v112, v114, v116\n\tv_or_b32 v106, v106, v112\n\tv_cmp_ne_u32 vcc, 0, v106\n\ts_cbranch_vccnz 30f\n\t31:\n"
            "s_mov_b64 vcc, -1\n\t2:\ns_cbranch_vccz 1f\n\ts_waitcnt vmcnt(10)\n\t1:\ns_waitcnt vmcnt(11)\n\t"
            "s_barrier\n\ts_add_u32 s60, s17, 128\n\ts_mul_i32 s61, s60, 1920\n\ts_add_u32 m0, s66, 38912\n\ts_add_u32 s61, s61, s14\n\tbuffer_load_dwordx4 v40, s[40:43], s61 offen lds\n\t"
            "buffer_load_dwordx4 v41, s[40:43], s61 offen offset:1024 lds\n\tbuffer_load_dwordx4 v42, s[40:43], s61 offen offset:2048 lds\n\tbuffer_load_dwordx4 v43, s[40:43], s61 offen offset:3072 lds\n\ts_addk_i32 m0, 0x1000\n\ts_nop 0\n\tbuffer_load_dwordx4 v44, s[40:43], s61 offen lds\n\t"
            "buffer_load_dwordx4 v45, s[40:43], s61 offen offset:1024 lds\n\ts_add_u32 m0, s65, 38912\n\ts_cbranch_vccnz 1f\n\tbuffer_load_dwordx4 v39, s[36:39], s60 offen offset:3072 lds\n\t1:\nbuffer_load_dwordx4 v36, s[36:39], s60 offen lds\n\t"
            "buffer_load_dwordx4 v37, s[36:39], s60 offen offset:1024 lds\n\tbuffer_load_dwordx4 v38, s[36:39], s60 offen offset:2048 lds\n\ts_add_u32 m0, s15, 40960\n\ts_nop 0\n\tbuffer_load_dwordx4 v156, s[48:51], s21 offen offset:1024 lds\n\tbuffer_load_dwordx4 v156, s[48:51], s22 offen offset:2048 lds\n\t"
            "s_mov_b32 m0, s30\n\ts_nop 0\n\tbuffer_load_dword v97, s[44:47], 0 offen sc1 lds\n\tbuffer_load_dword v118, v96, s[44:47], 0 offen sc1\n\ts_cbranch_vccz 1f\n\ts_waitcnt vmcnt(13)\n\t"
            "1:\ns_waitcnt vmcnt(14)\n\ts_barrier\n\ts_add_u32 m0, s15, 40960\n\ts_nop 0\n\tbuffer_load_dwordx4 v156, s[48:51], s23 offen offset:3072 lds\n\t"
            "s_add_u32 m0, s15, 45056\n\ts_nop 0\n\tbuffer_load_dwordx4 v156, s[48:51], s24 offen lds\n\tbuffer_load_dwordx4 v156, s[48:51], s25 offen offset:1024 lds\n\tbuffer_load_dwordx4 v156, s[48:51], s26 offen offset:2048 lds\n\tbuffer_load_dwordx4 v156, s[48:51], s27 offen offset:3072 lds\n\t"
            "s_add_u32 m0, s15, 49152\n\ts_nop 0\n\tbuffer_load_dwordx4 v156, s[48:51], s28 offen lds\n\tbuffer_load_dwordx4 v156, s[48:51], s29 offen offset:1024 lds\n\ts_waitcnt vmcnt(7)\n\tv_cmp_ne_u32 vcc, s67, v118\n\t"
            "s_cmp_lg_u32 s56, 3\n\ts_cbranch_scc1 33f\n\ts_cbranch_vccnz 32f\n\t33:\ns_barrier\n\ts_add_u32 m0, s15, 0\n\t"
            "s_nop 0\n\tbuffer_load_dwordx4 v154, s[48:51], s20 offen lds\n\tbuffer_load_dwordx4 v154, s[48:51], s21 offen offset:1024 lds\n\tbuffer_load_dwordx4 v154, s[48:51], s22 offen offset:2048 lds\n\tbuffer_load_dwordx4 v154, s[48:51], s23 offen offset:3072 lds\n\ts_add_u32 m0, s15, 4096\n\t"
            "s_nop 0\n\tbuffer_load_dwordx4 v154, s[48:51], s24 offen lds\n\tbuffer_load_dwordx4 v154, s[48:51], s25 offen offset:1024 lds\n\tbuffer_load_dwordx4 v154, s[48:51], s26 offen offset:2048 lds\n\tbuffer_load_dwordx4 v154, s[48:51], s27 offen offset:3072 lds\n\ts_add_u32 m0, s15, 8192\n\t"
            "s_nop 0\n\tbuffer_load_dwordx4 v154, s[48:51], s28 offen lds\n\tbuffer_load_dwordx4 v154, s[48:51], s29 offen offset:1024 lds\n\ts_cmp_lg_u32 s56, 0\n\ts_cbranch_scc1 1f\n\ts_mov_b32 m0, 162624\n\t"
            "s_nop 0\n\tbuffer_load_dwordx4 v104, s[44:47], 0 offen sc1 lds\n\t1:\nbuffer_load_dwordx4 v[240:243], v3, s[36:39], 0 offen offset:0\n\tbuffer_load_dwordx4 v[244:247], v11, s[36:39], 0 offen offset:0\n\tbuffer_load_dwordx4 v[248:251], v3, s[36:39], 0 offen offset:64\n\t"
            "buffer_load_dwordx4 v[252:255], v11, s[36:39], 0 offen offset:64\n\tds_read_b32 v20, v9 offset:256\n\tds_read_b32 v21, v10 offset:832\n\tds_read2st64_b32 v[138:139], v153 offset1:1\n\tds_read2st64_b32 v[176:177], v160 offset1:1\n\tds_read2st64_b32 v[178:179], v161 offset1:1\n\t"
            "ds_read2st64_b32 v[180:181], v162 offset1:1\n\tds_read2st64_b32 v[182:183], v163 offset1:1\n\tds_read2st64_b32 v[184:185], v164 offset1:1\n\tds_read2st64_b32 v[186:187], v165 offset1:1\n\tds_read2st64_b32 v[188:189], v166 offset1:1\n\tds_read2st64_b32 v[190:191], v167 offset1:1\n\t"
            "s_mov_b32 s68, 0x200000\n\ts_waitcnt lgkmcnt(8)\n\tv_mul_f32 v138, 0x3ea52b5c, v138\n\tv_mul_f32 v139, 0x3ea52b5c, v139\n\ts_waitcnt lgkmcnt(7)\n\tv_mul_f32 v192, v176, v138\n\t"
            "v_mul_f32 v200, v177, v139\n\ts_waitcnt lgkmcnt(6)\n\tv_mul_f32 v193, v178, v138\n\tv_mul_f32 v201, v179, v139\n\ts_waitcnt lgkmcnt(5)\n\tv_mul_f32 v194, v180, v138\n\t"
            "v_mul_f32 v202, v181, v139\n\ts_waitcnt lgkmcnt(4)\n\tv_mul_f32 v195, v182, v138\n\tv_mul_f32 v203, v183, v139\n\ts_waitcnt lgkmcnt(3)\n\tv_mul_f32 v196, v184, v138\n\t"
            "v_mul_f32 v204, v185, v139\n\ts_waitcnt lgkmcnt(2)\n\tv_mul_f32 v197, v186, v138\n\tv_mul_f32 v205, v187, v139\n\ts_waitcnt lgkmcnt(1)\n\tv_mul_f32 v198, v188, v138\n\t"
            "v_mul_f32 v206, v189, v139\n\ts_waitcnt lgkmcnt(0)\n\tv_mul_f32 v199, v190, v138\n\tv_mul_f32 v207, v191, v139\n\ts_barrier\n\t"
            "50:\nds_read2_b64 v[208:211], v24 offset0:0 offset1:16\n\tds_read2_b64 v[212:215], v24 offset0:32 offset1:48\n\tds_read2_b64 v[216:219], v26 offset0:0 offset1:16\n\tds_read2_b64 v[220:223], v26 offset0:32 offset1:48\n\ts_cmp_eq_u32 s16, 0\n\t"
            "s_cbranch_scc1 56f\n\t58:\ns_cbranch_scc0 1f\n\tbuffer_load_dword v232, v31, s[44:47], s31 offen offset:0 sc1\n\tbuffer_load_dword v233, v31, s[44:47], s31 offen offset:256 sc1\n\tbuffer_load_dword v234, v31, s[44:47], s31 offen offset:512 sc1\n\t"
            "buffer_load_dword v235, v31, s[44:47], s31 offen offset:768 sc1\n\t1:\ns_waitcnt lgkmcnt(1)\n\tv_add_f32 v208, v208, v216\n\tv_add_f32 v209, v209, v217\n\tv_add_f32 v210, v210, v218\n\t"
            "v_add_f32 v211, v211, v219\n\ts_waitcnt lgkmcnt(0)\n\tv_add_f32 v212, v212, v220\n\tv_add_f32 v213, v213, v221\n\tv_add_f32 v214, v214, v222\n\tv_add_f32 v215, v215, v223\n\t"
            "v_fma_f32 v208, v208, v192, v128\n\tv_fma_f32 v209, v209, v193, v129\n\tv_fma_f32 v210, v210, v194, v130\n\tv_fma_f32 v211, v211, v195, v131\n\tv_fma_f32 v212, v212, v196, v132\n\tv_fma_f32 v213, v213, v197, v133\n\t"
            "v_fma_f32 v214, v214, v198, v134\n\tv_fma_f32 v215, v215, v199, v135\n\tv_max3_f32 v136, v208, v209, v210\n\tv_max3_f32 v137, v211, v212, v213\n\tv_max3_f32 v136, v136, v214, v215\n\tv_max_f32 v136, v136, v137\n\t"
            "v_sub_f32 v208, v208, v136\n\tv_sub_f32 v209, v209, v136\n\tv_sub_f32 v210, v210, v136\n\tv_sub_f32 v211, v211, v136\n\tv_sub_f32 v212, v212, v136\n\tv_sub_f32 v213, v213, v136\n\t"
            "v_sub_f32 v214, v214, v136\n\tv_sub_f32 v215, v215, v136\n\tv_exp_f32 v208, v208\n\tv_exp_f32 v209, v209\n\tv_exp_f32 v210, v210\n\tv_exp_f32 v211, v211\n\t"
            "v_exp_f32 v212, v212\n\tv_exp_f32 v213, v213\n\tv_exp_f32 v214, v214\n\tv_exp_f32 v215, v215\n\tv_add_f32 v216, v208, v209\n\tv_add_f32 v217, v210, v211\n\t"
            "v_add_f32 v218, v212, v213\n\tv_add_f32 v219, v214, v215\n\tv_add_f32 v216, v216, v217\n\tv_add_f32 v218, v218, v219\n\tv_mul_f32 v208, v208, v176\n\tv_add_f32 v216, v216, v218\n\t"
            "v_mul_f32 v216, 0xbfb8aa3b, v216\n\tv_rcp_f32 v216, v216\n\tv_mul_f32 v209, v209, v178\n\tv_mul_f32 v210, v210, v180\n\tv_mul_f32 v211, v211, v182\n\tv_mul_f32 v212, v212, v184\n\t"
            "v_mul_f32 v213, v213, v186\n\tv_mul_f32 v214, v214, v188\n\tv_mul_f32 v215, v215, v190\n\tv_mul_f32 v208, v208, v216\n\tv_mul_f32 v209, v209, v216\n\tv_mul_f32 v210, v210, v216\n\t"
            "v_mul_f32 v211, v211, v216\n\tv_mul_f32 v212, v212, v216\n\tv_mul_f32 v213, v213, v216\n\tv_mul_f32 v214, v214, v216\n\tv_mul_f32 v215, v215, v216\n\tv_cvt_pk_bf16_f32 v220, v208, v209\n\t"
            "v_cvt_pk_bf16_f32 v221, v210, v211\n\tv_cvt_pk_bf16_f32 v222, v212, v213\n\tv_cvt_pk_bf16_f32 v223, v214, v215\n\tds_write_b128 v29, v[220:223] offset:0\n\tds_write_b32 v30, v142 offset:16\n\t"
            "51:\nds_read_b32 v136, v28\n\tds_read2_b64 v[208:211], v25 offset0:0 offset1:16\n\tds_read2_b64 v[212:215], v25 offset0:32 offset1:48\n\tds_read2_b64 v[216:219], v27 offset0:0 offset1:16\n\tds_read2_b64 v[220:223], v27 offset0:32 offset1:48\n\t"
            "s_waitcnt lgkmcnt(4)\n\tv_cmp_eq_u32 vcc, 0, v136\n\ts_cbranch_vccnz 53f\n\t55:\n"
            "s_setprio 3\n\t"
            "s_cmp_eq_u32 s16, 0\n\ts_cbranch_scc1 57f\n\t59:\ns_waitcnt lgkmcnt(1)\n\tv_add_f32 v208, v208, v216\n\tv_add_f32 v209, v209, v217\n\t"
            "v_add_f32 v210, v210, v218\n\tv_add_f32 v211, v211, v219\n\ts_waitcnt lgkmcnt(0)\n\tv_add_f32 v212, v212, v220\n\tv_add_f32 v213, v213, v221\n\tv_add_f32 v214, v214, v222\n\t"
            "v_add_f32 v215, v215, v223\n\tv_mul_f32 v208, v208, v200\n\tv_mul_f32 v209, v209, v201\n\tv_mul_f32 v210, v210, v202\n\tv_mul_f32 v211, v211, v203\n\tv_mul_f32 v212, v212, v204\n\t"
            "v_mul_f32 v213, v213, v205\n\tv_mul_f32 v214, v214, v206\n\tv_mul_f32 v215, v215, v207\n\tv_max3_f32 v136, v208, v209, v210\n\tv_max3_f32 v137, v211, v212, v213\n\tv_max3_f32 v136, v136, v214, v215\n\t"
            "v_max_f32 v136, v136, v137\n\tv_sub_f32 v208, v208, v136\n\tv_sub_f32 v209, v209, v136\n\tv_sub_f32 v210, v210, v136\n\tv_sub_f32 v211, v211, v136\n\tv_sub_f32 v212, v212, v136\n\t"
            "v_sub_f32 v213, v213, v136\n\tv_sub_f32 v214, v214, v136\n\tv_sub_f32 v215, v215, v136\n\tv_exp_f32 v208, v208\n\tv_exp_f32 v209, v209\n\tv_exp_f32 v210, v210\n\t"
            "v_exp_f32 v211, v211\n\tv_exp_f32 v212, v212\n\tv_exp_f32 v213, v213\n\tv_exp_f32 v214, v214\n\tv_exp_f32 v215, v215\n\tv_add_f32 v216, v208, v209\n\t"
            "v_add_f32 v217, v210, v211\n\tv_add_f32 v218, v212, v213\n\tv_add_f32 v219, v214, v215\n\tv_add_f32 v216, v216, v217\n\tv_add_f32 v218, v218, v219\n\tv_mul_f32 v208, v208, v177\n\t"
            "v_add_f32 v216, v216, v218\n\tv_mul_f32 v216, 0xbfb8aa3b, v216\n\tv_rcp_f32 v216, v216\n\tv_mul_f32 v209, v209, v179\n\tv_mul_f32 v210, v210, v181\n\tv_mul_f32 v211, v211, v183\n\t"
            "v_mul_f32 v212, v212, v185\n\tv_mul_f32 v213, v213, v187\n\tv_mul_f32 v214, v214, v189\n\tv_mul_f32 v215, v215, v191\n\tv_mul_f32 v208, v208, v216\n\tv_mul_f32 v209, v209, v216\n\t"
            "v_mul_f32 v210, v210, v216\n\tv_mul_f32 v211, v211, v216\n\tv_mul_f32 v212, v212, v216\n\tv_mul_f32 v213, v213, v216\n\tv_mul_f32 v214, v214, v216\n\tv_mul_f32 v215, v215, v216\n\t"
            "v_cvt_pk_bf16_f32 v220, v208, v209\n\tv_cvt_pk_bf16_f32 v221, v210, v211\n\tv_cvt_pk_bf16_f32 v222, v212, v213\n\tv_cvt_pk_bf16_f32 v223, v214, v215\n\tds_write_b128 v29, v[220:223] offset:38912\n\tds_write_b32 v30, v30 offset:16\n\t"
            "s_setprio 0\n\t"
            "s_waitcnt vmcnt(0)\n\tds_write_b32 v30, v30 offset:48\n\ts_branch .LOL\n\t32:\nbuffer_load_dword v118, v96, s[44:47], 0 offen sc1\n\t"
            "s_waitcnt vmcnt(0)\n\tv_cmp_ne_u32 vcc, s67, v118\n\ts_cbranch_vccz 33b\n\ts_sub_u32 s68, s68, 1\n\ts_cbranch_scc0 32b\n\ts_branch 33b\n\t"
            "30:\nbuffer_load_dwordx4 v[106:109], v158, s[44:47], 0 offen sc1\n\tbuffer_load_dwordx4 v[110:113], v159, s[44:47], 0 offen sc1\n\tbuffer_load_dwordx4 v[114:117], v104, s[44:47], 0 offen sc1\n\t"
            "s_waitcnt vmcnt(2)\n\tv_xor_b32 v106, s67, v107\n\tv_xor_b32 v108, s67, v109\n\ts_waitcnt vmcnt(1)\n\tv_xor_b32 v110, s67, v111\n\tv_xor_b32 v112, s67, v113\n\t"
            "s_waitcnt vmcnt(0)\n\tv_xor_b32 v114, s67, v115\n\tv_xor_b32 v116, s67, v117\n\tv_or3_b32 v106, v106, v108, v110\n\tv_or3_b32 v112, v112, v114, v116\n\tv_or_b32 v106, v106, v112\n\t"
            "v_cmp_ne_u32 vcc, 0, v106\n\ts_cbranch_vccz 31b\n\ts_sub_u32 s68, s68, 1\n\ts_cbranch_scc0 30b\n\ts_branch 31b\n\t53:\n"
            "s_sleep 1\n\t"
            "s_sub_u32 s68, s68, 1\n\ts_cbranch_scc0 51b\n\ts_branch 55b\n\t56:\n"
            "s_sleep 6\n\t"
            "buffer_load_dword v224, v31, s[44:47], 0 offen offset:0 sc1\n\tbuffer_load_dword v225, v31, s[44:47], 0 offen offset:256 sc1\n\tbuffer_load_dword v226, v31, s[44:47], 0 offen offset:512 sc1\n\tbuffer_load_dword v227, v31, s[44:47], 0 offen offset:768 sc1\n\ts_waitcnt vmcnt(4)\n\t"
            "s_setprio 0\n\t"
            "s_call_b64 s[76:77], .LG0\n\t"
            "s_setprio 3\n\t"
            "s_branch 2f\n\t1:\ns_waitcnt vmcnt(3)\n\tbuffer_load_dword v224, v31, s[44:47], 0 offen offset:0 sc1\n\ts_waitcnt vmcnt(3)\n\tbuffer_load_dword v225, v31, s[44:47], 0 offen offset:256 sc1\n\t"
            "s_waitcnt vmcnt(3)\n\tbuffer_load_dword v226, v31, s[44:47], 0 offen offset:512 sc1\n\ts_waitcnt vmcnt(3)\n\tbuffer_load_dword v227, v31, s[44:47], 0 offen offset:768 sc1\n\ts_cmp_lg_u32 s83, 0\n\ts_cbranch_scc1 3f\n\t"
            "s_call_b64 s[76:77], .LG1\n\t3:\ns_cmp_eq_u32 s83, 2\n\ts_cbranch_scc1 2f\n\ts_call_b64 s[76:77], .LG2\n\t2:\n"
            "s_waitcnt vmcnt(0) lgkmcnt(0)\n\t4:\nds_write_b32 v30, v30 offset:48\n\tv_max3_u32 v136, v224, v225, v226\n\tv_max_u32 v136, v136, v227\n\tv_cmp_eq_u32 vcc, -1, v136\n\t"
            "v_lshlrev_b32 v216, 16, v224\n\tv_and_b32 v217, 0xffff0000, v224\n\tv_lshlrev_b32 v218, 16, v225\n\tv_and_b32 v219, 0xffff0000, v225\n\tv_lshlrev_b32 v220, 16, v226\n\tv_and_b32 v221, 0xffff0000, v226\n\t"
            "v_lshlrev_b32 v222, 16, v227\n\tv_and_b32 v223, 0xffff0000, v227\n\t"
            "s_cmp_eq_u32 0, 0\n\ts_cbranch_vccz 58b\n\ts_sub_u32 s68, s68, 1\n\ts_cbranch_scc0 1b\n\ts_branch 58b\n\t57:\n"
            "s_branch 2f\n\t1:\nbuffer_load_dword v232, v31, s[44:47], s31 offen offset:0 sc1\n\tbuffer_load_dword v233, v31, s[44:47], s31 offen offset:256 sc1\n\tbuffer_load_dword v234, v31, s[44:47], s31 offen offset:512 sc1\n\tbuffer_load_dword v235, v31, s[44:47], s31 offen offset:768 sc1\n\t"
            "s_cmp_lg_u32 s83, 0\n\ts_cbranch_scc1 3f\n\ts_call_b64 s[76:77], .LG1\n\t3:\ns_cmp_eq_u32 s83, 2\n\ts_cbranch_scc1 2f\n\t"
            "s_call_b64 s[76:77], .LG2\n\t2:\ns_waitcnt vmcnt(0) lgkmcnt(0)\n\t4:\nv_max3_u32 v136, v232, v233, v234\n\tv_max_u32 v136, v136, v235\n\t"
            "v_cmp_eq_u32 vcc, -1, v136\n\tv_lshlrev_b32 v216, 16, v232\n\tv_and_b32 v217, 0xffff0000, v232\n\tv_lshlrev_b32 v218, 16, v233\n\tv_and_b32 v219, 0xffff0000, v233\n\tv_lshlrev_b32 v220, 16, v234\n\t"
            "v_and_b32 v221, 0xffff0000, v234\n\tv_lshlrev_b32 v222, 16, v235\n\tv_and_b32 v223, 0xffff0000, v235\n\t"
            "s_cmp_eq_u32 0, 0\n\ts_cbranch_vccz 59b\n\ts_sub_u32 s68, s68, 1\n\ts_cbranch_scc0 1b\n\ts_branch 59b\n\t"
            ".LOL:\ns_lshr_b32 s71, s56, 1\n\ts_mov_b32 s68, 0x200000\n\ts_cmp_eq_u32 s16, 0\n\ts_cbranch_scc1 3f\n\ts_call_b64 s[76:77], .LG0\n\t"
            "3:\nds_read_b64 v[14:15], v8 offset:32\n\ts_waitcnt lgkmcnt(0)\n\tv_min_u32 v12, v14, v15\n\tv_cmp_lt_u32 vcc, s71, v12\n\ts_cbranch_vccnz 4f\n\t"
            "s_sleep 1\n\ts_sub_u32 s68, s68, 1\n\ts_cbranch_scc0 3b\n\t4:\ns_cmp_eq_u32 s16, 0\n\ts_cbranch_scc1 1f\n\t"
            "s_call_b64 s[76:77], .LG1\n\t1:\ns_cmp_lt_u32 s56, 2\n\ts_cbranch_scc1 1f\n\ts_sleep 3\n\t1:\n"
            "buffer_load_dwordx4 v[176:179], v2, s[44:47], s4 offen offset:1024 sc1\n\tbuffer_load_dwordx4 v[180:183], v2, s[44:47], s4 offen offset:2048 sc1\n\tbuffer_load_dwordx4 v[184:187], v2, s[44:47], s5 offen offset:1024 sc1\n\tbuffer_load_dwordx4 v[188:191], v2, s[44:47], s5 offen offset:2048 sc1\n\tbuffer_load_dwordx4 v[192:195], v2, s[44:47], s6 offen offset:1024 sc1\n\tbuffer_load_dwordx4 v[196:199], v2, s[44:47], s6 offen offset:2048 sc1\n\t"
            "buffer_load_dwordx4 v[200:203], v2, s[44:47], s7 offen offset:1024 sc1\n\tbuffer_load_dwordx4 v[204:207], v2, s[44:47], s7 offen offset:2048 sc1\n\tbuffer_load_dwordx4 v[208:211], v2, s[44:47], s8 offen offset:1024 sc1\n\tbuffer_load_dwordx4 v[212:215], v2, s[44:47], s8 offen offset:2048 sc1\n\ts_cmp_lg_u32 s83, 0\n\ts_cbranch_scc1 1f\n\t"
            "s_call_b64 s[76:77], .LG1\n\t1:\ns_cmp_eq_u32 s83, 2\n\ts_cbranch_scc1 1f\n\ts_call_b64 s[76:77], .LG2\n\t1:\n"
            "ds_read_b128 v[16:19], v8 offset:48\n\tds_read_b64_tr_b16 v[128:129], v4 offset:40960\n\tds_read_b64_tr_b16 v[130:131], v4 offset:41472\n\tds_read_b64_tr_b16 v[132:133], v5 offset:40960\n\tds_read_b64_tr_b16 v[134:135], v5 offset:41472\n\tds_read_b64_tr_b16 v[136:137], v6 offset:40960\n\t"
            "ds_read_b64_tr_b16 v[138:139], v6 offset:41472\n\tds_read_b64_tr_b16 v[140:141], v7 offset:40960\n\tds_read_b64_tr_b16 v[142:143], v7 offset:41472\n\tds_read_b64_tr_b16 v[144:145], v4 offset:41984\n\tds_read_b64_tr_b16 v[146:147], v4 offset:42496\n\tds_read_b64_tr_b16 v[148:149], v5 offset:41984\n\t"
            "ds_read_b64_tr_b16 v[150:151], v5 offset:42496\n\tds_read_b64_tr_b16 v[152:153], v6 offset:41984\n\tds_read_b64_tr_b16 v[154:155], v6 offset:42496\n\tds_read_b64_tr_b16 v[156:157], v7 offset:41984\n\tds_read_b64_tr_b16 v[158:159], v7 offset:42496\n\tv_mul_f32 v48, v48, v112\n\t"
            "v_mul_f32 v49, v49, v113\n\tv_mul_f32 v50, v50, v114\n\tv_mul_f32 v51, v51, v115\n\tv_mul_f32 v52, v52, v116\n\tv_mul_f32 v53, v53, v117\n\tv_mul_f32 v54, v54, v118\n\t"
            "v_mul_f32 v55, v55, v119\n\tv_mul_f32 v56, v56, v120\n\tv_mul_f32 v57, v57, v121\n\tv_mul_f32 v58, v58, v122\n\tv_mul_f32 v59, v59, v123\n\tv_mul_f32 v60, v60, v124\n\t"
            "v_mul_f32 v61, v61, v125\n\tv_mul_f32 v62, v62, v126\n\tv_mul_f32 v63, v63, v127\n\t"
            "s_waitcnt lgkmcnt(15)\n\tv_min3_u32 v16, v16, v17, v18\n\tv_min_u32 v16, v16, v19\n\tv_cmp_eq_u32 vcc, 0, v16\n\ts_cbranch_vccnz 62f\n\t63:\n"
            "s_waitcnt vmcnt(8)\n\tv_max3_u32 v12, v176, v178, v180\n\tv_max_u32 v12, v12, v182\n\tv_cmp_eq_u32 vcc, -1, v12\n\ts_cbranch_vccnz 100f\n\t200:\n"
            "s_waitcnt lgkmcnt(8)\n\tv_mfma_f32_32x32x16_bf16 v[96:111], v[128:131], v[176:179], 0\n\tds_read_b64_tr_b16 v[160:161], v4 offset:43008\n\tds_read_b64_tr_b16 v[162:163], v4 offset:43520\n\tbuffer_load_dwordx4 v[216:219], v2, s[44:47], s9 offen offset:1024 sc1\n\tv_mfma_f32_32x32x16_bf16 v[112:127], v[132:135], v[176:179], 0\n\t"
            "ds_read_b64_tr_b16 v[164:165], v5 offset:43008\n\tds_read_b64_tr_b16 v[166:167], v5 offset:43520\n\tbuffer_load_dwordx4 v[220:223], v2, s[44:47], s9 offen offset:2048 sc1\n\tv_mfma_f32_32x32x16_bf16 v[96:111], v[136:139], v[180:183], v[96:111]\n\tds_read_b64_tr_b16 v[168:169], v6 offset:43008\n\tds_read_b64_tr_b16 v[170:171], v6 offset:43520\n\t"
            "s_waitcnt vmcnt(8)\n\tv_max3_u32 v12, v184, v186, v188\n\tv_max_u32 v12, v12, v190\n\tv_cmp_eq_u32 vcc, -1, v12\n\tv_mfma_f32_32x32x16_bf16 v[112:127], v[140:143], v[180:183], v[112:127]\n\tds_read_b64_tr_b16 v[172:173], v7 offset:43008\n\t"
            "ds_read_b64_tr_b16 v[174:175], v7 offset:43520\n\ts_cbranch_vccnz 101f\n\t201:\ns_waitcnt lgkmcnt(8)\n\tv_mfma_f32_32x32x16_bf16 v[96:111], v[144:147], v[184:187], v[96:111]\n\tds_read_b64_tr_b16 v[128:129], v4 offset:44032\n\t"
            "ds_read_b64_tr_b16 v[130:131], v4 offset:44544\n\tbuffer_load_dwordx4 v[224:227], v2, s[44:47], s10 offen offset:1024 sc1\n\tv_mfma_f32_32x32x16_bf16 v[112:127], v[148:151], v[184:187], v[112:127]\n\tds_read_b64_tr_b16 v[132:133], v5 offset:44032\n\tds_read_b64_tr_b16 v[134:135], v5 offset:44544\n\tbuffer_load_dwordx4 v[228:231], v2, s[44:47], s10 offen offset:2048 sc1\n\t"
            "v_mfma_f32_32x32x16_bf16 v[96:111], v[152:155], v[188:191], v[96:111]\n\tds_read_b64_tr_b16 v[136:137], v6 offset:44032\n\tds_read_b64_tr_b16 v[138:139], v6 offset:44544\n\ts_waitcnt vmcnt(8)\n\tv_max3_u32 v12, v192, v194, v196\n\tv_max_u32 v12, v12, v198\n\t"
            "v_cmp_eq_u32 vcc, -1, v12\n\tv_mfma_f32_32x32x16_bf16 v[112:127], v[156:159], v[188:191], v[112:127]\n\tds_read_b64_tr_b16 v[140:141], v7 offset:44032\n\tds_read_b64_tr_b16 v[142:143], v7 offset:44544\n\ts_cbranch_vccnz 102f\n\t202:\n"
            "s_waitcnt lgkmcnt(8)\n\tv_mfma_f32_32x32x16_bf16 v[96:111], v[160:163], v[192:195], v[96:111]\n\tds_read_b64_tr_b16 v[144:145], v4 offset:45056\n\tds_read_b64_tr_b16 v[146:147], v4 offset:45568\n\tbuffer_load_dwordx4 v[232:235], v2, s[44:47], s11 offen offset:1024 sc1\n\tv_mfma_f32_32x32x16_bf16 v[112:127], v[164:167], v[192:195], v[112:127]\n\t"
            "ds_read_b64_tr_b16 v[148:149], v5 offset:45056\n\tds_read_b64_tr_b16 v[150:151], v5 offset:45568\n\tbuffer_load_dwordx4 v[236:239], v2, s[44:47], s11 offen offset:2048 sc1\n\tv_mfma_f32_32x32x16_bf16 v[96:111], v[168:171], v[196:199], v[96:111]\n\tds_read_b64_tr_b16 v[152:153], v6 offset:45056\n\tds_read_b64_tr_b16 v[154:155], v6 offset:45568\n\t"
            "s_waitcnt vmcnt(8)\n\tv_max3_u32 v12, v200, v202, v204\n\tv_max_u32 v12, v12, v206\n\tv_cmp_eq_u32 vcc, -1, v12\n\tv_mfma_f32_32x32x16_bf16 v[112:127], v[172:175], v[196:199], v[112:127]\n\tds_read_b64_tr_b16 v[156:157], v7 offset:45056\n\t"
            "ds_read_b64_tr_b16 v[158:159], v7 offset:45568\n\ts_cbranch_vccnz 103f\n\t203:\ns_waitcnt lgkmcnt(8)\n\tv_mfma_f32_32x32x16_bf16 v[96:111], v[128:131], v[200:203], v[96:111]\n\tds_read_b64_tr_b16 v[160:161], v4 offset:46080\n\t"
            "ds_read_b64_tr_b16 v[162:163], v4 offset:46592\n\tbuffer_load_dwordx4 v[176:179], v2, s[44:47], s12 offen offset:1024 sc1\n\tv_mfma_f32_32x32x16_bf16 v[112:127], v[132:135], v[200:203], v[112:127]\n\tds_read_b64_tr_b16 v[164:165], v5 offset:46080\n\tds_read_b64_tr_b16 v[166:167], v5 offset:46592\n\tds_read_b64 v[14:15], v8 offset:40\n\t"
            "buffer_load_dwordx4 v[180:183], v2, s[44:47], s12 offen offset:2048 sc1\n\tv_mfma_f32_32x32x16_bf16 v[96:111], v[136:139], v[204:207], v[96:111]\n\tds_read_b64_tr_b16 v[168:169], v6 offset:46080\n\tds_read_b64_tr_b16 v[170:171], v6 offset:46592\n\ts_waitcnt vmcnt(8)\n\tv_max3_u32 v12, v208, v210, v212\n\t"
            "v_max_u32 v12, v12, v214\n\tv_cmp_eq_u32 vcc, -1, v12\n\tv_mfma_f32_32x32x16_bf16 v[112:127], v[140:143], v[204:207], v[112:127]\n\tds_read_b64_tr_b16 v[172:173], v7 offset:46080\n\tds_read_b64_tr_b16 v[174:175], v7 offset:46592\n\ts_cbranch_vccnz 104f\n\t"
            "204:\ns_waitcnt lgkmcnt(9)\n\tv_mfma_f32_32x32x16_bf16 v[96:111], v[144:147], v[208:211], v[96:111]\n\tds_read_b64_tr_b16 v[128:129], v4 offset:47104\n\tds_read_b64_tr_b16 v[130:131], v4 offset:47616\n\tbuffer_load_dwordx4 v[184:187], v2, s[44:47], s13 offen offset:1024 sc1\n\t"
            "v_mfma_f32_32x32x16_bf16 v[112:127], v[148:151], v[208:211], v[112:127]\n\tds_read_b64_tr_b16 v[132:133], v5 offset:47104\n\tds_read_b64_tr_b16 v[134:135], v5 offset:47616\n\tbuffer_load_dwordx4 v[188:191], v2, s[44:47], s13 offen offset:2048 sc1\n\tv_mfma_f32_32x32x16_bf16 v[96:111], v[152:155], v[212:215], v[96:111]\n\tds_read_b64_tr_b16 v[136:137], v6 offset:47104\n\t"
            "ds_read_b64_tr_b16 v[138:139], v6 offset:47616\n\ts_waitcnt vmcnt(8)\n\tv_max3_u32 v12, v216, v218, v220\n\tv_max_u32 v12, v12, v222\n\tv_cmp_eq_u32 vcc, -1, v12\n\tv_mfma_f32_32x32x16_bf16 v[112:127], v[156:159], v[212:215], v[112:127]\n\t"
            "ds_read_b64_tr_b16 v[140:141], v7 offset:47104\n\tds_read_b64_tr_b16 v[142:143], v7 offset:47616\n\ts_cbranch_vccnz 105f\n\t205:\ns_waitcnt lgkmcnt(8)\n\tv_mfma_f32_32x32x16_bf16 v[96:111], v[160:163], v[216:219], v[96:111]\n\t"
            "v_min_u32 v14, v14, v15\n\tv_cmp_ge_u32 vcc, s71, v14\n\tds_read_b64_tr_b16 v[144:145], v4 offset:48128\n\tds_read_b64_tr_b16 v[146:147], v4 offset:48640\n\tv_mfma_f32_32x32x16_bf16 v[112:127], v[164:167], v[216:219], v[112:127]\n\ts_cbranch_vccnz 5f\n\t"
            "6:\nds_read_b64_tr_b16 v[148:149], v5 offset:48128\n\tds_read_b64_tr_b16 v[150:151], v5 offset:48640\n\tbuffer_load_dwordx4 v[192:195], v2, s[44:47], s4 offen offset:0 sc1\n\tbuffer_load_dwordx4 v[196:199], v2, s[44:47], s4 offen offset:3072 sc1\n\tv_mfma_f32_32x32x16_bf16 v[96:111], v[168:171], v[220:223], v[96:111]\n\t"
            "ds_read_b64_tr_b16 v[152:153], v6 offset:48128\n\tds_read_b64_tr_b16 v[154:155], v6 offset:48640\n\ts_waitcnt vmcnt(8)\n\tv_max3_u32 v12, v224, v226, v228\n\tv_max_u32 v12, v12, v230\n\tv_cmp_eq_u32 vcc, -1, v12\n\t"
            "v_mfma_f32_32x32x16_bf16 v[112:127], v[172:175], v[220:223], v[112:127]\n\tds_read_b64_tr_b16 v[156:157], v7 offset:48128\n\tds_read_b64_tr_b16 v[158:159], v7 offset:48640\n\ts_cbranch_vccnz 106f\n\t206:\ns_waitcnt lgkmcnt(8)\n\t"
            "v_mfma_f32_32x32x16_bf16 v[96:111], v[128:131], v[224:227], v[96:111]\n\tds_read_b64_tr_b16 v[160:161], v4 offset:49152\n\tds_read_b64_tr_b16 v[162:163], v4 offset:49664\n\tbuffer_load_dwordx4 v[200:203], v2, s[44:47], s5 offen offset:0 sc1\n\tv_mfma_f32_32x32x16_bf16 v[112:127], v[132:135], v[224:227], v[112:127]\n\tds_read_b64_tr_b16 v[164:165], v5 offset:49152\n\t"
            "ds_read_b64_tr_b16 v[166:167], v5 offset:49664\n\tbuffer_load_dwordx4 v[204:207], v2, s[44:47], s5 offen offset:3072 sc1\n\tv_mfma_f32_32x32x16_bf16 v[96:111], v[136:139], v[228:231], v[96:111]\n\tds_read_b64_tr_b16 v[168:169], v6 offset:49152\n\tds_read_b64_tr_b16 v[170:171], v6 offset:49664\n\ts_waitcnt vmcnt(8)\n\t"
            "v_max3_u32 v12, v232, v234, v236\n\tv_max_u32 v12, v12, v238\n\tv_cmp_eq_u32 vcc, -1, v12\n\tv_mfma_f32_32x32x16_bf16 v[112:127], v[140:143], v[228:231], v[112:127]\n\tds_read_b64_tr_b16 v[172:173], v7 offset:49152\n\tds_read_b64_tr_b16 v[174:175], v7 offset:49664\n\t"
            "s_cbranch_vccnz 107f\n\t207:\ns_waitcnt lgkmcnt(8)\n\tv_mfma_f32_32x32x16_bf16 v[96:111], v[144:147], v[232:235], v[96:111]\n\tds_read_b64_tr_b16 v[128:129], v4 offset:50176\n\tds_read_b64_tr_b16 v[130:131], v4 offset:50688\n\t"
            "buffer_load_dwordx4 v[208:211], v2, s[44:47], s6 offen offset:0 sc1\n\tv_mfma_f32_32x32x16_bf16 v[112:127], v[148:151], v[232:235], v[112:127]\n\tds_read_b64_tr_b16 v[132:133], v5 offset:50176\n\tds_read_b64_tr_b16 v[134:135], v5 offset:50688\n\tbuffer_load_dwordx4 v[212:215], v2, s[44:47], s6 offen offset:3072 sc1\n\tv_mfma_f32_32x32x16_bf16 v[96:111], v[152:155], v[236:239], v[96:111]\n\t"
            "ds_read_b64_tr_b16 v[136:137], v6 offset:50176\n\tds_read_b64_tr_b16 v[138:139], v6 offset:50688\n\ts_waitcnt vmcnt(8)\n\tv_max3_u32 v12, v176, v178, v180\n\tv_max_u32 v12, v12, v182\n\tv_cmp_eq_u32 vcc, -1, v12\n\t"
            "v_mfma_f32_32x32x16_bf16 v[112:127], v[156:159], v[236:239], v[112:127]\n\tds_read_b64_tr_b16 v[140:141], v7 offset:50176\n\tds_read_b64_tr_b16 v[142:143], v7 offset:50688\n\ts_cbranch_vccnz 108f\n\t208:\ns_waitcnt lgkmcnt(8)\n\t"
            "v_mfma_f32_32x32x16_bf16 v[96:111], v[160:163], v[176:179], v[96:111]\n\tds_read_b64_tr_b16 v[144:145], v4 offset:0\n\tds_read_b64_tr_b16 v[146:147], v4 offset:512\n\tbuffer_load_dwordx4 v[216:219], v2, s[44:47], s7 offen offset:0 sc1\n\tv_mfma_f32_32x32x16_bf16 v[112:127], v[164:167], v[176:179], v[112:127]\n\tds_read_b64_tr_b16 v[148:149], v5 offset:0\n\t"
            "ds_read_b64_tr_b16 v[150:151], v5 offset:512\n\tbuffer_load_dwordx4 v[220:223], v2, s[44:47], s7 offen offset:3072 sc1\n\tv_mfma_f32_32x32x16_bf16 v[96:111], v[168:171], v[180:183], v[96:111]\n\tds_read_b64_tr_b16 v[152:153], v6 offset:0\n\tds_read_b64_tr_b16 v[154:155], v6 offset:512\n\ts_waitcnt vmcnt(8)\n\t"
            "v_max3_u32 v12, v184, v186, v188\n\tv_max_u32 v12, v12, v190\n\tv_cmp_eq_u32 vcc, -1, v12\n\tv_mfma_f32_32x32x16_bf16 v[112:127], v[172:175], v[180:183], v[112:127]\n\tds_read_b64_tr_b16 v[156:157], v7 offset:0\n\tds_read_b64_tr_b16 v[158:159], v7 offset:512\n\t"
            "s_cbranch_vccnz 109f\n\t209:\ns_waitcnt lgkmcnt(8)\n\tv_mfma_f32_32x32x16_bf16 v[96:111], v[128:131], v[184:187], v[96:111]\n\tds_read_b64_tr_b16 v[160:161], v4 offset:1024\n\tds_read_b64_tr_b16 v[162:163], v4 offset:1536\n\t"
            "buffer_load_dwordx4 v[224:227], v2, s[44:47], s8 offen offset:0 sc1\n\tv_mfma_f32_32x32x16_bf16 v[112:127], v[132:135], v[184:187], v[112:127]\n\tds_read_b64_tr_b16 v[164:165], v5 offset:1024\n\tds_read_b64_tr_b16 v[166:167], v5 offset:1536\n\tbuffer_load_dwordx4 v[228:231], v2, s[44:47], s8 offen offset:3072 sc1\n\tv_mfma_f32_32x32x16_bf16 v[96:111], v[136:139], v[188:191], v[96:111]\n\t"
            "ds_read_b64_tr_b16 v[168:169], v6 offset:1024\n\tds_read_b64_tr_b16 v[170:171], v6 offset:1536\n\ts_waitcnt vmcnt(8)\n\tv_max3_u32 v12, v192, v194, v196\n\tv_max_u32 v12, v12, v198\n\tv_cmp_eq_u32 vcc, -1, v12\n\t"
            "v_mfma_f32_32x32x16_bf16 v[112:127], v[140:143], v[188:191], v[112:127]\n\tds_read_b64_tr_b16 v[172:173], v7 offset:1024\n\tds_read_b64_tr_b16 v[174:175], v7 offset:1536\n\ts_cbranch_vccnz 110f\n\t210:\ns_waitcnt lgkmcnt(8)\n\t"
            "v_mfma_f32_32x32x16_bf16 v[96:111], v[144:147], v[192:195], v[96:111]\n\tds_read_b64_tr_b16 v[128:129], v4 offset:2048\n\tds_read_b64_tr_b16 v[130:131], v4 offset:2560\n\tbuffer_load_dwordx4 v[232:235], v2, s[44:47], s9 offen offset:0 sc1\n\tv_mfma_f32_32x32x16_bf16 v[112:127], v[148:151], v[192:195], v[112:127]\n\tds_read_b64_tr_b16 v[132:133], v5 offset:2048\n\t"
            "ds_read_b64_tr_b16 v[134:135], v5 offset:2560\n\tbuffer_load_dwordx4 v[236:239], v2, s[44:47], s9 offen offset:3072 sc1\n\tv_mfma_f32_32x32x16_bf16 v[96:111], v[152:155], v[196:199], v[96:111]\n\tds_read_b64_tr_b16 v[136:137], v6 offset:2048\n\tds_read_b64_tr_b16 v[138:139], v6 offset:2560\n\ts_waitcnt vmcnt(8)\n\t"
            "v_max3_u32 v12, v200, v202, v204\n\tv_max_u32 v12, v12, v206\n\tv_cmp_eq_u32 vcc, -1, v12\n\tv_mfma_f32_32x32x16_bf16 v[112:127], v[156:159], v[196:199], v[112:127]\n\tds_read_b64_tr_b16 v[140:141], v7 offset:2048\n\tds_read_b64_tr_b16 v[142:143], v7 offset:2560\n\t"
            "s_cbranch_vccnz 111f\n\t211:\ns_waitcnt lgkmcnt(8)\n\tv_mfma_f32_32x32x16_bf16 v[96:111], v[160:163], v[200:203], v[96:111]\n\tds_read_b64_tr_b16 v[144:145], v4 offset:3072\n\tds_read_b64_tr_b16 v[146:147], v4 offset:3584\n\t"
            "buffer_load_dwordx4 v[176:179], v2, s[44:47], s10 offen offset:0 sc1\n\tv_mfma_f32_32x32x16_bf16 v[112:127], v[164:167], v[200:203], v[112:127]\n\tds_read_b64_tr_b16 v[148:149], v5 offset:3072\n\tds_read_b64_tr_b16 v[150:151], v5 offset:3584\n\tbuffer_load_dwordx4 v[180:183], v2, s[44:47], s10 offen offset:3072 sc1\n\tv_mfma_f32_32x32x16_bf16 v[96:111], v[168:171], v[204:207], v[96:111]\n\t"
            "ds_read_b64_tr_b16 v[152:153], v6 offset:3072\n\tds_read_b64_tr_b16 v[154:155], v6 offset:3584\n\ts_waitcnt vmcnt(8)\n\tv_max3_u32 v12, v208, v210, v212\n\tv_max_u32 v12, v12, v214\n\tv_cmp_eq_u32 vcc, -1, v12\n\t"
            "v_mfma_f32_32x32x16_bf16 v[112:127], v[172:175], v[204:207], v[112:127]\n\tds_read_b64_tr_b16 v[156:157], v7 offset:3072\n\tds_read_b64_tr_b16 v[158:159], v7 offset:3584\n\ts_cbranch_vccnz 112f\n\t212:\ns_waitcnt lgkmcnt(8)\n\t"
            "v_mfma_f32_32x32x16_bf16 v[96:111], v[128:131], v[208:211], v[96:111]\n\tds_read_b64_tr_b16 v[160:161], v4 offset:4096\n\tds_read_b64_tr_b16 v[162:163], v4 offset:4608\n\tbuffer_load_dwordx4 v[184:187], v2, s[44:47], s11 offen offset:0 sc1\n\tv_mfma_f32_32x32x16_bf16 v[112:127], v[132:135], v[208:211], v[112:127]\n\tds_read_b64_tr_b16 v[164:165], v5 offset:4096\n\t"
            "ds_read_b64_tr_b16 v[166:167], v5 offset:4608\n\tbuffer_load_dwordx4 v[188:191], v2, s[44:47], s11 offen offset:3072 sc1\n\tv_mfma_f32_32x32x16_bf16 v[96:111], v[136:139], v[212:215], v[96:111]\n\tds_read_b64_tr_b16 v[168:169], v6 offset:4096\n\tds_read_b64_tr_b16 v[170:171], v6 offset:4608\n\ts_waitcnt vmcnt(8)\n\t"
            "v_max3_u32 v12, v216, v218, v220\n\tv_max_u32 v12, v12, v222\n\tv_cmp_eq_u32 vcc, -1, v12\n\tv_mfma_f32_32x32x16_bf16 v[112:127], v[140:143], v[212:215], v[112:127]\n\tds_read_b64_tr_b16 v[172:173], v7 offset:4096\n\tds_read_b64_tr_b16 v[174:175], v7 offset:4608\n\t"
            "s_cbranch_vccnz 113f\n\t213:\ns_waitcnt lgkmcnt(8)\n\tv_mfma_f32_32x32x16_bf16 v[96:111], v[144:147], v[216:219], v[96:111]\n\tds_read_b64_tr_b16 v[128:129], v4 offset:5120\n\tds_read_b64_tr_b16 v[130:131], v4 offset:5632\n\t"
            "buffer_load_dwordx4 v[192:195], v2, s[44:47], s12 offen offset:0 sc1\n\tv_mfma_f32_32x32x16_bf16 v[112:127], v[148:151], v[216:219], v[112:127]\n\tds_read_b64_tr_b16 v[132:133], v5 offset:5120\n\tds_read_b64_tr_b16 v[134:135], v5 offset:5632\n\tbuffer_load_dwordx4 v[196:199], v2, s[44:47], s12 offen offset:3072 sc1\n\tv_mfma_f32_32x32x16_bf16 v[96:111], v[152:155], v[220:223], v[96:111]\n\t"
            "ds_read_b64_tr_b16 v[136:137], v6 offset:5120\n\tds_read_b64_tr_b16 v[138:139], v6 offset:5632\n\ts_waitcnt vmcnt(8)\n\tv_max3_u32 v12, v224, v226, v228\n\tv_max_u32 v12, v12, v230\n\tv_cmp_eq_u32 vcc, -1, v12\n\t"
            "v_mfma_f32_32x32x16_bf16 v[112:127], v[156:159], v[220:223], v[112:127]\n\tds_read_b64_tr_b16 v[140:141], v7 offset:5120\n\tds_read_b64_tr_b16 v[142:143], v7 offset:5632\n\ts_cbranch_vccnz 114f\n\t214:\ns_waitcnt lgkmcnt(8)\n\t"
            "v_mfma_f32_32x32x16_bf16 v[96:111], v[160:163], v[224:227], v[96:111]\n\tds_read_b64_tr_b16 v[144:145], v4 offset:6144\n\tds_read_b64_tr_b16 v[146:147], v4 offset:6656\n\tbuffer_load_dwordx4 v[200:203], v2, s[44:47], s13 offen offset:0 sc1\n\tv_mfma_f32_32x32x16_bf16 v[112:127], v[164:167], v[224:227], v[112:127]\n\tds_read_b64_tr_b16 v[148:149], v5 offset:6144\n\t"
            "ds_read_b64_tr_b16 v[150:151], v5 offset:6656\n\tbuffer_load_dwordx4 v[204:207], v2, s[44:47], s13 offen offset:3072 sc1\n\tv_mfma_f32_32x32x16_bf16 v[96:111], v[168:171], v[228:231], v[96:111]\n\tds_read_b64_tr_b16 v[152:153], v6 offset:6144\n\tds_read_b64_tr_b16 v[154:155], v6 offset:6656\n\ts_waitcnt vmcnt(8)\n\t"
            "v_max3_u32 v12, v232, v234, v236\n\tv_max_u32 v12, v12, v238\n\tv_cmp_eq_u32 vcc, -1, v12\n\tv_mfma_f32_32x32x16_bf16 v[112:127], v[172:175], v[228:231], v[112:127]\n\tds_read_b64_tr_b16 v[156:157], v7 offset:6144\n\tds_read_b64_tr_b16 v[158:159], v7 offset:6656\n\t"
            "s_cbranch_vccnz 115f\n\t215:\ns_waitcnt lgkmcnt(8)\n\tv_mfma_f32_32x32x16_bf16 v[96:111], v[128:131], v[232:235], v[96:111]\n\tds_read_b64_tr_b16 v[160:161], v4 offset:7168\n\tds_read_b64_tr_b16 v[162:163], v4 offset:7680\n\t"
            "v_mfma_f32_32x32x16_bf16 v[112:127], v[132:135], v[232:235], v[112:127]\n\tds_read_b64_tr_b16 v[164:165], v5 offset:7168\n\tds_read_b64_tr_b16 v[166:167], v5 offset:7680\n\tv_mfma_f32_32x32x16_bf16 v[96:111], v[136:139], v[236:239], v[96:111]\n\tds_read_b64_tr_b16 v[168:169], v6 offset:7168\n\tds_read_b64_tr_b16 v[170:171], v6 offset:7680\n\t"
            "s_waitcnt vmcnt(6)\n\tv_max3_u32 v12, v176, v178, v180\n\tv_max_u32 v12, v12, v182\n\tv_cmp_eq_u32 vcc, -1, v12\n\tv_mfma_f32_32x32x16_bf16 v[112:127], v[140:143], v[236:239], v[112:127]\n\tds_read_b64_tr_b16 v[172:173], v7 offset:7168\n\t"
            "ds_read_b64_tr_b16 v[174:175], v7 offset:7680\n\ts_cbranch_vccnz 116f\n\t216:\ns_waitcnt lgkmcnt(8)\n\tv_mfma_f32_32x32x16_bf16 v[96:111], v[144:147], v[176:179], v[96:111]\n\tds_read_b64_tr_b16 v[128:129], v4 offset:8192\n\t"
            "ds_read_b64_tr_b16 v[130:131], v4 offset:8704\n\tv_mfma_f32_32x32x16_bf16 v[112:127], v[148:151], v[176:179], v[112:127]\n\tds_read_b64_tr_b16 v[132:133], v5 offset:8192\n\tds_read_b64_tr_b16 v[134:135], v5 offset:8704\n\tv_mfma_f32_32x32x16_bf16 v[96:111], v[152:155], v[180:183], v[96:111]\n\tds_read_b64_tr_b16 v[136:137], v6 offset:8192\n\t"
            "ds_read_b64_tr_b16 v[138:139], v6 offset:8704\n\ts_waitcnt vmcnt(4)\n\tv_max3_u32 v12, v184, v186, v188\n\tv_max_u32 v12, v12, v190\n\tv_cmp_eq_u32 vcc, -1, v12\n\tv_mfma_f32_32x32x16_bf16 v[112:127], v[156:159], v[180:183], v[112:127]\n\t"
            "ds_read_b64_tr_b16 v[140:141], v7 offset:8192\n\tds_read_b64_tr_b16 v[142:143], v7 offset:8704\n\ts_cbranch_vccnz 117f\n\t217:\nds_read_b32 v16, v8 offset:64\n\tds_read_b32 v17, v9 offset:1344\n\t"
            "s_waitcnt lgkmcnt(10)\n\tv_mfma_f32_32x32x16_bf16 v[96:111], v[160:163], v[184:187], v[96:111]\n\tds_read_b64_tr_b16 v[144:145], v4 offset:9216\n\tds_read_b64_tr_b16 v[146:147], v4 offset:9728\n\tds_read_b64_tr_b16 v[148:149], v5 offset:9216\n\tds_read_b64_tr_b16 v[150:151], v5 offset:9728\n\t"
            "ds_read_b64_tr_b16 v[152:153], v6 offset:9216\n\tds_read_b64_tr_b16 v[154:155], v6 offset:9728\n\ts_waitcnt vmcnt(2)\n\tv_max3_u32 v12, v192, v194, v196\n\tv_max_u32 v12, v12, v198\n\tv_cmp_eq_u32 vcc, -1, v12\n\t"
            "v_mfma_f32_32x32x16_bf16 v[96:111], v[168:171], v[188:191], v[96:111]\n\tds_read_b64_tr_b16 v[156:157], v7 offset:9216\n\tds_read_b64_tr_b16 v[158:159], v7 offset:9728\n\ts_cbranch_vccnz 118f\n\t218:\ns_waitcnt lgkmcnt(10)\n\t"
            "v_mfma_f32_32x32x16_bf16 v[96:111], v[128:131], v[192:195], v[96:111]\n\ts_waitcnt vmcnt(0)\n\tv_max3_u32 v12, v200, v202, v204\n\tv_max_u32 v12, v12, v206\n\tv_cmp_eq_u32 vcc, -1, v12\n\tv_mfma_f32_32x32x16_bf16 v[96:111], v[136:139], v[196:199], v[96:111]\n\t"
            "s_cbranch_vccnz 119f\n\t219:\ns_waitcnt lgkmcnt(0)\n\tv_mfma_f32_32x32x16_bf16 v[96:111], v[144:147], v[200:203], v[96:111]\n\tv_cmp_ne_u32 s[74:75], 0, v17\n\tv_cmp_eq_u32 vcc, 0, v16\n\t"
            "v_mfma_f32_32x32x16_bf16 v[96:111], v[152:155], v[204:207], v[96:111]\n\ts_or_b64 vcc, vcc, s[74:75]\n\tv_mfma_f32_32x32x16_bf16 v[112:127], v[164:167], v[184:187], v[112:127]\n\ts_cbranch_scc1 7f\n\t8:\ns_nop 5\n\t"
            "v_mfma_f32_32x32x16_bf16 v[112:127], v[172:175], v[188:191], v[112:127]\n\ts_nop 1\n\tv_fmac_f32 v64, v96, v32\n\tv_fmac_f32 v65, v97, v33\n\tv_fmac_f32 v66, v98, v34\n\tv_fmac_f32 v67, v99, v35\n\t"
            "v_fmac_f32 v68, v100, v36\n\tv_fmac_f32 v69, v101, v37\n\tv_fmac_f32 v70, v102, v38\n\tv_mfma_f32_32x32x16_bf16 v[112:127], v[132:135], v[192:195], v[112:127]\n\tv_fmac_f32 v71, v103, v39\n\tv_fmac_f32 v72, v104, v40\n\t"
            "v_fmac_f32 v73, v105, v41\n\tv_fmac_f32 v74, v106, v42\n\tv_fmac_f32 v75, v107, v43\n\tv_fmac_f32 v76, v108, v44\n\tv_fmac_f32 v77, v109, v45\n\tv_mfma_f32_32x32x16_bf16 v[112:127], v[140:143], v[196:199], v[112:127]\n\t"
            "v_fmac_f32 v78, v110, v46\n\tv_fmac_f32 v79, v111, v47\n\tv_cvt_pk_bf16_f32 v240, v64, v65\n\tv_cvt_pk_bf16_f32 v242, v68, v69\n\tv_cvt_pk_bf16_f32 v241, v66, v67\n\tv_cvt_pk_bf16_f32 v243, v70, v71\n\t"
            "v_cvt_pk_bf16_f32 v244, v72, v73\n\tv_mfma_f32_32x32x16_bf16 v[112:127], v[148:151], v[200:203], v[112:127]\n\tv_cvt_pk_bf16_f32 v246, v76, v77\n\tv_cvt_pk_bf16_f32 v245, v74, v75\n\tv_cvt_pk_bf16_f32 v247, v78, v79\n\tv_permlane32_swap_b32 v240, v242\n\t"
            "v_permlane32_swap_b32 v241, v243\n\tv_permlane32_swap_b32 v244, v246\n\tv_permlane32_swap_b32 v245, v247\n\tv_mfma_f32_32x32x16_bf16 v[112:127], v[156:159], v[204:207], v[112:127]\n\tv_permlane16_swap_b32 v240, v244\n\tv_permlane16_swap_b32 v242, v246\n\t"
            "v_permlane16_swap_b32 v241, v245\n\tv_permlane16_swap_b32 v243, v247\n\tbuffer_store_dwordx4 v[240:243], v3, s[52:55], 0 offen offset:0 sc1\n\tbuffer_store_dwordx4 v[244:247], v11, s[52:55], 0 offen offset:0 sc1\n\t"
            "s_cmp_lg_u32 s74, 0\n\ts_cbranch_scc1 73f\n\t74:\ns_nop 3\n\tv_fmac_f32 v80, v112, v48\n\tv_fmac_f32 v81, v113, v49\n\t"
            "v_fmac_f32 v82, v114, v50\n\tv_fmac_f32 v83, v115, v51\n\tv_fmac_f32 v84, v116, v52\n\tv_fmac_f32 v85, v117, v53\n\tv_fmac_f32 v86, v118, v54\n\tv_fmac_f32 v87, v119, v55\n\t"
            "v_fmac_f32 v88, v120, v56\n\tv_fmac_f32 v89, v121, v57\n\tv_fmac_f32 v90, v122, v58\n\tv_fmac_f32 v91, v123, v59\n\tv_fmac_f32 v92, v124, v60\n\tv_fmac_f32 v93, v125, v61\n\t"
            "v_fmac_f32 v94, v126, v62\n\tv_fmac_f32 v95, v127, v63\n\tv_cvt_pk_bf16_f32 v248, v80, v81\n\tv_cvt_pk_bf16_f32 v250, v84, v85\n\tv_cvt_pk_bf16_f32 v249, v82, v83\n\tv_cvt_pk_bf16_f32 v251, v86, v87\n\t"
            "v_cvt_pk_bf16_f32 v252, v88, v89\n\tv_cvt_pk_bf16_f32 v254, v92, v93\n\tv_cvt_pk_bf16_f32 v253, v90, v91\n\tv_cvt_pk_bf16_f32 v255, v94, v95\n\tv_permlane32_swap_b32 v248, v250\n\tv_permlane32_swap_b32 v249, v251\n\t"
            "v_permlane32_swap_b32 v252, v254\n\tv_permlane32_swap_b32 v253, v255\n\ts_nop 0\n\tv_permlane16_swap_b32 v248, v252\n\tv_permlane16_swap_b32 v250, v254\n\tv_permlane16_swap_b32 v249, v253\n\t"
            "v_permlane16_swap_b32 v251, v255\n\tbuffer_store_dwordx4 v[248:251], v3, s[52:55], 0 offen offset:64 sc1\n\tbuffer_store_dwordx4 v[252:255], v11, s[52:55], 0 offen offset:64 sc1\n\t9:\n"
            "s_endpgm\n\t.LG0:\nv_mul_f32 v21, 0xbfb8aa3b, v21\n\tv_mul_f32 v20, v20, v21\n\tv_permlane16_swap_b32 v240, v244\n\tv_permlane16_swap_b32 v241, v245\n\t"
            "v_permlane16_swap_b32 v242, v246\n\tv_permlane16_swap_b32 v243, v247\n\tv_permlane16_swap_b32 v248, v252\n\tv_permlane16_swap_b32 v249, v253\n\tv_permlane16_swap_b32 v250, v254\n\tv_permlane16_swap_b32 v251, v255\n\t"
            "v_permlane32_swap_b32 v240, v242\n\tv_permlane32_swap_b32 v241, v243\n\tv_permlane32_swap_b32 v244, v246\n\tv_permlane32_swap_b32 v245, v247\n\tv_permlane32_swap_b32 v248, v250\n\tv_permlane32_swap_b32 v249, v251\n\t"
            "v_permlane32_swap_b32 v252, v254\n\tv_permlane32_swap_b32 v253, v255\n\tv_lshlrev_b32 v64, 16, v240\n\tv_and_b32 v65, 0xffff0000, v240\n\tv_lshlrev_b32 v66, 16, v241\n\tv_and_b32 v67, 0xffff0000, v241\n\t"
            "v_lshlrev_b32 v68, 16, v242\n\tv_and_b32 v69, 0xffff0000, v242\n\tv_lshlrev_b32 v70, 16, v243\n\tv_and_b32 v71, 0xffff0000, v243\n\tv_lshlrev_b32 v72, 16, v244\n\tv_and_b32 v73, 0xffff0000, v244\n\t"
            "v_lshlrev_b32 v74, 16, v245\n\tv_and_b32 v75, 0xffff0000, v245\n\tv_lshlrev_b32 v76, 16, v246\n\tv_and_b32 v77, 0xffff0000, v246\n\tv_lshlrev_b32 v78, 16, v247\n\tv_and_b32 v79, 0xffff0000, v247\n\t"
            "v_lshlrev_b32 v80, 16, v248\n\tv_and_b32 v81, 0xffff0000, v248\n\tv_lshlrev_b32 v82, 16, v249\n\tv_and_b32 v83, 0xffff0000, v249\n\tv_lshlrev_b32 v84, 16, v250\n\tv_and_b32 v85, 0xffff0000, v250\n\t"
            "v_lshlrev_b32 v86, 16, v251\n\tv_and_b32 v87, 0xffff0000, v251\n\tv_lshlrev_b32 v88, 16, v252\n\tv_and_b32 v89, 0xffff0000, v252\n\tv_lshlrev_b32 v90, 16, v253\n\tv_and_b32 v91, 0xffff0000, v253\n\t"
            "v_lshlrev_b32 v92, 16, v254\n\tv_and_b32 v93, 0xffff0000, v254\n\tv_lshlrev_b32 v94, 16, v255\n\tv_and_b32 v95, 0xffff0000, v255\n\tv_fma_f32 v96, v64, v21, v20\n\tv_fma_f32 v97, v65, v21, v20\n\t"
            "v_fma_f32 v98, v66, v21, v20\n\tv_fma_f32 v99, v67, v21, v20\n\tv_fma_f32 v100, v68, v21, v20\n\tv_fma_f32 v101, v69, v21, v20\n\tv_fma_f32 v102, v70, v21, v20\n\tv_fma_f32 v103, v71, v21, v20\n\t"
            "v_fma_f32 v104, v72, v21, v20\n\tv_fma_f32 v105, v73, v21, v20\n\tv_fma_f32 v106, v74, v21, v20\n\tv_fma_f32 v107, v75, v21, v20\n\tv_fma_f32 v108, v76, v21, v20\n\tv_fma_f32 v109, v77, v21, v20\n\t"
            "v_fma_f32 v110, v78, v21, v20\n\tv_fma_f32 v111, v79, v21, v20\n\tv_fma_f32 v112, v80, v21, v20\n\tv_fma_f32 v113, v81, v21, v20\n\tv_fma_f32 v114, v82, v21, v20\n\tv_fma_f32 v115, v83, v21, v20\n\t"
            "v_fma_f32 v116, v84, v21, v20\n\tv_fma_f32 v117, v85, v21, v20\n\tv_fma_f32 v118, v86, v21, v20\n\tv_fma_f32 v119, v87, v21, v20\n\tv_fma_f32 v120, v88, v21, v20\n\tv_fma_f32 v121, v89, v21, v20\n\t"
            "v_fma_f32 v122, v90, v21, v20\n\tv_fma_f32 v123, v91, v21, v20\n\tv_fma_f32 v124, v92, v21, v20\n\tv_fma_f32 v125, v93, v21, v20\n\tv_fma_f32 v126, v94, v21, v20\n\tv_fma_f32 v127, v95, v21, v20\n\t"
            "s_setpc_b64 s[76:77]\n\t.LG1:\nv_exp_f32 v32, v96\n\tv_exp_f32 v33, v97\n\tv_exp_f32 v34, v98\n\tv_exp_f32 v35, v99\n\t"
            "v_exp_f32 v36, v100\n\tv_exp_f32 v37, v101\n\tv_exp_f32 v38, v102\n\tv_exp_f32 v39, v103\n\tv_exp_f32 v40, v104\n\tv_exp_f32 v41, v105\n\t"
            "v_exp_f32 v42, v106\n\tv_exp_f32 v43, v107\n\tv_exp_f32 v44, v108\n\tv_exp_f32 v45, v109\n\tv_exp_f32 v46, v110\n\tv_exp_f32 v47, v111\n\t"
            "v_exp_f32 v48, v112\n\tv_exp_f32 v49, v113\n\tv_exp_f32 v50, v114\n\tv_exp_f32 v51, v115\n\tv_exp_f32 v52, v116\n\tv_exp_f32 v53, v117\n\t"
            "v_exp_f32 v54, v118\n\tv_exp_f32 v55, v119\n\tv_exp_f32 v56, v120\n\tv_exp_f32 v57, v121\n\tv_exp_f32 v58, v122\n\tv_exp_f32 v59, v123\n\t"
            "v_exp_f32 v60, v124\n\tv_exp_f32 v61, v125\n\tv_exp_f32 v62, v126\n\tv_exp_f32 v63, v127\n\tv_add_f32 v32, 1.0, v32\n\tv_add_f32 v33, 1.0, v33\n\t"
            "v_add_f32 v34, 1.0, v34\n\tv_add_f32 v35, 1.0, v35\n\tv_add_f32 v36, 1.0, v36\n\tv_add_f32 v37, 1.0, v37\n\tv_add_f32 v38, 1.0, v38\n\tv_add_f32 v39, 1.0, v39\n\t"
            "v_add_f32 v40, 1.0, v40\n\tv_add_f32 v41, 1.0, v41\n\tv_add_f32 v42, 1.0, v42\n\tv_add_f32 v43, 1.0, v43\n\tv_add_f32 v44, 1.0, v44\n\tv_add_f32 v45, 1.0, v45\n\t"
            "v_add_f32 v46, 1.0, v46\n\tv_add_f32 v47, 1.0, v47\n\tv_add_f32 v48, 1.0, v48\n\tv_add_f32 v49, 1.0, v49\n\tv_add_f32 v50, 1.0, v50\n\tv_add_f32 v51, 1.0, v51\n\t"
            "v_add_f32 v52, 1.0, v52\n\tv_add_f32 v53, 1.0, v53\n\tv_add_f32 v54, 1.0, v54\n\tv_add_f32 v55, 1.0, v55\n\tv_add_f32 v56, 1.0, v56\n\tv_add_f32 v57, 1.0, v57\n\t"
            "s_mov_b32 s83, 1\n\ts_setpc_b64 s[76:77]\n\t.LG2:\nv_add_f32 v58, 1.0, v58\n\tv_add_f32 v59, 1.0, v59\n\tv_add_f32 v60, 1.0, v60\n\t"
            "v_add_f32 v61, 1.0, v61\n\tv_add_f32 v62, 1.0, v62\n\tv_add_f32 v63, 1.0, v63\n\tv_rcp_f32 v32, v32\n\tv_rcp_f32 v33, v33\n\tv_rcp_f32 v34, v34\n\t"
            "v_rcp_f32 v35, v35\n\tv_rcp_f32 v36, v36\n\tv_rcp_f32 v37, v37\n\tv_rcp_f32 v38, v38\n\tv_rcp_f32 v39, v39\n\tv_rcp_f32 v40, v40\n\t"
            "v_rcp_f32 v41, v41\n\tv_rcp_f32 v42, v42\n\tv_rcp_f32 v43, v43\n\tv_rcp_f32 v44, v44\n\tv_rcp_f32 v45, v45\n\tv_rcp_f32 v46, v46\n\t"
            "v_rcp_f32 v47, v47\n\tv_rcp_f32 v48, v48\n\tv_rcp_f32 v49, v49\n\tv_rcp_f32 v50, v50\n\tv_rcp_f32 v51, v51\n\tv_rcp_f32 v52, v52\n\t"
            "v_rcp_f32 v53, v53\n\tv_rcp_f32 v54, v54\n\tv_rcp_f32 v55, v55\n\tv_rcp_f32 v56, v56\n\tv_rcp_f32 v57, v57\n\tv_rcp_f32 v58, v58\n\t"
            "v_rcp_f32 v59, v59\n\tv_rcp_f32 v60, v60\n\tv_rcp_f32 v61, v61\n\tv_rcp_f32 v62, v62\n\tv_rcp_f32 v63, v63\n\tv_mul_f32 v32, v32, v96\n\t"
            "v_mul_f32 v33, v33, v97\n\tv_mul_f32 v34, v34, v98\n\tv_mul_f32 v35, v35, v99\n\tv_mul_f32 v36, v36, v100\n\tv_mul_f32 v37, v37, v101\n\tv_mul_f32 v38, v38, v102\n\t"
            "v_mul_f32 v39, v39, v103\n\tv_mul_f32 v40, v40, v104\n\tv_mul_f32 v41, v41, v105\n\tv_mul_f32 v42, v42, v106\n\tv_mul_f32 v43, v43, v107\n\tv_mul_f32 v44, v44, v108\n\t"
            "v_mul_f32 v45, v45, v109\n\tv_mul_f32 v46, v46, v110\n\tv_mul_f32 v47, v47, v111\n\ts_mov_b32 s83, 2\n\ts_setpc_b64 s[76:77]\n\t7:\n"
            "ds_read_b32 v16, v8 offset:64\n\tds_read_b32 v17, v9 offset:1344\n\ts_waitcnt lgkmcnt(0)\n\tv_cmp_ne_u32 s[74:75], 0, v17\n\tv_cmp_eq_u32 vcc, 0, v16\n\ts_cbranch_vccz 71f\n\t"
            "s_sleep 1\n\ts_sub_u32 s68, s68, 1\n\ts_cbranch_scc0 7b\n\ts_branch 9b\n\t71:\nv_lshrrev_b32 v12, 5, v2\n\t"
            "v_and_b32 v12, 16, v12\n\tv_add_u32 v12, v12, v17\n\ts_pack_ll_b32_b16 s78, s74, s74\n\ts_mov_b32 s79, s78\n\ts_pack_hh_b32_b16 s80, s74, s74\n\ts_mov_b32 s81, s80\n\t"
            "v_cndmask_b32 v3, v3, 2.0, s[78:79]\n\tv_cndmask_b32 v11, v11, 2.0, s[80:81]\n\ts_mov_b64 exec, s[74:75]\n\ts_cbranch_execz 72f\n\tds_write_b128 v12, v[96:99] offset:0\n\tds_write_b128 v12, v[100:103] offset:32\n\t"
            "ds_write_b128 v12, v[104:107] offset:64\n\tds_write_b128 v12, v[108:111] offset:96\n\t72:\ns_mov_b64 exec, -1\n\ts_branch 8b\n\t73:\n"
            "s_nop 7\n\ts_nop 3\n\ts_mov_b64 exec, s[74:75]\n\tds_write_b128 v12, v[112:115] offset:128\n\tds_write_b128 v12, v[116:119] offset:160\n\tds_write_b128 v12, v[120:123] offset:192\n\t"
            "ds_write_b128 v12, v[124:127] offset:224\n\tds_write_b32 v17, v17 offset:256\n\ts_mov_b64 exec, -1\n\ts_branch 74b\n\t5:\ns_sleep 1\n\t"
            "ds_read_b64 v[14:15], v8 offset:40\n\ts_waitcnt lgkmcnt(0)\n\tv_min_u32 v14, v14, v15\n\tv_cmp_ge_u32 vcc, s71, v14\n\ts_cbranch_vccz 61f\n\ts_sub_u32 s68, s68, 1\n\t"
            "s_cbranch_scc0 5b\n\ts_branch 9b\n\t61:\n"
            "s_branch 6b\n\t62:\nds_read_b128 v[16:19], v8 offset:48\n\tds_read_b64_tr_b16 v[128:129], v4 offset:40960\n\tds_read_b64_tr_b16 v[130:131], v4 offset:41472\n\tds_read_b64_tr_b16 v[132:133], v5 offset:40960\n\t"
            "ds_read_b64_tr_b16 v[134:135], v5 offset:41472\n\tds_read_b64_tr_b16 v[136:137], v6 offset:40960\n\tds_read_b64_tr_b16 v[138:139], v6 offset:41472\n\tds_read_b64_tr_b16 v[140:141], v7 offset:40960\n\tds_read_b64_tr_b16 v[142:143], v7 offset:41472\n\tds_read_b64_tr_b16 v[144:145], v4 offset:41984\n\t"
            "ds_read_b64_tr_b16 v[146:147], v4 offset:42496\n\tds_read_b64_tr_b16 v[148:149], v5 offset:41984\n\tds_read_b64_tr_b16 v[150:151], v5 offset:42496\n\tds_read_b64_tr_b16 v[152:153], v6 offset:41984\n\tds_read_b64_tr_b16 v[154:155], v6 offset:42496\n\tds_read_b64_tr_b16 v[156:157], v7 offset:41984\n\t"
            "ds_read_b64_tr_b16 v[158:159], v7 offset:42496\n\ts_waitcnt lgkmcnt(15)\n\tv_min3_u32 v16, v16, v17, v18\n\tv_min_u32 v16, v16, v19\n\tv_cmp_eq_u32 vcc, 0, v16\n\ts_cbranch_vccz 63b\n\t"
            "s_sub_u32 s68, s68, 1\n\ts_cbranch_scc0 62b\n\ts_branch 9b\n\t100:\ns_mov_b32 s70, 4\n\ts_call_b64 s[72:73], 300f\n\t"
            "v_max3_u32 v12, v176, v178, v180\n\tv_max_u32 v12, v12, v182\n\tv_cmp_eq_u32 vcc, -1, v12\n\ts_cbranch_vccnz 100b\n\ts_branch 200b\n\t101:\n"
            "s_mov_b32 s70, 5\n\ts_call_b64 s[72:73], 301f\n\tv_max3_u32 v12, v184, v186, v188\n\tv_max_u32 v12, v12, v190\n\tv_cmp_eq_u32 vcc, -1, v12\n\ts_cbranch_vccnz 101b\n\t"
            "s_branch 201b\n\t102:\ns_mov_b32 s70, 6\n\ts_call_b64 s[72:73], 302f\n\tv_max3_u32 v12, v192, v194, v196\n\tv_max_u32 v12, v12, v198\n\t"
            "v_cmp_eq_u32 vcc, -1, v12\n\ts_cbranch_vccnz 102b\n\ts_branch 202b\n\t103:\ns_mov_b32 s70, 7\n\ts_call_b64 s[72:73], 303f\n\t"
            "v_max3_u32 v12, v200, v202, v204\n\tv_max_u32 v12, v12, v206\n\tv_cmp_eq_u32 vcc, -1, v12\n\ts_cbranch_vccnz 103b\n\ts_branch 203b\n\t104:\n"
            "s_mov_b32 s70, 8\n\ts_call_b64 s[72:73], 304f\n\tv_max3_u32 v12, v208, v210, v212\n\tv_max_u32 v12, v12, v214\n\tv_cmp_eq_u32 vcc, -1, v12\n\ts_cbranch_vccnz 104b\n\t"
            "s_branch 204b\n\t105:\ns_mov_b32 s70, 9\n\ts_call_b64 s[72:73], 305f\n\tv_max3_u32 v12, v216, v218, v220\n\tv_max_u32 v12, v12, v222\n\t"
            "v_cmp_eq_u32 vcc, -1, v12\n\ts_cbranch_vccnz 105b\n\ts_branch 205b\n\t106:\ns_mov_b32 s70, 10\n\ts_call_b64 s[72:73], 306f\n\t"
            "v_max3_u32 v12, v224, v226, v228\n\tv_max_u32 v12, v12, v230\n\tv_cmp_eq_u32 vcc, -1, v12\n\ts_cbranch_vccnz 106b\n\ts_branch 206b\n\t107:\n"
            "s_mov_b32 s70, 11\n\ts_call_b64 s[72:73], 307f\n\tv_max3_u32 v12, v232, v234, v236\n\tv_max_u32 v12, v12, v238\n\tv_cmp_eq_u32 vcc, -1, v12\n\ts_cbranch_vccnz 107b\n\t"
            "s_branch 207b\n\t108:\ns_mov_b32 s70, 12\n\ts_call_b64 s[72:73], 308f\n\tv_max3_u32 v12, v176, v178, v180\n\tv_max_u32 v12, v12, v182\n\t"
            "v_cmp_eq_u32 vcc, -1, v12\n\ts_cbranch_vccnz 108b\n\ts_branch 208b\n\t109:\ns_mov_b32 s70, 13\n\ts_call_b64 s[72:73], 309f\n\t"
            "v_max3_u32 v12, v184, v186, v188\n\tv_max_u32 v12, v12, v190\n\tv_cmp_eq_u32 vcc, -1, v12\n\ts_cbranch_vccnz 109b\n\ts_branch 209b\n\t110:\n"
            "s_mov_b32 s70, 14\n\ts_call_b64 s[72:73], 310f\n\tv_max3_u32 v12, v192, v194, v196\n\tv_max_u32 v12, v12, v198\n\tv_cmp_eq_u32 vcc, -1, v12\n\ts_cbranch_vccnz 110b\n\t"
            "s_branch 210b\n\t111:\ns_mov_b32 s70, 15\n\ts_call_b64 s[72:73], 311f\n\tv_max3_u32 v12, v200, v202, v204\n\tv_max_u32 v12, v12, v206\n\t"
            "v_cmp_eq_u32 vcc, -1, v12\n\ts_cbranch_vccnz 111b\n\ts_branch 211b\n\t112:\ns_mov_b32 s70, 16\n\ts_call_b64 s[72:73], 312f\n\t"
            "v_max3_u32 v12, v208, v210, v212\n\tv_max_u32 v12, v12, v214\n\tv_cmp_eq_u32 vcc, -1, v12\n\ts_cbranch_vccnz 112b\n\ts_branch 212b\n\t113:\n"
            "s_mov_b32 s70, 17\n\ts_call_b64 s[72:73], 313f\n\tv_max3_u32 v12, v216, v218, v220\n\tv_max_u32 v12, v12, v222\n\tv_cmp_eq_u32 vcc, -1, v12\n\ts_cbranch_vccnz 113b\n\t"
            "s_branch 213b\n\t114:\ns_mov_b32 s70, 18\n\ts_call_b64 s[72:73], 314f\n\tv_max3_u32 v12, v224, v226, v228\n\tv_max_u32 v12, v12, v230\n\t"
            "v_cmp_eq_u32 vcc, -1, v12\n\ts_cbranch_vccnz 114b\n\ts_branch 214b\n\t115:\ns_mov_b32 s70, 19\n\ts_call_b64 s[72:73], 315f\n\t"
            "v_max3_u32 v12, v232, v234, v236\n\tv_max_u32 v12, v12, v238\n\tv_cmp_eq_u32 vcc, -1, v12\n\ts_cbranch_vccnz 115b\n\ts_branch 215b\n\t116:\n"
            "s_mov_b32 s70, 19\n\ts_call_b64 s[72:73], 316f\n\tv_max3_u32 v12, v176, v178, v180\n\tv_max_u32 v12, v12, v182\n\tv_cmp_eq_u32 vcc, -1, v12\n\ts_cbranch_vccnz 116b\n\t"
            "s_branch 216b\n\t117:\ns_mov_b32 s70, 19\n\ts_call_b64 s[72:73], 317f\n\tv_max3_u32 v12, v184, v186, v188\n\tv_max_u32 v12, v12, v190\n\t"
            "v_cmp_eq_u32 vcc, -1, v12\n\ts_cbranch_vccnz 117b\n\ts_branch 217b\n\t118:\ns_mov_b32 s70, 19\n\ts_call_b64 s[72:73], 318f\n\t"
            "v_max3_u32 v12, v192, v194, v196\n\tv_max_u32 v12, v12, v198\n\tv_cmp_eq_u32 vcc, -1, v12\n\ts_cbranch_vccnz 118b\n\ts_branch 218b\n\t119:\n"
            "s_mov_b32 s70, 19\n\ts_call_b64 s[72:73], 319f\n\tv_max3_u32 v12, v200, v202, v204\n\tv_max_u32 v12, v12, v206\n\tv_cmp_eq_u32 vcc, -1, v12\n\ts_cbranch_vccnz 119b\n\t"
            "s_branch 219b\n\t400:\nv_max3_u32 v12, v176, v178, v180\n\tv_max_u32 v12, v12, v182\n\tv_cmp_eq_u32 vcc, -1, v12\n\ts_cbranch_vccz 11f\n\t"
            "buffer_load_dwordx4 v[176:179], v2, s[44:47], s4 offen offset:1024 sc1\n\tbuffer_load_dwordx4 v[180:183], v2, s[44:47], s4 offen offset:2048 sc1\n\ts_branch 11f\n\t300:\nbuffer_load_dwordx4 v[176:179], v2, s[44:47], s4 offen offset:1024 sc1\n\tbuffer_load_dwordx4 v[180:183], v2, s[44:47], s4 offen offset:2048 sc1\n\t"
            "s_waitcnt vmcnt(2)\n\t11:\ns_cmp_eq_u32 s70, 0\n\ts_cbranch_scc0 401f\n\ts_branch 12f\n\t401:\n"
            "v_max3_u32 v12, v184, v186, v188\n\tv_max_u32 v12, v12, v190\n\tv_cmp_eq_u32 vcc, -1, v12\n\ts_cbranch_vccz 11f\n\tbuffer_load_dwordx4 v[184:187], v2, s[44:47], s5 offen offset:1024 sc1\n\tbuffer_load_dwordx4 v[188:191], v2, s[44:47], s5 offen offset:2048 sc1\n\t"
            "s_branch 11f\n\t301:\nbuffer_load_dwordx4 v[184:187], v2, s[44:47], s5 offen offset:1024 sc1\n\tbuffer_load_dwordx4 v[188:191], v2, s[44:47], s5 offen offset:2048 sc1\n\ts_waitcnt vmcnt(2)\n\t11:\n"
            "s_cmp_eq_u32 s70, 1\n\ts_cbranch_scc0 402f\n\ts_branch 12f\n\t402:\nv_max3_u32 v12, v192, v194, v196\n\tv_max_u32 v12, v12, v198\n\t"
            "v_cmp_eq_u32 vcc, -1, v12\n\ts_cbranch_vccz 11f\n\tbuffer_load_dwordx4 v[192:195], v2, s[44:47], s6 offen offset:1024 sc1\n\tbuffer_load_dwordx4 v[196:199], v2, s[44:47], s6 offen offset:2048 sc1\n\ts_branch 11f\n\t302:\n"
            "buffer_load_dwordx4 v[192:195], v2, s[44:47], s6 offen offset:1024 sc1\n\tbuffer_load_dwordx4 v[196:199], v2, s[44:47], s6 offen offset:2048 sc1\n\ts_waitcnt vmcnt(2)\n\t11:\ns_cmp_eq_u32 s70, 2\n\ts_cbranch_scc0 403f\n\t"
            "s_branch 12f\n\t403:\nv_max3_u32 v12, v200, v202, v204\n\tv_max_u32 v12, v12, v206\n\tv_cmp_eq_u32 vcc, -1, v12\n\ts_cbranch_vccz 11f\n\t"
            "buffer_load_dwordx4 v[200:203], v2, s[44:47], s7 offen offset:1024 sc1\n\tbuffer_load_dwordx4 v[204:207], v2, s[44:47], s7 offen offset:2048 sc1\n\ts_branch 11f\n\t303:\nbuffer_load_dwordx4 v[200:203], v2, s[44:47], s7 offen offset:1024 sc1\n\tbuffer_load_dwordx4 v[204:207], v2, s[44:47], s7 offen offset:2048 sc1\n\t"
            "s_waitcnt vmcnt(2)\n\t11:\ns_cmp_eq_u32 s70, 3\n\ts_cbranch_scc0 404f\n\ts_branch 12f\n\t404:\n"
            "v_max3_u32 v12, v208, v210, v212\n\tv_max_u32 v12, v12, v214\n\tv_cmp_eq_u32 vcc, -1, v12\n\ts_cbranch_vccz 11f\n\tbuffer_load_dwordx4 v[208:211], v2, s[44:47], s8 offen offset:1024 sc1\n\tbuffer_load_dwordx4 v[212:215], v2, s[44:47], s8 offen offset:2048 sc1\n\t"
            "s_branch 11f\n\t304:\nbuffer_load_dwordx4 v[208:211], v2, s[44:47], s8 offen offset:1024 sc1\n\tbuffer_load_dwordx4 v[212:215], v2, s[44:47], s8 offen offset:2048 sc1\n\ts_waitcnt vmcnt(2)\n\t11:\n"
            "s_cmp_eq_u32 s70, 4\n\ts_cbranch_scc0 405f\n\ts_branch 12f\n\t405:\nv_max3_u32 v12, v216, v218, v220\n\tv_max_u32 v12, v12, v222\n\t"
            "v_cmp_eq_u32 vcc, -1, v12\n\ts_cbranch_vccz 11f\n\tbuffer_load_dwordx4 v[216:219], v2, s[44:47], s9 offen offset:1024 sc1\n\tbuffer_load_dwordx4 v[220:223], v2, s[44:47], s9 offen offset:2048 sc1\n\ts_branch 11f\n\t305:\n"
            "buffer_load_dwordx4 v[216:219], v2, s[44:47], s9 offen offset:1024 sc1\n\tbuffer_load_dwordx4 v[220:223], v2, s[44:47], s9 offen offset:2048 sc1\n\ts_waitcnt vmcnt(2)\n\t11:\ns_cmp_eq_u32 s70, 5\n\ts_cbranch_scc0 406f\n\t"
            "s_branch 12f\n\t406:\nv_max3_u32 v12, v224, v226, v228\n\tv_max_u32 v12, v12, v230\n\tv_cmp_eq_u32 vcc, -1, v12\n\ts_cbranch_vccz 11f\n\t"
            "buffer_load_dwordx4 v[224:227], v2, s[44:47], s10 offen offset:1024 sc1\n\tbuffer_load_dwordx4 v[228:231], v2, s[44:47], s10 offen offset:2048 sc1\n\ts_branch 11f\n\t306:\nbuffer_load_dwordx4 v[224:227], v2, s[44:47], s10 offen offset:1024 sc1\n\tbuffer_load_dwordx4 v[228:231], v2, s[44:47], s10 offen offset:2048 sc1\n\t"
            "s_waitcnt vmcnt(2)\n\t11:\ns_cmp_eq_u32 s70, 6\n\ts_cbranch_scc0 407f\n\ts_branch 12f\n\t407:\n"
            "v_max3_u32 v12, v232, v234, v236\n\tv_max_u32 v12, v12, v238\n\tv_cmp_eq_u32 vcc, -1, v12\n\ts_cbranch_vccz 11f\n\tbuffer_load_dwordx4 v[232:235], v2, s[44:47], s11 offen offset:1024 sc1\n\tbuffer_load_dwordx4 v[236:239], v2, s[44:47], s11 offen offset:2048 sc1\n\t"
            "s_branch 11f\n\t307:\nbuffer_load_dwordx4 v[232:235], v2, s[44:47], s11 offen offset:1024 sc1\n\tbuffer_load_dwordx4 v[236:239], v2, s[44:47], s11 offen offset:2048 sc1\n\ts_waitcnt vmcnt(2)\n\t11:\n"
            "s_cmp_eq_u32 s70, 7\n\ts_cbranch_scc0 408f\n\ts_branch 12f\n\t408:\nv_max3_u32 v12, v176, v178, v180\n\tv_max_u32 v12, v12, v182\n\t"
            "v_cmp_eq_u32 vcc, -1, v12\n\ts_cbranch_vccz 11f\n\tbuffer_load_dwordx4 v[176:179], v2, s[44:47], s12 offen offset:1024 sc1\n\tbuffer_load_dwordx4 v[180:183], v2, s[44:47], s12 offen offset:2048 sc1\n\ts_branch 11f\n\t308:\n"
            "buffer_load_dwordx4 v[176:179], v2, s[44:47], s12 offen offset:1024 sc1\n\tbuffer_load_dwordx4 v[180:183], v2, s[44:47], s12 offen offset:2048 sc1\n\ts_waitcnt vmcnt(2)\n\t11:\ns_cmp_eq_u32 s70, 8\n\ts_cbranch_scc0 409f\n\t"
            "s_branch 12f\n\t409:\nv_max3_u32 v12, v184, v186, v188\n\tv_max_u32 v12, v12, v190\n\tv_cmp_eq_u32 vcc, -1, v12\n\ts_cbranch_vccz 11f\n\t"
            "buffer_load_dwordx4 v[184:187], v2, s[44:47], s13 offen offset:1024 sc1\n\tbuffer_load_dwordx4 v[188:191], v2, s[44:47], s13 offen offset:2048 sc1\n\ts_branch 11f\n\t309:\nbuffer_load_dwordx4 v[184:187], v2, s[44:47], s13 offen offset:1024 sc1\n\tbuffer_load_dwordx4 v[188:191], v2, s[44:47], s13 offen offset:2048 sc1\n\t"
            "s_waitcnt vmcnt(2)\n\t11:\ns_cmp_eq_u32 s70, 9\n\ts_cbranch_scc0 410f\n\ts_branch 12f\n\t410:\n"
            "v_max3_u32 v12, v192, v194, v196\n\tv_max_u32 v12, v12, v198\n\tv_cmp_eq_u32 vcc, -1, v12\n\ts_cbranch_vccz 11f\n\tbuffer_load_dwordx4 v[192:195], v2, s[44:47], s4 offen offset:0 sc1\n\tbuffer_load_dwordx4 v[196:199], v2, s[44:47], s4 offen offset:3072 sc1\n\t"
            "s_branch 11f\n\t310:\nbuffer_load_dwordx4 v[192:195], v2, s[44:47], s4 offen offset:0 sc1\n\tbuffer_load_dwordx4 v[196:199], v2, s[44:47], s4 offen offset:3072 sc1\n\ts_waitcnt vmcnt(2)\n\t11:\n"
            "s_cmp_eq_u32 s70, 10\n\ts_cbranch_scc0 411f\n\ts_branch 12f\n\t411:\nv_max3_u32 v12, v200, v202, v204\n\tv_max_u32 v12, v12, v206\n\t"
            "v_cmp_eq_u32 vcc, -1, v12\n\ts_cbranch_vccz 11f\n\tbuffer_load_dwordx4 v[200:203], v2, s[44:47], s5 offen offset:0 sc1\n\tbuffer_load_dwordx4 v[204:207], v2, s[44:47], s5 offen offset:3072 sc1\n\ts_branch 11f\n\t311:\n"
            "buffer_load_dwordx4 v[200:203], v2, s[44:47], s5 offen offset:0 sc1\n\tbuffer_load_dwordx4 v[204:207], v2, s[44:47], s5 offen offset:3072 sc1\n\ts_waitcnt vmcnt(2)\n\t11:\ns_cmp_eq_u32 s70, 11\n\ts_cbranch_scc0 412f\n\t"
            "s_branch 12f\n\t412:\nv_max3_u32 v12, v208, v210, v212\n\tv_max_u32 v12, v12, v214\n\tv_cmp_eq_u32 vcc, -1, v12\n\ts_cbranch_vccz 11f\n\t"
            "buffer_load_dwordx4 v[208:211], v2, s[44:47], s6 offen offset:0 sc1\n\tbuffer_load_dwordx4 v[212:215], v2, s[44:47], s6 offen offset:3072 sc1\n\ts_branch 11f\n\t312:\nbuffer_load_dwordx4 v[208:211], v2, s[44:47], s6 offen offset:0 sc1\n\tbuffer_load_dwordx4 v[212:215], v2, s[44:47], s6 offen offset:3072 sc1\n\t"
            "s_waitcnt vmcnt(2)\n\t11:\ns_cmp_eq_u32 s70, 12\n\ts_cbranch_scc0 413f\n\ts_branch 12f\n\t413:\n"
            "v_max3_u32 v12, v216, v218, v220\n\tv_max_u32 v12, v12, v222\n\tv_cmp_eq_u32 vcc, -1, v12\n\ts_cbranch_vccz 11f\n\tbuffer_load_dwordx4 v[216:219], v2, s[44:47], s7 offen offset:0 sc1\n\tbuffer_load_dwordx4 v[220:223], v2, s[44:47], s7 offen offset:3072 sc1\n\t"
            "s_branch 11f\n\t313:\nbuffer_load_dwordx4 v[216:219], v2, s[44:47], s7 offen offset:0 sc1\n\tbuffer_load_dwordx4 v[220:223], v2, s[44:47], s7 offen offset:3072 sc1\n\ts_waitcnt vmcnt(2)\n\t11:\n"
            "s_cmp_eq_u32 s70, 13\n\ts_cbranch_scc0 414f\n\ts_branch 12f\n\t414:\nv_max3_u32 v12, v224, v226, v228\n\tv_max_u32 v12, v12, v230\n\t"
            "v_cmp_eq_u32 vcc, -1, v12\n\ts_cbranch_vccz 11f\n\tbuffer_load_dwordx4 v[224:227], v2, s[44:47], s8 offen offset:0 sc1\n\tbuffer_load_dwordx4 v[228:231], v2, s[44:47], s8 offen offset:3072 sc1\n\ts_branch 11f\n\t314:\n"
            "buffer_load_dwordx4 v[224:227], v2, s[44:47], s8 offen offset:0 sc1\n\tbuffer_load_dwordx4 v[228:231], v2, s[44:47], s8 offen offset:3072 sc1\n\ts_waitcnt vmcnt(2)\n\t11:\ns_cmp_eq_u32 s70, 14\n\ts_cbranch_scc0 415f\n\t"
            "s_branch 12f\n\t415:\nv_max3_u32 v12, v232, v234, v236\n\tv_max_u32 v12, v12, v238\n\tv_cmp_eq_u32 vcc, -1, v12\n\ts_cbranch_vccz 11f\n\t"
            "buffer_load_dwordx4 v[232:235], v2, s[44:47], s9 offen offset:0 sc1\n\tbuffer_load_dwordx4 v[236:239], v2, s[44:47], s9 offen offset:3072 sc1\n\ts_branch 11f\n\t315:\nbuffer_load_dwordx4 v[232:235], v2, s[44:47], s9 offen offset:0 sc1\n\tbuffer_load_dwordx4 v[236:239], v2, s[44:47], s9 offen offset:3072 sc1\n\t"
            "s_waitcnt vmcnt(2)\n\t11:\ns_cmp_eq_u32 s70, 15\n\ts_cbranch_scc0 416f\n\ts_branch 12f\n\t416:\n"
            "v_max3_u32 v12, v176, v178, v180\n\tv_max_u32 v12, v12, v182\n\tv_cmp_eq_u32 vcc, -1, v12\n\ts_cbranch_vccz 11f\n\tbuffer_load_dwordx4 v[176:179], v2, s[44:47], s10 offen offset:0 sc1\n\tbuffer_load_dwordx4 v[180:183], v2, s[44:47], s10 offen offset:3072 sc1\n\t"
            "s_branch 11f\n\t316:\nbuffer_load_dwordx4 v[176:179], v2, s[44:47], s10 offen offset:0 sc1\n\tbuffer_load_dwordx4 v[180:183], v2, s[44:47], s10 offen offset:3072 sc1\n\ts_waitcnt vmcnt(2)\n\t11:\n"
            "s_cmp_eq_u32 s70, 16\n\ts_cbranch_scc0 417f\n\ts_branch 12f\n\t417:\nv_max3_u32 v12, v184, v186, v188\n\tv_max_u32 v12, v12, v190\n\t"
            "v_cmp_eq_u32 vcc, -1, v12\n\ts_cbranch_vccz 11f\n\tbuffer_load_dwordx4 v[184:187], v2, s[44:47], s11 offen offset:0 sc1\n\tbuffer_load_dwordx4 v[188:191], v2, s[44:47], s11 offen offset:3072 sc1\n\ts_branch 11f\n\t317:\n"
            "buffer_load_dwordx4 v[184:187], v2, s[44:47], s11 offen offset:0 sc1\n\tbuffer_load_dwordx4 v[188:191], v2, s[44:47], s11 offen offset:3072 sc1\n\ts_waitcnt vmcnt(2)\n\t11:\ns_cmp_eq_u32 s70, 17\n\ts_cbranch_scc0 418f\n\t"
            "s_branch 12f\n\t418:\nv_max3_u32 v12, v192, v194, v196\n\tv_max_u32 v12, v12, v198\n\tv_cmp_eq_u32 vcc, -1, v12\n\ts_cbranch_vccz 11f\n\t"
            "buffer_load_dwordx4 v[192:195], v2, s[44:47], s12 offen offset:0 sc1\n\tbuffer_load_dwordx4 v[196:199], v2, s[44:47], s12 offen offset:3072 sc1\n\ts_branch 11f\n\t318:\nbuffer_load_dwordx4 v[192:195], v2, s[44:47], s12 offen offset:0 sc1\n\tbuffer_load_dwordx4 v[196:199], v2, s[44:47], s12 offen offset:3072 sc1\n\t"
            "s_waitcnt vmcnt(2)\n\t11:\ns_cmp_eq_u32 s70, 18\n\ts_cbranch_scc0 419f\n\ts_branch 12f\n\t419:\n"
            "v_max3_u32 v12, v200, v202, v204\n\tv_max_u32 v12, v12, v206\n\tv_cmp_eq_u32 vcc, -1, v12\n\ts_cbranch_vccz 11f\n\tbuffer_load_dwordx4 v[200:203], v2, s[44:47], s13 offen offset:0 sc1\n\tbuffer_load_dwordx4 v[204:207], v2, s[44:47], s13 offen offset:3072 sc1\n\t"
            "s_branch 11f\n\t319:\nbuffer_load_dwordx4 v[200:203], v2, s[44:47], s13 offen offset:0 sc1\n\tbuffer_load_dwordx4 v[204:207], v2, s[44:47], s13 offen offset:3072 sc1\n\ts_waitcnt vmcnt(2)\n\t11:\n"
            "12:\n"
            "s_sub_u32 s68, s68, 1\n\ts_cbranch_scc1 9b\n\ts_waitcnt vmcnt(0)\n\ts_setpc_b64 s[72:73]\n\t.subsection 1\n\t.Lce:\n"
            ".subsection 0\n\t.Lout:\ns_waitcnt lgkmcnt(0)\n\t"
            : "=&{s67}"(tag), "=&{v[52:83]}"(sa), "=&{v[84:115]}"(sb), "=&{v[116:123]}"(sc) : [t] "v"(tid), [b] "s"(bid), [k] "s"(ka)
            : "memory", "scc", "vcc", "s4", "s5", "s6", "s7", "s8", "s9", "s10", "s11", "s12", "s13", "s14", "s15", "s16", "s17", "s18", "s19", "s20", "s21", "s22", "s23", "s24", "s25", "s26", "s27", "s28", "s29", "s30", "s31", "s33", "s34", "s35", "s36", "s37", "s38", "s39", "s40", "s41", "s42", "s43", "s44", "s45", "s46", "s47", "s48", "s49", "s50", "s51", "s52", "s53", "s54", "s55", "s56", "s57", "s58", "s59", "s60", "s61", "s62", "s63", "s64", "s65", "s66", "s68", "s69", "s70", "s71", "s72", "s73", "s74", "s75", "s76", "s77", "s78", "s79", "s80", "s81", "s82", "s83", "v2", "v3", "v4", "v5", "v6", "v7", "v8", "v9", "v10", "v11", "v12", "v13", "v14", "v15", "v16", "v17", "v18", "v19", "v20", "v21", "v22", "v23", "v24", "v25", "v26", "v27", "v28", "v29", "v30", "v31", "v32", "v33", "v34", "v35", "v36", "v37", "v38", "v39", "v40", "v41", "v42", "v43", "v44", "v45", "v46", "v47", "v48", "v49", "v50", "v51", "v124", "v125", "v126", "v127", "v128", "v129", "v130", "v131", "v132", "v133", "v134", "v135", "v136", "v137", "v138", "v139", "v140", "v141", "v142", "v143", "v144", "v145", "v146", "v147", "v148", "v149", "v150", "v151", "v152", "v153", "v154", "v155", "v156", "v157", "v158", "v159", "v160", "v161", "v162", "v163", "v164", "v165", "v166", "v167", "v168", "v169", "v170", "v171", "v172", "v173", "v174", "v175", "v176", "v177", "v178", "v179", "v180", "v181", "v182", "v183", "v184", "v185", "v186", "v187", "v188", "v189", "v190", "v191", "v192", "v193", "v194", "v195", "v196", "v197", "v198", "v199", "v200", "v201", "v202", "v203", "v204", "v205", "v206", "v207", "v208", "v209", "v210", "v211", "v212", "v213", "v214", "v215", "v216", "v217", "v218", "v219", "v220", "v221", "v222", "v223", "v224", "v225", "v226", "v227", "v228", "v229", "v230", "v231", "v232", "v233", "v234", "v235", "v236", "v237", "v238", "v239", "v240", "v241", "v242", "v243", "v244", "v245", "v246", "v247", "v248", "v249", "v250", "v251", "v252", "v253", "v254", "v255");
    tag = RFL(tag);
    asm volatile("" : "+s"(bid), "+s"(ka)); asm volatile("" : "+v"(tid));
    const u16 *x = (const u16*)ka[0], *wo = (const u16*)ka[2], *w_r = (const u16*)ka[3], *wg = (const u16*)ka[4], *wu = (const u16*)ka[5], *wd = (const u16*)ka[6]; char* ws = (char*)ka[7];
    const int wv = __builtin_amdgcn_readfirstlane(tid >> 6);
    const unsigned lds0 = (unsigned)(size_t)(lchar*)smem;
#define LANE() const int lane = lane_id(); const int g = lane >> 4, li = lane & 15; (void)g; (void)li
    const int xcd = bid & 7, j = ((bid >> 3) + 25) & 31;
    const bool gemm = j < 25;
    const int fp = xcd >> 1, bl = (j * 13) >> 6, cl = j - bl * 5;
    const int cg = (xcd & 1) * 5 + cl;
    const int par = fp >> 1, I0 = ((fp & 1) * 5 + bl) * 104 - 8;
    const __amdgpu_buffer_rsrc_t rs = __builtin_amdgcn_make_buffer_rsrc((void*)ws, 0, W_END, 0x00020000);
    auto wait_flag = [&](unsigned a) { SPIN { if (RFL(ldsrv<unsigned>(a))) break; __builtin_amdgcn_s_sleep(1); } MEMB(); };
    unsigned ep = 0;
    auto epld = [&]() { asm volatile("s_load_dword %0, %1, 0x0" : "=s"(ep) : "s"(ws) : "memory"); };
    auto mktag = [&]() { asm volatile("s_waitcnt lgkmcnt(0)" : "+s"(ep) :: "memory"); tag = ep * 1664525u + 1013904223u; };
        if (gemm) {
            LANE();
            wait_flag(lds0 + FL_W);
            const u32x4 kk = ldsrv<u32x4>(lds0 + L_KEY + lane * 16);
            const uint2 t2 = top2wave(lane < 52 ? kk.x : 0u, lane < 52 ? kk.z : 0u);
            {
                const unsigned rr = (0xffffu - ((lane ? t2.y : t2.x) & 0xffffu)) & 2047u; const int pos = (int)(rr >> 1) - I0;
                if (lane < 2 && (int)(rr & 1) == par && pos >= 8 && pos < 112) ldsw<unsigned>(lds0 + L_RM + ((pos & ~15) + 4 * (pos & 3) + ((pos >> 2) & 3)) * 4, L_RG + lane * 288);
                ldsw<unsigned>(lds0 + FL_R, 1u);
            }
            unsigned rr[2]; bool own[2]; float xr_[2], xn[2], sg[2];
#pragma unroll
            for (int k = 0; k < 2; ++k) { rr[k] = (0xffffu - ((k ? t2.y : t2.x) & 0xffffu)) & 2047u; const int pos = (int)(rr[k] >> 1) - I0; own[k] = (int)(rr[k] & 1) == par && pos >= 8 && pos < 112; }
            if (__builtin_expect(own[0] || own[1], 0)) {
                __builtin_amdgcn_s_setprio(3);
#pragma unroll
                for (int k = 0; k < 2; ++k) {
                    const int pos = own[k] ? (int)(rr[k] >> 1) - I0 : 8, m = (pos & ~15) + 4 * (pos & 3) + ((pos >> 2) & 3);
                    xr_[k] = lo2f(x[rr[k] * 640 + cg * 64 + lane]);
                    sg[k] = rbf(__builtin_amdgcn_rcpf(1.f + __builtin_amdgcn_exp2f(keyscore(k ? t2.y : t2.x) * -1.4426950408889634f))); NOPK(sg[k]);
                    xn[k] = rbf(xr_[k] + ldsrv<float>(lds0 + L_NM + m * 4)); NOPK(xn[k]); xn[k] = rbf(xn[k] * ldsrv<float>(lds0 + L_RS + pos * 4)); OPQ(xn[k]);
                }
                __builtin_amdgcn_s_sleep(18);
                u32x4 y[5];
                SPIN {
                    MEMB();
                    bool old = false;
#pragma unroll
                    for (int i = 0; i < 5; ++i) y[i] = __builtin_amdgcn_raw_buffer_load_b128(rs, W_Y + (cg * 64 + lane) * 16, i * 10240, SC1);
#pragma unroll
                    for (int i = 0; i < 5; ++i) old = old || y[i].y != tag || y[i].w != tag;
                    if (__builtin_amdgcn_ballot_w64(old) == 0) break;
                }
#pragma unroll
                for (int k = 0; k < 2; ++k) if (own[k]) {
                    float ya = __uint_as_float(y[0][2 * k]) + __uint_as_float(y[1][2 * k]); NOPK(ya);
                    const float ys = (ya + (__uint_as_float(y[2][2 * k]) + __uint_as_float(y[3][2 * k]))) + __uint_as_float(y[4][2 * k]);
                    float gt = silu_b(rbf(xn[k] + rbf(rbf(ys) * sg[k]))) * -1.4426950408889634f, sv; OPQ(gt);
                    SPIN {
                        const unsigned fl = ldsrv<unsigned>(lds0 + L_RG + k * 288 + 256); sv = ldsrv<float>(lds0 + L_RG + k * 288 + lane * 4);
                        if (RFL(fl)) break;
                        __builtin_amdgcn_s_sleep(1);
                    }
                    const float o = fmaf(sv, gt, xr_[k]);
                    asm volatile("global_store_short %0, %1, %2 sc1" :: "v"((rr[k] * 640 + cg * 64 + lane) * 2), "v"(cvtpk(o, o)), "s"(ka[8]) : "memory");
                }
            }
            END();
        }
    {
        LANE();
        const int fq = (j - 25) * 2 + (xcd & 1), f = fp * 14 + fq;
        const int n0 = fq * 40 + wv * 4 + g;
        epld();
        if (wv == 7 && lane < 16) ldsw<u32x4>(lds0 + MISC + lane * 16, (u32x4){0u, 0u, 0u, 0u});
        const int u2 = max(f - 6, 0), kr = (u2 * 26) >> 8, fc = u2 - kr * 10, n1 = wv < 4 ? 3 : 2;
        const int d0 = kr * 11 + wv, d1 = kr * 11 + 4 + wv; const bool two = wv >= 4 && wv < 7 && d1 < 54;
        const char* w1 = (const char*)(f & 1 ? wu : wg) + min(f >> 1, 26) * 128; const char* w2 = (const char*)wd + fc * 128;
        unsigned fv[2][4];
#pragma unroll
        for (int i = 0; i < 4; ++i) { const unsigned c = (((lane & 7) ^ (2 * ((lane >> 4) & 1) + 4 * (i & 1))) * 16) - i * 1024; fv[0][i] = ((lane >> 3) + i * 8) * (F_ * 2) + c; fv[1][i] = ((lane >> 3) + i * 8) * 1280 + c; }
        auto issue = [&](int m, int ks, int slot) {
            asm volatile("s_mov_b32 m0, %[l]\n\ts_nop 0\n\tglobal_load_lds_dwordx4 %0, %[b] nt\n\tglobal_load_lds_dwordx4 %1, %[b] offset:1024 nt\n\tglobal_load_lds_dwordx4 %2, %[b] offset:2048 nt\n\tglobal_load_lds_dwordx4 %3, %[b] offset:3072 nt"
                         :: "v"(fv[m][0]), "v"(fv[m][1]), "v"(fv[m][2]), "v"(fv[m][3]), [b] "s"((m ? w2 : w1) + (unsigned)(ks * 32 * (m ? 1280 : F_ * 2))), [l] "s"(lds0 + wv * 16384 + slot * 4096) : "memory");
        };
        if (wv == 4) {
            const int l = min(f * 14 + lane, 770), o = l < 280 ? W_KEY + l * 128 : l < 536 ? W_ST + (l - 280) * 128 : W_Y + (l - 536) * 256;
            if (lane < 14) asm volatile("s_mov_b32 m0, %[l]\n\ts_nop 0\n\tbuffer_load_dword %0, %1, 0 offen lds" :: "v"(o), "s"(rs), [l] "s"(lds0 + L_DUM) : "memory");
        }
        if (wv >= 5) {
            const int i = min((j - 25) * 172 + (7 - wv) * 64 + lane, 1199), n = (i * 4370) >> 16, e = i - n * 15, m = (e * 52) >> 8, rw = 4 * n + fp;
            asm volatile("s_mov_b32 m0, %[l]\n\ts_nop 0\n\tglobal_load_lds_dword %0, %[b]" :: "v"(rw * 3840 + ((rw >> 6) + 1) * 245760 + e * 128 + m * 640 + (xcd & 1) * 640), [b] "s"(ka[1]), [l] "s"(lds0 + L_DUM) : "memory");
        }
        BARN();
        asm volatile("s_waitcnt vmcnt(%2)" : "+{v[84:115]}"(sb), "+{v[116:123]}"(sc) : "n"(5) : "memory");
#define SXR(i) ((i) < 20 ? sa[(12 + (i)) & 31] : (i) < 52 ? sb[((i) - 20) & 31] : sc[((i) - 52) & 7])
        mktag();
        {
            const unsigned one2 = 0x3F803F80u;
            float wsum = 0.f;
#pragma unroll
            for (int e = 0; e < 20; ++e) wsum = dot2(SXR(40 + e), one2, wsum);
            wsum = sum16(wsum);
            auto stat = [&](int p) {
                float s1 = 0.f, s2 = 0.f, sr = 0.f;
#pragma unroll
                for (int e = 0; e < 20; ++e) { const unsigned u = SXR(20 * p + e); s1 = dot2(u, one2, s1); s2 = dot2(u, u, s2); sr = dot2(u, SXR(40 + e), sr); }
                const float mean = sum16(s1) * (1.f / 640.f);
                const unsigned mb2 = cvtpk(mean, mean); const float mb = lo2f(mb2);
                const float q = fmaf(40.f * mb, mb, fmaf(-2.f * mb, s1, s2));
                float dm = mean - mb; NOPK(dm);
                dm *= dm; NOPK(dm);
                const float var = fmaxf(sum16(q) * (1.f / 640.f) - dm, 0.f);
                const float sd = rbf(__builtin_amdgcn_sqrtf(rbf(rbf(var) + 1e-5f)));
                const float rsc = rbf(fmaf(-mb, wsum, sum16(sr)) / sd);
                const int n = n0 + 32 * p, i = (fp & 1) * 512 + n;
                if (li == 0) {
                    asm volatile("buffer_store_dwordx4 %0, %1, %2, 0 offen sc1\n\ts_nop 1" :: "v"((u32x4){(mb2 << 16) ^ 0x80000000u, tag, __float_as_uint(__builtin_amdgcn_rcpf(sd)), tag}), "v"(n < 512 ? W_ST + (par * 1024 + i) * 16 : 0x40000000), "s"(rs) : "memory");
                    ldsw<unsigned>(lds0 + LS_ROW + (p * 32 + wv * 4 + g) * 4, n < 512 ? rkey(rsc, 2 * i + par) : 0u);
                }
            };
            asm volatile("" : "+v"(wsum));
            if (wv < 2) stat(1);
            asm volatile("s_bitcmp1_b32 0xf3, %2\n\ts_cbranch_scc1 1f\n\ts_waitcnt vmcnt(%1)\n1:\n\ts_waitcnt vmcnt(%1 + 1)" : "+{v[52:83]}"(sa) : "n"(0), "s"(wv) : "memory", "scc");
            if (wv < 5) issue(0, wv, 0);
            stat(0);
        }
        BAR();
        if (wv == 6) asm volatile("buffer_store_dword %0, %1, %2, 0 offen sc1" :: "v"(tag), "v"(lane ? 0x40000000 : W_ACK + (f < 40 ? (f >> 1) * 128 + 40 + (f & 1) * 4 : 2560 + (f - 40) * 4)), "s"(rs) : "memory");
        if (wv == 7) {
            const unsigned k = ldsrv<unsigned>(lds0 + LS_ROW + min(lane, 39) * 4);
            const uint2 t2 = top2wave(lane < 40 ? k : 0u, 0u);
            if (lane == 0 && fq < 13) asm volatile("buffer_store_dwordx4 %0, %1, %2, 0 offen sc1\n\ts_nop 1" :: "v"((u32x4){t2.x, tag, t2.y, tag}), "v"(W_KEY + (fp * 13 + fq) * 16), "s"(rs) : "memory");
        }
        BARN();
        if (wv >= 5) issue(0, wv, 0);
        issue(0, wv + 8, 1);
        auto wh = [&]() { const int i = min((j - 25) * 116 + (wv - 4) * 29 + lane, 799), n = (i * 13108) >> 16; return (4 * n + fp) * 1280 + (i - 5 * n) * 128 + (xcd & 1) * 640; };
        if (wv < 4) issue(0, wv + 16, 2);
        else asm volatile("s_mov_b32 m0, %[l]\n\ts_mov_b32 exec_hi, 0\n\tglobal_load_lds_dword %0, %[b]\n\ts_mov_b32 exec_hi, -1" :: "v"(wh()), [b] "s"(wo), [l] "s"(lds0 + L_DUM) : "memory");
        u32x4 kk;
        BARN();
        asm volatile("s_cmp_lg_u32 %3, 7\n\ts_cbranch_scc1 1f\n\ts_sleep 0\n\tbuffer_load_dwordx4 %0, %1, %2, 0 offen sc1\n1:\n\ts_barrier" : "=&v"(kk) : "v"(W_KEY + min(lane, 51) * 16), "s"(rs), "s"(wv) : "memory", "scc");
        if (wv != 7) issue(1, d0, 3);
        asm volatile("s_cmp_lg_u32 %1, 7\n\ts_cbranch_scc1 1f\n\ts_waitcnt vmcnt(0)\n1:" : "+v"(kk) : "s"(wv) : "memory", "scc");
        if (wv == 7) {
            SPIN {
                if (__builtin_amdgcn_ballot_w64(kk.y != tag || kk.w != tag) == 0) break;
                asm volatile("buffer_load_dwordx4 %0, %1, %2, 0 offen sc1\n\ts_waitcnt vmcnt(0)" : "=&v"(kk) : "v"(W_KEY + min(lane, 51) * 16), "s"(rs) : "memory");
            }
            const uint2 t2 = top2wave(lane < 52 ? kk.x : 0u, lane < 52 ? kk.z : 0u);
            if (lane < 2) ldsw<unsigned>(lds0 + ROUT + lane * 8, (0xffffu - ((lane ? t2.y : t2.x) & 0xffffu)) & 2047u);
        }
        BAR();
        const unsigned row = ldsrv<unsigned>(lds0 + ROUT + (li & 1) * 8);
        u32x4 af[3], st;
        asm volatile("global_load_dwordx4 %0, %4, %5\n\tglobal_load_dwordx4 %1, %4, %5 offset:512\n\tglobal_load_dwordx4 %2, %6, %5 offset:1024\n\tbuffer_load_dwordx4 %3, %7, %8, 0 offen sc1"
                     : "=&v"(af[0]), "=&v"(af[1]), "=&v"(af[2]), "=&v"(st) : "v"(row * 1280u + wv * 64 + g * 16), "s"(x), "v"(row * 1280u + (wv & 3) * 64 + g * 16), "v"(wv ? 0x40000000 : W_ST + ((row & 1) * 1024 + (row >> 1)) * 16), "s"(rs) : "memory");
        bf16x8 wf[3][4];
        unsigned ra[4];
#pragma unroll
        for (int q = 0; q < 4; ++q) ra[q] = lds0 + wv * 16384 + (unsigned)((8 * g + (li >> 2)) * 128 + (((q ^ (((li >> 3) & 1) + 2 * (g & 1))) * 32) + (li & 3) * 8));
        auto rdf = [&](int k, int slot) {
#pragma unroll
            for (int q = 0; q < 4; ++q) wf[k][q] = cat8(trread(ra[q] + slot * 4096), trread(ra[q] + slot * 4096 + 512));
        };
        if (wv == 7) issue(1, d0, 3);
        if (two) issue(1, d1, 2);
        if (two) wvm<12>(); else wvm<8>();
        rdf(0, 0); rdf(1, 1); if (wv < 4) rdf(2, 2);
        asm volatile("s_cmp_eq_u32 %4, 0\n\ts_cbranch_scc0 1f\n\ts_waitcnt vmcnt(0)\n1:\n\ts_waitcnt vmcnt(4)" : "+v"(af[0]), "+v"(af[1]), "+v"(af[2]), "+v"(st) : "s"(RFL(two || wv == 7)) : "memory", "scc");
        f32x4 c[4];
#pragma unroll
        for (int q = 0; q < 4; ++q) c[q] = (f32x4){0.f, 0.f, 0.f, 0.f};
#pragma unroll
        for (int k = 0; k < 3; ++k) if (k < n1) {
#pragma unroll
            for (int e = 0; e < 4; ++e) af[k][e] = li & 2 ? 0x3F803F80u : af[k][e];
#pragma unroll
            for (int q = 0; q < 4; ++q) c[q] = mfma32(__builtin_bit_cast(bf16x8, af[k]), wf[k][q], c[q]);
        }
        auto total = [&](int off) {
            f32x4 m;
#pragma unroll
            for (int e = 0; e < 4; ++e) { float a_ = g & 1 ? c[1][e] : c[0][e], b_ = g & 1 ? c[3][e] : c[2][e]; NOPK(a_); NOPK(b_); m[e] = g & 2 ? b_ : a_; }
            ldsw<f32x4>(lds0 + wv * 16384 + off + lane * 16, m);
            BAR();
            if (wv) return m;
            f32x4 pr[8];
#pragma unroll
            for (int w = 0; w < 8; ++w) pr[w] = ldsrv<f32x4>(lds0 + w * 16384 + off + lane * 16);
#pragma unroll
            for (int e = 0; e < 3; ++e) { float t[4];
#pragma unroll
                for (int w = 0; w < 4; ++w) { t[w] = pr[2 * w][e] + pr[2 * w + 1][e]; NOPK(t[w]); }
                float u = t[0] + t[1], v = t[2] + t[3]; NOPK(u); NOPK(v); u += v; NOPK(u); m[e] = u; }
            return m;
        };
        {
            const f32x4 s = total(0);
            if (wv == 0 && f < 54) {
                SPIN {
                    if (__builtin_amdgcn_ballot_w64(st.y != tag || st.w != tag) == 0) break;
                    asm volatile("buffer_load_dwordx4 %0, %1, %2, 0 offen sc1\n\ts_waitcnt vmcnt(0)" : "=&v"(st) : "v"(W_ST + ((row & 1) * 1024 + (row >> 1)) * 16), "s"(rs) : "memory");
                }
                const float n0_ = __uint_as_float(__builtin_amdgcn_readlane(st.x, 0)), n1_ = __uint_as_float(__builtin_amdgcn_readlane(st.x, 1)), i0 = __uint_as_float(__builtin_amdgcn_readlane(st.z, 0)), i1 = __uint_as_float(__builtin_amdgcn_readlane(st.z, 1));
                float v0 = rbf(fmaf(n0_, s.z, s.x) * i0), v1 = rbf(fmaf(n1_, s.z, s.y) * i1);
                if (!(f & 1)) { v0 = silu_b(v0); NOPK(v0); v1 = silu_b(v1); }
                const unsigned hb = cvtpk(v0, v1), nb = dppu<0xB1>(hb);
                const unsigned pay = (lane & 1) ? __builtin_amdgcn_perm(hb, nb, 0x07060302u) : __builtin_amdgcn_perm(nb, hb, 0x05040100u);
                asm volatile("buffer_store_dwordx2 %0, %1, %2, 0 offen sc1" :: "v"((u32x2){pay, tag}), "v"(W_H + ((f & 1) * 2 + (lane & 1)) * 6912 + ((f >> 1) * 32 + (lane >> 1)) * 8), "s"(rs) : "memory");
            }
        }
        if (f == 0 && wv == 1) {
            SPIN {
                u32x4 a_;
                asm volatile("buffer_load_dwordx4 %0, %1, %2, 0 offen sc1\n\ts_waitcnt vmcnt(0)" : "=&v"(a_) : "v"(W_ACK + (lane < 60 ? ((lane * 43) >> 7) * 80 + lane * 16 : 1600 + lane * 16)), "s"(rs) : "memory");
                if (__builtin_amdgcn_ballot_w64(a_.x != tag || a_.y != tag || a_.z != tag || a_.w != tag) == 0) break;
                __builtin_amdgcn_s_sleep(8);
            }
            if (lane == 0) __builtin_amdgcn_raw_buffer_store_b32(tag, rs, W_EP, 0, SC1);
        }
        if (f >= 6) {
            wvm<0>();
            rdf(0, 3); rdf(1, 2);
            const int ho = W_H + ((lane >> 4) & 1) * 6912 + ((lane >> 5 && two ? d1 : d0) * 16 + li) * 8;
            u32x2 hg, hu;
            __builtin_amdgcn_s_sleep(26);
            SPIN {
                asm volatile("buffer_load_dwordx2 %0, %2, %3, 0 offen sc1\n\tbuffer_load_dwordx2 %1, %2, %3, %4 offen sc1\n\ts_waitcnt vmcnt(0)" : "=&v"(hg), "=&v"(hu) : "v"(ho), "s"(rs), "s"(13824) : "memory");
                if (__builtin_amdgcn_ballot_w64(hg.y != tag || hu.y != tag) == 0) break;
                __builtin_amdgcn_s_sleep(10);
            }
            float h0 = lo2f(hg.x) * lo2f(hu.x), h1 = hi2f(hg.x); NOPK(h0); NOPK(h1); h1 *= hi2f(hu.x); NOPK(h1);
            ldsw<unsigned>(lds0 + wv * 16384 + 4096 + lane * 4, cvtpk(h0, h1));
#pragma unroll
            for (int q = 0; q < 4; ++q) c[q] = (f32x4){0.f, 0.f, 0.f, 0.f};
#pragma unroll
            for (int k = 0; k < 2; ++k) if (k == 0 || two) {
                const u32x4 h = ldsrv<u32x4>(lds0 + wv * 16384 + 4096 + k * 128 + (li & 1) * 64 + g * 16);
#pragma unroll
                for (int q = 0; q < 4; ++q) c[q] = mfma32(__builtin_bit_cast(bf16x8, h), wf[k][q], c[q]);
            }
            const f32x4 ys = total(1024);
            if (wv == 0) __builtin_amdgcn_raw_buffer_store_b128((u32x4){__float_as_uint(ys.x), tag, __float_as_uint(ys.y), tag}, rs, W_Y + kr * 10240 + (fc * 64 + lane) * 16, 0, SC1);
        }
        wvm<0>();
    }
}

extern "C" void kernel_launch(void* const* d_in, const int* in_sizes, int n_in, void* d_out, int out_size, void* d_ws,
                              size_t ws_size, hipStream_t stream) {
    (void)hipFuncSetAttribute((const void*)fused, hipFuncAttributeMaxDynamicSharedMemorySize, LDSZ);
    fused<<<256, 512, LDSZ, stream>>>((const u16*)d_in[0], (const u16*)d_in[1], (const u16*)d_in[2], (const u16*)d_in[4],
                                      (const u16*)d_in[5], (const u16*)d_in[6], (const u16*)d_in[7], (char*)d_ws, (u16*)d_out);
}
```

```cpp
#include <hip/hip_runtime.h>

typedef __attribute__((ext_vector_type(8))) short bf16x8;
typedef __attribute__((ext_vector_type(4))) short bf16x4;
typedef __attribute__((ext_vector_type(4))) float f32x4;
typedef __attribute__((ext_vector_type(2))) float f32x2;
typedef __attribute__((ext_vector_type(4))) unsigned u32x4;
typedef __attribute__((ext_vector_type(2))) unsigned u32x2;
typedef __attribute__((ext_vector_type(2))) __bf16 bf16x2v;
typedef __attribute__((ext_vector_type(8))) unsigned u32x8;
typedef unsigned short u16;
typedef __attribute__((address_space(3))) char lchar;


#define F_ 1728
constexpr int W_EP = 0;
constexpr int W_KEY = 4096;
constexpr int W_ACK = 8192;
constexpr int W_H = 12288;
constexpr int W_ST = 65536;
constexpr int W_Y = 98304;
constexpr int W_O = 155648;
constexpr int W_END = W_O + 80 * 40960;
constexpr int XS = 14336, SLOT = XS + 24576;
constexpr int WO0 = 2 * SLOT;
constexpr int MISC = WO0 + 81920;
constexpr int FL_W = MISC + 48, FL_R = MISC + 64, ROUT = MISC + 144;
constexpr int L_NM = MISC + 256, L_RS = L_NM + 512 + 64, L_RM = L_RS + 512, L_RG = L_RM + 512, L_DUM = L_RG + 512, L_KEY = L_DUM + 512;
constexpr int LS_ROW = L_NM;
constexpr int LDSZ = 163840;
static_assert(L_KEY + 1024 <= LDSZ, "lds");
#define SPIN_MAX (1 << 21)
#define SPIN _Pragma("nounroll") for (int it = 0; it < SPIN_MAX; ++it)

__device__ __forceinline__ unsigned cvtpk(float lo, float hi) { return __builtin_bit_cast(unsigned, __builtin_convertvector((f32x2){lo, hi}, bf16x2v)); }
__device__ __forceinline__ float lo2f(unsigned u) { return __uint_as_float(u << 16); }
__device__ __forceinline__ float hi2f(unsigned u) { return __uint_as_float(u & 0xffff0000u); }
__device__ __forceinline__ float rbf(float f) { return lo2f(cvtpk(f, f)); }
template <int CTRL> __device__ __forceinline__ float dppf(float x) { return __builtin_bit_cast(float, __builtin_amdgcn_mov_dpp(__builtin_bit_cast(int, x), CTRL, 0xf, 0xf, true)); }
template <int CTRL> __device__ __forceinline__ unsigned dppu(unsigned x) { return (unsigned)__builtin_amdgcn_mov_dpp((int)x, CTRL, 0xf, 0xf, true); }
__device__ __forceinline__ float sum16(float x) { x += dppf<0xB1>(x); x += dppf<0x4E>(x); x += dppf<0x141>(x); x += dppf<0x140>(x); return x; }
__device__ __forceinline__ float dot2(unsigned a, unsigned b, float c) { return __builtin_amdgcn_fdot2_f32_bf16(__builtin_bit_cast(bf16x2v, a), __builtin_bit_cast(bf16x2v, b), c, false); }
__device__ __forceinline__ float silu_b(float v) { return v * __builtin_amdgcn_rcpf(__builtin_amdgcn_exp2f(v * -1.4426950408889634f) + 1.f); }
__device__ __forceinline__ unsigned rkey(float rb, int row) {
    unsigned b = __float_as_uint(rb) >> 16; b = (b & 0x8000u) ? (~b & 0xffffu) : (b | 0x8000u);
    return (b << 16) | (unsigned)(0xffff - row);
}
__device__ __forceinline__ float keyscore(unsigned k) { const unsigned b = k >> 16; return __uint_as_float(((b & 0x8000u) ? (b & 0x7fffu) : (~b & 0xffffu)) << 16); }
#define T2M(b0, b1) { const unsigned _b0 = (b0), _b1 = (b1); const unsigned _m = min(k0, _b0); k0 = max(k0, _b0); k1 = max(_m, max(k1, _b1)); }
__device__ __forceinline__ uint2 top2wave(unsigned k0, unsigned k1) {
    T2M(dppu<0xB1>(k0), dppu<0xB1>(k1)); T2M(dppu<0x4E>(k0), dppu<0x4E>(k1));
    T2M(dppu<0x141>(k0), dppu<0x141>(k1)); T2M(dppu<0x140>(k0), dppu<0x140>(k1));
    const unsigned a0 = __builtin_amdgcn_readlane(k0, 0), a1 = __builtin_amdgcn_readlane(k1, 0);
    const unsigned c0 = __builtin_amdgcn_readlane(k0, 16), c1 = __builtin_amdgcn_readlane(k1, 16);
    const unsigned d0 = __builtin_amdgcn_readlane(k0, 32), d1 = __builtin_amdgcn_readlane(k1, 32);
    const unsigned e0 = __builtin_amdgcn_readlane(k0, 48), e1 = __builtin_amdgcn_readlane(k1, 48);
    k0 = a0; k1 = a1; T2M(c0, c1); T2M(d0, d1); T2M(e0, e1);
    return make_uint2(k0, k1);
}
template <int N> __device__ __forceinline__ void wvm() { asm volatile("" ::: "memory"); __builtin_amdgcn_s_waitcnt((N & 15) | ((N >> 4) << 14) | 0x0F70); asm volatile("" ::: "memory"); }
__device__ __forceinline__ bf16x4 trread(unsigned a) { return __builtin_amdgcn_ds_read_tr16_b64_v4i16((__attribute__((address_space(3))) bf16x4*)(size_t)a); }
template <class T> __device__ __forceinline__ T ldsrv(unsigned a) { return *(volatile __attribute__((address_space(3))) T*)(size_t)a; }
template <class T> __device__ __forceinline__ void ldsw(unsigned a, T v) { *(volatile __attribute__((address_space(3))) T*)(size_t)a = v; }
__device__ __forceinline__ bf16x8 cat8(bf16x4 lo, bf16x4 hi) { return (bf16x8){lo[0], lo[1], lo[2], lo[3], hi[0], hi[1], hi[2], hi[3]}; }
__device__ __forceinline__ f32x4 mfma32(bf16x8 a, bf16x8 b, f32x4 c) { return __builtin_amdgcn_mfma_f32_16x16x32_bf16(a, b, c, 0, 0, 0); }
__device__ __forceinline__ int lane_id() { int l; asm volatile("v_mbcnt_lo_u32_b32 %0, -1, 0\n\tv_mbcnt_hi_u32_b32 %0, -1, %0" : "=v"(l)); __builtin_assume(l >= 0 && l < 64); return l; }
#define BAR() asm volatile("s_waitcnt lgkmcnt(0)\n\ts_barrier" ::: "memory")
#define MEMB() asm volatile("" ::: "memory")
#define RFL(x) ((unsigned)__builtin_amdgcn_readfirstlane((int)(x)))
#define SC1 16

#define NOPK(v) asm("" : "+v"(v))
#define OPQ(v) asm volatile("" : "+v"(v))
#define END() do { asm volatile("s_endpgm"); __builtin_amdgcn_s_waitcnt(0); return; } while (0)
#define BARN() asm volatile("s_barrier" ::: "memory")

__global__ __launch_bounds__(512, 2) void fused(const u16* x_, const u16* wqkv_, const u16* wo_, const u16* w_r_, const u16* wg_, const u16* wu_, const u16* wd_, char* ws_, u16* out_) {
    extern __shared__ __attribute__((aligned(16))) char smem[];
    typedef __attribute__((ext_vector_type(32))) unsigned u32x32;
    u32x32 sa, sb; u32x8 sc;
    unsigned tag, bid = blockIdx.x; int tid = threadIdx.x;
    typedef __attribute__((address_space(1))) char gchar;
    typedef gchar* const __attribute__((address_space(4)))* karg_t;
    karg_t ka = (karg_t)__builtin_amdgcn_kernarg_segment_ptr();
    asm volatile(
            "s_load_dwordx2 s[40:41], %[k], 0x8\n\ts_load_dwordx2 s[36:37], %[k], 0x0\n\tv_readfirstlane_b32 s69, %[t]\n\ts_lshr_b32 s70, %[b], 3\n\ts_cmpk_lg_u32 s69, 192\n\ts_cbranch_scc1 1f\n\t"
            "s_getpc_b64 s[72:73]\n\t.Lch:\ns_lshl3_add_u32 s71, s70, -192\n\tv_add_lshl_u32 v36, s71, %[t], 7\n\tv_min_u32 v36, .Lce-.Lch-4, v36\n\ts_mov_b32 m0, 162368\n\t"
            "s_mov_b64 exec, 0xff\n\tglobal_load_lds_dword v36, s[72:73]\n\ts_mov_b64 exec, -1\n\t1:\ns_add_i32 s70, s70, 8\n\ts_and_b32 s70, s70, 31\n\t"
            "s_cmp_lt_u32 s70, 25\n\ts_cbranch_scc0 .LF\n\tv_bfe_u32 v36, %[t], 3, 3\n\tv_and_b32 v37, 7, %[t]\n\ts_mul_i32 s71, s70, 13\n\ts_lshr_b32 s71, s71, 6\n\t"
            "v_and_b32 v38, 2, v36\n\tv_xor_b32 v46, v38, v37\n\ts_lshl_b32 s64, s71, 2\n\ts_sub_i32 s64, s70, s64\n\ts_sub_i32 s57, s64, s71\n\tv_mul_u32_u24 v40, 0xf00, v36\n\t"
            "s_sub_i32 s72, s64, 5\n\ts_min_u32 s64, s64, s72\n\tv_lshl_add_u32 v40, v46, 4, v40\n\ts_bfe_u32 s56, s69, 0x20006\n\ts_and_b32 s72, %[b], 1\n\ts_mul_i32 s72, s72, 5\n\t"
            "s_add_i32 s58, s57, s72\n\ts_lshl_b32 s58, s58, 7\n\ts_mul_i32 s61, s64, 491520\n\ts_mul_i32 s73, s56, 0xf000\n\ts_add_i32 s73, s73, s58\n\ts_add_i32 s61, s61, s73\n\t"
            "s_mul_i32 s66, s56, 0x1800\n\ts_add_i32 m0, s66, 53248\n\ts_mov_b32 s42, 0x258000\n\ts_mov_b32 s43, 0x20000\n\tv_add_u32 v41, 0x100, v40\n\tv_add_u32 v42, 0x200, v40\n\t"
            "v_xor_b32 v43, 64, v40\n\tv_add_u32 v43, 0x6c00, v43\n\tv_add_u32 v44, 0x1100, v43\n\tv_add_u32 v45, 0x1200, v43\n\ts_cmpk_lt_u32 s69, 0x100\n\ts_cbranch_scc0 1f\n\t"
            "s_waitcnt lgkmcnt(0)\n\tbuffer_load_dwordx4 v40, s[40:43], s61 offen lds\n\tbuffer_load_dwordx4 v41, s[40:43], s61 offen offset:1024 lds\n\tbuffer_load_dwordx4 v42, s[40:43], s61 offen offset:2048 lds\n\tbuffer_load_dwordx4 v43, s[40:43], s61 offen offset:3072 lds\n\ts_addk_i32 m0, 0x1000\n\t"
            "s_nop 0\n\tbuffer_load_dwordx4 v44, s[40:43], s61 offen lds\n\tbuffer_load_dwordx4 v45, s[40:43], s61 offen offset:1024 lds\n\t1:\ns_lshl_b32 s72, s56, 5\n\ts_min_u32 s72, s72, 88\n\t"
            "v_add_u32 v39, s72, v36\n\ts_bfe_u32 s62, %[b], 0x10001\n\ts_mul_i32 s62, s62, 5\n\ts_add_i32 s62, s62, s71\n\ts_mul_i32 s62, s62, 104\n\ts_sub_i32 s62, s62, 8\n\t"
            "v_and_b32 v47, 3, v39\n\tv_lshl_add_u32 v47, v47, 2, s62\n\tv_and_b32 v48, 0x70, v39\n\tv_bfe_u32 v49, v39, 2, 2\n\tv_add3_u32 v48, v48, v49, v47\n\tv_bfe_u32 v49, v39, 1, 3\n\t"
            "v_xor_b32 v50, v49, v37\n\tv_mul_i32_i24 v48, 0xa00, v48\n\ts_bfe_u32 s63, %[b], 0x10002\n\ts_mul_i32 s63, s63, 0x500\n\ts_lshl_b32 s60, s64, 8\n\tv_mov_b32 v51, v36\n\t"
            "v_lshl_add_u32 v36, v50, 4, v48\n\ts_add_i32 s60, s60, s63\n\ts_lshl_b32 s65, s72, 7\n\ts_add_i32 m0, s65, 38912\n\ts_mov_b32 s38, 0x280000\n\ts_mov_b32 s39, 0x20000\n\t"
            "v_add_u32 v38, 0x9800, v36\n\ts_cmpk_lt_u32 s69, 0x100\n\ts_cbranch_scc0 1f\n\tbuffer_load_dwordx4 v36, s[36:39], s60 offen lds\n\tbuffer_load_dwordx4 v38, s[36:39], s60 offen offset:2048 lds\n\t1:\n"
            "s_barrier\n\ts_load_dwordx2 s[44:45], %[k], 0x38\n\ts_load_dwordx2 s[48:49], %[k], 0x10\n\ts_load_dwordx2 s[52:53], %[k], 0x40\n\tv_add_u32 v39, 8, v39\n\tv_and_b32 v48, 0x70, v39\n\t"
            "v_bfe_u32 v49, v39, 2, 2\n\tv_add3_u32 v48, v48, v49, v47\n\tv_xor_b32 v50, 4, v50\n\tv_mul_i32_i24 v48, 0xa00, v48\n\tv_lshl_add_u32 v37, v50, 4, v48\n\tv_subrev_u32 v37, 0x400, v37\n\t"
            "v_add_u32 v39, 0x9800, v37\n\ts_addk_i32 s66, 14336\n\ts_bfe_u32 s59, %[b], 0x20001\n\ts_mul_i32 s59, s59, 5\n\ts_add_i32 s59, s59, s71\n\ts_mov_b32 s46, 3432448\n\t"
            "s_mov_b32 s47, 0x20000\n\ts_mov_b32 s54, 0x280000\n\ts_mov_b32 s55, 0x20000\n\tv_and_b32 v52, 63, %[t]\n\tv_bfe_u32 v53, %[t], 4, 2\n\tv_and_b32 v54, 15, %[t]\n\t"
            "v_and_b32 v55, 3, %[t]\n\tv_bfe_u32 v56, %[t], 2, 2\n\tv_lshl_add_u32 v57, v55, 2, v56\n\ts_lshr_b32 s69, s69, 6\n\ts_cmp_lt_u32 s69, 4\n\ts_cbranch_scc0 .LB\n\t"
            "s_branch .LA\n\t.LF:\ns_load_dwordx2 s[72:73], %[k], 0x18\n\ts_and_b32 s71, %[b], 1\n\ts_lshl1_add_u32 s71, s70, s71\n\ts_mul_i32 s71, s71, 40\n\t"
            "s_sub_u32 s71, s71, 2000\n\tv_lshrrev_b32 v2, 4, %[t]\n\tv_add_u32 v2, s71, v2\n\tv_add_u32 v3, 32, v2\n\tv_min_u32 v2, 0x1ff, v2\n\tv_min_u32 v3, 0x1ff, v3\n\t"
            "v_and_b32 v4, 15, %[t]\n\tv_lshlrev_b32 v4, 4, v4\n\ts_bfe_u32 s71, %[b], 0x10001\n\ts_mul_i32 s71, s71, 0x140000\n\ts_bfe_u32 s74, %[b], 0x10002\n\ts_mul_i32 s74, s74, 0x500\n\t"
            "s_add_u32 s71, s71, s74\n\tv_add_u32 v5, s71, v4\n\ts_movk_i32 s71, 0xa00\n\tv_mad_u32_u24 v2, v2, s71, v5\n\tv_mad_u32_u24 v3, v3, s71, v5\n\ts_waitcnt lgkmcnt(0)\n\t"
            "global_load_dwordx4 v[104:107], v4, s[72:73] offset:0\n\tglobal_load_dwordx4 v[108:111], v4, s[72:73] offset:256\n\tglobal_load_dwordx4 v[112:115], v4, s[72:73] offset:512\n\tglobal_load_dwordx4 v[116:119], v4, s[72:73] offset:768\n\tglobal_load_dwordx4 v[120:123], v4, s[72:73] offset:1024\n\ts_cmpk_ge_u32 s69, 0x80\n\t"
            "s_cbranch_scc1 1f\n\tglobal_load_dwordx4 v[84:87], v3, s[36:37] offset:0\n\tglobal_load_dwordx4 v[88:91], v3, s[36:37] offset:256\n\tglobal_load_dwordx4 v[92:95], v3, s[36:37] offset:512\n\tglobal_load_dwordx4 v[96:99], v3, s[36:37] offset:768\n\tglobal_load_dwordx4 v[100:103], v3, s[36:37] offset:1024\n\t"
            "1:\nglobal_load_dwordx4 v[64:67], v2, s[36:37] offset:0\n\tglobal_load_dwordx4 v[68:71], v2, s[36:37] offset:256\n\tglobal_load_dwordx4 v[72:75], v2, s[36:37] offset:512\n\tglobal_load_dwordx4 v[76:79], v2, s[36:37] offset:768\n\tglobal_load_dwordx4 v[80:83], v2, s[36:37] offset:1024\n\t"
            "s_branch .Lout\n\t.LA:\n"
            "v_lshrrev_b32 v2, 1, v54\n\tv_xor_b32 v2, v2, v53\n\tv_lshlrev_b32 v3, 7, v54\n\tv_lshl_add_u32 v2, v2, 4, v3\n\tv_xor_b32 v3, 64, v2\n\tv_add_u32 v4, 38912, v2\n\t"
            "v_add_u32 v5, 38912, v3\n\tv_add_u32 v10, 77824, v2\n\tv_add_u32 v11, 77824, v3\n\tv_mov_b32 v12, 159872\n\tv_cmp_eq_u32 vcc, 0, v54\n\tv_mov_b32 v60, 0\n\t"
            "v_mov_b32 v61, 0\n\tv_mov_b32 v62, 0x3f803f80\n\tv_lshlrev_b32 v63, 3, v52\n\tv_cndmask_b32 v6, v2, v12, vcc\n\tv_cndmask_b32 v7, v3, v12, vcc\n\tv_cndmask_b32 v8, v4, v12, vcc\n\t"
            "v_cndmask_b32 v9, v5, v12, vcc\n\tv_cndmask_b32 v10, v10, v12, vcc\n\tv_cndmask_b32 v11, v11, v12, vcc\n\tv_lshlrev_b32 v64, 2, v52\n\tv_lshrrev_b32 v65, 2, v52\n\tv_cmp_eq_u32 vcc, 8, v65\n\t"
            "v_add_u32 v66, 161088, v63\n\tv_add_u32 v64, 159744, v64\n\tv_cndmask_b32 v62, 0, v62, vcc\n\ts_cmp_lg_u32 s69, 1\n\ts_cbranch_scc1 1f\n\tds_write_b32 v64, v62\n\t"
            "ds_write_b64 v66, v[60:61]\n\tds_write_b64 v66, v[60:61] offset:576\n\t1:\nv_bfe_u32 v12, v52, 3, 2\n\tv_xor_b32 v12, s69, v12\n\tv_lshlrev_b32 v13, 7, v56\n\t"
            "v_lshl_add_u32 v13, v55, 3, v13\n\tv_lshl_add_u32 v12, v12, 5, v13\n\tv_mul_u32_u24 v13, 0xc00, v53\n\ts_movk_i32 s72, 14336\n\tv_add3_u32 v12, v12, v13, s72\n\tv_add_u32 v13, 38912, v12\n\t"
            "s_mov_b32 s72, 160000\n\tv_lshl_add_u32 v14, v54, 2, s72\n\tv_lshl_add_u32 v15, v53, 4, s72\n\ts_cmp_eq_u32 s69, 3\n\ts_cselect_b32 s74, 1, 0\n\ts_cselect_b64 s[76:77], -1, 0\n\t"
            "s_cselect_b32 s75, 4, 0\n\ts_add_i32 s72, s69, 1\n\ts_bfe_u32 s72, s72, 0x10001\n\ts_xor_b32 s72, s72, 1\n\ts_lshr_b32 s73, s69, 1\n\ts_lshl1_add_u32 s78, s72, s73\n\t"
            "s_lshl2_add_u32 s78, s78, 159776\n\tv_mov_b32 v34, s78\n\ts_lshl2_add_u32 s78, s69, 159744\n\tv_mov_b32 v33, s78\n\ts_mul_i32 s78, s59, 163840\n\ts_lshl_b32 s79, s58, 5\n\t"
            "s_add_i32 s78, s78, s79\n\ts_lshl_b32 s79, s69, 10\n\ts_add_i32 s78, s78, s79\n\ts_add_i32 s78, s78, 155648\n\tv_lshl_add_u32 v16, v52, 4, s78\n\tv_and_b32 v215, 1, v53\n\t"
            "v_lshl_add_u32 v215, v215, 30, v16\n\ts_lshl_b32 s79, s74, 11\n\ts_add_i32 s78, s78, s79\n\ts_sub_i32 s78, s78, 0x400\n\ts_cmp_lg_u32 s57, s75\n\ts_cselect_b32 s79, s72, 0\n\t"
            "s_cmp_lg_u32 s79, 0\n\ts_cselect_b32 s78, s78, 0x40000000\n\tv_lshl_add_u32 v17, v52, 4, s78\n\ts_sub_i32 s78, s69, s57\n\ts_add_i32 s79, s78, 5\n\ts_min_u32 s78, s78, s79\n\t"
            "v_cmp_gt_u32 s[80:81], s78, v53\n\tv_cmp_gt_u32 s[82:83], s78, v56\n\ts_mov_b32 s14, 0x33363333\n\ts_mov_b32 s15, 0x36663366\n\ts_mov_b32 s16, 0x666c6666\n\ts_mov_b32 s17, 0x6ccc66cc\n\t"
            "s_mov_b32 s18, 0x33333333\n\ts_mov_b32 s19, 0x33333333\n\ts_xor_b64 s[80:81], s[80:81], s[76:77]\n\ts_xor_b64 s[82:83], s[82:83], s[76:77]\n\ts_andn2_b64 vcc, s[80:81], s[18:19]\n\tv_cndmask_b32 v18, 0, -1, vcc\n\t"
            "s_and_b64 vcc, s[80:81], s[18:19]\n\tv_cndmask_b32 v19, 0, -1, vcc\n\ts_nor_b64 vcc, s[80:81], s[18:19]\n\tv_cndmask_b32 v20, 0, -1, vcc\n\ts_andn2_b64 vcc, s[18:19], s[80:81]\n\tv_cndmask_b32 v21, 0, -1, vcc\n\t"
            "v_cndmask_b32 v22, 0, -1, s[82:83]\n\tv_cndmask_b32 v23, -1, 0, s[82:83]\n\tv_sub_u32 v58, v57, v53\n\tv_add_u32 v58, 4, v58\n\tv_and_b32 v58, 28, v58\n\tv_lshlrev_b32 v58, 1, v58\n\t"
            "v_mov_b32 v24, -1\n\tv_mov_b32 v25, 0\n\tv_lshlrev_b64 v[24:25], v58, v[24:25]\n\tv_lshlrev_b64 v[24:25], v58, v[24:25]\n\tv_sub_u32 v58, 32, v58\n\tv_min_u32 v58, 16, v58\n\t"
            "v_lshrrev_b32 v26, v58, -1\n\tv_lshrrev_b32 v26, v58, v26\n\ts_lshl1_add_u32 s78, s69, s74\n\ts_xor_b32 s79, s78, 1\n\ts_mul_i32 s78, s78, 0xc00\n\ts_mul_i32 s79, s79, 0xc00\n\t"
            "s_addk_i32 s78, 14336\n\ts_addk_i32 s79, 14336\n\tv_add_u32 v28, s78, v63\n\tv_add_u32 v29, s79, v63\n\ts_cmp_eq_u32 s69, s57\n\ts_addc_u32 s78, s69, 0\n\t"
            "s_add_i32 s79, s69, 1\n\ts_cmp_eq_u32 s79, s57\n\ts_subb_u32 s79, s79, 0\n\ts_cmp_eq_u32 s74, 1\n\ts_cselect_b32 s80, s79, s78\n\ts_cselect_b32 s81, s78, s79\n\t"
            "v_lshlrev_b32 v58, 2, v53\n\tv_lshl_add_u32 v58, v54, 4, v58\n\ts_cmp_gt_u32 s80, s57\n\ts_subb_u32 s82, s80, 0\n\ts_lshl2_add_u32 s82, s82, 159760\n\tv_mov_b32 v59, s82\n\t"
            "s_lshl_b32 s82, s80, 1\n\ts_max_u32 s82, s82, 1\n\ts_mul_i32 s82, s82, 0xc00\n\ts_addk_i32 s82, 11264\n\tv_add_u32 v30, s82, v58\n\ts_cmp_gt_u32 s81, s57\n\t"
            "s_subb_u32 s82, s81, 0\n\ts_lshl2_add_u32 s82, s82, 159760\n\tv_mov_b32 v60, s82\n\ts_lshl_b32 s82, s81, 1\n\ts_max_u32 s82, s82, 1\n\ts_mul_i32 s82, s82, 0xc00\n\t"
            "s_addk_i32 s82, 11264\n\tv_add_u32 v31, s82, v58\n\tv_cndmask_b32 v32, v59, v60, s[18:19]\n\tv_mov_b32 v35, 1\n\ts_waitcnt lgkmcnt(0)\n\ts_load_dword s67, s[44:45], 0x0\n\t"
            "s_mov_b32 s20, 0\n\ts_mov_b32 s21, 40960\n\ts_mov_b32 s22, 81920\n\ts_mov_b32 s23, 122880\n\ts_cmp_ge_u32 s56, 2\n\ts_cselect_b64 vcc, -1, 0\n\t"
            "s_add_u32 m0, s65, 38912\n\ts_nop 0\n\tbuffer_load_dwordx4 v37, s[36:39], s60 offen offset:1024 lds\n\ts_cbranch_vccnz 1f\n\tbuffer_load_dwordx4 v39, s[36:39], s60 offen offset:3072 lds\n\t1:\n"
            "s_cmp_eq_u32 s64, 4\n\ts_cselect_b32 s72, -1280, 0\n\ts_add_i32 s72, s72, 256\n\ts_bitcmp1_b32 31, s64\n\ts_cselect_b32 s72, s72, 128\n\ts_mul_i32 s73, s72, 1920\n\t"
            "s_add_u32 s60, s60, s72\n\ts_add_u32 m0, s66, 77824\n\ts_add_u32 s61, s61, s73\n\tbuffer_load_dwordx4 v40, s[40:43], s61 offen lds\n\tbuffer_load_dwordx4 v41, s[40:43], s61 offen offset:1024 lds\n\tbuffer_load_dwordx4 v42, s[40:43], s61 offen offset:2048 lds\n\t"
            "buffer_load_dwordx4 v43, s[40:43], s61 offen offset:3072 lds\n\ts_addk_i32 m0, 0x1000\n\ts_nop 0\n\tbuffer_load_dwordx4 v44, s[40:43], s61 offen lds\n\tbuffer_load_dwordx4 v45, s[40:43], s61 offen offset:1024 lds\n\ts_add_u32 m0, s65, 77824\n\t"
            "s_nop 0\n\tbuffer_load_dwordx4 v36, s[36:39], s60 offen lds\n\tbuffer_load_dwordx4 v37, s[36:39], s60 offen offset:1024 lds\n\tbuffer_load_dwordx4 v38, s[36:39], s60 offen offset:2048 lds\n\ts_cbranch_vccnz 1f\n\tbuffer_load_dwordx4 v39, s[36:39], s60 offen offset:3072 lds\n\t"
            "1:\ns_mov_b32 s68, 0x200000\n\ts_waitcnt lgkmcnt(0)\n\ts_mul_i32 s67, s67, 0x19660d\n\ts_add_u32 s67, s67, 0x3c6ef35f\n\ts_cbranch_vccz 1f\n\t"
            "s_waitcnt vmcnt(9)\n\t1:\ns_waitcnt vmcnt(10)\n\ts_barrier\n\t"
            "ds_read_b64_tr_b16 v[176:177], v13 offset:0\n\tds_read_b64_tr_b16 v[178:179], v13 offset:512\n\tds_read_b128 v[120:123], v8\n\tds_read_b128 v[124:127], v4 offset:2048\n\tds_read_b128 v[128:131], v4 offset:4096\n\tds_read_b128 v[132:135], v4 offset:6144\n\t"
            "ds_read_b128 v[136:139], v4 offset:8192\n\tds_read_b128 v[140:143], v4 offset:10240\n\tds_read_b128 v[144:147], v4 offset:12288\n\tds_read_b64_tr_b16 v[180:181], v13 offset:1024\n\tds_read_b64_tr_b16 v[182:183], v13 offset:1536\n\tds_read_b64_tr_b16 v[184:185], v13 offset:2048\n\t"
            "ds_read_b64_tr_b16 v[186:187], v13 offset:2560\n\ts_waitcnt lgkmcnt(10)\n\tv_mfma_f32_16x16x32_bf16 v[36:39], v[176:179], v[120:123], 0\n\ts_waitcnt lgkmcnt(9)\n\tv_mfma_f32_16x16x32_bf16 v[48:51], v[176:179], v[124:127], 0\n\ts_waitcnt lgkmcnt(8)\n\t"
            "v_mfma_f32_16x16x32_bf16 v[60:63], v[176:179], v[128:131], 0\n\tds_read_b64_tr_b16 v[188:189], v13 offset:12288\n\tds_read_b64_tr_b16 v[190:191], v13 offset:12800\n\ts_waitcnt lgkmcnt(9)\n\tv_mfma_f32_16x16x32_bf16 v[72:75], v[176:179], v[132:135], 0\n\tds_read_b128 v[148:151], v9\n\t"
            "s_waitcnt lgkmcnt(9)\n\tv_mfma_f32_16x16x32_bf16 v[84:87], v[176:179], v[136:139], 0\n\tds_read_b128 v[152:155], v5 offset:2048\n\ts_waitcnt lgkmcnt(9)\n\tv_mfma_f32_16x16x32_bf16 v[96:99], v[176:179], v[140:143], 0\n\tds_read_b128 v[156:159], v5 offset:4096\n\t"
            "s_waitcnt lgkmcnt(9)\n\tv_mfma_f32_16x16x32_bf16 v[108:111], v[176:179], v[144:147], 0\n\tds_read_b128 v[160:163], v5 offset:6144\n\ts_waitcnt lgkmcnt(8)\n\tv_mfma_f32_16x16x32_bf16 v[40:43], v[180:183], v[120:123], 0\n\tds_read_b128 v[164:167], v5 offset:8192\n\t"
            "v_mfma_f32_16x16x32_bf16 v[52:55], v[180:183], v[124:127], 0\n\tds_read_b128 v[168:171], v5 offset:10240\n\tv_mfma_f32_16x16x32_bf16 v[64:67], v[180:183], v[128:131], 0\n\tds_read_b128 v[172:175], v5 offset:12288\n\tv_mfma_f32_16x16x32_bf16 v[76:79], v[180:183], v[132:135], 0\n\tds_read_b64_tr_b16 v[192:193], v13 offset:13312\n\t"
            "ds_read_b64_tr_b16 v[194:195], v13 offset:13824\n\tv_mfma_f32_16x16x32_bf16 v[88:91], v[180:183], v[136:139], 0\n\tds_read_b64_tr_b16 v[196:197], v13 offset:14336\n\tds_read_b64_tr_b16 v[198:199], v13 offset:14848\n\tv_mfma_f32_16x16x32_bf16 v[100:103], v[180:183], v[140:143], 0\n\tv_mfma_f32_16x16x32_bf16 v[112:115], v[180:183], v[144:147], 0\n\t"
            "s_waitcnt lgkmcnt(13)\n\tv_mfma_f32_16x16x32_bf16 v[44:47], v[120:123], v[184:187], 0\n\tv_mfma_f32_16x16x32_bf16 v[56:59], v[124:127], v[184:187], 0\n\tv_mfma_f32_16x16x32_bf16 v[68:71], v[128:131], v[184:187], 0\n\tv_mfma_f32_16x16x32_bf16 v[80:83], v[132:135], v[184:187], 0\n\tv_mfma_f32_16x16x32_bf16 v[92:95], v[136:139], v[184:187], 0\n\t"
            "v_mfma_f32_16x16x32_bf16 v[104:107], v[140:143], v[184:187], 0\n\tv_mfma_f32_16x16x32_bf16 v[116:119], v[144:147], v[184:187], 0\n\ts_waitcnt lgkmcnt(10)\n\tv_mfma_f32_16x16x32_bf16 v[36:39], v[188:191], v[148:151], v[36:39]\n\ts_waitcnt lgkmcnt(9)\n\tv_mfma_f32_16x16x32_bf16 v[48:51], v[188:191], v[152:155], v[48:51]\n\t"
            "s_waitcnt lgkmcnt(8)\n\tv_mfma_f32_16x16x32_bf16 v[60:63], v[188:191], v[156:159], v[60:63]\n\ts_waitcnt lgkmcnt(7)\n\tv_mfma_f32_16x16x32_bf16 v[72:75], v[188:191], v[160:163], v[72:75]\n\ts_waitcnt lgkmcnt(6)\n\tv_mfma_f32_16x16x32_bf16 v[84:87], v[188:191], v[164:167], v[84:87]\n\t"
            "s_waitcnt lgkmcnt(5)\n\tv_mfma_f32_16x16x32_bf16 v[96:99], v[188:191], v[168:171], v[96:99]\n\ts_waitcnt lgkmcnt(4)\n\tv_mfma_f32_16x16x32_bf16 v[108:111], v[188:191], v[172:175], v[108:111]\n\ts_waitcnt lgkmcnt(2)\n\tv_mfma_f32_16x16x32_bf16 v[40:43], v[192:195], v[148:151], v[40:43]\n\t"
            "v_mfma_f32_16x16x32_bf16 v[52:55], v[192:195], v[152:155], v[52:55]\n\tv_mfma_f32_16x16x32_bf16 v[64:67], v[192:195], v[156:159], v[64:67]\n\tv_mfma_f32_16x16x32_bf16 v[76:79], v[192:195], v[160:163], v[76:79]\n\ts_waitcnt vmcnt(0) lgkmcnt(0)\n\ts_barrier\n\t"
            "ds_read_b64_tr_b16 v[176:177], v13 offset:38912\n\tds_read_b64_tr_b16 v[178:179], v13 offset:39424\n\tv_mfma_f32_16x16x32_bf16 v[88:91], v[192:195], v[164:167], v[88:91]\n\tds_read_b128 v[120:123], v10\n\tv_mfma_f32_16x16x32_bf16 v[100:103], v[192:195], v[168:171], v[100:103]\n\tds_read_b128 v[124:127], v4 offset:40960\n\t"
            "v_mfma_f32_16x16x32_bf16 v[112:115], v[192:195], v[172:175], v[112:115]\n\tds_read_b128 v[128:131], v4 offset:43008\n\tv_mfma_f32_16x16x32_bf16 v[44:47], v[148:151], v[196:199], v[44:47]\n\tds_read_b128 v[132:135], v4 offset:45056\n\tv_mfma_f32_16x16x32_bf16 v[56:59], v[152:155], v[196:199], v[56:59]\n\tds_read_b128 v[136:139], v4 offset:47104\n\t"
            "v_mfma_f32_16x16x32_bf16 v[68:71], v[156:159], v[196:199], v[68:71]\n\tds_read_b128 v[140:143], v4 offset:49152\n\tv_mfma_f32_16x16x32_bf16 v[80:83], v[160:163], v[196:199], v[80:83]\n\tds_read_b128 v[144:147], v4 offset:51200\n\tv_mfma_f32_16x16x32_bf16 v[92:95], v[164:167], v[196:199], v[92:95]\n\tds_read_b64_tr_b16 v[180:181], v13 offset:39936\n\t"
            "ds_read_b64_tr_b16 v[182:183], v13 offset:40448\n\tv_mfma_f32_16x16x32_bf16 v[104:107], v[168:171], v[196:199], v[104:107]\n\tds_read_b64_tr_b16 v[184:185], v13 offset:40960\n\tds_read_b64_tr_b16 v[186:187], v13 offset:41472\n\tv_mfma_f32_16x16x32_bf16 v[116:119], v[172:175], v[196:199], v[116:119]\n\ts_waitcnt lgkmcnt(10)\n\t"
            "v_mfma_f32_16x16x32_bf16 v[36:39], v[176:179], v[120:123], v[36:39]\n\ts_waitcnt lgkmcnt(9)\n\tv_mfma_f32_16x16x32_bf16 v[48:51], v[176:179], v[124:127], v[48:51]\n\ts_waitcnt lgkmcnt(8)\n\tv_mfma_f32_16x16x32_bf16 v[60:63], v[176:179], v[128:131], v[60:63]\n\tds_read_b64_tr_b16 v[188:189], v13 offset:51200\n\t"
            "ds_read_b64_tr_b16 v[190:191], v13 offset:51712\n\ts_waitcnt lgkmcnt(9)\n\tv_mfma_f32_16x16x32_bf16 v[72:75], v[176:179], v[132:135], v[72:75]\n\tds_read_b128 v[148:151], v11\n\ts_waitcnt lgkmcnt(9)\n\tv_mfma_f32_16x16x32_bf16 v[84:87], v[176:179], v[136:139], v[84:87]\n\t"
            "ds_read_b128 v[152:155], v5 offset:40960\n\ts_waitcnt lgkmcnt(9)\n\tv_mfma_f32_16x16x32_bf16 v[96:99], v[176:179], v[140:143], v[96:99]\n\tds_read_b128 v[156:159], v5 offset:43008\n\ts_waitcnt lgkmcnt(9)\n\tv_mfma_f32_16x16x32_bf16 v[108:111], v[176:179], v[144:147], v[108:111]\n\t"
            "ds_read_b128 v[160:163], v5 offset:45056\n\ts_waitcnt lgkmcnt(8)\n\tv_mfma_f32_16x16x32_bf16 v[40:43], v[180:183], v[120:123], v[40:43]\n\tds_read_b128 v[164:167], v5 offset:47104\n\tv_mfma_f32_16x16x32_bf16 v[52:55], v[180:183], v[124:127], v[52:55]\n\tds_read_b128 v[168:171], v5 offset:49152\n\t"
            "v_mfma_f32_16x16x32_bf16 v[64:67], v[180:183], v[128:131], v[64:67]\n\tds_read_b128 v[172:175], v5 offset:51200\n\tv_mfma_f32_16x16x32_bf16 v[76:79], v[180:183], v[132:135], v[76:79]\n\tds_read_b64_tr_b16 v[192:193], v13 offset:52224\n\tds_read_b64_tr_b16 v[194:195], v13 offset:52736\n\tv_mfma_f32_16x16x32_bf16 v[88:91], v[180:183], v[136:139], v[88:91]\n\t"
            "ds_read_b64_tr_b16 v[196:197], v13 offset:53248\n\tds_read_b64_tr_b16 v[198:199], v13 offset:53760\n\tv_mfma_f32_16x16x32_bf16 v[100:103], v[180:183], v[140:143], v[100:103]\n\tv_mfma_f32_16x16x32_bf16 v[112:115], v[180:183], v[144:147], v[112:115]\n\ts_waitcnt lgkmcnt(13)\n\tv_mfma_f32_16x16x32_bf16 v[44:47], v[120:123], v[184:187], v[44:47]\n\t"
            "v_mfma_f32_16x16x32_bf16 v[56:59], v[124:127], v[184:187], v[56:59]\n\tv_mfma_f32_16x16x32_bf16 v[68:71], v[128:131], v[184:187], v[68:71]\n\tv_mfma_f32_16x16x32_bf16 v[80:83], v[132:135], v[184:187], v[80:83]\n\tv_mfma_f32_16x16x32_bf16 v[92:95], v[136:139], v[184:187], v[92:95]\n\tv_mfma_f32_16x16x32_bf16 v[104:107], v[140:143], v[184:187], v[104:107]\n\tv_mfma_f32_16x16x32_bf16 v[116:119], v[144:147], v[184:187], v[116:119]\n\t"
            "s_waitcnt lgkmcnt(10)\n\tv_mfma_f32_16x16x32_bf16 v[36:39], v[188:191], v[148:151], v[36:39]\n\ts_waitcnt lgkmcnt(9)\n\tv_mfma_f32_16x16x32_bf16 v[48:51], v[188:191], v[152:155], v[48:51]\n\ts_waitcnt lgkmcnt(8)\n\tv_mfma_f32_16x16x32_bf16 v[60:63], v[188:191], v[156:159], v[60:63]\n\t"
            "s_waitcnt lgkmcnt(7)\n\tv_mfma_f32_16x16x32_bf16 v[72:75], v[188:191], v[160:163], v[72:75]\n\ts_waitcnt lgkmcnt(6)\n\tv_mfma_f32_16x16x32_bf16 v[84:87], v[188:191], v[164:167], v[84:87]\n\ts_waitcnt lgkmcnt(5)\n\tv_mfma_f32_16x16x32_bf16 v[96:99], v[188:191], v[168:171], v[96:99]\n\t"
            "s_waitcnt lgkmcnt(4)\n\tv_mfma_f32_16x16x32_bf16 v[108:111], v[188:191], v[172:175], v[108:111]\n\ts_waitcnt lgkmcnt(2)\n\tv_mfma_f32_16x16x32_bf16 v[40:43], v[192:195], v[148:151], v[40:43]\n\tv_mfma_f32_16x16x32_bf16 v[52:55], v[192:195], v[152:155], v[52:55]\n\tv_mfma_f32_16x16x32_bf16 v[64:67], v[192:195], v[156:159], v[64:67]\n\t"
            "v_mfma_f32_16x16x32_bf16 v[76:79], v[192:195], v[160:163], v[76:79]\n\ts_waitcnt lgkmcnt(0)\n\ts_barrier\n\t"
            "ds_read_b64_tr_b16 v[176:177], v12 offset:0\n\tds_read_b64_tr_b16 v[178:179], v12 offset:512\n\tv_mfma_f32_16x16x32_bf16 v[88:91], v[192:195], v[164:167], v[88:91]\n\tds_read_b128 v[120:123], v6\n\tv_mfma_f32_16x16x32_bf16 v[100:103], v[192:195], v[168:171], v[100:103]\n\tds_read_b128 v[124:127], v2 offset:2048\n\t"
            "v_mfma_f32_16x16x32_bf16 v[112:115], v[192:195], v[172:175], v[112:115]\n\tds_read_b128 v[128:131], v2 offset:4096\n\tv_mfma_f32_16x16x32_bf16 v[44:47], v[148:151], v[196:199], v[44:47]\n\tds_read_b128 v[132:135], v2 offset:6144\n\tv_mfma_f32_16x16x32_bf16 v[56:59], v[152:155], v[196:199], v[56:59]\n\tds_read_b128 v[136:139], v2 offset:8192\n\t"
            "v_mfma_f32_16x16x32_bf16 v[68:71], v[156:159], v[196:199], v[68:71]\n\tds_read_b128 v[140:143], v2 offset:10240\n\tv_mfma_f32_16x16x32_bf16 v[80:83], v[160:163], v[196:199], v[80:83]\n\tds_read_b128 v[144:147], v2 offset:12288\n\tv_mfma_f32_16x16x32_bf16 v[92:95], v[164:167], v[196:199], v[92:95]\n\tds_read_b64_tr_b16 v[180:181], v12 offset:1024\n\t"
            "ds_read_b64_tr_b16 v[182:183], v12 offset:1536\n\tv_mfma_f32_16x16x32_bf16 v[104:107], v[168:171], v[196:199], v[104:107]\n\tds_read_b64_tr_b16 v[184:185], v12 offset:2048\n\tds_read_b64_tr_b16 v[186:187], v12 offset:2560\n\tv_mfma_f32_16x16x32_bf16 v[116:119], v[172:175], v[196:199], v[116:119]\n\ts_waitcnt lgkmcnt(10)\n\t"
            "v_mfma_f32_16x16x32_bf16 v[36:39], v[176:179], v[120:123], v[36:39]\n\ts_waitcnt lgkmcnt(9)\n\tv_mfma_f32_16x16x32_bf16 v[48:51], v[176:179], v[124:127], v[48:51]\n\ts_waitcnt lgkmcnt(8)\n\tv_mfma_f32_16x16x32_bf16 v[60:63], v[176:179], v[128:131], v[60:63]\n\tds_read_b64_tr_b16 v[188:189], v12 offset:12288\n\t"
            "ds_read_b64_tr_b16 v[190:191], v12 offset:12800\n\ts_waitcnt lgkmcnt(9)\n\tv_mfma_f32_16x16x32_bf16 v[72:75], v[176:179], v[132:135], v[72:75]\n\tds_read_b128 v[148:151], v7\n\ts_waitcnt lgkmcnt(9)\n\tv_mfma_f32_16x16x32_bf16 v[84:87], v[176:179], v[136:139], v[84:87]\n\t"
            "ds_read_b128 v[152:155], v3 offset:2048\n\ts_waitcnt lgkmcnt(9)\n\tv_mfma_f32_16x16x32_bf16 v[96:99], v[176:179], v[140:143], v[96:99]\n\tds_read_b128 v[156:159], v3 offset:4096\n\ts_waitcnt lgkmcnt(9)\n\tv_mfma_f32_16x16x32_bf16 v[108:111], v[176:179], v[144:147], v[108:111]\n\t"
            "ds_read_b128 v[160:163], v3 offset:6144\n\ts_waitcnt lgkmcnt(8)\n\tv_mfma_f32_16x16x32_bf16 v[40:43], v[180:183], v[120:123], v[40:43]\n\tds_read_b128 v[164:167], v3 offset:8192\n\tv_mfma_f32_16x16x32_bf16 v[52:55], v[180:183], v[124:127], v[52:55]\n\tds_read_b128 v[168:171], v3 offset:10240\n\t"
            "v_mfma_f32_16x16x32_bf16 v[64:67], v[180:183], v[128:131], v[64:67]\n\tds_read_b128 v[172:175], v3 offset:12288\n\tv_mfma_f32_16x16x32_bf16 v[76:79], v[180:183], v[132:135], v[76:79]\n\tds_read_b64_tr_b16 v[192:193], v12 offset:13312\n\tds_read_b64_tr_b16 v[194:195], v12 offset:13824\n\tv_mfma_f32_16x16x32_bf16 v[88:91], v[180:183], v[136:139], v[88:91]\n\t"
            "ds_read_b64_tr_b16 v[196:197], v12 offset:14336\n\tds_read_b64_tr_b16 v[198:199], v12 offset:14848\n\tv_mfma_f32_16x16x32_bf16 v[100:103], v[180:183], v[140:143], v[100:103]\n\tv_mfma_f32_16x16x32_bf16 v[112:115], v[180:183], v[144:147], v[112:115]\n\ts_waitcnt lgkmcnt(13)\n\tv_mfma_f32_16x16x32_bf16 v[44:47], v[120:123], v[184:187], v[44:47]\n\t"
            "v_mfma_f32_16x16x32_bf16 v[56:59], v[124:127], v[184:187], v[56:59]\n\tv_mfma_f32_16x16x32_bf16 v[68:71], v[128:131], v[184:187], v[68:71]\n\tv_mfma_f32_16x16x32_bf16 v[80:83], v[132:135], v[184:187], v[80:83]\n\tv_mfma_f32_16x16x32_bf16 v[92:95], v[136:139], v[184:187], v[92:95]\n\tv_mfma_f32_16x16x32_bf16 v[104:107], v[140:143], v[184:187], v[104:107]\n\tv_mfma_f32_16x16x32_bf16 v[116:119], v[144:147], v[184:187], v[116:119]\n\t"
            "s_waitcnt lgkmcnt(10)\n\tv_mfma_f32_16x16x32_bf16 v[36:39], v[188:191], v[148:151], v[36:39]\n\ts_waitcnt lgkmcnt(9)\n\tv_mfma_f32_16x16x32_bf16 v[48:51], v[188:191], v[152:155], v[48:51]\n\ts_waitcnt lgkmcnt(8)\n\tv_mfma_f32_16x16x32_bf16 v[60:63], v[188:191], v[156:159], v[60:63]\n\t"
            "s_waitcnt lgkmcnt(7)\n\tv_mfma_f32_16x16x32_bf16 v[72:75], v[188:191], v[160:163], v[72:75]\n\ts_waitcnt lgkmcnt(6)\n\tv_mfma_f32_16x16x32_bf16 v[84:87], v[188:191], v[164:167], v[84:87]\n\ts_waitcnt lgkmcnt(5)\n\tv_mfma_f32_16x16x32_bf16 v[96:99], v[188:191], v[168:171], v[96:99]\n\t"
            "s_waitcnt lgkmcnt(4)\n\tv_mfma_f32_16x16x32_bf16 v[108:111], v[188:191], v[172:175], v[108:111]\n\ts_waitcnt lgkmcnt(2)\n\tv_mfma_f32_16x16x32_bf16 v[40:43], v[192:195], v[148:151], v[40:43]\n\tv_mfma_f32_16x16x32_bf16 v[52:55], v[192:195], v[152:155], v[52:55]\n\tv_mfma_f32_16x16x32_bf16 v[64:67], v[192:195], v[156:159], v[64:67]\n\t"
            "v_mfma_f32_16x16x32_bf16 v[76:79], v[192:195], v[160:163], v[76:79]\n\ts_waitcnt lgkmcnt(0)\n\ts_barrier\n\t"
            "ds_read_b64_tr_b16 v[176:177], v13 offset:0\n\tds_read_b64_tr_b16 v[178:179], v13 offset:512\n\tv_mfma_f32_16x16x32_bf16 v[88:91], v[192:195], v[164:167], v[88:91]\n\tds_read_b128 v[120:123], v8\n\tv_mfma_f32_16x16x32_bf16 v[100:103], v[192:195], v[168:171], v[100:103]\n\tds_read_b128 v[124:127], v4 offset:2048\n\t"
            "v_mfma_f32_16x16x32_bf16 v[112:115], v[192:195], v[172:175], v[112:115]\n\tds_read_b128 v[128:131], v4 offset:4096\n\tv_mfma_f32_16x16x32_bf16 v[44:47], v[148:151], v[196:199], v[44:47]\n\tds_read_b128 v[132:135], v4 offset:6144\n\tv_mfma_f32_16x16x32_bf16 v[56:59], v[152:155], v[196:199], v[56:59]\n\tds_read_b128 v[136:139], v4 offset:8192\n\t"
            "v_mfma_f32_16x16x32_bf16 v[68:71], v[156:159], v[196:199], v[68:71]\n\tds_read_b128 v[140:143], v4 offset:10240\n\tv_mfma_f32_16x16x32_bf16 v[80:83], v[160:163], v[196:199], v[80:83]\n\tds_read_b128 v[144:147], v4 offset:12288\n\tv_mfma_f32_16x16x32_bf16 v[92:95], v[164:167], v[196:199], v[92:95]\n\tds_read_b64_tr_b16 v[180:181], v13 offset:1024\n\t"
            "ds_read_b64_tr_b16 v[182:183], v13 offset:1536\n\tv_mfma_f32_16x16x32_bf16 v[104:107], v[168:171], v[196:199], v[104:107]\n\tds_read_b64_tr_b16 v[184:185], v13 offset:2048\n\tds_read_b64_tr_b16 v[186:187], v13 offset:2560\n\tv_mfma_f32_16x16x32_bf16 v[116:119], v[172:175], v[196:199], v[116:119]\n\ts_waitcnt lgkmcnt(10)\n\t"
            "v_mfma_f32_16x16x32_bf16 v[36:39], v[176:179], v[120:123], v[36:39]\n\ts_waitcnt lgkmcnt(9)\n\tv_mfma_f32_16x16x32_bf16 v[48:51], v[176:179], v[124:127], v[48:51]\n\ts_waitcnt lgkmcnt(8)\n\tv_mfma_f32_16x16x32_bf16 v[60:63], v[176:179], v[128:131], v[60:63]\n\tds_read_b64_tr_b16 v[188:189], v13 offset:12288\n\t"
            "ds_read_b64_tr_b16 v[190:191], v13 offset:12800\n\ts_waitcnt lgkmcnt(9)\n\tv_mfma_f32_16x16x32_bf16 v[72:75], v[176:179], v[132:135], v[72:75]\n\tds_read_b128 v[148:151], v9\n\ts_waitcnt lgkmcnt(9)\n\tv_mfma_f32_16x16x32_bf16 v[84:87], v[176:179], v[136:139], v[84:87]\n\t"
            "ds_read_b128 v[152:155], v5 offset:2048\n\ts_waitcnt lgkmcnt(9)\n\tv_mfma_f32_16x16x32_bf16 v[96:99], v[176:179], v[140:143], v[96:99]\n\tds_read_b128 v[156:159], v5 offset:4096\n\ts_waitcnt lgkmcnt(9)\n\tv_mfma_f32_16x16x32_bf16 v[108:111], v[176:179], v[144:147], v[108:111]\n\t"
            "ds_read_b128 v[160:163], v5 offset:6144\n\ts_waitcnt lgkmcnt(8)\n\tv_mfma_f32_16x16x32_bf16 v[40:43], v[180:183], v[120:123], v[40:43]\n\tds_read_b128 v[164:167], v5 offset:8192\n\tv_mfma_f32_16x16x32_bf16 v[52:55], v[180:183], v[124:127], v[52:55]\n\tds_read_b128 v[168:171], v5 offset:10240\n\t"
            "v_mfma_f32_16x16x32_bf16 v[64:67], v[180:183], v[128:131], v[64:67]\n\tds_read_b128 v[172:175], v5 offset:12288\n\tv_mfma_f32_16x16x32_bf16 v[76:79], v[180:183], v[132:135], v[76:79]\n\tds_read_b64_tr_b16 v[192:193], v13 offset:13312\n\tds_read_b64_tr_b16 v[194:195], v13 offset:13824\n\tv_mfma_f32_16x16x32_bf16 v[88:91], v[180:183], v[136:139], v[88:91]\n\t"
            "ds_read_b64_tr_b16 v[196:197], v13 offset:14336\n\tds_read_b64_tr_b16 v[198:199], v13 offset:14848\n\tv_mfma_f32_16x16x32_bf16 v[100:103], v[180:183], v[140:143], v[100:103]\n\tv_mfma_f32_16x16x32_bf16 v[112:115], v[180:183], v[144:147], v[112:115]\n\ts_waitcnt lgkmcnt(13)\n\tv_mfma_f32_16x16x32_bf16 v[44:47], v[120:123], v[184:187], v[44:47]\n\t"
            "v_mfma_f32_16x16x32_bf16 v[56:59], v[124:127], v[184:187], v[56:59]\n\tv_mfma_f32_16x16x32_bf16 v[68:71], v[128:131], v[184:187], v[68:71]\n\tv_mfma_f32_16x16x32_bf16 v[80:83], v[132:135], v[184:187], v[80:83]\n\tv_mfma_f32_16x16x32_bf16 v[92:95], v[136:139], v[184:187], v[92:95]\n\tv_mfma_f32_16x16x32_bf16 v[104:107], v[140:143], v[184:187], v[104:107]\n\tv_mfma_f32_16x16x32_bf16 v[116:119], v[144:147], v[184:187], v[116:119]\n\t"
            "s_waitcnt lgkmcnt(10)\n\tv_mfma_f32_16x16x32_bf16 v[36:39], v[188:191], v[148:151], v[36:39]\n\ts_waitcnt lgkmcnt(9)\n\tv_mfma_f32_16x16x32_bf16 v[48:51], v[188:191], v[152:155], v[48:51]\n\ts_waitcnt lgkmcnt(8)\n\tv_mfma_f32_16x16x32_bf16 v[60:63], v[188:191], v[156:159], v[60:63]\n\t"
            "s_waitcnt lgkmcnt(7)\n\tv_mfma_f32_16x16x32_bf16 v[72:75], v[188:191], v[160:163], v[72:75]\n\ts_waitcnt lgkmcnt(6)\n\tv_mfma_f32_16x16x32_bf16 v[84:87], v[188:191], v[164:167], v[84:87]\n\ts_waitcnt lgkmcnt(5)\n\tv_mfma_f32_16x16x32_bf16 v[96:99], v[188:191], v[168:171], v[96:99]\n\t"
            "s_waitcnt lgkmcnt(4)\n\tv_mfma_f32_16x16x32_bf16 v[108:111], v[188:191], v[172:175], v[108:111]\n\ts_waitcnt lgkmcnt(2)\n\tv_mfma_f32_16x16x32_bf16 v[40:43], v[192:195], v[148:151], v[40:43]\n\tv_mfma_f32_16x16x32_bf16 v[52:55], v[192:195], v[152:155], v[52:55]\n\tv_mfma_f32_16x16x32_bf16 v[64:67], v[192:195], v[156:159], v[64:67]\n\t"
            "v_mfma_f32_16x16x32_bf16 v[76:79], v[192:195], v[160:163], v[76:79]\n\ts_waitcnt lgkmcnt(0)\n\ts_barrier\n\t"
            "ds_read_b64_tr_b16 v[176:177], v13 offset:38912\n\tds_read_b64_tr_b16 v[178:179], v13 offset:39424\n\tv_mfma_f32_16x16x32_bf16 v[88:91], v[192:195], v[164:167], v[88:91]\n\tds_read_b128 v[120:123], v10\n\tv_mfma_f32_16x16x32_bf16 v[100:103], v[192:195], v[168:171], v[100:103]\n\tds_read_b128 v[124:127], v4 offset:40960\n\t"
            "v_mfma_f32_16x16x32_bf16 v[112:115], v[192:195], v[172:175], v[112:115]\n\tds_read_b128 v[128:131], v4 offset:43008\n\tv_mfma_f32_16x16x32_bf16 v[44:47], v[148:151], v[196:199], v[44:47]\n\tds_read_b128 v[132:135], v4 offset:45056\n\tv_mfma_f32_16x16x32_bf16 v[56:59], v[152:155], v[196:199], v[56:59]\n\tds_read_b128 v[136:139], v4 offset:47104\n\t"
            "v_mfma_f32_16x16x32_bf16 v[68:71], v[156:159], v[196:199], v[68:71]\n\tds_read_b128 v[140:143], v4 offset:49152\n\tv_mfma_f32_16x16x32_bf16 v[80:83], v[160:163], v[196:199], v[80:83]\n\tds_read_b128 v[144:147], v4 offset:51200\n\tv_mfma_f32_16x16x32_bf16 v[92:95], v[164:167], v[196:199], v[92:95]\n\tds_read_b64_tr_b16 v[180:181], v13 offset:39936\n\t"
            "ds_read_b64_tr_b16 v[182:183], v13 offset:40448\n\tv_mfma_f32_16x16x32_bf16 v[104:107], v[168:171], v[196:199], v[104:107]\n\tds_read_b64_tr_b16 v[184:185], v13 offset:40960\n\tds_read_b64_tr_b16 v[186:187], v13 offset:41472\n\tv_mfma_f32_16x16x32_bf16 v[116:119], v[172:175], v[196:199], v[116:119]\n\ts_waitcnt lgkmcnt(10)\n\t"
            "v_mfma_f32_16x16x32_bf16 v[36:39], v[176:179], v[120:123], v[36:39]\n\ts_waitcnt lgkmcnt(9)\n\tv_mfma_f32_16x16x32_bf16 v[48:51], v[176:179], v[124:127], v[48:51]\n\ts_waitcnt lgkmcnt(8)\n\tv_mfma_f32_16x16x32_bf16 v[60:63], v[176:179], v[128:131], v[60:63]\n\tds_read_b64_tr_b16 v[188:189], v13 offset:51200\n\t"
            "ds_read_b64_tr_b16 v[190:191], v13 offset:51712\n\ts_waitcnt lgkmcnt(9)\n\tv_mfma_f32_16x16x32_bf16 v[72:75], v[176:179], v[132:135], v[72:75]\n\tds_read_b128 v[148:151], v11\n\ts_waitcnt lgkmcnt(9)\n\tv_mfma_f32_16x16x32_bf16 v[84:87], v[176:179], v[136:139], v[84:87]\n\t"
            "ds_read_b128 v[152:155], v5 offset:40960\n\ts_waitcnt lgkmcnt(9)\n\tv_mfma_f32_16x16x32_bf16 v[96:99], v[176:179], v[140:143], v[96:99]\n\tds_read_b128 v[156:159], v5 offset:43008\n\ts_waitcnt lgkmcnt(9)\n\tv_mfma_f32_16x16x32_bf16 v[108:111], v[176:179], v[144:147], v[108:111]\n\t"
            "ds_read_b128 v[160:163], v5 offset:45056\n\ts_waitcnt lgkmcnt(8)\n\tv_mfma_f32_16x16x32_bf16 v[40:43], v[180:183], v[120:123], v[40:43]\n\tds_read_b128 v[164:167], v5 offset:47104\n\tv_mfma_f32_16x16x32_bf16 v[52:55], v[180:183], v[124:127], v[52:55]\n\tds_read_b128 v[168:171], v5 offset:49152\n\t"
            "v_mfma_f32_16x16x32_bf16 v[64:67], v[180:183], v[128:131], v[64:67]\n\tds_read_b128 v[172:175], v5 offset:51200\n\tv_mfma_f32_16x16x32_bf16 v[76:79], v[180:183], v[132:135], v[76:79]\n\tds_read_b64_tr_b16 v[192:193], v13 offset:52224\n\tds_read_b64_tr_b16 v[194:195], v13 offset:52736\n\tv_mfma_f32_16x16x32_bf16 v[88:91], v[180:183], v[136:139], v[88:91]\n\t"
            "ds_read_b64_tr_b16 v[196:197], v13 offset:53248\n\tds_read_b64_tr_b16 v[198:199], v13 offset:53760\n\tv_mfma_f32_16x16x32_bf16 v[100:103], v[180:183], v[140:143], v[100:103]\n\tv_mfma_f32_16x16x32_bf16 v[112:115], v[180:183], v[144:147], v[112:115]\n\ts_waitcnt lgkmcnt(13)\n\tv_mfma_f32_16x16x32_bf16 v[44:47], v[120:123], v[184:187], v[44:47]\n\t"
            "v_mfma_f32_16x16x32_bf16 v[56:59], v[124:127], v[184:187], v[56:59]\n\tv_mfma_f32_16x16x32_bf16 v[68:71], v[128:131], v[184:187], v[68:71]\n\tv_mfma_f32_16x16x32_bf16 v[80:83], v[132:135], v[184:187], v[80:83]\n\tv_mfma_f32_16x16x32_bf16 v[92:95], v[136:139], v[184:187], v[92:95]\n\tv_mfma_f32_16x16x32_bf16 v[104:107], v[140:143], v[184:187], v[104:107]\n\tv_mfma_f32_16x16x32_bf16 v[116:119], v[144:147], v[184:187], v[116:119]\n\t"
            "s_waitcnt lgkmcnt(10)\n\tv_mfma_f32_16x16x32_bf16 v[36:39], v[188:191], v[148:151], v[36:39]\n\ts_waitcnt lgkmcnt(9)\n\tv_mfma_f32_16x16x32_bf16 v[48:51], v[188:191], v[152:155], v[48:51]\n\ts_waitcnt lgkmcnt(8)\n\tv_mfma_f32_16x16x32_bf16 v[60:63], v[188:191], v[156:159], v[60:63]\n\t"
            "s_waitcnt lgkmcnt(7)\n\tv_mfma_f32_16x16x32_bf16 v[72:75], v[188:191], v[160:163], v[72:75]\n\ts_waitcnt lgkmcnt(6)\n\tv_mfma_f32_16x16x32_bf16 v[84:87], v[188:191], v[164:167], v[84:87]\n\ts_waitcnt lgkmcnt(5)\n\tv_mfma_f32_16x16x32_bf16 v[96:99], v[188:191], v[168:171], v[96:99]\n\t"
            "s_waitcnt lgkmcnt(4)\n\tv_mfma_f32_16x16x32_bf16 v[108:111], v[188:191], v[172:175], v[108:111]\n\ts_waitcnt lgkmcnt(2)\n\tv_mfma_f32_16x16x32_bf16 v[40:43], v[192:195], v[148:151], v[40:43]\n\tv_mfma_f32_16x16x32_bf16 v[52:55], v[192:195], v[152:155], v[52:55]\n\tv_mfma_f32_16x16x32_bf16 v[64:67], v[192:195], v[156:159], v[64:67]\n\t"
            "v_mfma_f32_16x16x32_bf16 v[76:79], v[192:195], v[160:163], v[76:79]\n\ts_waitcnt lgkmcnt(0)\n\ts_barrier\n\t"
            "ds_read_b64_tr_b16 v[176:177], v12 offset:0\n\tds_read_b64_tr_b16 v[178:179], v12 offset:512\n\tv_mfma_f32_16x16x32_bf16 v[88:91], v[192:195], v[164:167], v[88:91]\n\tds_read_b128 v[120:123], v6\n\tv_mfma_f32_16x16x32_bf16 v[100:103], v[192:195], v[168:171], v[100:103]\n\tds_read_b128 v[124:127], v2 offset:2048\n\t"
            "v_mfma_f32_16x16x32_bf16 v[112:115], v[192:195], v[172:175], v[112:115]\n\tds_read_b128 v[128:131], v2 offset:4096\n\tv_mfma_f32_16x16x32_bf16 v[44:47], v[148:151], v[196:199], v[44:47]\n\tds_read_b128 v[132:135], v2 offset:6144\n\tv_mfma_f32_16x16x32_bf16 v[56:59], v[152:155], v[196:199], v[56:59]\n\tds_read_b128 v[136:139], v2 offset:8192\n\t"
            "v_mfma_f32_16x16x32_bf16 v[68:71], v[156:159], v[196:199], v[68:71]\n\tds_read_b128 v[140:143], v2 offset:10240\n\tv_mfma_f32_16x16x32_bf16 v[80:83], v[160:163], v[196:199], v[80:83]\n\tds_read_b128 v[144:147], v2 offset:12288\n\tv_mfma_f32_16x16x32_bf16 v[92:95], v[164:167], v[196:199], v[92:95]\n\tds_read_b64_tr_b16 v[180:181], v12 offset:1024\n\t"
            "ds_read_b64_tr_b16 v[182:183], v12 offset:1536\n\tv_mfma_f32_16x16x32_bf16 v[104:107], v[168:171], v[196:199], v[104:107]\n\tds_read_b64_tr_b16 v[184:185], v12 offset:2048\n\tds_read_b64_tr_b16 v[186:187], v12 offset:2560\n\tv_mfma_f32_16x16x32_bf16 v[116:119], v[172:175], v[196:199], v[116:119]\n\ts_waitcnt lgkmcnt(10)\n\t"
            "v_mfma_f32_16x16x32_bf16 v[36:39], v[176:179], v[120:123], v[36:39]\n\ts_waitcnt lgkmcnt(9)\n\tv_mfma_f32_16x16x32_bf16 v[48:51], v[176:179], v[124:127], v[48:51]\n\ts_waitcnt lgkmcnt(8)\n\tv_mfma_f32_16x16x32_bf16 v[60:63], v[176:179], v[128:131], v[60:63]\n\tds_read_b64_tr_b16 v[188:189], v12 offset:12288\n\t"
            "ds_read_b64_tr_b16 v[190:191], v12 offset:12800\n\ts_waitcnt lgkmcnt(9)\n\tv_mfma_f32_16x16x32_bf16 v[72:75], v[176:179], v[132:135], v[72:75]\n\tds_read_b128 v[148:151], v7\n\ts_waitcnt lgkmcnt(9)\n\tv_mfma_f32_16x16x32_bf16 v[84:87], v[176:179], v[136:139], v[84:87]\n\t"
            "ds_read_b128 v[152:155], v3 offset:2048\n\ts_waitcnt lgkmcnt(9)\n\tv_mfma_f32_16x16x32_bf16 v[96:99], v[176:179], v[140:143], v[96:99]\n\tds_read_b128 v[156:159], v3 offset:4096\n\ts_waitcnt lgkmcnt(9)\n\tv_mfma_f32_16x16x32_bf16 v[108:111], v[176:179], v[144:147], v[108:111]\n\t"
            "ds_read_b128 v[160:163], v3 offset:6144\n\ts_waitcnt lgkmcnt(8)\n\tv_mfma_f32_16x16x32_bf16 v[40:43], v[180:183], v[120:123], v[40:43]\n\tds_read_b128 v[164:167], v3 offset:8192\n\tv_mfma_f32_16x16x32_bf16 v[52:55], v[180:183], v[124:127], v[52:55]\n\tds_read_b128 v[168:171], v3 offset:10240\n\t"
            "v_mfma_f32_16x16x32_bf16 v[64:67], v[180:183], v[128:131], v[64:67]\n\tds_read_b128 v[172:175], v3 offset:12288\n\tv_mfma_f32_16x16x32_bf16 v[76:79], v[180:183], v[132:135], v[76:79]\n\tds_read_b64_tr_b16 v[192:193], v12 offset:13312\n\tds_read_b64_tr_b16 v[194:195], v12 offset:13824\n\tv_mfma_f32_16x16x32_bf16 v[88:91], v[180:183], v[136:139], v[88:91]\n\t"
            "ds_read_b64_tr_b16 v[196:197], v12 offset:14336\n\tds_read_b64_tr_b16 v[198:199], v12 offset:14848\n\tv_mfma_f32_16x16x32_bf16 v[100:103], v[180:183], v[140:143], v[100:103]\n\tv_mfma_f32_16x16x32_bf16 v[112:115], v[180:183], v[144:147], v[112:115]\n\ts_waitcnt lgkmcnt(13)\n\tv_mfma_f32_16x16x32_bf16 v[44:47], v[120:123], v[184:187], v[44:47]\n\t"
            "v_mfma_f32_16x16x32_bf16 v[56:59], v[124:127], v[184:187], v[56:59]\n\tv_mfma_f32_16x16x32_bf16 v[68:71], v[128:131], v[184:187], v[68:71]\n\tv_mfma_f32_16x16x32_bf16 v[80:83], v[132:135], v[184:187], v[80:83]\n\tv_mfma_f32_16x16x32_bf16 v[92:95], v[136:139], v[184:187], v[92:95]\n\tv_mfma_f32_16x16x32_bf16 v[104:107], v[140:143], v[184:187], v[104:107]\n\tv_mfma_f32_16x16x32_bf16 v[116:119], v[144:147], v[184:187], v[116:119]\n\t"
            "s_waitcnt lgkmcnt(10)\n\tv_mfma_f32_16x16x32_bf16 v[36:39], v[188:191], v[148:151], v[36:39]\n\ts_waitcnt lgkmcnt(9)\n\tv_mfma_f32_16x16x32_bf16 v[48:51], v[188:191], v[152:155], v[48:51]\n\ts_waitcnt lgkmcnt(8)\n\tv_mfma_f32_16x16x32_bf16 v[60:63], v[188:191], v[156:159], v[60:63]\n\t"
            "s_waitcnt lgkmcnt(7)\n\tv_mfma_f32_16x16x32_bf16 v[72:75], v[188:191], v[160:163], v[72:75]\n\ts_waitcnt lgkmcnt(6)\n\tv_mfma_f32_16x16x32_bf16 v[84:87], v[188:191], v[164:167], v[84:87]\n\ts_waitcnt lgkmcnt(5)\n\tv_mfma_f32_16x16x32_bf16 v[96:99], v[188:191], v[168:171], v[96:99]\n\t"
            "s_waitcnt lgkmcnt(4)\n\tv_mfma_f32_16x16x32_bf16 v[108:111], v[188:191], v[172:175], v[108:111]\n\ts_waitcnt lgkmcnt(2)\n\tv_mfma_f32_16x16x32_bf16 v[40:43], v[192:195], v[148:151], v[40:43]\n\tv_mfma_f32_16x16x32_bf16 v[52:55], v[192:195], v[152:155], v[52:55]\n\tv_mfma_f32_16x16x32_bf16 v[64:67], v[192:195], v[156:159], v[64:67]\n\t"
            "v_mfma_f32_16x16x32_bf16 v[76:79], v[192:195], v[160:163], v[76:79]\n\ts_waitcnt lgkmcnt(0)\n\ts_barrier\n\t"
            "ds_read_b64_tr_b16 v[176:177], v13 offset:0\n\tds_read_b64_tr_b16 v[178:179], v13 offset:512\n\tv_mfma_f32_16x16x32_bf16 v[88:91], v[192:195], v[164:167], v[88:91]\n\tds_read_b128 v[120:123], v8\n\tv_mfma_f32_16x16x32_bf16 v[100:103], v[192:195], v[168:171], v[100:103]\n\tds_read_b128 v[124:127], v4 offset:2048\n\t"
            "v_mfma_f32_16x16x32_bf16 v[112:115], v[192:195], v[172:175], v[112:115]\n\tds_read_b128 v[128:131], v4 offset:4096\n\tv_mfma_f32_16x16x32_bf16 v[44:47], v[148:151], v[196:199], v[44:47]\n\tds_read_b128 v[132:135], v4 offset:6144\n\tv_mfma_f32_16x16x32_bf16 v[56:59], v[152:155], v[196:199], v[56:59]\n\tds_read_b128 v[136:139], v4 offset:8192\n\t"
            "v_mfma_f32_16x16x32_bf16 v[68:71], v[156:159], v[196:199], v[68:71]\n\tds_read_b128 v[140:143], v4 offset:10240\n\tv_mfma_f32_16x16x32_bf16 v[80:83], v[160:163], v[196:199], v[80:83]\n\tds_read_b128 v[144:147], v4 offset:12288\n\tv_mfma_f32_16x16x32_bf16 v[92:95], v[164:167], v[196:199], v[92:95]\n\tds_read_b64_tr_b16 v[180:181], v13 offset:1024\n\t"
            "ds_read_b64_tr_b16 v[182:183], v13 offset:1536\n\tv_mfma_f32_16x16x32_bf16 v[104:107], v[168:171], v[196:199], v[104:107]\n\tds_read_b64_tr_b16 v[184:185], v13 offset:2048\n\tds_read_b64_tr_b16 v[186:187], v13 offset:2560\n\tv_mfma_f32_16x16x32_bf16 v[116:119], v[172:175], v[196:199], v[116:119]\n\ts_waitcnt lgkmcnt(10)\n\t"
            "v_mfma_f32_16x16x32_bf16 v[36:39], v[176:179], v[120:123], v[36:39]\n\ts_waitcnt lgkmcnt(9)\n\tv_mfma_f32_16x16x32_bf16 v[48:51], v[176:179], v[124:127], v[48:51]\n\ts_waitcnt lgkmcnt(8)\n\tv_mfma_f32_16x16x32_bf16 v[60:63], v[176:179], v[128:131], v[60:63]\n\tds_read_b64_tr_b16 v[188:189], v13 offset:12288\n\t"
            "ds_read_b64_tr_b16 v[190:191], v13 offset:12800\n\ts_waitcnt lgkmcnt(9)\n\tv_mfma_f32_16x16x32_bf16 v[72:75], v[176:179], v[132:135], v[72:75]\n\tds_read_b128 v[148:151], v9\n\ts_waitcnt lgkmcnt(9)\n\tv_mfma_f32_16x16x32_bf16 v[84:87], v[176:179], v[136:139], v[84:87]\n\t"
            "ds_read_b128 v[152:155], v5 offset:2048\n\ts_waitcnt lgkmcnt(9)\n\tv_mfma_f32_16x16x32_bf16 v[96:99], v[176:179], v[140:143], v[96:99]\n\tds_read_b128 v[156:159], v5 offset:4096\n\ts_waitcnt lgkmcnt(9)\n\tv_mfma_f32_16x16x32_bf16 v[108:111], v[176:179], v[144:147], v[108:111]\n\t"
            "ds_read_b128 v[160:163], v5 offset:6144\n\ts_waitcnt lgkmcnt(8)\n\tv_mfma_f32_16x16x32_bf16 v[40:43], v[180:183], v[120:123], v[40:43]\n\tds_read_b128 v[164:167], v5 offset:8192\n\tv_mfma_f32_16x16x32_bf16 v[52:55], v[180:183], v[124:127], v[52:55]\n\tds_read_b128 v[168:171], v5 offset:10240\n\t"
            "v_mfma_f32_16x16x32_bf16 v[64:67], v[180:183], v[128:131], v[64:67]\n\tds_read_b128 v[172:175], v5 offset:12288\n\tv_mfma_f32_16x16x32_bf16 v[76:79], v[180:183], v[132:135], v[76:79]\n\tds_read_b64_tr_b16 v[192:193], v13 offset:13312\n\tds_read_b64_tr_b16 v[194:195], v13 offset:13824\n\tv_mfma_f32_16x16x32_bf16 v[88:91], v[180:183], v[136:139], v[88:91]\n\t"
            "ds_read_b64_tr_b16 v[196:197], v13 offset:14336\n\tds_read_b64_tr_b16 v[198:199], v13 offset:14848\n\tv_mfma_f32_16x16x32_bf16 v[100:103], v[180:183], v[140:143], v[100:103]\n\tv_mfma_f32_16x16x32_bf16 v[112:115], v[180:183], v[144:147], v[112:115]\n\ts_waitcnt lgkmcnt(13)\n\tv_mfma_f32_16x16x32_bf16 v[44:47], v[120:123], v[184:187], v[44:47]\n\t"
            "v_mfma_f32_16x16x32_bf16 v[56:59], v[124:127], v[184:187], v[56:59]\n\tv_mfma_f32_16x16x32_bf16 v[68:71], v[128:131], v[184:187], v[68:71]\n\tv_mfma_f32_16x16x32_bf16 v[80:83], v[132:135], v[184:187], v[80:83]\n\tv_mfma_f32_16x16x32_bf16 v[92:95], v[136:139], v[184:187], v[92:95]\n\tv_mfma_f32_16x16x32_bf16 v[104:107], v[140:143], v[184:187], v[104:107]\n\tv_mfma_f32_16x16x32_bf16 v[116:119], v[144:147], v[184:187], v[116:119]\n\t"
            "s_waitcnt lgkmcnt(10)\n\tv_mfma_f32_16x16x32_bf16 v[36:39], v[188:191], v[148:151], v[36:39]\n\ts_waitcnt lgkmcnt(9)\n\tv_mfma_f32_16x16x32_bf16 v[48:51], v[188:191], v[152:155], v[48:51]\n\ts_waitcnt lgkmcnt(8)\n\tv_mfma_f32_16x16x32_bf16 v[60:63], v[188:191], v[156:159], v[60:63]\n\t"
            "s_waitcnt lgkmcnt(7)\n\tv_mfma_f32_16x16x32_bf16 v[72:75], v[188:191], v[160:163], v[72:75]\n\ts_waitcnt lgkmcnt(6)\n\tv_mfma_f32_16x16x32_bf16 v[84:87], v[188:191], v[164:167], v[84:87]\n\ts_waitcnt lgkmcnt(5)\n\tv_mfma_f32_16x16x32_bf16 v[96:99], v[188:191], v[168:171], v[96:99]\n\t"
            "s_waitcnt lgkmcnt(4)\n\tv_mfma_f32_16x16x32_bf16 v[108:111], v[188:191], v[172:175], v[108:111]\n\ts_waitcnt lgkmcnt(2)\n\tv_mfma_f32_16x16x32_bf16 v[40:43], v[192:195], v[148:151], v[40:43]\n\tv_mfma_f32_16x16x32_bf16 v[52:55], v[192:195], v[152:155], v[52:55]\n\tv_mfma_f32_16x16x32_bf16 v[64:67], v[192:195], v[156:159], v[64:67]\n\t"
            "v_mfma_f32_16x16x32_bf16 v[76:79], v[192:195], v[160:163], v[76:79]\n\ts_waitcnt lgkmcnt(0)\n\ts_barrier\n\t"
            "ds_read_b64_tr_b16 v[176:177], v13 offset:38912\n\tds_read_b64_tr_b16 v[178:179], v13 offset:39424\n\tv_mfma_f32_16x16x32_bf16 v[88:91], v[192:195], v[164:167], v[88:91]\n\tds_read_b128 v[120:123], v10\n\tv_mfma_f32_16x16x32_bf16 v[100:103], v[192:195], v[168:171], v[100:103]\n\tds_read_b128 v[124:127], v4 offset:40960\n\t"
            "v_mfma_f32_16x16x32_bf16 v[112:115], v[192:195], v[172:175], v[112:115]\n\tds_read_b128 v[128:131], v4 offset:43008\n\tv_mfma_f32_16x16x32_bf16 v[44:47], v[148:151], v[196:199], v[44:47]\n\tds_read_b128 v[132:135], v4 offset:45056\n\tv_mfma_f32_16x16x32_bf16 v[56:59], v[152:155], v[196:199], v[56:59]\n\tds_read_b128 v[136:139], v4 offset:47104\n\t"
            "v_mfma_f32_16x16x32_bf16 v[68:71], v[156:159], v[196:199], v[68:71]\n\tds_read_b128 v[140:143], v4 offset:49152\n\tv_mfma_f32_16x16x32_bf16 v[80:83], v[160:163], v[196:199], v[80:83]\n\tds_read_b128 v[144:147], v4 offset:51200\n\tv_mfma_f32_16x16x32_bf16 v[92:95], v[164:167], v[196:199], v[92:95]\n\tds_read_b64_tr_b16 v[180:181], v13 offset:39936\n\t"
            "ds_read_b64_tr_b16 v[182:183], v13 offset:40448\n\tv_mfma_f32_16x16x32_bf16 v[104:107], v[168:171], v[196:199], v[104:107]\n\tds_read_b64_tr_b16 v[184:185], v13 offset:40960\n\tds_read_b64_tr_b16 v[186:187], v13 offset:41472\n\tv_mfma_f32_16x16x32_bf16 v[116:119], v[172:175], v[196:199], v[116:119]\n\ts_waitcnt lgkmcnt(10)\n\t"
            "v_mfma_f32_16x16x32_bf16 v[36:39], v[176:179], v[120:123], v[36:39]\n\ts_waitcnt lgkmcnt(9)\n\tv_mfma_f32_16x16x32_bf16 v[48:51], v[176:179], v[124:127], v[48:51]\n\ts_waitcnt lgkmcnt(8)\n\tv_mfma_f32_16x16x32_bf16 v[60:63], v[176:179], v[128:131], v[60:63]\n\tds_read_b64_tr_b16 v[188:189], v13 offset:51200\n\t"
            "ds_read_b64_tr_b16 v[190:191], v13 offset:51712\n\ts_waitcnt lgkmcnt(9)\n\tv_mfma_f32_16x16x32_bf16 v[72:75], v[176:179], v[132:135], v[72:75]\n\tds_read_b128 v[148:151], v11\n\ts_waitcnt lgkmcnt(9)\n\tv_mfma_f32_16x16x32_bf16 v[84:87], v[176:179], v[136:139], v[84:87]\n\t"
            "ds_read_b128 v[152:155], v5 offset:40960\n\ts_waitcnt lgkmcnt(9)\n\tv_mfma_f32_16x16x32_bf16 v[96:99], v[176:179], v[140:143], v[96:99]\n\tds_read_b128 v[156:159], v5 offset:43008\n\ts_waitcnt lgkmcnt(9)\n\tv_mfma_f32_16x16x32_bf16 v[108:111], v[176:179], v[144:147], v[108:111]\n\t"
            "ds_read_b128 v[160:163], v5 offset:45056\n\ts_waitcnt lgkmcnt(8)\n\tv_mfma_f32_16x16x32_bf16 v[40:43], v[180:183], v[120:123], v[40:43]\n\tds_read_b128 v[164:167], v5 offset:47104\n\tv_mfma_f32_16x16x32_bf16 v[52:55], v[180:183], v[124:127], v[52:55]\n\tds_read_b128 v[168:171], v5 offset:49152\n\t"
            "v_mfma_f32_16x16x32_bf16 v[64:67], v[180:183], v[128:131], v[64:67]\n\tds_read_b128 v[172:175], v5 offset:51200\n\tv_mfma_f32_16x16x32_bf16 v[76:79], v[180:183], v[132:135], v[76:79]\n\tds_read_b64_tr_b16 v[192:193], v13 offset:52224\n\tds_read_b64_tr_b16 v[194:195], v13 offset:52736\n\tv_mfma_f32_16x16x32_bf16 v[88:91], v[180:183], v[136:139], v[88:91]\n\t"
            "ds_read_b64_tr_b16 v[196:197], v13 offset:53248\n\tds_read_b64_tr_b16 v[198:199], v13 offset:53760\n\tv_mfma_f32_16x16x32_bf16 v[100:103], v[180:183], v[140:143], v[100:103]\n\tv_mfma_f32_16x16x32_bf16 v[112:115], v[180:183], v[144:147], v[112:115]\n\ts_waitcnt lgkmcnt(13)\n\tv_mfma_f32_16x16x32_bf16 v[44:47], v[120:123], v[184:187], v[44:47]\n\t"
            "v_mfma_f32_16x16x32_bf16 v[56:59], v[124:127], v[184:187], v[56:59]\n\tv_mfma_f32_16x16x32_bf16 v[68:71], v[128:131], v[184:187], v[68:71]\n\tv_mfma_f32_16x16x32_bf16 v[80:83], v[132:135], v[184:187], v[80:83]\n\tv_mfma_f32_16x16x32_bf16 v[92:95], v[136:139], v[184:187], v[92:95]\n\tv_mfma_f32_16x16x32_bf16 v[104:107], v[140:143], v[184:187], v[104:107]\n\tv_mfma_f32_16x16x32_bf16 v[116:119], v[144:147], v[184:187], v[116:119]\n\t"
            "s_waitcnt lgkmcnt(10)\n\tv_mfma_f32_16x16x32_bf16 v[36:39], v[188:191], v[148:151], v[36:39]\n\ts_waitcnt lgkmcnt(9)\n\tv_mfma_f32_16x16x32_bf16 v[48:51], v[188:191], v[152:155], v[48:51]\n\ts_waitcnt lgkmcnt(8)\n\tv_mfma_f32_16x16x32_bf16 v[60:63], v[188:191], v[156:159], v[60:63]\n\t"
            "s_waitcnt lgkmcnt(7)\n\tv_mfma_f32_16x16x32_bf16 v[72:75], v[188:191], v[160:163], v[72:75]\n\ts_waitcnt lgkmcnt(6)\n\tv_mfma_f32_16x16x32_bf16 v[84:87], v[188:191], v[164:167], v[84:87]\n\ts_waitcnt lgkmcnt(5)\n\tv_mfma_f32_16x16x32_bf16 v[96:99], v[188:191], v[168:171], v[96:99]\n\t"
            "s_waitcnt lgkmcnt(4)\n\tv_mfma_f32_16x16x32_bf16 v[108:111], v[188:191], v[172:175], v[108:111]\n\ts_waitcnt lgkmcnt(2)\n\tv_mfma_f32_16x16x32_bf16 v[40:43], v[192:195], v[148:151], v[40:43]\n\tv_mfma_f32_16x16x32_bf16 v[52:55], v[192:195], v[152:155], v[52:55]\n\tv_mfma_f32_16x16x32_bf16 v[64:67], v[192:195], v[156:159], v[64:67]\n\t"
            "v_mfma_f32_16x16x32_bf16 v[76:79], v[192:195], v[160:163], v[76:79]\n\ts_waitcnt lgkmcnt(0)\n\ts_barrier\n\t"
            "ds_read_b64_tr_b16 v[176:177], v12 offset:0\n\tds_read_b64_tr_b16 v[178:179], v12 offset:512\n\tv_mfma_f32_16x16x32_bf16 v[88:91], v[192:195], v[164:167], v[88:91]\n\tds_read_b128 v[120:123], v6\n\tv_mfma_f32_16x16x32_bf16 v[100:103], v[192:195], v[168:171], v[100:103]\n\tds_read_b128 v[124:127], v2 offset:2048\n\t"
            "v_mfma_f32_16x16x32_bf16 v[112:115], v[192:195], v[172:175], v[112:115]\n\tds_read_b128 v[128:131], v2 offset:4096\n\tv_mfma_f32_16x16x32_bf16 v[44:47], v[148:151], v[196:199], v[44:47]\n\tds_read_b128 v[132:135], v2 offset:6144\n\tv_mfma_f32_16x16x32_bf16 v[56:59], v[152:155], v[196:199], v[56:59]\n\tds_read_b128 v[136:139], v2 offset:8192\n\t"
            "v_mfma_f32_16x16x32_bf16 v[68:71], v[156:159], v[196:199], v[68:71]\n\tds_read_b128 v[140:143], v2 offset:10240\n\tv_mfma_f32_16x16x32_bf16 v[80:83], v[160:163], v[196:199], v[80:83]\n\tds_read_b128 v[144:147], v2 offset:12288\n\tv_mfma_f32_16x16x32_bf16 v[92:95], v[164:167], v[196:199], v[92:95]\n\tds_read_b64_tr_b16 v[180:181], v12 offset:1024\n\t"
            "ds_read_b64_tr_b16 v[182:183], v12 offset:1536\n\tv_mfma_f32_16x16x32_bf16 v[104:107], v[168:171], v[196:199], v[104:107]\n\tv_mfma_f32_16x16x32_bf16 v[116:119], v[172:175], v[196:199], v[116:119]\n\ts_waitcnt lgkmcnt(8)\n\tv_mfma_f32_16x16x32_bf16 v[36:39], v[176:179], v[120:123], v[36:39]\n\ts_waitcnt lgkmcnt(7)\n\t"
            "v_mfma_f32_16x16x32_bf16 v[48:51], v[176:179], v[124:127], v[48:51]\n\ts_waitcnt lgkmcnt(6)\n\tv_mfma_f32_16x16x32_bf16 v[60:63], v[176:179], v[128:131], v[60:63]\n\tds_read_b64_tr_b16 v[188:189], v12 offset:12288\n\tds_read_b64_tr_b16 v[190:191], v12 offset:12800\n\ts_waitcnt lgkmcnt(7)\n\t"
            "v_mfma_f32_16x16x32_bf16 v[72:75], v[176:179], v[132:135], v[72:75]\n\tds_read_b128 v[148:151], v7\n\ts_waitcnt lgkmcnt(7)\n\tv_mfma_f32_16x16x32_bf16 v[84:87], v[176:179], v[136:139], v[84:87]\n\tds_read_b128 v[152:155], v3 offset:2048\n\ts_waitcnt lgkmcnt(7)\n\t"
            "v_mfma_f32_16x16x32_bf16 v[96:99], v[176:179], v[140:143], v[96:99]\n\tds_read_b128 v[156:159], v3 offset:4096\n\ts_waitcnt lgkmcnt(7)\n\tv_mfma_f32_16x16x32_bf16 v[108:111], v[176:179], v[144:147], v[108:111]\n\tds_read_b128 v[160:163], v3 offset:6144\n\ts_waitcnt lgkmcnt(6)\n\t"
            "v_mfma_f32_16x16x32_bf16 v[40:43], v[180:183], v[120:123], v[40:43]\n\tds_read_b128 v[164:167], v3 offset:8192\n\tv_mfma_f32_16x16x32_bf16 v[52:55], v[180:183], v[124:127], v[52:55]\n\tds_read_b128 v[168:171], v3 offset:10240\n\tv_mfma_f32_16x16x32_bf16 v[64:67], v[180:183], v[128:131], v[64:67]\n\tds_read_b128 v[172:175], v3 offset:12288\n\t"
            "v_mfma_f32_16x16x32_bf16 v[76:79], v[180:183], v[132:135], v[76:79]\n\tds_read_b64_tr_b16 v[192:193], v12 offset:13312\n\tds_read_b64_tr_b16 v[194:195], v12 offset:13824\n\tv_mfma_f32_16x16x32_bf16 v[88:91], v[180:183], v[136:139], v[88:91]\n\tv_mfma_f32_16x16x32_bf16 v[100:103], v[180:183], v[140:143], v[100:103]\n\tv_mfma_f32_16x16x32_bf16 v[112:115], v[180:183], v[144:147], v[112:115]\n\t"
            "s_waitcnt lgkmcnt(8)\n\tv_mfma_f32_16x16x32_bf16 v[36:39], v[188:191], v[148:151], v[36:39]\n\ts_waitcnt lgkmcnt(7)\n\tv_mfma_f32_16x16x32_bf16 v[48:51], v[188:191], v[152:155], v[48:51]\n\ts_waitcnt lgkmcnt(6)\n\tv_mfma_f32_16x16x32_bf16 v[60:63], v[188:191], v[156:159], v[60:63]\n\t"
            "s_waitcnt lgkmcnt(5)\n\tv_mfma_f32_16x16x32_bf16 v[72:75], v[188:191], v[160:163], v[72:75]\n\ts_waitcnt lgkmcnt(0)\n\ts_barrier\n\t"
            "ds_read_b64_tr_b16 v[176:177], v13 offset:0\n\tds_read_b64_tr_b16 v[178:179], v13 offset:512\n\tv_mfma_f32_16x16x32_bf16 v[84:87], v[188:191], v[164:167], v[84:87]\n\tds_read_b128 v[120:123], v8\n\tv_mfma_f32_16x16x32_bf16 v[96:99], v[188:191], v[168:171], v[96:99]\n\tds_read_b128 v[124:127], v4 offset:2048\n\t"
            "v_mfma_f32_16x16x32_bf16 v[108:111], v[188:191], v[172:175], v[108:111]\n\tds_read_b128 v[128:131], v4 offset:4096\n\tv_mfma_f32_16x16x32_bf16 v[40:43], v[192:195], v[148:151], v[40:43]\n\tds_read_b128 v[132:135], v4 offset:6144\n\tv_mfma_f32_16x16x32_bf16 v[52:55], v[192:195], v[152:155], v[52:55]\n\tds_read_b128 v[136:139], v4 offset:8192\n\t"
            "v_mfma_f32_16x16x32_bf16 v[64:67], v[192:195], v[156:159], v[64:67]\n\tds_read_b128 v[140:143], v4 offset:10240\n\tv_mfma_f32_16x16x32_bf16 v[76:79], v[192:195], v[160:163], v[76:79]\n\tds_read_b128 v[144:147], v4 offset:12288\n\tv_mfma_f32_16x16x32_bf16 v[88:91], v[192:195], v[164:167], v[88:91]\n\tds_read_b64_tr_b16 v[180:181], v13 offset:1024\n\t"
            "ds_read_b64_tr_b16 v[182:183], v13 offset:1536\n\tv_mfma_f32_16x16x32_bf16 v[100:103], v[192:195], v[168:171], v[100:103]\n\tds_read_b64_tr_b16 v[184:185], v13 offset:2048\n\tds_read_b64_tr_b16 v[186:187], v13 offset:2560\n\tv_mfma_f32_16x16x32_bf16 v[112:115], v[192:195], v[172:175], v[112:115]\n\tds_read2_b32 v[208:209], v14 offset1:16\n\t"
            "s_setprio 2\n\t"
            "s_mov_b64 vcc, s[18:19]\n\ts_bitcmp1_b32 9, s69\n\tds_read_b64_tr_b16 v[188:189], v13 offset:12288\n\tv_mov_b32 v252, -1\n\tds_read_b64_tr_b16 v[190:191], v13 offset:12800\n\tv_mov_b32 v254, -1\n\t"
            "ds_read_b128 v[148:151], v9\n\ts_waitcnt lgkmcnt(6)\n\tv_mfma_f32_16x16x32_bf16 v[40:43], v[180:183], v[120:123], v[40:43]\n\tds_read_b128 v[152:155], v5 offset:2048\n\tv_mfma_f32_16x16x32_bf16 v[36:39], v[176:179], v[120:123], v[36:39]\n\tds_read_b128 v[156:159], v5 offset:4096\n\t"
            "v_mfma_f32_16x16x32_bf16 v[48:51], v[176:179], v[124:127], v[48:51]\n\tds_read_b128 v[160:163], v5 offset:6144\n\tv_mfma_f32_16x16x32_bf16 v[60:63], v[176:179], v[128:131], v[60:63]\n\tds_read_b128 v[164:167], v5 offset:8192\n\tv_mfma_f32_16x16x32_bf16 v[52:55], v[180:183], v[124:127], v[52:55]\n\tds_read_b128 v[168:171], v5 offset:10240\n\t"
            "v_mfma_f32_16x16x32_bf16 v[64:67], v[180:183], v[128:131], v[64:67]\n\tds_read_b128 v[172:175], v5 offset:12288\n\tv_mfma_f32_16x16x32_bf16 v[76:79], v[180:183], v[132:135], v[76:79]\n\tds_read_b64_tr_b16 v[192:193], v13 offset:13312\n\ts_waitcnt lgkmcnt(7)\n\tv_mfma_f32_16x16x32_bf16 v[36:39], v[188:191], v[148:151], v[36:39]\n\t"
            "ds_read_b64_tr_b16 v[194:195], v13 offset:13824\n\tds_read_b64_tr_b16 v[196:197], v13 offset:14336\n\ts_waitcnt lgkmcnt(8)\n\tv_mfma_f32_16x16x32_bf16 v[48:51], v[188:191], v[152:155], v[48:51]\n\tds_read_b64_tr_b16 v[198:199], v13 offset:14848\n\tds_read2_b32 v[210:211], v14 offset0:32 offset1:48\n\t"
            "v_mfma_f32_16x16x32_bf16 v[72:75], v[176:179], v[132:135], v[72:75]\n\ts_nop 0\n\tv_mov_b32_dpp v200, v36 row_newbcast:0 row_mask:0xf bank_mask:0xf bound_ctrl:1\n\tds_read2_b32 v[212:213], v14 offset0:64 offset1:80\n\ts_waitcnt lgkmcnt(9)\n\tv_mfma_f32_16x16x32_bf16 v[60:63], v[188:191], v[156:159], v[60:63]\n\t"
            "v_mov_b32_dpp v201, v37 row_newbcast:0 row_mask:0xf bank_mask:0xf bound_ctrl:1\n\tds_read_b32 v214, v14 offset:384\n\tv_mfma_f32_16x16x32_bf16 v[72:75], v[188:191], v[160:163], v[72:75]\n\tv_mov_b32_dpp v202, v38 row_newbcast:0 row_mask:0xf bank_mask:0xf bound_ctrl:1\n\tv_mov_b32_dpp v203, v39 row_newbcast:0 row_mask:0xf bank_mask:0xf bound_ctrl:1\n\tv_fmac_f32 v36, v208, v200\n\t"
            "v_fmac_f32 v37, v208, v201\n\tv_fmac_f32 v38, v208, v202\n\tv_fmac_f32 v39, v208, v203\n\tv_cvt_pk_bf16_f32 v250, v36, v37\n\tv_cvt_pk_bf16_f32 v251, v38, v39\n\tv_and_b32 v36, v250, v18\n\t"
            "v_and_b32 v37, v251, v18\n\ts_waitcnt lgkmcnt(2)\n\tv_mfma_f32_16x16x32_bf16 v[40:43], v[192:195], v[148:151], v[40:43]\n\tv_and_b32 v38, v250, v19\n\tv_and_b32 v39, v251, v19\n\tv_mfma_f32_16x16x32_bf16 v[52:55], v[192:195], v[152:155], v[52:55]\n\t"
            "v_and_b32 v222, v250, v20\n\tv_and_b32 v223, v251, v20\n\tv_mfma_f32_16x16x32_bf16 v[64:67], v[192:195], v[156:159], v[64:67]\n\ts_nop 1\n\tv_mov_b32_dpp v204, v40 row_newbcast:0 row_mask:0xf bank_mask:0xf bound_ctrl:1\n\tv_mov_b32_dpp v205, v41 row_newbcast:0 row_mask:0xf bank_mask:0xf bound_ctrl:1\n\t"
            "v_mfma_f32_16x16x32_bf16 v[76:79], v[192:195], v[160:163], v[76:79]\n\tv_mov_b32_dpp v206, v42 row_newbcast:0 row_mask:0xf bank_mask:0xf bound_ctrl:1\n\tv_mov_b32_dpp v207, v43 row_newbcast:0 row_mask:0xf bank_mask:0xf bound_ctrl:1\n\tv_fmac_f32 v40, v208, v204\n\tv_fmac_f32 v41, v208, v205\n\tv_fmac_f32 v42, v208, v206\n\t"
            "v_fmac_f32 v43, v208, v207\n\tv_cvt_pk_bf16_f32 v40, v40, v41\n\tv_cvt_pk_bf16_f32 v41, v42, v43\n\tv_cndmask_b32 v42, v40, v40, vcc\n\tv_cndmask_b32 v43, v41, v41, vcc\n\tv_and_b32 v224, v250, v21\n\t"
            "v_and_b32 v225, v251, v21\n\tv_mfma_f32_16x16x32_bf16 v[218:221], v[40:43], v[36:39], 0\n\tv_fmac_f32 v48, v209, v200\n\tv_fmac_f32 v49, v209, v201\n\tv_mfma_f32_16x16x32_bf16 v[226:229], v[40:43], v[222:225], 0\n\tv_fmac_f32 v50, v209, v202\n\t"
            "v_fmac_f32 v51, v209, v203\n\tv_cvt_pk_bf16_f32 v250, v48, v49\n\ts_nop 1\n\tv_cndmask_b32 v36, v220, v218, s[14:15]\n\tv_cndmask_b32 v37, v221, v219, s[16:17]\n\tv_cvt_pk_bf16_f32 v251, v50, v51\n\t"
            "v_and_b32 v48, v250, v18\n\tv_cndmask_b32 v38, v228, v226, s[14:15]\n\tv_cndmask_b32 v39, v229, v227, s[16:17]\n\tv_and_b32 v49, v251, v18\n\tv_and_b32 v50, v250, v19\n\tv_and_b32 v51, v251, v19\n\t"
            "v_and_b32 v234, v250, v20\n\tv_and_b32 v235, v251, v20\n\tv_and_b32 v236, v250, v21\n\tv_and_b32 v237, v251, v21\n\tv_fmac_f32 v60, v210, v200\n\tv_fmac_f32 v61, v210, v201\n\t"
            "v_fmac_f32 v62, v210, v202\n\tv_fmac_f32 v63, v210, v203\n\tv_fmac_f32 v52, v209, v204\n\tv_fmac_f32 v53, v209, v205\n\tv_fmac_f32 v64, v210, v204\n\tv_fmac_f32 v65, v210, v205\n\t"
            "v_cvt_pk_bf16_f32 v250, v60, v61\n\tv_cvt_pk_bf16_f32 v251, v62, v63\n\tv_fmac_f32 v76, v211, v204\n\tv_fmac_f32 v77, v211, v205\n\tv_fmac_f32 v54, v209, v206\n\tv_fmac_f32 v55, v209, v207\n\t"
            "v_cvt_pk_bf16_f32 v52, v52, v53\n\tv_fmac_f32 v66, v210, v206\n\tv_fmac_f32 v67, v210, v207\n\tv_cvt_pk_bf16_f32 v64, v64, v65\n\tv_and_b32 v60, v250, v18\n\tv_and_b32 v61, v251, v18\n\t"
            "v_and_b32 v62, v250, v19\n\tv_and_b32 v63, v251, v19\n\tv_and_b32 v246, v250, v20\n\tv_and_b32 v247, v251, v20\n\tv_and_b32 v248, v250, v21\n\tv_and_b32 v249, v251, v21\n\t"
            "v_fmac_f32 v78, v211, v206\n\tv_fmac_f32 v79, v211, v207\n\tv_fmac_f32 v72, v211, v200\n\tv_fmac_f32 v73, v211, v201\n\tv_fmac_f32 v74, v211, v202\n\tv_fmac_f32 v75, v211, v203\n\t"
            "v_cvt_pk_bf16_f32 v76, v76, v77\n\tv_cvt_pk_bf16_f32 v53, v54, v55\n\tv_cvt_pk_bf16_f32 v65, v66, v67\n\tv_cvt_pk_bf16_f32 v77, v78, v79\n\tv_cvt_pk_bf16_f32 v250, v72, v73\n\tv_cvt_pk_bf16_f32 v251, v74, v75\n\t"
            "v_cndmask_b32 v54, v40, v52, vcc\n\tv_cndmask_b32 v55, v41, v53, vcc\n\tv_cndmask_b32 v66, v52, v64, vcc\n\tv_cndmask_b32 v67, v53, v65, vcc\n\tv_mfma_f32_16x16x32_bf16 v[230:233], v[52:55], v[48:51], 0\n\tv_cndmask_b32 v78, v64, v76, vcc\n\t"
            "v_cndmask_b32 v79, v65, v77, vcc\n\tv_mfma_f32_16x16x32_bf16 v[242:245], v[64:67], v[60:63], 0\n\tv_and_b32 v72, v250, v18\n\tv_and_b32 v73, v251, v18\n\tv_mfma_f32_16x16x32_bf16 v[238:241], v[52:55], v[234:237], 0\n\tv_and_b32 v74, v250, v19\n\t"
            "v_and_b32 v75, v251, v19\n\tv_mfma_f32_16x16x32_bf16 v[218:221], v[64:67], v[246:249], 0\n\tv_cndmask_b32 v48, v232, v230, s[14:15]\n\tv_cndmask_b32 v49, v233, v231, s[16:17]\n\tv_mfma_f32_16x16x32_bf16 v[222:225], v[76:79], v[72:75], 0\n\tv_cndmask_b32 v60, v244, v242, s[14:15]\n\t"
            "v_cndmask_b32 v61, v245, v243, s[16:17]\n\tv_mfma_f32_16x16x32_bf16 v[84:87], v[176:179], v[136:139], v[84:87]\n\tv_cndmask_b32 v50, v240, v238, s[14:15]\n\tv_cndmask_b32 v51, v241, v239, s[16:17]\n\tv_mfma_f32_16x16x32_bf16 v[96:99], v[176:179], v[140:143], v[96:99]\n\ts_nop 1\n\t"
            "v_cndmask_b32 v72, v224, v222, s[14:15]\n\tv_cndmask_b32 v73, v225, v223, s[16:17]\n\tv_mfma_f32_16x16x32_bf16 v[88:91], v[180:183], v[136:139], v[88:91]\n\ts_cbranch_scc0 1f\n\tv_cvt_pk_bf16_f32 v253, v36, v37\n\tv_min_u32 v253, -2, v253\n\t"
            "v_cvt_pk_bf16_f32 v255, v48, v49\n\tv_min_u32 v255, -2, v255\n\tbuffer_store_dwordx4 v[252:255], v17, s[44:47], s20 offen sc1\n\ts_nop 1\n\tv_cvt_pk_bf16_f32 v253, v60, v61\n\tv_min_u32 v253, -2, v253\n\t"
            "v_cvt_pk_bf16_f32 v255, v72, v73\n\tv_min_u32 v255, -2, v255\n\tbuffer_store_dwordx4 v[252:255], v17, s[44:47], s21 offen sc1\n\t1:\nv_cndmask_b32 v62, v220, v218, s[14:15]\n\tv_mfma_f32_16x16x32_bf16 v[100:103], v[180:183], v[140:143], v[100:103]\n\t"
            "v_cndmask_b32 v63, v221, v219, s[16:17]\n\tv_and_b32 v226, v250, v20\n\tv_mfma_f32_16x16x32_bf16 v[112:115], v[180:183], v[144:147], v[112:115]\n\tv_and_b32 v227, v251, v20\n\tv_and_b32 v228, v250, v21\n\tv_mfma_f32_16x16x32_bf16 v[84:87], v[188:191], v[164:167], v[84:87]\n\t"
            "v_and_b32 v229, v251, v21\n\tv_mov_b32 v180, 0\n\tv_mfma_f32_16x16x32_bf16 v[108:111], v[176:179], v[144:147], v[108:111]\n\tv_mov_b32 v176, 0\n\tds_write_b64 v28, v[36:37] offset:0\n\tv_mfma_f32_16x16x32_bf16 v[230:233], v[76:79], v[226:229], 0\n\t"
            "s_waitcnt lgkmcnt(1)\n\ts_nop 0\n\tv_fmac_f32 v84, v212, v200\n\tv_fmac_f32 v85, v212, v201\n\tv_mfma_f32_16x16x32_bf16 v[96:99], v[188:191], v[168:171], v[96:99]\n\tds_write_b64 v29, v[38:39] offset:0\n\t"
            "v_fmac_f32 v86, v212, v202\n\tv_mfma_f32_16x16x32_bf16 v[88:91], v[192:195], v[164:167], v[88:91]\n\tv_cndmask_b32 v74, v232, v230, s[14:15]\n\tv_cndmask_b32 v75, v233, v231, s[16:17]\n\tds_write_b64 v28, v[48:49] offset:512\n\tv_mfma_f32_16x16x32_bf16 v[100:103], v[192:195], v[168:171], v[100:103]\n\t"
            "v_fmac_f32 v87, v212, v203\n\tv_cvt_pk_bf16_f32 v250, v84, v85\n\tv_mfma_f32_16x16x32_bf16 v[112:115], v[192:195], v[172:175], v[112:115]\n\tds_write_b64 v29, v[50:51] offset:512\n\tv_cvt_pk_bf16_f32 v251, v86, v87\n\tv_mfma_f32_16x16x32_bf16 v[108:111], v[188:191], v[172:175], v[108:111]\n\t"
            "v_and_b32 v84, v250, v18\n\tv_and_b32 v85, v251, v18\n\tds_write_b64 v28, v[60:61] offset:1024\n\tv_and_b32 v86, v250, v19\n\tv_and_b32 v87, v251, v19\n\tv_and_b32 v238, v250, v20\n\t"
            "v_and_b32 v239, v251, v20\n\tv_and_b32 v240, v250, v21\n\tds_write_b64 v29, v[62:63] offset:1024\n\tv_and_b32 v241, v251, v21\n\tv_fmac_f32 v96, v213, v200\n\tv_fmac_f32 v97, v213, v201\n\t"
            "v_fmac_f32 v98, v213, v202\n\tv_fmac_f32 v99, v213, v203\n\tds_write_b64 v28, v[72:73] offset:1536\n\tv_fmac_f32 v88, v212, v204\n\tv_fmac_f32 v89, v212, v205\n\tv_fmac_f32 v100, v213, v204\n\t"
            "v_fmac_f32 v101, v213, v205\n\tv_cvt_pk_bf16_f32 v250, v96, v97\n\tds_write_b64 v29, v[74:75] offset:1536\n\tv_cvt_pk_bf16_f32 v251, v98, v99\n\tv_fmac_f32 v112, v214, v204\n\tv_fmac_f32 v113, v214, v205\n\t"
            "v_fmac_f32 v90, v212, v206\n\tv_fmac_f32 v91, v212, v207\n\tds_read_b128 v[36:39], v15 offset:0\n\tv_cvt_pk_bf16_f32 v88, v88, v89\n\tv_fmac_f32 v102, v213, v206\n\tv_fmac_f32 v103, v213, v207\n\t"
            "ds_read_b128 v[48:51], v15 offset:64\n\tv_cvt_pk_bf16_f32 v100, v100, v101\n\ts_waitcnt lgkmcnt(0)\n\ts_barrier\n\tv_and_b32 v96, v250, v18\n\tds_read_b128 v[60:63], v15 offset:128\n\t"
            "v_and_b32 v97, v251, v18\n\tv_and_b32 v98, v250, v19\n\tv_and_b32 v99, v251, v19\n\tds_read_b128 v[72:75], v15 offset:192\n\tv_and_b32 v218, v250, v20\n\tv_and_b32 v219, v251, v20\n\t"
            "v_and_b32 v220, v250, v21\n\tv_and_b32 v221, v251, v21\n\tv_fmac_f32 v114, v214, v206\n\tv_fmac_f32 v115, v214, v207\n\tv_fmac_f32 v108, v214, v200\n\tv_fmac_f32 v109, v214, v201\n\t"
            "v_fmac_f32 v110, v214, v202\n\tv_fmac_f32 v111, v214, v203\n\tv_cvt_pk_bf16_f32 v112, v112, v113\n\tv_cvt_pk_bf16_f32 v89, v90, v91\n\tv_cvt_pk_bf16_f32 v101, v102, v103\n\tv_cvt_pk_bf16_f32 v113, v114, v115\n\t"
            "v_cvt_pk_bf16_f32 v250, v108, v109\n\tv_cvt_pk_bf16_f32 v251, v110, v111\n\tv_cndmask_b32 v90, v76, v88, vcc\n\tv_cndmask_b32 v91, v77, v89, vcc\n\tv_cndmask_b32 v102, v88, v100, vcc\n\tv_cndmask_b32 v103, v89, v101, vcc\n\t"
            "v_mfma_f32_16x16x32_bf16 v[234:237], v[88:91], v[84:87], 0\n\tv_cndmask_b32 v114, v100, v112, vcc\n\tv_cndmask_b32 v115, v101, v113, vcc\n\tv_mfma_f32_16x16x32_bf16 v[246:249], v[100:103], v[96:99], 0\n\tv_and_b32 v108, v250, v18\n\tv_and_b32 v109, v251, v18\n\t"
            "v_mfma_f32_16x16x32_bf16 v[242:245], v[88:91], v[238:241], 0\n\tv_and_b32 v110, v250, v19\n\tv_and_b32 v111, v251, v19\n\tv_mfma_f32_16x16x32_bf16 v[222:225], v[100:103], v[218:221], 0\n\tv_cndmask_b32 v84, v236, v234, s[14:15]\n\tv_cndmask_b32 v85, v237, v235, s[16:17]\n\t"
            "ds_write_b64 v28, v[84:85] offset:38912\n\tv_mfma_f32_16x16x32_bf16 v[226:229], v[112:115], v[108:111], 0\n\tv_cndmask_b32 v96, v248, v246, s[14:15]\n\tv_cndmask_b32 v97, v249, v247, s[16:17]\n\tv_cndmask_b32 v86, v244, v242, s[14:15]\n\tds_write_b64 v28, v[96:97] offset:39424\n\t"
            "v_cndmask_b32 v87, v245, v243, s[16:17]\n\tv_cndmask_b32 v98, v224, v222, s[14:15]\n\tv_cndmask_b32 v99, v225, v223, s[16:17]\n\ts_nop 0\n\tv_cndmask_b32 v108, v228, v226, s[14:15]\n\tv_cndmask_b32 v109, v229, v227, s[16:17]\n\t"
            "ds_write_b64 v29, v[86:87] offset:38912\n\ts_cbranch_scc0 1f\n\tv_cvt_pk_bf16_f32 v253, v84, v85\n\tv_min_u32 v253, -2, v253\n\tv_cvt_pk_bf16_f32 v255, v96, v97\n\tv_min_u32 v255, -2, v255\n\t"
            "buffer_store_dwordx4 v[252:255], v17, s[44:47], s22 offen sc1\n\ts_nop 1\n\tv_cvt_pk_bf16_f32 v253, v108, v109\n\tv_min_u32 v253, -2, v253\n\tbuffer_store_dwordx4 v[252:255], v17, s[44:47], s23 offen sc1\n\t1:\n"
            "v_and_b32 v230, v250, v20\n\tv_and_b32 v231, v251, v20\n\tv_and_b32 v232, v250, v21\n\tv_and_b32 v233, v251, v21\n\tds_write_b64 v29, v[98:99] offset:39424\n\tv_mfma_f32_16x16x32_bf16 v[44:47], v[120:123], v[184:187], v[44:47]\n\t"
            "v_mov_b32 v218, 0\n\tv_mov_b32 v219, 0\n\tv_mfma_f32_16x16x32_bf16 v[234:237], v[112:115], v[230:233], 0\n\tds_write_b64 v28, v[108:109] offset:39936\n\tv_mov_b32 v188, 0\n\tv_mfma_f32_16x16x32_bf16 v[56:59], v[124:127], v[184:187], v[56:59]\n\t"
            "v_mov_b32 v192, 0\n\tds_read_b128 v[120:123], v6\n\ts_nop 2\n\tv_cndmask_b32 v110, v236, v234, s[14:15]\n\tv_cndmask_b32 v111, v237, v235, s[16:17]\n\tv_mfma_f32_16x16x32_bf16 v[68:71], v[128:131], v[184:187], v[68:71]\n\t"
            "ds_write_b64 v29, v[110:111] offset:39936\n\tv_mov_b32 v234, 0\n\tv_mfma_f32_16x16x32_bf16 v[80:83], v[132:135], v[184:187], v[80:83]\n\tv_mov_b32 v235, 0\n\tds_write_b32 v33, v33\n\t"
            "v_mfma_f32_16x16x32_bf16 v[92:95], v[136:139], v[184:187], v[92:95]\n\tds_read_b128 v[124:127], v2 offset:2048\n\tv_mfma_f32_16x16x32_bf16 v[104:107], v[140:143], v[184:187], v[104:107]\n\tds_read_b128 v[128:131], v2 offset:4096\n\tv_mfma_f32_16x16x32_bf16 v[116:119], v[144:147], v[184:187], v[116:119]\n\tds_read_b64_tr_b16 v[184:185], v12 offset:2048\n\t"
            "v_mfma_f32_16x16x32_bf16 v[44:47], v[148:151], v[196:199], v[44:47]\n\tds_read_b64_tr_b16 v[186:187], v12 offset:2560\n\tds_read_b128 v[148:151], v7\n\tv_mfma_f32_16x16x32_bf16 v[56:59], v[152:155], v[196:199], v[56:59]\n\tds_read_b128 v[152:155], v3 offset:2048\n\tv_mfma_f32_16x16x32_bf16 v[68:71], v[156:159], v[196:199], v[68:71]\n\t"
            "ds_read_b128 v[132:135], v2 offset:6144\n\tv_mfma_f32_16x16x32_bf16 v[80:83], v[160:163], v[196:199], v[80:83]\n\tds_read_b128 v[156:159], v3 offset:4096\n\tv_mfma_f32_16x16x32_bf16 v[92:95], v[164:167], v[196:199], v[92:95]\n\tds_read_b128 v[160:163], v3 offset:6144\n\tv_mfma_f32_16x16x32_bf16 v[104:107], v[168:171], v[196:199], v[104:107]\n\t"
            "ds_read_b128 v[136:139], v2 offset:8192\n\tv_mfma_f32_16x16x32_bf16 v[116:119], v[172:175], v[196:199], v[116:119]\n\tds_read_b64_tr_b16 v[196:197], v12 offset:14336\n\tds_read_b64_tr_b16 v[198:199], v12 offset:14848\n\ts_waitcnt lgkmcnt(8)\n\tv_mfma_f32_16x16x32_bf16 v[44:47], v[120:123], v[184:187], v[44:47]\n\t"
            "ds_read_b128 v[140:143], v2 offset:10240\n\tv_mfma_f32_16x16x32_bf16 v[56:59], v[124:127], v[184:187], v[56:59]\n\tds_read_b128 v[144:147], v2 offset:12288\n\tv_mfma_f32_16x16x32_bf16 v[68:71], v[128:131], v[184:187], v[68:71]\n\tds_read_b128 v[164:167], v3 offset:8192\n\ts_waitcnt lgkmcnt(8)\n\t"
            "v_mfma_f32_16x16x32_bf16 v[80:83], v[132:135], v[184:187], v[80:83]\n\tds_read_b128 v[168:171], v3 offset:10240\n\tds_read_b128 v[172:175], v3 offset:12288\n\ts_waitcnt lgkmcnt(7)\n\tv_mfma_f32_16x16x32_bf16 v[92:95], v[136:139], v[184:187], v[92:95]\n\tds_read_b128 v[84:87], v15 offset:256\n\t"
            "s_waitcnt lgkmcnt(6)\n\tv_mfma_f32_16x16x32_bf16 v[44:47], v[148:151], v[196:199], v[44:47]\n\tds_read_b128 v[96:99], v15 offset:320\n\tv_mfma_f32_16x16x32_bf16 v[56:59], v[152:155], v[196:199], v[56:59]\n\tds_read_b128 v[108:111], v15 offset:384\n\tv_mfma_f32_16x16x32_bf16 v[68:71], v[156:159], v[196:199], v[68:71]\n\t"
            "s_nop 3\n\tv_mov_b32 v216, v44\n\tv_mov_b32 v217, v44\n\tv_mfma_f32_16x16x32_bf16 v[80:83], v[160:163], v[196:199], v[80:83]\n\ts_nop 0\n\tv_permlane32_swap_b32 v216, v217\n\t"
            "v_mov_b32 v217, v216\n\ts_waitcnt lgkmcnt(5)\n\tv_mfma_f32_16x16x32_bf16 v[104:107], v[140:143], v[184:187], v[104:107]\n\tv_permlane16_swap_b32 v216, v217\n\tv_fmac_f32 v44, v36, v216\n\tv_mfma_f32_16x16x32_bf16 v[116:119], v[144:147], v[184:187], v[116:119]\n\t"
            "v_fmac_f32 v45, v37, v216\n\tv_fmac_f32 v46, v38, v216\n\tv_fmac_f32 v47, v39, v216\n\tv_cvt_pk_bf16_f32 v44, v44, v45\n\tv_cvt_pk_bf16_f32 v45, v46, v47\n\tv_mfma_f32_16x16x32_bf16 v[92:95], v[164:167], v[196:199], v[92:95]\n\t"
            "v_and_b32 v220, v44, v22\n\tv_and_b32 v221, v45, v22\n\ts_waitcnt lgkmcnt(0)\n\tv_mfma_f32_16x16x32_bf16 v[104:107], v[168:171], v[196:199], v[104:107]\n\tv_and_b32 v236, v44, v23\n\tv_and_b32 v237, v45, v23\n\t"
            "v_mfma_f32_16x16x32_bf16 v[116:119], v[172:175], v[196:199], v[116:119]\n\tv_fmac_f32 v56, v48, v216\n\tv_fmac_f32 v57, v49, v216\n\tv_fmac_f32 v58, v50, v216\n\tv_fmac_f32 v59, v51, v216\n\tv_cvt_pk_bf16_f32 v56, v56, v57\n\t"
            "v_cvt_pk_bf16_f32 v57, v58, v59\n\tv_and_b32 v222, v56, v22\n\tv_and_b32 v223, v57, v22\n\tv_and_b32 v238, v56, v23\n\tv_and_b32 v239, v57, v23\n\tv_fmac_f32 v68, v60, v216\n\t"
            "v_fmac_f32 v69, v61, v216\n\tv_fmac_f32 v70, v62, v216\n\tv_fmac_f32 v71, v63, v216\n\tds_read_b32 v27, v32\n\tds_read2st64_b32 v[42:43], v31 offset0:0 offset1:1\n\tds_read2st64_b32 v[40:41], v30 offset0:0 offset1:1\n\t"
            "ds_read2st64_b32 v[66:67], v31 offset0:2 offset1:3\n\tds_read2st64_b32 v[64:65], v30 offset0:2 offset1:3\n\tv_cvt_pk_bf16_f32 v68, v68, v69\n\tv_cvt_pk_bf16_f32 v69, v70, v71\n\tv_and_b32 v224, v68, v22\n\tv_and_b32 v225, v69, v22\n\t"
            "v_and_b32 v240, v68, v23\n\tv_and_b32 v241, v69, v23\n\tv_fmac_f32 v80, v72, v216\n\tv_fmac_f32 v81, v73, v216\n\tv_fmac_f32 v82, v74, v216\n\tv_fmac_f32 v83, v75, v216\n\t"
            "v_cvt_pk_bf16_f32 v80, v80, v81\n\tv_cvt_pk_bf16_f32 v81, v82, v83\n\tv_and_b32 v226, v80, v22\n\tv_and_b32 v227, v81, v22\n\tv_and_b32 v242, v80, v23\n\tv_and_b32 v243, v81, v23\n\t"
            "s_waitcnt lgkmcnt(1)\n\tv_cmp_ge_u32 vcc, 0, v27\n\ts_cbranch_vccnz 10f\n\t20:\nv_and_b32 v177, v42, v24\n\tv_and_b32 v178, v42, v25\n\t"
            "v_and_b32 v179, v42, v26\n\tv_and_b32 v189, v43, v24\n\tv_and_b32 v190, v43, v25\n\tv_and_b32 v191, v43, v26\n\tv_mfma_f32_16x16x32_bf16 v[44:47], v[234:237], v[176:179], 0\n\tv_and_b32 v181, v40, v24\n\t"
            "v_and_b32 v182, v40, v25\n\tv_mfma_f32_16x16x32_bf16 v[56:59], v[236:239], v[188:191], 0\n\tv_and_b32 v183, v40, v26\n\tv_and_b32 v193, v41, v24\n\tv_and_b32 v194, v41, v25\n\tv_and_b32 v195, v41, v26\n\t"
            "v_mfma_f32_16x16x32_bf16 v[44:47], v[218:221], v[180:183], v[44:47]\n\tv_and_b32 v177, v66, v24\n\tv_and_b32 v178, v66, v25\n\tv_mfma_f32_16x16x32_bf16 v[56:59], v[220:223], v[192:195], v[56:59]\n\tv_and_b32 v179, v66, v26\n\tv_and_b32 v189, v67, v24\n\t"
            "v_and_b32 v190, v67, v25\n\ts_nop 1\n\tv_cvt_pk_bf16_f32 v36, v44, v45\n\tv_cvt_pk_bf16_f32 v37, v46, v47\n\tv_min_u32 v36, -2, v36\n\tv_and_b32 v191, v67, v26\n\t"
            "v_cvt_pk_bf16_f32 v38, v56, v57\n\tv_cvt_pk_bf16_f32 v39, v58, v59\n\tv_min_u32 v38, -2, v38\n\tv_mfma_f32_16x16x32_bf16 v[68:71], v[238:241], v[176:179], 0\n\ts_nop 0\n\tv_permlane16_swap_b32 v36, v38\n\t"
            "v_permlane16_swap_b32 v37, v39\n\tbuffer_store_dwordx4 v[36:39], v16, s[44:47], s20 offen sc1\n\tv_mfma_f32_16x16x32_bf16 v[80:83], v[240:243], v[188:191], 0\n\ts_waitcnt lgkmcnt(0)\n\tv_and_b32 v181, v64, v24\n\tv_and_b32 v182, v64, v25\n\t"
            "v_and_b32 v183, v64, v26\n\tv_and_b32 v193, v65, v24\n\tv_and_b32 v194, v65, v25\n\tv_and_b32 v195, v65, v26\n\tv_mfma_f32_16x16x32_bf16 v[68:71], v[222:225], v[180:183], v[68:71]\n\tv_fmac_f32 v92, v84, v216\n\t"
            "v_fmac_f32 v93, v85, v216\n\tv_mfma_f32_16x16x32_bf16 v[80:83], v[224:227], v[192:195], v[80:83]\n\tv_fmac_f32 v94, v86, v216\n\tv_fmac_f32 v95, v87, v216\n\tv_cvt_pk_bf16_f32 v92, v92, v93\n\ts_nop 1\n\t"
            "v_cvt_pk_bf16_f32 v60, v68, v69\n\tv_cvt_pk_bf16_f32 v61, v70, v71\n\tv_min_u32 v60, -2, v60\n\tv_fmac_f32 v104, v96, v216\n\tv_cvt_pk_bf16_f32 v62, v80, v81\n\tv_cvt_pk_bf16_f32 v63, v82, v83\n\t"
            "v_min_u32 v62, -2, v62\n\tv_fmac_f32 v105, v97, v216\n\tv_permlane16_swap_b32 v61, v63\n\tv_permlane16_swap_b32 v60, v62\n\tbuffer_store_dwordx4 v[60:63], v16, s[44:47], s21 offen sc1\n\tds_write_b32 v34, v35\n\t"
            "v_cvt_pk_bf16_f32 v93, v94, v95\n\tv_fmac_f32 v106, v98, v216\n\tv_fmac_f32 v107, v99, v216\n\tv_cvt_pk_bf16_f32 v104, v104, v105\n\tv_fmac_f32 v116, v108, v216\n\tv_fmac_f32 v117, v109, v216\n\t"
            "v_and_b32 v228, v92, v22\n\tds_read_b32 v27, v32\n\tds_read2st64_b32 v[102:103], v31 offset0:152 offset1:153\n\tds_read2st64_b32 v[100:101], v30 offset0:152 offset1:153\n\tds_read2st64_b32 v[78:79], v31 offset0:154 offset1:155\n\tds_read2st64_b32 v[76:77], v30 offset0:154 offset1:155\n\t"
            "v_and_b32 v229, v93, v22\n\tv_and_b32 v244, v92, v23\n\tv_and_b32 v245, v93, v23\n\tv_cvt_pk_bf16_f32 v105, v106, v107\n\tv_fmac_f32 v118, v110, v216\n\tv_fmac_f32 v119, v111, v216\n\t"
            "v_cvt_pk_bf16_f32 v116, v116, v117\n\tv_and_b32 v230, v104, v22\n\tv_and_b32 v231, v105, v22\n\tv_and_b32 v246, v104, v23\n\tv_and_b32 v247, v105, v23\n\tv_cvt_pk_bf16_f32 v117, v118, v119\n\t"
            "v_and_b32 v232, v116, v22\n\tv_and_b32 v233, v117, v22\n\tv_and_b32 v248, v116, v23\n\tv_and_b32 v249, v117, v23\n\ts_waitcnt lgkmcnt(3)\n\tv_cmp_ge_u32 vcc, 1, v27\n\t"
            "s_cbranch_vccnz 11f\n\t21:\nv_and_b32 v177, v102, v24\n\tv_and_b32 v178, v102, v25\n\tv_and_b32 v179, v102, v26\n\tv_and_b32 v189, v103, v24\n\t"
            "v_and_b32 v190, v103, v25\n\tv_and_b32 v191, v103, v26\n\tv_mfma_f32_16x16x32_bf16 v[92:95], v[242:245], v[176:179], 0\n\ts_waitcnt lgkmcnt(0)\n\tv_and_b32 v181, v100, v24\n\tv_and_b32 v182, v100, v25\n\t"
            "v_mfma_f32_16x16x32_bf16 v[104:107], v[244:247], v[188:191], 0\n\tv_and_b32 v183, v100, v26\n\tv_and_b32 v193, v101, v24\n\tv_and_b32 v194, v101, v25\n\tv_and_b32 v195, v101, v26\n\tv_mfma_f32_16x16x32_bf16 v[92:95], v[226:229], v[180:183], v[92:95]\n\t"
            "v_and_b32 v177, v78, v24\n\tv_and_b32 v178, v78, v25\n\tv_mfma_f32_16x16x32_bf16 v[104:107], v[228:231], v[192:195], v[104:107]\n\tv_and_b32 v179, v78, v26\n\tv_and_b32 v181, v76, v24\n\tv_and_b32 v182, v76, v25\n\t"
            "v_mfma_f32_16x16x32_bf16 v[116:119], v[246:249], v[176:179], 0\n\tv_and_b32 v183, v76, v26\n\tv_cvt_pk_bf16_f32 v84, v92, v93\n\ts_nop 1\n\tv_cvt_pk_bf16_f32 v86, v104, v105\n\tv_mfma_f32_16x16x32_bf16 v[116:119], v[230:233], v[180:183], v[116:119]\n\t"
            "v_cvt_pk_bf16_f32 v85, v94, v95\n\tv_min_u32 v84, -2, v84\n\tv_cvt_pk_bf16_f32 v87, v106, v107\n\tv_min_u32 v86, -2, v86\n\ts_nop 0\n\tv_permlane16_swap_b32 v85, v87\n\t"
            "v_permlane16_swap_b32 v84, v86\n\tbuffer_store_dwordx4 v[84:87], v16, s[44:47], s22 offen sc1\n\tv_cvt_pk_bf16_f32 v108, v116, v117\n\tv_cvt_pk_bf16_f32 v109, v118, v119\n\tv_min_u32 v108, -2, v108\n\ts_nop 0\n\t"
            "v_permlane16_swap_b32 v109, v111\n\tv_permlane16_swap_b32 v108, v110\n\tbuffer_store_dwordx4 v[108:111], v215, s[44:47], s23 offen sc1\n\tds_write_b32 v34, v34\n\t"
            "s_setprio 0\n\t"
            "s_cmp_eq_u32 s69, 1\n\ts_cbranch_scc1 .Lout\n\ts_endpgm\n\t10:\nds_read_b32 v27, v32\n\tds_read2st64_b32 v[42:43], v31 offset0:0 offset1:1\n\t"
            "ds_read2st64_b32 v[40:41], v30 offset0:0 offset1:1\n\tds_read2st64_b32 v[66:67], v31 offset0:2 offset1:3\n\tds_read2st64_b32 v[64:65], v30 offset0:2 offset1:3\n\ts_waitcnt lgkmcnt(0)\n\tv_cmp_ge_u32 vcc, 0, v27\n\ts_cbranch_vccz 20b\n\t"
            "s_sleep 1\n\t"
            "s_sub_u32 s68, s68, 1\n\ts_cbranch_scc0 10b\n\ts_branch 20b\n\t11:\nds_read_b32 v27, v32\n\tds_read2st64_b32 v[102:103], v31 offset0:152 offset1:153\n\t"
            "ds_read2st64_b32 v[100:101], v30 offset0:152 offset1:153\n\tds_read2st64_b32 v[78:79], v31 offset0:154 offset1:155\n\tds_read2st64_b32 v[76:77], v30 offset0:154 offset1:155\n\ts_waitcnt lgkmcnt(0)\n\tv_cmp_ge_u32 vcc, 1, v27\n\ts_cbranch_vccz 21b\n\t"
            "s_sleep 1\n\t"
            "s_sub_u32 s68, s68, 1\n\ts_cbranch_scc0 11b\n\ts_branch 21b\n\t"
            ".LB:\ns_and_b32 s72, s56, 1\n\ts_lshr_b32 s73, s56, 1\n\tv_and_b32 v156, 2, v51\n\tv_lshlrev_b32 v156, 1, v156\n\tv_and_b32 v157, 7, v52\n\t"
            "v_xor_b32 v157, v156, v157\n\tv_mul_u32_u24 v156, 0x500, v51\n\tv_lshl_add_u32 v156, v157, 4, v156\n\ts_mul_i32 s74, s72, 0x2800\n\ts_mul_i32 s75, s73, 0xf000\n\ts_add_i32 s75, s75, s74\n\t"
            "v_add_u32 v154, s75, v156\n\ts_mul_i32 s75, s73, 0x5000\n\ts_add_i32 s75, s75, s74\n\ts_addk_i32 s75, 0x5000\n\tv_add_u32 v156, s75, v156\n\ts_cmp_lg_u32 s63, 0\n\t"
            "s_cselect_b32 s74, 81920, 0\n\ts_max_u32 s74, s74, 65536\n\tv_add_u32 v158, s62, v52\n\tv_min_u32 v159, 47, v52\n\ts_add_i32 s75, s62, 64\n\tv_add_u32 v159, s75, v159\n\t"
            "v_max_i32 v158, 0, v158\n\tv_min_i32 v159, 0x3ff, v159\n\tv_lshl_add_u32 v158, v158, 4, s74\n\tv_lshl_add_u32 v159, v159, 4, s74\n\tv_lshl_add_u32 v59, v53, 4, v57\n\ts_cmp_lg_u32 s73, 0\n\t"
            "s_cselect_b64 vcc, -1, 0\n\tv_cndmask_b32 v97, v59, v52, vcc\n\ts_lshl_b32 s75, s72, 6\n\ts_add_i32 s75, s75, s62\n\tv_add_u32 v97, s75, v97\n\tv_mov_b32 v60, s62\n\t"
            "v_add_u32 v60, 0x6f, v60\n\tv_min_i32 v60, 0x3ff, v60\n\tv_med3_i32 v97, v97, 0, v60\n\ts_lshl3_add_u32 s74, s73, s74\n\tv_lshl_add_u32 v97, v97, 4, s74\n\ts_mul_i32 s30, s73, 576\n\t"
            "s_lshl_b32 s75, s72, 8\n\ts_add_i32 s30, s30, s75\n\ts_add_i32 s30, s30, 160000\n\ts_lshl_b32 s74, s59, 7\n\ts_add_i32 s74, s74, 8192\n\tv_min_u32 v96, 9, v52\n\t"
            "v_lshl_add_u32 v96, v96, 2, s74\n\ts_lshr_b32 s75, s58, 5\n\ts_add_i32 s74, s74, s75\n\ts_cmp_eq_u32 s56, 3\n\ts_cselect_b32 s74, s74, 0x40000000\n\tv_lshl_add_u32 v98, v52, 24, s74\n\t"
            "s_cselect_b32 s75, 0, 0x40000000\n\tv_add_u32 v96, s75, v96\n\ts_cselect_b32 s75, 1, 0\n\ts_lshl2_add_u32 s74, s59, s56\n\ts_mul_i32 s74, s74, 0xa000\n\ts_add_i32 s74, s74, 155648\n\t"
            "v_lshl_add_u32 v101, v52, 4, s74\n\tv_and_b32 v60, s75, v53\n\tv_lshl_add_u32 v2, v60, 30, v101\n\ts_xor_b32 s82, s56, 0\n\ts_cmp_ge_u32 s82, s57\n\ts_addc_u32 s76, s82, 0\n\t"
            "s_and_b32 s16, s76, 3\n\ts_lshl_b32 s77, s76, 1\n\ts_max_u32 s78, s77, 1\n\ts_sub_u32 s78, s78, 1\n\ts_min_u32 s79, s77, 7\n\tv_lshlrev_b32 v60, 9, v53\n\t"
            "v_lshl_add_u32 v60, v54, 3, v60\n\ts_mul_i32 s80, s78, 0xc00\n\ts_mul_i32 s81, s79, 0xc00\n\ts_addk_i32 s80, 14336\n\ts_addk_i32 s81, 14336\n\tv_add_u32 v24, s80, v60\n\t"
            "v_add_u32 v26, s81, v60\n\tv_add_u32 v25, 38912, v24\n\tv_add_u32 v27, 38912, v26\n\tv_lshl_add_u32 v29, v52, 4, s80\n\ts_lshr_b32 s78, s78, 1\n\ts_lshr_b32 s79, s79, 1\n\t"
            "s_sub_u32 s79, s79, s78\n\ts_lshl2_add_u32 s78, s78, 159744\n\tv_and_b32 v28, s79, v52\n\tv_lshl_add_u32 v28, v28, 2, s78\n\ts_lshl2_add_u32 s78, s82, 159744\n\tv_mov_b32 v30, s78\n\t"
            "s_mov_b32 s78, 160576\n\tv_lshl_add_u32 v153, v59, 2, s78\n\ts_mul_i32 s79, s59, 163840\n\ts_lshl_b32 s80, s58, 5\n\ts_add_i32 s79, s79, s80\n\ts_cmp_eq_u32 s76, 0\n\t"
            "s_cselect_b32 s80, 0, 0xc00\n\ts_add_i32 s79, s79, s80\n\ts_add_i32 s79, s79, 155652\n\tv_lshl_add_u32 v31, v54, 4, s79\n\tv_lshrrev_b32 v60, 1, v53\n\tv_mul_u32_u24 v60, 0xa000, v60\n\t"
            "v_add_u32 v31, v60, v31\n\tv_and_b32 v60, 1, v53\n\tv_lshl_add_u32 v31, v60, 3, v31\n\ts_mov_b32 s31, 0x14000\n\ts_cmp_lt_i32 s62, 0\n\ts_cselect_b32 s79, 8, -16\n\t"
            "v_mov_b32 v61, 0xff800000\n\tv_mov_b32 v142, 1\n\tv_add_u32 v60, 0, v59\n\tv_and_b32 v60, -8, v60\n\tv_add_u32 v60, 0, v60\n\tv_cmp_gt_i32 vcc, s79, v60\n\t"
            "v_lshl_add_u32 v160, v60, 2, s78\n\ts_nop 0\n\tv_cndmask_b32 v128, 0, v61, vcc\n\tv_add_u32 v60, 4, v59\n\tv_and_b32 v60, -8, v60\n\tv_add_u32 v60, -4, v60\n\t"
            "v_cmp_gt_i32 vcc, s79, v60\n\tv_lshl_add_u32 v161, v60, 2, s78\n\ts_nop 0\n\tv_cndmask_b32 v129, 0, v61, vcc\n\tv_add_u32 v60, -1, v59\n\tv_and_b32 v60, -8, v60\n\t"
            "v_add_u32 v60, 1, v60\n\tv_cmp_gt_i32 vcc, s79, v60\n\tv_lshl_add_u32 v162, v60, 2, s78\n\ts_nop 0\n\tv_cndmask_b32 v130, 0, v61, vcc\n\tv_add_u32 v60, 3, v59\n\t"
            "v_and_b32 v60, -8, v60\n\tv_add_u32 v60, -3, v60\n\tv_cmp_gt_i32 vcc, s79, v60\n\tv_lshl_add_u32 v163, v60, 2, s78\n\ts_nop 0\n\tv_cndmask_b32 v131, 0, v61, vcc\n\t"
            "v_add_u32 v60, -2, v59\n\tv_and_b32 v60, -8, v60\n\tv_add_u32 v60, 2, v60\n\tv_cmp_gt_i32 vcc, s79, v60\n\tv_lshl_add_u32 v164, v60, 2, s78\n\ts_nop 0\n\t"
            "v_cndmask_b32 v132, 0, v61, vcc\n\tv_add_u32 v60, 2, v59\n\tv_and_b32 v60, -8, v60\n\tv_add_u32 v60, -2, v60\n\tv_cmp_gt_i32 vcc, s79, v60\n\tv_lshl_add_u32 v165, v60, 2, s78\n\t"
            "s_nop 0\n\tv_cndmask_b32 v133, 0, v61, vcc\n\tv_add_u32 v60, -3, v59\n\tv_and_b32 v60, -8, v60\n\tv_add_u32 v60, 3, v60\n\tv_cmp_gt_i32 vcc, s79, v60\n\t"
            "v_lshl_add_u32 v166, v60, 2, s78\n\ts_nop 0\n\tv_cndmask_b32 v134, 0, v61, vcc\n\tv_add_u32 v60, 1, v59\n\tv_and_b32 v60, -8, v60\n\tv_add_u32 v60, -1, v60\n\t"
            "v_cmp_gt_i32 vcc, s79, v60\n\tv_lshl_add_u32 v167, v60, 2, s78\n\ts_nop 0\n\tv_cndmask_b32 v135, 0, v61, vcc\n\ts_lshl2_add_u32 s80, s71, s56\n\ts_lshl_b32 s80, s80, 5\n\t"
            "v_add_u32 v100, s80, v52\n\tv_mul_u32_u24 v100, 0x500, v100\n\ts_add_i32 s80, s58, 3072\n\tv_add_u32 v100, s80, v100\n\tv_min_u32 v104, 51, v52\n\tv_lshlrev_b32 v104, 4, v104\n\t"
            "v_add_u32 v104, 0x1000, v104\n\ts_lshl_b32 s4, s64, 12\n\ts_add_u32 s5, s4, 0x1000\n\ts_cmpk_eq_u32 s5, 0x5000\n\ts_cselect_b32 s5, 0, s5\n\ts_add_u32 s6, s5, 0x1000\n\t"
            "s_cmpk_eq_u32 s6, 0x5000\n\ts_cselect_b32 s6, 0, s6\n\ts_add_u32 s7, s6, 0x1000\n\ts_cmpk_eq_u32 s7, 0x5000\n\ts_cselect_b32 s7, 0, s7\n\ts_add_u32 s8, s7, 0x1000\n\t"
            "s_cmpk_eq_u32 s8, 0x5000\n\ts_cselect_b32 s8, 0, s8\n\ts_cmpk_lt_u32 s58, 0x280\n\ts_cselect_b32 s80, 0, 0x5000\n\ts_xor_b32 s81, s80, 0x5000\n\ts_add_u32 s9, s4, s81\n\t"
            "s_add_u32 s4, s4, s80\n\ts_add_u32 s10, s5, s81\n\ts_add_u32 s5, s5, s80\n\ts_add_u32 s11, s6, s81\n\ts_add_u32 s6, s6, s80\n\ts_add_u32 s12, s7, s81\n\t"
            "s_add_u32 s7, s7, s80\n\ts_add_u32 s13, s8, s81\n\ts_add_u32 s8, s8, s80\n\ts_mul_i32 s20, s4, 20\n\ts_add_i32 s20, s20, s58\n\ts_addk_i32 s20, 3072\n\t"
            "s_mul_i32 s21, s5, 20\n\ts_add_i32 s21, s21, s58\n\ts_addk_i32 s21, 2048\n\ts_mul_i32 s22, s6, 20\n\ts_add_i32 s22, s22, s58\n\ts_addk_i32 s22, 1024\n\t"
            "s_mul_i32 s23, s7, 20\n\ts_add_i32 s23, s23, s58\n\ts_mul_i32 s24, s8, 20\n\ts_add_i32 s24, s24, s58\n\ts_addk_i32 s24, 3072\n\ts_mul_i32 s25, s9, 20\n\t"
            "s_add_i32 s25, s25, s58\n\ts_addk_i32 s25, 2048\n\ts_mul_i32 s26, s10, 20\n\ts_add_i32 s26, s26, s58\n\ts_addk_i32 s26, 1024\n\ts_mul_i32 s27, s11, 20\n\t"
            "s_add_i32 s27, s27, s58\n\ts_mul_i32 s28, s12, 20\n\ts_add_i32 s28, s28, s58\n\ts_addk_i32 s28, 3072\n\ts_mul_i32 s29, s13, 20\n\ts_add_i32 s29, s29, s58\n\t"
            "s_addk_i32 s29, 2048\n\ts_mul_i32 s72, s60, 1920\n\ts_sub_u32 s14, s61, s72\n\ts_mul_i32 s15, s56, 0x2800\n\ts_add_u32 s15, s15, 77824\n\tv_lshl_add_u32 v152, v52, 4, s15\n\t"
            "s_lshl_b32 s18, s58, 5\n\ts_mov_b32 s50, 822272\n\ts_mov_b32 s51, 0x20000\n\ts_waitcnt lgkmcnt(0)\n\ts_load_dword s67, s[44:45], 0x0\n\ts_sub_u32 s48, s48, 3072\n\t"
            "s_subb_u32 s49, s49, 0\n\ts_lshl_b32 s78, s56, 5\n\tv_add_u32 v12, s78, v57\n\tv_add_u32 v3, s62, v12\n\tv_and_b32 v13, 1, v53\n\tv_lshrrev_b32 v14, 1, v53\n\t"
            "v_lshl_add_u32 v15, v13, 1, v14\n\ts_add_u32 s79, s63, s58\n\tv_lshl_add_u32 v15, v15, 4, s79\n\ts_movk_i32 s79, 0xa00\n\tv_mad_i32_i24 v3, v3, s79, v15\n\tv_cmp_gt_u32 vcc, 8, v12\n\t"
            "s_cmp_eq_u32 s56, 3\n\ts_cselect_b32 s79, 2.0, 0xa000\n\tv_add_u32 v11, s79, v3\n\tv_cndmask_b32 v3, v3, 2.0, vcc\n\tv_mov_b32 v8, 159744\n\tv_and_b32 v9, 31, v52\n\t"
            "v_add_u32 v9, s78, v9\n\tv_lshl_add_u32 v9, v9, 2, v8\n\tv_lshl_add_u32 v10, v13, 4, v12\n\tv_lshl_add_u32 v10, v10, 2, v8\n\tv_mul_u32_u24 v4, 0x2800, v14\n\tv_lshl_add_u32 v4, v56, 7, v4\n\t"
            "v_lshl_add_u32 v4, v55, 3, v4\n\tv_lshl_add_u32 v4, v13, 5, v4\n\tv_bfe_u32 v5, v52, 3, 1\n\tv_lshl_add_u32 v4, v5, 6, v4\n\tv_add_u32 v4, 77824, v4\n\tv_xor_b32 v5, 64, v4\n\t"
            "v_add_u32 v6, 0x5000, v4\n\tv_add_u32 v7, 0x5000, v5\n\ts_add_u32 s78, s63, 1280\n\ts_mov_b32 s32, s60\n\ts_mov_b32 s83, 0\n\ts_add_u32 s33, s32, 256\n\t"
            "s_cmp_eq_u32 s33, s78\n\ts_cselect_b32 s33, s63, s33\n\ts_add_u32 s34, s33, 256\n\ts_cmp_eq_u32 s34, s78\n\ts_cselect_b32 s34, s63, s34\n\ts_add_u32 s35, s34, 256\n\t"
            "s_cmp_eq_u32 s35, s78\n\ts_cselect_b32 s35, s63, s35\n\ts_add_u32 s17, s35, 256\n\ts_cmp_eq_u32 s17, s78\n\ts_cselect_b32 s17, s63, s17\n\ts_add_u32 s80, s32, 128\n\t"
            "s_bitcmp1_b32 31, s64\n\ts_cselect_b32 s82, s34, s33\n\ts_bitcmp1_b32 0, s64\n\ts_cselect_b32 s82, s80, s82\n\ts_bitcmp1_b32 30, s64\n\ts_cselect_b32 s81, s35, s34\n\t"
            "s_bitcmp1_b32 1, s64\n\ts_cselect_b32 s81, s80, s81\n\ts_bitcmp1_b32 28, s64\n\ts_cselect_b32 s79, s17, s35\n\ts_bitcmp1_b32 2, s64\n\ts_cselect_b32 s79, s80, s79\n\t"
            "s_bitcmp1_b32 28, s64\n\ts_cselect_b32 s78, s80, s17\n\ts_waitcnt vmcnt(0) lgkmcnt(0)\n\ts_barrier\n\t"
            "v_mov_b32 v136, -1\n\tv_mov_b32 v137, -1\n\tv_mov_b32 v138, -1\n\tv_mov_b32 v139, -1\n\ts_cmp_ge_u32 s56, 2\n\ts_cselect_b64 vcc, -1, 0\n\t"
            "s_mov_b32 s60, s82\n\ts_mul_i32 s61, s60, 1920\n\ts_add_u32 m0, s66, 0\n\ts_add_u32 s61, s61, s14\n\tbuffer_load_dwordx4 v40, s[40:43], s61 offen lds\n\tbuffer_load_dwordx4 v41, s[40:43], s61 offen offset:1024 lds\n\t"
            "buffer_load_dwordx4 v42, s[40:43], s61 offen offset:2048 lds\n\tbuffer_load_dwordx4 v43, s[40:43], s61 offen offset:3072 lds\n\ts_addk_i32 m0, 0x1000\n\ts_nop 0\n\tbuffer_load_dwordx4 v44, s[40:43], s61 offen lds\n\tbuffer_load_dwordx4 v45, s[40:43], s61 offen offset:1024 lds\n\t"
            "s_add_u32 m0, s65, 0\n\ts_cbranch_vccnz 1f\n\tbuffer_load_dwordx4 v39, s[36:39], s60 offen offset:3072 lds\n\t1:\nbuffer_load_dwordx4 v36, s[36:39], s60 offen lds\n\tbuffer_load_dwordx4 v37, s[36:39], s60 offen offset:1024 lds\n\t"
            "buffer_load_dwordx4 v38, s[36:39], s60 offen offset:2048 lds\n\ts_mul_i32 s67, s67, 0x19660d\n\ts_add_u32 s67, s67, 0x3c6ef35f\n\ts_barrier\n\ts_mov_b32 s60, s81\n\ts_mul_i32 s61, s60, 1920\n\t"
            "s_add_u32 m0, s66, 38912\n\ts_add_u32 s61, s61, s14\n\tbuffer_load_dwordx4 v40, s[40:43], s61 offen lds\n\tbuffer_load_dwordx4 v41, s[40:43], s61 offen offset:1024 lds\n\tbuffer_load_dwordx4 v42, s[40:43], s61 offen offset:2048 lds\n\tbuffer_load_dwordx4 v43, s[40:43], s61 offen offset:3072 lds\n\t"
            "s_addk_i32 m0, 0x1000\n\ts_nop 0\n\tbuffer_load_dwordx4 v44, s[40:43], s61 offen lds\n\tbuffer_load_dwordx4 v45, s[40:43], s61 offen offset:1024 lds\n\ts_add_u32 m0, s65, 38912\n\ts_cbranch_vccnz 1f\n\t"
            "buffer_load_dwordx4 v39, s[36:39], s60 offen offset:3072 lds\n\t1:\nbuffer_load_dwordx4 v36, s[36:39], s60 offen lds\n\tbuffer_load_dwordx4 v37, s[36:39], s60 offen offset:1024 lds\n\tbuffer_load_dwordx4 v38, s[36:39], s60 offen offset:2048 lds\n\ts_bitcmp1_b32 13, s64\n\t"
            "s_cbranch_scc0 1f\n\tbuffer_store_dwordx4 v[136:139], v101, s[44:47], s18 offen offset:0 sc1\n\tbuffer_store_dwordx4 v[136:139], v2, s[44:47], s18 offen offset:1024 sc1\n\tbuffer_store_dwordx4 v[136:139], v2, s[44:47], s18 offen offset:2048 sc1\n\tbuffer_store_dwordx4 v[136:139], v101, s[44:47], s18 offen offset:3072 sc1\n\t1:\n"
            "s_bitcmp1_b32 13, s64\n\ts_cbranch_scc1 2f\n\ts_cbranch_vccz 1f\n\ts_waitcnt vmcnt(9)\n\t1:\ns_waitcnt vmcnt(10)\n\t"
            "2:\ns_cbranch_vccz 1f\n\ts_waitcnt vmcnt(13)\n\t1:\ns_waitcnt vmcnt(14)\n\ts_barrier\n\t"
            "s_mov_b32 s60, s79\n\ts_mul_i32 s61, s60, 1920\n\ts_add_u32 m0, s66, 77824\n\ts_add_u32 s61, s61, s14\n\tbuffer_load_dwordx4 v40, s[40:43], s61 offen lds\n\tbuffer_load_dwordx4 v41, s[40:43], s61 offen offset:1024 lds\n\t"
            "buffer_load_dwordx4 v42, s[40:43], s61 offen offset:2048 lds\n\tbuffer_load_dwordx4 v43, s[40:43], s61 offen offset:3072 lds\n\ts_addk_i32 m0, 0x1000\n\ts_nop 0\n\tbuffer_load_dwordx4 v44, s[40:43], s61 offen lds\n\tbuffer_load_dwordx4 v45, s[40:43], s61 offen offset:1024 lds\n\t"
            "s_add_u32 m0, s65, 77824\n\ts_cbranch_vccnz 1f\n\tbuffer_load_dwordx4 v39, s[36:39], s60 offen offset:3072 lds\n\t1:\nbuffer_load_dwordx4 v36, s[36:39], s60 offen lds\n\tbuffer_load_dwordx4 v37, s[36:39], s60 offen offset:1024 lds\n\t"
            "buffer_load_dwordx4 v38, s[36:39], s60 offen offset:2048 lds\n\ts_bitcmp1_b32 18, s64\n\ts_cbranch_scc0 1f\n\tbuffer_store_dwordx4 v[136:139], v101, s[44:47], s18 offen offset:0 sc1\n\tbuffer_store_dwordx4 v[136:139], v2, s[44:47], s18 offen offset:1024 sc1\n\tbuffer_store_dwordx4 v[136:139], v2, s[44:47], s18 offen offset:2048 sc1\n\t"
            "buffer_store_dwordx4 v[136:139], v101, s[44:47], s18 offen offset:3072 sc1\n\t1:\ns_bitcmp1_b32 31, s64\n\ts_cbranch_scc1 2f\n\ts_cbranch_vccz 1f\n\ts_waitcnt vmcnt(9)\n\t"
            "1:\ns_waitcnt vmcnt(10)\n\t2:\ns_cbranch_vccz 1f\n\ts_waitcnt vmcnt(13)\n\t1:\n"
            "s_waitcnt vmcnt(14)\n\ts_barrier\n\ts_mov_b32 s60, s78\n\ts_mul_i32 s61, s60, 1920\n\ts_add_u32 m0, s66, 0\n\ts_add_u32 s61, s61, s14\n\t"
            "buffer_load_dwordx4 v40, s[40:43], s61 offen lds\n\tbuffer_load_dwordx4 v41, s[40:43], s61 offen offset:1024 lds\n\tbuffer_load_dwordx4 v42, s[40:43], s61 offen offset:2048 lds\n\tbuffer_load_dwordx4 v43, s[40:43], s61 offen offset:3072 lds\n\ts_addk_i32 m0, 0x1000\n\ts_nop 0\n\t"
            "buffer_load_dwordx4 v44, s[40:43], s61 offen lds\n\tbuffer_load_dwordx4 v45, s[40:43], s61 offen offset:1024 lds\n\ts_add_u32 m0, s65, 0\n\ts_cbranch_vccnz 1f\n\tbuffer_load_dwordx4 v39, s[36:39], s60 offen offset:3072 lds\n\t1:\n"
            "buffer_load_dwordx4 v36, s[36:39], s60 offen lds\n\tbuffer_load_dwordx4 v37, s[36:39], s60 offen offset:1024 lds\n\tbuffer_load_dwordx4 v38, s[36:39], s60 offen offset:2048 lds\n\ts_cbranch_vccz 1f\n\ts_waitcnt vmcnt(9)\n\t1:\n"
            "s_waitcnt vmcnt(10)\n\ts_barrier\n\ts_add_u32 s60, s33, 128\n\ts_mul_i32 s61, s60, 1920\n\ts_add_u32 m0, s66, 38912\n\ts_add_u32 s61, s61, s14\n\t"
            "buffer_load_dwordx4 v40, s[40:43], s61 offen lds\n\tbuffer_load_dwordx4 v41, s[40:43], s61 offen offset:1024 lds\n\tbuffer_load_dwordx4 v42, s[40:43], s61 offen offset:2048 lds\n\tbuffer_load_dwordx4 v43, s[40:43], s61 offen offset:3072 lds\n\ts_addk_i32 m0, 0x1000\n\ts_nop 0\n\t"
            "buffer_load_dwordx4 v44, s[40:43], s61 offen lds\n\tbuffer_load_dwordx4 v45, s[40:43], s61 offen offset:1024 lds\n\ts_add_u32 m0, s65, 38912\n\ts_cbranch_vccnz 1f\n\tbuffer_load_dwordx4 v39, s[36:39], s60 offen offset:3072 lds\n\t1:\n"
            "buffer_load_dwordx4 v36, s[36:39], s60 offen lds\n\tbuffer_load_dwordx4 v37, s[36:39], s60 offen offset:1024 lds\n\tbuffer_load_dwordx4 v38, s[36:39], s60 offen offset:2048 lds\n\tv_mov_b32 v140, s67\n\tbuffer_store_dword v140, v98, s[44:47], 0 offen sc1\n\ts_cbranch_vccz 1f\n\t"
            "s_waitcnt vmcnt(10)\n\t1:\ns_waitcnt vmcnt(11)\n\ts_barrier\n\ts_add_u32 s60, s34, 128\n\ts_mul_i32 s61, s60, 1920\n\t"
            "s_add_u32 m0, s66, 77824\n\ts_add_u32 s61, s61, s14\n\tbuffer_load_dwordx4 v40, s[40:43], s61 offen lds\n\tbuffer_load_dwordx4 v41, s[40:43], s61 offen offset:1024 lds\n\tbuffer_load_dwordx4 v42, s[40:43], s61 offen offset:2048 lds\n\tbuffer_load_dwordx4 v43, s[40:43], s61 offen offset:3072 lds\n\t"
            "s_addk_i32 m0, 0x1000\n\ts_nop 0\n\tbuffer_load_dwordx4 v44, s[40:43], s61 offen lds\n\tbuffer_load_dwordx4 v45, s[40:43], s61 offen offset:1024 lds\n\ts_add_u32 m0, s65, 77824\n\ts_cbranch_vccnz 1f\n\t"
            "buffer_load_dwordx4 v39, s[36:39], s60 offen offset:3072 lds\n\t1:\nbuffer_load_dwordx4 v36, s[36:39], s60 offen lds\n\tbuffer_load_dwordx4 v37, s[36:39], s60 offen offset:1024 lds\n\tbuffer_load_dwordx4 v38, s[36:39], s60 offen offset:2048 lds\n\ts_cbranch_vccz 1f\n\t"
            "s_waitcnt vmcnt(10)\n\t1:\ns_waitcnt vmcnt(11)\n\ts_cmp_lg_u32 s56, 3\n\ts_cbranch_scc1 1f\n\tbuffer_load_dwordx4 v[106:109], v158, s[44:47], 0 offen sc1\n\t"
            "buffer_load_dwordx4 v[110:113], v159, s[44:47], 0 offen sc1\n\tbuffer_load_dwordx4 v[114:117], v104, s[44:47], 0 offen sc1\n\t1:\ns_barrier\n\ts_add_u32 s60, s35, 128\n\ts_mul_i32 s61, s60, 1920\n\t"
            "s_add_u32 m0, s66, 0\n\ts_add_u32 s61, s61, s14\n\tbuffer_load_dwordx4 v40, s[40:43], s61 offen lds\n\tbuffer_load_dwordx4 v41, s[40:43], s61 offen offset:1024 lds\n\tbuffer_load_dwordx4 v42, s[40:43], s61 offen offset:2048 lds\n\tbuffer_load_dwordx4 v43, s[40:43], s61 offen offset:3072 lds\n\t"
            "s_addk_i32 m0, 0x1000\n\ts_nop 0\n\tbuffer_load_dwordx4 v44, s[40:43], s61 offen lds\n\tbuffer_load_dwordx4 v45, s[40:43], s61 offen offset:1024 lds\n\ts_add_u32 m0, s65, 0\n\ts_cbranch_vccnz 1f\n\t"
            "buffer_load_dwordx4 v39, s[36:39], s60 offen offset:3072 lds\n\t1:\nbuffer_load_dwordx4 v36, s[36:39], s60 offen lds\n\tbuffer_load_dwordx4 v37, s[36:39], s60 offen offset:1024 lds\n\tbuffer_load_dwordx4 v38, s[36:39], s60 offen offset:2048 lds\n\ts_add_u32 m0, s15, 40960\n\t"
            "s_nop 0\n\tbuffer_load_dwordx4 v156, s[48:51], s20 offen lds\n\ts_cmp_lg_u32 s56, 3\n\ts_cbranch_scc1 2f\n\ts_waitcnt vmcnt(10)\n\ts_mov_b32 s68, 0x200000\n\t"
            "v_xor_b32 v106, s67, v107\n\tv_xor_b32 v108, s67, v109\n\tv_xor_b32 v110, s67, v111\n\tv_xor_b32 v112, s67, v113\n\tv_xor_b32 v114, s67, v115\n\tv_xor_b32 v116, s67, v117\n\t"
            "v_or3_b32 v106, v106, v108, v110\n\tv_or3_b32 v112, v112, v114, v116\n\tv_or_b32 v106, v106, v112\n\tv_cmp_ne_u32 vcc, 0, v106\n\ts_cbranch_vccnz 30f\n\t31:\n"
            "s_mov_b64 vcc, -1\n\t2:\ns_cbranch_vccz 1f\n\ts_waitcnt vmcnt(10)\n\t1:\ns_waitcnt vmcnt(11)\n\t"
            "s_barrier\n\ts_add_u32 s60, s17, 128\n\ts_mul_i32 s61, s60, 1920\n\ts_add_u32 m0, s66, 38912\n\ts_add_u32 s61, s61, s14\n\tbuffer_load_dwordx4 v40, s[40:43], s61 offen lds\n\t"
            "buffer_load_dwordx4 v41, s[40:43], s61 offen offset:1024 lds\n\tbuffer_load_dwordx4 v42, s[40:43], s61 offen offset:2048 lds\n\tbuffer_load_dwordx4 v43, s[40:43], s61 offen offset:3072 lds\n\ts_addk_i32 m0, 0x1000\n\ts_nop 0\n\tbuffer_load_dwordx4 v44, s[40:43], s61 offen lds\n\t"
            "buffer_load_dwordx4 v45, s[40:43], s61 offen offset:1024 lds\n\ts_add_u32 m0, s65, 38912\n\ts_cbranch_vccnz 1f\n\tbuffer_load_dwordx4 v39, s[36:39], s60 offen offset:3072 lds\n\t1:\nbuffer_load_dwordx4 v36, s[36:39], s60 offen lds\n\t"
            "buffer_load_dwordx4 v37, s[36:39], s60 offen offset:1024 lds\n\tbuffer_load_dwordx4 v38, s[36:39], s60 offen offset:2048 lds\n\ts_add_u32 m0, s15, 40960\n\ts_nop 0\n\tbuffer_load_dwordx4 v156, s[48:51], s21 offen offset:1024 lds\n\tbuffer_load_dwordx4 v156, s[48:51], s22 offen offset:2048 lds\n\t"
            "s_mov_b32 m0, s30\n\ts_nop 0\n\tbuffer_load_dword v97, s[44:47], 0 offen sc1 lds\n\tbuffer_load_dword v118, v96, s[44:47], 0 offen sc1\n\ts_cbranch_vccz 1f\n\ts_waitcnt vmcnt(13)\n\t"
            "1:\ns_waitcnt vmcnt(14)\n\ts_barrier\n\ts_add_u32 m0, s15, 40960\n\ts_nop 0\n\tbuffer_load_dwordx4 v156, s[48:51], s23 offen offset:3072 lds\n\t"
            "s_add_u32 m0, s15, 45056\n\ts_nop 0\n\tbuffer_load_dwordx4 v156, s[48:51], s24 offen lds\n\tbuffer_load_dwordx4 v156, s[48:51], s25 offen offset:1024 lds\n\tbuffer_load_dwordx4 v156, s[48:51], s26 offen offset:2048 lds\n\tbuffer_load_dwordx4 v156, s[48:51], s27 offen offset:3072 lds\n\t"
            "s_add_u32 m0, s15, 49152\n\ts_nop 0\n\tbuffer_load_dwordx4 v156, s[48:51], s28 offen lds\n\tbuffer_load_dwordx4 v156, s[48:51], s29 offen offset:1024 lds\n\ts_waitcnt vmcnt(7)\n\tv_cmp_ne_u32 vcc, s67, v118\n\t"
            "s_cmp_lg_u32 s56, 3\n\ts_cbranch_scc1 33f\n\ts_cbranch_vccnz 32f\n\t33:\ns_barrier\n\ts_add_u32 m0, s15, 0\n\t"
            "s_nop 0\n\tbuffer_load_dwordx4 v154, s[48:51], s20 offen lds\n\tbuffer_load_dwordx4 v154, s[48:51], s21 offen offset:1024 lds\n\tbuffer_load_dwordx4 v154, s[48:51], s22 offen offset:2048 lds\n\tbuffer_load_dwordx4 v154, s[48:51], s23 offen offset:3072 lds\n\ts_add_u32 m0, s15, 4096\n\t"
            "s_nop 0\n\tbuffer_load_dwordx4 v154, s[48:51], s24 offen lds\n\tbuffer_load_dwordx4 v154, s[48:51], s25 offen offset:1024 lds\n\tbuffer_load_dwordx4 v154, s[48:51], s26 offen offset:2048 lds\n\tbuffer_load_dwordx4 v154, s[48:51], s27 offen offset:3072 lds\n\ts_add_u32 m0, s15, 8192\n\t"
            "s_nop 0\n\tbuffer_load_dwordx4 v154, s[48:51], s28 offen lds\n\tbuffer_load_dwordx4 v154, s[48:51], s29 offen offset:1024 lds\n\ts_cmp_lg_u32 s56, 0\n\ts_cbranch_scc1 1f\n\ts_mov_b32 m0, 162624\n\t"
            "s_nop 0\n\tbuffer_load_dwordx4 v104, s[44:47], 0 offen sc1 lds\n\t1:\nbuffer_load_dwordx4 v[240:243], v3, s[36:39], 0 offen offset:0\n\tbuffer_load_dwordx4 v[244:247], v11, s[36:39], 0 offen offset:0\n\tbuffer_load_dwordx4 v[248:251], v3, s[36:39], 0 offen offset:64\n\t"
            "buffer_load_dwordx4 v[252:255], v11, s[36:39], 0 offen offset:64\n\tds_read_b32 v20, v9 offset:256\n\tds_read_b32 v21, v10 offset:832\n\tds_read2st64_b32 v[138:139], v153 offset1:1\n\tds_read2st64_b32 v[176:177], v160 offset1:1\n\tds_read2st64_b32 v[178:179], v161 offset1:1\n\t"
            "ds_read2st64_b32 v[180:181], v162 offset1:1\n\tds_read2st64_b32 v[182:183], v163 offset1:1\n\tds_read2st64_b32 v[184:185], v164 offset1:1\n\tds_read2st64_b32 v[186:187], v165 offset1:1\n\tds_read2st64_b32 v[188:189], v166 offset1:1\n\tds_read2st64_b32 v[190:191], v167 offset1:1\n\t"
            "s_mov_b32 s68, 0x200000\n\ts_waitcnt lgkmcnt(8)\n\tv_mul_f32 v138, 0x3ea52b5c, v138\n\tv_mul_f32 v139, 0x3ea52b5c, v139\n\ts_waitcnt lgkmcnt(7)\n\tv_mul_f32 v192, v176, v138\n\t"
            "v_mul_f32 v200, v177, v139\n\ts_waitcnt lgkmcnt(6)\n\tv_mul_f32 v193, v178, v138\n\tv_mul_f32 v201, v179, v139\n\ts_waitcnt lgkmcnt(5)\n\tv_mul_f32 v194, v180, v138\n\t"
            "v_mul_f32 v202, v181, v139\n\ts_waitcnt lgkmcnt(4)\n\tv_mul_f32 v195, v182, v138\n\tv_mul_f32 v203, v183, v139\n\ts_waitcnt lgkmcnt(3)\n\tv_mul_f32 v196, v184, v138\n\t"
            "v_mul_f32 v204, v185, v139\n\ts_waitcnt lgkmcnt(2)\n\tv_mul_f32 v197, v186, v138\n\tv_mul_f32 v205, v187, v139\n\ts_waitcnt lgkmcnt(1)\n\tv_mul_f32 v198, v188, v138\n\t"
            "v_mul_f32 v206, v189, v139\n\ts_waitcnt lgkmcnt(0)\n\tv_mul_f32 v199, v190, v138\n\tv_mul_f32 v207, v191, v139\n\ts_barrier\n\t"
            "50:\nds_read2_b64 v[208:211], v24 offset0:0 offset1:16\n\tds_read2_b64 v[212:215], v24 offset0:32 offset1:48\n\tds_read2_b64 v[216:219], v26 offset0:0 offset1:16\n\tds_read2_b64 v[220:223], v26 offset0:32 offset1:48\n\ts_cmp_eq_u32 s16, 0\n\t"
            "s_cbranch_scc1 56f\n\t58:\ns_cbranch_scc0 1f\n\tbuffer_load_dword v232, v31, s[44:47], s31 offen offset:0 sc1\n\tbuffer_load_dword v233, v31, s[44:47], s31 offen offset:256 sc1\n\tbuffer_load_dword v234, v31, s[44:47], s31 offen offset:512 sc1\n\t"
            "buffer_load_dword v235, v31, s[44:47], s31 offen offset:768 sc1\n\t1:\ns_waitcnt lgkmcnt(1)\n\tv_add_f32 v208, v208, v216\n\tv_add_f32 v209, v209, v217\n\tv_add_f32 v210, v210, v218\n\t"
            "v_add_f32 v211, v211, v219\n\ts_waitcnt lgkmcnt(0)\n\tv_add_f32 v212, v212, v220\n\tv_add_f32 v213, v213, v221\n\tv_add_f32 v214, v214, v222\n\tv_add_f32 v215, v215, v223\n\t"
            "v_fma_f32 v208, v208, v192, v128\n\tv_fma_f32 v209, v209, v193, v129\n\tv_fma_f32 v210, v210, v194, v130\n\tv_fma_f32 v211, v211, v195, v131\n\tv_fma_f32 v212, v212, v196, v132\n\tv_fma_f32 v213, v213, v197, v133\n\t"
            "v_fma_f32 v214, v214, v198, v134\n\tv_fma_f32 v215, v215, v199, v135\n\tv_max3_f32 v136, v208, v209, v210\n\tv_max3_f32 v137, v211, v212, v213\n\tv_max3_f32 v136, v136, v214, v215\n\tv_max_f32 v136, v136, v137\n\t"
            "v_sub_f32 v208, v208, v136\n\tv_sub_f32 v209, v209, v136\n\tv_sub_f32 v210, v210, v136\n\tv_sub_f32 v211, v211, v136\n\tv_sub_f32 v212, v212, v136\n\tv_sub_f32 v213, v213, v136\n\t"
            "v_sub_f32 v214, v214, v136\n\tv_sub_f32 v215, v215, v136\n\tv_exp_f32 v208, v208\n\tv_exp_f32 v209, v209\n\tv_exp_f32 v210, v210\n\tv_exp_f32 v211, v211\n\t"
            "v_exp_f32 v212, v212\n\tv_exp_f32 v213, v213\n\tv_exp_f32 v214, v214\n\tv_exp_f32 v215, v215\n\tv_add_f32 v216, v208, v209\n\tv_add_f32 v217, v210, v211\n\t"
            "v_add_f32 v218, v212, v213\n\tv_add_f32 v219, v214, v215\n\tv_add_f32 v216, v216, v217\n\tv_add_f32 v218, v218, v219\n\tv_mul_f32 v208, v208, v176\n\tv_add_f32 v216, v216, v218\n\t"
            "v_mul_f32 v216, 0xbfb8aa3b, v216\n\tv_rcp_f32 v216, v216\n\tv_mul_f32 v209, v209, v178\n\tv_mul_f32 v210, v210, v180\n\tv_mul_f32 v211, v211, v182\n\tv_mul_f32 v212, v212, v184\n\t"
            "v_mul_f32 v213, v213, v186\n\tv_mul_f32 v214, v214, v188\n\tv_mul_f32 v215, v215, v190\n\tv_mul_f32 v208, v208, v216\n\tv_mul_f32 v209, v209, v216\n\tv_mul_f32 v210, v210, v216\n\t"
            "v_mul_f32 v211, v211, v216\n\tv_mul_f32 v212, v212, v216\n\tv_mul_f32 v213, v213, v216\n\tv_mul_f32 v214, v214, v216\n\tv_mul_f32 v215, v215, v216\n\tv_cvt_pk_bf16_f32 v220, v208, v209\n\t"
            "v_cvt_pk_bf16_f32 v221, v210, v211\n\tv_cvt_pk_bf16_f32 v222, v212, v213\n\tv_cvt_pk_bf16_f32 v223, v214, v215\n\tds_write_b128 v29, v[220:223] offset:0\n\tds_write_b32 v30, v142 offset:16\n\t"
            "51:\nds_read_b32 v136, v28\n\tds_read2_b64 v[208:211], v25 offset0:0 offset1:16\n\tds_read2_b64 v[212:215], v25 offset0:32 offset1:48\n\tds_read2_b64 v[216:219], v27 offset0:0 offset1:16\n\tds_read2_b64 v[220:223], v27 offset0:32 offset1:48\n\t"
            "s_waitcnt lgkmcnt(4)\n\tv_cmp_eq_u32 vcc, 0, v136\n\ts_cbranch_vccnz 53f\n\t55:\n"
            "s_setprio 3\n\t"
            "s_cmp_eq_u32 s16, 0\n\ts_cbranch_scc1 57f\n\t59:\ns_waitcnt lgkmcnt(1)\n\tv_add_f32 v208, v208, v216\n\tv_add_f32 v209, v209, v217\n\t"
            "v_add_f32 v210, v210, v218\n\tv_add_f32 v211, v211, v219\n\ts_waitcnt lgkmcnt(0)\n\tv_add_f32 v212, v212, v220\n\tv_add_f32 v213, v213, v221\n\tv_add_f32 v214, v214, v222\n\t"
            "v_add_f32 v215, v215, v223\n\tv_mul_f32 v208, v208, v200\n\tv_mul_f32 v209, v209, v201\n\tv_mul_f32 v210, v210, v202\n\tv_mul_f32 v211, v211, v203\n\tv_mul_f32 v212, v212, v204\n\t"
            "v_mul_f32 v213, v213, v205\n\tv_mul_f32 v214, v214, v206\n\tv_mul_f32 v215, v215, v207\n\tv_max3_f32 v136, v208, v209, v210\n\tv_max3_f32 v137, v211, v212, v213\n\tv_max3_f32 v136, v136, v214, v215\n\t"
            "v_max_f32 v136, v136, v137\n\tv_sub_f32 v208, v208, v136\n\tv_sub_f32 v209, v209, v136\n\tv_sub_f32 v210, v210, v136\n\tv_sub_f32 v211, v211, v136\n\tv_sub_f32 v212, v212, v136\n\t"
            "v_sub_f32 v213, v213, v136\n\tv_sub_f32 v214, v214, v136\n\tv_sub_f32 v215, v215, v136\n\tv_exp_f32 v208, v208\n\tv_exp_f32 v209, v209\n\tv_exp_f32 v210, v210\n\t"
            "v_exp_f32 v211, v211\n\tv_exp_f32 v212, v212\n\tv_exp_f32 v213, v213\n\tv_exp_f32 v214, v214\n\tv_exp_f32 v215, v215\n\tv_add_f32 v216, v208, v209\n\t"
            "v_add_f32 v217, v210, v211\n\tv_add_f32 v218, v212, v213\n\tv_add_f32 v219, v214, v215\n\tv_add_f32 v216, v216, v217\n\tv_add_f32 v218, v218, v219\n\tv_mul_f32 v208, v208, v177\n\t"
            "v_add_f32 v216, v216, v218\n\tv_mul_f32 v216, 0xbfb8aa3b, v216\n\tv_rcp_f32 v216, v216\n\tv_mul_f32 v209, v209, v179\n\tv_mul_f32 v210, v210, v181\n\tv_mul_f32 v211, v211, v183\n\t"
            "v_mul_f32 v212, v212, v185\n\tv_mul_f32 v213, v213, v187\n\tv_mul_f32 v214, v214, v189\n\tv_mul_f32 v215, v215, v191\n\tv_mul_f32 v208, v208, v216\n\tv_mul_f32 v209, v209, v216\n\t"
            "v_mul_f32 v210, v210, v216\n\tv_mul_f32 v211, v211, v216\n\tv_mul_f32 v212, v212, v216\n\tv_mul_f32 v213, v213, v216\n\tv_mul_f32 v214, v214, v216\n\tv_mul_f32 v215, v215, v216\n\t"
            "v_cvt_pk_bf16_f32 v220, v208, v209\n\tv_cvt_pk_bf16_f32 v221, v210, v211\n\tv_cvt_pk_bf16_f32 v222, v212, v213\n\tv_cvt_pk_bf16_f32 v223, v214, v215\n\tds_write_b128 v29, v[220:223] offset:38912\n\tds_write_b32 v30, v30 offset:16\n\t"
            "s_setprio 0\n\t"
            "s_waitcnt vmcnt(0)\n\tds_write_b32 v30, v30 offset:48\n\ts_branch .LOL\n\t32:\nbuffer_load_dword v118, v96, s[44:47], 0 offen sc1\n\t"
            "s_waitcnt vmcnt(0)\n\tv_cmp_ne_u32 vcc, s67, v118\n\ts_cbranch_vccz 33b\n\ts_sub_u32 s68, s68, 1\n\ts_cbranch_scc0 32b\n\ts_branch 33b\n\t"
            "30:\nbuffer_load_dwordx4 v[106:109], v158, s[44:47], 0 offen sc1\n\tbuffer_load_dwordx4 v[110:113], v159, s[44:47], 0 offen sc1\n\tbuffer_load_dwordx4 v[114:117], v104, s[44:47], 0 offen sc1\n\t"
            "s_waitcnt vmcnt(2)\n\tv_xor_b32 v106, s67, v107\n\tv_xor_b32 v108, s67, v109\n\ts_waitcnt vmcnt(1)\n\tv_xor_b32 v110, s67, v111\n\tv_xor_b32 v112, s67, v113\n\t"
            "s_waitcnt vmcnt(0)\n\tv_xor_b32 v114, s67, v115\n\tv_xor_b32 v116, s67, v117\n\tv_or3_b32 v106, v106, v108, v110\n\tv_or3_b32 v112, v112, v114, v116\n\tv_or_b32 v106, v106, v112\n\t"
            "v_cmp_ne_u32 vcc, 0, v106\n\ts_cbranch_vccz 31b\n\ts_sub_u32 s68, s68, 1\n\ts_cbranch_scc0 30b\n\ts_branch 31b\n\t53:\n"
            "s_sleep 1\n\t"
            "s_sub_u32 s68, s68, 1\n\ts_cbranch_scc0 51b\n\ts_branch 55b\n\t56:\n"
            "s_sleep 6\n\t"
            "buffer_load_dword v224, v31, s[44:47], 0 offen offset:0 sc1\n\tbuffer_load_dword v225, v31, s[44:47], 0 offen offset:256 sc1\n\tbuffer_load_dword v226, v31, s[44:47], 0 offen offset:512 sc1\n\tbuffer_load_dword v227, v31, s[44:47], 0 offen offset:768 sc1\n\ts_waitcnt vmcnt(4)\n\t"
            "s_setprio 0\n\t"
            "s_call_b64 s[76:77], .LG0\n\t"
            "s_setprio 3\n\t"
            "s_branch 2f\n\t1:\ns_waitcnt vmcnt(3)\n\tbuffer_load_dword v224, v31, s[44:47], 0 offen offset:0 sc1\n\ts_waitcnt vmcnt(3)\n\tbuffer_load_dword v225, v31, s[44:47], 0 offen offset:256 sc1\n\t"
            "s_waitcnt vmcnt(3)\n\tbuffer_load_dword v226, v31, s[44:47], 0 offen offset:512 sc1\n\ts_waitcnt vmcnt(3)\n\tbuffer_load_dword v227, v31, s[44:47], 0 offen offset:768 sc1\n\ts_cmp_lg_u32 s83, 0\n\ts_cbranch_scc1 3f\n\t"
            "s_call_b64 s[76:77], .LG1\n\t3:\ns_cmp_eq_u32 s83, 2\n\ts_cbranch_scc1 2f\n\ts_call_b64 s[76:77], .LG2\n\t2:\n"
            "s_waitcnt vmcnt(0) lgkmcnt(0)\n\t4:\nds_write_b32 v30, v30 offset:48\n\tv_max3_u32 v136, v224, v225, v226\n\tv_max_u32 v136, v136, v227\n\tv_cmp_eq_u32 vcc, -1, v136\n\t"
            "v_lshlrev_b32 v216, 16, v224\n\tv_and_b32 v217, 0xffff0000, v224\n\tv_lshlrev_b32 v218, 16, v225\n\tv_and_b32 v219, 0xffff0000, v225\n\tv_lshlrev_b32 v220, 16, v226\n\tv_and_b32 v221, 0xffff0000, v226\n\t"
            "v_lshlrev_b32 v222, 16, v227\n\tv_and_b32 v223, 0xffff0000, v227\n\t"
            "s_cmp_eq_u32 0, 0\n\ts_cbranch_vccz 58b\n\ts_sub_u32 s68, s68, 1\n\ts_cbranch_scc0 1b\n\ts_branch 58b\n\t57:\n"
            "s_branch 2f\n\t1:\nbuffer_load_dword v232, v31, s[44:47], s31 offen offset:0 sc1\n\tbuffer_load_dword v233, v31, s[44:47], s31 offen offset:256 sc1\n\tbuffer_load_dword v234, v31, s[44:47], s31 offen offset:512 sc1\n\tbuffer_load_dword v235, v31, s[44:47], s31 offen offset:768 sc1\n\t"
            "s_cmp_lg_u32 s83, 0\n\ts_cbranch_scc1 3f\n\ts_call_b64 s[76:77], .LG1\n\t3:\ns_cmp_eq_u32 s83, 2\n\ts_cbranch_scc1 2f\n\t"
            "s_call_b64 s[76:77], .LG2\n\t2:\ns_waitcnt vmcnt(0) lgkmcnt(0)\n\t4:\nv_max3_u32 v136, v232, v233, v234\n\tv_max_u32 v136, v136, v235\n\t"
            "v_cmp_eq_u32 vcc, -1, v136\n\tv_lshlrev_b32 v216, 16, v232\n\tv_and_b32 v217, 0xffff0000, v232\n\tv_lshlrev_b32 v218, 16, v233\n\tv_and_b32 v219, 0xffff0000, v233\n\tv_lshlrev_b32 v220, 16, v234\n\t"
            "v_and_b32 v221, 0xffff0000, v234\n\tv_lshlrev_b32 v222, 16, v235\n\tv_and_b32 v223, 0xffff0000, v235\n\t"
            "s_cmp_eq_u32 0, 0\n\ts_cbranch_vccz 59b\n\ts_sub_u32 s68, s68, 1\n\ts_cbranch_scc0 1b\n\ts_branch 59b\n\t"
            ".LOL:\ns_lshr_b32 s71, s56, 1\n\ts_mov_b32 s68, 0x200000\n\ts_cmp_eq_u32 s16, 0\n\ts_cbranch_scc1 3f\n\ts_call_b64 s[76:77], .LG0\n\t"
            "3:\nds_read_b64 v[14:15], v8 offset:32\n\ts_waitcnt lgkmcnt(0)\n\tv_min_u32 v12, v14, v15\n\tv_cmp_lt_u32 vcc, s71, v12\n\ts_cbranch_vccnz 4f\n\t"
            "s_sleep 1\n\ts_sub_u32 s68, s68, 1\n\ts_cbranch_scc0 3b\n\t4:\ns_cmp_eq_u32 s16, 0\n\ts_cbranch_scc1 1f\n\t"
            "s_call_b64 s[76:77], .LG1\n\t1:\ns_cmp_lt_u32 s56, 2\n\ts_cbranch_scc1 1f\n\ts_sleep 3\n\t1:\n"
            "buffer_load_dwordx4 v[176:179], v2, s[44:47], s4 offen offset:1024 sc1\n\tbuffer_load_dwordx4 v[180:183], v2, s[44:47], s4 offen offset:2048 sc1\n\tbuffer_load_dwordx4 v[184:187], v2, s[44:47], s5 offen offset:1024 sc1\n\tbuffer_load_dwordx4 v[188:191], v2, s[44:47], s5 offen offset:2048 sc1\n\tbuffer_load_dwordx4 v[192:195], v2, s[44:47], s6 offen offset:1024 sc1\n\tbuffer_load_dwordx4 v[196:199], v2, s[44:47], s6 offen offset:2048 sc1\n\t"
            "buffer_load_dwordx4 v[200:203], v2, s[44:47], s7 offen offset:1024 sc1\n\tbuffer_load_dwordx4 v[204:207], v2, s[44:47], s7 offen offset:2048 sc1\n\tbuffer_load_dwordx4 v[208:211], v2, s[44:47], s8 offen offset:1024 sc1\n\tbuffer_load_dwordx4 v[212:215], v2, s[44:47], s8 offen offset:2048 sc1\n\ts_cmp_lg_u32 s83, 0\n\ts_cbranch_scc1 1f\n\t"
            "s_call_b64 s[76:77], .LG1\n\t1:\ns_cmp_eq_u32 s83, 2\n\ts_cbranch_scc1 1f\n\ts_call_b64 s[76:77], .LG2\n\t1:\n"
            "ds_read_b128 v[16:19], v8 offset:48\n\tds_read_b64_tr_b16 v[128:129], v4 offset:40960\n\tds_read_b64_tr_b16 v[130:131], v4 offset:41472\n\tds_read_b64_tr_b16 v[132:133], v5 offset:40960\n\tds_read_b64_tr_b16 v[134:135], v5 offset:41472\n\tds_read_b64_tr_b16 v[136:137], v6 offset:40960\n\t"
            "ds_read_b64_tr_b16 v[138:139], v6 offset:41472\n\tds_read_b64_tr_b16 v[140:141], v7 offset:40960\n\tds_read_b64_tr_b16 v[142:143], v7 offset:41472\n\tds_read_b64_tr_b16 v[144:145], v4 offset:41984\n\tds_read_b64_tr_b16 v[146:147], v4 offset:42496\n\tds_read_b64_tr_b16 v[148:149], v5 offset:41984\n\t"
            "ds_read_b64_tr_b16 v[150:151], v5 offset:42496\n\tds_read_b64_tr_b16 v[152:153], v6 offset:41984\n\tds_read_b64_tr_b16 v[154:155], v6 offset:42496\n\tds_read_b64_tr_b16 v[156:157], v7 offset:41984\n\tds_read_b64_tr_b16 v[158:159], v7 offset:42496\n\tv_mul_f32 v48, v48, v112\n\t"
            "v_mul_f32 v49, v49, v113\n\tv_mul_f32 v50, v50, v114\n\tv_mul_f32 v51, v51, v115\n\tv_mul_f32 v52, v52, v116\n\tv_mul_f32 v53, v53, v117\n\tv_mul_f32 v54, v54, v118\n\t"
            "v_mul_f32 v55, v55, v119\n\tv_mul_f32 v56, v56, v120\n\tv_mul_f32 v57, v57, v121\n\tv_mul_f32 v58, v58, v122\n\tv_mul_f32 v59, v59, v123\n\tv_mul_f32 v60, v60, v124\n\t"
            "v_mul_f32 v61, v61, v125\n\tv_mul_f32 v62, v62, v126\n\tv_mul_f32 v63, v63, v127\n\t"
            "s_waitcnt lgkmcnt(15)\n\tv_min3_u32 v16, v16, v17, v18\n\tv_min_u32 v16, v16, v19\n\tv_cmp_eq_u32 vcc, 0, v16\n\ts_cbranch_vccnz 62f\n\t63:\n"
            "s_waitcnt vmcnt(8)\n\tv_max3_u32 v12, v176, v178, v180\n\tv_max_u32 v12, v12, v182\n\tv_cmp_eq_u32 vcc, -1, v12\n\ts_cbranch_vccnz 100f\n\t200:\n"
            "s_waitcnt lgkmcnt(8)\n\tv_mfma_f32_32x32x16_bf16 v[96:111], v[128:131], v[176:179], 0\n\tds_read_b64_tr_b16 v[160:161], v4 offset:43008\n\tds_read_b64_tr_b16 v[162:163], v4 offset:43520\n\tbuffer_load_dwordx4 v[216:219], v2, s[44:47], s9 offen offset:1024 sc1\n\tv_mfma_f32_32x32x16_bf16 v[112:127], v[132:135], v[176:179], 0\n\t"
            "ds_read_b64_tr_b16 v[164:165], v5 offset:43008\n\tds_read_b64_tr_b16 v[166:167], v5 offset:43520\n\tbuffer_load_dwordx4 v[220:223], v2, s[44:47], s9 offen offset:2048 sc1\n\tv_mfma_f32_32x32x16_bf16 v[96:111], v[136:139], v[180:183], v[96:111]\n\tds_read_b64_tr_b16 v[168:169], v6 offset:43008\n\tds_read_b64_tr_b16 v[170:171], v6 offset:43520\n\t"
            "s_waitcnt vmcnt(8)\n\tv_max3_u32 v12, v184, v186, v188\n\tv_max_u32 v12, v12, v190\n\tv_cmp_eq_u32 vcc, -1, v12\n\tv_mfma_f32_32x32x16_bf16 v[112:127], v[140:143], v[180:183], v[112:127]\n\tds_read_b64_tr_b16 v[172:173], v7 offset:43008\n\t"
            "ds_read_b64_tr_b16 v[174:175], v7 offset:43520\n\ts_cbranch_vccnz 101f\n\t201:\ns_waitcnt lgkmcnt(8)\n\tv_mfma_f32_32x32x16_bf16 v[96:111], v[144:147], v[184:187], v[96:111]\n\tds_read_b64_tr_b16 v[128:129], v4 offset:44032\n\t"
            "ds_read_b64_tr_b16 v[130:131], v4 offset:44544\n\tbuffer_load_dwordx4 v[224:227], v2, s[44:47], s10 offen offset:1024 sc1\n\tv_mfma_f32_32x32x16_bf16 v[112:127], v[148:151], v[184:187], v[112:127]\n\tds_read_b64_tr_b16 v[132:133], v5 offset:44032\n\tds_read_b64_tr_b16 v[134:135], v5 offset:44544\n\tbuffer_load_dwordx4 v[228:231], v2, s[44:47], s10 offen offset:2048 sc1\n\t"
            "v_mfma_f32_32x32x16_bf16 v[96:111], v[152:155], v[188:191], v[96:111]\n\tds_read_b64_tr_b16 v[136:137], v6 offset:44032\n\tds_read_b64_tr_b16 v[138:139], v6 offset:44544\n\ts_waitcnt vmcnt(8)\n\tv_max3_u32 v12, v192, v194, v196\n\tv_max_u32 v12, v12, v198\n\t"
            "v_cmp_eq_u32 vcc, -1, v12\n\tv_mfma_f32_32x32x16_bf16 v[112:127], v[156:159], v[188:191], v[112:127]\n\tds_read_b64_tr_b16 v[140:141], v7 offset:44032\n\tds_read_b64_tr_b16 v[142:143], v7 offset:44544\n\ts_cbranch_vccnz 102f\n\t202:\n"
            "s_waitcnt lgkmcnt(8)\n\tv_mfma_f32_32x32x16_bf16 v[96:111], v[160:163], v[192:195], v[96:111]\n\tds_read_b64_tr_b16 v[144:145], v4 offset:45056\n\tds_read_b64_tr_b16 v[146:147], v4 offset:45568\n\tbuffer_load_dwordx4 v[232:235], v2, s[44:47], s11 offen offset:1024 sc1\n\tv_mfma_f32_32x32x16_bf16 v[112:127], v[164:167], v[192:195], v[112:127]\n\t"
            "ds_read_b64_tr_b16 v[148:149], v5 offset:45056\n\tds_read_b64_tr_b16 v[150:151], v5 offset:45568\n\tbuffer_load_dwordx4 v[236:239], v2, s[44:47], s11 offen offset:2048 sc1\n\tv_mfma_f32_32x32x16_bf16 v[96:111], v[168:171], v[196:199], v[96:111]\n\tds_read_b64_tr_b16 v[152:153], v6 offset:45056\n\tds_read_b64_tr_b16 v[154:155], v6 offset:45568\n\t"
            "s_waitcnt vmcnt(8)\n\tv_max3_u32 v12, v200, v202, v204\n\tv_max_u32 v12, v12, v206\n\tv_cmp_eq_u32 vcc, -1, v12\n\tv_mfma_f32_32x32x16_bf16 v[112:127], v[172:175], v[196:199], v[112:127]\n\tds_read_b64_tr_b16 v[156:157], v7 offset:45056\n\t"
            "ds_read_b64_tr_b16 v[158:159], v7 offset:45568\n\ts_cbranch_vccnz 103f\n\t203:\ns_waitcnt lgkmcnt(8)\n\tv_mfma_f32_32x32x16_bf16 v[96:111], v[128:131], v[200:203], v[96:111]\n\tds_read_b64_tr_b16 v[160:161], v4 offset:46080\n\t"
            "ds_read_b64_tr_b16 v[162:163], v4 offset:46592\n\tbuffer_load_dwordx4 v[176:179], v2, s[44:47], s12 offen offset:1024 sc1\n\tv_mfma_f32_32x32x16_bf16 v[112:127], v[132:135], v[200:203], v[112:127]\n\tds_read_b64_tr_b16 v[164:165], v5 offset:46080\n\tds_read_b64_tr_b16 v[166:167], v5 offset:46592\n\tds_read_b64 v[14:15], v8 offset:40\n\t"
            "buffer_load_dwordx4 v[180:183], v2, s[44:47], s12 offen offset:2048 sc1\n\tv_mfma_f32_32x32x16_bf16 v[96:111], v[136:139], v[204:207], v[96:111]\n\tds_read_b64_tr_b16 v[168:169], v6 offset:46080\n\tds_read_b64_tr_b16 v[170:171], v6 offset:46592\n\ts_waitcnt vmcnt(8)\n\tv_max3_u32 v12, v208, v210, v212\n\t"
            "v_max_u32 v12, v12, v214\n\tv_cmp_eq_u32 vcc, -1, v12\n\tv_mfma_f32_32x32x16_bf16 v[112:127], v[140:143], v[204:207], v[112:127]\n\tds_read_b64_tr_b16 v[172:173], v7 offset:46080\n\tds_read_b64_tr_b16 v[174:175], v7 offset:46592\n\ts_cbranch_vccnz 104f\n\t"
            "204:\ns_waitcnt lgkmcnt(9)\n\tv_mfma_f32_32x32x16_bf16 v[96:111], v[144:147], v[208:211], v[96:111]\n\tds_read_b64_tr_b16 v[128:129], v4 offset:47104\n\tds_read_b64_tr_b16 v[130:131], v4 offset:47616\n\tbuffer_load_dwordx4 v[184:187], v2, s[44:47], s13 offen offset:1024 sc1\n\t"
            "v_mfma_f32_32x32x16_bf16 v[112:127], v[148:151], v[208:211], v[112:127]\n\tds_read_b64_tr_b16 v[132:133], v5 offset:47104\n\tds_read_b64_tr_b16 v[134:135], v5 offset:47616\n\tbuffer_load_dwordx4 v[188:191], v2, s[44:47], s13 offen offset:2048 sc1\n\tv_mfma_f32_32x32x16_bf16 v[96:111], v[152:155], v[212:215], v[96:111]\n\tds_read_b64_tr_b16 v[136:137], v6 offset:47104\n\t"
            "ds_read_b64_tr_b16 v[138:139], v6 offset:47616\n\ts_waitcnt vmcnt(8)\n\tv_max3_u32 v12, v216, v218, v220\n\tv_max_u32 v12, v12, v222\n\tv_cmp_eq_u32 vcc, -1, v12\n\tv_mfma_f32_32x32x16_bf16 v[112:127], v[156:159], v[212:215], v[112:127]\n\t"
            "ds_read_b64_tr_b16 v[140:141], v7 offset:47104\n\tds_read_b64_tr_b16 v[142:143], v7 offset:47616\n\ts_cbranch_vccnz 105f\n\t205:\ns_waitcnt lgkmcnt(8)\n\tv_mfma_f32_32x32x16_bf16 v[96:111], v[160:163], v[216:219], v[96:111]\n\t"
            "v_min_u32 v14, v14, v15\n\tv_cmp_ge_u32 vcc, s71, v14\n\tds_read_b64_tr_b16 v[144:145], v4 offset:48128\n\tds_read_b64_tr_b16 v[146:147], v4 offset:48640\n\tv_mfma_f32_32x32x16_bf16 v[112:127], v[164:167], v[216:219], v[112:127]\n\ts_cbranch_vccnz 5f\n\t"
            "6:\nds_read_b64_tr_b16 v[148:149], v5 offset:48128\n\tds_read_b64_tr_b16 v[150:151], v5 offset:48640\n\tbuffer_load_dwordx4 v[192:195], v2, s[44:47], s4 offen offset:0 sc1\n\tbuffer_load_dwordx4 v[196:199], v2, s[44:47], s4 offen offset:3072 sc1\n\tv_mfma_f32_32x32x16_bf16 v[96:111], v[168:171], v[220:223], v[96:111]\n\t"
            "ds_read_b64_tr_b16 v[152:153], v6 offset:48128\n\tds_read_b64_tr_b16 v[154:155], v6 offset:48640\n\ts_waitcnt vmcnt(8)\n\tv_max3_u32 v12, v224, v226, v228\n\tv_max_u32 v12, v12, v230\n\tv_cmp_eq_u32 vcc, -1, v12\n\t"
            "v_mfma_f32_32x32x16_bf16 v[112:127], v[172:175], v[220:223], v[112:127]\n\tds_read_b64_tr_b16 v[156:157], v7 offset:48128\n\tds_read_b64_tr_b16 v[158:159], v7 offset:48640\n\ts_cbranch_vccnz 106f\n\t206:\ns_waitcnt lgkmcnt(8)\n\t"
            "v_mfma_f32_32x32x16_bf16 v[96:111], v[128:131], v[224:227], v[96:111]\n\tds_read_b64_tr_b16 v[160:161], v4 offset:49152\n\tds_read_b64_tr_b16 v[162:163], v4 offset:49664\n\tbuffer_load_dwordx4 v[200:203], v2, s[44:47], s5 offen offset:0 sc1\n\tv_mfma_f32_32x32x16_bf16 v[112:127], v[132:135], v[224:227], v[112:127]\n\tds_read_b64_tr_b16 v[164:165], v5 offset:49152\n\t"
            "ds_read_b64_tr_b16 v[166:167], v5 offset:49664\n\tbuffer_load_dwordx4 v[204:207], v2, s[44:47], s5 offen offset:3072 sc1\n\tv_mfma_f32_32x32x16_bf16 v[96:111], v[136:139], v[228:231], v[96:111]\n\tds_read_b64_tr_b16 v[168:169], v6 offset:49152\n\tds_read_b64_tr_b16 v[170:171], v6 offset:49664\n\ts_waitcnt vmcnt(8)\n\t"
            "v_max3_u32 v12, v232, v234, v236\n\tv_max_u32 v12, v12, v238\n\tv_cmp_eq_u32 vcc, -1, v12\n\tv_mfma_f32_32x32x16_bf16 v[112:127], v[140:143], v[228:231], v[112:127]\n\tds_read_b64_tr_b16 v[172:173], v7 offset:49152\n\tds_read_b64_tr_b16 v[174:175], v7 offset:49664\n\t"
            "s_cbranch_vccnz 107f\n\t207:\ns_waitcnt lgkmcnt(8)\n\tv_mfma_f32_32x32x16_bf16 v[96:111], v[144:147], v[232:235], v[96:111]\n\tds_read_b64_tr_b16 v[128:129], v4 offset:50176\n\tds_read_b64_tr_b16 v[130:131], v4 offset:50688\n\t"
            "buffer_load_dwordx4 v[208:211], v2, s[44:47], s6 offen offset:0 sc1\n\tv_mfma_f32_32x32x16_bf16 v[112:127], v[148:151], v[232:235], v[112:127]\n\tds_read_b64_tr_b16 v[132:133], v5 offset:50176\n\tds_read_b64_tr_b16 v[134:135], v5 offset:50688\n\tbuffer_load_dwordx4 v[212:215], v2, s[44:47], s6 offen offset:3072 sc1\n\tv_mfma_f32_32x32x16_bf16 v[96:111], v[152:155], v[236:239], v[96:111]\n\t"
            "ds_read_b64_tr_b16 v[136:137], v6 offset:50176\n\tds_read_b64_tr_b16 v[138:139], v6 offset:50688\n\ts_waitcnt vmcnt(8)\n\tv_max3_u32 v12, v176, v178, v180\n\tv_max_u32 v12, v12, v182\n\tv_cmp_eq_u32 vcc, -1, v12\n\t"
            "v_mfma_f32_32x32x16_bf16 v[112:127], v[156:159], v[236:239], v[112:127]\n\tds_read_b64_tr_b16 v[140:141], v7 offset:50176\n\tds_read_b64_tr_b16 v[142:143], v7 offset:50688\n\ts_cbranch_vccnz 108f\n\t208:\ns_waitcnt lgkmcnt(8)\n\t"
            "v_mfma_f32_32x32x16_bf16 v[96:111], v[160:163], v[176:179], v[96:111]\n\tds_read_b64_tr_b16 v[144:145], v4 offset:0\n\tds_read_b64_tr_b16 v[146:147], v4 offset:512\n\tbuffer_load_dwordx4 v[216:219], v2, s[44:47], s7 offen offset:0 sc1\n\tv_mfma_f32_32x32x16_bf16 v[112:127], v[164:167], v[176:179], v[112:127]\n\tds_read_b64_tr_b16 v[148:149], v5 offset:0\n\t"
            "ds_read_b64_tr_b16 v[150:151], v5 offset:512\n\tbuffer_load_dwordx4 v[220:223], v2, s[44:47], s7 offen offset:3072 sc1\n\tv_mfma_f32_32x32x16_bf16 v[96:111], v[168:171], v[180:183], v[96:111]\n\tds_read_b64_tr_b16 v[152:153], v6 offset:0\n\tds_read_b64_tr_b16 v[154:155], v6 offset:512\n\ts_waitcnt vmcnt(8)\n\t"
            "v_max3_u32 v12, v184, v186, v188\n\tv_max_u32 v12, v12, v190\n\tv_cmp_eq_u32 vcc, -1, v12\n\tv_mfma_f32_32x32x16_bf16 v[112:127], v[172:175], v[180:183], v[112:127]\n\tds_read_b64_tr_b16 v[156:157], v7 offset:0\n\tds_read_b64_tr_b16 v[158:159], v7 offset:512\n\t"
            "s_cbranch_vccnz 109f\n\t209:\ns_waitcnt lgkmcnt(8)\n\tv_mfma_f32_32x32x16_bf16 v[96:111], v[128:131], v[184:187], v[96:111]\n\tds_read_b64_tr_b16 v[160:161], v4 offset:1024\n\tds_read_b64_tr_b16 v[162:163], v4 offset:1536\n\t"
            "buffer_load_dwordx4 v[224:227], v2, s[44:47], s8 offen offset:0 sc1\n\tv_mfma_f32_32x32x16_bf16 v[112:127], v[132:135], v[184:187], v[112:127]\n\tds_read_b64_tr_b16 v[164:165], v5 offset:1024\n\tds_read_b64_tr_b16 v[166:167], v5 offset:1536\n\tbuffer_load_dwordx4 v[228:231], v2, s[44:47], s8 offen offset:3072 sc1\n\tv_mfma_f32_32x32x16_bf16 v[96:111], v[136:139], v[188:191], v[96:111]\n\t"
            "ds_read_b64_tr_b16 v[168:169], v6 offset:1024\n\tds_read_b64_tr_b16 v[170:171], v6 offset:1536\n\ts_waitcnt vmcnt(8)\n\tv_max3_u32 v12, v192, v194, v196\n\tv_max_u32 v12, v12, v198\n\tv_cmp_eq_u32 vcc, -1, v12\n\t"
            "v_mfma_f32_32x32x16_bf16 v[112:127], v[140:143], v[188:191], v[112:127]\n\tds_read_b64_tr_b16 v[172:173], v7 offset:1024\n\tds_read_b64_tr_b16 v[174:175], v7 offset:1536\n\ts_cbranch_vccnz 110f\n\t210:\ns_waitcnt lgkmcnt(8)\n\t"
            "v_mfma_f32_32x32x16_bf16 v[96:111], v[144:147], v[192:195], v[96:111]\n\tds_read_b64_tr_b16 v[128:129], v4 offset:2048\n\tds_read_b64_tr_b16 v[130:131], v4 offset:2560\n\tbuffer_load_dwordx4 v[232:235], v2, s[44:47], s9 offen offset:0 sc1\n\tv_mfma_f32_32x32x16_bf16 v[112:127], v[148:151], v[192:195], v[112:127]\n\tds_read_b64_tr_b16 v[132:133], v5 offset:2048\n\t"
            "ds_read_b64_tr_b16 v[134:135], v5 offset:2560\n\tbuffer_load_dwordx4 v[236:239], v2, s[44:47], s9 offen offset:3072 sc1\n\tv_mfma_f32_32x32x16_bf16 v[96:111], v[152:155], v[196:199], v[96:111]\n\tds_read_b64_tr_b16 v[136:137], v6 offset:2048\n\tds_read_b64_tr_b16 v[138:139], v6 offset:2560\n\ts_waitcnt vmcnt(8)\n\t"
            "v_max3_u32 v12, v200, v202, v204\n\tv_max_u32 v12, v12, v206\n\tv_cmp_eq_u32 vcc, -1, v12\n\tv_mfma_f32_32x32x16_bf16 v[112:127], v[156:159], v[196:199], v[112:127]\n\tds_read_b64_tr_b16 v[140:141], v7 offset:2048\n\tds_read_b64_tr_b16 v[142:143], v7 offset:2560\n\t"
            "s_cbranch_vccnz 111f\n\t211:\ns_waitcnt lgkmcnt(8)\n\tv_mfma_f32_32x32x16_bf16 v[96:111], v[160:163], v[200:203], v[96:111]\n\tds_read_b64_tr_b16 v[144:145], v4 offset:3072\n\tds_read_b64_tr_b16 v[146:147], v4 offset:3584\n\t"
            "buffer_load_dwordx4 v[176:179], v2, s[44:47], s10 offen offset:0 sc1\n\tv_mfma_f32_32x32x16_bf16 v[112:127], v[164:167], v[200:203], v[112:127]\n\tds_read_b64_tr_b16 v[148:149], v5 offset:3072\n\tds_read_b64_tr_b16 v[150:151], v5 offset:3584\n\tbuffer_load_dwordx4 v[180:183], v2, s[44:47], s10 offen offset:3072 sc1\n\tv_mfma_f32_32x32x16_bf16 v[96:111], v[168:171], v[204:207], v[96:111]\n\t"
            "ds_read_b64_tr_b16 v[152:153], v6 offset:3072\n\tds_read_b64_tr_b16 v[154:155], v6 offset:3584\n\ts_waitcnt vmcnt(8)\n\tv_max3_u32 v12, v208, v210, v212\n\tv_max_u32 v12, v12, v214\n\tv_cmp_eq_u32 vcc, -1, v12\n\t"
            "v_mfma_f32_32x32x16_bf16 v[112:127], v[172:175], v[204:207], v[112:127]\n\tds_read_b64_tr_b16 v[156:157], v7 offset:3072\n\tds_read_b64_tr_b16 v[158:159], v7 offset:3584\n\ts_cbranch_vccnz 112f\n\t212:\ns_waitcnt lgkmcnt(8)\n\t"
            "v_mfma_f32_32x32x16_bf16 v[96:111], v[128:131], v[208:211], v[96:111]\n\tds_read_b64_tr_b16 v[160:161], v4 offset:4096\n\tds_read_b64_tr_b16 v[162:163], v4 offset:4608\n\tbuffer_load_dwordx4 v[184:187], v2, s[44:47], s11 offen offset:0 sc1\n\tv_mfma_f32_32x32x16_bf16 v[112:127], v[132:135], v[208:211], v[112:127]\n\tds_read_b64_tr_b16 v[164:165], v5 offset:4096\n\t"
            "ds_read_b64_tr_b16 v[166:167], v5 offset:4608\n\tbuffer_load_dwordx4 v[188:191], v2, s[44:47], s11 offen offset:3072 sc1\n\tv_mfma_f32_32x32x16_bf16 v[96:111], v[136:139], v[212:215], v[96:111]\n\tds_read_b64_tr_b16 v[168:169], v6 offset:4096\n\tds_read_b64_tr_b16 v[170:171], v6 offset:4608\n\ts_waitcnt vmcnt(8)\n\t"
            "v_max3_u32 v12, v216, v218, v220\n\tv_max_u32 v12, v12, v222\n\tv_cmp_eq_u32 vcc, -1, v12\n\tv_mfma_f32_32x32x16_bf16 v[112:127], v[140:143], v[212:215], v[112:127]\n\tds_read_b64_tr_b16 v[172:173], v7 offset:4096\n\tds_read_b64_tr_b16 v[174:175], v7 offset:4608\n\t"
            "s_cbranch_vccnz 113f\n\t213:\ns_waitcnt lgkmcnt(8)\n\tv_mfma_f32_32x32x16_bf16 v[96:111], v[144:147], v[216:219], v[96:111]\n\tds_read_b64_tr_b16 v[128:129], v4 offset:5120\n\tds_read_b64_tr_b16 v[130:131], v4 offset:5632\n\t"
            "buffer_load_dwordx4 v[192:195], v2, s[44:47], s12 offen offset:0 sc1\n\tv_mfma_f32_32x32x16_bf16 v[112:127], v[148:151], v[216:219], v[112:127]\n\tds_read_b64_tr_b16 v[132:133], v5 offset:5120\n\tds_read_b64_tr_b16 v[134:135], v5 offset:5632\n\tbuffer_load_dwordx4 v[196:199], v2, s[44:47], s12 offen offset:3072 sc1\n\tv_mfma_f32_32x32x16_bf16 v[96:111], v[152:155], v[220:223], v[96:111]\n\t"
            "ds_read_b64_tr_b16 v[136:137], v6 offset:5120\n\tds_read_b64_tr_b16 v[138:139], v6 offset:5632\n\ts_waitcnt vmcnt(8)\n\tv_max3_u32 v12, v224, v226, v228\n\tv_max_u32 v12, v12, v230\n\tv_cmp_eq_u32 vcc, -1, v12\n\t"
            "v_mfma_f32_32x32x16_bf16 v[112:127], v[156:159], v[220:223], v[112:127]\n\tds_read_b64_tr_b16 v[140:141], v7 offset:5120\n\tds_read_b64_tr_b16 v[142:143], v7 offset:5632\n\ts_cbranch_vccnz 114f\n\t214:\ns_waitcnt lgkmcnt(8)\n\t"
            "v_mfma_f32_32x32x16_bf16 v[96:111], v[160:163], v[224:227], v[96:111]\n\tds_read_b64_tr_b16 v[144:145], v4 offset:6144\n\tds_read_b64_tr_b16 v[146:147], v4 offset:6656\n\tbuffer_load_dwordx4 v[200:203], v2, s[44:47], s13 offen offset:0 sc1\n\tv_mfma_f32_32x32x16_bf16 v[112:127], v[164:167], v[224:227], v[112:127]\n\tds_read_b64_tr_b16 v[148:149], v5 offset:6144\n\t"
            "ds_read_b64_tr_b16 v[150:151], v5 offset:6656\n\tbuffer_load_dwordx4 v[204:207], v2, s[44:47], s13 offen offset:3072 sc1\n\tv_mfma_f32_32x32x16_bf16 v[96:111], v[168:171], v[228:231], v[96:111]\n\tds_read_b64_tr_b16 v[152:153], v6 offset:6144\n\tds_read_b64_tr_b16 v[154:155], v6 offset:6656\n\ts_waitcnt vmcnt(8)\n\t"
            "v_max3_u32 v12, v232, v234, v236\n\tv_max_u32 v12, v12, v238\n\tv_cmp_eq_u32 vcc, -1, v12\n\tv_mfma_f32_32x32x16_bf16 v[112:127], v[172:175], v[228:231], v[112:127]\n\tds_read_b64_tr_b16 v[156:157], v7 offset:6144\n\tds_read_b64_tr_b16 v[158:159], v7 offset:6656\n\t"
            "s_cbranch_vccnz 115f\n\t215:\ns_waitcnt lgkmcnt(8)\n\tv_mfma_f32_32x32x16_bf16 v[96:111], v[128:131], v[232:235], v[96:111]\n\tds_read_b64_tr_b16 v[160:161], v4 offset:7168\n\tds_read_b64_tr_b16 v[162:163], v4 offset:7680\n\t"
            "v_mfma_f32_32x32x16_bf16 v[112:127], v[132:135], v[232:235], v[112:127]\n\tds_read_b64_tr_b16 v[164:165], v5 offset:7168\n\tds_read_b64_tr_b16 v[166:167], v5 offset:7680\n\tv_mfma_f32_32x32x16_bf16 v[96:111], v[136:139], v[236:239], v[96:111]\n\tds_read_b64_tr_b16 v[168:169], v6 offset:7168\n\tds_read_b64_tr_b16 v[170:171], v6 offset:7680\n\t"
            "s_waitcnt vmcnt(6)\n\tv_max3_u32 v12, v176, v178, v180\n\tv_max_u32 v12, v12, v182\n\tv_cmp_eq_u32 vcc, -1, v12\n\tv_mfma_f32_32x32x16_bf16 v[112:127], v[140:143], v[236:239], v[112:127]\n\tds_read_b64_tr_b16 v[172:173], v7 offset:7168\n\t"
            "ds_read_b64_tr_b16 v[174:175], v7 offset:7680\n\ts_cbranch_vccnz 116f\n\t216:\ns_waitcnt lgkmcnt(8)\n\tv_mfma_f32_32x32x16_bf16 v[96:111], v[144:147], v[176:179], v[96:111]\n\tds_read_b64_tr_b16 v[128:129], v4 offset:8192\n\t"
            "ds_read_b64_tr_b16 v[130:131], v4 offset:8704\n\tv_mfma_f32_32x32x16_bf16 v[112:127], v[148:151], v[176:179], v[112:127]\n\tds_read_b64_tr_b16 v[132:133], v5 offset:8192\n\tds_read_b64_tr_b16 v[134:135], v5 offset:8704\n\tv_mfma_f32_32x32x16_bf16 v[96:111], v[152:155], v[180:183], v[96:111]\n\tds_read_b64_tr_b16 v[136:137], v6 offset:8192\n\t"
            "ds_read_b64_tr_b16 v[138:139], v6 offset:8704\n\ts_waitcnt vmcnt(4)\n\tv_max3_u32 v12, v184, v186, v188\n\tv_max_u32 v12, v12, v190\n\tv_cmp_eq_u32 vcc, -1, v12\n\tv_mfma_f32_32x32x16_bf16 v[112:127], v[156:159], v[180:183], v[112:127]\n\t"
            "ds_read_b64_tr_b16 v[140:141], v7 offset:8192\n\tds_read_b64_tr_b16 v[142:143], v7 offset:8704\n\ts_cbranch_vccnz 117f\n\t217:\nds_read_b32 v16, v8 offset:64\n\tds_read_b32 v17, v9 offset:1344\n\t"
            "s_waitcnt lgkmcnt(10)\n\tv_mfma_f32_32x32x16_bf16 v[96:111], v[160:163], v[184:187], v[96:111]\n\tds_read_b64_tr_b16 v[144:145], v4 offset:9216\n\tds_read_b64_tr_b16 v[146:147], v4 offset:9728\n\tds_read_b64_tr_b16 v[148:149], v5 offset:9216\n\tds_read_b64_tr_b16 v[150:151], v5 offset:9728\n\t"
            "ds_read_b64_tr_b16 v[152:153], v6 offset:9216\n\tds_read_b64_tr_b16 v[154:155], v6 offset:9728\n\ts_waitcnt vmcnt(2)\n\tv_max3_u32 v12, v192, v194, v196\n\tv_max_u32 v12, v12, v198\n\tv_cmp_eq_u32 vcc, -1, v12\n\t"
            "v_mfma_f32_32x32x16_bf16 v[96:111], v[168:171], v[188:191], v[96:111]\n\tds_read_b64_tr_b16 v[156:157], v7 offset:9216\n\tds_read_b64_tr_b16 v[158:159], v7 offset:9728\n\ts_cbranch_vccnz 118f\n\t218:\ns_waitcnt lgkmcnt(10)\n\t"
            "v_mfma_f32_32x32x16_bf16 v[96:111], v[128:131], v[192:195], v[96:111]\n\ts_waitcnt vmcnt(0)\n\tv_max3_u32 v12, v200, v202, v204\n\tv_max_u32 v12, v12, v206\n\tv_cmp_eq_u32 vcc, -1, v12\n\tv_mfma_f32_32x32x16_bf16 v[96:111], v[136:139], v[196:199], v[96:111]\n\t"
            "s_cbranch_vccnz 119f\n\t219:\ns_waitcnt lgkmcnt(0)\n\tv_mfma_f32_32x32x16_bf16 v[96:111], v[144:147], v[200:203], v[96:111]\n\tv_cmp_ne_u32 s[74:75], 0, v17\n\tv_cmp_eq_u32 vcc, 0, v16\n\t"
            "v_mfma_f32_32x32x16_bf16 v[96:111], v[152:155], v[204:207], v[96:111]\n\ts_or_b64 vcc, vcc, s[74:75]\n\tv_mfma_f32_32x32x16_bf16 v[112:127], v[164:167], v[184:187], v[112:127]\n\ts_cbranch_scc1 7f\n\t8:\ns_nop 5\n\t"
            "v_mfma_f32_32x32x16_bf16 v[112:127], v[172:175], v[188:191], v[112:127]\n\ts_nop 1\n\tv_fmac_f32 v64, v96, v32\n\tv_fmac_f32 v65, v97, v33\n\tv_fmac_f32 v66, v98, v34\n\tv_fmac_f32 v67, v99, v35\n\t"
            "v_fmac_f32 v68, v100, v36\n\tv_fmac_f32 v69, v101, v37\n\tv_fmac_f32 v70, v102, v38\n\tv_mfma_f32_32x32x16_bf16 v[112:127], v[132:135], v[192:195], v[112:127]\n\tv_fmac_f32 v71, v103, v39\n\tv_fmac_f32 v72, v104, v40\n\t"
            "v_fmac_f32 v73, v105, v41\n\tv_fmac_f32 v74, v106, v42\n\tv_fmac_f32 v75, v107, v43\n\tv_fmac_f32 v76, v108, v44\n\tv_fmac_f32 v77, v109, v45\n\tv_mfma_f32_32x32x16_bf16 v[112:127], v[140:143], v[196:199], v[112:127]\n\t"
            "v_fmac_f32 v78, v110, v46\n\tv_fmac_f32 v79, v111, v47\n\tv_cvt_pk_bf16_f32 v240, v64, v65\n\tv_cvt_pk_bf16_f32 v242, v68, v69\n\tv_cvt_pk_bf16_f32 v241, v66, v67\n\tv_cvt_pk_bf16_f32 v243, v70, v71\n\t"
            "v_cvt_pk_bf16_f32 v244, v72, v73\n\tv_mfma_f32_32x32x16_bf16 v[112:127], v[148:151], v[200:203], v[112:127]\n\tv_cvt_pk_bf16_f32 v246, v76, v77\n\tv_cvt_pk_bf16_f32 v245, v74, v75\n\tv_cvt_pk_bf16_f32 v247, v78, v79\n\tv_permlane32_swap_b32 v240, v242\n\t"
            "v_permlane32_swap_b32 v241, v243\n\tv_permlane32_swap_b32 v244, v246\n\tv_permlane32_swap_b32 v245, v247\n\tv_mfma_f32_32x32x16_bf16 v[112:127], v[156:159], v[204:207], v[112:127]\n\tv_permlane16_swap_b32 v240, v244\n\tv_permlane16_swap_b32 v242, v246\n\t"
            "v_permlane16_swap_b32 v241, v245\n\tv_permlane16_swap_b32 v243, v247\n\tbuffer_store_dwordx4 v[240:243], v3, s[52:55], 0 offen offset:0 sc1\n\tbuffer_store_dwordx4 v[244:247], v11, s[52:55], 0 offen offset:0 sc1\n\t"
            "s_cmp_lg_u32 s74, 0\n\ts_cbranch_scc1 73f\n\t74:\ns_nop 3\n\tv_fmac_f32 v80, v112, v48\n\tv_fmac_f32 v81, v113, v49\n\t"
            "v_fmac_f32 v82, v114, v50\n\tv_fmac_f32 v83, v115, v51\n\tv_fmac_f32 v84, v116, v52\n\tv_fmac_f32 v85, v117, v53\n\tv_fmac_f32 v86, v118, v54\n\tv_fmac_f32 v87, v119, v55\n\t"
            "v_fmac_f32 v88, v120, v56\n\tv_fmac_f32 v89, v121, v57\n\tv_fmac_f32 v90, v122, v58\n\tv_fmac_f32 v91, v123, v59\n\tv_fmac_f32 v92, v124, v60\n\tv_fmac_f32 v93, v125, v61\n\t"
            "v_fmac_f32 v94, v126, v62\n\tv_fmac_f32 v95, v127, v63\n\tv_cvt_pk_bf16_f32 v248, v80, v81\n\tv_cvt_pk_bf16_f32 v250, v84, v85\n\tv_cvt_pk_bf16_f32 v249, v82, v83\n\tv_cvt_pk_bf16_f32 v251, v86, v87\n\t"
            "v_cvt_pk_bf16_f32 v252, v88, v89\n\tv_cvt_pk_bf16_f32 v254, v92, v93\n\tv_cvt_pk_bf16_f32 v253, v90, v91\n\tv_cvt_pk_bf16_f32 v255, v94, v95\n\tv_permlane32_swap_b32 v248, v250\n\tv_permlane32_swap_b32 v249, v251\n\t"
            "v_permlane32_swap_b32 v252, v254\n\tv_permlane32_swap_b32 v253, v255\n\ts_nop 0\n\tv_permlane16_swap_b32 v248, v252\n\tv_permlane16_swap_b32 v250, v254\n\tv_permlane16_swap_b32 v249, v253\n\t"
            "v_permlane16_swap_b32 v251, v255\n\tbuffer_store_dwordx4 v[248:251], v3, s[52:55], 0 offen offset:64 sc1\n\tbuffer_store_dwordx4 v[252:255], v11, s[52:55], 0 offen offset:64 sc1\n\t9:\n"
            "s_endpgm\n\t.LG0:\nv_mul_f32 v21, 0xbfb8aa3b, v21\n\tv_mul_f32 v20, v20, v21\n\tv_permlane16_swap_b32 v240, v244\n\tv_permlane16_swap_b32 v241, v245\n\t"
            "v_permlane16_swap_b32 v242, v246\n\tv_permlane16_swap_b32 v243, v247\n\tv_permlane16_swap_b32 v248, v252\n\tv_permlane16_swap_b32 v249, v253\n\tv_permlane16_swap_b32 v250, v254\n\tv_permlane16_swap_b32 v251, v255\n\t"
            "v_permlane32_swap_b32 v240, v242\n\tv_permlane32_swap_b32 v241, v243\n\tv_permlane32_swap_b32 v244, v246\n\tv_permlane32_swap_b32 v245, v247\n\tv_permlane32_swap_b32 v248, v250\n\tv_permlane32_swap_b32 v249, v251\n\t"
            "v_permlane32_swap_b32 v252, v254\n\tv_permlane32_swap_b32 v253, v255\n\tv_lshlrev_b32 v64, 16, v240\n\tv_and_b32 v65, 0xffff0000, v240\n\tv_lshlrev_b32 v66, 16, v241\n\tv_and_b32 v67, 0xffff0000, v241\n\t"
            "v_lshlrev_b32 v68, 16, v242\n\tv_and_b32 v69, 0xffff0000, v242\n\tv_lshlrev_b32 v70, 16, v243\n\tv_and_b32 v71, 0xffff0000, v243\n\tv_lshlrev_b32 v72, 16, v244\n\tv_and_b32 v73, 0xffff0000, v244\n\t"
            "v_lshlrev_b32 v74, 16, v245\n\tv_and_b32 v75, 0xffff0000, v245\n\tv_lshlrev_b32 v76, 16, v246\n\tv_and_b32 v77, 0xffff0000, v246\n\tv_lshlrev_b32 v78, 16, v247\n\tv_and_b32 v79, 0xffff0000, v247\n\t"
            "v_lshlrev_b32 v80, 16, v248\n\tv_and_b32 v81, 0xffff0000, v248\n\tv_lshlrev_b32 v82, 16, v249\n\tv_and_b32 v83, 0xffff0000, v249\n\tv_lshlrev_b32 v84, 16, v250\n\tv_and_b32 v85, 0xffff0000, v250\n\t"
            "v_lshlrev_b32 v86, 16, v251\n\tv_and_b32 v87, 0xffff0000, v251\n\tv_lshlrev_b32 v88, 16, v252\n\tv_and_b32 v89, 0xffff0000, v252\n\tv_lshlrev_b32 v90, 16, v253\n\tv_and_b32 v91, 0xffff0000, v253\n\t"
            "v_lshlrev_b32 v92, 16, v254\n\tv_and_b32 v93, 0xffff0000, v254\n\tv_lshlrev_b32 v94, 16, v255\n\tv_and_b32 v95, 0xffff0000, v255\n\tv_fma_f32 v96, v64, v21, v20\n\tv_fma_f32 v97, v65, v21, v20\n\t"
            "v_fma_f32 v98, v66, v21, v20\n\tv_fma_f32 v99, v67, v21, v20\n\tv_fma_f32 v100, v68, v21, v20\n\tv_fma_f32 v101, v69, v21, v20\n\tv_fma_f32 v102, v70, v21, v20\n\tv_fma_f32 v103, v71, v21, v20\n\t"
            "v_fma_f32 v104, v72, v21, v20\n\tv_fma_f32 v105, v73, v21, v20\n\tv_fma_f32 v106, v74, v21, v20\n\tv_fma_f32 v107, v75, v21, v20\n\tv_fma_f32 v108, v76, v21, v20\n\tv_fma_f32 v109, v77, v21, v20\n\t"
            "v_fma_f32 v110, v78, v21, v20\n\tv_fma_f32 v111, v79, v21, v20\n\tv_fma_f32 v112, v80, v21, v20\n\tv_fma_f32 v113, v81, v21, v20\n\tv_fma_f32 v114, v82, v21, v20\n\tv_fma_f32 v115, v83, v21, v20\n\t"
            "v_fma_f32 v116, v84, v21, v20\n\tv_fma_f32 v117, v85, v21, v20\n\tv_fma_f32 v118, v86, v21, v20\n\tv_fma_f32 v119, v87, v21, v20\n\tv_fma_f32 v120, v88, v21, v20\n\tv_fma_f32 v121, v89, v21, v20\n\t"
            "v_fma_f32 v122, v90, v21, v20\n\tv_fma_f32 v123, v91, v21, v20\n\tv_fma_f32 v124, v92, v21, v20\n\tv_fma_f32 v125, v93, v21, v20\n\tv_fma_f32 v126, v94, v21, v20\n\tv_fma_f32 v127, v95, v21, v20\n\t"
            "s_setpc_b64 s[76:77]\n\t.LG1:\nv_exp_f32 v32, v96\n\tv_exp_f32 v33, v97\n\tv_exp_f32 v34, v98\n\tv_exp_f32 v35, v99\n\t"
            "v_exp_f32 v36, v100\n\tv_exp_f32 v37, v101\n\tv_exp_f32 v38, v102\n\tv_exp_f32 v39, v103\n\tv_exp_f32 v40, v104\n\tv_exp_f32 v41, v105\n\t"
            "v_exp_f32 v42, v106\n\tv_exp_f32 v43, v107\n\tv_exp_f32 v44, v108\n\tv_exp_f32 v45, v109\n\tv_exp_f32 v46, v110\n\tv_exp_f32 v47, v111\n\t"
            "v_exp_f32 v48, v112\n\tv_exp_f32 v49, v113\n\tv_exp_f32 v50, v114\n\tv_exp_f32 v51, v115\n\tv_exp_f32 v52, v116\n\tv_exp_f32 v53, v117\n\t"
            "v_exp_f32 v54, v118\n\tv_exp_f32 v55, v119\n\tv_exp_f32 v56, v120\n\tv_exp_f32 v57, v121\n\tv_exp_f32 v58, v122\n\tv_exp_f32 v59, v123\n\t"
            "v_exp_f32 v60, v124\n\tv_exp_f32 v61, v125\n\tv_exp_f32 v62, v126\n\tv_exp_f32 v63, v127\n\tv_add_f32 v32, 1.0, v32\n\tv_add_f32 v33, 1.0, v33\n\t"
            "v_add_f32 v34, 1.0, v34\n\tv_add_f32 v35, 1.0, v35\n\tv_add_f32 v36, 1.0, v36\n\tv_add_f32 v37, 1.0, v37\n\tv_add_f32 v38, 1.0, v38\n\tv_add_f32 v39, 1.0, v39\n\t"
            "v_add_f32 v40, 1.0, v40\n\tv_add_f32 v41, 1.0, v41\n\tv_add_f32 v42, 1.0, v42\n\tv_add_f32 v43, 1.0, v43\n\tv_add_f32 v44, 1.0, v44\n\tv_add_f32 v45, 1.0, v45\n\t"
            "v_add_f32 v46, 1.0, v46\n\tv_add_f32 v47, 1.0, v47\n\tv_add_f32 v48, 1.0, v48\n\tv_add_f32 v49, 1.0, v49\n\tv_add_f32 v50, 1.0, v50\n\tv_add_f32 v51, 1.0, v51\n\t"
            "v_add_f32 v52, 1.0, v52\n\tv_add_f32 v53, 1.0, v53\n\tv_add_f32 v54, 1.0, v54\n\tv_add_f32 v55, 1.0, v55\n\tv_add_f32 v56, 1.0, v56\n\tv_add_f32 v57, 1.0, v57\n\t"
            "s_mov_b32 s83, 1\n\ts_setpc_b64 s[76:77]\n\t.LG2:\nv_add_f32 v58, 1.0, v58\n\tv_add_f32 v59, 1.0, v59\n\tv_add_f32 v60, 1.0, v60\n\t"
            "v_add_f32 v61, 1.0, v61\n\tv_add_f32 v62, 1.0, v62\n\tv_add_f32 v63, 1.0, v63\n\tv_rcp_f32 v32, v32\n\tv_rcp_f32 v33, v33\n\tv_rcp_f32 v34, v34\n\t"
            "v_rcp_f32 v35, v35\n\tv_rcp_f32 v36, v36\n\tv_rcp_f32 v37, v37\n\tv_rcp_f32 v38, v38\n\tv_rcp_f32 v39, v39\n\tv_rcp_f32 v40, v40\n\t"
            "v_rcp_f32 v41, v41\n\tv_rcp_f32 v42, v42\n\tv_rcp_f32 v43, v43\n\tv_rcp_f32 v44, v44\n\tv_rcp_f32 v45, v45\n\tv_rcp_f32 v46, v46\n\t"
            "v_rcp_f32 v47, v47\n\tv_rcp_f32 v48, v48\n\tv_rcp_f32 v49, v49\n\tv_rcp_f32 v50, v50\n\tv_rcp_f32 v51, v51\n\tv_rcp_f32 v52, v52\n\t"
            "v_rcp_f32 v53, v53\n\tv_rcp_f32 v54, v54\n\tv_rcp_f32 v55, v55\n\tv_rcp_f32 v56, v56\n\tv_rcp_f32 v57, v57\n\tv_rcp_f32 v58, v58\n\t"
            "v_rcp_f32 v59, v59\n\tv_rcp_f32 v60, v60\n\tv_rcp_f32 v61, v61\n\tv_rcp_f32 v62, v62\n\tv_rcp_f32 v63, v63\n\tv_mul_f32 v32, v32, v96\n\t"
            "v_mul_f32 v33, v33, v97\n\tv_mul_f32 v34, v34, v98\n\tv_mul_f32 v35, v35, v99\n\tv_mul_f32 v36, v36, v100\n\tv_mul_f32 v37, v37, v101\n\tv_mul_f32 v38, v38, v102\n\t"
            "v_mul_f32 v39, v39, v103\n\tv_mul_f32 v40, v40, v104\n\tv_mul_f32 v41, v41, v105\n\tv_mul_f32 v42, v42, v106\n\tv_mul_f32 v43, v43, v107\n\tv_mul_f32 v44, v44, v108\n\t"
            "v_mul_f32 v45, v45, v109\n\tv_mul_f32 v46, v46, v110\n\tv_mul_f32 v47, v47, v111\n\ts_mov_b32 s83, 2\n\ts_setpc_b64 s[76:77]\n\t7:\n"
            "ds_read_b32 v16, v8 offset:64\n\tds_read_b32 v17, v9 offset:1344\n\ts_waitcnt lgkmcnt(0)\n\tv_cmp_ne_u32 s[74:75], 0, v17\n\tv_cmp_eq_u32 vcc, 0, v16\n\ts_cbranch_vccz 71f\n\t"
            "s_sleep 1\n\ts_sub_u32 s68, s68, 1\n\ts_cbranch_scc0 7b\n\ts_branch 9b\n\t71:\nv_lshrrev_b32 v12, 5, v2\n\t"
            "v_and_b32 v12, 16, v12\n\tv_add_u32 v12, v12, v17\n\ts_pack_ll_b32_b16 s78, s74, s74\n\ts_mov_b32 s79, s78\n\ts_pack_hh_b32_b16 s80, s74, s74\n\ts_mov_b32 s81, s80\n\t"
            "v_cndmask_b32 v3, v3, 2.0, s[78:79]\n\tv_cndmask_b32 v11, v11, 2.0, s[80:81]\n\ts_mov_b64 exec, s[74:75]\n\ts_cbranch_execz 72f\n\tds_write_b128 v12, v[96:99] offset:0\n\tds_write_b128 v12, v[100:103] offset:32\n\t"
            "ds_write_b128 v12, v[104:107] offset:64\n\tds_write_b128 v12, v[108:111] offset:96\n\t72:\ns_mov_b64 exec, -1\n\ts_branch 8b\n\t73:\n"
            "s_nop 7\n\ts_nop 3\n\ts_mov_b64 exec, s[74:75]\n\tds_write_b128 v12, v[112:115] offset:128\n\tds_write_b128 v12, v[116:119] offset:160\n\tds_write_b128 v12, v[120:123] offset:192\n\t"
            "ds_write_b128 v12, v[124:127] offset:224\n\tds_write_b32 v17, v17 offset:256\n\ts_mov_b64 exec, -1\n\ts_branch 74b\n\t5:\ns_sleep 1\n\t"
            "ds_read_b64 v[14:15], v8 offset:40\n\ts_waitcnt lgkmcnt(0)\n\tv_min_u32 v14, v14, v15\n\tv_cmp_ge_u32 vcc, s71, v14\n\ts_cbranch_vccz 61f\n\ts_sub_u32 s68, s68, 1\n\t"
            "s_cbranch_scc0 5b\n\ts_branch 9b\n\t61:\n"
            "s_branch 6b\n\t62:\nds_read_b128 v[16:19], v8 offset:48\n\tds_read_b64_tr_b16 v[128:129], v4 offset:40960\n\tds_read_b64_tr_b16 v[130:131], v4 offset:41472\n\tds_read_b64_tr_b16 v[132:133], v5 offset:40960\n\t"
            "ds_read_b64_tr_b16 v[134:135], v5 offset:41472\n\tds_read_b64_tr_b16 v[136:137], v6 offset:40960\n\tds_read_b64_tr_b16 v[138:139], v6 offset:41472\n\tds_read_b64_tr_b16 v[140:141], v7 offset:40960\n\tds_read_b64_tr_b16 v[142:143], v7 offset:41472\n\tds_read_b64_tr_b16 v[144:145], v4 offset:41984\n\t"
            "ds_read_b64_tr_b16 v[146:147], v4 offset:42496\n\tds_read_b64_tr_b16 v[148:149], v5 offset:41984\n\tds_read_b64_tr_b16 v[150:151], v5 offset:42496\n\tds_read_b64_tr_b16 v[152:153], v6 offset:41984\n\tds_read_b64_tr_b16 v[154:155], v6 offset:42496\n\tds_read_b64_tr_b16 v[156:157], v7 offset:41984\n\t"
            "ds_read_b64_tr_b16 v[158:159], v7 offset:42496\n\ts_waitcnt lgkmcnt(15)\n\tv_min3_u32 v16, v16, v17, v18\n\tv_min_u32 v16, v16, v19\n\tv_cmp_eq_u32 vcc, 0, v16\n\ts_cbranch_vccz 63b\n\t"
            "s_sub_u32 s68, s68, 1\n\ts_cbranch_scc0 62b\n\ts_branch 9b\n\t100:\ns_mov_b32 s70, 4\n\ts_call_b64 s[72:73], 300f\n\t"
            "v_max3_u32 v12, v176, v178, v180\n\tv_max_u32 v12, v12, v182\n\tv_cmp_eq_u32 vcc, -1, v12\n\ts_cbranch_vccnz 100b\n\ts_branch 200b\n\t101:\n"
            "s_mov_b32 s70, 5\n\ts_call_b64 s[72:73], 301f\n\tv_max3_u32 v12, v184, v186, v188\n\tv_max_u32 v12, v12, v190\n\tv_cmp_eq_u32 vcc, -1, v12\n\ts_cbranch_vccnz 101b\n\t"
            "s_branch 201b\n\t102:\ns_mov_b32 s70, 6\n\ts_call_b64 s[72:73], 302f\n\tv_max3_u32 v12, v192, v194, v196\n\tv_max_u32 v12, v12, v198\n\t"
            "v_cmp_eq_u32 vcc, -1, v12\n\ts_cbranch_vccnz 102b\n\ts_branch 202b\n\t103:\ns_mov_b32 s70, 7\n\ts_call_b64 s[72:73], 303f\n\t"
            "v_max3_u32 v12, v200, v202, v204\n\tv_max_u32 v12, v12, v206\n\tv_cmp_eq_u32 vcc, -1, v12\n\ts_cbranch_vccnz 103b\n\ts_branch 203b\n\t104:\n"
            "s_mov_b32 s70, 8\n\ts_call_b64 s[72:73], 304f\n\tv_max3_u32 v12, v208, v210, v212\n\tv_max_u32 v12, v12, v214\n\tv_cmp_eq_u32 vcc, -1, v12\n\ts_cbranch_vccnz 104b\n\t"
            "s_branch 204b\n\t105:\ns_mov_b32 s70, 9\n\ts_call_b64 s[72:73], 305f\n\tv_max3_u32 v12, v216, v218, v220\n\tv_max_u32 v12, v12, v222\n\t"
            "v_cmp_eq_u32 vcc, -1, v12\n\ts_cbranch_vccnz 105b\n\ts_branch 205b\n\t106:\ns_mov_b32 s70, 10\n\ts_call_b64 s[72:73], 306f\n\t"
            "v_max3_u32 v12, v224, v226, v228\n\tv_max_u32 v12, v12, v230\n\tv_cmp_eq_u32 vcc, -1, v12\n\ts_cbranch_vccnz 106b\n\ts_branch 206b\n\t107:\n"
            "s_mov_b32 s70, 11\n\ts_call_b64 s[72:73], 307f\n\tv_max3_u32 v12, v232, v234, v236\n\tv_max_u32 v12, v12, v238\n\tv_cmp_eq_u32 vcc, -1, v12\n\ts_cbranch_vccnz 107b\n\t"
            "s_branch 207b\n\t108:\ns_mov_b32 s70, 12\n\ts_call_b64 s[72:73], 308f\n\tv_max3_u32 v12, v176, v178, v180\n\tv_max_u32 v12, v12, v182\n\t"
            "v_cmp_eq_u32 vcc, -1, v12\n\ts_cbranch_vccnz 108b\n\ts_branch 208b\n\t109:\ns_mov_b32 s70, 13\n\ts_call_b64 s[72:73], 309f\n\t"
            "v_max3_u32 v12, v184, v186, v188\n\tv_max_u32 v12, v12, v190\n\tv_cmp_eq_u32 vcc, -1, v12\n\ts_cbranch_vccnz 109b\n\ts_branch 209b\n\t110:\n"
            "s_mov_b32 s70, 14\n\ts_call_b64 s[72:73], 310f\n\tv_max3_u32 v12, v192, v194, v196\n\tv_max_u32 v12, v12, v198\n\tv_cmp_eq_u32 vcc, -1, v12\n\ts_cbranch_vccnz 110b\n\t"
            "s_branch 210b\n\t111:\ns_mov_b32 s70, 15\n\ts_call_b64 s[72:73], 311f\n\tv_max3_u32 v12, v200, v202, v204\n\tv_max_u32 v12, v12, v206\n\t"
            "v_cmp_eq_u32 vcc, -1, v12\n\ts_cbranch_vccnz 111b\n\ts_branch 211b\n\t112:\ns_mov_b32 s70, 16\n\ts_call_b64 s[72:73], 312f\n\t"
            "v_max3_u32 v12, v208, v210, v212\n\tv_max_u32 v12, v12, v214\n\tv_cmp_eq_u32 vcc, -1, v12\n\ts_cbranch_vccnz 112b\n\ts_branch 212b\n\t113:\n"
            "s_mov_b32 s70, 17\n\ts_call_b64 s[72:73], 313f\n\tv_max3_u32 v12, v216, v218, v220\n\tv_max_u32 v12, v12, v222\n\tv_cmp_eq_u32 vcc, -1, v12\n\ts_cbranch_vccnz 113b\n\t"
            "s_branch 213b\n\t114:\ns_mov_b32 s70, 18\n\ts_call_b64 s[72:73], 314f\n\tv_max3_u32 v12, v224, v226, v228\n\tv_max_u32 v12, v12, v230\n\t"
            "v_cmp_eq_u32 vcc, -1, v12\n\ts_cbranch_vccnz 114b\n\ts_branch 214b\n\t115:\ns_mov_b32 s70, 19\n\ts_call_b64 s[72:73], 315f\n\t"
            "v_max3_u32 v12, v232, v234, v236\n\tv_max_u32 v12, v12, v238\n\tv_cmp_eq_u32 vcc, -1, v12\n\ts_cbranch_vccnz 115b\n\ts_branch 215b\n\t116:\n"
            "s_mov_b32 s70, 19\n\ts_call_b64 s[72:73], 316f\n\tv_max3_u32 v12, v176, v178, v180\n\tv_max_u32 v12, v12, v182\n\tv_cmp_eq_u32 vcc, -1, v12\n\ts_cbranch_vccnz 116b\n\t"
            "s_branch 216b\n\t117:\ns_mov_b32 s70, 19\n\ts_call_b64 s[72:73], 317f\n\tv_max3_u32 v12, v184, v186, v188\n\tv_max_u32 v12, v12, v190\n\t"
            "v_cmp_eq_u32 vcc, -1, v12\n\ts_cbranch_vccnz 117b\n\ts_branch 217b\n\t118:\ns_mov_b32 s70, 19\n\ts_call_b64 s[72:73], 318f\n\t"
            "v_max3_u32 v12, v192, v194, v196\n\tv_max_u32 v12, v12, v198\n\tv_cmp_eq_u32 vcc, -1, v12\n\ts_cbranch_vccnz 118b\n\ts_branch 218b\n\t119:\n"
            "s_mov_b32 s70, 19\n\ts_call_b64 s[72:73], 319f\n\tv_max3_u32 v12, v200, v202, v204\n\tv_max_u32 v12, v12, v206\n\tv_cmp_eq_u32 vcc, -1, v12\n\ts_cbranch_vccnz 119b\n\t"
            "s_branch 219b\n\t400:\nv_max3_u32 v12, v176, v178, v180\n\tv_max_u32 v12, v12, v182\n\tv_cmp_eq_u32 vcc, -1, v12\n\ts_cbranch_vccz 11f\n\t"
            "buffer_load_dwordx4 v[176:179], v2, s[44:47], s4 offen offset:1024 sc1\n\tbuffer_load_dwordx4 v[180:183], v2, s[44:47], s4 offen offset:2048 sc1\n\ts_branch 11f\n\t300:\nbuffer_load_dwordx4 v[176:179], v2, s[44:47], s4 offen offset:1024 sc1\n\tbuffer_load_dwordx4 v[180:183], v2, s[44:47], s4 offen offset:2048 sc1\n\t"
            "s_waitcnt vmcnt(2)\n\t11:\ns_cmp_eq_u32 s70, 0\n\ts_cbranch_scc0 401f\n\ts_branch 12f\n\t401:\n"
            "v_max3_u32 v12, v184, v186, v188\n\tv_max_u32 v12, v12, v190\n\tv_cmp_eq_u32 vcc, -1, v12\n\ts_cbranch_vccz 11f\n\tbuffer_load_dwordx4 v[184:187], v2, s[44:47], s5 offen offset:1024 sc1\n\tbuffer_load_dwordx4 v[188:191], v2, s[44:47], s5 offen offset:2048 sc1\n\t"
            "s_branch 11f\n\t301:\nbuffer_load_dwordx4 v[184:187], v2, s[44:47], s5 offen offset:1024 sc1\n\tbuffer_load_dwordx4 v[188:191], v2, s[44:47], s5 offen offset:2048 sc1\n\ts_waitcnt vmcnt(2)\n\t11:\n"
            "s_cmp_eq_u32 s70, 1\n\ts_cbranch_scc0 402f\n\ts_branch 12f\n\t402:\nv_max3_u32 v12, v192, v194, v196\n\tv_max_u32 v12, v12, v198\n\t"
            "v_cmp_eq_u32 vcc, -1, v12\n\ts_cbranch_vccz 11f\n\tbuffer_load_dwordx4 v[192:195], v2, s[44:47], s6 offen offset:1024 sc1\n\tbuffer_load_dwordx4 v[196:199], v2, s[44:47], s6 offen offset:2048 sc1\n\ts_branch 11f\n\t302:\n"
            "buffer_load_dwordx4 v[192:195], v2, s[44:47], s6 offen offset:1024 sc1\n\tbuffer_load_dwordx4 v[196:199], v2, s[44:47], s6 offen offset:2048 sc1\n\ts_waitcnt vmcnt(2)\n\t11:\ns_cmp_eq_u32 s70, 2\n\ts_cbranch_scc0 403f\n\t"
            "s_branch 12f\n\t403:\nv_max3_u32 v12, v200, v202, v204\n\tv_max_u32 v12, v12, v206\n\tv_cmp_eq_u32 vcc, -1, v12\n\ts_cbranch_vccz 11f\n\t"
            "buffer_load_dwordx4 v[200:203], v2, s[44:47], s7 offen offset:1024 sc1\n\tbuffer_load_dwordx4 v[204:207], v2, s[44:47], s7 offen offset:2048 sc1\n\ts_branch 11f\n\t303:\nbuffer_load_dwordx4 v[200:203], v2, s[44:47], s7 offen offset:1024 sc1\n\tbuffer_load_dwordx4 v[204:207], v2, s[44:47], s7 offen offset:2048 sc1\n\t"
            "s_waitcnt vmcnt(2)\n\t11:\ns_cmp_eq_u32 s70, 3\n\ts_cbranch_scc0 404f\n\ts_branch 12f\n\t404:\n"
            "v_max3_u32 v12, v208, v210, v212\n\tv_max_u32 v12, v12, v214\n\tv_cmp_eq_u32 vcc, -1, v12\n\ts_cbranch_vccz 11f\n\tbuffer_load_dwordx4 v[208:211], v2, s[44:47], s8 offen offset:1024 sc1\n\tbuffer_load_dwordx4 v[212:215], v2, s[44:47], s8 offen offset:2048 sc1\n\t"
            "s_branch 11f\n\t304:\nbuffer_load_dwordx4 v[208:211], v2, s[44:47], s8 offen offset:1024 sc1\n\tbuffer_load_dwordx4 v[212:215], v2, s[44:47], s8 offen offset:2048 sc1\n\ts_waitcnt vmcnt(2)\n\t11:\n"
            "s_cmp_eq_u32 s70, 4\n\ts_cbranch_scc0 405f\n\ts_branch 12f\n\t405:\nv_max3_u32 v12, v216, v218, v220\n\tv_max_u32 v12, v12, v222\n\t"
            "v_cmp_eq_u32 vcc, -1, v12\n\ts_cbranch_vccz 11f\n\tbuffer_load_dwordx4 v[216:219], v2, s[44:47], s9 offen offset:1024 sc1\n\tbuffer_load_dwordx4 v[220:223], v2, s[44:47], s9 offen offset:2048 sc1\n\ts_branch 11f\n\t305:\n"
            "buffer_load_dwordx4 v[216:219], v2, s[44:47], s9 offen offset:1024 sc1\n\tbuffer_load_dwordx4 v[220:223], v2, s[44:47], s9 offen offset:2048 sc1\n\ts_waitcnt vmcnt(2)\n\t11:\ns_cmp_eq_u32 s70, 5\n\ts_cbranch_scc0 406f\n\t"
            "s_branch 12f\n\t406:\nv_max3_u32 v12, v224, v226, v228\n\tv_max_u32 v12, v12, v230\n\tv_cmp_eq_u32 vcc, -1, v12\n\ts_cbranch_vccz 11f\n\t"
            "buffer_load_dwordx4 v[224:227], v2, s[44:47], s10 offen offset:1024 sc1\n\tbuffer_load_dwordx4 v[228:231], v2, s[44:47], s10 offen offset:2048 sc1\n\ts_branch 11f\n\t306:\nbuffer_load_dwordx4 v[224:227], v2, s[44:47], s10 offen offset:1024 sc1\n\tbuffer_load_dwordx4 v[228:231], v2, s[44:47], s10 offen offset:2048 sc1\n\t"
            "s_waitcnt vmcnt(2)\n\t11:\ns_cmp_eq_u32 s70, 6\n\ts_cbranch_scc0 407f\n\ts_branch 12f\n\t407:\n"
            "v_max3_u32 v12, v232, v234, v236\n\tv_max_u32 v12, v12, v238\n\tv_cmp_eq_u32 vcc, -1, v12\n\ts_cbranch_vccz 11f\n\tbuffer_load_dwordx4 v[232:235], v2, s[44:47], s11 offen offset:1024 sc1\n\tbuffer_load_dwordx4 v[236:239], v2, s[44:47], s11 offen offset:2048 sc1\n\t"
            "s_branch 11f\n\t307:\nbuffer_load_dwordx4 v[232:235], v2, s[44:47], s11 offen offset:1024 sc1\n\tbuffer_load_dwordx4 v[236:239], v2, s[44:47], s11 offen offset:2048 sc1\n\ts_waitcnt vmcnt(2)\n\t11:\n"
            "s_cmp_eq_u32 s70, 7\n\ts_cbranch_scc0 408f\n\ts_branch 12f\n\t408:\nv_max3_u32 v12, v176, v178, v180\n\tv_max_u32 v12, v12, v182\n\t"
            "v_cmp_eq_u32 vcc, -1, v12\n\ts_cbranch_vccz 11f\n\tbuffer_load_dwordx4 v[176:179], v2, s[44:47], s12 offen offset:1024 sc1\n\tbuffer_load_dwordx4 v[180:183], v2, s[44:47], s12 offen offset:2048 sc1\n\ts_branch 11f\n\t308:\n"
            "buffer_load_dwordx4 v[176:179], v2, s[44:47], s12 offen offset:1024 sc1\n\tbuffer_load_dwordx4 v[180:183], v2, s[44:47], s12 offen offset:2048 sc1\n\ts_waitcnt vmcnt(2)\n\t11:\ns_cmp_eq_u32 s70, 8\n\ts_cbranch_scc0 409f\n\t"
            "s_branch 12f\n\t409:\nv_max3_u32 v12, v184, v186, v188\n\tv_max_u32 v12, v12, v190\n\tv_cmp_eq_u32 vcc, -1, v12\n\ts_cbranch_vccz 11f\n\t"
            "buffer_load_dwordx4 v[184:187], v2, s[44:47], s13 offen offset:1024 sc1\n\tbuffer_load_dwordx4 v[188:191], v2, s[44:47], s13 offen offset:2048 sc1\n\ts_branch 11f\n\t309:\nbuffer_load_dwordx4 v[184:187], v2, s[44:47], s13 offen offset:1024 sc1\n\tbuffer_load_dwordx4 v[188:191], v2, s[44:47], s13 offen offset:2048 sc1\n\t"
            "s_waitcnt vmcnt(2)\n\t11:\ns_cmp_eq_u32 s70, 9\n\ts_cbranch_scc0 410f\n\ts_branch 12f\n\t410:\n"
            "v_max3_u32 v12, v192, v194, v196\n\tv_max_u32 v12, v12, v198\n\tv_cmp_eq_u32 vcc, -1, v12\n\ts_cbranch_vccz 11f\n\tbuffer_load_dwordx4 v[192:195], v2, s[44:47], s4 offen offset:0 sc1\n\tbuffer_load_dwordx4 v[196:199], v2, s[44:47], s4 offen offset:3072 sc1\n\t"
            "s_branch 11f\n\t310:\nbuffer_load_dwordx4 v[192:195], v2, s[44:47], s4 offen offset:0 sc1\n\tbuffer_load_dwordx4 v[196:199], v2, s[44:47], s4 offen offset:3072 sc1\n\ts_waitcnt vmcnt(2)\n\t11:\n"
            "s_cmp_eq_u32 s70, 10\n\ts_cbranch_scc0 411f\n\ts_branch 12f\n\t411:\nv_max3_u32 v12, v200, v202, v204\n\tv_max_u32 v12, v12, v206\n\t"
            "v_cmp_eq_u32 vcc, -1, v12\n\ts_cbranch_vccz 11f\n\tbuffer_load_dwordx4 v[200:203], v2, s[44:47], s5 offen offset:0 sc1\n\tbuffer_load_dwordx4 v[204:207], v2, s[44:47], s5 offen offset:3072 sc1\n\ts_branch 11f\n\t311:\n"
            "buffer_load_dwordx4 v[200:203], v2, s[44:47], s5 offen offset:0 sc1\n\tbuffer_load_dwordx4 v[204:207], v2, s[44:47], s5 offen offset:3072 sc1\n\ts_waitcnt vmcnt(2)\n\t11:\ns_cmp_eq_u32 s70, 11\n\ts_cbranch_scc0 412f\n\t"
            "s_branch 12f\n\t412:\nv_max3_u32 v12, v208, v210, v212\n\tv_max_u32 v12, v12, v214\n\tv_cmp_eq_u32 vcc, -1, v12\n\ts_cbranch_vccz 11f\n\t"
            "buffer_load_dwordx4 v[208:211], v2, s[44:47], s6 offen offset:0 sc1\n\tbuffer_load_dwordx4 v[212:215], v2, s[44:47], s6 offen offset:3072 sc1\n\ts_branch 11f\n\t312:\nbuffer_load_dwordx4 v[208:211], v2, s[44:47], s6 offen offset:0 sc1\n\tbuffer_load_dwordx4 v[212:215], v2, s[44:47], s6 offen offset:3072 sc1\n\t"
            "s_waitcnt vmcnt(2)\n\t11:\ns_cmp_eq_u32 s70, 12\n\ts_cbranch_scc0 413f\n\ts_branch 12f\n\t413:\n"
            "v_max3_u32 v12, v216, v218, v220\n\tv_max_u32 v12, v12, v222\n\tv_cmp_eq_u32 vcc, -1, v12\n\ts_cbranch_vccz 11f\n\tbuffer_load_dwordx4 v[216:219], v2, s[44:47], s7 offen offset:0 sc1\n\tbuffer_load_dwordx4 v[220:223], v2, s[44:47], s7 offen offset:3072 sc1\n\t"
            "s_branch 11f\n\t313:\nbuffer_load_dwordx4 v[216:219], v2, s[44:47], s7 offen offset:0 sc1\n\tbuffer_load_dwordx4 v[220:223], v2, s[44:47], s7 offen offset:3072 sc1\n\ts_waitcnt vmcnt(2)\n\t11:\n"
            "s_cmp_eq_u32 s70, 13\n\ts_cbranch_scc0 414f\n\ts_branch 12f\n\t414:\nv_max3_u32 v12, v224, v226, v228\n\tv_max_u32 v12, v12, v230\n\t"
            "v_cmp_eq_u32 vcc, -1, v12\n\ts_cbranch_vccz 11f\n\tbuffer_load_dwordx4 v[224:227], v2, s[44:47], s8 offen offset:0 sc1\n\tbuffer_load_dwordx4 v[228:231], v2, s[44:47], s8 offen offset:3072 sc1\n\ts_branch 11f\n\t314:\n"
            "buffer_load_dwordx4 v[224:227], v2, s[44:47], s8 offen offset:0 sc1\n\tbuffer_load_dwordx4 v[228:231], v2, s[44:47], s8 offen offset:3072 sc1\n\ts_waitcnt vmcnt(2)\n\t11:\ns_cmp_eq_u32 s70, 14\n\ts_cbranch_scc0 415f\n\t"
            "s_branch 12f\n\t415:\nv_max3_u32 v12, v232, v234, v236\n\tv_max_u32 v12, v12, v238\n\tv_cmp_eq_u32 vcc, -1, v12\n\ts_cbranch_vccz 11f\n\t"
            "buffer_load_dwordx4 v[232:235], v2, s[44:47], s9 offen offset:0 sc1\n\tbuffer_load_dwordx4 v[236:239], v2, s[44:47], s9 offen offset:3072 sc1\n\ts_branch 11f\n\t315:\nbuffer_load_dwordx4 v[232:235], v2, s[44:47], s9 offen offset:0 sc1\n\tbuffer_load_dwordx4 v[236:239], v2, s[44:47], s9 offen offset:3072 sc1\n\t"
            "s_waitcnt vmcnt(2)\n\t11:\ns_cmp_eq_u32 s70, 15\n\ts_cbranch_scc0 416f\n\ts_branch 12f\n\t416:\n"
            "v_max3_u32 v12, v176, v178, v180\n\tv_max_u32 v12, v12, v182\n\tv_cmp_eq_u32 vcc, -1, v12\n\ts_cbranch_vccz 11f\n\tbuffer_load_dwordx4 v[176:179], v2, s[44:47], s10 offen offset:0 sc1\n\tbuffer_load_dwordx4 v[180:183], v2, s[44:47], s10 offen offset:3072 sc1\n\t"
            "s_branch 11f\n\t316:\nbuffer_load_dwordx4 v[176:179], v2, s[44:47], s10 offen offset:0 sc1\n\tbuffer_load_dwordx4 v[180:183], v2, s[44:47], s10 offen offset:3072 sc1\n\ts_waitcnt vmcnt(2)\n\t11:\n"
            "s_cmp_eq_u32 s70, 16\n\ts_cbranch_scc0 417f\n\ts_branch 12f\n\t417:\nv_max3_u32 v12, v184, v186, v188\n\tv_max_u32 v12, v12, v190\n\t"
            "v_cmp_eq_u32 vcc, -1, v12\n\ts_cbranch_vccz 11f\n\tbuffer_load_dwordx4 v[184:187], v2, s[44:47], s11 offen offset:0 sc1\n\tbuffer_load_dwordx4 v[188:191], v2, s[44:47], s11 offen offset:3072 sc1\n\ts_branch 11f\n\t317:\n"
            "buffer_load_dwordx4 v[184:187], v2, s[44:47], s11 offen offset:0 sc1\n\tbuffer_load_dwordx4 v[188:191], v2, s[44:47], s11 offen offset:3072 sc1\n\ts_waitcnt vmcnt(2)\n\t11:\ns_cmp_eq_u32 s70, 17\n\ts_cbranch_scc0 418f\n\t"
            "s_branch 12f\n\t418:\nv_max3_u32 v12, v192, v194, v196\n\tv_max_u32 v12, v12, v198\n\tv_cmp_eq_u32 vcc, -1, v12\n\ts_cbranch_vccz 11f\n\t"
            "buffer_load_dwordx4 v[192:195], v2, s[44:47], s12 offen offset:0 sc1\n\tbuffer_load_dwordx4 v[196:199], v2, s[44:47], s12 offen offset:3072 sc1\n\ts_branch 11f\n\t318:\nbuffer_load_dwordx4 v[192:195], v2, s[44:47], s12 offen offset:0 sc1\n\tbuffer_load_dwordx4 v[196:199], v2, s[44:47], s12 offen offset:3072 sc1\n\t"
            "s_waitcnt vmcnt(2)\n\t11:\ns_cmp_eq_u32 s70, 18\n\ts_cbranch_scc0 419f\n\ts_branch 12f\n\t419:\n"
            "v_max3_u32 v12, v200, v202, v204\n\tv_max_u32 v12, v12, v206\n\tv_cmp_eq_u32 vcc, -1, v12\n\ts_cbranch_vccz 11f\n\tbuffer_load_dwordx4 v[200:203], v2, s[44:47], s13 offen offset:0 sc1\n\tbuffer_load_dwordx4 v[204:207], v2, s[44:47], s13 offen offset:3072 sc1\n\t"
            "s_branch 11f\n\t319:\nbuffer_load_dwordx4 v[200:203], v2, s[44:47], s13 offen offset:0 sc1\n\tbuffer_load_dwordx4 v[204:207], v2, s[44:47], s13 offen offset:3072 sc1\n\ts_waitcnt vmcnt(2)\n\t11:\n"
            "12:\n"
            "s_sub_u32 s68, s68, 1\n\ts_cbranch_scc1 9b\n\ts_waitcnt vmcnt(0)\n\ts_setpc_b64 s[72:73]\n\t.subsection 1\n\t.Lce:\n"
            ".subsection 0\n\t.Lout:\ns_waitcnt lgkmcnt(0)\n\t"
            : "=&{s67}"(tag), "=&{v[52:83]}"(sa), "=&{v[84:115]}"(sb), "=&{v[116:123]}"(sc) : [t] "v"(tid), [b] "s"(bid), [k] "s"(ka)
            : "memory", "scc", "vcc", "s4", "s5", "s6", "s7", "s8", "s9", "s10", "s11", "s12", "s13", "s14", "s15", "s16", "s17", "s18", "s19", "s20", "s21", "s22", "s23", "s24", "s25", "s26", "s27", "s28", "s29", "s30", "s31", "s33", "s34", "s35", "s36", "s37", "s38", "s39", "s40", "s41", "s42", "s43", "s44", "s45", "s46", "s47", "s48", "s49", "s50", "s51", "s52", "s53", "s54", "s55", "s56", "s57", "s58", "s59", "s60", "s61", "s62", "s63", "s64", "s65", "s66", "s68", "s69", "s70", "s71", "s72", "s73", "s74", "s75", "s76", "s77", "s78", "s79", "s80", "s81", "s82", "s83", "v2", "v3", "v4", "v5", "v6", "v7", "v8", "v9", "v10", "v11", "v12", "v13", "v14", "v15", "v16", "v17", "v18", "v19", "v20", "v21", "v22", "v23", "v24", "v25", "v26", "v27", "v28", "v29", "v30", "v31", "v32", "v33", "v34", "v35", "v36", "v37", "v38", "v39", "v40", "v41", "v42", "v43", "v44", "v45", "v46", "v47", "v48", "v49", "v50", "v51", "v124", "v125", "v126", "v127", "v128", "v129", "v130", "v131", "v132", "v133", "v134", "v135", "v136", "v137", "v138", "v139", "v140", "v141", "v142", "v143", "v144", "v145", "v146", "v147", "v148", "v149", "v150", "v151", "v152", "v153", "v154", "v155", "v156", "v157", "v158", "v159", "v160", "v161", "v162", "v163", "v164", "v165", "v166", "v167", "v168", "v169", "v170", "v171", "v172", "v173", "v174", "v175", "v176", "v177", "v178", "v179", "v180", "v181", "v182", "v183", "v184", "v185", "v186", "v187", "v188", "v189", "v190", "v191", "v192", "v193", "v194", "v195", "v196", "v197", "v198", "v199", "v200", "v201", "v202", "v203", "v204", "v205", "v206", "v207", "v208", "v209", "v210", "v211", "v212", "v213", "v214", "v215", "v216", "v217", "v218", "v219", "v220", "v221", "v222", "v223", "v224", "v225", "v226", "v227", "v228", "v229", "v230", "v231", "v232", "v233", "v234", "v235", "v236", "v237", "v238", "v239", "v240", "v241", "v242", "v243", "v244", "v245", "v246", "v247", "v248", "v249", "v250", "v251", "v252", "v253", "v254", "v255");
    tag = RFL(tag);
    asm volatile("" : "+s"(bid), "+s"(ka)); asm volatile("" : "+v"(tid));
    const u16 *x = (const u16*)ka[0], *wo = (const u16*)ka[2], *w_r = (const u16*)ka[3], *wg = (const u16*)ka[4], *wu = (const u16*)ka[5], *wd = (const u16*)ka[6]; char* ws = (char*)ka[7];
    const int wv = __builtin_amdgcn_readfirstlane(tid >> 6);
    const unsigned lds0 = (unsigned)(size_t)(lchar*)smem;
#define LANE() const int lane = lane_id(); const int g = lane >> 4, li = lane & 15; (void)g; (void)li
    const int xcd = bid & 7, j = ((bid >> 3) + 8) & 31;
    const bool gemm = j < 25;
    const int fp = xcd >> 1, bl = (j * 13) >> 6, cl = j - bl * 5;
    const int cg = (xcd & 1) * 5 + cl;
    const int par = fp >> 1, I0 = ((fp & 1) * 5 + bl) * 104 - 8;
    const __amdgpu_buffer_rsrc_t rs = __builtin_amdgcn_make_buffer_rsrc((void*)ws, 0, W_END, 0x00020000);
    auto wait_flag = [&](unsigned a) { SPIN { if (RFL(ldsrv<unsigned>(a))) break; __builtin_amdgcn_s_sleep(1); } MEMB(); };
    unsigned ep = 0;
    auto epld = [&]() { asm volatile("s_load_dword %0, %1, 0x0" : "=s"(ep) : "s"(ws) : "memory"); };
    auto mktag = [&]() { asm volatile("s_waitcnt lgkmcnt(0)" : "+s"(ep) :: "memory"); tag = ep * 1664525u + 1013904223u; };
        if (gemm) {
            LANE();
            wait_flag(lds0 + FL_W);
            const u32x4 kk = ldsrv<u32x4>(lds0 + L_KEY + lane * 16);
            const uint2 t2 = top2wave(lane < 52 ? kk.x : 0u, lane < 52 ? kk.z : 0u);
            {
                const unsigned rr = (0xffffu - ((lane ? t2.y : t2.x) & 0xffffu)) & 2047u; const int pos = (int)(rr >> 1) - I0;
                if (lane < 2 && (int)(rr & 1) == par && pos >= 8 && pos < 112) ldsw<unsigned>(lds0 + L_RM + ((pos & ~15) + 4 * (pos & 3) + ((pos >> 2) & 3)) * 4, L_RG + lane * 288);
                ldsw<unsigned>(lds0 + FL_R, 1u);
            }
            unsigned rr[2]; bool own[2]; float xr_[2], xn[2], sg[2];
#pragma unroll
            for (int k = 0; k < 2; ++k) { rr[k] = (0xffffu - ((k ? t2.y : t2.x) & 0xffffu)) & 2047u; const int pos = (int)(rr[k] >> 1) - I0; own[k] = (int)(rr[k] & 1) == par && pos >= 8 && pos < 112; }
            if (__builtin_expect(own[0] || own[1], 0)) {
                __builtin_amdgcn_s_setprio(3);
#pragma unroll
                for (int k = 0; k < 2; ++k) {
                    const int pos = own[k] ? (int)(rr[k] >> 1) - I0 : 8, m = (pos & ~15) + 4 * (pos & 3) + ((pos >> 2) & 3);
                    xr_[k] = lo2f(x[rr[k] * 640 + cg * 64 + lane]);
                    sg[k] = rbf(__builtin_amdgcn_rcpf(1.f + __builtin_amdgcn_exp2f(keyscore(k ? t2.y : t2.x) * -1.4426950408889634f))); NOPK(sg[k]);
                    xn[k] = rbf(xr_[k] + ldsrv<float>(lds0 + L_NM + m * 4)); NOPK(xn[k]); xn[k] = rbf(xn[k] * ldsrv<float>(lds0 + L_RS + pos * 4)); OPQ(xn[k]);
                }
                __builtin_amdgcn_s_sleep(18);
                u32x4 y[5];
                SPIN {
                    MEMB();
                    bool old = false;
#pragma unroll
                    for (int i = 0; i < 5; ++i) y[i] = __builtin_amdgcn_raw_buffer_load_b128(rs, W_Y + (cg * 64 + lane) * 16, i * 10240, SC1);
#pragma unroll
                    for (int i = 0; i < 5; ++i) old = old || y[i].y != tag || y[i].w != tag;
                    if (__builtin_amdgcn_ballot_w64(old) == 0) break;
                }
#pragma unroll
                for (int k = 0; k < 2; ++k) if (own[k]) {
                    float ya = __uint_as_float(y[0][2 * k]) + __uint_as_float(y[1][2 * k]); NOPK(ya);
                    const float ys = (ya + (__uint_as_float(y[2][2 * k]) + __uint_as_float(y[3][2 * k]))) + __uint_as_float(y[4][2 * k]);
                    float gt = silu_b(rbf(xn[k] + rbf(rbf(ys) * sg[k]))) * -1.4426950408889634f, sv; OPQ(gt);
                    SPIN {
                        const unsigned fl = ldsrv<unsigned>(lds0 + L_RG + k * 288 + 256); sv = ldsrv<float>(lds0 + L_RG + k * 288 + lane * 4);
                        if (RFL(fl)) break;
                        __builtin_amdgcn_s_sleep(1);
                    }
                    const float o = fmaf(sv, gt, xr_[k]);
                    asm volatile("global_store_short %0, %1, %2 sc1" :: "v"((rr[k] * 640 + cg * 64 + lane) * 2), "v"(cvtpk(o, o)), "s"(ka[8]) : "memory");
                }
            }
            END();
        }
    {
        LANE();
        const int fq = (j - 25) * 2 + (xcd & 1), f = fp * 14 + fq;
        const int n0 = fq * 40 + wv * 4 + g;
        epld();
        if (wv == 7 && lane < 16) ldsw<u32x4>(lds0 + MISC + lane * 16, (u32x4){0u, 0u, 0u, 0u});
        const int u2 = max(f - 6, 0), kr = (u2 * 26) >> 8, fc = u2 - kr * 10, n1 = wv < 4 ? 3 : 2;
        const int d0 = kr * 11 + wv, d1 = kr * 11 + 4 + wv; const bool two = wv >= 4 && wv < 7 && d1 < 54;
        const char* w1 = (const char*)(f & 1 ? wu : wg) + min(f >> 1, 26) * 128; const char* w2 = (const char*)wd + fc * 128;
        unsigned fv[2][4];
#pragma unroll
        for (int i = 0; i < 4; ++i) { const unsigned c = (((lane & 7) ^ (2 * ((lane >> 4) & 1) + 4 * (i & 1))) * 16) - i * 1024; fv[0][i] = ((lane >> 3) + i * 8) * (F_ * 2) + c; fv[1][i] = ((lane >> 3) + i * 8) * 1280 + c; }
        auto issue = [&](int m, int ks, int slot) {
            asm volatile("s_mov_b32 m0, %[l]\n\ts_nop 0\n\tglobal_load_lds_dwordx4 %0, %[b] nt\n\tglobal_load_lds_dwordx4 %1, %[b] offset:1024 nt\n\tglobal_load_lds_dwordx4 %2, %[b] offset:2048 nt\n\tglobal_load_lds_dwordx4 %3, %[b] offset:3072 nt"
                         :: "v"(fv[m][0]), "v"(fv[m][1]), "v"(fv[m][2]), "v"(fv[m][3]), [b] "s"((m ? w2 : w1) + (unsigned)(ks * 32 * (m ? 1280 : F_ * 2))), [l] "s"(lds0 + wv * 16384 + slot * 4096) : "memory");
        };
        if (wv == 4) {
            const int l = min(f * 14 + lane, 770), o = l < 280 ? W_KEY + l * 128 : l < 536 ? W_ST + (l - 280) * 128 : W_Y + (l - 536) * 256;
            if (lane < 14) asm volatile("s_mov_b32 m0, %[l]\n\ts_nop 0\n\tbuffer_load_dword %0, %1, 0 offen lds" :: "v"(o), "s"(rs), [l] "s"(lds0 + L_DUM) : "memory");
        }
        if (wv >= 5) {
            const int i = min((j - 25) * 172 + (7 - wv) * 64 + lane, 1199), n = (i * 4370) >> 16, e = i - n * 15, m = (e * 52) >> 8, rw = 4 * n + fp;
            asm volatile("s_mov_b32 m0, %[l]\n\ts_nop 0\n\tglobal_load_lds_dword %0, %[b]" :: "v"(rw * 3840 + ((rw >> 6) + 1) * 245760 + e * 128 + m * 640 + (xcd & 1) * 640), [b] "s"(ka[1]), [l] "s"(lds0 + L_DUM) : "memory");
        }
        BARN();
        asm volatile("s_waitcnt vmcnt(%2)" : "+{v[84:115]}"(sb), "+{v[116:123]}"(sc) : "n"(5) : "memory");
#define SXR(i) ((i) < 20 ? sa[(12 + (i)) & 31] : (i) < 52 ? sb[((i) - 20) & 31] : sc[((i) - 52) & 7])
        mktag();
        {
            const unsigned one2 = 0x3F803F80u;
            float wsum = 0.f;
#pragma unroll
            for (int e = 0; e < 20; ++e) wsum = dot2(SXR(40 + e), one2, wsum);
            wsum = sum16(wsum);
            auto stat = [&](int p) {
                float s1 = 0.f, s2 = 0.f, sr = 0.f;
#pragma unroll
                for (int e = 0; e < 20; ++e) { const unsigned u = SXR(20 * p + e); s1 = dot2(u, one2, s1); s2 = dot2(u, u, s2); sr = dot2(u, SXR(40 + e), sr); }
                const float mean = sum16(s1) * (1.f / 640.f);
                const unsigned mb2 = cvtpk(mean, mean); const float mb = lo2f(mb2);
                const float q = fmaf(40.f * mb, mb, fmaf(-2.f * mb, s1, s2));
                float dm = mean - mb; NOPK(dm);
                dm *= dm; NOPK(dm);
                const float var = fmaxf(sum16(q) * (1.f / 640.f) - dm, 0.f);
                const float sd = rbf(__builtin_amdgcn_sqrtf(rbf(rbf(var) + 1e-5f)));
                const float rsc = rbf(fmaf(-mb, wsum, sum16(sr)) / sd);
                const int n = n0 + 32 * p, i = (fp & 1) * 512 + n;
                if (li == 0) {
                    asm volatile("buffer_store_dwordx4 %0, %1, %2, 0 offen sc1\n\ts_nop 1" :: "v"((u32x4){(mb2 << 16) ^ 0x80000000u, tag, __float_as_uint(__builtin_amdgcn_rcpf(sd)), tag}), "v"(n < 512 ? W_ST + (par * 1024 + i) * 16 : 0x40000000), "s"(rs) : "memory");
                    ldsw<unsigned>(lds0 + LS_ROW + (p * 32 + wv * 4 + g) * 4, n < 512 ? rkey(rsc, 2 * i + par) : 0u);
                }
            };
            asm volatile("" : "+v"(wsum));
            if (wv < 2) stat(1);
            asm volatile("s_bitcmp1_b32 0xf3, %2\n\ts_cbranch_scc1 1f\n\ts_waitcnt vmcnt(%1)\n1:\n\ts_waitcnt vmcnt(%1 + 1)" : "+{v[52:83]}"(sa) : "n"(0), "s"(wv) : "memory", "scc");
            if (wv < 5) issue(0, wv, 0);
            stat(0);
        }
        BAR();
        if (wv == 6) asm volatile("buffer_store_dword %0, %1, %2, 0 offen sc1" :: "v"(tag), "v"(lane ? 0x40000000 : W_ACK + (f < 40 ? (f >> 1) * 128 + 40 + (f & 1) * 4 : 2560 + (f - 40) * 4)), "s"(rs) : "memory");
        if (wv == 7) {
            const unsigned k = ldsrv<unsigned>(lds0 + LS_ROW + min(lane, 39) * 4);
            const uint2 t2 = top2wave(lane < 40 ? k : 0u, 0u);
            if (lane == 0 && fq < 13) asm volatile("buffer_store_dwordx4 %0, %1, %2, 0 offen sc1\n\ts_nop 1" :: "v"((u32x4){t2.x, tag, t2.y, tag}), "v"(W_KEY + (fp * 13 + fq) * 16), "s"(rs) : "memory");
        }
        BARN();
        if (wv >= 5) issue(0, wv, 0);
        issue(0, wv + 8, 1);
        auto wh = [&]() { const int i = min((j - 25) * 116 + (wv - 4) * 29 + lane, 799), n = (i * 13108) >> 16; return (4 * n + fp) * 1280 + (i - 5 * n) * 128 + (xcd & 1) * 640; };
        if (wv < 4) issue(0, wv + 16, 2);
        else asm volatile("s_mov_b32 m0, %[l]\n\ts_mov_b32 exec_hi, 0\n\tglobal_load_lds_dword %0, %[b]\n\ts_mov_b32 exec_hi, -1" :: "v"(wh()), [b] "s"(wo), [l] "s"(lds0 + L_DUM) : "memory");
        u32x4 kk;
        BARN();
        asm volatile("s_cmp_lg_u32 %3, 7\n\ts_cbranch_scc1 1f\n\ts_sleep 0\n\tbuffer_load_dwordx4 %0, %1, %2, 0 offen sc1\n1:\n\ts_barrier" : "=&v"(kk) : "v"(W_KEY + min(lane, 51) * 16), "s"(rs), "s"(wv) : "memory", "scc");
        if (wv != 7) issue(1, d0, 3);
        asm volatile("s_cmp_lg_u32 %1, 7\n\ts_cbranch_scc1 1f\n\ts_waitcnt vmcnt(0)\n1:" : "+v"(kk) : "s"(wv) : "memory", "scc");
        if (wv == 7) {
            SPIN {
                if (__builtin_amdgcn_ballot_w64(kk.y != tag || kk.w != tag) == 0) break;
                asm volatile("buffer_load_dwordx4 %0, %1, %2, 0 offen sc1\n\ts_waitcnt vmcnt(0)" : "=&v"(kk) : "v"(W_KEY + min(lane, 51) * 16), "s"(rs) : "memory");
            }
            const uint2 t2 = top2wave(lane < 52 ? kk.x : 0u, lane < 52 ? kk.z : 0u);
            if (lane < 2) ldsw<unsigned>(lds0 + ROUT + lane * 8, (0xffffu - ((lane ? t2.y : t2.x) & 0xffffu)) & 2047u);
        }
        BAR();
        const unsigned row = ldsrv<unsigned>(lds0 + ROUT + (li & 1) * 8);
        u32x4 af[3], st;
        asm volatile("global_load_dwordx4 %0, %4, %5\n\tglobal_load_dwordx4 %1, %4, %5 offset:512\n\tglobal_load_dwordx4 %2, %6, %5 offset:1024\n\tbuffer_load_dwordx4 %3, %7, %8, 0 offen sc1"
                     : "=&v"(af[0]), "=&v"(af[1]), "=&v"(af[2]), "=&v"(st) : "v"(row * 1280u + wv * 64 + g * 16), "s"(x), "v"(row * 1280u + (wv & 3) * 64 + g * 16), "v"(wv ? 0x40000000 : W_ST + ((row & 1) * 1024 + (row >> 1)) * 16), "s"(rs) : "memory");
        bf16x8 wf[3][4];
        unsigned ra[4];
#pragma unroll
        for (int q = 0; q < 4; ++q) ra[q] = lds0 + wv * 16384 + (unsigned)((8 * g + (li >> 2)) * 128 + (((q ^ (((li >> 3) & 1) + 2 * (g & 1))) * 32) + (li & 3) * 8));
        auto rdf = [&](int k, int slot) {
#pragma unroll
            for (int q = 0; q < 4; ++q) wf[k][q] = cat8(trread(ra[q] + slot * 4096), trread(ra[q] + slot * 4096 + 512));
        };
        if (wv == 7) issue(1, d0, 3);
        if (two) issue(1, d1, 2);
        if (two) wvm<12>(); else wvm<8>();
        rdf(0, 0); rdf(1, 1); if (wv < 4) rdf(2, 2);
        asm volatile("s_cmp_eq_u32 %4, 0\n\ts_cbranch_scc0 1f\n\ts_waitcnt vmcnt(0)\n1:\n\ts_waitcnt vmcnt(4)" : "+v"(af[0]), "+v"(af[1]), "+v"(af[2]), "+v"(st) : "s"(RFL(two || wv == 7)) : "memory", "scc");
        f32x4 c[4];
#pragma unroll
        for (int q = 0; q < 4; ++q) c[q] = (f32x4){0.f, 0.f, 0.f, 0.f};
#pragma unroll
        for (int k = 0; k < 3; ++k) if (k < n1) {
#pragma unroll
            for (int e = 0; e < 4; ++e) af[k][e] = li & 2 ? 0x3F803F80u : af[k][e];
#pragma unroll
            for (int q = 0; q < 4; ++q) c[q] = mfma32(__builtin_bit_cast(bf16x8, af[k]), wf[k][q], c[q]);
        }
        auto total = [&](int off) {
            f32x4 m;
#pragma unroll
            for (int e = 0; e < 4; ++e) { float a_ = g & 1 ? c[1][e] : c[0][e], b_ = g & 1 ? c[3][e] : c[2][e]; NOPK(a_); NOPK(b_); m[e] = g & 2 ? b_ : a_; }
            ldsw<f32x4>(lds0 + wv * 16384 + off + lane * 16, m);
            BAR();
            if (wv) return m;
            f32x4 pr[8];
#pragma unroll
            for (int w = 0; w < 8; ++w) pr[w] = ldsrv<f32x4>(lds0 + w * 16384 + off + lane * 16);
#pragma unroll
            for (int e = 0; e < 3; ++e) { float t[4];
#pragma unroll
                for (int w = 0; w < 4; ++w) { t[w] = pr[2 * w][e] + pr[2 * w + 1][e]; NOPK(t[w]); }
                float u = t[0] + t[1], v = t[2] + t[3]; NOPK(u); NOPK(v); u += v; NOPK(u); m[e] = u; }
            return m;
        };
        {
            const f32x4 s = total(0);
            if (wv == 0 && f < 54) {
                SPIN {
                    if (__builtin_amdgcn_ballot_w64(st.y != tag || st.w != tag) == 0) break;
                    asm volatile("buffer_load_dwordx4 %0, %1, %2, 0 offen sc1\n\ts_waitcnt vmcnt(0)" : "=&v"(st) : "v"(W_ST + ((row & 1) * 1024 + (row >> 1)) * 16), "s"(rs) : "memory");
                }
                const float n0_ = __uint_as_float(__builtin_amdgcn_readlane(st.x, 0)), n1_ = __uint_as_float(__builtin_amdgcn_readlane(st.x, 1)), i0 = __uint_as_float(__builtin_amdgcn_readlane(st.z, 0)), i1 = __uint_as_float(__builtin_amdgcn_readlane(st.z, 1));
                float v0 = rbf(fmaf(n0_, s.z, s.x) * i0), v1 = rbf(fmaf(n1_, s.z, s.y) * i1);
                if (!(f & 1)) { v0 = silu_b(v0); NOPK(v0); v1 = silu_b(v1); }
                const unsigned hb = cvtpk(v0, v1), nb = dppu<0xB1>(hb);
                const unsigned pay = (lane & 1) ? __builtin_amdgcn_perm(hb, nb, 0x07060302u) : __builtin_amdgcn_perm(nb, hb, 0x05040100u);
                asm volatile("buffer_store_dwordx2 %0, %1, %2, 0 offen sc1" :: "v"((u32x2){pay, tag}), "v"(W_H + ((f & 1) * 2 + (lane & 1)) * 6912 + ((f >> 1) * 32 + (lane >> 1)) * 8), "s"(rs) : "memory");
            }
        }
        if (f == 0 && wv == 1) {
            SPIN {
                u32x4 a_;
                asm volatile("buffer_load_dwordx4 %0, %1, %2, 0 offen sc1\n\ts_waitcnt vmcnt(0)" : "=&v"(a_) : "v"(W_ACK + (lane < 60 ? ((lane * 43) >> 7) * 80 + lane * 16 : 1600 + lane * 16)), "s"(rs) : "memory");
                if (__builtin_amdgcn_ballot_w64(a_.x != tag || a_.y != tag || a_.z != tag || a_.w != tag) == 0) break;
                __builtin_amdgcn_s_sleep(8);
            }
            if (lane == 0) __builtin_amdgcn_raw_buffer_store_b32(tag, rs, W_EP, 0, SC1);
        }
        if (f >= 6) {
            wvm<0>();
            rdf(0, 3); rdf(1, 2);
            const int ho = W_H + ((lane >> 4) & 1) * 6912 + ((lane >> 5 && two ? d1 : d0) * 16 + li) * 8;
            u32x2 hg, hu;
            __builtin_amdgcn_s_sleep(26);
            SPIN {
                asm volatile("buffer_load_dwordx2 %0, %2, %3, 0 offen sc1\n\tbuffer_load_dwordx2 %1, %2, %3, %4 offen sc1\n\ts_waitcnt vmcnt(0)" : "=&v"(hg), "=&v"(hu) : "v"(ho), "s"(rs), "s"(13824) : "memory");
                if (__builtin_amdgcn_ballot_w64(hg.y != tag || hu.y != tag) == 0) break;
                __builtin_amdgcn_s_sleep(10);
            }
            float h0 = lo2f(hg.x) * lo2f(hu.x), h1 = hi2f(hg.x); NOPK(h0); NOPK(h1); h1 *= hi2f(hu.x); NOPK(h1);
            ldsw<unsigned>(lds0 + wv * 16384 + 4096 + lane * 4, cvtpk(h0, h1));
#pragma unroll
            for (int q = 0; q < 4; ++q) c[q] = (f32x4){0.f, 0.f, 0.f, 0.f};
#pragma unroll
            for (int k = 0; k < 2; ++k) if (k == 0 || two) {
                const u32x4 h = ldsrv<u32x4>(lds0 + wv * 16384 + 4096 + k * 128 + (li & 1) * 64 + g * 16);
#pragma unroll
                for (int q = 0; q < 4; ++q) c[q] = mfma32(__builtin_bit_cast(bf16x8, h), wf[k][q], c[q]);
            }
            const f32x4 ys = total(1024);
            if (wv == 0) __builtin_amdgcn_raw_buffer_store_b128((u32x4){__float_as_uint(ys.x), tag, __float_as_uint(ys.y), tag}, rs, W_Y + kr * 10240 + (fc * 64 + lane) * 16, 0, SC1);
        }
        wvm<0>();
    }
}

extern "C" void kernel_launch(void* const* d_in, const int* in_sizes, int n_in, void* d_out, int out_size, void* d_ws,
                              size_t ws_size, hipStream_t stream) {
    (void)hipFuncSetAttribute((const void*)fused, hipFuncAttributeMaxDynamicSharedMemorySize, LDSZ);
    fused<<<256, 512, LDSZ, stream>>>((const u16*)d_in[0], (const u16*)d_in[1], (const u16*)d_in[2], (const u16*)d_in[4],
                                      (const u16*)d_in[5], (const u16*)d_in[6], (const u16*)d_in[7], (char*)d_ws, (u16*)d_out);
}
```

```cpp
#include <hip/hip_runtime.h>

typedef __attribute__((ext_vector_type(8))) short bf16x8;
typedef __attribute__((ext_vector_type(4))) short bf16x4;
typedef __attribute__((ext_vector_type(4))) float f32x4;
typedef __attribute__((ext_vector_type(2))) float f32x2;
typedef __attribute__((ext_vector_type(4))) unsigned u32x4;
typedef __attribute__((ext_vector_type(2))) unsigned u32x2;
typedef __attribute__((ext_vector_type(2))) __bf16 bf16x2v;
typedef __attribute__((ext_vector_type(8))) unsigned u32x8;
typedef unsigned short u16;
typedef __attribute__((address_space(3))) char lchar;


#define F_ 1728
constexpr int W_EP = 0;
constexpr int W_KEY = 4096;
constexpr int W_ACK = 8192;
constexpr int W_H = 12288;
constexpr int W_ST = 65536;
constexpr int W_Y = 98304;
constexpr int W_O = 155648;
constexpr int W_END = W_O + 80 * 40960;
constexpr int XS = 14336, SLOT = XS + 24576;
constexpr int WO0 = 2 * SLOT;
constexpr int MISC = WO0 + 81920;
constexpr int FL_W = MISC + 48, FL_R = MISC + 64, ROUT = MISC + 144;
constexpr int L_NM = MISC + 256, L_RS = L_NM + 512 + 64, L_RM = L_RS + 512, L_RG = L_RM + 512, L_DUM = L_RG + 512, L_KEY = L_DUM + 512;
constexpr int LS_ROW = L_NM;
constexpr int LDSZ = 163840;
static_assert(L_KEY + 1024 <= LDSZ, "lds");
#define SPIN_MAX (1 << 21)
#define SPIN _Pragma("nounroll") for (int it = 0; it < SPIN_MAX; ++it)

__device__ __forceinline__ unsigned cvtpk(float lo, float hi) { return __builtin_bit_cast(unsigned, __builtin_convertvector((f32x2){lo, hi}, bf16x2v)); }
__device__ __forceinline__ float lo2f(unsigned u) { return __uint_as_float(u << 16); }
__device__ __forceinline__ float hi2f(unsigned u) { return __uint_as_float(u & 0xffff0000u); }
__device__ __forceinline__ float rbf(float f) { return lo2f(cvtpk(f, f)); }
template <int CTRL> __device__ __forceinline__ float dppf(float x) { return __builtin_bit_cast(float, __builtin_amdgcn_mov_dpp(__builtin_bit_cast(int, x), CTRL, 0xf, 0xf, true)); }
template <int CTRL> __device__ __forceinline__ unsigned dppu(unsigned x) { return (unsigned)__builtin_amdgcn_mov_dpp((int)x, CTRL, 0xf, 0xf, true); }
__device__ __forceinline__ float sum16(float x) { x += dppf<0xB1>(x); x += dppf<0x4E>(x); x += dppf<0x141>(x); x += dppf<0x140>(x); return x; }
__device__ __forceinline__ float dot2(unsigned a, unsigned b, float c) { return __builtin_amdgcn_fdot2_f32_bf16(__builtin_bit_cast(bf16x2v, a), __builtin_bit_cast(bf16x2v, b), c, false); }
__device__ __forceinline__ float silu_b(float v) { return v * __builtin_amdgcn_rcpf(__builtin_amdgcn_exp2f(v * -1.4426950408889634f) + 1.f); }
__device__ __forceinline__ unsigned rkey(float rb, int row) {
    unsigned b = __float_as_uint(rb) >> 16; b = (b & 0x8000u) ? (~b & 0xffffu) : (b | 0x8000u);
    return (b << 16) | (unsigned)(0xffff - row);
}
__device__ __forceinline__ float keyscore(unsigned k) { const unsigned b = k >> 16; return __uint_as_float(((b & 0x8000u) ? (b & 0x7fffu) : (~b & 0xffffu)) << 16); }
#define T2M(b0, b1) { const unsigned _b0 = (b0), _b1 = (b1); const unsigned _m = min(k0, _b0); k0 = max(k0, _b0); k1 = max(_m, max(k1, _b1)); }
__device__ __forceinline__ uint2 top2wave(unsigned k0, unsigned k1) {
    T2M(dppu<0xB1>(k0), dppu<0xB1>(k1)); T2M(dppu<0x4E>(k0), dppu<0x4E>(k1));
    T2M(dppu<0x141>(k0), dppu<0x141>(k1)); T2M(dppu<0x140>(k0), dppu<0x140>(k1));
    const unsigned a0 = __builtin_amdgcn_readlane(k0, 0), a1 = __builtin_amdgcn_readlane(k1, 0);
    const unsigned c0 = __builtin_amdgcn_readlane(k0, 16), c1 = __builtin_amdgcn_readlane(k1, 16);
    const unsigned d0 = __builtin_amdgcn_readlane(k0, 32), d1 = __builtin_amdgcn_readlane(k1, 32);
    const unsigned e0 = __builtin_amdgcn_readlane(k0, 48), e1 = __builtin_amdgcn_readlane(k1, 48);
    k0 = a0; k1 = a1; T2M(c0, c1); T2M(d0, d1); T2M(e0, e1);
    return make_uint2(k0, k1);
}
template <int N> __device__ __forceinline__ void wvm() { asm volatile("" ::: "memory"); __builtin_amdgcn_s_waitcnt((N & 15) | ((N >> 4) << 14) | 0x0F70); asm volatile("" ::: "memory"); }
__device__ __forceinline__ bf16x4 trread(unsigned a) { return __builtin_amdgcn_ds_read_tr16_b64_v4i16((__attribute__((address_space(3))) bf16x4*)(size_t)a); }
template <class T> __device__ __forceinline__ T ldsrv(unsigned a) { return *(volatile __attribute__((address_space(3))) T*)(size_t)a; }
template <class T> __device__ __forceinline__ void ldsw(unsigned a, T v) { *(volatile __attribute__((address_space(3))) T*)(size_t)a = v; }
__device__ __forceinline__ bf16x8 cat8(bf16x4 lo, bf16x4 hi) { return (bf16x8){lo[0], lo[1], lo[2], lo[3], hi[0], hi[1], hi[2], hi[3]}; }
__device__ __forceinline__ f32x4 mfma32(bf16x8 a, bf16x8 b, f32x4 c) { return __builtin_amdgcn_mfma_f32_16x16x32_bf16(a, b, c, 0, 0, 0); }
__device__ __forceinline__ int lane_id() { int l; asm volatile("v_mbcnt_lo_u32_b32 %0, -1, 0\n\tv_mbcnt_hi_u32_b32 %0, -1, %0" : "=v"(l)); __builtin_assume(l >= 0 && l < 64); return l; }
#define BAR() asm volatile("s_waitcnt lgkmcnt(0)\n\ts_barrier" ::: "memory")
#define MEMB() asm volatile("" ::: "memory")
#define RFL(x) ((unsigned)__builtin_amdgcn_readfirstlane((int)(x)))
#define SC1 16

#define NOPK(v) asm("" : "+v"(v))
#define OPQ(v) asm volatile("" : "+v"(v))
#define END() do { asm volatile("s_endpgm"); __builtin_amdgcn_s_waitcnt(0); return; } while (0)
#define BARN() asm volatile("s_barrier" ::: "memory")

__global__ __launch_bounds__(512, 2) void fused(const u16* x_, const u16* wqkv_, const u16* wo_, const u16* w_r_, const u16* wg_, const u16* wu_, const u16* wd_, char* ws_, u16* out_) {
    extern __shared__ __attribute__((aligned(16))) char smem[];
    typedef __attribute__((ext_vector_type(32))) unsigned u32x32;
    u32x32 sa, sb; u32x8 sc;
    unsigned tag, bid = blockIdx.x; int tid = threadIdx.x;
    typedef __attribute__((address_space(1))) char gchar;
    typedef gchar* const __attribute__((address_space(4)))* karg_t;
    karg_t ka = (karg_t)__builtin_amdgcn_kernarg_segment_ptr();
    asm volatile(
            "s_load_dwordx2 s[40:41], %[k], 0x8\n\ts_load_dwordx2 s[36:37], %[k], 0x0\n\tv_readfirstlane_b32 s69, %[t]\n\ts_lshr_b32 s70, %[b], 3\n\ts_cmpk_lg_u32 s69, 192\n\ts_cbranch_scc1 1f\n\t"
            "s_getpc_b64 s[72:73]\n\t.Lch:\ns_mul_i32 s71, s70, 9\n\ts_add_i32 s71, s71, -192\n\tv_add_lshl_u32 v36, s71, %[t], 7\n\tv_min_u32 v36, .Lce-.Lch-4, v36\n\t"
            "s_mov_b32 m0, 162368\n\ts_mov_b64 exec, 0x1ff\n\tglobal_load_lds_dword v36, s[72:73]\n\ts_mov_b64 exec, -1\n\t1:\ns_add_i32 s70, s70, 8\n\t"
            "s_and_b32 s70, s70, 31\n\ts_cmp_lt_u32 s70, 25\n\ts_cbranch_scc0 .LF\n\tv_bfe_u32 v36, %[t], 3, 3\n\tv_and_b32 v37, 7, %[t]\n\ts_mul_i32 s71, s70, 13\n\t"
            "s_lshr_b32 s71, s71, 6\n\tv_and_b32 v38, 2, v36\n\tv_xor_b32 v46, v38, v37\n\ts_lshl_b32 s64, s71, 2\n\ts_sub_i32 s64, s70, s64\n\ts_sub_i32 s57, s64, s71\n\t"
            "v_mul_u32_u24 v40, 0xf00, v36\n\ts_sub_i32 s72, s64, 5\n\ts_min_u32 s64, s64, s72\n\tv_lshl_add_u32 v40, v46, 4, v40\n\ts_bfe_u32 s56, s69, 0x20006\n\ts_and_b32 s72, %[b], 1\n\t"
            "s_mul_i32 s72, s72, 5\n\ts_add_i32 s58, s57, s72\n\ts_lshl_b32 s58, s58, 7\n\ts_mul_i32 s61, s64, 491520\n\ts_mul_i32 s73, s56, 0xf000\n\ts_add_i32 s73, s73, s58\n\t"
            "s_add_i32 s61, s61, s73\n\ts_mul_i32 s66, s56, 0x1800\n\ts_add_i32 m0, s66, 53248\n\ts_mov_b32 s42, 0x258000\n\ts_mov_b32 s43, 0x20000\n\tv_add_u32 v41, 0x100, v40\n\t"
            "v_add_u32 v42, 0x200, v40\n\tv_xor_b32 v43, 64, v40\n\tv_add_u32 v43, 0x6c00, v43\n\tv_add_u32 v44, 0x1100, v43\n\tv_add_u32 v45, 0x1200, v43\n\ts_cmpk_lt_u32 s69, 0x100\n\t"
            "s_cbranch_scc0 1f\n\ts_waitcnt lgkmcnt(0)\n\tbuffer_load_dwordx4 v40, s[40:43], s61 offen lds\n\tbuffer_load_dwordx4 v41, s[40:43], s61 offen offset:1024 lds\n\tbuffer_load_dwordx4 v42, s[40:43], s61 offen offset:2048 lds\n\tbuffer_load_dwordx4 v43, s[40:43], s61 offen offset:3072 lds\n\t"
            "s_addk_i32 m0, 0x1000\n\ts_nop 0\n\tbuffer_load_dwordx4 v44, s[40:43], s61 offen lds\n\tbuffer_load_dwordx4 v45, s[40:43], s61 offen offset:1024 lds\n\t1:\ns_lshl_b32 s72, s56, 5\n\t"
            "s_min_u32 s72, s72, 88\n\tv_add_u32 v39, s72, v36\n\ts_bfe_u32 s62, %[b], 0x10001\n\ts_mul_i32 s62, s62, 5\n\ts_add_i32 s62, s62, s71\n\ts_mul_i32 s62, s62, 104\n\t"
            "s_sub_i32 s62, s62, 8\n\tv_and_b32 v47, 3, v39\n\tv_lshl_add_u32 v47, v47, 2, s62\n\tv_and_b32 v48, 0x70, v39\n\tv_bfe_u32 v49, v39, 2, 2\n\tv_add3_u32 v48, v48, v49, v47\n\t"
            "v_bfe_u32 v49, v39, 1, 3\n\tv_xor_b32 v50, v49, v37\n\tv_mul_i32_i24 v48, 0xa00, v48\n\ts_bfe_u32 s63, %[b], 0x10002\n\ts_mul_i32 s63, s63, 0x500\n\ts_lshl_b32 s60, s64, 8\n\t"
            "v_mov_b32 v51, v36\n\tv_lshl_add_u32 v36, v50, 4, v48\n\ts_add_i32 s60, s60, s63\n\ts_lshl_b32 s65, s72, 7\n\ts_add_i32 m0, s65, 38912\n\ts_mov_b32 s38, 0x280000\n\t"
            "s_mov_b32 s39, 0x20000\n\tv_add_u32 v38, 0x9800, v36\n\ts_cmpk_lt_u32 s69, 0x100\n\ts_cbranch_scc0 1f\n\tbuffer_load_dwordx4 v36, s[36:39], s60 offen lds\n\tbuffer_load_dwordx4 v38, s[36:39], s60 offen offset:2048 lds\n\t"
            "1:\ns_barrier\n\ts_load_dwordx2 s[44:45], %[k], 0x38\n\ts_load_dwordx2 s[48:49], %[k], 0x10\n\ts_load_dwordx2 s[52:53], %[k], 0x40\n\tv_add_u32 v39, 8, v39\n\t"
            "v_and_b32 v48, 0x70, v39\n\tv_bfe_u32 v49, v39, 2, 2\n\tv_add3_u32 v48, v48, v49, v47\n\tv_xor_b32 v50, 4, v50\n\tv_mul_i32_i24 v48, 0xa00, v48\n\tv_lshl_add_u32 v37, v50, 4, v48\n\t"
            "v_subrev_u32 v37, 0x400, v37\n\tv_add_u32 v39, 0x9800, v37\n\ts_addk_i32 s66, 14336\n\ts_bfe_u32 s59, %[b], 0x20001\n\ts_mul_i32 s59, s59, 5\n\ts_add_i32 s59, s59, s71\n\t"
            "s_mov_b32 s46, 3432448\n\ts_mov_b32 s47, 0x20000\n\ts_mov_b32 s54, 0x280000\n\ts_mov_b32 s55, 0x20000\n\tv_and_b32 v52, 63, %[t]\n\tv_bfe_u32 v53, %[t], 4, 2\n\t"
            "v_and_b32 v54, 15, %[t]\n\tv_and_b32 v55, 3, %[t]\n\tv_bfe_u32 v56, %[t], 2, 2\n\tv_lshl_add_u32 v57, v55, 2, v56\n\ts_lshr_b32 s69, s69, 6\n\ts_cmp_lt_u32 s69, 4\n\t"
            "s_cbranch_scc0 .LB\n\ts_branch .LA\n\t.LF:\ns_load_dwordx2 s[72:73], %[k], 0x18\n\ts_and_b32 s71, %[b], 1\n\ts_lshl1_add_u32 s71, s70, s71\n\t"
            "s_mul_i32 s71, s71, 40\n\ts_sub_u32 s71, s71, 2000\n\tv_lshrrev_b32 v2, 4, %[t]\n\tv_add_u32 v2, s71, v2\n\tv_add_u32 v3, 32, v2\n\tv_min_u32 v2, 0x1ff, v2\n\t"
            "v_min_u32 v3, 0x1ff, v3\n\tv_and_b32 v4, 15, %[t]\n\tv_lshlrev_b32 v4, 4, v4\n\ts_bfe_u32 s71, %[b], 0x10001\n\ts_mul_i32 s71, s71, 0x140000\n\ts_bfe_u32 s74, %[b], 0x10002\n\t"
            "s_mul_i32 s74, s74, 0x500\n\ts_add_u32 s71, s71, s74\n\tv_add_u32 v5, s71, v4\n\ts_movk_i32 s71, 0xa00\n\tv_mad_u32_u24 v2, v2, s71, v5\n\tv_mad_u32_u24 v3, v3, s71, v5\n\t"
            "s_waitcnt lgkmcnt(0)\n\tglobal_load_dwordx4 v[104:107], v4, s[72:73] offset:0\n\tglobal_load_dwordx4 v[108:111], v4, s[72:73] offset:256\n\tglobal_load_dwordx4 v[112:115], v4, s[72:73] offset:512\n\tglobal_load_dwordx4 v[116:119], v4, s[72:73] offset:768\n\tglobal_load_dwordx4 v[120:123], v4, s[72:73] offset:1024\n\t"
            "s_cmpk_ge_u32 s69, 0x80\n\ts_cbranch_scc1 1f\n\tglobal_load_dwordx4 v[84:87], v3, s[36:37] offset:0\n\tglobal_load_dwordx4 v[88:91], v3, s[36:37] offset:256\n\tglobal_load_dwordx4 v[92:95], v3, s[36:37] offset:512\n\tglobal_load_dwordx4 v[96:99], v3, s[36:37] offset:768\n\t"
            "global_load_dwordx4 v[100:103], v3, s[36:37] offset:1024\n\t1:\nglobal_load_dwordx4 v[64:67], v2, s[36:37] offset:0\n\tglobal_load_dwordx4 v[68:71], v2, s[36:37] offset:256\n\tglobal_load_dwordx4 v[72:75], v2, s[36:37] offset:512\n\tglobal_load_dwordx4 v[76:79], v2, s[36:37] offset:768\n\t"
            "global_load_dwordx4 v[80:83], v2, s[36:37] offset:1024\n\ts_branch .Lout\n\t.LA:\n"
            "v_lshrrev_b32 v2, 1, v54\n\tv_xor_b32 v2, v2, v53\n\tv_lshlrev_b32 v3, 7, v54\n\tv_lshl_add_u32 v2, v2, 4, v3\n\tv_xor_b32 v3, 64, v2\n\tv_add_u32 v4, 38912, v2\n\t"
            "v_add_u32 v5, 38912, v3\n\tv_add_u32 v10, 77824, v2\n\tv_add_u32 v11, 77824, v3\n\tv_mov_b32 v12, 159872\n\tv_cmp_eq_u32 vcc, 0, v54\n\tv_mov_b32 v60, 0\n\t"
            "v_mov_b32 v61, 0\n\tv_mov_b32 v62, 0x3f803f80\n\tv_lshlrev_b32 v63, 3, v52\n\tv_cndmask_b32 v6, v2, v12, vcc\n\tv_cndmask_b32 v7, v3, v12, vcc\n\tv_cndmask_b32 v8, v4, v12, vcc\n\t"
            "v_cndmask_b32 v9, v5, v12, vcc\n\tv_cndmask_b32 v10, v10, v12, vcc\n\tv_cndmask_b32 v11, v11, v12, vcc\n\tv_lshlrev_b32 v64, 2, v52\n\tv_lshrrev_b32 v65, 2, v52\n\tv_cmp_eq_u32 vcc, 8, v65\n\t"
            "v_add_u32 v66, 161088, v63\n\tv_add_u32 v64, 159744, v64\n\tv_cndmask_b32 v62, 0, v62, vcc\n\ts_cmp_lg_u32 s69, 1\n\ts_cbranch_scc1 1f\n\tds_write_b32 v64, v62\n\t"
            "ds_write_b64 v66, v[60:61]\n\tds_write_b64 v66, v[60:61] offset:576\n\t1:\nv_bfe_u32 v12, v52, 3, 2\n\tv_xor_b32 v12, s69, v12\n\tv_lshlrev_b32 v13, 7, v56\n\t"
            "v_lshl_add_u32 v13, v55, 3, v13\n\tv_lshl_add_u32 v12, v12, 5, v13\n\tv_mul_u32_u24 v13, 0xc00, v53\n\ts_movk_i32 s72, 14336\n\tv_add3_u32 v12, v12, v13, s72\n\tv_add_u32 v13, 38912, v12\n\t"
            "s_mov_b32 s72, 160000\n\tv_lshl_add_u32 v14, v54, 2, s72\n\tv_lshl_add_u32 v15, v53, 4, s72\n\ts_cmp_eq_u32 s69, 3\n\ts_cselect_b32 s74, 1, 0\n\ts_cselect_b64 s[76:77], -1, 0\n\t"
            "s_cselect_b32 s75, 4, 0\n\ts_add_i32 s72, s69, 1\n\ts_bfe_u32 s72, s72, 0x10001\n\ts_xor_b32 s72, s72, 1\n\ts_lshr_b32 s73, s69, 1\n\ts_lshl1_add_u32 s78, s72, s73\n\t"
            "s_lshl2_add_u32 s78, s78, 159776\n\tv_mov_b32 v34, s78\n\ts_lshl2_add_u32 s78, s69, 159744\n\tv_mov_b32 v33, s78\n\ts_mul_i32 s78, s59, 163840\n\ts_lshl_b32 s79, s58, 5\n\t"
            "s_add_i32 s78, s78, s79\n\ts_lshl_b32 s79, s69, 10\n\ts_add_i32 s78, s78, s79\n\ts_add_i32 s78, s78, 155648\n\tv_lshl_add_u32 v16, v52, 4, s78\n\tv_and_b32 v215, 1, v53\n\t"
            "v_lshl_add_u32 v215, v215, 30, v16\n\ts_lshl_b32 s79, s74, 11\n\ts_add_i32 s78, s78, s79\n\ts_sub_i32 s78, s78, 0x400\n\ts_cmp_lg_u32 s57, s75\n\ts_cselect_b32 s79, s72, 0\n\t"
            "s_cmp_lg_u32 s79, 0\n\ts_cselect_b32 s78, s78, 0x40000000\n\tv_lshl_add_u32 v17, v52, 4, s78\n\ts_sub_i32 s78, s69, s57\n\ts_add_i32 s79, s78, 5\n\ts_min_u32 s78, s78, s79\n\t"
            "v_cmp_gt_u32 s[80:81], s78, v53\n\tv_cmp_gt_u32 s[82:83], s78, v56\n\ts_mov_b32 s14, 0x33363333\n\ts_mov_b32 s15, 0x36663366\n\ts_mov_b32 s16, 0x666c6666\n\ts_mov_b32 s17, 0x6ccc66cc\n\t"
            "s_mov_b32 s18, 0x33333333\n\ts_mov_b32 s19, 0x33333333\n\ts_xor_b64 s[80:81], s[80:81], s[76:77]\n\ts_xor_b64 s[82:83], s[82:83], s[76:77]\n\ts_andn2_b64 vcc, s[80:81], s[18:19]\n\tv_cndmask_b32 v18, 0, -1, vcc\n\t"
            "s_and_b64 vcc, s[80:81], s[18:19]\n\tv_cndmask_b32 v19, 0, -1, vcc\n\ts_nor_b64 vcc, s[80:81], s[18:19]\n\tv_cndmask_b32 v20, 0, -1, vcc\n\ts_andn2_b64 vcc, s[18:19], s[80:81]\n\tv_cndmask_b32 v21, 0, -1, vcc\n\t"
            "v_cndmask_b32 v22, 0, -1, s[82:83]\n\tv_cndmask_b32 v23, -1, 0, s[82:83]\n\tv_sub_u32 v58, v57, v53\n\tv_add_u32 v58, 4, v58\n\tv_and_b32 v58, 28, v58\n\tv_lshlrev_b32 v58, 1, v58\n\t"
            "v_mov_b32 v24, -1\n\tv_mov_b32 v25, 0\n\tv_lshlrev_b64 v[24:25], v58, v[24:25]\n\tv_lshlrev_b64 v[24:25], v58, v[24:25]\n\tv_sub_u32 v58, 32, v58\n\tv_min_u32 v58, 16, v58\n\t"
            "v_lshrrev_b32 v26, v58, -1\n\tv_lshrrev_b32 v26, v58, v26\n\ts_lshl1_add_u32 s78, s69, s74\n\ts_xor_b32 s79, s78, 1\n\ts_mul_i32 s78, s78, 0xc00\n\ts_mul_i32 s79, s79, 0xc00\n\t"
            "s_addk_i32 s78, 14336\n\ts_addk_i32 s79, 14336\n\tv_add_u32 v28, s78, v63\n\tv_add_u32 v29, s79, v63\n\ts_cmp_eq_u32 s69, s57\n\ts_addc_u32 s78, s69, 0\n\t"
            "s_add_i32 s79, s69, 1\n\ts_cmp_eq_u32 s79, s57\n\ts_subb_u32 s79, s79, 0\n\ts_cmp_eq_u32 s74, 1\n\ts_cselect_b32 s80, s79, s78\n\ts_cselect_b32 s81, s78, s79\n\t"
            "v_lshlrev_b32 v58, 2, v53\n\tv_lshl_add_u32 v58, v54, 4, v58\n\ts_cmp_gt_u32 s80, s57\n\ts_subb_u32 s82, s80, 0\n\ts_lshl2_add_u32 s82, s82, 159760\n\tv_mov_b32 v59, s82\n\t"
            "s_lshl_b32 s82, s80, 1\n\ts_max_u32 s82, s82, 1\n\ts_mul_i32 s82, s82, 0xc00\n\ts_addk_i32 s82, 11264\n\tv_add_u32 v30, s82, v58\n\ts_cmp_gt_u32 s81, s57\n\t"
            "s_subb_u32 s82, s81, 0\n\ts_lshl2_add_u32 s82, s82, 159760\n\tv_mov_b32 v60, s82\n\ts_lshl_b32 s82, s81, 1\n\ts_max_u32 s82, s82, 1\n\ts_mul_i32 s82, s82, 0xc00\n\t"
            "s_addk_i32 s82, 11264\n\tv_add_u32 v31, s82, v58\n\tv_cndmask_b32 v32, v59, v60, s[18:19]\n\tv_mov_b32 v35, 1\n\ts_waitcnt lgkmcnt(0)\n\ts_load_dword s67, s[44:45], 0x0\n\t"
            "s_mov_b32 s20, 0\n\ts_mov_b32 s21, 40960\n\ts_mov_b32 s22, 81920\n\ts_mov_b32 s23, 122880\n\ts_cmp_ge_u32 s56, 2\n\ts_cselect_b64 vcc, -1, 0\n\t"
            "s_add_u32 m0, s65, 38912\n\ts_nop 0\n\tbuffer_load_dwordx4 v37, s[36:39], s60 offen offset:1024 lds\n\ts_cbranch_vccnz 1f\n\tbuffer_load_dwordx4 v39, s[36:39], s60 offen offset:3072 lds\n\t1:\n"
            "s_cmp_eq_u32 s64, 4\n\ts_cselect_b32 s72, -1280, 0\n\ts_add_i32 s72, s72, 256\n\ts_bitcmp1_b32 31, s64\n\ts_cselect_b32 s72, s72, 128\n\ts_mul_i32 s73, s72, 1920\n\t"
            "s_add_u32 s60, s60, s72\n\ts_add_u32 m0, s66, 77824\n\ts_add_u32 s61, s61, s73\n\tbuffer_load_dwordx4 v40, s[40:43], s61 offen lds\n\tbuffer_load_dwordx4 v41, s[40:43], s61 offen offset:1024 lds\n\tbuffer_load_dwordx4 v42, s[40:43], s61 offen offset:2048 lds\n\t"
            "buffer_load_dwordx4 v43, s[40:43], s61 offen offset:3072 lds\n\ts_addk_i32 m0, 0x1000\n\ts_nop 0\n\tbuffer_load_dwordx4 v44, s[40:43], s61 offen lds\n\tbuffer_load_dwordx4 v45, s[40:43], s61 offen offset:1024 lds\n\ts_add_u32 m0, s65, 77824\n\t"
            "s_nop 0\n\tbuffer_load_dwordx4 v36, s[36:39], s60 offen lds\n\tbuffer_load_dwordx4 v37, s[36:39], s60 offen offset:1024 lds\n\tbuffer_load_dwordx4 v38, s[36:39], s60 offen offset:2048 lds\n\ts_cbranch_vccnz 1f\n\tbuffer_load_dwordx4 v39, s[36:39], s60 offen offset:3072 lds\n\t"
            "1:\ns_mov_b32 s68, 0x200000\n\ts_waitcnt lgkmcnt(0)\n\ts_mul_i32 s67, s67, 0x19660d\n\ts_add_u32 s67, s67, 0x3c6ef35f\n\ts_cbranch_vccz 1f\n\t"
            "s_waitcnt vmcnt(9)\n\t1:\ns_waitcnt vmcnt(10)\n\ts_barrier\n\t"
            "ds_read_b64_tr_b16 v[176:177], v13 offset:0\n\tds_read_b64_tr_b16 v[178:179], v13 offset:512\n\tds_read_b128 v[120:123], v8\n\tds_read_b128 v[124:127], v4 offset:2048\n\tds_read_b128 v[128:131], v4 offset:4096\n\tds_read_b128 v[132:135], v4 offset:6144\n\t"
            "ds_read_b128 v[136:139], v4 offset:8192\n\tds_read_b128 v[140:143], v4 offset:10240\n\tds_read_b128 v[144:147], v4 offset:12288\n\tds_read_b64_tr_b16 v[180:181], v13 offset:1024\n\tds_read_b64_tr_b16 v[182:183], v13 offset:1536\n\tds_read_b64_tr_b16 v[184:185], v13 offset:2048\n\t"
            "ds_read_b64_tr_b16 v[186:187], v13 offset:2560\n\ts_waitcnt lgkmcnt(10)\n\tv_mfma_f32_16x16x32_bf16 v[36:39], v[176:179], v[120:123], 0\n\ts_waitcnt lgkmcnt(9)\n\tv_mfma_f32_16x16x32_bf16 v[48:51], v[176:179], v[124:127], 0\n\ts_waitcnt lgkmcnt(8)\n\t"
            "v_mfma_f32_16x16x32_bf16 v[60:63], v[176:179], v[128:131], 0\n\tds_read_b64_tr_b16 v[188:189], v13 offset:12288\n\tds_read_b64_tr_b16 v[190:191], v13 offset:12800\n\ts_waitcnt lgkmcnt(9)\n\tv_mfma_f32_16x16x32_bf16 v[72:75], v[176:179], v[132:135], 0\n\tds_read_b128 v[148:151], v9\n\t"
            "s_waitcnt lgkmcnt(9)\n\tv_mfma_f32_16x16x32_bf16 v[84:87], v[176:179], v[136:139], 0\n\tds_read_b128 v[152:155], v5 offset:2048\n\ts_waitcnt lgkmcnt(9)\n\tv_mfma_f32_16x16x32_bf16 v[96:99], v[176:179], v[140:143], 0\n\tds_read_b128 v[156:159], v5 offset:4096\n\t"
            "s_waitcnt lgkmcnt(9)\n\tv_mfma_f32_16x16x32_bf16 v[108:111], v[176:179], v[144:147], 0\n\tds_read_b128 v[160:163], v5 offset:6144\n\ts_waitcnt lgkmcnt(8)\n\tv_mfma_f32_16x16x32_bf16 v[40:43], v[180:183], v[120:123], 0\n\tds_read_b128 v[164:167], v5 offset:8192\n\t"
            "v_mfma_f32_16x16x32_bf16 v[52:55], v[180:183], v[124:127], 0\n\tds_read_b128 v[168:171], v5 offset:10240\n\tv_mfma_f32_16x16x32_bf16 v[64:67], v[180:183], v[128:131], 0\n\tds_read_b128 v[172:175], v5 offset:12288\n\tv_mfma_f32_16x16x32_bf16 v[76:79], v[180:183], v[132:135], 0\n\tds_read_b64_tr_b16 v[192:193], v13 offset:13312\n\t"
            "ds_read_b64_tr_b16 v[194:195], v13 offset:13824\n\tv_mfma_f32_16x16x32_bf16 v[88:91], v[180:183], v[136:139], 0\n\tds_read_b64_tr_b16 v[196:197], v13 offset:14336\n\tds_read_b64_tr_b16 v[198:199], v13 offset:14848\n\tv_mfma_f32_16x16x32_bf16 v[100:103], v[180:183], v[140:143], 0\n\tv_mfma_f32_16x16x32_bf16 v[112:115], v[180:183], v[144:147], 0\n\t"
            "s_waitcnt lgkmcnt(13)\n\tv_mfma_f32_16x16x32_bf16 v[44:47], v[120:123], v[184:187], 0\n\tv_mfma_f32_16x16x32_bf16 v[56:59], v[124:127], v[184:187], 0\n\tv_mfma_f32_16x16x32_bf16 v[68:71], v[128:131], v[184:187], 0\n\tv_mfma_f32_16x16x32_bf16 v[80:83], v[132:135], v[184:187], 0\n\tv_mfma_f32_16x16x32_bf16 v[92:95], v[136:139], v[184:187], 0\n\t"
            "v_mfma_f32_16x16x32_bf16 v[104:107], v[140:143], v[184:187], 0\n\tv_mfma_f32_16x16x32_bf16 v[116:119], v[144:147], v[184:187], 0\n\ts_waitcnt lgkmcnt(10)\n\tv_mfma_f32_16x16x32_bf16 v[36:39], v[188:191], v[148:151], v[36:39]\n\ts_waitcnt lgkmcnt(9)\n\tv_mfma_f32_16x16x32_bf16 v[48:51], v[188:191], v[152:155], v[48:51]\n\t"
            "s_waitcnt lgkmcnt(8)\n\tv_mfma_f32_16x16x32_bf16 v[60:63], v[188:191], v[156:159], v[60:63]\n\ts_waitcnt lgkmcnt(7)\n\tv_mfma_f32_16x16x32_bf16 v[72:75], v[188:191], v[160:163], v[72:75]\n\ts_waitcnt lgkmcnt(6)\n\tv_mfma_f32_16x16x32_bf16 v[84:87], v[188:191], v[164:167], v[84:87]\n\t"
            "s_waitcnt lgkmcnt(5)\n\tv_mfma_f32_16x16x32_bf16 v[96:99], v[188:191], v[168:171], v[96:99]\n\ts_waitcnt lgkmcnt(4)\n\tv_mfma_f32_16x16x32_bf16 v[108:111], v[188:191], v[172:175], v[108:111]\n\ts_waitcnt lgkmcnt(2)\n\tv_mfma_f32_16x16x32_bf16 v[40:43], v[192:195], v[148:151], v[40:43]\n\t"
            "v_mfma_f32_16x16x32_bf16 v[52:55], v[192:195], v[152:155], v[52:55]\n\tv_mfma_f32_16x16x32_bf16 v[64:67], v[192:195], v[156:159], v[64:67]\n\tv_mfma_f32_16x16x32_bf16 v[76:79], v[192:195], v[160:163], v[76:79]\n\ts_waitcnt vmcnt(0) lgkmcnt(0)\n\ts_barrier\n\t"
            "ds_read_b64_tr_b16 v[176:177], v13 offset:38912\n\tds_read_b64_tr_b16 v[178:179], v13 offset:39424\n\tv_mfma_f32_16x16x32_bf16 v[88:91], v[192:195], v[164:167], v[88:91]\n\tds_read_b128 v[120:123], v10\n\tv_mfma_f32_16x16x32_bf16 v[100:103], v[192:195], v[168:171], v[100:103]\n\tds_read_b128 v[124:127], v4 offset:40960\n\t"
            "v_mfma_f32_16x16x32_bf16 v[112:115], v[192:195], v[172:175], v[112:115]\n\tds_read_b128 v[128:131], v4 offset:43008\n\tv_mfma_f32_16x16x32_bf16 v[44:47], v[148:151], v[196:199], v[44:47]\n\tds_read_b128 v[132:135], v4 offset:45056\n\tv_mfma_f32_16x16x32_bf16 v[56:59], v[152:155], v[196:199], v[56:59]\n\tds_read_b128 v[136:139], v4 offset:47104\n\t"
            "v_mfma_f32_16x16x32_bf16 v[68:71], v[156:159], v[196:199], v[68:71]\n\tds_read_b128 v[140:143], v4 offset:49152\n\tv_mfma_f32_16x16x32_bf16 v[80:83], v[160:163], v[196:199], v[80:83]\n\tds_read_b128 v[144:147], v4 offset:51200\n\tv_mfma_f32_16x16x32_bf16 v[92:95], v[164:167], v[196:199], v[92:95]\n\tds_read_b64_tr_b16 v[180:181], v13 offset:39936\n\t"
            "ds_read_b64_tr_b16 v[182:183], v13 offset:40448\n\tv_mfma_f32_16x16x32_bf16 v[104:107], v[168:171], v[196:199], v[104:107]\n\tds_read_b64_tr_b16 v[184:185], v13 offset:40960\n\tds_read_b64_tr_b16 v[186:187], v13 offset:41472\n\tv_mfma_f32_16x16x32_bf16 v[116:119], v[172:175], v[196:199], v[116:119]\n\ts_waitcnt lgkmcnt(10)\n\t"
            "v_mfma_f32_16x16x32_bf16 v[36:39], v[176:179], v[120:123], v[36:39]\n\ts_waitcnt lgkmcnt(9)\n\tv_mfma_f32_16x16x32_bf16 v[48:51], v[176:179], v[124:127], v[48:51]\n\ts_waitcnt lgkmcnt(8)\n\tv_mfma_f32_16x16x32_bf16 v[60:63], v[176:179], v[128:131], v[60:63]\n\tds_read_b64_tr_b16 v[188:189], v13 offset:51200\n\t"
            "ds_read_b64_tr_b16 v[190:191], v13 offset:51712\n\ts_waitcnt lgkmcnt(9)\n\tv_mfma_f32_16x16x32_bf16 v[72:75], v[176:179], v[132:135], v[72:75]\n\tds_read_b128 v[148:151], v11\n\ts_waitcnt lgkmcnt(9)\n\tv_mfma_f32_16x16x32_bf16 v[84:87], v[176:179], v[136:139], v[84:87]\n\t"
            "ds_read_b128 v[152:155], v5 offset:40960\n\ts_waitcnt lgkmcnt(9)\n\tv_mfma_f32_16x16x32_bf16 v[96:99], v[176:179], v[140:143], v[96:99]\n\tds_read_b128 v[156:159], v5 offset:43008\n\ts_waitcnt lgkmcnt(9)\n\tv_mfma_f32_16x16x32_bf16 v[108:111], v[176:179], v[144:147], v[108:111]\n\t"
            "ds_read_b128 v[160:163], v5 offset:45056\n\ts_waitcnt lgkmcnt(8)\n\tv_mfma_f32_16x16x32_bf16 v[40:43], v[180:183], v[120:123], v[40:43]\n\tds_read_b128 v[164:167], v5 offset:47104\n\tv_mfma_f32_16x16x32_bf16 v[52:55], v[180:183], v[124:127], v[52:55]\n\tds_read_b128 v[168:171], v5 offset:49152\n\t"
            "v_mfma_f32_16x16x32_bf16 v[64:67], v[180:183], v[128:131], v[64:67]\n\tds_read_b128 v[172:175], v5 offset:51200\n\tv_mfma_f32_16x16x32_bf16 v[76:79], v[180:183], v[132:135], v[76:79]\n\tds_read_b64_tr_b16 v[192:193], v13 offset:52224\n\tds_read_b64_tr_b16 v[194:195], v13 offset:52736\n\tv_mfma_f32_16x16x32_bf16 v[88:91], v[180:183], v[136:139], v[88:91]\n\t"
            "ds_read_b64_tr_b16 v[196:197], v13 offset:53248\n\tds_read_b64_tr_b16 v[198:199], v13 offset:53760\n\tv_mfma_f32_16x16x32_bf16 v[100:103], v[180:183], v[140:143], v[100:103]\n\tv_mfma_f32_16x16x32_bf16 v[112:115], v[180:183], v[144:147], v[112:115]\n\ts_waitcnt lgkmcnt(13)\n\tv_mfma_f32_16x16x32_bf16 v[44:47], v[120:123], v[184:187], v[44:47]\n\t"
            "v_mfma_f32_16x16x32_bf16 v[56:59], v[124:127], v[184:187], v[56:59]\n\tv_mfma_f32_16x16x32_bf16 v[68:71], v[128:131], v[184:187], v[68:71]\n\tv_mfma_f32_16x16x32_bf16 v[80:83], v[132:135], v[184:187], v[80:83]\n\tv_mfma_f32_16x16x32_bf16 v[92:95], v[136:139], v[184:187], v[92:95]\n\tv_mfma_f32_16x16x32_bf16 v[104:107], v[140:143], v[184:187], v[104:107]\n\tv_mfma_f32_16x16x32_bf16 v[116:119], v[144:147], v[184:187], v[116:119]\n\t"
            "s_waitcnt lgkmcnt(10)\n\tv_mfma_f32_16x16x32_bf16 v[36:39], v[188:191], v[148:151], v[36:39]\n\ts_waitcnt lgkmcnt(9)\n\tv_mfma_f32_16x16x32_bf16 v[48:51], v[188:191], v[152:155], v[48:51]\n\ts_waitcnt lgkmcnt(8)\n\tv_mfma_f32_16x16x32_bf16 v[60:63], v[188:191], v[156:159], v[60:63]\n\t"
            "s_waitcnt lgkmcnt(7)\n\tv_mfma_f32_16x16x32_bf16 v[72:75], v[188:191], v[160:163], v[72:75]\n\ts_waitcnt lgkmcnt(6)\n\tv_mfma_f32_16x16x32_bf16 v[84:87], v[188:191], v[164:167], v[84:87]\n\ts_waitcnt lgkmcnt(5)\n\tv_mfma_f32_16x16x32_bf16 v[96:99], v[188:191], v[168:171], v[96:99]\n\t"
            "s_waitcnt lgkmcnt(4)\n\tv_mfma_f32_16x16x32_bf16 v[108:111], v[188:191], v[172:175], v[108:111]\n\ts_waitcnt lgkmcnt(2)\n\tv_mfma_f32_16x16x32_bf16 v[40:43], v[192:195], v[148:151], v[40:43]\n\tv_mfma_f32_16x16x32_bf16 v[52:55], v[192:195], v[152:155], v[52:55]\n\tv_mfma_f32_16x16x32_bf16 v[64:67], v[192:195], v[156:159], v[64:67]\n\t"
            "v_mfma_f32_16x16x32_bf16 v[76:79], v[192:195], v[160:163], v[76:79]\n\ts_waitcnt lgkmcnt(0)\n\ts_barrier\n\t"
            "ds_read_b64_tr_b16 v[176:177], v12 offset:0\n\tds_read_b64_tr_b16 v[178:179], v12 offset:512\n\tv_mfma_f32_16x16x32_bf16 v[88:91], v[192:195], v[164:167], v[88:91]\n\tds_read_b128 v[120:123], v6\n\tv_mfma_f32_16x16x32_bf16 v[100:103], v[192:195], v[168:171], v[100:103]\n\tds_read_b128 v[124:127], v2 offset:2048\n\t"
            "v_mfma_f32_16x16x32_bf16 v[112:115], v[192:195], v[172:175], v[112:115]\n\tds_read_b128 v[128:131], v2 offset:4096\n\tv_mfma_f32_16x16x32_bf16 v[44:47], v[148:151], v[196:199], v[44:47]\n\tds_read_b128 v[132:135], v2 offset:6144\n\tv_mfma_f32_16x16x32_bf16 v[56:59], v[152:155], v[196:199], v[56:59]\n\tds_read_b128 v[136:139], v2 offset:8192\n\t"
            "v_mfma_f32_16x16x32_bf16 v[68:71], v[156:159], v[196:199], v[68:71]\n\tds_read_b128 v[140:143], v2 offset:10240\n\tv_mfma_f32_16x16x32_bf16 v[80:83], v[160:163], v[196:199], v[80:83]\n\tds_read_b128 v[144:147], v2 offset:12288\n\tv_mfma_f32_16x16x32_bf16 v[92:95], v[164:167], v[196:199], v[92:95]\n\tds_read_b64_tr_b16 v[180:181], v12 offset:1024\n\t"
            "ds_read_b64_tr_b16 v[182:183], v12 offset:1536\n\tv_mfma_f32_16x16x32_bf16 v[104:107], v[168:171], v[196:199], v[104:107]\n\tds_read_b64_tr_b16 v[184:185], v12 offset:2048\n\tds_read_b64_tr_b16 v[186:187], v12 offset:2560\n\tv_mfma_f32_16x16x32_bf16 v[116:119], v[172:175], v[196:199], v[116:119]\n\ts_waitcnt lgkmcnt(10)\n\t"
            "v_mfma_f32_16x16x32_bf16 v[36:39], v[176:179], v[120:123], v[36:39]\n\ts_waitcnt lgkmcnt(9)\n\tv_mfma_f32_16x16x32_bf16 v[48:51], v[176:179], v[124:127], v[48:51]\n\ts_waitcnt lgkmcnt(8)\n\tv_mfma_f32_16x16x32_bf16 v[60:63], v[176:179], v[128:131], v[60:63]\n\tds_read_b64_tr_b16 v[188:189], v12 offset:12288\n\t"
            "ds_read_b64_tr_b16 v[190:191], v12 offset:12800\n\ts_waitcnt lgkmcnt(9)\n\tv_mfma_f32_16x16x32_bf16 v[72:75], v[176:179], v[132:135], v[72:75]\n\tds_read_b128 v[148:151], v7\n\ts_waitcnt lgkmcnt(9)\n\tv_mfma_f32_16x16x32_bf16 v[84:87], v[176:179], v[136:139], v[84:87]\n\t"
            "ds_read_b128 v[152:155], v3 offset:2048\n\ts_waitcnt lgkmcnt(9)\n\tv_mfma_f32_16x16x32_bf16 v[96:99], v[176:179], v[140:143], v[96:99]\n\tds_read_b128 v[156:159], v3 offset:4096\n\ts_waitcnt lgkmcnt(9)\n\tv_mfma_f32_16x16x32_bf16 v[108:111], v[176:179], v[144:147], v[108:111]\n\t"
            "ds_read_b128 v[160:163], v3 offset:6144\n\ts_waitcnt lgkmcnt(8)\n\tv_mfma_f32_16x16x32_bf16 v[40:43], v[180:183], v[120:123], v[40:43]\n\tds_read_b128 v[164:167], v3 offset:8192\n\tv_mfma_f32_16x16x32_bf16 v[52:55], v[180:183], v[124:127], v[52:55]\n\tds_read_b128 v[168:171], v3 offset:10240\n\t"
            "v_mfma_f32_16x16x32_bf16 v[64:67], v[180:183], v[128:131], v[64:67]\n\tds_read_b128 v[172:175], v3 offset:12288\n\tv_mfma_f32_16x16x32_bf16 v[76:79], v[180:183], v[132:135], v[76:79]\n\tds_read_b64_tr_b16 v[192:193], v12 offset:13312\n\tds_read_b64_tr_b16 v[194:195], v12 offset:13824\n\tv_mfma_f32_16x16x32_bf16 v[88:91], v[180:183], v[136:139], v[88:91]\n\t"
            "ds_read_b64_tr_b16 v[196:197], v12 offset:14336\n\tds_read_b64_tr_b16 v[198:199], v12 offset:14848\n\tv_mfma_f32_16x16x32_bf16 v[100:103], v[180:183], v[140:143], v[100:103]\n\tv_mfma_f32_16x16x32_bf16 v[112:115], v[180:183], v[144:147], v[112:115]\n\ts_waitcnt lgkmcnt(13)\n\tv_mfma_f32_16x16x32_bf16 v[44:47], v[120:123], v[184:187], v[44:47]\n\t"
            "v_mfma_f32_16x16x32_bf16 v[56:59], v[124:127], v[184:187], v[56:59]\n\tv_mfma_f32_16x16x32_bf16 v[68:71], v[128:131], v[184:187], v[68:71]\n\tv_mfma_f32_16x16x32_bf16 v[80:83], v[132:135], v[184:187], v[80:83]\n\tv_mfma_f32_16x16x32_bf16 v[92:95], v[136:139], v[184:187], v[92:95]\n\tv_mfma_f32_16x16x32_bf16 v[104:107], v[140:143], v[184:187], v[104:107]\n\tv_mfma_f32_16x16x32_bf16 v[116:119], v[144:147], v[184:187], v[116:119]\n\t"
            "s_waitcnt lgkmcnt(10)\n\tv_mfma_f32_16x16x32_bf16 v[36:39], v[188:191], v[148:151], v[36:39]\n\ts_waitcnt lgkmcnt(9)\n\tv_mfma_f32_16x16x32_bf16 v[48:51], v[188:191], v[152:155], v[48:51]\n\ts_waitcnt lgkmcnt(8)\n\tv_mfma_f32_16x16x32_bf16 v[60:63], v[188:191], v[156:159], v[60:63]\n\t"
            "s_waitcnt lgkmcnt(7)\n\tv_mfma_f32_16x16x32_bf16 v[72:75], v[188:191], v[160:163], v[72:75]\n\ts_waitcnt lgkmcnt(6)\n\tv_mfma_f32_16x16x32_bf16 v[84:87], v[188:191], v[164:167], v[84:87]\n\ts_waitcnt lgkmcnt(5)\n\tv_mfma_f32_16x16x32_bf16 v[96:99], v[188:191], v[168:171], v[96:99]\n\t"
            "s_waitcnt lgkmcnt(4)\n\tv_mfma_f32_16x16x32_bf16 v[108:111], v[188:191], v[172:175], v[108:111]\n\ts_waitcnt lgkmcnt(2)\n\tv_mfma_f32_16x16x32_bf16 v[40:43], v[192:195], v[148:151], v[40:43]\n\tv_mfma_f32_16x16x32_bf16 v[52:55], v[192:195], v[152:155], v[52:55]\n\tv_mfma_f32_16x16x32_bf16 v[64:67], v[192:195], v[156:159], v[64:67]\n\t"
            "v_mfma_f32_16x16x32_bf16 v[76:79], v[192:195], v[160:163], v[76:79]\n\ts_waitcnt lgkmcnt(0)\n\ts_barrier\n\t"
            "ds_read_b64_tr_b16 v[176:177], v13 offset:0\n\tds_read_b64_tr_b16 v[178:179], v13 offset:512\n\tv_mfma_f32_16x16x32_bf16 v[88:91], v[192:195], v[164:167], v[88:91]\n\tds_read_b128 v[120:123], v8\n\tv_mfma_f32_16x16x32_bf16 v[100:103], v[192:195], v[168:171], v[100:103]\n\tds_read_b128 v[124:127], v4 offset:2048\n\t"
            "v_mfma_f32_16x16x32_bf16 v[112:115], v[192:195], v[172:175], v[112:115]\n\tds_read_b128 v[128:131], v4 offset:4096\n\tv_mfma_f32_16x16x32_bf16 v[44:47], v[148:151], v[196:199], v[44:47]\n\tds_read_b128 v[132:135], v4 offset:6144\n\tv_mfma_f32_16x16x32_bf16 v[56:59], v[152:155], v[196:199], v[56:59]\n\tds_read_b128 v[136:139], v4 offset:8192\n\t"
            "v_mfma_f32_16x16x32_bf16 v[68:71], v[156:159], v[196:199], v[68:71]\n\tds_read_b128 v[140:143], v4 offset:10240\n\tv_mfma_f32_16x16x32_bf16 v[80:83], v[160:163], v[196:199], v[80:83]\n\tds_read_b128 v[144:147], v4 offset:12288\n\tv_mfma_f32_16x16x32_bf16 v[92:95], v[164:167], v[196:199], v[92:95]\n\tds_read_b64_tr_b16 v[180:181], v13 offset:1024\n\t"
            "ds_read_b64_tr_b16 v[182:183], v13 offset:1536\n\tv_mfma_f32_16x16x32_bf16 v[104:107], v[168:171], v[196:199], v[104:107]\n\tds_read_b64_tr_b16 v[184:185], v13 offset:2048\n\tds_read_b64_tr_b16 v[186:187], v13 offset:2560\n\tv_mfma_f32_16x16x32_bf16 v[116:119], v[172:175], v[196:199], v[116:119]\n\ts_waitcnt lgkmcnt(10)\n\t"
            "v_mfma_f32_16x16x32_bf16 v[36:39], v[176:179], v[120:123], v[36:39]\n\ts_waitcnt lgkmcnt(9)\n\tv_mfma_f32_16x16x32_bf16 v[48:51], v[176:179], v[124:127], v[48:51]\n\ts_waitcnt lgkmcnt(8)\n\tv_mfma_f32_16x16x32_bf16 v[60:63], v[176:179], v[128:131], v[60:63]\n\tds_read_b64_tr_b16 v[188:189], v13 offset:12288\n\t"
            "ds_read_b64_tr_b16 v[190:191], v13 offset:12800\n\ts_waitcnt lgkmcnt(9)\n\tv_mfma_f32_16x16x32_bf16 v[72:75], v[176:179], v[132:135], v[72:75]\n\tds_read_b128 v[148:151], v9\n\ts_waitcnt lgkmcnt(9)\n\tv_mfma_f32_16x16x32_bf16 v[84:87], v[176:179], v[136:139], v[84:87]\n\t"
            "ds_read_b128 v[152:155], v5 offset:2048\n\ts_waitcnt lgkmcnt(9)\n\tv_mfma_f32_16x16x32_bf16 v[96:99], v[176:179], v[140:143], v[96:99]\n\tds_read_b128 v[156:159], v5 offset:4096\n\ts_waitcnt lgkmcnt(9)\n\tv_mfma_f32_16x16x32_bf16 v[108:111], v[176:179], v[144:147], v[108:111]\n\t"
            "ds_read_b128 v[160:163], v5 offset:6144\n\ts_waitcnt lgkmcnt(8)\n\tv_mfma_f32_16x16x32_bf16 v[40:43], v[180:183], v[120:123], v[40:43]\n\tds_read_b128 v[164:167], v5 offset:8192\n\tv_mfma_f32_16x16x32_bf16 v[52:55], v[180:183], v[124:127], v[52:55]\n\tds_read_b128 v[168:171], v5 offset:10240\n\t"
            "v_mfma_f32_16x16x32_bf16 v[64:67], v[180:183], v[128:131], v[64:67]\n\tds_read_b128 v[172:175], v5 offset:12288\n\tv_mfma_f32_16x16x32_bf16 v[76:79], v[180:183], v[132:135], v[76:79]\n\tds_read_b64_tr_b16 v[192:193], v13 offset:13312\n\tds_read_b64_tr_b16 v[194:195], v13 offset:13824\n\tv_mfma_f32_16x16x32_bf16 v[88:91], v[180:183], v[136:139], v[88:91]\n\t"
            "ds_read_b64_tr_b16 v[196:197], v13 offset:14336\n\tds_read_b64_tr_b16 v[198:199], v13 offset:14848\n\tv_mfma_f32_16x16x32_bf16 v[100:103], v[180:183], v[140:143], v[100:103]\n\tv_mfma_f32_16x16x32_bf16 v[112:115], v[180:183], v[144:147], v[112:115]\n\ts_waitcnt lgkmcnt(13)\n\tv_mfma_f32_16x16x32_bf16 v[44:47], v[120:123], v[184:187], v[44:47]\n\t"
            "v_mfma_f32_16x16x32_bf16 v[56:59], v[124:127], v[184:187], v[56:59]\n\tv_mfma_f32_16x16x32_bf16 v[68:71], v[128:131], v[184:187], v[68:71]\n\tv_mfma_f32_16x16x32_bf16 v[80:83], v[132:135], v[184:187], v[80:83]\n\tv_mfma_f32_16x16x32_bf16 v[92:95], v[136:139], v[184:187], v[92:95]\n\tv_mfma_f32_16x16x32_bf16 v[104:107], v[140:143], v[184:187], v[104:107]\n\tv_mfma_f32_16x16x32_bf16 v[116:119], v[144:147], v[184:187], v[116:119]\n\t"
            "s_waitcnt lgkmcnt(10)\n\tv_mfma_f32_16x16x32_bf16 v[36:39], v[188:191], v[148:151], v[36:39]\n\ts_waitcnt lgkmcnt(9)\n\tv_mfma_f32_16x16x32_bf16 v[48:51], v[188:191], v[152:155], v[48:51]\n\ts_waitcnt lgkmcnt(8)\n\tv_mfma_f32_16x16x32_bf16 v[60:63], v[188:191], v[156:159], v[60:63]\n\t"
            "s_waitcnt lgkmcnt(7)\n\tv_mfma_f32_16x16x32_bf16 v[72:75], v[188:191], v[160:163], v[72:75]\n\ts_waitcnt lgkmcnt(6)\n\tv_mfma_f32_16x16x32_bf16 v[84:87], v[188:191], v[164:167], v[84:87]\n\ts_waitcnt lgkmcnt(5)\n\tv_mfma_f32_16x16x32_bf16 v[96:99], v[188:191], v[168:171], v[96:99]\n\t"
            "s_waitcnt lgkmcnt(4)\n\tv_mfma_f32_16x16x32_bf16 v[108:111], v[188:191], v[172:175], v[108:111]\n\ts_waitcnt lgkmcnt(2)\n\tv_mfma_f32_16x16x32_bf16 v[40:43], v[192:195], v[148:151], v[40:43]\n\tv_mfma_f32_16x16x32_bf16 v[52:55], v[192:195], v[152:155], v[52:55]\n\tv_mfma_f32_16x16x32_bf16 v[64:67], v[192:195], v[156:159], v[64:67]\n\t"
            "v_mfma_f32_16x16x32_bf16 v[76:79], v[192:195], v[160:163], v[76:79]\n\ts_waitcnt lgkmcnt(0)\n\ts_barrier\n\t"
            "ds_read_b64_tr_b16 v[176:177], v13 offset:38912\n\tds_read_b64_tr_b16 v[178:179], v13 offset:39424\n\tv_mfma_f32_16x16x32_bf16 v[88:91], v[192:195], v[164:167], v[88:91]\n\tds_read_b128 v[120:123], v10\n\tv_mfma_f32_16x16x32_bf16 v[100:103], v[192:195], v[168:171], v[100:103]\n\tds_read_b128 v[124:127], v4 offset:40960\n\t"
            "v_mfma_f32_16x16x32_bf16 v[112:115], v[192:195], v[172:175], v[112:115]\n\tds_read_b128 v[128:131], v4 offset:43008\n\tv_mfma_f32_16x16x32_bf16 v[44:47], v[148:151], v[196:199], v[44:47]\n\tds_read_b128 v[132:135], v4 offset:45056\n\tv_mfma_f32_16x16x32_bf16 v[56:59], v[152:155], v[196:199], v[56:59]\n\tds_read_b128 v[136:139], v4 offset:47104\n\t"
            "v_mfma_f32_16x16x32_bf16 v[68:71], v[156:159], v[196:199], v[68:71]\n\tds_read_b128 v[140:143], v4 offset:49152\n\tv_mfma_f32_16x16x32_bf16 v[80:83], v[160:163], v[196:199], v[80:83]\n\tds_read_b128 v[144:147], v4 offset:51200\n\tv_mfma_f32_16x16x32_bf16 v[92:95], v[164:167], v[196:199], v[92:95]\n\tds_read_b64_tr_b16 v[180:181], v13 offset:39936\n\t"
            "ds_read_b64_tr_b16 v[182:183], v13 offset:40448\n\tv_mfma_f32_16x16x32_bf16 v[104:107], v[168:171], v[196:199], v[104:107]\n\tds_read_b64_tr_b16 v[184:185], v13 offset:40960\n\tds_read_b64_tr_b16 v[186:187], v13 offset:41472\n\tv_mfma_f32_16x16x32_bf16 v[116:119], v[172:175], v[196:199], v[116:119]\n\ts_waitcnt lgkmcnt(10)\n\t"
            "v_mfma_f32_16x16x32_bf16 v[36:39], v[176:179], v[120:123], v[36:39]\n\ts_waitcnt lgkmcnt(9)\n\tv_mfma_f32_16x16x32_bf16 v[48:51], v[176:179], v[124:127], v[48:51]\n\ts_waitcnt lgkmcnt(8)\n\tv_mfma_f32_16x16x32_bf16 v[60:63], v[176:179], v[128:131], v[60:63]\n\tds_read_b64_tr_b16 v[188:189], v13 offset:51200\n\t"
            "ds_read_b64_tr_b16 v[190:191], v13 offset:51712\n\ts_waitcnt lgkmcnt(9)\n\tv_mfma_f32_16x16x32_bf16 v[72:75], v[176:179], v[132:135], v[72:75]\n\tds_read_b128 v[148:151], v11\n\ts_waitcnt lgkmcnt(9)\n\tv_mfma_f32_16x16x32_bf16 v[84:87], v[176:179], v[136:139], v[84:87]\n\t"
            "ds_read_b128 v[152:155], v5 offset:40960\n\ts_waitcnt lgkmcnt(9)\n\tv_mfma_f32_16x16x32_bf16 v[96:99], v[176:179], v[140:143], v[96:99]\n\tds_read_b128 v[156:159], v5 offset:43008\n\ts_waitcnt lgkmcnt(9)\n\tv_mfma_f32_16x16x32_bf16 v[108:111], v[176:179], v[144:147], v[108:111]\n\t"
            "ds_read_b128 v[160:163], v5 offset:45056\n\ts_waitcnt lgkmcnt(8)\n\tv_mfma_f32_16x16x32_bf16 v[40:43], v[180:183], v[120:123], v[40:43]\n\tds_read_b128 v[164:167], v5 offset:47104\n\tv_mfma_f32_16x16x32_bf16 v[52:55], v[180:183], v[124:127], v[52:55]\n\tds_read_b128 v[168:171], v5 offset:49152\n\t"
            "v_mfma_f32_16x16x32_bf16 v[64:67], v[180:183], v[128:131], v[64:67]\n\tds_read_b128 v[172:175], v5 offset:51200\n\tv_mfma_f32_16x16x32_bf16 v[76:79], v[180:183], v[132:135], v[76:79]\n\tds_read_b64_tr_b16 v[192:193], v13 offset:52224\n\tds_read_b64_tr_b16 v[194:195], v13 offset:52736\n\tv_mfma_f32_16x16x32_bf16 v[88:91], v[180:183], v[136:139], v[88:91]\n\t"
            "ds_read_b64_tr_b16 v[196:197], v13 offset:53248\n\tds_read_b64_tr_b16 v[198:199], v13 offset:53760\n\tv_mfma_f32_16x16x32_bf16 v[100:103], v[180:183], v[140:143], v[100:103]\n\tv_mfma_f32_16x16x32_bf16 v[112:115], v[180:183], v[144:147], v[112:115]\n\ts_waitcnt lgkmcnt(13)\n\tv_mfma_f32_16x16x32_bf16 v[44:47], v[120:123], v[184:187], v[44:47]\n\t"
            "v_mfma_f32_16x16x32_bf16 v[56:59], v[124:127], v[184:187], v[56:59]\n\tv_mfma_f32_16x16x32_bf16 v[68:71], v[128:131], v[184:187], v[68:71]\n\tv_mfma_f32_16x16x32_bf16 v[80:83], v[132:135], v[184:187], v[80:83]\n\tv_mfma_f32_16x16x32_bf16 v[92:95], v[136:139], v[184:187], v[92:95]\n\tv_mfma_f32_16x16x32_bf16 v[104:107], v[140:143], v[184:187], v[104:107]\n\tv_mfma_f32_16x16x32_bf16 v[116:119], v[144:147], v[184:187], v[116:119]\n\t"
            "s_waitcnt lgkmcnt(10)\n\tv_mfma_f32_16x16x32_bf16 v[36:39], v[188:191], v[148:151], v[36:39]\n\ts_waitcnt lgkmcnt(9)\n\tv_mfma_f32_16x16x32_bf16 v[48:51], v[188:191], v[152:155], v[48:51]\n\ts_waitcnt lgkmcnt(8)\n\tv_mfma_f32_16x16x32_bf16 v[60:63], v[188:191], v[156:159], v[60:63]\n\t"
            "s_waitcnt lgkmcnt(7)\n\tv_mfma_f32_16x16x32_bf16 v[72:75], v[188:191], v[160:163], v[72:75]\n\ts_waitcnt lgkmcnt(6)\n\tv_mfma_f32_16x16x32_bf16 v[84:87], v[188:191], v[164:167], v[84:87]\n\ts_waitcnt lgkmcnt(5)\n\tv_mfma_f32_16x16x32_bf16 v[96:99], v[188:191], v[168:171], v[96:99]\n\t"
            "s_waitcnt lgkmcnt(4)\n\tv_mfma_f32_16x16x32_bf16 v[108:111], v[188:191], v[172:175], v[108:111]\n\ts_waitcnt lgkmcnt(2)\n\tv_mfma_f32_16x16x32_bf16 v[40:43], v[192:195], v[148:151], v[40:43]\n\tv_mfma_f32_16x16x32_bf16 v[52:55], v[192:195], v[152:155], v[52:55]\n\tv_mfma_f32_16x16x32_bf16 v[64:67], v[192:195], v[156:159], v[64:67]\n\t"
            "v_mfma_f32_16x16x32_bf16 v[76:79], v[192:195], v[160:163], v[76:79]\n\ts_waitcnt lgkmcnt(0)\n\ts_barrier\n\t"
            "ds_read_b64_tr_b16 v[176:177], v12 offset:0\n\tds_read_b64_tr_b16 v[178:179], v12 offset:512\n\tv_mfma_f32_16x16x32_bf16 v[88:91], v[192:195], v[164:167], v[88:91]\n\tds_read_b128 v[120:123], v6\n\tv_mfma_f32_16x16x32_bf16 v[100:103], v[192:195], v[168:171], v[100:103]\n\tds_read_b128 v[124:127], v2 offset:2048\n\t"
            "v_mfma_f32_16x16x32_bf16 v[112:115], v[192:195], v[172:175], v[112:115]\n\tds_read_b128 v[128:131], v2 offset:4096\n\tv_mfma_f32_16x16x32_bf16 v[44:47], v[148:151], v[196:199], v[44:47]\n\tds_read_b128 v[132:135], v2 offset:6144\n\tv_mfma_f32_16x16x32_bf16 v[56:59], v[152:155], v[196:199], v[56:59]\n\tds_read_b128 v[136:139], v2 offset:8192\n\t"
            "v_mfma_f32_16x16x32_bf16 v[68:71], v[156:159], v[196:199], v[68:71]\n\tds_read_b128 v[140:143], v2 offset:10240\n\tv_mfma_f32_16x16x32_bf16 v[80:83], v[160:163], v[196:199], v[80:83]\n\tds_read_b128 v[144:147], v2 offset:12288\n\tv_mfma_f32_16x16x32_bf16 v[92:95], v[164:167], v[196:199], v[92:95]\n\tds_read_b64_tr_b16 v[180:181], v12 offset:1024\n\t"
            "ds_read_b64_tr_b16 v[182:183], v12 offset:1536\n\tv_mfma_f32_16x16x32_bf16 v[104:107], v[168:171], v[196:199], v[104:107]\n\tds_read_b64_tr_b16 v[184:185], v12 offset:2048\n\tds_read_b64_tr_b16 v[186:187], v12 offset:2560\n\tv_mfma_f32_16x16x32_bf16 v[116:119], v[172:175], v[196:199], v[116:119]\n\ts_waitcnt lgkmcnt(10)\n\t"
            "v_mfma_f32_16x16x32_bf16 v[36:39], v[176:179], v[120:123], v[36:39]\n\ts_waitcnt lgkmcnt(9)\n\tv_mfma_f32_16x16x32_bf16 v[48:51], v[176:179], v[124:127], v[48:51]\n\ts_waitcnt lgkmcnt(8)\n\tv_mfma_f32_16x16x32_bf16 v[60:63], v[176:179], v[128:131], v[60:63]\n\tds_read_b64_tr_b16 v[188:189], v12 offset:12288\n\t"
            "ds_read_b64_tr_b16 v[190:191], v12 offset:12800\n\ts_waitcnt lgkmcnt(9)\n\tv_mfma_f32_16x16x32_bf16 v[72:75], v[176:179], v[132:135], v[72:75]\n\tds_read_b128 v[148:151], v7\n\ts_waitcnt lgkmcnt(9)\n\tv_mfma_f32_16x16x32_bf16 v[84:87], v[176:179], v[136:139], v[84:87]\n\t"
            "ds_read_b128 v[152:155], v3 offset:2048\n\ts_waitcnt lgkmcnt(9)\n\tv_mfma_f32_16x16x32_bf16 v[96:99], v[176:179], v[140:143], v[96:99]\n\tds_read_b128 v[156:159], v3 offset:4096\n\ts_waitcnt lgkmcnt(9)\n\tv_mfma_f32_16x16x32_bf16 v[108:111], v[176:179], v[144:147], v[108:111]\n\t"
            "ds_read_b128 v[160:163], v3 offset:6144\n\ts_waitcnt lgkmcnt(8)\n\tv_mfma_f32_16x16x32_bf16 v[40:43], v[180:183], v[120:123], v[40:43]\n\tds_read_b128 v[164:167], v3 offset:8192\n\tv_mfma_f32_16x16x32_bf16 v[52:55], v[180:183], v[124:127], v[52:55]\n\tds_read_b128 v[168:171], v3 offset:10240\n\t"
            "v_mfma_f32_16x16x32_bf16 v[64:67], v[180:183], v[128:131], v[64:67]\n\tds_read_b128 v[172:175], v3 offset:12288\n\tv_mfma_f32_16x16x32_bf16 v[76:79], v[180:183], v[132:135], v[76:79]\n\tds_read_b64_tr_b16 v[192:193], v12 offset:13312\n\tds_read_b64_tr_b16 v[194:195], v12 offset:13824\n\tv_mfma_f32_16x16x32_bf16 v[88:91], v[180:183], v[136:139], v[88:91]\n\t"
            "ds_read_b64_tr_b16 v[196:197], v12 offset:14336\n\tds_read_b64_tr_b16 v[198:199], v12 offset:14848\n\tv_mfma_f32_16x16x32_bf16 v[100:103], v[180:183], v[140:143], v[100:103]\n\tv_mfma_f32_16x16x32_bf16 v[112:115], v[180:183], v[144:147], v[112:115]\n\ts_waitcnt lgkmcnt(13)\n\tv_mfma_f32_16x16x32_bf16 v[44:47], v[120:123], v[184:187], v[44:47]\n\t"
            "v_mfma_f32_16x16x32_bf16 v[56:59], v[124:127], v[184:187], v[56:59]\n\tv_mfma_f32_16x16x32_bf16 v[68:71], v[128:131], v[184:187], v[68:71]\n\tv_mfma_f32_16x16x32_bf16 v[80:83], v[132:135], v[184:187], v[80:83]\n\tv_mfma_f32_16x16x32_bf16 v[92:95], v[136:139], v[184:187], v[92:95]\n\tv_mfma_f32_16x16x32_bf16 v[104:107], v[140:143], v[184:187], v[104:107]\n\tv_mfma_f32_16x16x32_bf16 v[116:119], v[144:147], v[184:187], v[116:119]\n\t"
            "s_waitcnt lgkmcnt(10)\n\tv_mfma_f32_16x16x32_bf16 v[36:39], v[188:191], v[148:151], v[36:39]\n\ts_waitcnt lgkmcnt(9)\n\tv_mfma_f32_16x16x32_bf16 v[48:51], v[188:191], v[152:155], v[48:51]\n\ts_waitcnt lgkmcnt(8)\n\tv_mfma_f32_16x16x32_bf16 v[60:63], v[188:191], v[156:159], v[60:63]\n\t"
            "s_waitcnt lgkmcnt(7)\n\tv_mfma_f32_16x16x32_bf16 v[72:75], v[188:191], v[160:163], v[72:75]\n\ts_waitcnt lgkmcnt(6)\n\tv_mfma_f32_16x16x32_bf16 v[84:87], v[188:191], v[164:167], v[84:87]\n\ts_waitcnt lgkmcnt(5)\n\tv_mfma_f32_16x16x32_bf16 v[96:99], v[188:191], v[168:171], v[96:99]\n\t"
            "s_waitcnt lgkmcnt(4)\n\tv_mfma_f32_16x16x32_bf16 v[108:111], v[188:191], v[172:175], v[108:111]\n\ts_waitcnt lgkmcnt(2)\n\tv_mfma_f32_16x16x32_bf16 v[40:43], v[192:195], v[148:151], v[40:43]\n\tv_mfma_f32_16x16x32_bf16 v[52:55], v[192:195], v[152:155], v[52:55]\n\tv_mfma_f32_16x16x32_bf16 v[64:67], v[192:195], v[156:159], v[64:67]\n\t"
            "v_mfma_f32_16x16x32_bf16 v[76:79], v[192:195], v[160:163], v[76:79]\n\ts_waitcnt lgkmcnt(0)\n\ts_barrier\n\t"
            "ds_read_b64_tr_b16 v[176:177], v13 offset:0\n\tds_read_b64_tr_b16 v[178:179], v13 offset:512\n\tv_mfma_f32_16x16x32_bf16 v[88:91], v[192:195], v[164:167], v[88:91]\n\tds_read_b128 v[120:123], v8\n\tv_mfma_f32_16x16x32_bf16 v[100:103], v[192:195], v[168:171], v[100:103]\n\tds_read_b128 v[124:127], v4 offset:2048\n\t"
            "v_mfma_f32_16x16x32_bf16 v[112:115], v[192:195], v[172:175], v[112:115]\n\tds_read_b128 v[128:131], v4 offset:4096\n\tv_mfma_f32_16x16x32_bf16 v[44:47], v[148:151], v[196:199], v[44:47]\n\tds_read_b128 v[132:135], v4 offset:6144\n\tv_mfma_f32_16x16x32_bf16 v[56:59], v[152:155], v[196:199], v[56:59]\n\tds_read_b128 v[136:139], v4 offset:8192\n\t"
            "v_mfma_f32_16x16x32_bf16 v[68:71], v[156:159], v[196:199], v[68:71]\n\tds_read_b128 v[140:143], v4 offset:10240\n\tv_mfma_f32_16x16x32_bf16 v[80:83], v[160:163], v[196:199], v[80:83]\n\tds_read_b128 v[144:147], v4 offset:12288\n\tv_mfma_f32_16x16x32_bf16 v[92:95], v[164:167], v[196:199], v[92:95]\n\tds_read_b64_tr_b16 v[180:181], v13 offset:1024\n\t"
            "ds_read_b64_tr_b16 v[182:183], v13 offset:1536\n\tv_mfma_f32_16x16x32_bf16 v[104:107], v[168:171], v[196:199], v[104:107]\n\tds_read_b64_tr_b16 v[184:185], v13 offset:2048\n\tds_read_b64_tr_b16 v[186:187], v13 offset:2560\n\tv_mfma_f32_16x16x32_bf16 v[116:119], v[172:175], v[196:199], v[116:119]\n\ts_waitcnt lgkmcnt(10)\n\t"
            "v_mfma_f32_16x16x32_bf16 v[36:39], v[176:179], v[120:123], v[36:39]\n\ts_waitcnt lgkmcnt(9)\n\tv_mfma_f32_16x16x32_bf16 v[48:51], v[176:179], v[124:127], v[48:51]\n\ts_waitcnt lgkmcnt(8)\n\tv_mfma_f32_16x16x32_bf16 v[60:63], v[176:179], v[128:131], v[60:63]\n\tds_read_b64_tr_b16 v[188:189], v13 offset:12288\n\t"
            "ds_read_b64_tr_b16 v[190:191], v13 offset:12800\n\ts_waitcnt lgkmcnt(9)\n\tv_mfma_f32_16x16x32_bf16 v[72:75], v[176:179], v[132:135], v[72:75]\n\tds_read_b128 v[148:151], v9\n\ts_waitcnt lgkmcnt(9)\n\tv_mfma_f32_16x16x32_bf16 v[84:87], v[176:179], v[136:139], v[84:87]\n\t"
            "ds_read_b128 v[152:155], v5 offset:2048\n\ts_waitcnt lgkmcnt(9)\n\tv_mfma_f32_16x16x32_bf16 v[96:99], v[176:179], v[140:143], v[96:99]\n\tds_read_b128 v[156:159], v5 offset:4096\n\ts_waitcnt lgkmcnt(9)\n\tv_mfma_f32_16x16x32_bf16 v[108:111], v[176:179], v[144:147], v[108:111]\n\t"
            "ds_read_b128 v[160:163], v5 offset:6144\n\ts_waitcnt lgkmcnt(8)\n\tv_mfma_f32_16x16x32_bf16 v[40:43], v[180:183], v[120:123], v[40:43]\n\tds_read_b128 v[164:167], v5 offset:8192\n\tv_mfma_f32_16x16x32_bf16 v[52:55], v[180:183], v[124:127], v[52:55]\n\tds_read_b128 v[168:171], v5 offset:10240\n\t"
            "v_mfma_f32_16x16x32_bf16 v[64:67], v[180:183], v[128:131], v[64:67]\n\tds_read_b128 v[172:175], v5 offset:12288\n\tv_mfma_f32_16x16x32_bf16 v[76:79], v[180:183], v[132:135], v[76:79]\n\tds_read_b64_tr_b16 v[192:193], v13 offset:13312\n\tds_read_b64_tr_b16 v[194:195], v13 offset:13824\n\tv_mfma_f32_16x16x32_bf16 v[88:91], v[180:183], v[136:139], v[88:91]\n\t"
            "ds_read_b64_tr_b16 v[196:197], v13 offset:14336\n\tds_read_b64_tr_b16 v[198:199], v13 offset:14848\n\tv_mfma_f32_16x16x32_bf16 v[100:103], v[180:183], v[140:143], v[100:103]\n\tv_mfma_f32_16x16x32_bf16 v[112:115], v[180:183], v[144:147], v[112:115]\n\ts_waitcnt lgkmcnt(13)\n\tv_mfma_f32_16x16x32_bf16 v[44:47], v[120:123], v[184:187], v[44:47]\n\t"
            "v_mfma_f32_16x16x32_bf16 v[56:59], v[124:127], v[184:187], v[56:59]\n\tv_mfma_f32_16x16x32_bf16 v[68:71], v[128:131], v[184:187], v[68:71]\n\tv_mfma_f32_16x16x32_bf16 v[80:83], v[132:135], v[184:187], v[80:83]\n\tv_mfma_f32_16x16x32_bf16 v[92:95], v[136:139], v[184:187], v[92:95]\n\tv_mfma_f32_16x16x32_bf16 v[104:107], v[140:143], v[184:187], v[104:107]\n\tv_mfma_f32_16x16x32_bf16 v[116:119], v[144:147], v[184:187], v[116:119]\n\t"
            "s_waitcnt lgkmcnt(10)\n\tv_mfma_f32_16x16x32_bf16 v[36:39], v[188:191], v[148:151], v[36:39]\n\ts_waitcnt lgkmcnt(9)\n\tv_mfma_f32_16x16x32_bf16 v[48:51], v[188:191], v[152:155], v[48:51]\n\ts_waitcnt lgkmcnt(8)\n\tv_mfma_f32_16x16x32_bf16 v[60:63], v[188:191], v[156:159], v[60:63]\n\t"
            "s_waitcnt lgkmcnt(7)\n\tv_mfma_f32_16x16x32_bf16 v[72:75], v[188:191], v[160:163], v[72:75]\n\ts_waitcnt lgkmcnt(6)\n\tv_mfma_f32_16x16x32_bf16 v[84:87], v[188:191], v[164:167], v[84:87]\n\ts_waitcnt lgkmcnt(5)\n\tv_mfma_f32_16x16x32_bf16 v[96:99], v[188:191], v[168:171], v[96:99]\n\t"
            "s_waitcnt lgkmcnt(4)\n\tv_mfma_f32_16x16x32_bf16 v[108:111], v[188:191], v[172:175], v[108:111]\n\ts_waitcnt lgkmcnt(2)\n\tv_mfma_f32_16x16x32_bf16 v[40:43], v[192:195], v[148:151], v[40:43]\n\tv_mfma_f32_16x16x32_bf16 v[52:55], v[192:195], v[152:155], v[52:55]\n\tv_mfma_f32_16x16x32_bf16 v[64:67], v[192:195], v[156:159], v[64:67]\n\t"
            "v_mfma_f32_16x16x32_bf16 v[76:79], v[192:195], v[160:163], v[76:79]\n\ts_waitcnt lgkmcnt(0)\n\ts_barrier\n\t"
            "ds_read_b64_tr_b16 v[176:177], v13 offset:38912\n\tds_read_b64_tr_b16 v[178:179], v13 offset:39424\n\tv_mfma_f32_16x16x32_bf16 v[88:91], v[192:195], v[164:167], v[88:91]\n\tds_read_b128 v[120:123], v10\n\tv_mfma_f32_16x16x32_bf16 v[100:103], v[192:195], v[168:171], v[100:103]\n\tds_read_b128 v[124:127], v4 offset:40960\n\t"
            "v_mfma_f32_16x16x32_bf16 v[112:115], v[192:195], v[172:175], v[112:115]\n\tds_read_b128 v[128:131], v4 offset:43008\n\tv_mfma_f32_16x16x32_bf16 v[44:47], v[148:151], v[196:199], v[44:47]\n\tds_read_b128 v[132:135], v4 offset:45056\n\tv_mfma_f32_16x16x32_bf16 v[56:59], v[152:155], v[196:199], v[56:59]\n\tds_read_b128 v[136:139], v4 offset:47104\n\t"
            "v_mfma_f32_16x16x32_bf16 v[68:71], v[156:159], v[196:199], v[68:71]\n\tds_read_b128 v[140:143], v4 offset:49152\n\tv_mfma_f32_16x16x32_bf16 v[80:83], v[160:163], v[196:199], v[80:83]\n\tds_read_b128 v[144:147], v4 offset:51200\n\tv_mfma_f32_16x16x32_bf16 v[92:95], v[164:167], v[196:199], v[92:95]\n\tds_read_b64_tr_b16 v[180:181], v13 offset:39936\n\t"
            "ds_read_b64_tr_b16 v[182:183], v13 offset:40448\n\tv_mfma_f32_16x16x32_bf16 v[104:107], v[168:171], v[196:199], v[104:107]\n\tds_read_b64_tr_b16 v[184:185], v13 offset:40960\n\tds_read_b64_tr_b16 v[186:187], v13 offset:41472\n\tv_mfma_f32_16x16x32_bf16 v[116:119], v[172:175], v[196:199], v[116:119]\n\ts_waitcnt lgkmcnt(10)\n\t"
            "v_mfma_f32_16x16x32_bf16 v[36:39], v[176:179], v[120:123], v[36:39]\n\ts_waitcnt lgkmcnt(9)\n\tv_mfma_f32_16x16x32_bf16 v[48:51], v[176:179], v[124:127], v[48:51]\n\ts_waitcnt lgkmcnt(8)\n\tv_mfma_f32_16x16x32_bf16 v[60:63], v[176:179], v[128:131], v[60:63]\n\tds_read_b64_tr_b16 v[188:189], v13 offset:51200\n\t"
            "ds_read_b64_tr_b16 v[190:191], v13 offset:51712\n\ts_waitcnt lgkmcnt(9)\n\tv_mfma_f32_16x16x32_bf16 v[72:75], v[176:179], v[132:135], v[72:75]\n\tds_read_b128 v[148:151], v11\n\ts_waitcnt lgkmcnt(9)\n\tv_mfma_f32_16x16x32_bf16 v[84:87], v[176:179], v[136:139], v[84:87]\n\t"
            "ds_read_b128 v[152:155], v5 offset:40960\n\ts_waitcnt lgkmcnt(9)\n\tv_mfma_f32_16x16x32_bf16 v[96:99], v[176:179], v[140:143], v[96:99]\n\tds_read_b128 v[156:159], v5 offset:43008\n\ts_waitcnt lgkmcnt(9)\n\tv_mfma_f32_16x16x32_bf16 v[108:111], v[176:179], v[144:147], v[108:111]\n\t"
            "ds_read_b128 v[160:163], v5 offset:45056\n\ts_waitcnt lgkmcnt(8)\n\tv_mfma_f32_16x16x32_bf16 v[40:43], v[180:183], v[120:123], v[40:43]\n\tds_read_b128 v[164:167], v5 offset:47104\n\tv_mfma_f32_16x16x32_bf16 v[52:55], v[180:183], v[124:127], v[52:55]\n\tds_read_b128 v[168:171], v5 offset:49152\n\t"
            "v_mfma_f32_16x16x32_bf16 v[64:67], v[180:183], v[128:131], v[64:67]\n\tds_read_b128 v[172:175], v5 offset:51200\n\tv_mfma_f32_16x16x32_bf16 v[76:79], v[180:183], v[132:135], v[76:79]\n\tds_read_b64_tr_b16 v[192:193], v13 offset:52224\n\tds_read_b64_tr_b16 v[194:195], v13 offset:52736\n\tv_mfma_f32_16x16x32_bf16 v[88:91], v[180:183], v[136:139], v[88:91]\n\t"
            "ds_read_b64_tr_b16 v[196:197], v13 offset:53248\n\tds_read_b64_tr_b16 v[198:199], v13 offset:53760\n\tv_mfma_f32_16x16x32_bf16 v[100:103], v[180:183], v[140:143], v[100:103]\n\tv_mfma_f32_16x16x32_bf16 v[112:115], v[180:183], v[144:147], v[112:115]\n\ts_waitcnt lgkmcnt(13)\n\tv_mfma_f32_16x16x32_bf16 v[44:47], v[120:123], v[184:187], v[44:47]\n\t"
            "v_mfma_f32_16x16x32_bf16 v[56:59], v[124:127], v[184:187], v[56:59]\n\tv_mfma_f32_16x16x32_bf16 v[68:71], v[128:131], v[184:187], v[68:71]\n\tv_mfma_f32_16x16x32_bf16 v[80:83], v[132:135], v[184:187], v[80:83]\n\tv_mfma_f32_16x16x32_bf16 v[92:95], v[136:139], v[184:187], v[92:95]\n\tv_mfma_f32_16x16x32_bf16 v[104:107], v[140:143], v[184:187], v[104:107]\n\tv_mfma_f32_16x16x32_bf16 v[116:119], v[144:147], v[184:187], v[116:119]\n\t"
            "s_waitcnt lgkmcnt(10)\n\tv_mfma_f32_16x16x32_bf16 v[36:39], v[188:191], v[148:151], v[36:39]\n\ts_waitcnt lgkmcnt(9)\n\tv_mfma_f32_16x16x32_bf16 v[48:51], v[188:191], v[152:155], v[48:51]\n\ts_waitcnt lgkmcnt(8)\n\tv_mfma_f32_16x16x32_bf16 v[60:63], v[188:191], v[156:159], v[60:63]\n\t"
            "s_waitcnt lgkmcnt(7)\n\tv_mfma_f32_16x16x32_bf16 v[72:75], v[188:191], v[160:163], v[72:75]\n\ts_waitcnt lgkmcnt(6)\n\tv_mfma_f32_16x16x32_bf16 v[84:87], v[188:191], v[164:167], v[84:87]\n\ts_waitcnt lgkmcnt(5)\n\tv_mfma_f32_16x16x32_bf16 v[96:99], v[188:191], v[168:171], v[96:99]\n\t"
            "s_waitcnt lgkmcnt(4)\n\tv_mfma_f32_16x16x32_bf16 v[108:111], v[188:191], v[172:175], v[108:111]\n\ts_waitcnt lgkmcnt(2)\n\tv_mfma_f32_16x16x32_bf16 v[40:43], v[192:195], v[148:151], v[40:43]\n\tv_mfma_f32_16x16x32_bf16 v[52:55], v[192:195], v[152:155], v[52:55]\n\tv_mfma_f32_16x16x32_bf16 v[64:67], v[192:195], v[156:159], v[64:67]\n\t"
            "v_mfma_f32_16x16x32_bf16 v[76:79], v[192:195], v[160:163], v[76:79]\n\ts_waitcnt lgkmcnt(0)\n\ts_barrier\n\t"
            "ds_read_b64_tr_b16 v[176:177], v12 offset:0\n\tds_read_b64_tr_b16 v[178:179], v12 offset:512\n\tv_mfma_f32_16x16x32_bf16 v[88:91], v[192:195], v[164:167], v[88:91]\n\tds_read_b128 v[120:123], v6\n\tv_mfma_f32_16x16x32_bf16 v[100:103], v[192:195], v[168:171], v[100:103]\n\tds_read_b128 v[124:127], v2 offset:2048\n\t"
            "v_mfma_f32_16x16x32_bf16 v[112:115], v[192:195], v[172:175], v[112:115]\n\tds_read_b128 v[128:131], v2 offset:4096\n\tv_mfma_f32_16x16x32_bf16 v[44:47], v[148:151], v[196:199], v[44:47]\n\tds_read_b128 v[132:135], v2 offset:6144\n\tv_mfma_f32_16x16x32_bf16 v[56:59], v[152:155], v[196:199], v[56:59]\n\tds_read_b128 v[136:139], v2 offset:8192\n\t"
            "v_mfma_f32_16x16x32_bf16 v[68:71], v[156:159], v[196:199], v[68:71]\n\tds_read_b128 v[140:143], v2 offset:10240\n\tv_mfma_f32_16x16x32_bf16 v[80:83], v[160:163], v[196:199], v[80:83]\n\tds_read_b128 v[144:147], v2 offset:12288\n\tv_mfma_f32_16x16x32_bf16 v[92:95], v[164:167], v[196:199], v[92:95]\n\tds_read_b64_tr_b16 v[180:181], v12 offset:1024\n\t"
            "ds_read_b64_tr_b16 v[182:183], v12 offset:1536\n\tv_mfma_f32_16x16x32_bf16 v[104:107], v[168:171], v[196:199], v[104:107]\n\tv_mfma_f32_16x16x32_bf16 v[116:119], v[172:175], v[196:199], v[116:119]\n\ts_waitcnt lgkmcnt(8)\n\tv_mfma_f32_16x16x32_bf16 v[36:39], v[176:179], v[120:123], v[36:39]\n\ts_waitcnt lgkmcnt(7)\n\t"
            "v_mfma_f32_16x16x32_bf16 v[48:51], v[176:179], v[124:127], v[48:51]\n\ts_waitcnt lgkmcnt(6)\n\tv_mfma_f32_16x16x32_bf16 v[60:63], v[176:179], v[128:131], v[60:63]\n\tds_read_b64_tr_b16 v[188:189], v12 offset:12288\n\tds_read_b64_tr_b16 v[190:191], v12 offset:12800\n\ts_waitcnt lgkmcnt(7)\n\t"
            "v_mfma_f32_16x16x32_bf16 v[72:75], v[176:179], v[132:135], v[72:75]\n\tds_read_b128 v[148:151], v7\n\ts_waitcnt lgkmcnt(7)\n\tv_mfma_f32_16x16x32_bf16 v[84:87], v[176:179], v[136:139], v[84:87]\n\tds_read_b128 v[152:155], v3 offset:2048\n\ts_waitcnt lgkmcnt(7)\n\t"
            "v_mfma_f32_16x16x32_bf16 v[96:99], v[176:179], v[140:143], v[96:99]\n\tds_read_b128 v[156:159], v3 offset:4096\n\ts_waitcnt lgkmcnt(7)\n\tv_mfma_f32_16x16x32_bf16 v[108:111], v[176:179], v[144:147], v[108:111]\n\tds_read_b128 v[160:163], v3 offset:6144\n\ts_waitcnt lgkmcnt(6)\n\t"
            "v_mfma_f32_16x16x32_bf16 v[40:43], v[180:183], v[120:123], v[40:43]\n\tds_read_b128 v[164:167], v3 offset:8192\n\tv_mfma_f32_16x16x32_bf16 v[52:55], v[180:183], v[124:127], v[52:55]\n\tds_read_b128 v[168:171], v3 offset:10240\n\tv_mfma_f32_16x16x32_bf16 v[64:67], v[180:183], v[128:131], v[64:67]\n\tds_read_b128 v[172:175], v3 offset:12288\n\t"
            "v_mfma_f32_16x16x32_bf16 v[76:79], v[180:183], v[132:135], v[76:79]\n\tds_read_b64_tr_b16 v[192:193], v12 offset:13312\n\tds_read_b64_tr_b16 v[194:195], v12 offset:13824\n\tv_mfma_f32_16x16x32_bf16 v[88:91], v[180:183], v[136:139], v[88:91]\n\tv_mfma_f32_16x16x32_bf16 v[100:103], v[180:183], v[140:143], v[100:103]\n\tv_mfma_f32_16x16x32_bf16 v[112:115], v[180:183], v[144:147], v[112:115]\n\t"
            "s_waitcnt lgkmcnt(8)\n\tv_mfma_f32_16x16x32_bf16 v[36:39], v[188:191], v[148:151], v[36:39]\n\ts_waitcnt lgkmcnt(7)\n\tv_mfma_f32_16x16x32_bf16 v[48:51], v[188:191], v[152:155], v[48:51]\n\ts_waitcnt lgkmcnt(6)\n\tv_mfma_f32_16x16x32_bf16 v[60:63], v[188:191], v[156:159], v[60:63]\n\t"
            "s_waitcnt lgkmcnt(5)\n\tv_mfma_f32_16x16x32_bf16 v[72:75], v[188:191], v[160:163], v[72:75]\n\ts_waitcnt lgkmcnt(0)\n\ts_barrier\n\t"
            "ds_read_b64_tr_b16 v[176:177], v13 offset:0\n\tds_read_b64_tr_b16 v[178:179], v13 offset:512\n\tv_mfma_f32_16x16x32_bf16 v[84:87], v[188:191], v[164:167], v[84:87]\n\tds_read_b128 v[120:123], v8\n\tv_mfma_f32_16x16x32_bf16 v[96:99], v[188:191], v[168:171], v[96:99]\n\tds_read_b128 v[124:127], v4 offset:2048\n\t"
            "v_mfma_f32_16x16x32_bf16 v[108:111], v[188:191], v[172:175], v[108:111]\n\tds_read_b128 v[128:131], v4 offset:4096\n\tv_mfma_f32_16x16x32_bf16 v[40:43], v[192:195], v[148:151], v[40:43]\n\tds_read_b128 v[132:135], v4 offset:6144\n\tv_mfma_f32_16x16x32_bf16 v[52:55], v[192:195], v[152:155], v[52:55]\n\tds_read_b128 v[136:139], v4 offset:8192\n\t"
            "v_mfma_f32_16x16x32_bf16 v[64:67], v[192:195], v[156:159], v[64:67]\n\tds_read_b128 v[140:143], v4 offset:10240\n\tv_mfma_f32_16x16x32_bf16 v[76:79], v[192:195], v[160:163], v[76:79]\n\tds_read_b128 v[144:147], v4 offset:12288\n\tv_mfma_f32_16x16x32_bf16 v[88:91], v[192:195], v[164:167], v[88:91]\n\tds_read_b64_tr_b16 v[180:181], v13 offset:1024\n\t"
            "ds_read_b64_tr_b16 v[182:183], v13 offset:1536\n\tv_mfma_f32_16x16x32_bf16 v[100:103], v[192:195], v[168:171], v[100:103]\n\tds_read_b64_tr_b16 v[184:185], v13 offset:2048\n\tds_read_b64_tr_b16 v[186:187], v13 offset:2560\n\tv_mfma_f32_16x16x32_bf16 v[112:115], v[192:195], v[172:175], v[112:115]\n\tds_read2_b32 v[208:209], v14 offset1:16\n\t"
            "s_setprio 2\n\t"
            "s_mov_b64 vcc, s[18:19]\n\ts_bitcmp1_b32 9, s69\n\tds_read_b64_tr_b16 v[188:189], v13 offset:12288\n\tv_mov_b32 v252, -1\n\tds_read_b64_tr_b16 v[190:191], v13 offset:12800\n\tv_mov_b32 v254, -1\n\t"
            "ds_read_b128 v[148:151], v9\n\ts_waitcnt lgkmcnt(6)\n\tv_mfma_f32_16x16x32_bf16 v[40:43], v[180:183], v[120:123], v[40:43]\n\tds_read_b128 v[152:155], v5 offset:2048\n\tv_mfma_f32_16x16x32_bf16 v[36:39], v[176:179], v[120:123], v[36:39]\n\tds_read_b128 v[156:159], v5 offset:4096\n\t"
            "v_mfma_f32_16x16x32_bf16 v[48:51], v[176:179], v[124:127], v[48:51]\n\tds_read_b128 v[160:163], v5 offset:6144\n\tv_mfma_f32_16x16x32_bf16 v[60:63], v[176:179], v[128:131], v[60:63]\n\tds_read_b128 v[164:167], v5 offset:8192\n\tv_mfma_f32_16x16x32_bf16 v[52:55], v[180:183], v[124:127], v[52:55]\n\tds_read_b128 v[168:171], v5 offset:10240\n\t"
            "v_mfma_f32_16x16x32_bf16 v[64:67], v[180:183], v[128:131], v[64:67]\n\tds_read_b128 v[172:175], v5 offset:12288\n\tv_mfma_f32_16x16x32_bf16 v[76:79], v[180:183], v[132:135], v[76:79]\n\tds_read_b64_tr_b16 v[192:193], v13 offset:13312\n\ts_waitcnt lgkmcnt(7)\n\tv_mfma_f32_16x16x32_bf16 v[36:39], v[188:191], v[148:151], v[36:39]\n\t"
            "ds_read_b64_tr_b16 v[194:195], v13 offset:13824\n\tds_read_b64_tr_b16 v[196:197], v13 offset:14336\n\ts_waitcnt lgkmcnt(8)\n\tv_mfma_f32_16x16x32_bf16 v[48:51], v[188:191], v[152:155], v[48:51]\n\tds_read_b64_tr_b16 v[198:199], v13 offset:14848\n\tds_read2_b32 v[210:211], v14 offset0:32 offset1:48\n\t"
            "v_mfma_f32_16x16x32_bf16 v[72:75], v[176:179], v[132:135], v[72:75]\n\ts_nop 0\n\tv_mov_b32_dpp v200, v36 row_newbcast:0 row_mask:0xf bank_mask:0xf bound_ctrl:1\n\tds_read2_b32 v[212:213], v14 offset0:64 offset1:80\n\ts_waitcnt lgkmcnt(9)\n\tv_mfma_f32_16x16x32_bf16 v[60:63], v[188:191], v[156:159], v[60:63]\n\t"
            "v_mov_b32_dpp v201, v37 row_newbcast:0 row_mask:0xf bank_mask:0xf bound_ctrl:1\n\tds_read_b32 v214, v14 offset:384\n\tv_mfma_f32_16x16x32_bf16 v[72:75], v[188:191], v[160:163], v[72:75]\n\tv_mov_b32_dpp v202, v38 row_newbcast:0 row_mask:0xf bank_mask:0xf bound_ctrl:1\n\tv_mov_b32_dpp v203, v39 row_newbcast:0 row_mask:0xf bank_mask:0xf bound_ctrl:1\n\tv_fmac_f32 v36, v208, v200\n\t"
            "v_fmac_f32 v37, v208, v201\n\tv_fmac_f32 v38, v208, v202\n\tv_fmac_f32 v39, v208, v203\n\tv_cvt_pk_bf16_f32 v250, v36, v37\n\tv_cvt_pk_bf16_f32 v251, v38, v39\n\tv_and_b32 v36, v250, v18\n\t"
            "v_and_b32 v37, v251, v18\n\ts_waitcnt lgkmcnt(2)\n\tv_mfma_f32_16x16x32_bf16 v[40:43], v[192:195], v[148:151], v[40:43]\n\tv_and_b32 v38, v250, v19\n\tv_and_b32 v39, v251, v19\n\tv_mfma_f32_16x16x32_bf16 v[52:55], v[192:195], v[152:155], v[52:55]\n\t"
            "v_and_b32 v222, v250, v20\n\tv_and_b32 v223, v251, v20\n\tv_mfma_f32_16x16x32_bf16 v[64:67], v[192:195], v[156:159], v[64:67]\n\ts_nop 1\n\tv_mov_b32_dpp v204, v40 row_newbcast:0 row_mask:0xf bank_mask:0xf bound_ctrl:1\n\tv_mov_b32_dpp v205, v41 row_newbcast:0 row_mask:0xf bank_mask:0xf bound_ctrl:1\n\t"
            "v_mfma_f32_16x16x32_bf16 v[76:79], v[192:195], v[160:163], v[76:79]\n\tv_mov_b32_dpp v206, v42 row_newbcast:0 row_mask:0xf bank_mask:0xf bound_ctrl:1\n\tv_mov_b32_dpp v207, v43 row_newbcast:0 row_mask:0xf bank_mask:0xf bound_ctrl:1\n\tv_fmac_f32 v40, v208, v204\n\tv_fmac_f32 v41, v208, v205\n\tv_fmac_f32 v42, v208, v206\n\t"
            "v_fmac_f32 v43, v208, v207\n\tv_cvt_pk_bf16_f32 v40, v40, v41\n\tv_cvt_pk_bf16_f32 v41, v42, v43\n\tv_cndmask_b32 v42, v40, v40, vcc\n\tv_cndmask_b32 v43, v41, v41, vcc\n\tv_and_b32 v224, v250, v21\n\t"
            "v_and_b32 v225, v251, v21\n\tv_mfma_f32_16x16x32_bf16 v[218:221], v[40:43], v[36:39], 0\n\tv_fmac_f32 v48, v209, v200\n\tv_fmac_f32 v49, v209, v201\n\tv_mfma_f32_16x16x32_bf16 v[226:229], v[40:43], v[222:225], 0\n\tv_fmac_f32 v50, v209, v202\n\t"
            "v_fmac_f32 v51, v209, v203\n\tv_cvt_pk_bf16_f32 v250, v48, v49\n\ts_nop 1\n\tv_cndmask_b32 v36, v220, v218, s[14:15]\n\tv_cndmask_b32 v37, v221, v219, s[16:17]\n\tv_cvt_pk_bf16_f32 v251, v50, v51\n\t"
            "v_and_b32 v48, v250, v18\n\tv_cndmask_b32 v38, v228, v226, s[14:15]\n\tv_cndmask_b32 v39, v229, v227, s[16:17]\n\tv_and_b32 v49, v251, v18\n\tv_and_b32 v50, v250, v19\n\tv_and_b32 v51, v251, v19\n\t"
            "v_and_b32 v234, v250, v20\n\tv_and_b32 v235, v251, v20\n\tv_and_b32 v236, v250, v21\n\tv_and_b32 v237, v251, v21\n\tv_fmac_f32 v60, v210, v200\n\tv_fmac_f32 v61, v210, v201\n\t"
            "v_fmac_f32 v62, v210, v202\n\tv_fmac_f32 v63, v210, v203\n\tv_fmac_f32 v52, v209, v204\n\tv_fmac_f32 v53, v209, v205\n\tv_fmac_f32 v64, v210, v204\n\tv_fmac_f32 v65, v210, v205\n\t"
            "v_cvt_pk_bf16_f32 v250, v60, v61\n\tv_cvt_pk_bf16_f32 v251, v62, v63\n\tv_fmac_f32 v76, v211, v204\n\tv_fmac_f32 v77, v211, v205\n\tv_fmac_f32 v54, v209, v206\n\tv_fmac_f32 v55, v209, v207\n\t"
            "v_cvt_pk_bf16_f32 v52, v52, v53\n\tv_fmac_f32 v66, v210, v206\n\tv_fmac_f32 v67, v210, v207\n\tv_cvt_pk_bf16_f32 v64, v64, v65\n\tv_and_b32 v60, v250, v18\n\tv_and_b32 v61, v251, v18\n\t"
            "v_and_b32 v62, v250, v19\n\tv_and_b32 v63, v251, v19\n\tv_and_b32 v246, v250, v20\n\tv_and_b32 v247, v251, v20\n\tv_and_b32 v248, v250, v21\n\tv_and_b32 v249, v251, v21\n\t"
            "v_fmac_f32 v78, v211, v206\n\tv_fmac_f32 v79, v211, v207\n\tv_fmac_f32 v72, v211, v200\n\tv_fmac_f32 v73, v211, v201\n\tv_fmac_f32 v74, v211, v202\n\tv_fmac_f32 v75, v211, v203\n\t"
            "v_cvt_pk_bf16_f32 v76, v76, v77\n\tv_cvt_pk_bf16_f32 v53, v54, v55\n\tv_cvt_pk_bf16_f32 v65, v66, v67\n\tv_cvt_pk_bf16_f32 v77, v78, v79\n\tv_cvt_pk_bf16_f32 v250, v72, v73\n\tv_cvt_pk_bf16_f32 v251, v74, v75\n\t"
            "v_cndmask_b32 v54, v40, v52, vcc\n\tv_cndmask_b32 v55, v41, v53, vcc\n\tv_cndmask_b32 v66, v52, v64, vcc\n\tv_cndmask_b32 v67, v53, v65, vcc\n\tv_mfma_f32_16x16x32_bf16 v[230:233], v[52:55], v[48:51], 0\n\tv_cndmask_b32 v78, v64, v76, vcc\n\t"
            "v_cndmask_b32 v79, v65, v77, vcc\n\tv_mfma_f32_16x16x32_bf16 v[242:245], v[64:67], v[60:63], 0\n\tv_and_b32 v72, v250, v18\n\tv_and_b32 v73, v251, v18\n\tv_mfma_f32_16x16x32_bf16 v[238:241], v[52:55], v[234:237], 0\n\tv_and_b32 v74, v250, v19\n\t"
            "v_and_b32 v75, v251, v19\n\tv_mfma_f32_16x16x32_bf16 v[218:221], v[64:67], v[246:249], 0\n\tv_cndmask_b32 v48, v232, v230, s[14:15]\n\tv_cndmask_b32 v49, v233, v231, s[16:17]\n\tv_mfma_f32_16x16x32_bf16 v[222:225], v[76:79], v[72:75], 0\n\tv_cndmask_b32 v60, v244, v242, s[14:15]\n\t"
            "v_cndmask_b32 v61, v245, v243, s[16:17]\n\tv_mfma_f32_16x16x32_bf16 v[84:87], v[176:179], v[136:139], v[84:87]\n\tv_cndmask_b32 v50, v240, v238, s[14:15]\n\tv_cndmask_b32 v51, v241, v239, s[16:17]\n\tv_mfma_f32_16x16x32_bf16 v[96:99], v[176:179], v[140:143], v[96:99]\n\ts_nop 1\n\t"
            "v_cndmask_b32 v72, v224, v222, s[14:15]\n\tv_cndmask_b32 v73, v225, v223, s[16:17]\n\tv_mfma_f32_16x16x32_bf16 v[88:91], v[180:183], v[136:139], v[88:91]\n\ts_cbranch_scc0 1f\n\tv_cvt_pk_bf16_f32 v253, v36, v37\n\tv_min_u32 v253, -2, v253\n\t"
            "v_cvt_pk_bf16_f32 v255, v48, v49\n\tv_min_u32 v255, -2, v255\n\tbuffer_store_dwordx4 v[252:255], v17, s[44:47], s20 offen sc1\n\ts_nop 1\n\tv_cvt_pk_bf16_f32 v253, v60, v61\n\tv_min_u32 v253, -2, v253\n\t"
            "v_cvt_pk_bf16_f32 v255, v72, v73\n\tv_min_u32 v255, -2, v255\n\tbuffer_store_dwordx4 v[252:255], v17, s[44:47], s21 offen sc1\n\t1:\nv_cndmask_b32 v62, v220, v218, s[14:15]\n\tv_mfma_f32_16x16x32_bf16 v[100:103], v[180:183], v[140:143], v[100:103]\n\t"
            "v_cndmask_b32 v63, v221, v219, s[16:17]\n\tv_and_b32 v226, v250, v20\n\tv_mfma_f32_16x16x32_bf16 v[112:115], v[180:183], v[144:147], v[112:115]\n\tv_and_b32 v227, v251, v20\n\tv_and_b32 v228, v250, v21\n\tv_mfma_f32_16x16x32_bf16 v[84:87], v[188:191], v[164:167], v[84:87]\n\t"
            "v_and_b32 v229, v251, v21\n\tv_mov_b32 v180, 0\n\tv_mfma_f32_16x16x32_bf16 v[108:111], v[176:179], v[144:147], v[108:111]\n\tv_mov_b32 v176, 0\n\tds_write_b64 v28, v[36:37] offset:0\n\tv_mfma_f32_16x16x32_bf16 v[230:233], v[76:79], v[226:229], 0\n\t"
            "s_waitcnt lgkmcnt(1)\n\ts_nop 0\n\tv_fmac_f32 v84, v212, v200\n\tv_fmac_f32 v85, v212, v201\n\tv_mfma_f32_16x16x32_bf16 v[96:99], v[188:191], v[168:171], v[96:99]\n\tds_write_b64 v29, v[38:39] offset:0\n\t"
            "v_fmac_f32 v86, v212, v202\n\tv_mfma_f32_16x16x32_bf16 v[88:91], v[192:195], v[164:167], v[88:91]\n\tv_cndmask_b32 v74, v232, v230, s[14:15]\n\tv_cndmask_b32 v75, v233, v231, s[16:17]\n\tds_write_b64 v28, v[48:49] offset:512\n\tv_mfma_f32_16x16x32_bf16 v[100:103], v[192:195], v[168:171], v[100:103]\n\t"
            "v_fmac_f32 v87, v212, v203\n\tv_cvt_pk_bf16_f32 v250, v84, v85\n\tv_mfma_f32_16x16x32_bf16 v[112:115], v[192:195], v[172:175], v[112:115]\n\tds_write_b64 v29, v[50:51] offset:512\n\tv_cvt_pk_bf16_f32 v251, v86, v87\n\tv_mfma_f32_16x16x32_bf16 v[108:111], v[188:191], v[172:175], v[108:111]\n\t"
            "v_and_b32 v84, v250, v18\n\tv_and_b32 v85, v251, v18\n\tds_write_b64 v28, v[60:61] offset:1024\n\tv_and_b32 v86, v250, v19\n\tv_and_b32 v87, v251, v19\n\tv_and_b32 v238, v250, v20\n\t"
            "v_and_b32 v239, v251, v20\n\tv_and_b32 v240, v250, v21\n\tds_write_b64 v29, v[62:63] offset:1024\n\tv_and_b32 v241, v251, v21\n\tv_fmac_f32 v96, v213, v200\n\tv_fmac_f32 v97, v213, v201\n\t"
            "v_fmac_f32 v98, v213, v202\n\tv_fmac_f32 v99, v213, v203\n\tds_write_b64 v28, v[72:73] offset:1536\n\tv_fmac_f32 v88, v212, v204\n\tv_fmac_f32 v89, v212, v205\n\tv_fmac_f32 v100, v213, v204\n\t"
            "v_fmac_f32 v101, v213, v205\n\tv_cvt_pk_bf16_f32 v250, v96, v97\n\tds_write_b64 v29, v[74:75] offset:1536\n\tv_cvt_pk_bf16_f32 v251, v98, v99\n\tv_fmac_f32 v112, v214, v204\n\tv_fmac_f32 v113, v214, v205\n\t"
            "v_fmac_f32 v90, v212, v206\n\tv_fmac_f32 v91, v212, v207\n\tds_read_b128 v[36:39], v15 offset:0\n\tv_cvt_pk_bf16_f32 v88, v88, v89\n\tv_fmac_f32 v102, v213, v206\n\tv_fmac_f32 v103, v213, v207\n\t"
            "ds_read_b128 v[48:51], v15 offset:64\n\tv_cvt_pk_bf16_f32 v100, v100, v101\n\ts_waitcnt lgkmcnt(0)\n\ts_barrier\n\tv_and_b32 v96, v250, v18\n\tds_read_b128 v[60:63], v15 offset:128\n\t"
            "v_and_b32 v97, v251, v18\n\tv_and_b32 v98, v250, v19\n\tv_and_b32 v99, v251, v19\n\tds_read_b128 v[72:75], v15 offset:192\n\tv_and_b32 v218, v250, v20\n\tv_and_b32 v219, v251, v20\n\t"
            "v_and_b32 v220, v250, v21\n\tv_and_b32 v221, v251, v21\n\tv_fmac_f32 v114, v214, v206\n\tv_fmac_f32 v115, v214, v207\n\tv_fmac_f32 v108, v214, v200\n\tv_fmac_f32 v109, v214, v201\n\t"
            "v_fmac_f32 v110, v214, v202\n\tv_fmac_f32 v111, v214, v203\n\tv_cvt_pk_bf16_f32 v112, v112, v113\n\tv_cvt_pk_bf16_f32 v89, v90, v91\n\tv_cvt_pk_bf16_f32 v101, v102, v103\n\tv_cvt_pk_bf16_f32 v113, v114, v115\n\t"
            "v_cvt_pk_bf16_f32 v250, v108, v109\n\tv_cvt_pk_bf16_f32 v251, v110, v111\n\tv_cndmask_b32 v90, v76, v88, vcc\n\tv_cndmask_b32 v91, v77, v89, vcc\n\tv_cndmask_b32 v102, v88, v100, vcc\n\tv_cndmask_b32 v103, v89, v101, vcc\n\t"
            "v_mfma_f32_16x16x32_bf16 v[234:237], v[88:91], v[84:87], 0\n\tv_cndmask_b32 v114, v100, v112, vcc\n\tv_cndmask_b32 v115, v101, v113, vcc\n\tv_mfma_f32_16x16x32_bf16 v[246:249], v[100:103], v[96:99], 0\n\tv_and_b32 v108, v250, v18\n\tv_and_b32 v109, v251, v18\n\t"
            "v_mfma_f32_16x16x32_bf16 v[242:245], v[88:91], v[238:241], 0\n\tv_and_b32 v110, v250, v19\n\tv_and_b32 v111, v251, v19\n\tv_mfma_f32_16x16x32_bf16 v[222:225], v[100:103], v[218:221], 0\n\tv_cndmask_b32 v84, v236, v234, s[14:15]\n\tv_cndmask_b32 v85, v237, v235, s[16:17]\n\t"
            "ds_write_b64 v28, v[84:85] offset:38912\n\tv_mfma_f32_16x16x32_bf16 v[226:229], v[112:115], v[108:111], 0\n\tv_cndmask_b32 v96, v248, v246, s[14:15]\n\tv_cndmask_b32 v97, v249, v247, s[16:17]\n\tv_cndmask_b32 v86, v244, v242, s[14:15]\n\tds_write_b64 v28, v[96:97] offset:39424\n\t"
            "v_cndmask_b32 v87, v245, v243, s[16:17]\n\tv_cndmask_b32 v98, v224, v222, s[14:15]\n\tv_cndmask_b32 v99, v225, v223, s[16:17]\n\ts_nop 0\n\tv_cndmask_b32 v108, v228, v226, s[14:15]\n\tv_cndmask_b32 v109, v229, v227, s[16:17]\n\t"
            "ds_write_b64 v29, v[86:87] offset:38912\n\ts_cbranch_scc0 1f\n\tv_cvt_pk_bf16_f32 v253, v84, v85\n\tv_min_u32 v253, -2, v253\n\tv_cvt_pk_bf16_f32 v255, v96, v97\n\tv_min_u32 v255, -2, v255\n\t"
            "buffer_store_dwordx4 v[252:255], v17, s[44:47], s22 offen sc1\n\ts_nop 1\n\tv_cvt_pk_bf16_f32 v253, v108, v109\n\tv_min_u32 v253, -2, v253\n\tbuffer_store_dwordx4 v[252:255], v17, s[44:47], s23 offen sc1\n\t1:\n"
            "v_and_b32 v230, v250, v20\n\tv_and_b32 v231, v251, v20\n\tv_and_b32 v232, v250, v21\n\tv_and_b32 v233, v251, v21\n\tds_write_b64 v29, v[98:99] offset:39424\n\tv_mfma_f32_16x16x32_bf16 v[44:47], v[120:123], v[184:187], v[44:47]\n\t"
            "v_mov_b32 v218, 0\n\tv_mov_b32 v219, 0\n\tv_mfma_f32_16x16x32_bf16 v[234:237], v[112:115], v[230:233], 0\n\tds_write_b64 v28, v[108:109] offset:39936\n\tv_mov_b32 v188, 0\n\tv_mfma_f32_16x16x32_bf16 v[56:59], v[124:127], v[184:187], v[56:59]\n\t"
            "v_mov_b32 v192, 0\n\tds_read_b128 v[120:123], v6\n\ts_nop 2\n\tv_cndmask_b32 v110, v236, v234, s[14:15]\n\tv_cndmask_b32 v111, v237, v235, s[16:17]\n\tv_mfma_f32_16x16x32_bf16 v[68:71], v[128:131], v[184:187], v[68:71]\n\t"
            "ds_write_b64 v29, v[110:111] offset:39936\n\tv_mov_b32 v234, 0\n\tv_mfma_f32_16x16x32_bf16 v[80:83], v[132:135], v[184:187], v[80:83]\n\tv_mov_b32 v235, 0\n\tds_write_b32 v33, v33\n\t"
            "v_mfma_f32_16x16x32_bf16 v[92:95], v[136:139], v[184:187], v[92:95]\n\tds_read_b128 v[124:127], v2 offset:2048\n\tv_mfma_f32_16x16x32_bf16 v[104:107], v[140:143], v[184:187], v[104:107]\n\tds_read_b128 v[128:131], v2 offset:4096\n\tv_mfma_f32_16x16x32_bf16 v[116:119], v[144:147], v[184:187], v[116:119]\n\tds_read_b64_tr_b16 v[184:185], v12 offset:2048\n\t"
            "v_mfma_f32_16x16x32_bf16 v[44:47], v[148:151], v[196:199], v[44:47]\n\tds_read_b64_tr_b16 v[186:187], v12 offset:2560\n\tds_read_b128 v[148:151], v7\n\tv_mfma_f32_16x16x32_bf16 v[56:59], v[152:155], v[196:199], v[56:59]\n\tds_read_b128 v[152:155], v3 offset:2048\n\tv_mfma_f32_16x16x32_bf16 v[68:71], v[156:159], v[196:199], v[68:71]\n\t"
            "ds_read_b128 v[132:135], v2 offset:6144\n\tv_mfma_f32_16x16x32_bf16 v[80:83], v[160:163], v[196:199], v[80:83]\n\tds_read_b128 v[156:159], v3 offset:4096\n\tv_mfma_f32_16x16x32_bf16 v[92:95], v[164:167], v[196:199], v[92:95]\n\tds_read_b128 v[160:163], v3 offset:6144\n\tv_mfma_f32_16x16x32_bf16 v[104:107], v[168:171], v[196:199], v[104:107]\n\t"
            "ds_read_b128 v[136:139], v2 offset:8192\n\tv_mfma_f32_16x16x32_bf16 v[116:119], v[172:175], v[196:199], v[116:119]\n\tds_read_b64_tr_b16 v[196:197], v12 offset:14336\n\tds_read_b64_tr_b16 v[198:199], v12 offset:14848\n\ts_waitcnt lgkmcnt(8)\n\tv_mfma_f32_16x16x32_bf16 v[44:47], v[120:123], v[184:187], v[44:47]\n\t"
            "ds_read_b128 v[140:143], v2 offset:10240\n\tv_mfma_f32_16x16x32_bf16 v[56:59], v[124:127], v[184:187], v[56:59]\n\tds_read_b128 v[144:147], v2 offset:12288\n\tv_mfma_f32_16x16x32_bf16 v[68:71], v[128:131], v[184:187], v[68:71]\n\tds_read_b128 v[164:167], v3 offset:8192\n\ts_waitcnt lgkmcnt(8)\n\t"
            "v_mfma_f32_16x16x32_bf16 v[80:83], v[132:135], v[184:187], v[80:83]\n\tds_read_b128 v[168:171], v3 offset:10240\n\tds_read_b128 v[172:175], v3 offset:12288\n\ts_waitcnt lgkmcnt(7)\n\tv_mfma_f32_16x16x32_bf16 v[92:95], v[136:139], v[184:187], v[92:95]\n\tds_read_b128 v[84:87], v15 offset:256\n\t"
            "s_waitcnt lgkmcnt(6)\n\tv_mfma_f32_16x16x32_bf16 v[44:47], v[148:151], v[196:199], v[44:47]\n\tds_read_b128 v[96:99], v15 offset:320\n\tv_mfma_f32_16x16x32_bf16 v[56:59], v[152:155], v[196:199], v[56:59]\n\tds_read_b128 v[108:111], v15 offset:384\n\tv_mfma_f32_16x16x32_bf16 v[68:71], v[156:159], v[196:199], v[68:71]\n\t"
            "s_nop 3\n\tv_mov_b32 v216, v44\n\tv_mov_b32 v217, v44\n\tv_mfma_f32_16x16x32_bf16 v[80:83], v[160:163], v[196:199], v[80:83]\n\ts_nop 0\n\tv_permlane32_swap_b32 v216, v217\n\t"
            "v_mov_b32 v217, v216\n\ts_waitcnt lgkmcnt(5)\n\tv_mfma_f32_16x16x32_bf16 v[104:107], v[140:143], v[184:187], v[104:107]\n\tv_permlane16_swap_b32 v216, v217\n\tv_fmac_f32 v44, v36, v216\n\tv_mfma_f32_16x16x32_bf16 v[116:119], v[144:147], v[184:187], v[116:119]\n\t"
            "v_fmac_f32 v45, v37, v216\n\tv_fmac_f32 v46, v38, v216\n\tv_fmac_f32 v47, v39, v216\n\tv_cvt_pk_bf16_f32 v44, v44, v45\n\tv_cvt_pk_bf16_f32 v45, v46, v47\n\tv_mfma_f32_16x16x32_bf16 v[92:95], v[164:167], v[196:199], v[92:95]\n\t"
            "v_and_b32 v220, v44, v22\n\tv_and_b32 v221, v45, v22\n\ts_waitcnt lgkmcnt(0)\n\tv_mfma_f32_16x16x32_bf16 v[104:107], v[168:171], v[196:199], v[104:107]\n\tv_and_b32 v236, v44, v23\n\tv_and_b32 v237, v45, v23\n\t"
            "v_mfma_f32_16x16x32_bf16 v[116:119], v[172:175], v[196:199], v[116:119]\n\tv_fmac_f32 v56, v48, v216\n\tv_fmac_f32 v57, v49, v216\n\tv_fmac_f32 v58, v50, v216\n\tv_fmac_f32 v59, v51, v216\n\tv_cvt_pk_bf16_f32 v56, v56, v57\n\t"
            "v_cvt_pk_bf16_f32 v57, v58, v59\n\tv_and_b32 v222, v56, v22\n\tv_and_b32 v223, v57, v22\n\tv_and_b32 v238, v56, v23\n\tv_and_b32 v239, v57, v23\n\tv_fmac_f32 v68, v60, v216\n\t"
            "v_fmac_f32 v69, v61, v216\n\tv_fmac_f32 v70, v62, v216\n\tv_fmac_f32 v71, v63, v216\n\tds_read_b32 v27, v32\n\tds_read2st64_b32 v[42:43], v31 offset0:0 offset1:1\n\tds_read2st64_b32 v[40:41], v30 offset0:0 offset1:1\n\t"
            "ds_read2st64_b32 v[66:67], v31 offset0:2 offset1:3\n\tds_read2st64_b32 v[64:65], v30 offset0:2 offset1:3\n\tv_cvt_pk_bf16_f32 v68, v68, v69\n\tv_cvt_pk_bf16_f32 v69, v70, v71\n\tv_and_b32 v224, v68, v22\n\tv_and_b32 v225, v69, v22\n\t"
            "v_and_b32 v240, v68, v23\n\tv_and_b32 v241, v69, v23\n\tv_fmac_f32 v80, v72, v216\n\tv_fmac_f32 v81, v73, v216\n\tv_fmac_f32 v82, v74, v216\n\tv_fmac_f32 v83, v75, v216\n\t"
            "v_cvt_pk_bf16_f32 v80, v80, v81\n\tv_cvt_pk_bf16_f32 v81, v82, v83\n\tv_and_b32 v226, v80, v22\n\tv_and_b32 v227, v81, v22\n\tv_and_b32 v242, v80, v23\n\tv_and_b32 v243, v81, v23\n\t"
            "s_waitcnt lgkmcnt(1)\n\tv_cmp_ge_u32 vcc, 0, v27\n\ts_cbranch_vccnz 10f\n\t20:\nv_and_b32 v177, v42, v24\n\tv_and_b32 v178, v42, v25\n\t"
            "v_and_b32 v179, v42, v26\n\tv_and_b32 v189, v43, v24\n\tv_and_b32 v190, v43, v25\n\tv_and_b32 v191, v43, v26\n\tv_mfma_f32_16x16x32_bf16 v[44:47], v[234:237], v[176:179], 0\n\tv_and_b32 v181, v40, v24\n\t"
            "v_and_b32 v182, v40, v25\n\tv_mfma_f32_16x16x32_bf16 v[56:59], v[236:239], v[188:191], 0\n\tv_and_b32 v183, v40, v26\n\tv_and_b32 v193, v41, v24\n\tv_and_b32 v194, v41, v25\n\tv_and_b32 v195, v41, v26\n\t"
            "v_mfma_f32_16x16x32_bf16 v[44:47], v[218:221], v[180:183], v[44:47]\n\tv_and_b32 v177, v66, v24\n\tv_and_b32 v178, v66, v25\n\tv_mfma_f32_16x16x32_bf16 v[56:59], v[220:223], v[192:195], v[56:59]\n\tv_and_b32 v179, v66, v26\n\tv_and_b32 v189, v67, v24\n\t"
            "v_and_b32 v190, v67, v25\n\ts_nop 1\n\tv_cvt_pk_bf16_f32 v36, v44, v45\n\tv_cvt_pk_bf16_f32 v37, v46, v47\n\tv_min_u32 v36, -2, v36\n\tv_and_b32 v191, v67, v26\n\t"
            "v_cvt_pk_bf16_f32 v38, v56, v57\n\tv_cvt_pk_bf16_f32 v39, v58, v59\n\tv_min_u32 v38, -2, v38\n\tv_mfma_f32_16x16x32_bf16 v[68:71], v[238:241], v[176:179], 0\n\ts_nop 0\n\tv_permlane16_swap_b32 v36, v38\n\t"
            "v_permlane16_swap_b32 v37, v39\n\tbuffer_store_dwordx4 v[36:39], v16, s[44:47], s20 offen sc1\n\tv_mfma_f32_16x16x32_bf16 v[80:83], v[240:243], v[188:191], 0\n\ts_waitcnt lgkmcnt(0)\n\tv_and_b32 v181, v64, v24\n\tv_and_b32 v182, v64, v25\n\t"
            "v_and_b32 v183, v64, v26\n\tv_and_b32 v193, v65, v24\n\tv_and_b32 v194, v65, v25\n\tv_and_b32 v195, v65, v26\n\tv_mfma_f32_16x16x32_bf16 v[68:71], v[222:225], v[180:183], v[68:71]\n\tv_fmac_f32 v92, v84, v216\n\t"
            "v_fmac_f32 v93, v85, v216\n\tv_mfma_f32_16x16x32_bf16 v[80:83], v[224:227], v[192:195], v[80:83]\n\tv_fmac_f32 v94, v86, v216\n\tv_fmac_f32 v95, v87, v216\n\tv_cvt_pk_bf16_f32 v92, v92, v93\n\ts_nop 1\n\t"
            "v_cvt_pk_bf16_f32 v60, v68, v69\n\tv_cvt_pk_bf16_f32 v61, v70, v71\n\tv_min_u32 v60, -2, v60\n\tv_fmac_f32 v104, v96, v216\n\tv_cvt_pk_bf16_f32 v62, v80, v81\n\tv_cvt_pk_bf16_f32 v63, v82, v83\n\t"
            "v_min_u32 v62, -2, v62\n\tv_fmac_f32 v105, v97, v216\n\tv_permlane16_swap_b32 v61, v63\n\tv_permlane16_swap_b32 v60, v62\n\tbuffer_store_dwordx4 v[60:63], v16, s[44:47], s21 offen sc1\n\tds_write_b32 v34, v35\n\t"
            "v_cvt_pk_bf16_f32 v93, v94, v95\n\tv_fmac_f32 v106, v98, v216\n\tv_fmac_f32 v107, v99, v216\n\tv_cvt_pk_bf16_f32 v104, v104, v105\n\tv_fmac_f32 v116, v108, v216\n\tv_fmac_f32 v117, v109, v216\n\t"
            "v_and_b32 v228, v92, v22\n\tds_read_b32 v27, v32\n\tds_read2st64_b32 v[102:103], v31 offset0:152 offset1:153\n\tds_read2st64_b32 v[100:101], v30 offset0:152 offset1:153\n\tds_read2st64_b32 v[78:79], v31 offset0:154 offset1:155\n\tds_read2st64_b32 v[76:77], v30 offset0:154 offset1:155\n\t"
            "v_and_b32 v229, v93, v22\n\tv_and_b32 v244, v92, v23\n\tv_and_b32 v245, v93, v23\n\tv_cvt_pk_bf16_f32 v105, v106, v107\n\tv_fmac_f32 v118, v110, v216\n\tv_fmac_f32 v119, v111, v216\n\t"
            "v_cvt_pk_bf16_f32 v116, v116, v117\n\tv_and_b32 v230, v104, v22\n\tv_and_b32 v231, v105, v22\n\tv_and_b32 v246, v104, v23\n\tv_and_b32 v247, v105, v23\n\tv_cvt_pk_bf16_f32 v117, v118, v119\n\t"
            "v_and_b32 v232, v116, v22\n\tv_and_b32 v233, v117, v22\n\tv_and_b32 v248, v116, v23\n\tv_and_b32 v249, v117, v23\n\ts_waitcnt lgkmcnt(3)\n\tv_cmp_ge_u32 vcc, 1, v27\n\t"
            "s_cbranch_vccnz 11f\n\t21:\nv_and_b32 v177, v102, v24\n\tv_and_b32 v178, v102, v25\n\tv_and_b32 v179, v102, v26\n\tv_and_b32 v189, v103, v24\n\t"
            "v_and_b32 v190, v103, v25\n\tv_and_b32 v191, v103, v26\n\tv_mfma_f32_16x16x32_bf16 v[92:95], v[242:245], v[176:179], 0\n\ts_waitcnt lgkmcnt(0)\n\tv_and_b32 v181, v100, v24\n\tv_and_b32 v182, v100, v25\n\t"
            "v_mfma_f32_16x16x32_bf16 v[104:107], v[244:247], v[188:191], 0\n\tv_and_b32 v183, v100, v26\n\tv_and_b32 v193, v101, v24\n\tv_and_b32 v194, v101, v25\n\tv_and_b32 v195, v101, v26\n\tv_mfma_f32_16x16x32_bf16 v[92:95], v[226:229], v[180:183], v[92:95]\n\t"
            "v_and_b32 v177, v78, v24\n\tv_and_b32 v178, v78, v25\n\tv_mfma_f32_16x16x32_bf16 v[104:107], v[228:231], v[192:195], v[104:107]\n\tv_and_b32 v179, v78, v26\n\tv_and_b32 v181, v76, v24\n\tv_and_b32 v182, v76, v25\n\t"
            "v_mfma_f32_16x16x32_bf16 v[116:119], v[246:249], v[176:179], 0\n\tv_and_b32 v183, v76, v26\n\tv_cvt_pk_bf16_f32 v84, v92, v93\n\ts_nop 1\n\tv_cvt_pk_bf16_f32 v86, v104, v105\n\tv_mfma_f32_16x16x32_bf16 v[116:119], v[230:233], v[180:183], v[116:119]\n\t"
            "v_cvt_pk_bf16_f32 v85, v94, v95\n\tv_min_u32 v84, -2, v84\n\tv_cvt_pk_bf16_f32 v87, v106, v107\n\tv_min_u32 v86, -2, v86\n\ts_nop 0\n\tv_permlane16_swap_b32 v85, v87\n\t"
            "v_permlane16_swap_b32 v84, v86\n\tbuffer_store_dwordx4 v[84:87], v16, s[44:47], s22 offen sc1\n\tv_cvt_pk_bf16_f32 v108, v116, v117\n\tv_cvt_pk_bf16_f32 v109, v118, v119\n\tv_min_u32 v108, -2, v108\n\ts_nop 0\n\t"
            "v_permlane16_swap_b32 v109, v111\n\tv_permlane16_swap_b32 v108, v110\n\tbuffer_store_dwordx4 v[108:111], v215, s[44:47], s23 offen sc1\n\tds_write_b32 v34, v34\n\t"
            "s_setprio 0\n\t"
            "s_cmp_eq_u32 s69, 1\n\ts_cbranch_scc1 .Lout\n\ts_endpgm\n\t10:\nds_read_b32 v27, v32\n\tds_read2st64_b32 v[42:43], v31 offset0:0 offset1:1\n\t"
            "ds_read2st64_b32 v[40:41], v30 offset0:0 offset1:1\n\tds_read2st64_b32 v[66:67], v31 offset0:2 offset1:3\n\tds_read2st64_b32 v[64:65], v30 offset0:2 offset1:3\n\ts_waitcnt lgkmcnt(0)\n\tv_cmp_ge_u32 vcc, 0, v27\n\ts_cbranch_vccz 20b\n\t"
            "s_sleep 1\n\t"
            "s_sub_u32 s68, s68, 1\n\ts_cbranch_scc0 10b\n\ts_branch 20b\n\t11:\nds_read_b32 v27, v32\n\tds_read2st64_b32 v[102:103], v31 offset0:152 offset1:153\n\t"
            "ds_read2st64_b32 v[100:101], v30 offset0:152 offset1:153\n\tds_read2st64_b32 v[78:79], v31 offset0:154 offset1:155\n\tds_read2st64_b32 v[76:77], v30 offset0:154 offset1:155\n\ts_waitcnt lgkmcnt(0)\n\tv_cmp_ge_u32 vcc, 1, v27\n\ts_cbranch_vccz 21b\n\t"
            "s_sleep 1\n\t"
            "s_sub_u32 s68, s68, 1\n\ts_cbranch_scc0 11b\n\ts_branch 21b\n\t"
            ".LB:\ns_and_b32 s72, s56, 1\n\ts_lshr_b32 s73, s56, 1\n\tv_and_b32 v156, 2, v51\n\tv_lshlrev_b32 v156, 1, v156\n\tv_and_b32 v157, 7, v52\n\t"
            "v_xor_b32 v157, v156, v157\n\tv_mul_u32_u24 v156, 0x500, v51\n\tv_lshl_add_u32 v156, v157, 4, v156\n\ts_mul_i32 s74, s72, 0x2800\n\ts_mul_i32 s75, s73, 0xf000\n\ts_add_i32 s75, s75, s74\n\t"
            "v_add_u32 v154, s75, v156\n\ts_mul_i32 s75, s73, 0x5000\n\ts_add_i32 s75, s75, s74\n\ts_addk_i32 s75, 0x5000\n\tv_add_u32 v156, s75, v156\n\ts_cmp_lg_u32 s63, 0\n\t"
            "s_cselect_b32 s74, 81920, 0\n\ts_max_u32 s74, s74, 65536\n\tv_add_u32 v158, s62, v52\n\tv_min_u32 v159, 47, v52\n\ts_add_i32 s75, s62, 64\n\tv_add_u32 v159, s75, v159\n\t"
            "v_max_i32 v158, 0, v158\n\tv_min_i32 v159, 0x3ff, v159\n\tv_lshl_add_u32 v158, v158, 4, s74\n\tv_lshl_add_u32 v159, v159, 4, s74\n\tv_lshl_add_u32 v59, v53, 4, v57\n\ts_cmp_lg_u32 s73, 0\n\t"
            "s_cselect_b64 vcc, -1, 0\n\tv_cndmask_b32 v97, v59, v52, vcc\n\ts_lshl_b32 s75, s72, 6\n\ts_add_i32 s75, s75, s62\n\tv_add_u32 v97, s75, v97\n\tv_mov_b32 v60, s62\n\t"
            "v_add_u32 v60, 0x6f, v60\n\tv_min_i32 v60, 0x3ff, v60\n\tv_med3_i32 v97, v97, 0, v60\n\ts_lshl3_add_u32 s74, s73, s74\n\tv_lshl_add_u32 v97, v97, 4, s74\n\ts_mul_i32 s30, s73, 576\n\t"
            "s_lshl_b32 s75, s72, 8\n\ts_add_i32 s30, s30, s75\n\ts_add_i32 s30, s30, 160000\n\ts_lshl_b32 s74, s59, 7\n\ts_add_i32 s74, s74, 8192\n\tv_min_u32 v96, 9, v52\n\t"
            "v_lshl_add_u32 v96, v96, 2, s74\n\ts_lshr_b32 s75, s58, 5\n\ts_add_i32 s74, s74, s75\n\ts_cmp_eq_u32 s56, 3\n\ts_cselect_b32 s74, s74, 0x40000000\n\tv_lshl_add_u32 v98, v52, 24, s74\n\t"
            "s_cselect_b32 s75, 0, 0x40000000\n\tv_add_u32 v96, s75, v96\n\ts_cselect_b32 s75, 1, 0\n\ts_lshl2_add_u32 s74, s59, s56\n\ts_mul_i32 s74, s74, 0xa000\n\ts_add_i32 s74, s74, 155648\n\t"
            "v_lshl_add_u32 v101, v52, 4, s74\n\tv_and_b32 v60, s75, v53\n\tv_lshl_add_u32 v2, v60, 30, v101\n\ts_xor_b32 s82, s56, 0\n\ts_cmp_ge_u32 s82, s57\n\ts_addc_u32 s76, s82, 0\n\t"
            "s_and_b32 s16, s76, 3\n\ts_lshl_b32 s77, s76, 1\n\ts_max_u32 s78, s77, 1\n\ts_sub_u32 s78, s78, 1\n\ts_min_u32 s79, s77, 7\n\tv_lshlrev_b32 v60, 9, v53\n\t"
            "v_lshl_add_u32 v60, v54, 3, v60\n\ts_mul_i32 s80, s78, 0xc00\n\ts_mul_i32 s81, s79, 0xc00\n\ts_addk_i32 s80, 14336\n\ts_addk_i32 s81, 14336\n\tv_add_u32 v24, s80, v60\n\t"
            "v_add_u32 v26, s81, v60\n\tv_add_u32 v25, 38912, v24\n\tv_add_u32 v27, 38912, v26\n\tv_lshl_add_u32 v29, v52, 4, s80\n\ts_lshr_b32 s78, s78, 1\n\ts_lshr_b32 s79, s79, 1\n\t"
            "s_sub_u32 s79, s79, s78\n\ts_lshl2_add_u32 s78, s78, 159744\n\tv_and_b32 v28, s79, v52\n\tv_lshl_add_u32 v28, v28, 2, s78\n\ts_lshl2_add_u32 s78, s82, 159744\n\tv_mov_b32 v30, s78\n\t"
            "s_mov_b32 s78, 160576\n\tv_lshl_add_u32 v153, v59, 2, s78\n\ts_mul_i32 s79, s59, 163840\n\ts_lshl_b32 s80, s58, 5\n\ts_add_i32 s79, s79, s80\n\ts_cmp_eq_u32 s76, 0\n\t"
            "s_cselect_b32 s80, 0, 0xc00\n\ts_add_i32 s79, s79, s80\n\ts_add_i32 s79, s79, 155652\n\tv_lshl_add_u32 v31, v54, 4, s79\n\tv_lshrrev_b32 v60, 1, v53\n\tv_mul_u32_u24 v60, 0xa000, v60\n\t"
            "v_add_u32 v31, v60, v31\n\tv_and_b32 v60, 1, v53\n\tv_lshl_add_u32 v31, v60, 3, v31\n\ts_mov_b32 s31, 0x14000\n\ts_cmp_lt_i32 s62, 0\n\ts_cselect_b32 s79, 8, -16\n\t"
            "v_mov_b32 v61, 0xff800000\n\tv_mov_b32 v142, 1\n\tv_add_u32 v60, 0, v59\n\tv_and_b32 v60, -8, v60\n\tv_add_u32 v60, 0, v60\n\tv_cmp_gt_i32 vcc, s79, v60\n\t"
            "v_lshl_add_u32 v160, v60, 2, s78\n\ts_nop 0\n\tv_cndmask_b32 v128, 0, v61, vcc\n\tv_add_u32 v60, 4, v59\n\tv_and_b32 v60, -8, v60\n\tv_add_u32 v60, -4, v60\n\t"
            "v_cmp_gt_i32 vcc, s79, v60\n\tv_lshl_add_u32 v161, v60, 2, s78\n\ts_nop 0\n\tv_cndmask_b32 v129, 0, v61, vcc\n\tv_add_u32 v60, -1, v59\n\tv_and_b32 v60, -8, v60\n\t"
            "v_add_u32 v60, 1, v60\n\tv_cmp_gt_i32 vcc, s79, v60\n\tv_lshl_add_u32 v162, v60, 2, s78\n\ts_nop 0\n\tv_cndmask_b32 v130, 0, v61, vcc\n\tv_add_u32 v60, 3, v59\n\t"
            "v_and_b32 v60, -8, v60\n\tv_add_u32 v60, -3, v60\n\tv_cmp_gt_i32 vcc, s79, v60\n\tv_lshl_add_u32 v163, v60, 2, s78\n\ts_nop 0\n\tv_cndmask_b32 v131, 0, v61, vcc\n\t"
            "v_add_u32 v60, -2, v59\n\tv_and_b32 v60, -8, v60\n\tv_add_u32 v60, 2, v60\n\tv_cmp_gt_i32 vcc, s79, v60\n\tv_lshl_add_u32 v164, v60, 2, s78\n\ts_nop 0\n\t"
            "v_cndmask_b32 v132, 0, v61, vcc\n\tv_add_u32 v60, 2, v59\n\tv_and_b32 v60, -8, v60\n\tv_add_u32 v60, -2, v60\n\tv_cmp_gt_i32 vcc, s79, v60\n\tv_lshl_add_u32 v165, v60, 2, s78\n\t"
            "s_nop 0\n\tv_cndmask_b32 v133, 0, v61, vcc\n\tv_add_u32 v60, -3, v59\n\tv_and_b32 v60, -8, v60\n\tv_add_u32 v60, 3, v60\n\tv_cmp_gt_i32 vcc, s79, v60\n\t"
            "v_lshl_add_u32 v166, v60, 2, s78\n\ts_nop 0\n\tv_cndmask_b32 v134, 0, v61, vcc\n\tv_add_u32 v60, 1, v59\n\tv_and_b32 v60, -8, v60\n\tv_add_u32 v60, -1, v60\n\t"
            "v_cmp_gt_i32 vcc, s79, v60\n\tv_lshl_add_u32 v167, v60, 2, s78\n\ts_nop 0\n\tv_cndmask_b32 v135, 0, v61, vcc\n\ts_lshl2_add_u32 s80, s71, s56\n\ts_lshl_b32 s80, s80, 5\n\t"
            "v_add_u32 v100, s80, v52\n\tv_mul_u32_u24 v100, 0x500, v100\n\ts_add_i32 s80, s58, 3072\n\tv_add_u32 v100, s80, v100\n\tv_min_u32 v104, 51, v52\n\tv_lshlrev_b32 v104, 4, v104\n\t"
            "v_add_u32 v104, 0x1000, v104\n\ts_lshl_b32 s4, s64, 12\n\ts_add_u32 s5, s4, 0x1000\n\ts_cmpk_eq_u32 s5, 0x5000\n\ts_cselect_b32 s5, 0, s5\n\ts_add_u32 s6, s5, 0x1000\n\t"
            "s_cmpk_eq_u32 s6, 0x5000\n\ts_cselect_b32 s6, 0, s6\n\ts_add_u32 s7, s6, 0x1000\n\ts_cmpk_eq_u32 s7, 0x5000\n\ts_cselect_b32 s7, 0, s7\n\ts_add_u32 s8, s7, 0x1000\n\t"
            "s_cmpk_eq_u32 s8, 0x5000\n\ts_cselect_b32 s8, 0, s8\n\ts_cmpk_lt_u32 s58, 0x280\n\ts_cselect_b32 s80, 0, 0x5000\n\ts_xor_b32 s81, s80, 0x5000\n\ts_add_u32 s9, s4, s81\n\t"
            "s_add_u32 s4, s4, s80\n\ts_add_u32 s10, s5, s81\n\ts_add_u32 s5, s5, s80\n\ts_add_u32 s11, s6, s81\n\ts_add_u32 s6, s6, s80\n\ts_add_u32 s12, s7, s81\n\t"
            "s_add_u32 s7, s7, s80\n\ts_add_u32 s13, s8, s81\n\ts_add_u32 s8, s8, s80\n\ts_mul_i32 s20, s4, 20\n\ts_add_i32 s20, s20, s58\n\ts_addk_i32 s20, 3072\n\t"
            "s_mul_i32 s21, s5, 20\n\ts_add_i32 s21, s21, s58\n\ts_addk_i32 s21, 2048\n\ts_mul_i32 s22, s6, 20\n\ts_add_i32 s22, s22, s58\n\ts_addk_i32 s22, 1024\n\t"
            "s_mul_i32 s23, s7, 20\n\ts_add_i32 s23, s23, s58\n\ts_mul_i32 s24, s8, 20\n\ts_add_i32 s24, s24, s58\n\ts_addk_i32 s24, 3072\n\ts_mul_i32 s25, s9, 20\n\t"
            "s_add_i32 s25, s25, s58\n\ts_addk_i32 s25, 2048\n\ts_mul_i32 s26, s10, 20\n\ts_add_i32 s26, s26, s58\n\ts_addk_i32 s26, 1024\n\ts_mul_i32 s27, s11, 20\n\t"
            "s_add_i32 s27, s27, s58\n\ts_mul_i32 s28, s12, 20\n\ts_add_i32 s28, s28, s58\n\ts_addk_i32 s28, 3072\n\ts_mul_i32 s29, s13, 20\n\ts_add_i32 s29, s29, s58\n\t"
            "s_addk_i32 s29, 2048\n\ts_mul_i32 s72, s60, 1920\n\ts_sub_u32 s14, s61, s72\n\ts_mul_i32 s15, s56, 0x2800\n\ts_add_u32 s15, s15, 77824\n\tv_lshl_add_u32 v152, v52, 4, s15\n\t"
            "s_lshl_b32 s18, s58, 5\n\ts_mov_b32 s50, 822272\n\ts_mov_b32 s51, 0x20000\n\ts_waitcnt lgkmcnt(0)\n\ts_load_dword s67, s[44:45], 0x0\n\ts_sub_u32 s48, s48, 3072\n\t"
            "s_subb_u32 s49, s49, 0\n\ts_lshl_b32 s78, s56, 5\n\tv_add_u32 v12, s78, v57\n\tv_add_u32 v3, s62, v12\n\tv_and_b32 v13, 1, v53\n\tv_lshrrev_b32 v14, 1, v53\n\t"
            "v_lshl_add_u32 v15, v13, 1, v14\n\ts_add_u32 s79, s63, s58\n\tv_lshl_add_u32 v15, v15, 4, s79\n\ts_movk_i32 s79, 0xa00\n\tv_mad_i32_i24 v3, v3, s79, v15\n\tv_cmp_gt_u32 vcc, 8, v12\n\t"
            "s_cmp_eq_u32 s56, 3\n\ts_cselect_b32 s79, 2.0, 0xa000\n\tv_add_u32 v11, s79, v3\n\tv_cndmask_b32 v3, v3, 2.0, vcc\n\tv_mov_b32 v8, 159744\n\tv_and_b32 v9, 31, v52\n\t"
            "v_add_u32 v9, s78, v9\n\tv_lshl_add_u32 v9, v9, 2, v8\n\tv_lshl_add_u32 v10, v13, 4, v12\n\tv_lshl_add_u32 v10, v10, 2, v8\n\tv_mul_u32_u24 v4, 0x2800, v14\n\tv_lshl_add_u32 v4, v56, 7, v4\n\t"
            "v_lshl_add_u32 v4, v55, 3, v4\n\tv_lshl_add_u32 v4, v13, 5, v4\n\tv_bfe_u32 v5, v52, 3, 1\n\tv_lshl_add_u32 v4, v5, 6, v4\n\tv_add_u32 v4, 77824, v4\n\tv_xor_b32 v5, 64, v4\n\t"
            "v_add_u32 v6, 0x5000, v4\n\tv_add_u32 v7, 0x5000, v5\n\ts_add_u32 s78, s63, 1280\n\tv_lshlrev_b32 v23, 4, v14\n\ts_cmp_lg_u32 s56, 3\n\ts_cbranch_scc1 1f\n\t"
            "v_lshlrev_b32 v12, 6, v13\n\tv_sub_u32 v9, v9, v12\n\tv_sub_u32 v10, v10, v12\n\tv_lshlrev_b32 v23, 4, v53\n\tv_lshl_add_u32 v22, v13, 8, v101\n\tv_lshl_add_u32 v22, v14, 9, v22\n\t"
            "v_lshl_add_u32 v11, v14, 11, v22\n\tv_mul_u32_u24 v4, 0x2800, v53\n\tv_lshl_add_u32 v4, v56, 7, v4\n\tv_lshl_add_u32 v4, v55, 3, v4\n\tv_bfe_u32 v5, v52, 3, 1\n\tv_lshl_add_u32 v4, v5, 6, v4\n\t"
            "v_add_u32 v4, 77824, v4\n\tv_xor_b32 v5, 64, v4\n\t1:\ns_mov_b32 s32, s60\n\ts_mov_b32 s83, 0\n\ts_add_u32 s33, s32, 256\n\t"
            "s_cmp_eq_u32 s33, s78\n\ts_cselect_b32 s33, s63, s33\n\ts_add_u32 s34, s33, 256\n\ts_cmp_eq_u32 s34, s78\n\ts_cselect_b32 s34, s63, s34\n\ts_add_u32 s35, s34, 256\n\t"
            "s_cmp_eq_u32 s35, s78\n\ts_cselect_b32 s35, s63, s35\n\ts_add_u32 s17, s35, 256\n\ts_cmp_eq_u32 s17, s78\n\ts_cselect_b32 s17, s63, s17\n\ts_add_u32 s80, s32, 128\n\t"
            "s_bitcmp1_b32 31, s64\n\ts_cselect_b32 s82, s34, s33\n\ts_bitcmp1_b32 0, s64\n\ts_cselect_b32 s82, s80, s82\n\ts_bitcmp1_b32 30, s64\n\ts_cselect_b32 s81, s35, s34\n\t"
            "s_bitcmp1_b32 1, s64\n\ts_cselect_b32 s81, s80, s81\n\ts_bitcmp1_b32 28, s64\n\ts_cselect_b32 s79, s17, s35\n\ts_bitcmp1_b32 2, s64\n\ts_cselect_b32 s79, s80, s79\n\t"
            "s_bitcmp1_b32 28, s64\n\ts_cselect_b32 s78, s80, s17\n\ts_waitcnt vmcnt(0) lgkmcnt(0)\n\ts_barrier\n\t"
            "v_mov_b32 v136, -1\n\tv_mov_b32 v137, -1\n\tv_mov_b32 v138, -1\n\tv_mov_b32 v139, -1\n\ts_cmp_ge_u32 s56, 2\n\ts_cselect_b64 vcc, -1, 0\n\t"
            "s_mov_b32 s60, s82\n\ts_mul_i32 s61, s60, 1920\n\ts_add_u32 m0, s66, 0\n\ts_add_u32 s61, s61, s14\n\tbuffer_load_dwordx4 v40, s[40:43], s61 offen lds\n\tbuffer_load_dwordx4 v41, s[40:43], s61 offen offset:1024 lds\n\t"
            "buffer_load_dwordx4 v42, s[40:43], s61 offen offset:2048 lds\n\tbuffer_load_dwordx4 v43, s[40:43], s61 offen offset:3072 lds\n\ts_addk_i32 m0, 0x1000\n\ts_nop 0\n\tbuffer_load_dwordx4 v44, s[40:43], s61 offen lds\n\tbuffer_load_dwordx4 v45, s[40:43], s61 offen offset:1024 lds\n\t"
            "s_add_u32 m0, s65, 0\n\ts_cbranch_vccnz 1f\n\tbuffer_load_dwordx4 v39, s[36:39], s60 offen offset:3072 lds\n\t1:\nbuffer_load_dwordx4 v36, s[36:39], s60 offen lds\n\tbuffer_load_dwordx4 v37, s[36:39], s60 offen offset:1024 lds\n\t"
            "buffer_load_dwordx4 v38, s[36:39], s60 offen offset:2048 lds\n\ts_mul_i32 s67, s67, 0x19660d\n\ts_add_u32 s67, s67, 0x3c6ef35f\n\ts_barrier\n\ts_mov_b32 s60, s81\n\ts_mul_i32 s61, s60, 1920\n\t"
            "s_add_u32 m0, s66, 38912\n\ts_add_u32 s61, s61, s14\n\tbuffer_load_dwordx4 v40, s[40:43], s61 offen lds\n\tbuffer_load_dwordx4 v41, s[40:43], s61 offen offset:1024 lds\n\tbuffer_load_dwordx4 v42, s[40:43], s61 offen offset:2048 lds\n\tbuffer_load_dwordx4 v43, s[40:43], s61 offen offset:3072 lds\n\t"
            "s_addk_i32 m0, 0x1000\n\ts_nop 0\n\tbuffer_load_dwordx4 v44, s[40:43], s61 offen lds\n\tbuffer_load_dwordx4 v45, s[40:43], s61 offen offset:1024 lds\n\ts_add_u32 m0, s65, 38912\n\ts_cbranch_vccnz 1f\n\t"
            "buffer_load_dwordx4 v39, s[36:39], s60 offen offset:3072 lds\n\t1:\nbuffer_load_dwordx4 v36, s[36:39], s60 offen lds\n\tbuffer_load_dwordx4 v37, s[36:39], s60 offen offset:1024 lds\n\tbuffer_load_dwordx4 v38, s[36:39], s60 offen offset:2048 lds\n\ts_bitcmp1_b32 13, s64\n\t"
            "s_cbranch_scc0 1f\n\tbuffer_store_dwordx4 v[136:139], v101, s[44:47], s18 offen offset:0 sc1\n\tbuffer_store_dwordx4 v[136:139], v2, s[44:47], s18 offen offset:1024 sc1\n\tbuffer_store_dwordx4 v[136:139], v2, s[44:47], s18 offen offset:2048 sc1\n\tbuffer_store_dwordx4 v[136:139], v101, s[44:47], s18 offen offset:3072 sc1\n\t1:\n"
            "s_bitcmp1_b32 13, s64\n\ts_cbranch_scc1 2f\n\ts_cbranch_vccz 1f\n\ts_waitcnt vmcnt(9)\n\t1:\ns_waitcnt vmcnt(10)\n\t"
            "2:\ns_cbranch_vccz 1f\n\ts_waitcnt vmcnt(13)\n\t1:\ns_waitcnt vmcnt(14)\n\ts_barrier\n\t"
            "s_mov_b32 s60, s79\n\ts_mul_i32 s61, s60, 1920\n\ts_add_u32 m0, s66, 77824\n\ts_add_u32 s61, s61, s14\n\tbuffer_load_dwordx4 v40, s[40:43], s61 offen lds\n\tbuffer_load_dwordx4 v41, s[40:43], s61 offen offset:1024 lds\n\t"
            "buffer_load_dwordx4 v42, s[40:43], s61 offen offset:2048 lds\n\tbuffer_load_dwordx4 v43, s[40:43], s61 offen offset:3072 lds\n\ts_addk_i32 m0, 0x1000\n\ts_nop 0\n\tbuffer_load_dwordx4 v44, s[40:43], s61 offen lds\n\tbuffer_load_dwordx4 v45, s[40:43], s61 offen offset:1024 lds\n\t"
            "s_add_u32 m0, s65, 77824\n\ts_cbranch_vccnz 1f\n\tbuffer_load_dwordx4 v39, s[36:39], s60 offen offset:3072 lds\n\t1:\nbuffer_load_dwordx4 v36, s[36:39], s60 offen lds\n\tbuffer_load_dwordx4 v37, s[36:39], s60 offen offset:1024 lds\n\t"
            "buffer_load_dwordx4 v38, s[36:39], s60 offen offset:2048 lds\n\ts_bitcmp1_b32 18, s64\n\ts_cbranch_scc0 1f\n\tbuffer_store_dwordx4 v[136:139], v101, s[44:47], s18 offen offset:0 sc1\n\tbuffer_store_dwordx4 v[136:139], v2, s[44:47], s18 offen offset:1024 sc1\n\tbuffer_store_dwordx4 v[136:139], v2, s[44:47], s18 offen offset:2048 sc1\n\t"
            "buffer_store_dwordx4 v[136:139], v101, s[44:47], s18 offen offset:3072 sc1\n\t1:\ns_bitcmp1_b32 31, s64\n\ts_cbranch_scc1 2f\n\ts_cbranch_vccz 1f\n\ts_waitcnt vmcnt(9)\n\t"
            "1:\ns_waitcnt vmcnt(10)\n\t2:\ns_cbranch_vccz 1f\n\ts_waitcnt vmcnt(13)\n\t1:\n"
            "s_waitcnt vmcnt(14)\n\ts_barrier\n\ts_mov_b32 s60, s78\n\ts_mul_i32 s61, s60, 1920\n\ts_add_u32 m0, s66, 0\n\ts_add_u32 s61, s61, s14\n\t"
            "buffer_load_dwordx4 v40, s[40:43], s61 offen lds\n\tbuffer_load_dwordx4 v41, s[40:43], s61 offen offset:1024 lds\n\tbuffer_load_dwordx4 v42, s[40:43], s61 offen offset:2048 lds\n\tbuffer_load_dwordx4 v43, s[40:43], s61 offen offset:3072 lds\n\ts_addk_i32 m0, 0x1000\n\ts_nop 0\n\t"
            "buffer_load_dwordx4 v44, s[40:43], s61 offen lds\n\tbuffer_load_dwordx4 v45, s[40:43], s61 offen offset:1024 lds\n\ts_add_u32 m0, s65, 0\n\ts_cbranch_vccnz 1f\n\tbuffer_load_dwordx4 v39, s[36:39], s60 offen offset:3072 lds\n\t1:\n"
            "buffer_load_dwordx4 v36, s[36:39], s60 offen lds\n\tbuffer_load_dwordx4 v37, s[36:39], s60 offen offset:1024 lds\n\tbuffer_load_dwordx4 v38, s[36:39], s60 offen offset:2048 lds\n\ts_cbranch_vccz 1f\n\ts_waitcnt vmcnt(9)\n\t1:\n"
            "s_waitcnt vmcnt(10)\n\ts_barrier\n\ts_add_u32 s60, s33, 128\n\ts_mul_i32 s61, s60, 1920\n\ts_add_u32 m0, s66, 38912\n\ts_add_u32 s61, s61, s14\n\t"
            "buffer_load_dwordx4 v40, s[40:43], s61 offen lds\n\tbuffer_load_dwordx4 v41, s[40:43], s61 offen offset:1024 lds\n\tbuffer_load_dwordx4 v42, s[40:43], s61 offen offset:2048 lds\n\tbuffer_load_dwordx4 v43, s[40:43], s61 offen offset:3072 lds\n\ts_addk_i32 m0, 0x1000\n\ts_nop 0\n\t"
            "buffer_load_dwordx4 v44, s[40:43], s61 offen lds\n\tbuffer_load_dwordx4 v45, s[40:43], s61 offen offset:1024 lds\n\ts_add_u32 m0, s65, 38912\n\ts_cbranch_vccnz 1f\n\tbuffer_load_dwordx4 v39, s[36:39], s60 offen offset:3072 lds\n\t1:\n"
            "buffer_load_dwordx4 v36, s[36:39], s60 offen lds\n\tbuffer_load_dwordx4 v37, s[36:39], s60 offen offset:1024 lds\n\tbuffer_load_dwordx4 v38, s[36:39], s60 offen offset:2048 lds\n\tv_mov_b32 v140, s67\n\tbuffer_store_dword v140, v98, s[44:47], 0 offen sc1\n\ts_cbranch_vccz 1f\n\t"
            "s_waitcnt vmcnt(10)\n\t1:\ns_waitcnt vmcnt(11)\n\ts_barrier\n\ts_add_u32 s60, s34, 128\n\ts_mul_i32 s61, s60, 1920\n\t"
            "s_add_u32 m0, s66, 77824\n\ts_add_u32 s61, s61, s14\n\tbuffer_load_dwordx4 v40, s[40:43], s61 offen lds\n\tbuffer_load_dwordx4 v41, s[40:43], s61 offen offset:1024 lds\n\tbuffer_load_dwordx4 v42, s[40:43], s61 offen offset:2048 lds\n\tbuffer_load_dwordx4 v43, s[40:43], s61 offen offset:3072 lds\n\t"
            "s_addk_i32 m0, 0x1000\n\ts_nop 0\n\tbuffer_load_dwordx4 v44, s[40:43], s61 offen lds\n\tbuffer_load_dwordx4 v45, s[40:43], s61 offen offset:1024 lds\n\ts_add_u32 m0, s65, 77824\n\ts_cbranch_vccnz 1f\n\t"
            "buffer_load_dwordx4 v39, s[36:39], s60 offen offset:3072 lds\n\t1:\nbuffer_load_dwordx4 v36, s[36:39], s60 offen lds\n\tbuffer_load_dwordx4 v37, s[36:39], s60 offen offset:1024 lds\n\tbuffer_load_dwordx4 v38, s[36:39], s60 offen offset:2048 lds\n\ts_cbranch_vccz 1f\n\t"
            "s_waitcnt vmcnt(10)\n\t1:\ns_waitcnt vmcnt(11)\n\ts_cmp_lg_u32 s56, 3\n\ts_cbranch_scc1 1f\n\tbuffer_load_dwordx4 v[106:109], v158, s[44:47], 0 offen sc1\n\t"
            "buffer_load_dwordx4 v[110:113], v159, s[44:47], 0 offen sc1\n\tbuffer_load_dwordx4 v[114:117], v104, s[44:47], 0 offen sc1\n\t1:\ns_barrier\n\ts_add_u32 s60, s35, 128\n\ts_mul_i32 s61, s60, 1920\n\t"
            "s_add_u32 m0, s66, 0\n\ts_add_u32 s61, s61, s14\n\tbuffer_load_dwordx4 v40, s[40:43], s61 offen lds\n\tbuffer_load_dwordx4 v41, s[40:43], s61 offen offset:1024 lds\n\tbuffer_load_dwordx4 v42, s[40:43], s61 offen offset:2048 lds\n\tbuffer_load_dwordx4 v43, s[40:43], s61 offen offset:3072 lds\n\t"
            "s_addk_i32 m0, 0x1000\n\ts_nop 0\n\tbuffer_load_dwordx4 v44, s[40:43], s61 offen lds\n\tbuffer_load_dwordx4 v45, s[40:43], s61 offen offset:1024 lds\n\ts_add_u32 m0, s65, 0\n\ts_cbranch_vccnz 1f\n\t"
            "buffer_load_dwordx4 v39, s[36:39], s60 offen offset:3072 lds\n\t1:\nbuffer_load_dwordx4 v36, s[36:39], s60 offen lds\n\tbuffer_load_dwordx4 v37, s[36:39], s60 offen offset:1024 lds\n\tbuffer_load_dwordx4 v38, s[36:39], s60 offen offset:2048 lds\n\ts_add_u32 m0, s15, 40960\n\t"
            "s_nop 0\n\tbuffer_load_dwordx4 v156, s[48:51], s20 offen lds\n\ts_cmp_lg_u32 s56, 3\n\ts_cbranch_scc1 2f\n\ts_waitcnt vmcnt(10)\n\ts_mov_b32 s68, 0x200000\n\t"
            "v_xor_b32 v106, s67, v107\n\tv_xor_b32 v108, s67, v109\n\tv_xor_b32 v110, s67, v111\n\tv_xor_b32 v112, s67, v113\n\tv_xor_b32 v114, s67, v115\n\tv_xor_b32 v116, s67, v117\n\t"
            "v_or3_b32 v106, v106, v108, v110\n\tv_or3_b32 v112, v112, v114, v116\n\tv_or_b32 v106, v106, v112\n\tv_cmp_ne_u32 vcc, 0, v106\n\ts_cbranch_vccnz 30f\n\t31:\n"
            "s_mov_b64 vcc, -1\n\t2:\ns_cbranch_vccz 1f\n\ts_waitcnt vmcnt(10)\n\t1:\ns_waitcnt vmcnt(11)\n\t"
            "s_barrier\n\ts_add_u32 s60, s17, 128\n\ts_mul_i32 s61, s60, 1920\n\ts_add_u32 m0, s66, 38912\n\ts_add_u32 s61, s61, s14\n\tbuffer_load_dwordx4 v40, s[40:43], s61 offen lds\n\t"
            "buffer_load_dwordx4 v41, s[40:43], s61 offen offset:1024 lds\n\tbuffer_load_dwordx4 v42, s[40:43], s61 offen offset:2048 lds\n\tbuffer_load_dwordx4 v43, s[40:43], s61 offen offset:3072 lds\n\ts_addk_i32 m0, 0x1000\n\ts_nop 0\n\tbuffer_load_dwordx4 v44, s[40:43], s61 offen lds\n\t"
            "buffer_load_dwordx4 v45, s[40:43], s61 offen offset:1024 lds\n\ts_add_u32 m0, s65, 38912\n\ts_cbranch_vccnz 1f\n\tbuffer_load_dwordx4 v39, s[36:39], s60 offen offset:3072 lds\n\t1:\nbuffer_load_dwordx4 v36, s[36:39], s60 offen lds\n\t"
            "buffer_load_dwordx4 v37, s[36:39], s60 offen offset:1024 lds\n\tbuffer_load_dwordx4 v38, s[36:39], s60 offen offset:2048 lds\n\ts_add_u32 m0, s15, 40960\n\ts_nop 0\n\tbuffer_load_dwordx4 v156, s[48:51], s21 offen offset:1024 lds\n\tbuffer_load_dwordx4 v156, s[48:51], s22 offen offset:2048 lds\n\t"
            "s_mov_b32 m0, s30\n\ts_nop 0\n\tbuffer_load_dword v97, s[44:47], 0 offen sc1 lds\n\tbuffer_load_dword v118, v96, s[44:47], 0 offen sc1\n\ts_cbranch_vccz 1f\n\ts_waitcnt vmcnt(13)\n\t"
            "1:\ns_waitcnt vmcnt(14)\n\ts_barrier\n\ts_add_u32 m0, s15, 40960\n\ts_nop 0\n\tbuffer_load_dwordx4 v156, s[48:51], s23 offen offset:3072 lds\n\t"
            "s_add_u32 m0, s15, 45056\n\ts_nop 0\n\tbuffer_load_dwordx4 v156, s[48:51], s24 offen lds\n\tbuffer_load_dwordx4 v156, s[48:51], s25 offen offset:1024 lds\n\tbuffer_load_dwordx4 v156, s[48:51], s26 offen offset:2048 lds\n\tbuffer_load_dwordx4 v156, s[48:51], s27 offen offset:3072 lds\n\t"
            "s_add_u32 m0, s15, 49152\n\ts_nop 0\n\tbuffer_load_dwordx4 v156, s[48:51], s28 offen lds\n\tbuffer_load_dwordx4 v156, s[48:51], s29 offen offset:1024 lds\n\ts_waitcnt vmcnt(7)\n\tv_cmp_ne_u32 vcc, s67, v118\n\t"
            "s_cmp_lg_u32 s56, 3\n\ts_cbranch_scc1 33f\n\ts_cbranch_vccnz 32f\n\t33:\ns_barrier\n\ts_add_u32 m0, s15, 0\n\t"
            "s_nop 0\n\tbuffer_load_dwordx4 v154, s[48:51], s20 offen lds\n\tbuffer_load_dwordx4 v154, s[48:51], s21 offen offset:1024 lds\n\tbuffer_load_dwordx4 v154, s[48:51], s22 offen offset:2048 lds\n\tbuffer_load_dwordx4 v154, s[48:51], s23 offen offset:3072 lds\n\ts_add_u32 m0, s15, 4096\n\t"
            "s_nop 0\n\tbuffer_load_dwordx4 v154, s[48:51], s24 offen lds\n\tbuffer_load_dwordx4 v154, s[48:51], s25 offen offset:1024 lds\n\tbuffer_load_dwordx4 v154, s[48:51], s26 offen offset:2048 lds\n\tbuffer_load_dwordx4 v154, s[48:51], s27 offen offset:3072 lds\n\ts_add_u32 m0, s15, 8192\n\t"
            "s_nop 0\n\tbuffer_load_dwordx4 v154, s[48:51], s28 offen lds\n\tbuffer_load_dwordx4 v154, s[48:51], s29 offen offset:1024 lds\n\ts_cmp_lg_u32 s56, 0\n\ts_cbranch_scc1 1f\n\ts_mov_b32 m0, 162624\n\t"
            "s_nop 0\n\tbuffer_load_dwordx4 v104, s[44:47], 0 offen sc1 lds\n\t1:\ns_cmp_eq_u32 s56, 3\n\ts_cbranch_scc1 1f\n\tbuffer_load_dwordx4 v[244:247], v11, s[36:39], 0 offen offset:0\n\t"
            "buffer_load_dwordx4 v[252:255], v11, s[36:39], 0 offen offset:64\n\t1:\nbuffer_load_dwordx4 v[240:243], v3, s[36:39], 0 offen offset:0\n\tbuffer_load_dwordx4 v[248:251], v3, s[36:39], 0 offen offset:64\n\tds_read_b32 v20, v9 offset:256\n\tds_read_b32 v21, v10 offset:832\n\t"
            "ds_read2st64_b32 v[138:139], v153 offset1:1\n\tds_read2st64_b32 v[176:177], v160 offset1:1\n\tds_read2st64_b32 v[178:179], v161 offset1:1\n\tds_read2st64_b32 v[180:181], v162 offset1:1\n\tds_read2st64_b32 v[182:183], v163 offset1:1\n\tds_read2st64_b32 v[184:185], v164 offset1:1\n\t"
            "ds_read2st64_b32 v[186:187], v165 offset1:1\n\tds_read2st64_b32 v[188:189], v166 offset1:1\n\tds_read2st64_b32 v[190:191], v167 offset1:1\n\ts_mov_b32 s68, 0x200000\n\ts_waitcnt lgkmcnt(8)\n\tv_mul_f32 v138, 0x3ea52b5c, v138\n\t"
            "v_mul_f32 v139, 0x3ea52b5c, v139\n\ts_waitcnt lgkmcnt(7)\n\tv_mul_f32 v192, v176, v138\n\tv_mul_f32 v200, v177, v139\n\ts_waitcnt lgkmcnt(6)\n\tv_mul_f32 v193, v178, v138\n\t"
            "v_mul_f32 v201, v179, v139\n\ts_waitcnt lgkmcnt(5)\n\tv_mul_f32 v194, v180, v138\n\tv_mul_f32 v202, v181, v139\n\ts_waitcnt lgkmcnt(4)\n\tv_mul_f32 v195, v182, v138\n\t"
            "v_mul_f32 v203, v183, v139\n\ts_waitcnt lgkmcnt(3)\n\tv_mul_f32 v196, v184, v138\n\tv_mul_f32 v204, v185, v139\n\ts_waitcnt lgkmcnt(2)\n\tv_mul_f32 v197, v186, v138\n\t"
            "v_mul_f32 v205, v187, v139\n\ts_waitcnt lgkmcnt(1)\n\tv_mul_f32 v198, v188, v138\n\tv_mul_f32 v206, v189, v139\n\ts_waitcnt lgkmcnt(0)\n\tv_mul_f32 v199, v190, v138\n\t"
            "v_mul_f32 v207, v191, v139\n\ts_barrier\n\t"
            "50:\nds_read2_b64 v[208:211], v24 offset0:0 offset1:16\n\tds_read2_b64 v[212:215], v24 offset0:32 offset1:48\n\tds_read2_b64 v[216:219], v26 offset0:0 offset1:16\n\tds_read2_b64 v[220:223], v26 offset0:32 offset1:48\n\ts_cmp_eq_u32 s16, 0\n\t"
            "s_cbranch_scc1 56f\n\t58:\ns_cbranch_scc0 1f\n\tbuffer_load_dword v232, v31, s[44:47], s31 offen offset:0 sc1\n\tbuffer_load_dword v233, v31, s[44:47], s31 offen offset:256 sc1\n\tbuffer_load_dword v234, v31, s[44:47], s31 offen offset:512 sc1\n\t"
            "buffer_load_dword v235, v31, s[44:47], s31 offen offset:768 sc1\n\t1:\ns_waitcnt lgkmcnt(1)\n\tv_add_f32 v208, v208, v216\n\tv_add_f32 v209, v209, v217\n\tv_add_f32 v210, v210, v218\n\t"
            "v_add_f32 v211, v211, v219\n\ts_waitcnt lgkmcnt(0)\n\tv_add_f32 v212, v212, v220\n\tv_add_f32 v213, v213, v221\n\tv_add_f32 v214, v214, v222\n\tv_add_f32 v215, v215, v223\n\t"
            "v_fma_f32 v208, v208, v192, v128\n\tv_fma_f32 v209, v209, v193, v129\n\tv_fma_f32 v210, v210, v194, v130\n\tv_fma_f32 v211, v211, v195, v131\n\tv_fma_f32 v212, v212, v196, v132\n\tv_fma_f32 v213, v213, v197, v133\n\t"
            "v_fma_f32 v214, v214, v198, v134\n\tv_fma_f32 v215, v215, v199, v135\n\tv_max3_f32 v136, v208, v209, v210\n\tv_max3_f32 v137, v211, v212, v213\n\tv_max3_f32 v136, v136, v214, v215\n\tv_max_f32 v136, v136, v137\n\t"
            "v_sub_f32 v208, v208, v136\n\tv_sub_f32 v209, v209, v136\n\tv_sub_f32 v210, v210, v136\n\tv_sub_f32 v211, v211, v136\n\tv_sub_f32 v212, v212, v136\n\tv_sub_f32 v213, v213, v136\n\t"
            "v_sub_f32 v214, v214, v136\n\tv_sub_f32 v215, v215, v136\n\tv_exp_f32 v208, v208\n\tv_exp_f32 v209, v209\n\tv_exp_f32 v210, v210\n\tv_exp_f32 v211, v211\n\t"
            "v_exp_f32 v212, v212\n\tv_exp_f32 v213, v213\n\tv_exp_f32 v214, v214\n\tv_exp_f32 v215, v215\n\tv_add_f32 v216, v208, v209\n\tv_add_f32 v217, v210, v211\n\t"
            "v_add_f32 v218, v212, v213\n\tv_add_f32 v219, v214, v215\n\tv_add_f32 v216, v216, v217\n\tv_add_f32 v218, v218, v219\n\tv_mul_f32 v208, v208, v176\n\tv_add_f32 v216, v216, v218\n\t"
            "v_mul_f32 v216, 0xbfb8aa3b, v216\n\tv_rcp_f32 v216, v216\n\tv_mul_f32 v209, v209, v178\n\tv_mul_f32 v210, v210, v180\n\tv_mul_f32 v211, v211, v182\n\tv_mul_f32 v212, v212, v184\n\t"
            "v_mul_f32 v213, v213, v186\n\tv_mul_f32 v214, v214, v188\n\tv_mul_f32 v215, v215, v190\n\tv_mul_f32 v208, v208, v216\n\tv_mul_f32 v209, v209, v216\n\tv_mul_f32 v210, v210, v216\n\t"
            "v_mul_f32 v211, v211, v216\n\tv_mul_f32 v212, v212, v216\n\tv_mul_f32 v213, v213, v216\n\tv_mul_f32 v214, v214, v216\n\tv_mul_f32 v215, v215, v216\n\tv_cvt_pk_bf16_f32 v220, v208, v209\n\t"
            "v_cvt_pk_bf16_f32 v221, v210, v211\n\tv_cvt_pk_bf16_f32 v222, v212, v213\n\tv_cvt_pk_bf16_f32 v223, v214, v215\n\tds_write_b128 v29, v[220:223] offset:0\n\tds_write_b32 v30, v142 offset:16\n\t"
            "51:\nds_read_b32 v136, v28\n\tds_read2_b64 v[208:211], v25 offset0:0 offset1:16\n\tds_read2_b64 v[212:215], v25 offset0:32 offset1:48\n\tds_read2_b64 v[216:219], v27 offset0:0 offset1:16\n\tds_read2_b64 v[220:223], v27 offset0:32 offset1:48\n\t"
            "s_waitcnt lgkmcnt(4)\n\tv_cmp_eq_u32 vcc, 0, v136\n\ts_cbranch_vccnz 53f\n\t55:\n"
            "s_setprio 3\n\t"
            "s_cmp_eq_u32 s16, 0\n\ts_cbranch_scc1 57f\n\t59:\ns_waitcnt lgkmcnt(1)\n\tv_add_f32 v208, v208, v216\n\tv_add_f32 v209, v209, v217\n\t"
            "v_add_f32 v210, v210, v218\n\tv_add_f32 v211, v211, v219\n\ts_waitcnt lgkmcnt(0)\n\tv_add_f32 v212, v212, v220\n\tv_add_f32 v213, v213, v221\n\tv_add_f32 v214, v214, v222\n\t"
            "v_add_f32 v215, v215, v223\n\tv_mul_f32 v208, v208, v200\n\tv_mul_f32 v209, v209, v201\n\tv_mul_f32 v210, v210, v202\n\tv_mul_f32 v211, v211, v203\n\tv_mul_f32 v212, v212, v204\n\t"
            "v_mul_f32 v213, v213, v205\n\tv_mul_f32 v214, v214, v206\n\tv_mul_f32 v215, v215, v207\n\tv_max3_f32 v136, v208, v209, v210\n\tv_max3_f32 v137, v211, v212, v213\n\tv_max3_f32 v136, v136, v214, v215\n\t"
            "v_max_f32 v136, v136, v137\n\tv_sub_f32 v208, v208, v136\n\tv_sub_f32 v209, v209, v136\n\tv_sub_f32 v210, v210, v136\n\tv_sub_f32 v211, v211, v136\n\tv_sub_f32 v212, v212, v136\n\t"
            "v_sub_f32 v213, v213, v136\n\tv_sub_f32 v214, v214, v136\n\tv_sub_f32 v215, v215, v136\n\tv_exp_f32 v208, v208\n\tv_exp_f32 v209, v209\n\tv_exp_f32 v210, v210\n\t"
            "v_exp_f32 v211, v211\n\tv_exp_f32 v212, v212\n\tv_exp_f32 v213, v213\n\tv_exp_f32 v214, v214\n\tv_exp_f32 v215, v215\n\tv_add_f32 v216, v208, v209\n\t"
            "v_add_f32 v217, v210, v211\n\tv_add_f32 v218, v212, v213\n\tv_add_f32 v219, v214, v215\n\tv_add_f32 v216, v216, v217\n\tv_add_f32 v218, v218, v219\n\tv_mul_f32 v208, v208, v177\n\t"
            "v_add_f32 v216, v216, v218\n\tv_mul_f32 v216, 0xbfb8aa3b, v216\n\tv_rcp_f32 v216, v216\n\tv_mul_f32 v209, v209, v179\n\tv_mul_f32 v210, v210, v181\n\tv_mul_f32 v211, v211, v183\n\t"
            "v_mul_f32 v212, v212, v185\n\tv_mul_f32 v213, v213, v187\n\tv_mul_f32 v214, v214, v189\n\tv_mul_f32 v215, v215, v191\n\tv_mul_f32 v208, v208, v216\n\tv_mul_f32 v209, v209, v216\n\t"
            "v_mul_f32 v210, v210, v216\n\tv_mul_f32 v211, v211, v216\n\tv_mul_f32 v212, v212, v216\n\tv_mul_f32 v213, v213, v216\n\tv_mul_f32 v214, v214, v216\n\tv_mul_f32 v215, v215, v216\n\t"
            "v_cvt_pk_bf16_f32 v220, v208, v209\n\tv_cvt_pk_bf16_f32 v221, v210, v211\n\tv_cvt_pk_bf16_f32 v222, v212, v213\n\tv_cvt_pk_bf16_f32 v223, v214, v215\n\tds_write_b128 v29, v[220:223] offset:38912\n\tds_write_b32 v30, v30 offset:16\n\t"
            "s_setprio 0\n\t"
            "s_waitcnt vmcnt(0)\n\tds_write_b32 v30, v30 offset:48\n\ts_branch .LOL\n\t32:\nbuffer_load_dword v118, v96, s[44:47], 0 offen sc1\n\t"
            "s_waitcnt vmcnt(0)\n\tv_cmp_ne_u32 vcc, s67, v118\n\ts_cbranch_vccz 33b\n\ts_sub_u32 s68, s68, 1\n\ts_cbranch_scc0 32b\n\ts_branch 33b\n\t"
            "30:\nbuffer_load_dwordx4 v[106:109], v158, s[44:47], 0 offen sc1\n\tbuffer_load_dwordx4 v[110:113], v159, s[44:47], 0 offen sc1\n\tbuffer_load_dwordx4 v[114:117], v104, s[44:47], 0 offen sc1\n\t"
            "s_waitcnt vmcnt(2)\n\tv_xor_b32 v106, s67, v107\n\tv_xor_b32 v108, s67, v109\n\ts_waitcnt vmcnt(1)\n\tv_xor_b32 v110, s67, v111\n\tv_xor_b32 v112, s67, v113\n\t"
            "s_waitcnt vmcnt(0)\n\tv_xor_b32 v114, s67, v115\n\tv_xor_b32 v116, s67, v117\n\tv_or3_b32 v106, v106, v108, v110\n\tv_or3_b32 v112, v112, v114, v116\n\tv_or_b32 v106, v106, v112\n\t"
            "v_cmp_ne_u32 vcc, 0, v106\n\ts_cbranch_vccz 31b\n\ts_sub_u32 s68, s68, 1\n\ts_cbranch_scc0 30b\n\ts_branch 31b\n\t53:\n"
            "s_sleep 1\n\t"
            "s_sub_u32 s68, s68, 1\n\ts_cbranch_scc0 51b\n\ts_branch 55b\n\t56:\n"
            "s_sleep 6\n\t"
            "buffer_load_dword v224, v31, s[44:47], 0 offen offset:0 sc1\n\tbuffer_load_dword v225, v31, s[44:47], 0 offen offset:256 sc1\n\tbuffer_load_dword v226, v31, s[44:47], 0 offen offset:512 sc1\n\tbuffer_load_dword v227, v31, s[44:47], 0 offen offset:768 sc1\n\ts_waitcnt vmcnt(4)\n\t"
            "s_setprio 0\n\t"
            "s_call_b64 s[76:77], .LG0\n\t"
            "s_setprio 3\n\t"
            "s_branch 2f\n\t1:\ns_waitcnt vmcnt(3)\n\tbuffer_load_dword v224, v31, s[44:47], 0 offen offset:0 sc1\n\ts_waitcnt vmcnt(3)\n\tbuffer_load_dword v225, v31, s[44:47], 0 offen offset:256 sc1\n\t"
            "s_waitcnt vmcnt(3)\n\tbuffer_load_dword v226, v31, s[44:47], 0 offen offset:512 sc1\n\ts_waitcnt vmcnt(3)\n\tbuffer_load_dword v227, v31, s[44:47], 0 offen offset:768 sc1\n\ts_cmp_lg_u32 s83, 0\n\ts_cbranch_scc1 3f\n\t"
            "s_call_b64 s[76:77], .LG1\n\t3:\ns_cmp_ge_u32 s83, 2\n\ts_cbranch_scc1 2f\n\ts_call_b64 s[76:77], .LG2\n\t2:\n"
            "s_waitcnt vmcnt(0) lgkmcnt(0)\n\t4:\nds_write_b32 v30, v30 offset:48\n\tv_max3_u32 v136, v224, v225, v226\n\tv_max_u32 v136, v136, v227\n\tv_cmp_eq_u32 vcc, -1, v136\n\t"
            "v_lshlrev_b32 v216, 16, v224\n\tv_and_b32 v217, 0xffff0000, v224\n\tv_lshlrev_b32 v218, 16, v225\n\tv_and_b32 v219, 0xffff0000, v225\n\tv_lshlrev_b32 v220, 16, v226\n\tv_and_b32 v221, 0xffff0000, v226\n\t"
            "v_lshlrev_b32 v222, 16, v227\n\tv_and_b32 v223, 0xffff0000, v227\n\t"
            "s_cmp_eq_u32 0, 0\n\ts_cbranch_vccz 58b\n\ts_sub_u32 s68, s68, 1\n\ts_cbranch_scc0 1b\n\ts_branch 58b\n\t57:\n"
            "s_branch 2f\n\t1:\nbuffer_load_dword v232, v31, s[44:47], s31 offen offset:0 sc1\n\tbuffer_load_dword v233, v31, s[44:47], s31 offen offset:256 sc1\n\tbuffer_load_dword v234, v31, s[44:47], s31 offen offset:512 sc1\n\tbuffer_load_dword v235, v31, s[44:47], s31 offen offset:768 sc1\n\t"
            "s_cmp_lg_u32 s83, 0\n\ts_cbranch_scc1 3f\n\ts_call_b64 s[76:77], .LG1\n\t3:\ns_cmp_ge_u32 s83, 2\n\ts_cbranch_scc1 2f\n\t"
            "s_call_b64 s[76:77], .LG2\n\t2:\ns_waitcnt vmcnt(0) lgkmcnt(0)\n\t4:\nv_max3_u32 v136, v232, v233, v234\n\tv_max_u32 v136, v136, v235\n\t"
            "v_cmp_eq_u32 vcc, -1, v136\n\tv_lshlrev_b32 v216, 16, v232\n\tv_and_b32 v217, 0xffff0000, v232\n\tv_lshlrev_b32 v218, 16, v233\n\tv_and_b32 v219, 0xffff0000, v233\n\tv_lshlrev_b32 v220, 16, v234\n\t"
            "v_and_b32 v221, 0xffff0000, v234\n\tv_lshlrev_b32 v222, 16, v235\n\tv_and_b32 v223, 0xffff0000, v235\n\t"
            "s_cmp_eq_u32 0, 0\n\ts_cbranch_vccz 59b\n\ts_sub_u32 s68, s68, 1\n\ts_cbranch_scc0 1b\n\ts_branch 59b\n\t"
            ".LOL:\ns_lshr_b32 s71, s56, 1\n\ts_mov_b32 s68, 0x200000\n\ts_cmp_eq_u32 s16, 0\n\ts_cbranch_scc1 3f\n\ts_call_b64 s[76:77], .LG0\n\t"
            "3:\nds_read_b64 v[14:15], v8 offset:32\n\ts_waitcnt lgkmcnt(0)\n\tv_min_u32 v12, v14, v15\n\tv_cmp_lt_u32 vcc, s71, v12\n\ts_cbranch_vccnz 4f\n\t"
            "s_sleep 1\n\ts_sub_u32 s68, s68, 1\n\ts_cbranch_scc0 3b\n\t4:\ns_cmp_eq_u32 s56, 3\n\ts_cbranch_scc1 .L3\n\t"
            "s_cmp_eq_u32 s16, 0\n\ts_cbranch_scc1 1f\n\ts_call_b64 s[76:77], .LG1\n\t1:\ns_cmp_lt_u32 s56, 2\n\ts_cbranch_scc1 1f\n\t"
            "s_sleep 3\n\t1:\n"
            "buffer_load_dwordx4 v[176:179], v2, s[44:47], s4 offen offset:1024 sc1\n\tbuffer_load_dwordx4 v[180:183], v2, s[44:47], s4 offen offset:2048 sc1\n\tbuffer_load_dwordx4 v[184:187], v2, s[44:47], s5 offen offset:1024 sc1\n\tbuffer_load_dwordx4 v[188:191], v2, s[44:47], s5 offen offset:2048 sc1\n\tbuffer_load_dwordx4 v[192:195], v2, s[44:47], s6 offen offset:1024 sc1\n\tbuffer_load_dwordx4 v[196:199], v2, s[44:47], s6 offen offset:2048 sc1\n\t"
            "buffer_load_dwordx4 v[200:203], v2, s[44:47], s7 offen offset:1024 sc1\n\tbuffer_load_dwordx4 v[204:207], v2, s[44:47], s7 offen offset:2048 sc1\n\tbuffer_load_dwordx4 v[208:211], v2, s[44:47], s8 offen offset:1024 sc1\n\tbuffer_load_dwordx4 v[212:215], v2, s[44:47], s8 offen offset:2048 sc1\n\ts_cmp_lg_u32 s83, 0\n\ts_cbranch_scc1 1f\n\t"
            "s_call_b64 s[76:77], .LG1\n\t1:\ns_cmp_ge_u32 s83, 2\n\ts_cbranch_scc1 1f\n\ts_call_b64 s[76:77], .LG2\n\t1:\n"
            "ds_read_b128 v[16:19], v8 offset:48\n\tds_read_b64_tr_b16 v[128:129], v4 offset:40960\n\tds_read_b64_tr_b16 v[130:131], v4 offset:41472\n\tds_read_b64_tr_b16 v[132:133], v5 offset:40960\n\tds_read_b64_tr_b16 v[134:135], v5 offset:41472\n\tds_read_b64_tr_b16 v[136:137], v6 offset:40960\n\t"
            "ds_read_b64_tr_b16 v[138:139], v6 offset:41472\n\tds_read_b64_tr_b16 v[140:141], v7 offset:40960\n\tds_read_b64_tr_b16 v[142:143], v7 offset:41472\n\tds_read_b64_tr_b16 v[144:145], v4 offset:41984\n\tds_read_b64_tr_b16 v[146:147], v4 offset:42496\n\tds_read_b64_tr_b16 v[148:149], v5 offset:41984\n\t"
            "ds_read_b64_tr_b16 v[150:151], v5 offset:42496\n\tds_read_b64_tr_b16 v[152:153], v6 offset:41984\n\tds_read_b64_tr_b16 v[154:155], v6 offset:42496\n\tds_read_b64_tr_b16 v[156:157], v7 offset:41984\n\tds_read_b64_tr_b16 v[158:159], v7 offset:42496\n\tv_mul_f32 v48, v48, v112\n\t"
            "v_mul_f32 v49, v49, v113\n\tv_mul_f32 v50, v50, v114\n\tv_mul_f32 v51, v51, v115\n\tv_mul_f32 v52, v52, v116\n\tv_mul_f32 v53, v53, v117\n\tv_mul_f32 v54, v54, v118\n\t"
            "v_mul_f32 v55, v55, v119\n\tv_mul_f32 v56, v56, v120\n\tv_mul_f32 v57, v57, v121\n\tv_mul_f32 v58, v58, v122\n\tv_mul_f32 v59, v59, v123\n\tv_mul_f32 v60, v60, v124\n\t"
            "v_mul_f32 v61, v61, v125\n\tv_mul_f32 v62, v62, v126\n\tv_mul_f32 v63, v63, v127\n\t"
            "s_waitcnt lgkmcnt(15)\n\tv_min3_u32 v16, v16, v17, v18\n\tv_min_u32 v16, v16, v19\n\tv_cmp_eq_u32 vcc, 0, v16\n\ts_cbranch_vccnz 62f\n\t63:\n"
            "s_waitcnt vmcnt(8)\n\tv_max3_u32 v12, v176, v178, v180\n\tv_max_u32 v12, v12, v182\n\tv_cmp_eq_u32 vcc, -1, v12\n\ts_cbranch_vccnz 100f\n\t200:\n"
            "s_waitcnt lgkmcnt(8)\n\tv_mfma_f32_32x32x16_bf16 v[96:111], v[128:131], v[176:179], 0\n\tds_read_b64_tr_b16 v[160:161], v4 offset:43008\n\tds_read_b64_tr_b16 v[162:163], v4 offset:43520\n\tbuffer_load_dwordx4 v[216:219], v2, s[44:47], s9 offen offset:1024 sc1\n\tv_mfma_f32_32x32x16_bf16 v[112:127], v[132:135], v[176:179], 0\n\t"
            "ds_read_b64_tr_b16 v[164:165], v5 offset:43008\n\tds_read_b64_tr_b16 v[166:167], v5 offset:43520\n\tbuffer_load_dwordx4 v[220:223], v2, s[44:47], s9 offen offset:2048 sc1\n\tv_mfma_f32_32x32x16_bf16 v[96:111], v[136:139], v[180:183], v[96:111]\n\tds_read_b64_tr_b16 v[168:169], v6 offset:43008\n\tds_read_b64_tr_b16 v[170:171], v6 offset:43520\n\t"
            "s_waitcnt vmcnt(8)\n\tv_max3_u32 v12, v184, v186, v188\n\tv_max_u32 v12, v12, v190\n\tv_cmp_eq_u32 vcc, -1, v12\n\tv_mfma_f32_32x32x16_bf16 v[112:127], v[140:143], v[180:183], v[112:127]\n\tds_read_b64_tr_b16 v[172:173], v7 offset:43008\n\t"
            "ds_read_b64_tr_b16 v[174:175], v7 offset:43520\n\ts_cbranch_vccnz 101f\n\t201:\ns_waitcnt lgkmcnt(8)\n\tv_mfma_f32_32x32x16_bf16 v[96:111], v[144:147], v[184:187], v[96:111]\n\tds_read_b64_tr_b16 v[128:129], v4 offset:44032\n\t"
            "ds_read_b64_tr_b16 v[130:131], v4 offset:44544\n\tbuffer_load_dwordx4 v[224:227], v2, s[44:47], s10 offen offset:1024 sc1\n\tv_mfma_f32_32x32x16_bf16 v[112:127], v[148:151], v[184:187], v[112:127]\n\tds_read_b64_tr_b16 v[132:133], v5 offset:44032\n\tds_read_b64_tr_b16 v[134:135], v5 offset:44544\n\tbuffer_load_dwordx4 v[228:231], v2, s[44:47], s10 offen offset:2048 sc1\n\t"
            "v_mfma_f32_32x32x16_bf16 v[96:111], v[152:155], v[188:191], v[96:111]\n\tds_read_b64_tr_b16 v[136:137], v6 offset:44032\n\tds_read_b64_tr_b16 v[138:139], v6 offset:44544\n\ts_waitcnt vmcnt(8)\n\tv_max3_u32 v12, v192, v194, v196\n\tv_max_u32 v12, v12, v198\n\t"
            "v_cmp_eq_u32 vcc, -1, v12\n\tv_mfma_f32_32x32x16_bf16 v[112:127], v[156:159], v[188:191], v[112:127]\n\tds_read_b64_tr_b16 v[140:141], v7 offset:44032\n\tds_read_b64_tr_b16 v[142:143], v7 offset:44544\n\ts_cbranch_vccnz 102f\n\t202:\n"
            "s_waitcnt lgkmcnt(8)\n\tv_mfma_f32_32x32x16_bf16 v[96:111], v[160:163], v[192:195], v[96:111]\n\tds_read_b64_tr_b16 v[144:145], v4 offset:45056\n\tds_read_b64_tr_b16 v[146:147], v4 offset:45568\n\tbuffer_load_dwordx4 v[232:235], v2, s[44:47], s11 offen offset:1024 sc1\n\tv_mfma_f32_32x32x16_bf16 v[112:127], v[164:167], v[192:195], v[112:127]\n\t"
            "ds_read_b64_tr_b16 v[148:149], v5 offset:45056\n\tds_read_b64_tr_b16 v[150:151], v5 offset:45568\n\tbuffer_load_dwordx4 v[236:239], v2, s[44:47], s11 offen offset:2048 sc1\n\tv_mfma_f32_32x32x16_bf16 v[96:111], v[168:171], v[196:199], v[96:111]\n\tds_read_b64_tr_b16 v[152:153], v6 offset:45056\n\tds_read_b64_tr_b16 v[154:155], v6 offset:45568\n\t"
            "s_waitcnt vmcnt(8)\n\tv_max3_u32 v12, v200, v202, v204\n\tv_max_u32 v12, v12, v206\n\tv_cmp_eq_u32 vcc, -1, v12\n\tv_mfma_f32_32x32x16_bf16 v[112:127], v[172:175], v[196:199], v[112:127]\n\tds_read_b64_tr_b16 v[156:157], v7 offset:45056\n\t"
            "ds_read_b64_tr_b16 v[158:159], v7 offset:45568\n\ts_cbranch_vccnz 103f\n\t203:\ns_waitcnt lgkmcnt(8)\n\tv_mfma_f32_32x32x16_bf16 v[96:111], v[128:131], v[200:203], v[96:111]\n\tds_read_b64_tr_b16 v[160:161], v4 offset:46080\n\t"
            "ds_read_b64_tr_b16 v[162:163], v4 offset:46592\n\tbuffer_load_dwordx4 v[176:179], v2, s[44:47], s12 offen offset:1024 sc1\n\tv_mfma_f32_32x32x16_bf16 v[112:127], v[132:135], v[200:203], v[112:127]\n\tds_read_b64_tr_b16 v[164:165], v5 offset:46080\n\tds_read_b64_tr_b16 v[166:167], v5 offset:46592\n\tds_read_b64 v[14:15], v8 offset:40\n\t"
            "buffer_load_dwordx4 v[180:183], v2, s[44:47], s12 offen offset:2048 sc1\n\tv_mfma_f32_32x32x16_bf16 v[96:111], v[136:139], v[204:207], v[96:111]\n\tds_read_b64_tr_b16 v[168:169], v6 offset:46080\n\tds_read_b64_tr_b16 v[170:171], v6 offset:46592\n\ts_waitcnt vmcnt(8)\n\tv_max3_u32 v12, v208, v210, v212\n\t"
            "v_max_u32 v12, v12, v214\n\tv_cmp_eq_u32 vcc, -1, v12\n\tv_mfma_f32_32x32x16_bf16 v[112:127], v[140:143], v[204:207], v[112:127]\n\tds_read_b64_tr_b16 v[172:173], v7 offset:46080\n\tds_read_b64_tr_b16 v[174:175], v7 offset:46592\n\ts_cbranch_vccnz 104f\n\t"
            "204:\ns_waitcnt lgkmcnt(9)\n\tv_mfma_f32_32x32x16_bf16 v[96:111], v[144:147], v[208:211], v[96:111]\n\tds_read_b64_tr_b16 v[128:129], v4 offset:47104\n\tds_read_b64_tr_b16 v[130:131], v4 offset:47616\n\tbuffer_load_dwordx4 v[184:187], v2, s[44:47], s13 offen offset:1024 sc1\n\t"
            "v_mfma_f32_32x32x16_bf16 v[112:127], v[148:151], v[208:211], v[112:127]\n\tds_read_b64_tr_b16 v[132:133], v5 offset:47104\n\tds_read_b64_tr_b16 v[134:135], v5 offset:47616\n\tbuffer_load_dwordx4 v[188:191], v2, s[44:47], s13 offen offset:2048 sc1\n\tv_mfma_f32_32x32x16_bf16 v[96:111], v[152:155], v[212:215], v[96:111]\n\tds_read_b64_tr_b16 v[136:137], v6 offset:47104\n\t"
            "ds_read_b64_tr_b16 v[138:139], v6 offset:47616\n\ts_waitcnt vmcnt(8)\n\tv_max3_u32 v12, v216, v218, v220\n\tv_max_u32 v12, v12, v222\n\tv_cmp_eq_u32 vcc, -1, v12\n\tv_mfma_f32_32x32x16_bf16 v[112:127], v[156:159], v[212:215], v[112:127]\n\t"
            "ds_read_b64_tr_b16 v[140:141], v7 offset:47104\n\tds_read_b64_tr_b16 v[142:143], v7 offset:47616\n\ts_cbranch_vccnz 105f\n\t205:\ns_waitcnt lgkmcnt(8)\n\tv_mfma_f32_32x32x16_bf16 v[96:111], v[160:163], v[216:219], v[96:111]\n\t"
            "v_min_u32 v14, v14, v15\n\tv_cmp_ge_u32 vcc, s71, v14\n\tds_read_b64_tr_b16 v[144:145], v4 offset:48128\n\tds_read_b64_tr_b16 v[146:147], v4 offset:48640\n\tv_mfma_f32_32x32x16_bf16 v[112:127], v[164:167], v[216:219], v[112:127]\n\ts_cbranch_vccnz 5f\n\t"
            "6:\nds_read_b64_tr_b16 v[148:149], v5 offset:48128\n\tds_read_b64_tr_b16 v[150:151], v5 offset:48640\n\tbuffer_load_dwordx4 v[192:195], v2, s[44:47], s4 offen offset:0 sc1\n\tbuffer_load_dwordx4 v[196:199], v2, s[44:47], s4 offen offset:3072 sc1\n\tv_mfma_f32_32x32x16_bf16 v[96:111], v[168:171], v[220:223], v[96:111]\n\t"
            "ds_read_b64_tr_b16 v[152:153], v6 offset:48128\n\tds_read_b64_tr_b16 v[154:155], v6 offset:48640\n\ts_waitcnt vmcnt(8)\n\tv_max3_u32 v12, v224, v226, v228\n\tv_max_u32 v12, v12, v230\n\tv_cmp_eq_u32 vcc, -1, v12\n\t"
            "v_mfma_f32_32x32x16_bf16 v[112:127], v[172:175], v[220:223], v[112:127]\n\tds_read_b64_tr_b16 v[156:157], v7 offset:48128\n\tds_read_b64_tr_b16 v[158:159], v7 offset:48640\n\ts_cbranch_vccnz 106f\n\t206:\ns_waitcnt lgkmcnt(8)\n\t"
            "v_mfma_f32_32x32x16_bf16 v[96:111], v[128:131], v[224:227], v[96:111]\n\tds_read_b64_tr_b16 v[160:161], v4 offset:49152\n\tds_read_b64_tr_b16 v[162:163], v4 offset:49664\n\tbuffer_load_dwordx4 v[200:203], v2, s[44:47], s5 offen offset:0 sc1\n\tv_mfma_f32_32x32x16_bf16 v[112:127], v[132:135], v[224:227], v[112:127]\n\tds_read_b64_tr_b16 v[164:165], v5 offset:49152\n\t"
            "ds_read_b64_tr_b16 v[166:167], v5 offset:49664\n\tbuffer_load_dwordx4 v[204:207], v2, s[44:47], s5 offen offset:3072 sc1\n\tv_mfma_f32_32x32x16_bf16 v[96:111], v[136:139], v[228:231], v[96:111]\n\tds_read_b64_tr_b16 v[168:169], v6 offset:49152\n\tds_read_b64_tr_b16 v[170:171], v6 offset:49664\n\ts_waitcnt vmcnt(8)\n\t"
            "v_max3_u32 v12, v232, v234, v236\n\tv_max_u32 v12, v12, v238\n\tv_cmp_eq_u32 vcc, -1, v12\n\tv_mfma_f32_32x32x16_bf16 v[112:127], v[140:143], v[228:231], v[112:127]\n\tds_read_b64_tr_b16 v[172:173], v7 offset:49152\n\tds_read_b64_tr_b16 v[174:175], v7 offset:49664\n\t"
            "s_cbranch_vccnz 107f\n\t207:\ns_waitcnt lgkmcnt(8)\n\tv_mfma_f32_32x32x16_bf16 v[96:111], v[144:147], v[232:235], v[96:111]\n\tds_read_b64_tr_b16 v[128:129], v4 offset:50176\n\tds_read_b64_tr_b16 v[130:131], v4 offset:50688\n\t"
            "buffer_load_dwordx4 v[208:211], v2, s[44:47], s6 offen offset:0 sc1\n\tv_mfma_f32_32x32x16_bf16 v[112:127], v[148:151], v[232:235], v[112:127]\n\tds_read_b64_tr_b16 v[132:133], v5 offset:50176\n\tds_read_b64_tr_b16 v[134:135], v5 offset:50688\n\tbuffer_load_dwordx4 v[212:215], v2, s[44:47], s6 offen offset:3072 sc1\n\tv_mfma_f32_32x32x16_bf16 v[96:111], v[152:155], v[236:239], v[96:111]\n\t"
            "ds_read_b64_tr_b16 v[136:137], v6 offset:50176\n\tds_read_b64_tr_b16 v[138:139], v6 offset:50688\n\ts_waitcnt vmcnt(8)\n\tv_max3_u32 v12, v176, v178, v180\n\tv_max_u32 v12, v12, v182\n\tv_cmp_eq_u32 vcc, -1, v12\n\t"
            "v_mfma_f32_32x32x16_bf16 v[112:127], v[156:159], v[236:239], v[112:127]\n\tds_read_b64_tr_b16 v[140:141], v7 offset:50176\n\tds_read_b64_tr_b16 v[142:143], v7 offset:50688\n\ts_cbranch_vccnz 108f\n\t208:\ns_waitcnt lgkmcnt(8)\n\t"
            "v_mfma_f32_32x32x16_bf16 v[96:111], v[160:163], v[176:179], v[96:111]\n\tds_read_b64_tr_b16 v[144:145], v4 offset:0\n\tds_read_b64_tr_b16 v[146:147], v4 offset:512\n\tbuffer_load_dwordx4 v[216:219], v2, s[44:47], s7 offen offset:0 sc1\n\tv_mfma_f32_32x32x16_bf16 v[112:127], v[164:167], v[176:179], v[112:127]\n\tds_read_b64_tr_b16 v[148:149], v5 offset:0\n\t"
            "ds_read_b64_tr_b16 v[150:151], v5 offset:512\n\tbuffer_load_dwordx4 v[220:223], v2, s[44:47], s7 offen offset:3072 sc1\n\tv_mfma_f32_32x32x16_bf16 v[96:111], v[168:171], v[180:183], v[96:111]\n\tds_read_b64_tr_b16 v[152:153], v6 offset:0\n\tds_read_b64_tr_b16 v[154:155], v6 offset:512\n\ts_waitcnt vmcnt(8)\n\t"
            "v_max3_u32 v12, v184, v186, v188\n\tv_max_u32 v12, v12, v190\n\tv_cmp_eq_u32 vcc, -1, v12\n\tv_mfma_f32_32x32x16_bf16 v[112:127], v[172:175], v[180:183], v[112:127]\n\tds_read_b64_tr_b16 v[156:157], v7 offset:0\n\tds_read_b64_tr_b16 v[158:159], v7 offset:512\n\t"
            "s_cbranch_vccnz 109f\n\t209:\ns_waitcnt lgkmcnt(8)\n\tv_mfma_f32_32x32x16_bf16 v[96:111], v[128:131], v[184:187], v[96:111]\n\tds_read_b64_tr_b16 v[160:161], v4 offset:1024\n\tds_read_b64_tr_b16 v[162:163], v4 offset:1536\n\t"
            "buffer_load_dwordx4 v[224:227], v2, s[44:47], s8 offen offset:0 sc1\n\tv_mfma_f32_32x32x16_bf16 v[112:127], v[132:135], v[184:187], v[112:127]\n\tds_read_b64_tr_b16 v[164:165], v5 offset:1024\n\tds_read_b64_tr_b16 v[166:167], v5 offset:1536\n\tbuffer_load_dwordx4 v[228:231], v2, s[44:47], s8 offen offset:3072 sc1\n\tv_mfma_f32_32x32x16_bf16 v[96:111], v[136:139], v[188:191], v[96:111]\n\t"
            "ds_read_b64_tr_b16 v[168:169], v6 offset:1024\n\tds_read_b64_tr_b16 v[170:171], v6 offset:1536\n\ts_waitcnt vmcnt(8)\n\tv_max3_u32 v12, v192, v194, v196\n\tv_max_u32 v12, v12, v198\n\tv_cmp_eq_u32 vcc, -1, v12\n\t"
            "v_mfma_f32_32x32x16_bf16 v[112:127], v[140:143], v[188:191], v[112:127]\n\tds_read_b64_tr_b16 v[172:173], v7 offset:1024\n\tds_read_b64_tr_b16 v[174:175], v7 offset:1536\n\ts_cbranch_vccnz 110f\n\t210:\ns_waitcnt lgkmcnt(8)\n\t"
            "v_mfma_f32_32x32x16_bf16 v[96:111], v[144:147], v[192:195], v[96:111]\n\tds_read_b64_tr_b16 v[128:129], v4 offset:2048\n\tds_read_b64_tr_b16 v[130:131], v4 offset:2560\n\tbuffer_load_dwordx4 v[232:235], v2, s[44:47], s9 offen offset:0 sc1\n\tv_mfma_f32_32x32x16_bf16 v[112:127], v[148:151], v[192:195], v[112:127]\n\tds_read_b64_tr_b16 v[132:133], v5 offset:2048\n\t"
            "ds_read_b64_tr_b16 v[134:135], v5 offset:2560\n\tbuffer_load_dwordx4 v[236:239], v2, s[44:47], s9 offen offset:3072 sc1\n\tv_mfma_f32_32x32x16_bf16 v[96:111], v[152:155], v[196:199], v[96:111]\n\tds_read_b64_tr_b16 v[136:137], v6 offset:2048\n\tds_read_b64_tr_b16 v[138:139], v6 offset:2560\n\ts_waitcnt vmcnt(8)\n\t"
            "v_max3_u32 v12, v200, v202, v204\n\tv_max_u32 v12, v12, v206\n\tv_cmp_eq_u32 vcc, -1, v12\n\tv_mfma_f32_32x32x16_bf16 v[112:127], v[156:159], v[196:199], v[112:127]\n\tds_read_b64_tr_b16 v[140:141], v7 offset:2048\n\tds_read_b64_tr_b16 v[142:143], v7 offset:2560\n\t"
            "s_cbranch_vccnz 111f\n\t211:\ns_waitcnt lgkmcnt(8)\n\tv_mfma_f32_32x32x16_bf16 v[96:111], v[160:163], v[200:203], v[96:111]\n\tds_read_b64_tr_b16 v[144:145], v4 offset:3072\n\tds_read_b64_tr_b16 v[146:147], v4 offset:3584\n\t"
            "buffer_load_dwordx4 v[176:179], v2, s[44:47], s10 offen offset:0 sc1\n\tv_mfma_f32_32x32x16_bf16 v[112:127], v[164:167], v[200:203], v[112:127]\n\tds_read_b64_tr_b16 v[148:149], v5 offset:3072\n\tds_read_b64_tr_b16 v[150:151], v5 offset:3584\n\tbuffer_load_dwordx4 v[180:183], v2, s[44:47], s10 offen offset:3072 sc1\n\tv_mfma_f32_32x32x16_bf16 v[96:111], v[168:171], v[204:207], v[96:111]\n\t"
            "ds_read_b64_tr_b16 v[152:153], v6 offset:3072\n\tds_read_b64_tr_b16 v[154:155], v6 offset:3584\n\ts_waitcnt vmcnt(8)\n\tv_max3_u32 v12, v208, v210, v212\n\tv_max_u32 v12, v12, v214\n\tv_cmp_eq_u32 vcc, -1, v12\n\t"
            "v_mfma_f32_32x32x16_bf16 v[112:127], v[172:175], v[204:207], v[112:127]\n\tds_read_b64_tr_b16 v[156:157], v7 offset:3072\n\tds_read_b64_tr_b16 v[158:159], v7 offset:3584\n\ts_cbranch_vccnz 112f\n\t212:\ns_waitcnt lgkmcnt(8)\n\t"
            "v_mfma_f32_32x32x16_bf16 v[96:111], v[128:131], v[208:211], v[96:111]\n\tds_read_b64_tr_b16 v[160:161], v4 offset:4096\n\tds_read_b64_tr_b16 v[162:163], v4 offset:4608\n\tbuffer_load_dwordx4 v[184:187], v2, s[44:47], s11 offen offset:0 sc1\n\tv_mfma_f32_32x32x16_bf16 v[112:127], v[132:135], v[208:211], v[112:127]\n\tds_read_b64_tr_b16 v[164:165], v5 offset:4096\n\t"
            "ds_read_b64_tr_b16 v[166:167], v5 offset:4608\n\tbuffer_load_dwordx4 v[188:191], v2, s[44:47], s11 offen offset:3072 sc1\n\tv_mfma_f32_32x32x16_bf16 v[96:111], v[136:139], v[212:215], v[96:111]\n\tds_read_b64_tr_b16 v[168:169], v6 offset:4096\n\tds_read_b64_tr_b16 v[170:171], v6 offset:4608\n\ts_waitcnt vmcnt(8)\n\t"
            "v_max3_u32 v12, v216, v218, v220\n\tv_max_u32 v12, v12, v222\n\tv_cmp_eq_u32 vcc, -1, v12\n\tv_mfma_f32_32x32x16_bf16 v[112:127], v[140:143], v[212:215], v[112:127]\n\tds_read_b64_tr_b16 v[172:173], v7 offset:4096\n\tds_read_b64_tr_b16 v[174:175], v7 offset:4608\n\t"
            "s_cbranch_vccnz 113f\n\t213:\ns_waitcnt lgkmcnt(8)\n\tv_mfma_f32_32x32x16_bf16 v[96:111], v[144:147], v[216:219], v[96:111]\n\tds_read_b64_tr_b16 v[128:129], v4 offset:5120\n\tds_read_b64_tr_b16 v[130:131], v4 offset:5632\n\t"
            "buffer_load_dwordx4 v[192:195], v2, s[44:47], s12 offen offset:0 sc1\n\tv_mfma_f32_32x32x16_bf16 v[112:127], v[148:151], v[216:219], v[112:127]\n\tds_read_b64_tr_b16 v[132:133], v5 offset:5120\n\tds_read_b64_tr_b16 v[134:135], v5 offset:5632\n\tbuffer_load_dwordx4 v[196:199], v2, s[44:47], s12 offen offset:3072 sc1\n\tv_mfma_f32_32x32x16_bf16 v[96:111], v[152:155], v[220:223], v[96:111]\n\t"
            "ds_read_b64_tr_b16 v[136:137], v6 offset:5120\n\tds_read_b64_tr_b16 v[138:139], v6 offset:5632\n\ts_waitcnt vmcnt(8)\n\tv_max3_u32 v12, v224, v226, v228\n\tv_max_u32 v12, v12, v230\n\tv_cmp_eq_u32 vcc, -1, v12\n\t"
            "v_mfma_f32_32x32x16_bf16 v[112:127], v[156:159], v[220:223], v[112:127]\n\tds_read_b64_tr_b16 v[140:141], v7 offset:5120\n\tds_read_b64_tr_b16 v[142:143], v7 offset:5632\n\ts_cbranch_vccnz 114f\n\t214:\ns_waitcnt lgkmcnt(8)\n\t"
            "v_mfma_f32_32x32x16_bf16 v[96:111], v[160:163], v[224:227], v[96:111]\n\tds_read_b64_tr_b16 v[144:145], v4 offset:6144\n\tds_read_b64_tr_b16 v[146:147], v4 offset:6656\n\tbuffer_load_dwordx4 v[200:203], v2, s[44:47], s13 offen offset:0 sc1\n\tv_mfma_f32_32x32x16_bf16 v[112:127], v[164:167], v[224:227], v[112:127]\n\tds_read_b64_tr_b16 v[148:149], v5 offset:6144\n\t"
            "ds_read_b64_tr_b16 v[150:151], v5 offset:6656\n\tbuffer_load_dwordx4 v[204:207], v2, s[44:47], s13 offen offset:3072 sc1\n\tv_mfma_f32_32x32x16_bf16 v[96:111], v[168:171], v[228:231], v[96:111]\n\tds_read_b64_tr_b16 v[152:153], v6 offset:6144\n\tds_read_b64_tr_b16 v[154:155], v6 offset:6656\n\ts_waitcnt vmcnt(8)\n\t"
            "v_max3_u32 v12, v232, v234, v236\n\tv_max_u32 v12, v12, v238\n\tv_cmp_eq_u32 vcc, -1, v12\n\tv_mfma_f32_32x32x16_bf16 v[112:127], v[172:175], v[228:231], v[112:127]\n\tds_read_b64_tr_b16 v[156:157], v7 offset:6144\n\tds_read_b64_tr_b16 v[158:159], v7 offset:6656\n\t"
            "s_cbranch_vccnz 115f\n\t215:\ns_waitcnt lgkmcnt(8)\n\tv_mfma_f32_32x32x16_bf16 v[96:111], v[128:131], v[232:235], v[96:111]\n\tds_read_b64_tr_b16 v[160:161], v4 offset:7168\n\tds_read_b64_tr_b16 v[162:163], v4 offset:7680\n\t"
            "v_mfma_f32_32x32x16_bf16 v[112:127], v[132:135], v[232:235], v[112:127]\n\tds_read_b64_tr_b16 v[164:165], v5 offset:7168\n\tds_read_b64_tr_b16 v[166:167], v5 offset:7680\n\tv_mfma_f32_32x32x16_bf16 v[96:111], v[136:139], v[236:239], v[96:111]\n\tds_read_b64_tr_b16 v[168:169], v6 offset:7168\n\tds_read_b64_tr_b16 v[170:171], v6 offset:7680\n\t"
            "s_waitcnt vmcnt(6)\n\tv_max3_u32 v12, v176, v178, v180\n\tv_max_u32 v12, v12, v182\n\tv_cmp_eq_u32 vcc, -1, v12\n\tv_mfma_f32_32x32x16_bf16 v[112:127], v[140:143], v[236:239], v[112:127]\n\tds_read_b64_tr_b16 v[172:173], v7 offset:7168\n\t"
            "ds_read_b64_tr_b16 v[174:175], v7 offset:7680\n\ts_cbranch_vccnz 116f\n\t216:\ns_waitcnt lgkmcnt(8)\n\tv_mfma_f32_32x32x16_bf16 v[96:111], v[144:147], v[176:179], v[96:111]\n\tds_read_b64_tr_b16 v[128:129], v4 offset:8192\n\t"
            "ds_read_b64_tr_b16 v[130:131], v4 offset:8704\n\tv_mfma_f32_32x32x16_bf16 v[112:127], v[148:151], v[176:179], v[112:127]\n\tds_read_b64_tr_b16 v[132:133], v5 offset:8192\n\tds_read_b64_tr_b16 v[134:135], v5 offset:8704\n\tv_mfma_f32_32x32x16_bf16 v[96:111], v[152:155], v[180:183], v[96:111]\n\tds_read_b64_tr_b16 v[136:137], v6 offset:8192\n\t"
            "ds_read_b64_tr_b16 v[138:139], v6 offset:8704\n\ts_waitcnt vmcnt(4)\n\tv_max3_u32 v12, v184, v186, v188\n\tv_max_u32 v12, v12, v190\n\tv_cmp_eq_u32 vcc, -1, v12\n\tv_mfma_f32_32x32x16_bf16 v[112:127], v[156:159], v[180:183], v[112:127]\n\t"
            "ds_read_b64_tr_b16 v[140:141], v7 offset:8192\n\tds_read_b64_tr_b16 v[142:143], v7 offset:8704\n\ts_cbranch_vccnz 117f\n\t217:\nds_read_b32 v16, v8 offset:64\n\tds_read_b32 v17, v9 offset:1344\n\t"
            "s_waitcnt lgkmcnt(10)\n\tv_mfma_f32_32x32x16_bf16 v[96:111], v[160:163], v[184:187], v[96:111]\n\tds_read_b64_tr_b16 v[144:145], v4 offset:9216\n\tds_read_b64_tr_b16 v[146:147], v4 offset:9728\n\tds_read_b64_tr_b16 v[148:149], v5 offset:9216\n\tds_read_b64_tr_b16 v[150:151], v5 offset:9728\n\t"
            "ds_read_b64_tr_b16 v[152:153], v6 offset:9216\n\tds_read_b64_tr_b16 v[154:155], v6 offset:9728\n\ts_waitcnt vmcnt(2)\n\tv_max3_u32 v12, v192, v194, v196\n\tv_max_u32 v12, v12, v198\n\tv_cmp_eq_u32 vcc, -1, v12\n\t"
            "v_mfma_f32_32x32x16_bf16 v[96:111], v[168:171], v[188:191], v[96:111]\n\tds_read_b64_tr_b16 v[156:157], v7 offset:9216\n\tds_read_b64_tr_b16 v[158:159], v7 offset:9728\n\ts_cbranch_vccnz 118f\n\t218:\ns_waitcnt lgkmcnt(10)\n\t"
            "v_mfma_f32_32x32x16_bf16 v[96:111], v[128:131], v[192:195], v[96:111]\n\ts_waitcnt vmcnt(0)\n\tv_max3_u32 v12, v200, v202, v204\n\tv_max_u32 v12, v12, v206\n\tv_cmp_eq_u32 vcc, -1, v12\n\tv_mfma_f32_32x32x16_bf16 v[96:111], v[136:139], v[196:199], v[96:111]\n\t"
            "s_cbranch_vccnz 119f\n\t219:\ns_waitcnt lgkmcnt(0)\n\tv_mfma_f32_32x32x16_bf16 v[96:111], v[144:147], v[200:203], v[96:111]\n\tv_cmp_ne_u32 s[74:75], 0, v17\n\tv_cmp_eq_u32 vcc, 0, v16\n\t"
            "v_mfma_f32_32x32x16_bf16 v[96:111], v[152:155], v[204:207], v[96:111]\n\tv_mfma_f32_32x32x16_bf16 v[112:127], v[164:167], v[184:187], v[112:127]\n\ts_cbranch_vccnz 7f\n\t8:\nv_add_u32 v12, v17, v23\n\ts_pack_ll_b32_b16 s78, s74, s74\n\t"
            "s_mov_b32 s79, s78\n\ts_pack_hh_b32_b16 s80, s74, s74\n\ts_mov_b32 s81, s80\n\tv_cndmask_b32 v3, v3, 2.0, s[78:79]\n\tv_cndmask_b32 v11, v11, 2.0, s[80:81]\n\ts_cmp_eq_u32 s74, 0\n\t"
            "v_mfma_f32_32x32x16_bf16 v[112:127], v[172:175], v[188:191], v[112:127]\n\ts_nop 0\n\tv_fmac_f32 v64, v96, v32\n\tv_fmac_f32 v65, v97, v33\n\tv_fmac_f32 v66, v98, v34\n\tv_fmac_f32 v67, v99, v35\n\t"
            "v_fmac_f32 v68, v100, v36\n\tv_fmac_f32 v69, v101, v37\n\tv_fmac_f32 v70, v102, v38\n\tv_mfma_f32_32x32x16_bf16 v[112:127], v[132:135], v[192:195], v[112:127]\n\ts_cbranch_scc1 1f\n\ts_mov_b64 exec, s[74:75]\n\t"
            "ds_write_b128 v12, v[96:99] offset:0\n\tds_write_b128 v12, v[100:103] offset:32\n\tds_write_b128 v12, v[104:107] offset:64\n\tds_write_b128 v12, v[108:111] offset:96\n\ts_mov_b64 exec, -1\n\t1:\n"
            "v_fmac_f32 v71, v103, v39\n\tv_fmac_f32 v72, v104, v40\n\tv_fmac_f32 v73, v105, v41\n\tv_fmac_f32 v74, v106, v42\n\tv_fmac_f32 v75, v107, v43\n\tv_fmac_f32 v76, v108, v44\n\t"
            "v_fmac_f32 v77, v109, v45\n\tv_mfma_f32_32x32x16_bf16 v[112:127], v[140:143], v[196:199], v[112:127]\n\tv_fmac_f32 v78, v110, v46\n\tv_fmac_f32 v79, v111, v47\n\tv_cvt_pk_bf16_f32 v240, v64, v65\n\tv_cvt_pk_bf16_f32 v242, v68, v69\n\t"
            "v_cvt_pk_bf16_f32 v241, v66, v67\n\tv_cvt_pk_bf16_f32 v243, v70, v71\n\tv_cvt_pk_bf16_f32 v244, v72, v73\n\tv_mfma_f32_32x32x16_bf16 v[112:127], v[148:151], v[200:203], v[112:127]\n\tv_cvt_pk_bf16_f32 v246, v76, v77\n\tv_cvt_pk_bf16_f32 v245, v74, v75\n\t"
            "v_cvt_pk_bf16_f32 v247, v78, v79\n\tv_permlane32_swap_b32 v240, v242\n\tv_permlane32_swap_b32 v241, v243\n\tv_permlane32_swap_b32 v244, v246\n\tv_permlane32_swap_b32 v245, v247\n\tv_mfma_f32_32x32x16_bf16 v[112:127], v[156:159], v[204:207], v[112:127]\n\t"
            "v_permlane16_swap_b32 v240, v244\n\tv_permlane16_swap_b32 v242, v246\n\tv_permlane16_swap_b32 v241, v245\n\tv_permlane16_swap_b32 v243, v247\n\tbuffer_store_dwordx4 v[240:243], v3, s[52:55], 0 offen offset:0 sc1\n\tbuffer_store_dwordx4 v[244:247], v11, s[52:55], 0 offen offset:0 sc1\n\t"
            "s_cbranch_scc1 1f\n\ts_mov_b64 exec, s[74:75]\n\ts_nop 3\n\tds_write_b128 v12, v[112:115] offset:128\n\tds_write_b128 v12, v[116:119] offset:160\n\tds_write_b128 v12, v[120:123] offset:192\n\t"
            "ds_write_b128 v12, v[124:127] offset:224\n\tds_write_b32 v17, v17 offset:256\n\ts_mov_b64 exec, -1\n\t1:\ns_nop 4\n\tv_fmac_f32 v80, v112, v48\n\t"
            "v_fmac_f32 v81, v113, v49\n\tv_fmac_f32 v82, v114, v50\n\tv_fmac_f32 v83, v115, v51\n\tv_fmac_f32 v84, v116, v52\n\tv_fmac_f32 v85, v117, v53\n\tv_fmac_f32 v86, v118, v54\n\t"
            "v_fmac_f32 v87, v119, v55\n\tv_fmac_f32 v88, v120, v56\n\tv_fmac_f32 v89, v121, v57\n\tv_fmac_f32 v90, v122, v58\n\tv_fmac_f32 v91, v123, v59\n\tv_fmac_f32 v92, v124, v60\n\t"
            "v_fmac_f32 v93, v125, v61\n\tv_fmac_f32 v94, v126, v62\n\tv_fmac_f32 v95, v127, v63\n\tv_cvt_pk_bf16_f32 v248, v80, v81\n\tv_cvt_pk_bf16_f32 v250, v84, v85\n\tv_cvt_pk_bf16_f32 v249, v82, v83\n\t"
            "v_cvt_pk_bf16_f32 v251, v86, v87\n\tv_cvt_pk_bf16_f32 v252, v88, v89\n\tv_cvt_pk_bf16_f32 v254, v92, v93\n\tv_cvt_pk_bf16_f32 v253, v90, v91\n\tv_cvt_pk_bf16_f32 v255, v94, v95\n\tv_permlane32_swap_b32 v248, v250\n\t"
            "v_permlane32_swap_b32 v249, v251\n\tv_permlane32_swap_b32 v252, v254\n\tv_permlane32_swap_b32 v253, v255\n\ts_nop 0\n\tv_permlane16_swap_b32 v248, v252\n\tv_permlane16_swap_b32 v250, v254\n\t"
            "v_permlane16_swap_b32 v249, v253\n\tv_permlane16_swap_b32 v251, v255\n\tbuffer_store_dwordx4 v[248:251], v3, s[52:55], 0 offen offset:64 sc1\n\tbuffer_store_dwordx4 v[252:255], v11, s[52:55], 0 offen offset:64 sc1\n\t9:\n"
            "s_endpgm\n\t.LG0:\ns_cmp_eq_u32 s56, 3\n\ts_cbranch_scc1 .LH\n\tv_mul_f32 v21, 0xbfb8aa3b, v21\n\tv_mul_f32 v20, v20, v21\n\t"
            "v_permlane16_swap_b32 v240, v244\n\tv_permlane16_swap_b32 v241, v245\n\tv_permlane16_swap_b32 v242, v246\n\tv_permlane16_swap_b32 v243, v247\n\tv_permlane16_swap_b32 v248, v252\n\tv_permlane16_swap_b32 v249, v253\n\t"
            "v_permlane16_swap_b32 v250, v254\n\tv_permlane16_swap_b32 v251, v255\n\tv_permlane32_swap_b32 v240, v242\n\tv_permlane32_swap_b32 v241, v243\n\tv_permlane32_swap_b32 v244, v246\n\tv_permlane32_swap_b32 v245, v247\n\t"
            "v_permlane32_swap_b32 v248, v250\n\tv_permlane32_swap_b32 v249, v251\n\tv_permlane32_swap_b32 v252, v254\n\tv_permlane32_swap_b32 v253, v255\n\tv_lshlrev_b32 v64, 16, v240\n\tv_and_b32 v65, 0xffff0000, v240\n\t"
            "v_lshlrev_b32 v66, 16, v241\n\tv_and_b32 v67, 0xffff0000, v241\n\tv_lshlrev_b32 v68, 16, v242\n\tv_and_b32 v69, 0xffff0000, v242\n\tv_lshlrev_b32 v70, 16, v243\n\tv_and_b32 v71, 0xffff0000, v243\n\t"
            "v_lshlrev_b32 v72, 16, v244\n\tv_and_b32 v73, 0xffff0000, v244\n\tv_lshlrev_b32 v74, 16, v245\n\tv_and_b32 v75, 0xffff0000, v245\n\tv_lshlrev_b32 v76, 16, v246\n\tv_and_b32 v77, 0xffff0000, v246\n\t"
            "v_lshlrev_b32 v78, 16, v247\n\tv_and_b32 v79, 0xffff0000, v247\n\tv_lshlrev_b32 v80, 16, v248\n\tv_and_b32 v81, 0xffff0000, v248\n\tv_lshlrev_b32 v82, 16, v249\n\tv_and_b32 v83, 0xffff0000, v249\n\t"
            "v_lshlrev_b32 v84, 16, v250\n\tv_and_b32 v85, 0xffff0000, v250\n\tv_lshlrev_b32 v86, 16, v251\n\tv_and_b32 v87, 0xffff0000, v251\n\tv_lshlrev_b32 v88, 16, v252\n\tv_and_b32 v89, 0xffff0000, v252\n\t"
            "v_lshlrev_b32 v90, 16, v253\n\tv_and_b32 v91, 0xffff0000, v253\n\tv_lshlrev_b32 v92, 16, v254\n\tv_and_b32 v93, 0xffff0000, v254\n\tv_lshlrev_b32 v94, 16, v255\n\tv_and_b32 v95, 0xffff0000, v255\n\t"
            "v_fma_f32 v96, v64, v21, v20\n\tv_fma_f32 v97, v65, v21, v20\n\tv_fma_f32 v98, v66, v21, v20\n\tv_fma_f32 v99, v67, v21, v20\n\tv_fma_f32 v100, v68, v21, v20\n\tv_fma_f32 v101, v69, v21, v20\n\t"
            "v_fma_f32 v102, v70, v21, v20\n\tv_fma_f32 v103, v71, v21, v20\n\tv_fma_f32 v104, v72, v21, v20\n\tv_fma_f32 v105, v73, v21, v20\n\tv_fma_f32 v106, v74, v21, v20\n\tv_fma_f32 v107, v75, v21, v20\n\t"
            "v_fma_f32 v108, v76, v21, v20\n\tv_fma_f32 v109, v77, v21, v20\n\tv_fma_f32 v110, v78, v21, v20\n\tv_fma_f32 v111, v79, v21, v20\n\tv_fma_f32 v112, v80, v21, v20\n\tv_fma_f32 v113, v81, v21, v20\n\t"
            "v_fma_f32 v114, v82, v21, v20\n\tv_fma_f32 v115, v83, v21, v20\n\tv_fma_f32 v116, v84, v21, v20\n\tv_fma_f32 v117, v85, v21, v20\n\tv_fma_f32 v118, v86, v21, v20\n\tv_fma_f32 v119, v87, v21, v20\n\t"
            "v_fma_f32 v120, v88, v21, v20\n\tv_fma_f32 v121, v89, v21, v20\n\tv_fma_f32 v122, v90, v21, v20\n\tv_fma_f32 v123, v91, v21, v20\n\tv_fma_f32 v124, v92, v21, v20\n\tv_fma_f32 v125, v93, v21, v20\n\t"
            "v_fma_f32 v126, v94, v21, v20\n\tv_fma_f32 v127, v95, v21, v20\n\ts_setpc_b64 s[76:77]\n\t.LG1:\nv_exp_f32 v32, v96\n\tv_exp_f32 v33, v97\n\t"
            "v_exp_f32 v34, v98\n\tv_exp_f32 v35, v99\n\tv_exp_f32 v36, v100\n\tv_exp_f32 v37, v101\n\tv_exp_f32 v38, v102\n\tv_exp_f32 v39, v103\n\t"
            "v_exp_f32 v40, v104\n\tv_exp_f32 v41, v105\n\tv_exp_f32 v42, v106\n\tv_exp_f32 v43, v107\n\tv_exp_f32 v44, v108\n\tv_exp_f32 v45, v109\n\t"
            "v_exp_f32 v46, v110\n\tv_exp_f32 v47, v111\n\tv_exp_f32 v48, v112\n\tv_exp_f32 v49, v113\n\tv_exp_f32 v50, v114\n\tv_exp_f32 v51, v115\n\t"
            "v_exp_f32 v52, v116\n\tv_exp_f32 v53, v117\n\tv_exp_f32 v54, v118\n\tv_exp_f32 v55, v119\n\tv_exp_f32 v56, v120\n\tv_exp_f32 v57, v121\n\t"
            "v_exp_f32 v58, v122\n\tv_exp_f32 v59, v123\n\tv_exp_f32 v60, v124\n\tv_exp_f32 v61, v125\n\tv_exp_f32 v62, v126\n\tv_exp_f32 v63, v127\n\t"
            "v_add_f32 v32, 1.0, v32\n\tv_add_f32 v33, 1.0, v33\n\tv_add_f32 v34, 1.0, v34\n\tv_add_f32 v35, 1.0, v35\n\tv_add_f32 v36, 1.0, v36\n\tv_add_f32 v37, 1.0, v37\n\t"
            "v_add_f32 v38, 1.0, v38\n\tv_add_f32 v39, 1.0, v39\n\tv_add_f32 v40, 1.0, v40\n\tv_add_f32 v41, 1.0, v41\n\tv_add_f32 v42, 1.0, v42\n\tv_add_f32 v43, 1.0, v43\n\t"
            "v_add_f32 v44, 1.0, v44\n\tv_add_f32 v45, 1.0, v45\n\tv_add_f32 v46, 1.0, v46\n\tv_add_f32 v47, 1.0, v47\n\tv_add_f32 v48, 1.0, v48\n\tv_add_f32 v49, 1.0, v49\n\t"
            "v_add_f32 v50, 1.0, v50\n\tv_add_f32 v51, 1.0, v51\n\tv_add_f32 v52, 1.0, v52\n\tv_add_f32 v53, 1.0, v53\n\tv_add_f32 v54, 1.0, v54\n\tv_add_f32 v55, 1.0, v55\n\t"
            "v_add_f32 v56, 1.0, v56\n\tv_add_f32 v57, 1.0, v57\n\ts_mov_b32 s83, 1\n\ts_setpc_b64 s[76:77]\n\t.LG2:\nv_add_f32 v58, 1.0, v58\n\t"
            "v_add_f32 v59, 1.0, v59\n\tv_add_f32 v60, 1.0, v60\n\tv_add_f32 v61, 1.0, v61\n\tv_add_f32 v62, 1.0, v62\n\tv_add_f32 v63, 1.0, v63\n\tv_rcp_f32 v32, v32\n\t"
            "v_rcp_f32 v33, v33\n\tv_rcp_f32 v34, v34\n\tv_rcp_f32 v35, v35\n\tv_rcp_f32 v36, v36\n\tv_rcp_f32 v37, v37\n\tv_rcp_f32 v38, v38\n\t"
            "v_rcp_f32 v39, v39\n\tv_rcp_f32 v40, v40\n\tv_rcp_f32 v41, v41\n\tv_rcp_f32 v42, v42\n\tv_rcp_f32 v43, v43\n\tv_rcp_f32 v44, v44\n\t"
            "v_rcp_f32 v45, v45\n\tv_rcp_f32 v46, v46\n\tv_rcp_f32 v47, v47\n\tv_rcp_f32 v48, v48\n\tv_rcp_f32 v49, v49\n\tv_rcp_f32 v50, v50\n\t"
            "v_rcp_f32 v51, v51\n\tv_rcp_f32 v52, v52\n\tv_rcp_f32 v53, v53\n\tv_rcp_f32 v54, v54\n\tv_rcp_f32 v55, v55\n\tv_rcp_f32 v56, v56\n\t"
            "v_rcp_f32 v57, v57\n\tv_rcp_f32 v58, v58\n\tv_rcp_f32 v59, v59\n\tv_rcp_f32 v60, v60\n\tv_rcp_f32 v61, v61\n\tv_rcp_f32 v62, v62\n\t"
            "v_rcp_f32 v63, v63\n\tv_mul_f32 v32, v32, v96\n\tv_mul_f32 v33, v33, v97\n\tv_mul_f32 v34, v34, v98\n\tv_mul_f32 v35, v35, v99\n\tv_mul_f32 v36, v36, v100\n\t"
            "v_mul_f32 v37, v37, v101\n\tv_mul_f32 v38, v38, v102\n\tv_mul_f32 v39, v39, v103\n\tv_mul_f32 v40, v40, v104\n\tv_mul_f32 v41, v41, v105\n\tv_mul_f32 v42, v42, v106\n\t"
            "v_mul_f32 v43, v43, v107\n\tv_mul_f32 v44, v44, v108\n\tv_mul_f32 v45, v45, v109\n\tv_mul_f32 v46, v46, v110\n\tv_mul_f32 v47, v47, v111\n\ts_mov_b32 s83, 2\n\t"
            "s_setpc_b64 s[76:77]\n\t7:\nds_read_b32 v16, v8 offset:64\n\tds_read_b32 v17, v9 offset:1344\n\ts_waitcnt lgkmcnt(0)\n\tv_cmp_ne_u32 s[74:75], 0, v17\n\t"
            "v_cmp_eq_u32 vcc, 0, v16\n\ts_cbranch_vccz 71f\n\ts_sleep 1\n\ts_sub_u32 s68, s68, 1\n\ts_cbranch_scc0 7b\n\ts_branch 9b\n\t"
            "71:\ns_branch 8b\n\t5:\ns_sleep 1\n\tds_read_b64 v[14:15], v8 offset:40\n\ts_waitcnt lgkmcnt(0)\n\t"
            "v_min_u32 v14, v14, v15\n\tv_cmp_ge_u32 vcc, s71, v14\n\ts_cbranch_vccz 61f\n\ts_sub_u32 s68, s68, 1\n\ts_cbranch_scc0 5b\n\ts_branch 9b\n\t"
            "61:\n"
            "s_branch 6b\n\t62:\nds_read_b128 v[16:19], v8 offset:48\n\tds_read_b64_tr_b16 v[128:129], v4 offset:40960\n\tds_read_b64_tr_b16 v[130:131], v4 offset:41472\n\tds_read_b64_tr_b16 v[132:133], v5 offset:40960\n\t"
            "ds_read_b64_tr_b16 v[134:135], v5 offset:41472\n\tds_read_b64_tr_b16 v[136:137], v6 offset:40960\n\tds_read_b64_tr_b16 v[138:139], v6 offset:41472\n\tds_read_b64_tr_b16 v[140:141], v7 offset:40960\n\tds_read_b64_tr_b16 v[142:143], v7 offset:41472\n\tds_read_b64_tr_b16 v[144:145], v4 offset:41984\n\t"
            "ds_read_b64_tr_b16 v[146:147], v4 offset:42496\n\tds_read_b64_tr_b16 v[148:149], v5 offset:41984\n\tds_read_b64_tr_b16 v[150:151], v5 offset:42496\n\tds_read_b64_tr_b16 v[152:153], v6 offset:41984\n\tds_read_b64_tr_b16 v[154:155], v6 offset:42496\n\tds_read_b64_tr_b16 v[156:157], v7 offset:41984\n\t"
            "ds_read_b64_tr_b16 v[158:159], v7 offset:42496\n\ts_waitcnt lgkmcnt(15)\n\tv_min3_u32 v16, v16, v17, v18\n\tv_min_u32 v16, v16, v19\n\tv_cmp_eq_u32 vcc, 0, v16\n\ts_cbranch_vccz 63b\n\t"
            "s_sub_u32 s68, s68, 1\n\ts_cbranch_scc0 62b\n\ts_branch 9b\n\t100:\ns_mov_b32 s70, 4\n\ts_call_b64 s[72:73], 300f\n\t"
            "v_max3_u32 v12, v176, v178, v180\n\tv_max_u32 v12, v12, v182\n\tv_cmp_eq_u32 vcc, -1, v12\n\ts_cbranch_vccnz 100b\n\ts_branch 200b\n\t101:\n"
            "s_mov_b32 s70, 5\n\ts_call_b64 s[72:73], 301f\n\tv_max3_u32 v12, v184, v186, v188\n\tv_max_u32 v12, v12, v190\n\tv_cmp_eq_u32 vcc, -1, v12\n\ts_cbranch_vccnz 101b\n\t"
            "s_branch 201b\n\t102:\ns_mov_b32 s70, 6\n\ts_call_b64 s[72:73], 302f\n\tv_max3_u32 v12, v192, v194, v196\n\tv_max_u32 v12, v12, v198\n\t"
            "v_cmp_eq_u32 vcc, -1, v12\n\ts_cbranch_vccnz 102b\n\ts_branch 202b\n\t103:\ns_mov_b32 s70, 7\n\ts_call_b64 s[72:73], 303f\n\t"
            "v_max3_u32 v12, v200, v202, v204\n\tv_max_u32 v12, v12, v206\n\tv_cmp_eq_u32 vcc, -1, v12\n\ts_cbranch_vccnz 103b\n\ts_branch 203b\n\t104:\n"
            "s_mov_b32 s70, 8\n\ts_call_b64 s[72:73], 304f\n\tv_max3_u32 v12, v208, v210, v212\n\tv_max_u32 v12, v12, v214\n\tv_cmp_eq_u32 vcc, -1, v12\n\ts_cbranch_vccnz 104b\n\t"
            "s_branch 204b\n\t105:\ns_mov_b32 s70, 9\n\ts_call_b64 s[72:73], 305f\n\tv_max3_u32 v12, v216, v218, v220\n\tv_max_u32 v12, v12, v222\n\t"
            "v_cmp_eq_u32 vcc, -1, v12\n\ts_cbranch_vccnz 105b\n\ts_branch 205b\n\t106:\ns_mov_b32 s70, 10\n\ts_call_b64 s[72:73], 306f\n\t"
            "v_max3_u32 v12, v224, v226, v228\n\tv_max_u32 v12, v12, v230\n\tv_cmp_eq_u32 vcc, -1, v12\n\ts_cbranch_vccnz 106b\n\ts_branch 206b\n\t107:\n"
            "s_mov_b32 s70, 11\n\ts_call_b64 s[72:73], 307f\n\tv_max3_u32 v12, v232, v234, v236\n\tv_max_u32 v12, v12, v238\n\tv_cmp_eq_u32 vcc, -1, v12\n\ts_cbranch_vccnz 107b\n\t"
            "s_branch 207b\n\t108:\ns_mov_b32 s70, 12\n\ts_call_b64 s[72:73], 308f\n\tv_max3_u32 v12, v176, v178, v180\n\tv_max_u32 v12, v12, v182\n\t"
            "v_cmp_eq_u32 vcc, -1, v12\n\ts_cbranch_vccnz 108b\n\ts_branch 208b\n\t109:\ns_mov_b32 s70, 13\n\ts_call_b64 s[72:73], 309f\n\t"
            "v_max3_u32 v12, v184, v186, v188\n\tv_max_u32 v12, v12, v190\n\tv_cmp_eq_u32 vcc, -1, v12\n\ts_cbranch_vccnz 109b\n\ts_branch 209b\n\t110:\n"
            "s_mov_b32 s70, 14\n\ts_call_b64 s[72:73], 310f\n\tv_max3_u32 v12, v192, v194, v196\n\tv_max_u32 v12, v12, v198\n\tv_cmp_eq_u32 vcc, -1, v12\n\ts_cbranch_vccnz 110b\n\t"
            "s_branch 210b\n\t111:\ns_mov_b32 s70, 15\n\ts_call_b64 s[72:73], 311f\n\tv_max3_u32 v12, v200, v202, v204\n\tv_max_u32 v12, v12, v206\n\t"
            "v_cmp_eq_u32 vcc, -1, v12\n\ts_cbranch_vccnz 111b\n\ts_branch 211b\n\t112:\ns_mov_b32 s70, 16\n\ts_call_b64 s[72:73], 312f\n\t"
            "v_max3_u32 v12, v208, v210, v212\n\tv_max_u32 v12, v12, v214\n\tv_cmp_eq_u32 vcc, -1, v12\n\ts_cbranch_vccnz 112b\n\ts_branch 212b\n\t113:\n"
            "s_mov_b32 s70, 17\n\ts_call_b64 s[72:73], 313f\n\tv_max3_u32 v12, v216, v218, v220\n\tv_max_u32 v12, v12, v222\n\tv_cmp_eq_u32 vcc, -1, v12\n\ts_cbranch_vccnz 113b\n\t"
            "s_branch 213b\n\t114:\ns_mov_b32 s70, 18\n\ts_call_b64 s[72:73], 314f\n\tv_max3_u32 v12, v224, v226, v228\n\tv_max_u32 v12, v12, v230\n\t"
            "v_cmp_eq_u32 vcc, -1, v12\n\ts_cbranch_vccnz 114b\n\ts_branch 214b\n\t115:\ns_mov_b32 s70, 19\n\ts_call_b64 s[72:73], 315f\n\t"
            "v_max3_u32 v12, v232, v234, v236\n\tv_max_u32 v12, v12, v238\n\tv_cmp_eq_u32 vcc, -1, v12\n\ts_cbranch_vccnz 115b\n\ts_branch 215b\n\t116:\n"
            "s_mov_b32 s70, 19\n\ts_call_b64 s[72:73], 316f\n\tv_max3_u32 v12, v176, v178, v180\n\tv_max_u32 v12, v12, v182\n\tv_cmp_eq_u32 vcc, -1, v12\n\ts_cbranch_vccnz 116b\n\t"
            "s_branch 216b\n\t117:\ns_mov_b32 s70, 19\n\ts_call_b64 s[72:73], 317f\n\tv_max3_u32 v12, v184, v186, v188\n\tv_max_u32 v12, v12, v190\n\t"
            "v_cmp_eq_u32 vcc, -1, v12\n\ts_cbranch_vccnz 117b\n\ts_branch 217b\n\t118:\ns_mov_b32 s70, 19\n\ts_call_b64 s[72:73], 318f\n\t"
            "v_max3_u32 v12, v192, v194, v196\n\tv_max_u32 v12, v12, v198\n\tv_cmp_eq_u32 vcc, -1, v12\n\ts_cbranch_vccnz 118b\n\ts_branch 218b\n\t119:\n"
            "s_mov_b32 s70, 19\n\ts_call_b64 s[72:73], 319f\n\tv_max3_u32 v12, v200, v202, v204\n\tv_max_u32 v12, v12, v206\n\tv_cmp_eq_u32 vcc, -1, v12\n\ts_cbranch_vccnz 119b\n\t"
            "s_branch 219b\n\t400:\nv_max3_u32 v12, v176, v178, v180\n\tv_max_u32 v12, v12, v182\n\tv_cmp_eq_u32 vcc, -1, v12\n\ts_cbranch_vccz 11f\n\t"
            "buffer_load_dwordx4 v[176:179], v2, s[44:47], s4 offen offset:1024 sc1\n\tbuffer_load_dwordx4 v[180:183], v2, s[44:47], s4 offen offset:2048 sc1\n\ts_branch 11f\n\t300:\nbuffer_load_dwordx4 v[176:179], v2, s[44:47], s4 offen offset:1024 sc1\n\tbuffer_load_dwordx4 v[180:183], v2, s[44:47], s4 offen offset:2048 sc1\n\t"
            "s_waitcnt vmcnt(2)\n\t11:\ns_cmp_eq_u32 s70, 0\n\ts_cbranch_scc0 401f\n\ts_branch 12f\n\t401:\n"
            "v_max3_u32 v12, v184, v186, v188\n\tv_max_u32 v12, v12, v190\n\tv_cmp_eq_u32 vcc, -1, v12\n\ts_cbranch_vccz 11f\n\tbuffer_load_dwordx4 v[184:187], v2, s[44:47], s5 offen offset:1024 sc1\n\tbuffer_load_dwordx4 v[188:191], v2, s[44:47], s5 offen offset:2048 sc1\n\t"
            "s_branch 11f\n\t301:\nbuffer_load_dwordx4 v[184:187], v2, s[44:47], s5 offen offset:1024 sc1\n\tbuffer_load_dwordx4 v[188:191], v2, s[44:47], s5 offen offset:2048 sc1\n\ts_waitcnt vmcnt(2)\n\t11:\n"
            "s_cmp_eq_u32 s70, 1\n\ts_cbranch_scc0 402f\n\ts_branch 12f\n\t402:\nv_max3_u32 v12, v192, v194, v196\n\tv_max_u32 v12, v12, v198\n\t"
            "v_cmp_eq_u32 vcc, -1, v12\n\ts_cbranch_vccz 11f\n\tbuffer_load_dwordx4 v[192:195], v2, s[44:47], s6 offen offset:1024 sc1\n\tbuffer_load_dwordx4 v[196:199], v2, s[44:47], s6 offen offset:2048 sc1\n\ts_branch 11f\n\t302:\n"
            "buffer_load_dwordx4 v[192:195], v2, s[44:47], s6 offen offset:1024 sc1\n\tbuffer_load_dwordx4 v[196:199], v2, s[44:47], s6 offen offset:2048 sc1\n\ts_waitcnt vmcnt(2)\n\t11:\ns_cmp_eq_u32 s70, 2\n\ts_cbranch_scc0 403f\n\t"
            "s_branch 12f\n\t403:\nv_max3_u32 v12, v200, v202, v204\n\tv_max_u32 v12, v12, v206\n\tv_cmp_eq_u32 vcc, -1, v12\n\ts_cbranch_vccz 11f\n\t"
            "buffer_load_dwordx4 v[200:203], v2, s[44:47], s7 offen offset:1024 sc1\n\tbuffer_load_dwordx4 v[204:207], v2, s[44:47], s7 offen offset:2048 sc1\n\ts_branch 11f\n\t303:\nbuffer_load_dwordx4 v[200:203], v2, s[44:47], s7 offen offset:1024 sc1\n\tbuffer_load_dwordx4 v[204:207], v2, s[44:47], s7 offen offset:2048 sc1\n\t"
            "s_waitcnt vmcnt(2)\n\t11:\ns_cmp_eq_u32 s70, 3\n\ts_cbranch_scc0 404f\n\ts_branch 12f\n\t404:\n"
            "v_max3_u32 v12, v208, v210, v212\n\tv_max_u32 v12, v12, v214\n\tv_cmp_eq_u32 vcc, -1, v12\n\ts_cbranch_vccz 11f\n\tbuffer_load_dwordx4 v[208:211], v2, s[44:47], s8 offen offset:1024 sc1\n\tbuffer_load_dwordx4 v[212:215], v2, s[44:47], s8 offen offset:2048 sc1\n\t"
            "s_branch 11f\n\t304:\nbuffer_load_dwordx4 v[208:211], v2, s[44:47], s8 offen offset:1024 sc1\n\tbuffer_load_dwordx4 v[212:215], v2, s[44:47], s8 offen offset:2048 sc1\n\ts_waitcnt vmcnt(2)\n\t11:\n"
            "s_cmp_eq_u32 s70, 4\n\ts_cbranch_scc0 405f\n\ts_branch 12f\n\t405:\nv_max3_u32 v12, v216, v218, v220\n\tv_max_u32 v12, v12, v222\n\t"
            "v_cmp_eq_u32 vcc, -1, v12\n\ts_cbranch_vccz 11f\n\tbuffer_load_dwordx4 v[216:219], v2, s[44:47], s9 offen offset:1024 sc1\n\tbuffer_load_dwordx4 v[220:223], v2, s[44:47], s9 offen offset:2048 sc1\n\ts_branch 11f\n\t305:\n"
            "buffer_load_dwordx4 v[216:219], v2, s[44:47], s9 offen offset:1024 sc1\n\tbuffer_load_dwordx4 v[220:223], v2, s[44:47], s9 offen offset:2048 sc1\n\ts_waitcnt vmcnt(2)\n\t11:\ns_cmp_eq_u32 s70, 5\n\ts_cbranch_scc0 406f\n\t"
            "s_branch 12f\n\t406:\nv_max3_u32 v12, v224, v226, v228\n\tv_max_u32 v12, v12, v230\n\tv_cmp_eq_u32 vcc, -1, v12\n\ts_cbranch_vccz 11f\n\t"
            "buffer_load_dwordx4 v[224:227], v2, s[44:47], s10 offen offset:1024 sc1\n\tbuffer_load_dwordx4 v[228:231], v2, s[44:47], s10 offen offset:2048 sc1\n\ts_branch 11f\n\t306:\nbuffer_load_dwordx4 v[224:227], v2, s[44:47], s10 offen offset:1024 sc1\n\tbuffer_load_dwordx4 v[228:231], v2, s[44:47], s10 offen offset:2048 sc1\n\t"
            "s_waitcnt vmcnt(2)\n\t11:\ns_cmp_eq_u32 s70, 6\n\ts_cbranch_scc0 407f\n\ts_branch 12f\n\t407:\n"
            "v_max3_u32 v12, v232, v234, v236\n\tv_max_u32 v12, v12, v238\n\tv_cmp_eq_u32 vcc, -1, v12\n\ts_cbranch_vccz 11f\n\tbuffer_load_dwordx4 v[232:235], v2, s[44:47], s11 offen offset:1024 sc1\n\tbuffer_load_dwordx4 v[236:239], v2, s[44:47], s11 offen offset:2048 sc1\n\t"
            "s_branch 11f\n\t307:\nbuffer_load_dwordx4 v[232:235], v2, s[44:47], s11 offen offset:1024 sc1\n\tbuffer_load_dwordx4 v[236:239], v2, s[44:47], s11 offen offset:2048 sc1\n\ts_waitcnt vmcnt(2)\n\t11:\n"
            "s_cmp_eq_u32 s70, 7\n\ts_cbranch_scc0 408f\n\ts_branch 12f\n\t408:\nv_max3_u32 v12, v176, v178, v180\n\tv_max_u32 v12, v12, v182\n\t"
            "v_cmp_eq_u32 vcc, -1, v12\n\ts_cbranch_vccz 11f\n\tbuffer_load_dwordx4 v[176:179], v2, s[44:47], s12 offen offset:1024 sc1\n\tbuffer_load_dwordx4 v[180:183], v2, s[44:47], s12 offen offset:2048 sc1\n\ts_branch 11f\n\t308:\n"
            "buffer_load_dwordx4 v[176:179], v2, s[44:47], s12 offen offset:1024 sc1\n\tbuffer_load_dwordx4 v[180:183], v2, s[44:47], s12 offen offset:2048 sc1\n\ts_waitcnt vmcnt(2)\n\t11:\ns_cmp_eq_u32 s70, 8\n\ts_cbranch_scc0 409f\n\t"
            "s_branch 12f\n\t409:\nv_max3_u32 v12, v184, v186, v188\n\tv_max_u32 v12, v12, v190\n\tv_cmp_eq_u32 vcc, -1, v12\n\ts_cbranch_vccz 11f\n\t"
            "buffer_load_dwordx4 v[184:187], v2, s[44:47], s13 offen offset:1024 sc1\n\tbuffer_load_dwordx4 v[188:191], v2, s[44:47], s13 offen offset:2048 sc1\n\ts_branch 11f\n\t309:\nbuffer_load_dwordx4 v[184:187], v2, s[44:47], s13 offen offset:1024 sc1\n\tbuffer_load_dwordx4 v[188:191], v2, s[44:47], s13 offen offset:2048 sc1\n\t"
            "s_waitcnt vmcnt(2)\n\t11:\ns_cmp_eq_u32 s70, 9\n\ts_cbranch_scc0 410f\n\ts_branch 12f\n\t410:\n"
            "v_max3_u32 v12, v192, v194, v196\n\tv_max_u32 v12, v12, v198\n\tv_cmp_eq_u32 vcc, -1, v12\n\ts_cbranch_vccz 11f\n\tbuffer_load_dwordx4 v[192:195], v2, s[44:47], s4 offen offset:0 sc1\n\tbuffer_load_dwordx4 v[196:199], v2, s[44:47], s4 offen offset:3072 sc1\n\t"
            "s_branch 11f\n\t310:\nbuffer_load_dwordx4 v[192:195], v2, s[44:47], s4 offen offset:0 sc1\n\tbuffer_load_dwordx4 v[196:199], v2, s[44:47], s4 offen offset:3072 sc1\n\ts_waitcnt vmcnt(2)\n\t11:\n"
            "s_cmp_eq_u32 s70, 10\n\ts_cbranch_scc0 411f\n\ts_branch 12f\n\t411:\nv_max3_u32 v12, v200, v202, v204\n\tv_max_u32 v12, v12, v206\n\t"
            "v_cmp_eq_u32 vcc, -1, v12\n\ts_cbranch_vccz 11f\n\tbuffer_load_dwordx4 v[200:203], v2, s[44:47], s5 offen offset:0 sc1\n\tbuffer_load_dwordx4 v[204:207], v2, s[44:47], s5 offen offset:3072 sc1\n\ts_branch 11f\n\t311:\n"
            "buffer_load_dwordx4 v[200:203], v2, s[44:47], s5 offen offset:0 sc1\n\tbuffer_load_dwordx4 v[204:207], v2, s[44:47], s5 offen offset:3072 sc1\n\ts_waitcnt vmcnt(2)\n\t11:\ns_cmp_eq_u32 s70, 11\n\ts_cbranch_scc0 412f\n\t"
            "s_branch 12f\n\t412:\nv_max3_u32 v12, v208, v210, v212\n\tv_max_u32 v12, v12, v214\n\tv_cmp_eq_u32 vcc, -1, v12\n\ts_cbranch_vccz 11f\n\t"
            "buffer_load_dwordx4 v[208:211], v2, s[44:47], s6 offen offset:0 sc1\n\tbuffer_load_dwordx4 v[212:215], v2, s[44:47], s6 offen offset:3072 sc1\n\ts_branch 11f\n\t312:\nbuffer_load_dwordx4 v[208:211], v2, s[44:47], s6 offen offset:0 sc1\n\tbuffer_load_dwordx4 v[212:215], v2, s[44:47], s6 offen offset:3072 sc1\n\t"
            "s_waitcnt vmcnt(2)\n\t11:\ns_cmp_eq_u32 s70, 12\n\ts_cbranch_scc0 413f\n\ts_branch 12f\n\t413:\n"
            "v_max3_u32 v12, v216, v218, v220\n\tv_max_u32 v12, v12, v222\n\tv_cmp_eq_u32 vcc, -1, v12\n\ts_cbranch_vccz 11f\n\tbuffer_load_dwordx4 v[216:219], v2, s[44:47], s7 offen offset:0 sc1\n\tbuffer_load_dwordx4 v[220:223], v2, s[44:47], s7 offen offset:3072 sc1\n\t"
            "s_branch 11f\n\t313:\nbuffer_load_dwordx4 v[216:219], v2, s[44:47], s7 offen offset:0 sc1\n\tbuffer_load_dwordx4 v[220:223], v2, s[44:47], s7 offen offset:3072 sc1\n\ts_waitcnt vmcnt(2)\n\t11:\n"
            "s_cmp_eq_u32 s70, 13\n\ts_cbranch_scc0 414f\n\ts_branch 12f\n\t414:\nv_max3_u32 v12, v224, v226, v228\n\tv_max_u32 v12, v12, v230\n\t"
            "v_cmp_eq_u32 vcc, -1, v12\n\ts_cbranch_vccz 11f\n\tbuffer_load_dwordx4 v[224:227], v2, s[44:47], s8 offen offset:0 sc1\n\tbuffer_load_dwordx4 v[228:231], v2, s[44:47], s8 offen offset:3072 sc1\n\ts_branch 11f\n\t314:\n"
            "buffer_load_dwordx4 v[224:227], v2, s[44:47], s8 offen offset:0 sc1\n\tbuffer_load_dwordx4 v[228:231], v2, s[44:47], s8 offen offset:3072 sc1\n\ts_waitcnt vmcnt(2)\n\t11:\ns_cmp_eq_u32 s70, 14\n\ts_cbranch_scc0 415f\n\t"
            "s_branch 12f\n\t415:\nv_max3_u32 v12, v232, v234, v236\n\tv_max_u32 v12, v12, v238\n\tv_cmp_eq_u32 vcc, -1, v12\n\ts_cbranch_vccz 11f\n\t"
            "buffer_load_dwordx4 v[232:235], v2, s[44:47], s9 offen offset:0 sc1\n\tbuffer_load_dwordx4 v[236:239], v2, s[44:47], s9 offen offset:3072 sc1\n\ts_branch 11f\n\t315:\nbuffer_load_dwordx4 v[232:235], v2, s[44:47], s9 offen offset:0 sc1\n\tbuffer_load_dwordx4 v[236:239], v2, s[44:47], s9 offen offset:3072 sc1\n\t"
            "s_waitcnt vmcnt(2)\n\t11:\ns_cmp_eq_u32 s70, 15\n\ts_cbranch_scc0 416f\n\ts_branch 12f\n\t416:\n"
            "v_max3_u32 v12, v176, v178, v180\n\tv_max_u32 v12, v12, v182\n\tv_cmp_eq_u32 vcc, -1, v12\n\ts_cbranch_vccz 11f\n\tbuffer_load_dwordx4 v[176:179], v2, s[44:47], s10 offen offset:0 sc1\n\tbuffer_load_dwordx4 v[180:183], v2, s[44:47], s10 offen offset:3072 sc1\n\t"
            "s_branch 11f\n\t316:\nbuffer_load_dwordx4 v[176:179], v2, s[44:47], s10 offen offset:0 sc1\n\tbuffer_load_dwordx4 v[180:183], v2, s[44:47], s10 offen offset:3072 sc1\n\ts_waitcnt vmcnt(2)\n\t11:\n"
            "s_cmp_eq_u32 s70, 16\n\ts_cbranch_scc0 417f\n\ts_branch 12f\n\t417:\nv_max3_u32 v12, v184, v186, v188\n\tv_max_u32 v12, v12, v190\n\t"
            "v_cmp_eq_u32 vcc, -1, v12\n\ts_cbranch_vccz 11f\n\tbuffer_load_dwordx4 v[184:187], v2, s[44:47], s11 offen offset:0 sc1\n\tbuffer_load_dwordx4 v[188:191], v2, s[44:47], s11 offen offset:3072 sc1\n\ts_branch 11f\n\t317:\n"
            "buffer_load_dwordx4 v[184:187], v2, s[44:47], s11 offen offset:0 sc1\n\tbuffer_load_dwordx4 v[188:191], v2, s[44:47], s11 offen offset:3072 sc1\n\ts_waitcnt vmcnt(2)\n\t11:\ns_cmp_eq_u32 s70, 17\n\ts_cbranch_scc0 418f\n\t"
            "s_branch 12f\n\t418:\nv_max3_u32 v12, v192, v194, v196\n\tv_max_u32 v12, v12, v198\n\tv_cmp_eq_u32 vcc, -1, v12\n\ts_cbranch_vccz 11f\n\t"
            "buffer_load_dwordx4 v[192:195], v2, s[44:47], s12 offen offset:0 sc1\n\tbuffer_load_dwordx4 v[196:199], v2, s[44:47], s12 offen offset:3072 sc1\n\ts_branch 11f\n\t318:\nbuffer_load_dwordx4 v[192:195], v2, s[44:47], s12 offen offset:0 sc1\n\tbuffer_load_dwordx4 v[196:199], v2, s[44:47], s12 offen offset:3072 sc1\n\t"
            "s_waitcnt vmcnt(2)\n\t11:\ns_cmp_eq_u32 s70, 18\n\ts_cbranch_scc0 419f\n\ts_branch 12f\n\t419:\n"
            "v_max3_u32 v12, v200, v202, v204\n\tv_max_u32 v12, v12, v206\n\tv_cmp_eq_u32 vcc, -1, v12\n\ts_cbranch_vccz 11f\n\tbuffer_load_dwordx4 v[200:203], v2, s[44:47], s13 offen offset:0 sc1\n\tbuffer_load_dwordx4 v[204:207], v2, s[44:47], s13 offen offset:3072 sc1\n\t"
            "s_branch 11f\n\t319:\nbuffer_load_dwordx4 v[200:203], v2, s[44:47], s13 offen offset:0 sc1\n\tbuffer_load_dwordx4 v[204:207], v2, s[44:47], s13 offen offset:3072 sc1\n\ts_waitcnt vmcnt(2)\n\t11:\n"
            "12:\n"
            "s_sub_u32 s68, s68, 1\n\ts_cbranch_scc1 9b\n\ts_waitcnt vmcnt(0)\n\ts_setpc_b64 s[72:73]\n\t"
            ".LH:\nv_mul_f32 v21, 0xbfb8aa3b, v21\n\tv_mul_f32 v20, v20, v21\n\tv_permlane16_swap_b32 v240, v242\n\tv_permlane16_swap_b32 v241, v243\n\tv_permlane16_swap_b32 v248, v250\n\t"
            "v_permlane16_swap_b32 v249, v251\n\tv_lshlrev_b32 v64, 16, v240\n\tv_and_b32 v65, 0xffff0000, v240\n\tv_lshlrev_b32 v66, 16, v241\n\tv_and_b32 v67, 0xffff0000, v241\n\tv_lshlrev_b32 v68, 16, v242\n\t"
            "v_and_b32 v69, 0xffff0000, v242\n\tv_lshlrev_b32 v70, 16, v243\n\tv_and_b32 v71, 0xffff0000, v243\n\tv_lshlrev_b32 v72, 16, v248\n\tv_and_b32 v73, 0xffff0000, v248\n\tv_lshlrev_b32 v74, 16, v249\n\t"
            "v_and_b32 v75, 0xffff0000, v249\n\tv_lshlrev_b32 v76, 16, v250\n\tv_and_b32 v77, 0xffff0000, v250\n\tv_lshlrev_b32 v78, 16, v251\n\tv_and_b32 v79, 0xffff0000, v251\n\tv_fma_f32 v96, v64, v21, v20\n\t"
            "v_fma_f32 v97, v65, v21, v20\n\tv_fma_f32 v98, v66, v21, v20\n\tv_fma_f32 v99, v67, v21, v20\n\tv_fma_f32 v100, v68, v21, v20\n\tv_fma_f32 v101, v69, v21, v20\n\tv_fma_f32 v102, v70, v21, v20\n\t"
            "v_fma_f32 v103, v71, v21, v20\n\tv_fma_f32 v104, v72, v21, v20\n\tv_fma_f32 v105, v73, v21, v20\n\tv_fma_f32 v106, v74, v21, v20\n\tv_fma_f32 v107, v75, v21, v20\n\tv_fma_f32 v108, v76, v21, v20\n\t"
            "v_fma_f32 v109, v77, v21, v20\n\tv_fma_f32 v110, v78, v21, v20\n\tv_fma_f32 v111, v79, v21, v20\n\tv_exp_f32 v32, v96\n\tv_exp_f32 v33, v97\n\tv_exp_f32 v34, v98\n\t"
            "v_exp_f32 v35, v99\n\tv_exp_f32 v36, v100\n\tv_exp_f32 v37, v101\n\tv_exp_f32 v38, v102\n\tv_exp_f32 v39, v103\n\tv_exp_f32 v40, v104\n\t"
            "v_exp_f32 v41, v105\n\tv_exp_f32 v42, v106\n\tv_exp_f32 v43, v107\n\tv_exp_f32 v44, v108\n\tv_exp_f32 v45, v109\n\tv_exp_f32 v46, v110\n\t"
            "v_exp_f32 v47, v111\n\ts_mov_b32 s83, 3\n\ts_setpc_b64 s[76:77]\n\t.L3:\nv_add_f32 v32, 1.0, v32\n\tv_add_f32 v33, 1.0, v33\n\t"
            "v_add_f32 v34, 1.0, v34\n\tv_add_f32 v35, 1.0, v35\n\tv_add_f32 v36, 1.0, v36\n\tv_add_f32 v37, 1.0, v37\n\tv_add_f32 v38, 1.0, v38\n\tv_add_f32 v39, 1.0, v39\n\t"
            "v_add_f32 v40, 1.0, v40\n\tv_add_f32 v41, 1.0, v41\n\tv_add_f32 v42, 1.0, v42\n\tv_add_f32 v43, 1.0, v43\n\tv_add_f32 v44, 1.0, v44\n\tv_add_f32 v45, 1.0, v45\n\t"
            "v_add_f32 v46, 1.0, v46\n\tv_add_f32 v47, 1.0, v47\n\tv_rcp_f32 v32, v32\n\tv_rcp_f32 v33, v33\n\tv_rcp_f32 v34, v34\n\tv_rcp_f32 v35, v35\n\t"
            "v_rcp_f32 v36, v36\n\tv_rcp_f32 v37, v37\n\tv_rcp_f32 v38, v38\n\tv_rcp_f32 v39, v39\n\tv_rcp_f32 v40, v40\n\tv_rcp_f32 v41, v41\n\t"
            "v_rcp_f32 v42, v42\n\tv_rcp_f32 v43, v43\n\tv_rcp_f32 v44, v44\n\tv_rcp_f32 v45, v45\n\tv_rcp_f32 v46, v46\n\tv_rcp_f32 v47, v47\n\t"
            "v_mul_f32 v32, v32, v96\n\tv_mul_f32 v33, v33, v97\n\tv_mul_f32 v34, v34, v98\n\tv_mul_f32 v35, v35, v99\n\tv_mul_f32 v36, v36, v100\n\tv_mul_f32 v37, v37, v101\n\t"
            "v_mul_f32 v38, v38, v102\n\tv_mul_f32 v39, v39, v103\n\tv_mul_f32 v40, v40, v104\n\tv_mul_f32 v41, v41, v105\n\tv_mul_f32 v42, v42, v106\n\tv_mul_f32 v43, v43, v107\n\t"
            "v_mul_f32 v44, v44, v108\n\tv_mul_f32 v45, v45, v109\n\tv_mul_f32 v46, v46, v110\n\tv_mul_f32 v47, v47, v111\n\ts_sleep 6\n\t"
            "buffer_load_dwordx4 v[176:179], v22, s[44:47], s4 offen offset:1024 sc1\n\tbuffer_load_dwordx4 v[180:183], v22, s[44:47], s5 offen offset:1024 sc1\n\tbuffer_load_dwordx4 v[184:187], v22, s[44:47], s6 offen offset:1024 sc1\n\tbuffer_load_dwordx4 v[188:191], v22, s[44:47], s7 offen offset:1024 sc1\n\tbuffer_load_dwordx4 v[192:195], v22, s[44:47], s8 offen offset:1024 sc1\n\tbuffer_load_dwordx4 v[196:199], v22, s[44:47], s9 offen offset:1024 sc1\n\t"
            "62:\nds_read_b128 v[16:19], v8 offset:48\n\tds_read_b64_tr_b16 v[128:129], v4 offset:40960\n\tds_read_b64_tr_b16 v[130:131], v4 offset:41472\n\tds_read_b64_tr_b16 v[132:133], v4 offset:40992\n\tds_read_b64_tr_b16 v[134:135], v4 offset:41504\n\t"
            "ds_read_b64_tr_b16 v[136:137], v5 offset:40960\n\tds_read_b64_tr_b16 v[138:139], v5 offset:41472\n\tds_read_b64_tr_b16 v[140:141], v5 offset:40992\n\tds_read_b64_tr_b16 v[142:143], v5 offset:41504\n\tds_read_b64_tr_b16 v[144:145], v4 offset:41984\n\tds_read_b64_tr_b16 v[146:147], v4 offset:42496\n\t"
            "ds_read_b64_tr_b16 v[148:149], v4 offset:42016\n\tds_read_b64_tr_b16 v[150:151], v4 offset:42528\n\tds_read_b64_tr_b16 v[152:153], v5 offset:41984\n\tds_read_b64_tr_b16 v[154:155], v5 offset:42496\n\tds_read_b64_tr_b16 v[156:157], v5 offset:42016\n\tds_read_b64_tr_b16 v[158:159], v5 offset:42528\n\t"
            "s_waitcnt lgkmcnt(15)\n\tv_min3_u32 v16, v16, v17, v18\n\tv_min_u32 v16, v16, v19\n\tv_cmp_eq_u32 vcc, 0, v16\n\ts_cbranch_vccnz 63f\n\t64:\n"
            "s_waitcnt vmcnt(4)\n\tv_max3_u32 v12, v176, v178, v180\n\tv_max_u32 v12, v12, v182\n\tv_cmp_eq_u32 vcc, -1, v12\n\ts_cbranch_vccnz 500f\n\t600:\n"
            "s_waitcnt lgkmcnt(8)\n\tv_mfma_f32_16x16x32_bf16 v[96:99], v[128:131], v[176:179], 0\n\tds_read_b64_tr_b16 v[160:161], v4 offset:43008\n\tds_read_b64_tr_b16 v[162:163], v4 offset:43520\n\tv_mfma_f32_16x16x32_bf16 v[100:103], v[132:135], v[176:179], 0\n\tds_read_b64_tr_b16 v[164:165], v4 offset:43040\n\t"
            "ds_read_b64_tr_b16 v[166:167], v4 offset:43552\n\tbuffer_load_dwordx4 v[200:203], v22, s[44:47], s10 offen offset:1024 sc1\n\tv_mfma_f32_16x16x32_bf16 v[104:107], v[136:139], v[176:179], 0\n\tds_read_b64_tr_b16 v[168:169], v5 offset:43008\n\tds_read_b64_tr_b16 v[170:171], v5 offset:43520\n\tv_mfma_f32_16x16x32_bf16 v[108:111], v[140:143], v[176:179], 0\n\t"
            "ds_read_b64_tr_b16 v[172:173], v5 offset:43040\n\tds_read_b64_tr_b16 v[174:175], v5 offset:43552\n\ts_waitcnt lgkmcnt(8)\n\tv_mfma_f32_16x16x32_bf16 v[96:99], v[144:147], v[180:183], v[96:99]\n\tds_read_b64_tr_b16 v[128:129], v4 offset:44032\n\tds_read_b64_tr_b16 v[130:131], v4 offset:44544\n\t"
            "v_mfma_f32_16x16x32_bf16 v[100:103], v[148:151], v[180:183], v[100:103]\n\tds_read_b64_tr_b16 v[132:133], v4 offset:44064\n\tds_read_b64_tr_b16 v[134:135], v4 offset:44576\n\tbuffer_load_dwordx4 v[204:207], v22, s[44:47], s11 offen offset:1024 sc1\n\tv_mfma_f32_16x16x32_bf16 v[104:107], v[152:155], v[180:183], v[104:107]\n\tds_read_b64_tr_b16 v[136:137], v5 offset:44032\n\t"
            "ds_read_b64_tr_b16 v[138:139], v5 offset:44544\n\ts_waitcnt vmcnt(4)\n\tv_max3_u32 v12, v184, v186, v188\n\tv_max_u32 v12, v12, v190\n\tv_cmp_eq_u32 vcc, -1, v12\n\tv_mfma_f32_16x16x32_bf16 v[108:111], v[156:159], v[180:183], v[108:111]\n\t"
            "ds_read_b64_tr_b16 v[140:141], v5 offset:44064\n\tds_read_b64_tr_b16 v[142:143], v5 offset:44576\n\ts_cbranch_vccnz 502f\n\t602:\ns_waitcnt lgkmcnt(8)\n\tv_mfma_f32_16x16x32_bf16 v[96:99], v[160:163], v[184:187], v[96:99]\n\t"
            "ds_read_b64_tr_b16 v[144:145], v4 offset:45056\n\tds_read_b64_tr_b16 v[146:147], v4 offset:45568\n\tv_mfma_f32_16x16x32_bf16 v[100:103], v[164:167], v[184:187], v[100:103]\n\tds_read_b64_tr_b16 v[148:149], v4 offset:45088\n\tds_read_b64_tr_b16 v[150:151], v4 offset:45600\n\tbuffer_load_dwordx4 v[208:211], v22, s[44:47], s12 offen offset:1024 sc1\n\t"
            "v_mfma_f32_16x16x32_bf16 v[104:107], v[168:171], v[184:187], v[104:107]\n\tds_read_b64_tr_b16 v[152:153], v5 offset:45056\n\tds_read_b64_tr_b16 v[154:155], v5 offset:45568\n\tds_read_b64 v[14:15], v8 offset:40\n\tv_mfma_f32_16x16x32_bf16 v[108:111], v[172:175], v[184:187], v[108:111]\n\tds_read_b64_tr_b16 v[156:157], v5 offset:45088\n\t"
            "ds_read_b64_tr_b16 v[158:159], v5 offset:45600\n\ts_waitcnt lgkmcnt(9)\n\tv_mfma_f32_16x16x32_bf16 v[96:99], v[128:131], v[188:191], v[96:99]\n\tds_read_b64_tr_b16 v[160:161], v4 offset:46080\n\tds_read_b64_tr_b16 v[162:163], v4 offset:46592\n\tv_mfma_f32_16x16x32_bf16 v[100:103], v[132:135], v[188:191], v[100:103]\n\t"
            "ds_read_b64_tr_b16 v[164:165], v4 offset:46112\n\tds_read_b64_tr_b16 v[166:167], v4 offset:46624\n\tbuffer_load_dwordx4 v[212:215], v22, s[44:47], s13 offen offset:1024 sc1\n\tv_mfma_f32_16x16x32_bf16 v[104:107], v[136:139], v[188:191], v[104:107]\n\tds_read_b64_tr_b16 v[168:169], v5 offset:46080\n\tds_read_b64_tr_b16 v[170:171], v5 offset:46592\n\t"
            "s_waitcnt vmcnt(4)\n\tv_max3_u32 v12, v192, v194, v196\n\tv_max_u32 v12, v12, v198\n\tv_cmp_eq_u32 vcc, -1, v12\n\tv_mfma_f32_16x16x32_bf16 v[108:111], v[140:143], v[188:191], v[108:111]\n\tds_read_b64_tr_b16 v[172:173], v5 offset:46112\n\t"
            "ds_read_b64_tr_b16 v[174:175], v5 offset:46624\n\ts_cbranch_vccnz 504f\n\t604:\ns_waitcnt lgkmcnt(8)\n\tv_mfma_f32_16x16x32_bf16 v[96:99], v[144:147], v[192:195], v[96:99]\n\tv_min_u32 v14, v14, v15\n\t"
            "v_cmp_ge_u32 vcc, s71, v14\n\ts_cbranch_vccnz 5f\n\t6:\nds_read_b64_tr_b16 v[128:129], v4 offset:47104\n\tds_read_b64_tr_b16 v[130:131], v4 offset:47616\n\tv_mfma_f32_16x16x32_bf16 v[100:103], v[148:151], v[192:195], v[100:103]\n\t"
            "ds_read_b64_tr_b16 v[132:133], v4 offset:47136\n\tds_read_b64_tr_b16 v[134:135], v4 offset:47648\n\tbuffer_load_dwordx4 v[216:219], v11, s[44:47], s4 offen sc1\n\tv_mfma_f32_16x16x32_bf16 v[104:107], v[152:155], v[192:195], v[104:107]\n\tds_read_b64_tr_b16 v[136:137], v5 offset:47104\n\tds_read_b64_tr_b16 v[138:139], v5 offset:47616\n\t"
            "v_mfma_f32_16x16x32_bf16 v[108:111], v[156:159], v[192:195], v[108:111]\n\tds_read_b64_tr_b16 v[140:141], v5 offset:47136\n\tds_read_b64_tr_b16 v[142:143], v5 offset:47648\n\ts_waitcnt lgkmcnt(8)\n\tv_mfma_f32_16x16x32_bf16 v[96:99], v[160:163], v[196:199], v[96:99]\n\tds_read_b64_tr_b16 v[144:145], v4 offset:48128\n\t"
            "ds_read_b64_tr_b16 v[146:147], v4 offset:48640\n\tv_mfma_f32_16x16x32_bf16 v[100:103], v[164:167], v[196:199], v[100:103]\n\tds_read_b64_tr_b16 v[148:149], v4 offset:48160\n\tds_read_b64_tr_b16 v[150:151], v4 offset:48672\n\tbuffer_load_dwordx4 v[220:223], v11, s[44:47], s5 offen sc1\n\tv_mfma_f32_16x16x32_bf16 v[104:107], v[168:171], v[196:199], v[104:107]\n\t"
            "ds_read_b64_tr_b16 v[152:153], v5 offset:48128\n\tds_read_b64_tr_b16 v[154:155], v5 offset:48640\n\ts_waitcnt vmcnt(4)\n\tv_max3_u32 v12, v200, v202, v204\n\tv_max_u32 v12, v12, v206\n\tv_cmp_eq_u32 vcc, -1, v12\n\t"
            "v_mfma_f32_16x16x32_bf16 v[108:111], v[172:175], v[196:199], v[108:111]\n\tds_read_b64_tr_b16 v[156:157], v5 offset:48160\n\tds_read_b64_tr_b16 v[158:159], v5 offset:48672\n\ts_cbranch_vccnz 506f\n\t606:\ns_waitcnt lgkmcnt(8)\n\t"
            "v_mfma_f32_16x16x32_bf16 v[96:99], v[128:131], v[200:203], v[96:99]\n\tds_read_b64_tr_b16 v[160:161], v4 offset:49152\n\tds_read_b64_tr_b16 v[162:163], v4 offset:49664\n\tv_mfma_f32_16x16x32_bf16 v[100:103], v[132:135], v[200:203], v[100:103]\n\tds_read_b64_tr_b16 v[164:165], v4 offset:49184\n\tds_read_b64_tr_b16 v[166:167], v4 offset:49696\n\t"
            "buffer_load_dwordx4 v[224:227], v11, s[44:47], s6 offen sc1\n\tv_mfma_f32_16x16x32_bf16 v[104:107], v[136:139], v[200:203], v[104:107]\n\tds_read_b64_tr_b16 v[168:169], v5 offset:49152\n\tds_read_b64_tr_b16 v[170:171], v5 offset:49664\n\tv_mfma_f32_16x16x32_bf16 v[108:111], v[140:143], v[200:203], v[108:111]\n\tds_read_b64_tr_b16 v[172:173], v5 offset:49184\n\t"
            "ds_read_b64_tr_b16 v[174:175], v5 offset:49696\n\ts_waitcnt lgkmcnt(8)\n\tv_mfma_f32_16x16x32_bf16 v[96:99], v[144:147], v[204:207], v[96:99]\n\tds_read_b64_tr_b16 v[128:129], v4 offset:50176\n\tds_read_b64_tr_b16 v[130:131], v4 offset:50688\n\tv_mfma_f32_16x16x32_bf16 v[100:103], v[148:151], v[204:207], v[100:103]\n\t"
            "ds_read_b64_tr_b16 v[132:133], v4 offset:50208\n\tds_read_b64_tr_b16 v[134:135], v4 offset:50720\n\tbuffer_load_dwordx4 v[228:231], v11, s[44:47], s7 offen sc1\n\tv_mfma_f32_16x16x32_bf16 v[104:107], v[152:155], v[204:207], v[104:107]\n\tds_read_b64_tr_b16 v[136:137], v5 offset:50176\n\tds_read_b64_tr_b16 v[138:139], v5 offset:50688\n\t"
            "s_waitcnt vmcnt(4)\n\tv_max3_u32 v12, v208, v210, v212\n\tv_max_u32 v12, v12, v214\n\tv_cmp_eq_u32 vcc, -1, v12\n\tv_mfma_f32_16x16x32_bf16 v[108:111], v[156:159], v[204:207], v[108:111]\n\tds_read_b64_tr_b16 v[140:141], v5 offset:50208\n\t"
            "ds_read_b64_tr_b16 v[142:143], v5 offset:50720\n\ts_cbranch_vccnz 508f\n\t608:\ns_waitcnt lgkmcnt(8)\n\tv_mfma_f32_16x16x32_bf16 v[96:99], v[160:163], v[208:211], v[96:99]\n\tds_read_b64_tr_b16 v[144:145], v4 offset:0\n\t"
            "ds_read_b64_tr_b16 v[146:147], v4 offset:512\n\tv_mfma_f32_16x16x32_bf16 v[100:103], v[164:167], v[208:211], v[100:103]\n\tds_read_b64_tr_b16 v[148:149], v4 offset:32\n\tds_read_b64_tr_b16 v[150:151], v4 offset:544\n\tbuffer_load_dwordx4 v[232:235], v11, s[44:47], s8 offen sc1\n\tv_mfma_f32_16x16x32_bf16 v[104:107], v[168:171], v[208:211], v[104:107]\n\t"
            "ds_read_b64_tr_b16 v[152:153], v5 offset:0\n\tds_read_b64_tr_b16 v[154:155], v5 offset:512\n\tv_mfma_f32_16x16x32_bf16 v[108:111], v[172:175], v[208:211], v[108:111]\n\tds_read_b64_tr_b16 v[156:157], v5 offset:32\n\tds_read_b64_tr_b16 v[158:159], v5 offset:544\n\ts_waitcnt lgkmcnt(8)\n\t"
            "v_mfma_f32_16x16x32_bf16 v[96:99], v[128:131], v[212:215], v[96:99]\n\tds_read_b64_tr_b16 v[160:161], v4 offset:1024\n\tds_read_b64_tr_b16 v[162:163], v4 offset:1536\n\tv_mfma_f32_16x16x32_bf16 v[100:103], v[132:135], v[212:215], v[100:103]\n\tds_read_b64_tr_b16 v[164:165], v4 offset:1056\n\tds_read_b64_tr_b16 v[166:167], v4 offset:1568\n\t"
            "buffer_load_dwordx4 v[236:239], v11, s[44:47], s9 offen sc1\n\tv_mfma_f32_16x16x32_bf16 v[104:107], v[136:139], v[212:215], v[104:107]\n\tds_read_b64_tr_b16 v[168:169], v5 offset:1024\n\tds_read_b64_tr_b16 v[170:171], v5 offset:1536\n\ts_waitcnt vmcnt(4)\n\tv_max3_u32 v12, v216, v218, v220\n\t"
            "v_max_u32 v12, v12, v222\n\tv_cmp_eq_u32 vcc, -1, v12\n\tv_mfma_f32_16x16x32_bf16 v[108:111], v[140:143], v[212:215], v[108:111]\n\tds_read_b64_tr_b16 v[172:173], v5 offset:1056\n\tds_read_b64_tr_b16 v[174:175], v5 offset:1568\n\ts_cbranch_vccnz 510f\n\t"
            "610:\ns_waitcnt lgkmcnt(8)\n\tv_mfma_f32_16x16x32_bf16 v[96:99], v[144:147], v[216:219], v[96:99]\n\tds_read_b64_tr_b16 v[128:129], v4 offset:2048\n\tds_read_b64_tr_b16 v[130:131], v4 offset:2560\n\tv_mfma_f32_16x16x32_bf16 v[100:103], v[148:151], v[216:219], v[100:103]\n\t"
            "ds_read_b64_tr_b16 v[132:133], v4 offset:2080\n\tds_read_b64_tr_b16 v[134:135], v4 offset:2592\n\tbuffer_load_dwordx4 v[176:179], v11, s[44:47], s10 offen sc1\n\tv_mfma_f32_16x16x32_bf16 v[104:107], v[152:155], v[216:219], v[104:107]\n\tds_read_b64_tr_b16 v[136:137], v5 offset:2048\n\tds_read_b64_tr_b16 v[138:139], v5 offset:2560\n\t"
            "v_mfma_f32_16x16x32_bf16 v[108:111], v[156:159], v[216:219], v[108:111]\n\tds_read_b64_tr_b16 v[140:141], v5 offset:2080\n\tds_read_b64_tr_b16 v[142:143], v5 offset:2592\n\ts_waitcnt lgkmcnt(8)\n\tv_mfma_f32_16x16x32_bf16 v[96:99], v[160:163], v[220:223], v[96:99]\n\tds_read_b64_tr_b16 v[144:145], v4 offset:3072\n\t"
            "ds_read_b64_tr_b16 v[146:147], v4 offset:3584\n\tv_mfma_f32_16x16x32_bf16 v[100:103], v[164:167], v[220:223], v[100:103]\n\tds_read_b64_tr_b16 v[148:149], v4 offset:3104\n\tds_read_b64_tr_b16 v[150:151], v4 offset:3616\n\tbuffer_load_dwordx4 v[180:183], v11, s[44:47], s11 offen sc1\n\tv_mfma_f32_16x16x32_bf16 v[104:107], v[168:171], v[220:223], v[104:107]\n\t"
            "ds_read_b64_tr_b16 v[152:153], v5 offset:3072\n\tds_read_b64_tr_b16 v[154:155], v5 offset:3584\n\ts_waitcnt vmcnt(4)\n\tv_max3_u32 v12, v224, v226, v228\n\tv_max_u32 v12, v12, v230\n\tv_cmp_eq_u32 vcc, -1, v12\n\t"
            "v_mfma_f32_16x16x32_bf16 v[108:111], v[172:175], v[220:223], v[108:111]\n\tds_read_b64_tr_b16 v[156:157], v5 offset:3104\n\tds_read_b64_tr_b16 v[158:159], v5 offset:3616\n\ts_cbranch_vccnz 512f\n\t612:\ns_waitcnt lgkmcnt(8)\n\t"
            "v_mfma_f32_16x16x32_bf16 v[96:99], v[128:131], v[224:227], v[96:99]\n\tds_read_b64_tr_b16 v[160:161], v4 offset:4096\n\tds_read_b64_tr_b16 v[162:163], v4 offset:4608\n\tv_mfma_f32_16x16x32_bf16 v[100:103], v[132:135], v[224:227], v[100:103]\n\tds_read_b64_tr_b16 v[164:165], v4 offset:4128\n\tds_read_b64_tr_b16 v[166:167], v4 offset:4640\n\t"
            "buffer_load_dwordx4 v[184:187], v11, s[44:47], s12 offen sc1\n\tv_mfma_f32_16x16x32_bf16 v[104:107], v[136:139], v[224:227], v[104:107]\n\tds_read_b64_tr_b16 v[168:169], v5 offset:4096\n\tds_read_b64_tr_b16 v[170:171], v5 offset:4608\n\tv_mfma_f32_16x16x32_bf16 v[108:111], v[140:143], v[224:227], v[108:111]\n\tds_read_b64_tr_b16 v[172:173], v5 offset:4128\n\t"
            "ds_read_b64_tr_b16 v[174:175], v5 offset:4640\n\ts_waitcnt lgkmcnt(8)\n\tv_mfma_f32_16x16x32_bf16 v[96:99], v[144:147], v[228:231], v[96:99]\n\tds_read_b64_tr_b16 v[128:129], v4 offset:5120\n\tds_read_b64_tr_b16 v[130:131], v4 offset:5632\n\tv_mfma_f32_16x16x32_bf16 v[100:103], v[148:151], v[228:231], v[100:103]\n\t"
            "ds_read_b64_tr_b16 v[132:133], v4 offset:5152\n\tds_read_b64_tr_b16 v[134:135], v4 offset:5664\n\tbuffer_load_dwordx4 v[188:191], v11, s[44:47], s13 offen sc1\n\tv_mfma_f32_16x16x32_bf16 v[104:107], v[152:155], v[228:231], v[104:107]\n\tds_read_b64_tr_b16 v[136:137], v5 offset:5120\n\tds_read_b64_tr_b16 v[138:139], v5 offset:5632\n\t"
            "s_waitcnt vmcnt(4)\n\tv_max3_u32 v12, v232, v234, v236\n\tv_max_u32 v12, v12, v238\n\tv_cmp_eq_u32 vcc, -1, v12\n\tv_mfma_f32_16x16x32_bf16 v[108:111], v[156:159], v[228:231], v[108:111]\n\tds_read_b64_tr_b16 v[140:141], v5 offset:5152\n\t"
            "ds_read_b64_tr_b16 v[142:143], v5 offset:5664\n\ts_cbranch_vccnz 514f\n\t614:\ns_waitcnt lgkmcnt(8)\n\tv_mfma_f32_16x16x32_bf16 v[96:99], v[160:163], v[232:235], v[96:99]\n\tds_read_b64_tr_b16 v[144:145], v4 offset:6144\n\t"
            "ds_read_b64_tr_b16 v[146:147], v4 offset:6656\n\tv_mfma_f32_16x16x32_bf16 v[100:103], v[164:167], v[232:235], v[100:103]\n\tds_read_b64_tr_b16 v[148:149], v4 offset:6176\n\tds_read_b64_tr_b16 v[150:151], v4 offset:6688\n\tv_mfma_f32_16x16x32_bf16 v[104:107], v[168:171], v[232:235], v[104:107]\n\tds_read_b64_tr_b16 v[152:153], v5 offset:6144\n\t"
            "ds_read_b64_tr_b16 v[154:155], v5 offset:6656\n\tv_mfma_f32_16x16x32_bf16 v[108:111], v[172:175], v[232:235], v[108:111]\n\tds_read_b64_tr_b16 v[156:157], v5 offset:6176\n\tds_read_b64_tr_b16 v[158:159], v5 offset:6688\n\ts_waitcnt lgkmcnt(8)\n\tv_mfma_f32_16x16x32_bf16 v[96:99], v[128:131], v[236:239], v[96:99]\n\t"
            "ds_read_b64_tr_b16 v[160:161], v4 offset:7168\n\tds_read_b64_tr_b16 v[162:163], v4 offset:7680\n\tv_mfma_f32_16x16x32_bf16 v[100:103], v[132:135], v[236:239], v[100:103]\n\tds_read_b64_tr_b16 v[164:165], v4 offset:7200\n\tds_read_b64_tr_b16 v[166:167], v4 offset:7712\n\tv_mfma_f32_16x16x32_bf16 v[104:107], v[136:139], v[236:239], v[104:107]\n\t"
            "ds_read_b64_tr_b16 v[168:169], v5 offset:7168\n\tds_read_b64_tr_b16 v[170:171], v5 offset:7680\n\ts_waitcnt vmcnt(2)\n\tv_max3_u32 v12, v176, v178, v180\n\tv_max_u32 v12, v12, v182\n\tv_cmp_eq_u32 vcc, -1, v12\n\t"
            "v_mfma_f32_16x16x32_bf16 v[108:111], v[140:143], v[236:239], v[108:111]\n\tds_read_b64_tr_b16 v[172:173], v5 offset:7200\n\tds_read_b64_tr_b16 v[174:175], v5 offset:7712\n\ts_cbranch_vccnz 516f\n\t616:\ns_waitcnt lgkmcnt(8)\n\t"
            "v_mfma_f32_16x16x32_bf16 v[96:99], v[144:147], v[176:179], v[96:99]\n\tds_read_b64_tr_b16 v[128:129], v4 offset:8192\n\tds_read_b64_tr_b16 v[130:131], v4 offset:8704\n\tv_mfma_f32_16x16x32_bf16 v[100:103], v[148:151], v[176:179], v[100:103]\n\tds_read_b64_tr_b16 v[132:133], v4 offset:8224\n\tds_read_b64_tr_b16 v[134:135], v4 offset:8736\n\t"
            "v_mfma_f32_16x16x32_bf16 v[104:107], v[152:155], v[176:179], v[104:107]\n\tds_read_b64_tr_b16 v[136:137], v5 offset:8192\n\tds_read_b64_tr_b16 v[138:139], v5 offset:8704\n\tv_mfma_f32_16x16x32_bf16 v[108:111], v[156:159], v[176:179], v[108:111]\n\tds_read_b64_tr_b16 v[140:141], v5 offset:8224\n\tds_read_b64_tr_b16 v[142:143], v5 offset:8736\n\t"
            "s_waitcnt lgkmcnt(8)\n\tv_mfma_f32_16x16x32_bf16 v[96:99], v[160:163], v[180:183], v[96:99]\n\tds_read_b64_tr_b16 v[144:145], v4 offset:9216\n\tds_read_b64_tr_b16 v[146:147], v4 offset:9728\n\tv_mfma_f32_16x16x32_bf16 v[100:103], v[164:167], v[180:183], v[100:103]\n\tds_read_b64_tr_b16 v[148:149], v4 offset:9248\n\t"
            "ds_read_b64_tr_b16 v[150:151], v4 offset:9760\n\tv_mfma_f32_16x16x32_bf16 v[104:107], v[168:171], v[180:183], v[104:107]\n\tds_read_b64_tr_b16 v[152:153], v5 offset:9216\n\tds_read_b64_tr_b16 v[154:155], v5 offset:9728\n\ts_waitcnt vmcnt(0)\n\tv_max3_u32 v12, v184, v186, v188\n\t"
            "v_max_u32 v12, v12, v190\n\tv_cmp_eq_u32 vcc, -1, v12\n\tv_mfma_f32_16x16x32_bf16 v[108:111], v[172:175], v[180:183], v[108:111]\n\tds_read_b64_tr_b16 v[156:157], v5 offset:9248\n\tds_read_b64_tr_b16 v[158:159], v5 offset:9760\n\ts_cbranch_vccnz 518f\n\t"
            "618:\nds_read_b32 v16, v8 offset:64\n\tds_read_b32 v17, v9 offset:1344\n\ts_waitcnt lgkmcnt(10)\n\tv_mfma_f32_16x16x32_bf16 v[96:99], v[128:131], v[184:187], v[96:99]\n\tv_mfma_f32_16x16x32_bf16 v[100:103], v[132:135], v[184:187], v[100:103]\n\t"
            "v_mfma_f32_16x16x32_bf16 v[104:107], v[136:139], v[184:187], v[104:107]\n\tv_mfma_f32_16x16x32_bf16 v[108:111], v[140:143], v[184:187], v[108:111]\n\ts_waitcnt lgkmcnt(2)\n\tv_mfma_f32_16x16x32_bf16 v[96:99], v[144:147], v[188:191], v[96:99]\n\ts_waitcnt lgkmcnt(0)\n\tv_cmp_ne_u32 s[74:75], 0, v17\n\t"
            "v_cmp_eq_u32 vcc, 0, v16\n\tv_mfma_f32_16x16x32_bf16 v[100:103], v[148:151], v[188:191], v[100:103]\n\tv_mfma_f32_16x16x32_bf16 v[104:107], v[152:155], v[188:191], v[104:107]\n\ts_cbranch_vccnz 7f\n\t8:\nv_mfma_f32_16x16x32_bf16 v[108:111], v[156:159], v[188:191], v[108:111]\n\t"
            "v_add_u32 v12, v17, v23\n\tv_cndmask_b32 v3, v3, 2.0, s[74:75]\n\ts_mov_b64 exec, s[74:75]\n\ts_cbranch_execz 1f\n\tds_write_b128 v12, v[96:99] offset:0\n\tds_write_b128 v12, v[100:103] offset:64\n\t"
            "ds_write_b128 v12, v[104:107] offset:128\n\ts_nop 0\n\tds_write_b128 v12, v[108:111] offset:192\n\tds_write_b32 v17, v17 offset:256\n\t1:\ns_mov_b64 exec, -1\n\t"
            "v_fmac_f32 v64, v96, v32\n\tv_fmac_f32 v65, v97, v33\n\tv_fmac_f32 v66, v98, v34\n\tv_fmac_f32 v67, v99, v35\n\tv_fmac_f32 v68, v100, v36\n\tv_fmac_f32 v69, v101, v37\n\t"
            "v_fmac_f32 v70, v102, v38\n\tv_fmac_f32 v71, v103, v39\n\tv_fmac_f32 v72, v104, v40\n\tv_fmac_f32 v73, v105, v41\n\tv_fmac_f32 v74, v106, v42\n\tv_fmac_f32 v75, v107, v43\n\t"
            "v_fmac_f32 v76, v108, v44\n\tv_fmac_f32 v77, v109, v45\n\tv_fmac_f32 v78, v110, v46\n\tv_fmac_f32 v79, v111, v47\n\tv_cvt_pk_bf16_f32 v240, v64, v65\n\tv_cvt_pk_bf16_f32 v242, v68, v69\n\t"
            "v_cvt_pk_bf16_f32 v241, v66, v67\n\tv_cvt_pk_bf16_f32 v243, v70, v71\n\tv_cvt_pk_bf16_f32 v244, v72, v73\n\tv_cvt_pk_bf16_f32 v246, v76, v77\n\tv_cvt_pk_bf16_f32 v245, v74, v75\n\tv_cvt_pk_bf16_f32 v247, v78, v79\n\t"
            "v_permlane16_swap_b32 v240, v242\n\tv_permlane16_swap_b32 v241, v243\n\tv_permlane16_swap_b32 v244, v246\n\tv_permlane16_swap_b32 v245, v247\n\tbuffer_store_dwordx4 v[240:243], v3, s[52:55], 0 offen offset:0 sc1\n\tbuffer_store_dwordx4 v[244:247], v3, s[52:55], 0 offen offset:64 sc1\n\t"
            "9:\n"
            "s_endpgm\n\t7:\nds_read_b32 v16, v8 offset:64\n\tds_read_b32 v17, v9 offset:1344\n\ts_waitcnt lgkmcnt(0)\n\tv_cmp_ne_u32 s[74:75], 0, v17\n\t"
            "v_cmp_eq_u32 vcc, 0, v16\n\ts_cbranch_vccz 8b\n\ts_sleep 1\n\ts_sub_u32 s68, s68, 1\n\ts_cbranch_scc0 7b\n\ts_branch 9b\n\t"
            "5:\ns_sleep 1\n\tds_read_b64 v[14:15], v8 offset:40\n\ts_waitcnt lgkmcnt(0)\n\tv_min_u32 v14, v14, v15\n\tv_cmp_ge_u32 vcc, s71, v14\n\t"
            "s_cbranch_vccz 61f\n\ts_sub_u32 s68, s68, 1\n\ts_cbranch_scc0 5b\n\ts_branch 9b\n\t61:\ns_sleep 8\n\t"
            "s_branch 6b\n\t63:\ns_sub_u32 s68, s68, 1\n\ts_cbranch_scc0 62b\n\ts_branch 9b\n\t500:\n"
            "s_waitcnt vmcnt(0)\n\tbuffer_load_dwordx4 v[176:179], v22, s[44:47], s4 offen offset:1024 sc1\n\tbuffer_load_dwordx4 v[180:183], v22, s[44:47], s5 offen offset:1024 sc1\n\tbuffer_load_dwordx4 v[184:187], v22, s[44:47], s6 offen offset:1024 sc1\n\tbuffer_load_dwordx4 v[188:191], v22, s[44:47], s7 offen offset:1024 sc1\n\tbuffer_load_dwordx4 v[192:195], v22, s[44:47], s8 offen offset:1024 sc1\n\t"
            "buffer_load_dwordx4 v[196:199], v22, s[44:47], s9 offen offset:1024 sc1\n\t"
            "s_waitcnt vmcnt(0)\n\tv_max3_u32 v12, v176, v178, v180\n\tv_max_u32 v12, v12, v182\n\tv_cmp_eq_u32 vcc, -1, v12\n\ts_cbranch_vccz 600b\n\ts_sub_u32 s68, s68, 1\n\t"
            "s_cbranch_scc0 500b\n\ts_branch 9b\n\t502:\ns_waitcnt vmcnt(0)\n\tbuffer_load_dwordx4 v[184:187], v22, s[44:47], s6 offen offset:1024 sc1\n\tbuffer_load_dwordx4 v[188:191], v22, s[44:47], s7 offen offset:1024 sc1\n\t"
            "buffer_load_dwordx4 v[192:195], v22, s[44:47], s8 offen offset:1024 sc1\n\tbuffer_load_dwordx4 v[196:199], v22, s[44:47], s9 offen offset:1024 sc1\n\tbuffer_load_dwordx4 v[200:203], v22, s[44:47], s10 offen offset:1024 sc1\n\tbuffer_load_dwordx4 v[204:207], v22, s[44:47], s11 offen offset:1024 sc1\n\t"
            "s_waitcnt vmcnt(0)\n\tv_max3_u32 v12, v184, v186, v188\n\tv_max_u32 v12, v12, v190\n\tv_cmp_eq_u32 vcc, -1, v12\n\ts_cbranch_vccz 602b\n\ts_sub_u32 s68, s68, 1\n\t"
            "s_cbranch_scc0 502b\n\ts_branch 9b\n\t504:\ns_waitcnt vmcnt(0)\n\tbuffer_load_dwordx4 v[192:195], v22, s[44:47], s8 offen offset:1024 sc1\n\tbuffer_load_dwordx4 v[196:199], v22, s[44:47], s9 offen offset:1024 sc1\n\t"
            "buffer_load_dwordx4 v[200:203], v22, s[44:47], s10 offen offset:1024 sc1\n\tbuffer_load_dwordx4 v[204:207], v22, s[44:47], s11 offen offset:1024 sc1\n\tbuffer_load_dwordx4 v[208:211], v22, s[44:47], s12 offen offset:1024 sc1\n\tbuffer_load_dwordx4 v[212:215], v22, s[44:47], s13 offen offset:1024 sc1\n\t"
            "s_waitcnt vmcnt(0)\n\tv_max3_u32 v12, v192, v194, v196\n\tv_max_u32 v12, v12, v198\n\tv_cmp_eq_u32 vcc, -1, v12\n\ts_cbranch_vccz 604b\n\ts_sub_u32 s68, s68, 1\n\t"
            "s_cbranch_scc0 504b\n\ts_branch 9b\n\t506:\ns_waitcnt vmcnt(0)\n\tbuffer_load_dwordx4 v[200:203], v22, s[44:47], s10 offen offset:1024 sc1\n\tbuffer_load_dwordx4 v[204:207], v22, s[44:47], s11 offen offset:1024 sc1\n\t"
            "buffer_load_dwordx4 v[208:211], v22, s[44:47], s12 offen offset:1024 sc1\n\tbuffer_load_dwordx4 v[212:215], v22, s[44:47], s13 offen offset:1024 sc1\n\tbuffer_load_dwordx4 v[216:219], v11, s[44:47], s4 offen sc1\n\tbuffer_load_dwordx4 v[220:223], v11, s[44:47], s5 offen sc1\n\t"
            "s_waitcnt vmcnt(0)\n\tv_max3_u32 v12, v200, v202, v204\n\tv_max_u32 v12, v12, v206\n\tv_cmp_eq_u32 vcc, -1, v12\n\ts_cbranch_vccz 606b\n\ts_sub_u32 s68, s68, 1\n\t"
            "s_cbranch_scc0 506b\n\ts_branch 9b\n\t508:\ns_waitcnt vmcnt(0)\n\tbuffer_load_dwordx4 v[208:211], v22, s[44:47], s12 offen offset:1024 sc1\n\tbuffer_load_dwordx4 v[212:215], v22, s[44:47], s13 offen offset:1024 sc1\n\t"
            "buffer_load_dwordx4 v[216:219], v11, s[44:47], s4 offen sc1\n\tbuffer_load_dwordx4 v[220:223], v11, s[44:47], s5 offen sc1\n\tbuffer_load_dwordx4 v[224:227], v11, s[44:47], s6 offen sc1\n\tbuffer_load_dwordx4 v[228:231], v11, s[44:47], s7 offen sc1\n\t"
            "s_waitcnt vmcnt(0)\n\tv_max3_u32 v12, v208, v210, v212\n\tv_max_u32 v12, v12, v214\n\tv_cmp_eq_u32 vcc, -1, v12\n\ts_cbranch_vccz 608b\n\ts_sub_u32 s68, s68, 1\n\t"
            "s_cbranch_scc0 508b\n\ts_branch 9b\n\t510:\ns_waitcnt vmcnt(0)\n\tbuffer_load_dwordx4 v[216:219], v11, s[44:47], s4 offen sc1\n\tbuffer_load_dwordx4 v[220:223], v11, s[44:47], s5 offen sc1\n\t"
            "buffer_load_dwordx4 v[224:227], v11, s[44:47], s6 offen sc1\n\tbuffer_load_dwordx4 v[228:231], v11, s[44:47], s7 offen sc1\n\tbuffer_load_dwordx4 v[232:235], v11, s[44:47], s8 offen sc1\n\tbuffer_load_dwordx4 v[236:239], v11, s[44:47], s9 offen sc1\n\t"
            "s_waitcnt vmcnt(0)\n\tv_max3_u32 v12, v216, v218, v220\n\tv_max_u32 v12, v12, v222\n\tv_cmp_eq_u32 vcc, -1, v12\n\ts_cbranch_vccz 610b\n\ts_sub_u32 s68, s68, 1\n\t"
            "s_cbranch_scc0 510b\n\ts_branch 9b\n\t512:\ns_waitcnt vmcnt(0)\n\tbuffer_load_dwordx4 v[224:227], v11, s[44:47], s6 offen sc1\n\tbuffer_load_dwordx4 v[228:231], v11, s[44:47], s7 offen sc1\n\t"
            "buffer_load_dwordx4 v[232:235], v11, s[44:47], s8 offen sc1\n\tbuffer_load_dwordx4 v[236:239], v11, s[44:47], s9 offen sc1\n\tbuffer_load_dwordx4 v[176:179], v11, s[44:47], s10 offen sc1\n\tbuffer_load_dwordx4 v[180:183], v11, s[44:47], s11 offen sc1\n\t"
            "s_waitcnt vmcnt(0)\n\tv_max3_u32 v12, v224, v226, v228\n\tv_max_u32 v12, v12, v230\n\tv_cmp_eq_u32 vcc, -1, v12\n\ts_cbranch_vccz 612b\n\ts_sub_u32 s68, s68, 1\n\t"
            "s_cbranch_scc0 512b\n\ts_branch 9b\n\t514:\ns_waitcnt vmcnt(0)\n\tbuffer_load_dwordx4 v[232:235], v11, s[44:47], s8 offen sc1\n\tbuffer_load_dwordx4 v[236:239], v11, s[44:47], s9 offen sc1\n\t"
            "buffer_load_dwordx4 v[176:179], v11, s[44:47], s10 offen sc1\n\tbuffer_load_dwordx4 v[180:183], v11, s[44:47], s11 offen sc1\n\tbuffer_load_dwordx4 v[184:187], v11, s[44:47], s12 offen sc1\n\tbuffer_load_dwordx4 v[188:191], v11, s[44:47], s13 offen sc1\n\t"
            "s_waitcnt vmcnt(0)\n\tv_max3_u32 v12, v232, v234, v236\n\tv_max_u32 v12, v12, v238\n\tv_cmp_eq_u32 vcc, -1, v12\n\ts_cbranch_vccz 614b\n\ts_sub_u32 s68, s68, 1\n\t"
            "s_cbranch_scc0 514b\n\ts_branch 9b\n\t516:\ns_waitcnt vmcnt(0)\n\tbuffer_load_dwordx4 v[176:179], v11, s[44:47], s10 offen sc1\n\tbuffer_load_dwordx4 v[180:183], v11, s[44:47], s11 offen sc1\n\t"
            "buffer_load_dwordx4 v[184:187], v11, s[44:47], s12 offen sc1\n\tbuffer_load_dwordx4 v[188:191], v11, s[44:47], s13 offen sc1\n\t"
            "s_waitcnt vmcnt(0)\n\tv_max3_u32 v12, v176, v178, v180\n\tv_max_u32 v12, v12, v182\n\tv_cmp_eq_u32 vcc, -1, v12\n\ts_cbranch_vccz 616b\n\ts_sub_u32 s68, s68, 1\n\t"
            "s_cbranch_scc0 516b\n\ts_branch 9b\n\t518:\ns_waitcnt vmcnt(0)\n\tbuffer_load_dwordx4 v[184:187], v11, s[44:47], s12 offen sc1\n\tbuffer_load_dwordx4 v[188:191], v11, s[44:47], s13 offen sc1\n\t"
            "s_waitcnt vmcnt(0)\n\tv_max3_u32 v12, v184, v186, v188\n\tv_max_u32 v12, v12, v190\n\tv_cmp_eq_u32 vcc, -1, v12\n\ts_cbranch_vccz 618b\n\ts_sub_u32 s68, s68, 1\n\t"
            "s_cbranch_scc0 518b\n\ts_branch 9b\n\t.subsection 1\n\t.Lce:\n.subsection 0\n\t.Lout:\n"
            "s_waitcnt lgkmcnt(0)\n\t"
            : "=&{s67}"(tag), "=&{v[52:83]}"(sa), "=&{v[84:115]}"(sb), "=&{v[116:123]}"(sc) : [t] "v"(tid), [b] "s"(bid), [k] "s"(ka)
            : "memory", "scc", "vcc", "s4", "s5", "s6", "s7", "s8", "s9", "s10", "s11", "s12", "s13", "s14", "s15", "s16", "s17", "s18", "s19", "s20", "s21", "s22", "s23", "s24", "s25", "s26", "s27", "s28", "s29", "s30", "s31", "s33", "s34", "s35", "s36", "s37", "s38", "s39", "s40", "s41", "s42", "s43", "s44", "s45", "s46", "s47", "s48", "s49", "s50", "s51", "s52", "s53", "s54", "s55", "s56", "s57", "s58", "s59", "s60", "s61", "s62", "s63", "s64", "s65", "s66", "s68", "s69", "s70", "s71", "s72", "s73", "s74", "s75", "s76", "s77", "s78", "s79", "s80", "s81", "s82", "s83", "v2", "v3", "v4", "v5", "v6", "v7", "v8", "v9", "v10", "v11", "v12", "v13", "v14", "v15", "v16", "v17", "v18", "v19", "v20", "v21", "v22", "v23", "v24", "v25", "v26", "v27", "v28", "v29", "v30", "v31", "v32", "v33", "v34", "v35", "v36", "v37", "v38", "v39", "v40", "v41", "v42", "v43", "v44", "v45", "v46", "v47", "v48", "v49", "v50", "v51", "v124", "v125", "v126", "v127", "v128", "v129", "v130", "v131", "v132", "v133", "v134", "v135", "v136", "v137", "v138", "v139", "v140", "v141", "v142", "v143", "v144", "v145", "v146", "v147", "v148", "v149", "v150", "v151", "v152", "v153", "v154", "v155", "v156", "v157", "v158", "v159", "v160", "v161", "v162", "v163", "v164", "v165", "v166", "v167", "v168", "v169", "v170", "v171", "v172", "v173", "v174", "v175", "v176", "v177", "v178", "v179", "v180", "v181", "v182", "v183", "v184", "v185", "v186", "v187", "v188", "v189", "v190", "v191", "v192", "v193", "v194", "v195", "v196", "v197", "v198", "v199", "v200", "v201", "v202", "v203", "v204", "v205", "v206", "v207", "v208", "v209", "v210", "v211", "v212", "v213", "v214", "v215", "v216", "v217", "v218", "v219", "v220", "v221", "v222", "v223", "v224", "v225", "v226", "v227", "v228", "v229", "v230", "v231", "v232", "v233", "v234", "v235", "v236", "v237", "v238", "v239", "v240", "v241", "v242", "v243", "v244", "v245", "v246", "v247", "v248", "v249", "v250", "v251", "v252", "v253", "v254", "v255");
    tag = RFL(tag);
    asm volatile("" : "+s"(bid), "+s"(ka)); asm volatile("" : "+v"(tid));
    const u16 *x = (const u16*)ka[0], *wo = (const u16*)ka[2], *w_r = (const u16*)ka[3], *wg = (const u16*)ka[4], *wu = (const u16*)ka[5], *wd = (const u16*)ka[6]; char* ws = (char*)ka[7];
    const int wv = __builtin_amdgcn_readfirstlane(tid >> 6);
    const unsigned lds0 = (unsigned)(size_t)(lchar*)smem;
#define LANE() const int lane = lane_id(); const int g = lane >> 4, li = lane & 15; (void)g; (void)li
    const int xcd = bid & 7, j = ((bid >> 3) + 8) & 31;
    const bool gemm = j < 25;
    const int fp = xcd >> 1, bl = (j * 13) >> 6, cl = j - bl * 5;
    const int cg = (xcd & 1) * 5 + cl;
    const int par = fp >> 1, I0 = ((fp & 1) * 5 + bl) * 104 - 8;
    const __amdgpu_buffer_rsrc_t rs = __builtin_amdgcn_make_buffer_rsrc((void*)ws, 0, W_END, 0x00020000);
    auto wait_flag = [&](unsigned a) { SPIN { if (RFL(ldsrv<unsigned>(a))) break; __builtin_amdgcn_s_sleep(1); } MEMB(); };
    unsigned ep = 0;
    auto epld = [&]() { asm volatile("s_load_dword %0, %1, 0x0" : "=s"(ep) : "s"(ws) : "memory"); };
    auto mktag = [&]() { asm volatile("s_waitcnt lgkmcnt(0)" : "+s"(ep) :: "memory"); tag = ep * 1664525u + 1013904223u; };
        if (gemm) {
            LANE();
            wait_flag(lds0 + FL_W);
            const u32x4 kk = ldsrv<u32x4>(lds0 + L_KEY + lane * 16);
            const uint2 t2 = top2wave(lane < 52 ? kk.x : 0u, lane < 52 ? kk.z : 0u);
            {
                const unsigned rr = (0xffffu - ((lane ? t2.y : t2.x) & 0xffffu)) & 2047u; const int pos = (int)(rr >> 1) - I0;
                if (lane < 2 && (int)(rr & 1) == par && pos >= 8 && pos < 112) ldsw<unsigned>(lds0 + L_RM + ((pos & ~15) + 4 * (pos & 3) + ((pos >> 2) & 3)) * 4, L_RG + lane * 288);
                ldsw<unsigned>(lds0 + FL_R, 1u);
            }
            unsigned rr[2]; bool own[2]; float xr_[2], xn[2], sg[2];
#pragma unroll
            for (int k = 0; k < 2; ++k) { rr[k] = (0xffffu - ((k ? t2.y : t2.x) & 0xffffu)) & 2047u; const int pos = (int)(rr[k] >> 1) - I0; own[k] = (int)(rr[k] & 1) == par && pos >= 8 && pos < 112; }
            if (__builtin_expect(own[0] || own[1], 0)) {
                __builtin_amdgcn_s_setprio(3);
#pragma unroll
                for (int k = 0; k < 2; ++k) {
                    const int pos = own[k] ? (int)(rr[k] >> 1) - I0 : 8, m = (pos & ~15) + 4 * (pos & 3) + ((pos >> 2) & 3);
                    xr_[k] = lo2f(x[rr[k] * 640 + cg * 64 + lane]);
                    sg[k] = rbf(__builtin_amdgcn_rcpf(1.f + __builtin_amdgcn_exp2f(keyscore(k ? t2.y : t2.x) * -1.4426950408889634f))); NOPK(sg[k]);
                    xn[k] = rbf(xr_[k] + ldsrv<float>(lds0 + L_NM + m * 4)); NOPK(xn[k]); xn[k] = rbf(xn[k] * ldsrv<float>(lds0 + L_RS + pos * 4)); OPQ(xn[k]);
                }
                __builtin_amdgcn_s_sleep(18);
                u32x4 y[5];
                SPIN {
                    MEMB();
                    bool old = false;
#pragma unroll
                    for (int i = 0; i < 5; ++i) y[i] = __builtin_amdgcn_raw_buffer_load_b128(rs, W_Y + (cg * 64 + lane) * 16, i * 10240, SC1);
#pragma unroll
                    for (int i = 0; i < 5; ++i) old = old || y[i].y != tag || y[i].w != tag;
                    if (__builtin_amdgcn_ballot_w64(old) == 0) break;
                }
#pragma unroll
                for (int k = 0; k < 2; ++k) if (own[k]) {
                    float ya = __uint_as_float(y[0][2 * k]) + __uint_as_float(y[1][2 * k]); NOPK(ya);
                    const float ys = (ya + (__uint_as_float(y[2][2 * k]) + __uint_as_float(y[3][2 * k]))) + __uint_as_float(y[4][2 * k]);
                    float gt = silu_b(rbf(xn[k] + rbf(rbf(ys) * sg[k]))) * -1.4426950408889634f, sv; OPQ(gt);
                    SPIN {
                        const unsigned fl = ldsrv<unsigned>(lds0 + L_RG + k * 288 + 256); sv = ldsrv<float>(lds0 + L_RG + k * 288 + lane * 4);
                        if (RFL(fl)) break;
                        __builtin_amdgcn_s_sleep(1);
                    }
                    const float o = fmaf(sv, gt, xr_[k]);
                    asm volatile("global_store_short %0, %1, %2 sc1" :: "v"((rr[k] * 640 + cg * 64 + lane) * 2), "v"(cvtpk(o, o)), "s"(ka[8]) : "memory");
                }
            }
            END();
        }
    {
        LANE();
        const int fq = (j - 25) * 2 + (xcd & 1), f = fp * 14 + fq;
        const int n0 = fq * 40 + wv * 4 + g;
        epld();
        if (wv == 7 && lane < 16) ldsw<u32x4>(lds0 + MISC + lane * 16, (u32x4){0u, 0u, 0u, 0u});
        const int u2 = max(f - 6, 0), kr = (u2 * 26) >> 8, fc = u2 - kr * 10, n1 = wv < 4 ? 3 : 2;
        const int d0 = kr * 11 + wv, d1 = kr * 11 + 4 + wv; const bool two = wv >= 4 && wv < 7 && d1 < 54;
        const char* w1 = (const char*)(f & 1 ? wu : wg) + min(f >> 1, 26) * 128; const char* w2 = (const char*)wd + fc * 128;
        unsigned fv[2][4];
#pragma unroll
        for (int i = 0; i < 4; ++i) { const unsigned c = (((lane & 7) ^ (2 * ((lane >> 4) & 1) + 4 * (i & 1))) * 16) - i * 1024; fv[0][i] = ((lane >> 3) + i * 8) * (F_ * 2) + c; fv[1][i] = ((lane >> 3) + i * 8) * 1280 + c; }
        auto issue = [&](int m, int ks, int slot) {
            asm volatile("s_mov_b32 m0, %[l]\n\ts_nop 0\n\tglobal_load_lds_dwordx4 %0, %[b] nt\n\tglobal_load_lds_dwordx4 %1, %[b] offset:1024 nt\n\tglobal_load_lds_dwordx4 %2, %[b] offset:2048 nt\n\tglobal_load_lds_dwordx4 %3, %[b] offset:3072 nt"
                         :: "v"(fv[m][0]), "v"(fv[m][1]), "v"(fv[m][2]), "v"(fv[m][3]), [b] "s"((m ? w2 : w1) + (unsigned)(ks * 32 * (m ? 1280 : F_ * 2))), [l] "s"(lds0 + wv * 16384 + slot * 4096) : "memory");
        };
        if (wv == 4) {
            const int l = min(f * 14 + lane, 770), o = l < 280 ? W_KEY + l * 128 : l < 536 ? W_ST + (l - 280) * 128 : W_Y + (l - 536) * 256;
            if (lane < 14) asm volatile("s_mov_b32 m0, %[l]\n\ts_nop 0\n\tbuffer_load_dword %0, %1, 0 offen lds" :: "v"(o), "s"(rs), [l] "s"(lds0 + L_DUM) : "memory");
        }
        if (wv >= 5) {
            const int i = min((j - 25) * 172 + (7 - wv) * 64 + lane, 1199), n = (i * 4370) >> 16, e = i - n * 15, m = (e * 52) >> 8, rw = 4 * n + fp;
            asm volatile("s_mov_b32 m0, %[l]\n\ts_nop 0\n\tglobal_load_lds_dword %0, %[b]" :: "v"(rw * 3840 + ((rw >> 6) + 1) * 245760 + e * 128 + m * 640 + (xcd & 1) * 640), [b] "s"(ka[1]), [l] "s"(lds0 + L_DUM) : "memory");
        }
        BARN();
        asm volatile("s_waitcnt vmcnt(%2)" : "+{v[84:115]}"(sb), "+{v[116:123]}"(sc) : "n"(5) : "memory");
#define SXR(i) ((i) < 20 ? sa[(12 + (i)) & 31] : (i) < 52 ? sb[((i) - 20) & 31] : sc[((i) - 52) & 7])
        mktag();
        {
            const unsigned one2 = 0x3F803F80u;
            float wsum = 0.f;
#pragma unroll
            for (int e = 0; e < 20; ++e) wsum = dot2(SXR(40 + e), one2, wsum);
            wsum = sum16(wsum);
            auto stat = [&](int p) {
                float s1 = 0.f, s2 = 0.f, sr = 0.f;
#pragma unroll
                for (int e = 0; e < 20; ++e) { const unsigned u = SXR(20 * p + e); s1 = dot2(u, one2, s1); s2 = dot2(u, u, s2); sr = dot2(u, SXR(40 + e), sr); }
                const float mean = sum16(s1) * (1.f / 640.f);
                const unsigned mb2 = cvtpk(mean, mean); const float mb = lo2f(mb2);
                const float q = fmaf(40.f * mb, mb, fmaf(-2.f * mb, s1, s2));
                float dm = mean - mb; NOPK(dm);
                dm *= dm; NOPK(dm);
                const float var = fmaxf(sum16(q) * (1.f / 640.f) - dm, 0.f);
                const float sd = rbf(__builtin_amdgcn_sqrtf(rbf(rbf(var) + 1e-5f)));
                const float rsc = rbf(fmaf(-mb, wsum, sum16(sr)) / sd);
                const int n = n0 + 32 * p, i = (fp & 1) * 512 + n;
                if (li == 0) {
                    asm volatile("buffer_store_dwordx4 %0, %1, %2, 0 offen sc1\n\ts_nop 1" :: "v"((u32x4){(mb2 << 16) ^ 0x80000000u, tag, __float_as_uint(__builtin_amdgcn_rcpf(sd)), tag}), "v"(n < 512 ? W_ST + (par * 1024 + i) * 16 : 0x40000000), "s"(rs) : "memory");
                    ldsw<unsigned>(lds0 + LS_ROW + (p * 32 + wv * 4 + g) * 4, n < 512 ? rkey(rsc, 2 * i + par) : 0u);
                }
            };
            asm volatile("" : "+v"(wsum));
            if (wv < 2) stat(1);
            asm volatile("s_bitcmp1_b32 0xf3, %2\n\ts_cbranch_scc1 1f\n\ts_waitcnt vmcnt(%1)\n1:\n\ts_waitcnt vmcnt(%1 + 1)" : "+{v[52:83]}"(sa) : "n"(0), "s"(wv) : "memory", "scc");
            if (wv < 5) issue(0, wv, 0);
            stat(0);
        }
        BAR();
        if (wv == 6) asm volatile("buffer_store_dword %0, %1, %2, 0 offen sc1" :: "v"(tag), "v"(lane ? 0x40000000 : W_ACK + (f < 40 ? (f >> 1) * 128 + 40 + (f & 1) * 4 : 2560 + (f - 40) * 4)), "s"(rs) : "memory");
        if (wv == 7) {
            const unsigned k = ldsrv<unsigned>(lds0 + LS_ROW + min(lane, 39) * 4);
            const uint2 t2 = top2wave(lane < 40 ? k : 0u, 0u);
            if (lane == 0 && fq < 13) asm volatile("buffer_store_dwordx4 %0, %1, %2, 0 offen sc1\n\ts_nop 1" :: "v"((u32x4){t2.x, tag, t2.y, tag}), "v"(W_KEY + (fp * 13 + fq) * 16), "s"(rs) : "memory");
        }
        BARN();
        if (wv >= 5) issue(0, wv, 0);
        issue(0, wv + 8, 1);
        auto wh = [&]() { const int i = min((j - 25) * 116 + (wv - 4) * 29 + lane, 799), n = (i * 13108) >> 16; return (4 * n + fp) * 1280 + (i - 5 * n) * 128 + (xcd & 1) * 640; };
        if (wv < 4) issue(0, wv + 16, 2);
        else asm volatile("s_mov_b32 m0, %[l]\n\ts_mov_b32 exec_hi, 0\n\tglobal_load_lds_dword %0, %[b]\n\ts_mov_b32 exec_hi, -1" :: "v"(wh()), [b] "s"(wo), [l] "s"(lds0 + L_DUM) : "memory");
        u32x4 kk;
        BARN();
        asm volatile("s_cmp_lg_u32 %3, 7\n\ts_cbranch_scc1 1f\n\ts_sleep 0\n\tbuffer_load_dwordx4 %0, %1, %2, 0 offen sc1\n1:\n\ts_barrier" : "=&v"(kk) : "v"(W_KEY + min(lane, 51) * 16), "s"(rs), "s"(wv) : "memory", "scc");
        if (wv != 7) issue(1, d0, 3);
        asm volatile("s_cmp_lg_u32 %1, 7\n\ts_cbranch_scc1 1f\n\ts_waitcnt vmcnt(0)\n1:" : "+v"(kk) : "s"(wv) : "memory", "scc");
        if (wv == 7) {
            SPIN {
                if (__builtin_amdgcn_ballot_w64(kk.y != tag || kk.w != tag) == 0) break;
                asm volatile("buffer_load_dwordx4 %0, %1, %2, 0 offen sc1\n\ts_waitcnt vmcnt(0)" : "=&v"(kk) : "v"(W_KEY + min(lane, 51) * 16), "s"(rs) : "memory");
            }
            const uint2 t2 = top2wave(lane < 52 ? kk.x : 0u, lane < 52 ? kk.z : 0u);
            if (lane < 2) ldsw<unsigned>(lds0 + ROUT + lane * 8, (0xffffu - ((lane ? t2.y : t2.x) & 0xffffu)) & 2047u);
        }
        BAR();
        const unsigned row = ldsrv<unsigned>(lds0 + ROUT + (li & 1) * 8);
        u32x4 af[3], st;
        asm volatile("global_load_dwordx4 %0, %4, %5\n\tglobal_load_dwordx4 %1, %4, %5 offset:512\n\tglobal_load_dwordx4 %2, %6, %5 offset:1024\n\tbuffer_load_dwordx4 %3, %7, %8, 0 offen sc1"
                     : "=&v"(af[0]), "=&v"(af[1]), "=&v"(af[2]), "=&v"(st) : "v"(row * 1280u + wv * 64 + g * 16), "s"(x), "v"(row * 1280u + (wv & 3) * 64 + g * 16), "v"(wv ? 0x40000000 : W_ST + ((row & 1) * 1024 + (row >> 1)) * 16), "s"(rs) : "memory");
        bf16x8 wf[3][4];
        unsigned ra[4];
#pragma unroll
        for (int q = 0; q < 4; ++q) ra[q] = lds0 + wv * 16384 + (unsigned)((8 * g + (li >> 2)) * 128 + (((q ^ (((li >> 3) & 1) + 2 * (g & 1))) * 32) + (li & 3) * 8));
        auto rdf = [&](int k, int slot) {
#pragma unroll
            for (int q = 0; q < 4; ++q) wf[k][q] = cat8(trread(ra[q] + slot * 4096), trread(ra[q] + slot * 4096 + 512));
        };
        if (wv == 7) issue(1, d0, 3);
        if (two) issue(1, d1, 2);
        if (two) wvm<12>(); else wvm<8>();
        rdf(0, 0); rdf(1, 1); if (wv < 4) rdf(2, 2);
        asm volatile("s_cmp_eq_u32 %4, 0\n\ts_cbranch_scc0 1f\n\ts_waitcnt vmcnt(0)\n1:\n\ts_waitcnt vmcnt(4)" : "+v"(af[0]), "+v"(af[1]), "+v"(af[2]), "+v"(st) : "s"(RFL(two || wv == 7)) : "memory", "scc");
        f32x4 c[4];
#pragma unroll
        for (int q = 0; q < 4; ++q) c[q] = (f32x4){0.f, 0.f, 0.f, 0.f};
#pragma unroll
        for (int k = 0; k < 3; ++k) if (k < n1) {
#pragma unroll
            for (int e = 0; e < 4; ++e) af[k][e] = li & 2 ? 0x3F803F80u : af[k][e];
#pragma unroll
            for (int q = 0; q < 4; ++q) c[q] = mfma32(__builtin_bit_cast(bf16x8, af[k]), wf[k][q], c[q]);
        }
        auto total = [&](int off) {
            f32x4 m;
#pragma unroll
            for (int e = 0; e < 4; ++e) { float a_ = g & 1 ? c[1][e] : c[0][e], b_ = g & 1 ? c[3][e] : c[2][e]; NOPK(a_); NOPK(b_); m[e] = g & 2 ? b_ : a_; }
            ldsw<f32x4>(lds0 + wv * 16384 + off + lane * 16, m);
            BAR();
            if (wv) return m;
            f32x4 pr[8];
#pragma unroll
            for (int w = 0; w < 8; ++w) pr[w] = ldsrv<f32x4>(lds0 + w * 16384 + off + lane * 16);
#pragma unroll
            for (int e = 0; e < 3; ++e) { float t[4];
#pragma unroll
                for (int w = 0; w < 4; ++w) { t[w] = pr[2 * w][e] + pr[2 * w + 1][e]; NOPK(t[w]); }
                float u = t[0] + t[1], v = t[2] + t[3]; NOPK(u); NOPK(v); u += v; NOPK(u); m[e] = u; }
            return m;
        };
        {
            const f32x4 s = total(0);
            if (wv == 0 && f < 54) {
                SPIN {
                    if (__builtin_amdgcn_ballot_w64(st.y != tag || st.w != tag) == 0) break;
                    asm volatile("buffer_load_dwordx4 %0, %1, %2, 0 offen sc1\n\ts_waitcnt vmcnt(0)" : "=&v"(st) : "v"(W_ST + ((row & 1) * 1024 + (row >> 1)) * 16), "s"(rs) : "memory");
                }
                const float n0_ = __uint_as_float(__builtin_amdgcn_readlane(st.x, 0)), n1_ = __uint_as_float(__builtin_amdgcn_readlane(st.x, 1)), i0 = __uint_as_float(__builtin_amdgcn_readlane(st.z, 0)), i1 = __uint_as_float(__builtin_amdgcn_readlane(st.z, 1));
                float v0 = rbf(fmaf(n0_, s.z, s.x) * i0), v1 = rbf(fmaf(n1_, s.z, s.y) * i1);
                if (!(f & 1)) { v0 = silu_b(v0); NOPK(v0); v1 = silu_b(v1); }
                const unsigned hb = cvtpk(v0, v1), nb = dppu<0xB1>(hb);
                const unsigned pay = (lane & 1) ? __builtin_amdgcn_perm(hb, nb, 0x07060302u) : __builtin_amdgcn_perm(nb, hb, 0x05040100u);
                asm volatile("buffer_store_dwordx2 %0, %1, %2, 0 offen sc1" :: "v"((u32x2){pay, tag}), "v"(W_H + ((f & 1) * 2 + (lane & 1)) * 6912 + ((f >> 1) * 32 + (lane >> 1)) * 8), "s"(rs) : "memory");
            }
        }
        if (f == 0 && wv == 1) {
            SPIN {
                u32x4 a_;
                asm volatile("buffer_load_dwordx4 %0, %1, %2, 0 offen sc1\n\ts_waitcnt vmcnt(0)" : "=&v"(a_) : "v"(W_ACK + (lane < 60 ? ((lane * 43) >> 7) * 80 + lane * 16 : 1600 + lane * 16)), "s"(rs) : "memory");
                if (__builtin_amdgcn_ballot_w64(a_.x != tag || a_.y != tag || a_.z != tag || a_.w != tag) == 0) break;
                __builtin_amdgcn_s_sleep(8);
            }
            if (lane == 0) __builtin_amdgcn_raw_buffer_store_b32(tag, rs, W_EP, 0, SC1);
        }
        if (f >= 6) {
            wvm<0>();
            rdf(0, 3); rdf(1, 2);
            const int ho = W_H + ((lane >> 4) & 1) * 6912 + ((lane >> 5 && two ? d1 : d0) * 16 + li) * 8;
            u32x2 hg, hu;
            __builtin_amdgcn_s_sleep(26);
            SPIN {
                asm volatile("buffer_load_dwordx2 %0, %2, %3, 0 offen sc1\n\tbuffer_load_dwordx2 %1, %2, %3, %4 offen sc1\n\ts_waitcnt vmcnt(0)" : "=&v"(hg), "=&v"(hu) : "v"(ho), "s"(rs), "s"(13824) : "memory");
                if (__builtin_amdgcn_ballot_w64(hg.y != tag || hu.y != tag) == 0) break;
                __builtin_amdgcn_s_sleep(10);
            }
            float h0 = lo2f(hg.x) * lo2f(hu.x), h1 = hi2f(hg.x); NOPK(h0); NOPK(h1); h1 *= hi2f(hu.x); NOPK(h1);
            ldsw<unsigned>(lds0 + wv * 16384 + 4096 + lane * 4, cvtpk(h0, h1));
#pragma unroll
            for (int q = 0; q < 4; ++q) c[q] = (f32x4){0.f, 0.f, 0.f, 0.f};
#pragma unroll
            for (int k = 0; k < 2; ++k) if (k == 0 || two) {
                const u32x4 h = ldsrv<u32x4>(lds0 + wv * 16384 + 4096 + k * 128 + (li & 1) * 64 + g * 16);
#pragma unroll
                for (int q = 0; q < 4; ++q) c[q] = mfma32(__builtin_bit_cast(bf16x8, h), wf[k][q], c[q]);
            }
            const f32x4 ys = total(1024);
            if (wv == 0) __builtin_amdgcn_raw_buffer_store_b128((u32x4){__float_as_uint(ys.x), tag, __float_as_uint(ys.y), tag}, rs, W_Y + kr * 10240 + (fc * 64 + lane) * 16, 0, SC1);
        }
        wvm<0>();
    }
}

extern "C" void kernel_launch(void* const* d_in, const int* in_sizes, int n_in, void* d_out, int out_size, void* d_ws,
                              size_t ws_size, hipStream_t stream) {
    (void)hipFuncSetAttribute((const void*)fused, hipFuncAttributeMaxDynamicSharedMemorySize, LDSZ);
    fused<<<256, 512, LDSZ, stream>>>((const u16*)d_in[0], (const u16*)d_in[1], (const u16*)d_in[2], (const u16*)d_in[4],
                                      (const u16*)d_in[5], (const u16*)d_in[6], (const u16*)d_in[7], (char*)d_ws, (u16*)d_out);
}
```
